```python
import torch

def _softmax(x, axis=-1):
    return torch.softmax(x, dim=axis)

def _gelu(x):
    return torch.nn.functional.gelu(x, approximate='tanh')

def _silu(x):
    return torch.nn.functional.silu(x)

def _sigmoid(x):
    return torch.sigmoid(x)

def _layernorm(x, axis=-1, eps=1e-05):
    m = x.mean(dim=axis, keepdim=True)
    v = x.var(dim=axis, keepdim=True, unbiased=False)
    return (x - m) / torch.sqrt(v + eps)

def _scatter_add(tgt, idx, src, axis=0):
    return tgt.clone().index_add_(axis, idx, src)

def _segment_sum(src, seg, n):
    out = torch.zeros((n,) + src.shape[1:], dtype=src.dtype)
    return out.index_add_(0, seg, src)

def _pad(x, pw):
    flat = tuple((v for pair in reversed(pw) for v in pair))
    return torch.nn.functional.pad(x, flat)
kernel_name = 'concat_heads__spectral_gfnet__memory_aug_recurrent_mem_tr__prompt'

def setup_inputs():

    _w = lambda a: (lambda t: t.to(torch.bfloat16) if t.is_floating_point() else t)(torch.as_tensor(a))

    def _A_setup():
        B, S, D = (1, 2048, 1024)
        Df = D // 2 + 1
        return {'x': _w((lambda *s: torch.randn(*s, dtype=torch.bfloat16) * 0.05)(B, S, D)), 'w_r': _w((lambda *s: torch.randn(*s, dtype=torch.bfloat16) * 3.2)(S, Df) * 0.02), 'w_i': _w((lambda *s: torch.randn(*s, dtype=torch.bfloat16) * 3.2)(S, Df) * 0.02)}
    _A_in = _A_setup()

    def _B_setup():
        B, S, Dm = (1, 2048, 1024)
        H, Dh = (32, 128)
        M, seg = (8, 32)
        seg = min(seg, S)
        S = S // seg * seg
        return {'x': _w((lambda *s: torch.randn(*s, dtype=torch.bfloat16) * 0.05)(B, S, Dm)), 'mem0': _w((lambda *s: torch.randn(*s, dtype=torch.bfloat16) * 0.4)(B, M, Dm)), 'Wq': _w((lambda *s: torch.randn(*s, dtype=torch.bfloat16) * 6.4)(Dm, H, Dh) * Dm ** (-0.5)), 'Wk': _w((lambda *s: torch.randn(*s, dtype=torch.bfloat16) * 6.4)(Dm, H, Dh) * Dm ** (-0.5)), 'Wv': _w((lambda *s: torch.randn(*s, dtype=torch.bfloat16) * 0.4)(Dm, H, Dh) * Dm ** (-0.5)), 'Wo': _w((lambda *s: torch.randn(*s, dtype=torch.bfloat16) * 0.4)(H, Dh, Dm) * (H * Dh) ** (-0.5)), 'seg_len': seg}
    _B_in = _B_setup()
    return {'A_x': _A_in['x'], 'A_w_r': _A_in['w_r'], 'A_w_i': _A_in['w_i'], 'B_x': _B_in['x'], 'B_mem0': _B_in['mem0'], 'B_Wq': _B_in['Wq'], 'B_Wk': _B_in['Wk'], 'B_Wv': _B_in['Wv'], 'B_Wo': _B_in['Wo'], 'B_seg_len': _B_in['seg_len']}

def reference(A_x, A_w_r, A_w_i, B_x, B_mem0, B_Wq, B_Wk, B_Wv, B_Wo, B_seg_len):

    def _A_ref(x, w_r, w_i):
        B, S, D = x.shape
        X = (lambda x, axes=(-2, -1): torch.fft.rfft2(x.float(), dim=axes))(x, axes=(-2, -1))
        W = w_r + 1j * w_i
        X = X * W
        y = (lambda x, s, axes=(-2, -1): torch.fft.irfft2(x, s=s, dim=axes).to(torch.bfloat16))(X, (S, D), axes=(-2, -1))
        return y

    def _B_ref(x, mem0, Wq, Wk, Wv, Wo, seg_len):
        B, S, Dm = x.shape
        H, Dh = (Wq.shape[1], Wq.shape[2])
        M = mem0.shape[1]
        scale = Dh ** (-0.5)
        n_seg = S // seg_len
        L = M + seg_len
        idx = torch.arange(L)
        mask = (idx[:, None] < M) | (idx[:, None] >= idx[None, :])
        mem = mem0
        outs = []
        for s in range(n_seg):
            xs = x[:, s * seg_len:(s + 1) * seg_len, :].clone()
            h = torch.concatenate([mem, xs], axis=1)
            q = torch.einsum('btd,dhe->bhte', h, Wq)
            k = torch.einsum('btd,dhe->bhte', h, Wk)
            v = torch.einsum('btd,dhe->bhte', h, Wv)
            scores = torch.einsum('bhqe,bhke->bhqk', q, k) * scale
            scores = torch.where(mask[None, None], scores, -1000000000.0)
            p = _softmax(scores, axis=-1)
            o = torch.einsum('bhqk,bhke->bhqe', p, v)
            o = torch.einsum('bhte,hed->btd', o, Wo)
            mem = o[:, :M, :].clone()
            outs.append(o[:, M:, :])
        return torch.concatenate(outs, axis=1)
    a = _A_ref(A_x, A_w_r, A_w_i)
    b = _B_ref(B_x, B_mem0, B_Wq, B_Wk, B_Wv, B_Wo, B_seg_len)
    return torch.concatenate([a, b], axis=-1)
```

```cpp
#include <hip/hip_runtime.h>
#include <hip/hip_bf16.h>
#include <stdint.h>
#include <type_traits>

typedef unsigned short u16;
typedef unsigned long long u64;
using bf16x8 = __attribute__((ext_vector_type(8))) __bf16;
using bf16x16 = __attribute__((ext_vector_type(16))) __bf16;
using f32x4 = __attribute__((ext_vector_type(4))) float;
typedef unsigned v32u __attribute__((ext_vector_type(32)));
typedef unsigned v16u __attribute__((ext_vector_type(16)));
typedef unsigned v8u __attribute__((ext_vector_type(8)));
typedef unsigned v4u __attribute__((ext_vector_type(4)));
typedef unsigned v2u __attribute__((ext_vector_type(2)));
typedef short s16x4 __attribute__((ext_vector_type(4)));
typedef float f32x2 __attribute__((ext_vector_type(2)));
#define GAS __attribute__((address_space(1)))
#define LAS __attribute__((address_space(3)))
typedef GAS unsigned gu32;
#define RLX_AGENT __ATOMIC_RELAXED, __HIP_MEMORY_SCOPE_AGENT

constexpr int S = 2048, D = 1024, H = 32, DH = 128, SEG = 32, NSEG = 64;
constexpr int DF = 513;

#define XSW 4
#define BIDX (blockIdx.x ^ XSW)
constexpr int NXC = 4;
constexpr int PQ = 32 / NXC, PO = 64 / NXC;

__shared__ __attribute__((aligned(16))) char g_lds[163840];
#define SPINCAP (1u << 18)
constexpr unsigned kSpinCap = SPINCAP;
constexpr int AUX_SC1 = 16;
constexpr int OOB = 0x7ff00000;
constexpr size_t UNIT = 524288;
constexpr int F_QK = 0, F_VW = 8192, CNT_FFT1 = 16384, CNT_FFT2 = 16512, CNT_ARM = 16640;
constexpr int Q_FFT1 = 16896, Q_FFT2 = 17024, Q_FFT3 = 17152;
constexpr size_t OFF_TMO = 20480;
constexpr size_t OFF_CAL = 24576, OFF_XID = 25088;
constexpr size_t ARM_HOST = 28672;
constexpr int OFF_REQ = 49152, OFF_ACK = 53248, CNT_HS = 17280, CNT_HSO = 17408;
constexpr size_t OFF_SCR = 65536;
constexpr size_t OFF_VM = UNIT;
constexpr int VM_SLOT = 4 << 20, VM_SLOT2 = 8 << 20;
constexpr size_t OFF_DBG = 3 * UNIT;
constexpr size_t OFF_STEP = 27 * UNIT;
constexpr int R_MEM = 0, R_EX = 131072, R_PM = 262144, R_SC = 294912, R_ST = 327680, R_PX = 393216, R_DUM = 475136;
constexpr size_t OFF_QK = (size_t)48 << 20;
constexpr size_t OFF_X1 = (size_t)217 << 20;
constexpr size_t OFF_VWX = (size_t)89 << 20;
constexpr size_t WS_USED = (size_t)217 << 20;
constexpr int NARM = 4;

__device__ __forceinline__ int olane() {
  int t;
  asm volatile("v_mbcnt_lo_u32_b32 %0, -1, 0\n\tv_mbcnt_hi_u32_b32 %0, -1, %0" : "=v"(t));
  __builtin_assume(t >= 0 && t < 64);
  return t;
}
__device__ __forceinline__ float bf2f(u16 v) { return __uint_as_float((unsigned)v << 16); }
__device__ __forceinline__ unsigned pk2(float a, float b) {
  typedef __bf16 b2 __attribute__((ext_vector_type(2)));
  f32x2 f = {a, b};
  b2 r = __builtin_convertvector(f, b2);
  return __builtin_bit_cast(unsigned, r);
}
__device__ __forceinline__ unsigned pk2s(float a, float b) { return min(pk2(a, b), 0xfffffffeu); }
__device__ __forceinline__ u16 f2bf(float f) { return (u16)(pk2(f, 0.f) & 0xffffu); }
__device__ __forceinline__ float rbf(float f) { return __uint_as_float(pk2(0.f, f)); }
__device__ __forceinline__ float opq_one() {
  float o = 1.f;
  asm("" : "+s"(o));
  return o;
}
__device__ __forceinline__ unsigned fold_pk(f32x4 a, float one) { return pk2(a[0] + a[2], __builtin_fmaf(a[1], one, a[3])); }
__device__ __forceinline__ float mul1(float a, float c) {
  float r;
  asm("v_mul_f32 %0, %2, %1" : "=v"(r) : "v"(a), "s"(c));
  return r;
}
__device__ __forceinline__ float fma1(float a, float c, float b) {
  float r;
  asm("v_fma_f32 %0, %1, %2, %3" : "=v"(r) : "v"(a), "s"(c), "v"(b));
  return r;
}
__device__ __forceinline__ int xreg(int r) { return ((r & 1) << 2) | ((r >> 1) & 1) | ((r >> 1) & 2); }

__device__ __forceinline__ void tmo_report(gu32* tmo, unsigned code) {
  unsigned z = 0, c = code;
  asm volatile("" : "+v"(z), "+v"(c));
  asm volatile("global_store_dword %0, %1, %2 sc1" ::"v"(z), "v"(c), "s"(tmo) : "memory");
}
__device__ __forceinline__ bool spin_fail(unsigned& spins, gu32* tmo, unsigned code) {
  asm volatile("" ::: "memory");
  if (__builtin_expect(++spins > kSpinCap, 0)) {
    tmo_report(tmo, code);
    return true;
  }
  return false;
}
__device__ __forceinline__ unsigned wave_min(unsigned v) {
  v = min(v, (unsigned)__builtin_amdgcn_mov_dpp((int)v, 0x121, 0xf, 0xf, true));
  v = min(v, (unsigned)__builtin_amdgcn_mov_dpp((int)v, 0x122, 0xf, 0xf, true));
  v = min(v, (unsigned)__builtin_amdgcn_mov_dpp((int)v, 0x124, 0xf, 0xf, true));
  v = min(v, (unsigned)__builtin_amdgcn_mov_dpp((int)v, 0x128, 0xf, 0xf, true));
  auto r = __builtin_amdgcn_permlane32_swap(v, v, false, false);
  v = min(r[0], r[1]);
  auto q = __builtin_amdgcn_permlane16_swap(v, v, false, false);
  return min(q[0], q[1]);
}
__device__ __forceinline__ float xmax16_32(float x) {
  auto r = __builtin_amdgcn_permlane32_swap(__float_as_uint(x), __float_as_uint(x), false, false);
  x = __builtin_amdgcn_fmed3f(__uint_as_float(r[0]), __uint_as_float(r[1]), __builtin_inff());
  auto q = __builtin_amdgcn_permlane16_swap(__float_as_uint(x), __float_as_uint(x), false, false);
  return __builtin_amdgcn_fmed3f(__uint_as_float(q[0]), __uint_as_float(q[1]), __builtin_inff());
}
__device__ __forceinline__ float xsum16(float x) {
  auto q = __builtin_amdgcn_permlane16_swap(__float_as_uint(x), __float_as_uint(x), false, false);
  return __uint_as_float(q[0]) + __uint_as_float(q[1]);
}
__device__ __forceinline__ float xsum16_32(float x) {
  auto r = __builtin_amdgcn_permlane32_swap(__float_as_uint(x), __float_as_uint(x), false, false);
  return xsum16(__uint_as_float(r[0]) + __uint_as_float(r[1]));
}
__device__ __forceinline__ unsigned mx4(unsigned m, v4u v) { return max(max(max(max(m, v[0]), v[1]), v[2]), v[3]); }
__device__ __forceinline__ bool anysent(unsigned m) { return __builtin_amdgcn_ballot_w64(m == 0xffffffffu) != 0ull; }

__device__ __forceinline__ unsigned now32() { return (unsigned)__builtin_amdgcn_s_memrealtime(); }
__device__ __forceinline__ unsigned cyc32() { return (unsigned)__builtin_amdgcn_s_memtime(); }
#define CMAX 14000
__device__ __forceinline__ void wait_cyc(unsigned t) {
  t = __builtin_amdgcn_readfirstlane(t);
  int d = (int)(t - cyc32());
  if (d > CMAX) return;
  while (d > 260) {
    __builtin_amdgcn_s_sleep(1);
    d = (int)(t - cyc32());
  }
  d = __builtin_amdgcn_readfirstlane(d - 70);
  if (d > 0) asm volatile("1: s_sub_u32 %0, %0, 28\n\ts_cbranch_scc0 1b" : "+s"(d)::"scc", "memory");
}
#define CLKSPAN 124u
struct Clk {
  unsigned r, m;
  unsigned cq, inv;
};
__device__ __forceinline__ bool clk_edge(unsigned& r, unsigned& m, int tries) {
  constexpr int NS = 14;
#pragma unroll 1
  for (int k = 0; k < tries; ++k) {
    u64 t[NS], m0, m1;
    asm volatile("s_memtime %0" : "=s"(m0));
#pragma unroll
    for (int i = 0; i < NS; ++i) {
      asm volatile("s_memrealtime %0\n\ts_nop 0" : "=s"(t[i]));
      if (i == NS - 2) asm volatile("s_memtime %0" : "=s"(m1));
    }
    asm volatile("s_waitcnt lgkmcnt(0)" : "+s"(m0), "+s"(m1), "+s"(t[0]), "+s"(t[1]), "+s"(t[2]), "+s"(t[3]), "+s"(t[4]), "+s"(t[5]),
                 "+s"(t[6]), "+s"(t[7]), "+s"(t[8]), "+s"(t[9]), "+s"(t[10]), "+s"(t[11]), "+s"(t[12]), "+s"(t[13]));
    unsigned n = 0;
#pragma unroll
    for (int i = 1; i < NS; ++i) asm("s_cmp_eq_u32 %1, %2\n\ts_addc_u32 %0, %0, 0" : "+s"(n) : "s"((unsigned)t[i]), "s"((unsigned)t[0]) : "scc");
    r = (unsigned)t[0] + 4;
    m = (unsigned)m0 + n * 8;
    asm volatile("" : "+s"(m));
    if ((unsigned)m1 - (unsigned)m0 <= CLKSPAN) return true;
  }
  return false;
}
__device__ __forceinline__ unsigned sdiv(unsigned a, unsigned b) {
  unsigned r;
  float t, u;
  asm volatile("v_cvt_f32_u32 %1, %4\n\tv_cvt_f32_u32 %2, %3\n\tv_rcp_f32 %1, %1\n\ts_nop 0\n\tv_mul_f32 %1, %1, %2\n\tv_cvt_u32_f32 %1, %1\n\t"
               "s_nop 0\n\tv_readfirstlane_b32 %0, %1\n\ts_nop 3"
               : "=s"(r), "=&v"(t), "=&v"(u)
               : "s"(a), "s"(b));
  return r;
}
__device__ __forceinline__ void clk_init(Clk& c) {
  clk_edge(c.r, c.m, 4);
  c.cq = 92500u;
  c.inv = 46432u;
}
__device__ __forceinline__ void clk_update(Clk& c, bool first = false) {
  unsigned r, m;
  if (!clk_edge(r, m, first ? 2 : 1) && r - c.r < 1500u) return;
  if (r - c.r >= 100u && r - c.r < 40000u) {
    unsigned q = sdiv((m - c.m) << 12, r - c.r);
    asm("s_max_u32 %0, %0, 0x101d0\n\ts_min_u32 %0, %0, 0x1b580" : "+s"(q)::"scc");
    c.cq = first ? q : (3 * c.cq + q) >> 2;
    c.inv = sdiv(0xffffffffu, c.cq);
  }
  c.r = r;
  c.m = m;
}
__device__ __forceinline__ void clk_fresh(Clk& c) {
  unsigned r, m;
  if (clk_edge(r, m, 1)) {
    c.r = r;
    c.m = m;
  }
}
__device__ __forceinline__ unsigned clk_fine(const Clk& c, unsigned m) {
  return (c.r << 4) + (unsigned)(((u64)(m - c.m) * c.inv) >> 16);
}
__device__ __forceinline__ int stamp_max(const Clk& c, unsigned st) {
  const unsigned tn = (c.r << 4) + 65536u;
  return 65536 - __builtin_amdgcn_readfirstlane((int)wave_min(tn - st));
}
__device__ __forceinline__ unsigned clk_cyc(const Clk& c, int d) { return c.m + (unsigned)(((long long)d * (int)c.cq) >> 16); }
#define NS2F(ns) (((ns) * 8) / 5)
struct Pace {
  unsigned rf;
  int ed, ea, dq;
};
__device__ __forceinline__ void pace_step(Pace& p, int s, unsigned rf, unsigned rep, int marg, int tol, int xlate) {
  const int arr = (int)(rf - p.rf) - p.dq;
  const bool late = s == 0 || arr > p.ea + tol;
  if (s > 0) {
    const int md = (int)~__builtin_amdgcn_readfirstlane(wave_min(~rep));
    p.ea = min(arr, p.ea + 5);
    p.ed = (int)min((unsigned)md, (unsigned)p.ed + 5u);
  }
  p.rf = rf;
  p.dq = p.ed + marg + (late ? xlate : 0);
}

template <int SLP>
__device__ __forceinline__ void wait_flag(__amdgpu_buffer_rsrc_t rs, int off, gu32* tmo, unsigned code) {
  _Pragma("clang loop unroll(disable)") for (unsigned spins = 0;;) {
    asm volatile("" : "+v"(off));
    unsigned v = __builtin_amdgcn_raw_buffer_load_b8(rs, off, 0, AUX_SC1);
    if (__all(v == 0u)) break;
    __builtin_amdgcn_s_sleep(SLP);
    if (spin_fail(spins, tmo, code)) break;
  }
}
#define RAW_BARRIER()                                    \
  do {                                                   \
    asm volatile("" ::: "memory");                       \
    __builtin_amdgcn_s_waitcnt(0xc07f);                  \
    __builtin_amdgcn_s_barrier();                        \
    asm volatile("" ::: "memory");                       \
  } while (0)
#define BARE_BARRIER()                 \
  do {                                 \
    asm volatile("" ::: "memory");     \
    __builtin_amdgcn_s_barrier();      \
    asm volatile("" ::: "memory");     \
  } while (0)
#define VMCNT(n) asm volatile("s_waitcnt vmcnt(" #n ")" ::: "memory")
#define SB() __builtin_amdgcn_sched_barrier(0)
#define OPQ(v, e) int v = (e); asm volatile("" : "+v"(v))
template <bool NT = false>
__device__ __forceinline__ void glds16(const void* base, int voff, unsigned dst) {
  unsigned keep;
  if constexpr (NT)
    asm volatile("s_mov_b32 %0, m0\n\ts_mov_b32 m0, %3\n\ts_nop 0\n\tglobal_load_lds_dwordx4 %1, %2 nt\n\ts_mov_b32 m0, %0"
                 : "=&s"(keep)
                 : "v"(voff), "s"(base), "s"(dst)
                 : "memory");
  else
  asm volatile("s_mov_b32 %0, m0\n\ts_mov_b32 m0, %3\n\ts_nop 0\n\tglobal_load_lds_dwordx4 %1, %2\n\ts_mov_b32 m0, %0"
               : "=&s"(keep)
               : "v"(voff), "s"(base), "s"(dst)
               : "memory");
}
__device__ __forceinline__ void touch4(const void* base, int voff, unsigned dst) {
  unsigned keep;
  asm volatile("s_mov_b32 %0, m0\n\ts_mov_b32 m0, %3\n\ts_nop 0\n\tglobal_load_lds_dword %1, %2\n\ts_mov_b32 m0, %0"
               : "=&s"(keep)
               : "v"(voff), "s"(base), "s"(dst)
               : "memory");
}
__device__ __forceinline__ f32x4 smf(v4u a, v8u b, f32x4 c, int idx) {
  return __builtin_amdgcn_smfmac_f32_16x16x64_bf16(__builtin_bit_cast(bf16x8, a), __builtin_bit_cast(bf16x16, b), c, idx, 0, 0);
}
__device__ __forceinline__ f32x4 smf1(v4u a, v8u b, f32x4 c, int idx) {
  return __builtin_amdgcn_smfmac_f32_16x16x64_bf16(__builtin_bit_cast(bf16x8, a), __builtin_bit_cast(bf16x16, b), c, idx, 0, 1);
}
__device__ __forceinline__ f32x4 mma(v4u a, v4u b, f32x4 c) {
  return __builtin_amdgcn_mfma_f32_16x16x32_bf16(__builtin_bit_cast(bf16x8, a), __builtin_bit_cast(bf16x8, b), c, 0, 0, 0);
}
__device__ __forceinline__ void st128(v4u d, __amdgpu_buffer_rsrc_t rs, int voff, int soff) {
  __builtin_amdgcn_raw_buffer_store_b128(d, rs, voff, soff, AUX_SC1);
  asm volatile("s_nop 1" ::"v"(d));
}
__device__ __forceinline__ f32x4 zero4() {
  f32x2 a, b;
  asm volatile("v_mov_b64 %0, 0" : "=v"(a));
  asm volatile("v_mov_b64 %0, 0" : "=v"(b));
  return f32x4{a[0], a[1], b[0], b[1]};
}
__device__ __forceinline__ f32x2 zero2() {
  f32x2 a;
  asm volatile("v_mov_b64 %0, 0" : "=v"(a));
  return a;
}
__device__ __forceinline__ v4u lo4(v8u v) { return v4u{v[0], v[1], v[2], v[3]}; }
__device__ __forceinline__ v4u hi4(v8u v) { return v4u{v[4], v[5], v[6], v[7]}; }
__device__ __forceinline__ v8u cat8(v4u a, v4u b) { return v8u{a[0], a[1], a[2], a[3], b[0], b[1], b[2], b[3]}; }
__device__ __forceinline__ v32u cat32(v8u a, v8u b, v8u c, v8u d) {
  const v16u x = __builtin_shufflevector(a, b, 0, 1, 2, 3, 4, 5, 6, 7, 8, 9, 10, 11, 12, 13, 14, 15);
  const v16u y = __builtin_shufflevector(c, d, 0, 1, 2, 3, 4, 5, 6, 7, 8, 9, 10, 11, 12, 13, 14, 15);
  return __builtin_shufflevector(x, y, 0, 1, 2, 3, 4, 5, 6, 7, 8, 9, 10, 11, 12, 13, 14, 15, 16, 17, 18, 19, 20, 21, 22, 23, 24, 25, 26,
                                 27, 28, 29, 30, 31);
}
__device__ __forceinline__ v2u trd(LAS char* p) {
  return __builtin_bit_cast(v2u, __builtin_amdgcn_ds_read_tr16_b64_v4i16((LAS s16x4*)p));
}
__device__ __forceinline__ int vm_slot(int s) { return (s & 1) * VM_SLOT + ((s >> 1) & 1) * VM_SLOT2; }
__device__ __forceinline__ int cls_of(unsigned perm, int x) { return (int)((perm >> (3 * x)) & 7u); }

__device__ __forceinline__ void arm_wait(__amdgpu_buffer_rsrc_t rs, gu32* tmo) {
  _Pragma("clang loop unroll(disable)") for (unsigned spins = 0;;) {
    OPQ(off, CNT_ARM);
    if (__all(__builtin_amdgcn_raw_buffer_load_b32(rs, off, 0, AUX_SC1) + 1u >= 64u)) break;
    __builtin_amdgcn_s_sleep(8);
    if (spin_fail(spins, tmo, 0x30007)) break;
  }
}
__device__ __forceinline__ v2u numa_load(__amdgpu_buffer_rsrc_t rs, gu32* tmo) {
  unsigned c, xid;
  _Pragma("clang loop unroll(disable)") for (unsigned spins = 0;;) {
    OPQ(a, olane() * 4);
    c = __builtin_amdgcn_raw_buffer_load_b32(rs, a, (int)OFF_CAL, AUX_SC1);
    xid = __builtin_amdgcn_raw_buffer_load_b32(rs, ((a >> 5) & (NXC - 1)) * 4, (int)OFF_XID, AUX_SC1);
    if (__all((c != 0xffffffffu) & (xid != 0xffffffffu))) break;
    __builtin_amdgcn_s_sleep(16);
    if (spin_fail(spins, tmo, 0x30008)) break;
  }
  return v2u{c, xid};
}
__device__ __forceinline__ unsigned numa_perm(v2u in, LAS unsigned* tab) {
  const int lane = olane();
  const unsigned c = in[0], xid = in[1];
  tab[lane] = 0xffffu;
  tab[64 + lane] = 0xffffu;
  tab[128 + lane] = 0u;
  __builtin_amdgcn_s_waitcnt(0xc07f);
  __hip_atomic_fetch_min(tab + ((c >> 16) & 127u), c & 0xffffu, __ATOMIC_RELAXED, __HIP_MEMORY_SCOPE_WORKGROUP);
  __hip_atomic_fetch_add(tab + 128 + ((c >> 21) & 3u) * 2 + ((c >> 17) & 1u), c & 0xffffu, __ATOMIC_RELAXED, __HIP_MEMORY_SCOPE_WORKGROUP);
  __builtin_amdgcn_s_waitcnt(0xc07f);
  asm volatile("" ::: "memory");
  const unsigned s0 = tab[128 + ((xid >> 2) & 3u) * 2], s1 = tab[129 + ((xid >> 2) & 3u) * 2];
  const bool far = ((lane >> 1) & 1) != (s1 < s0 ? 1 : 0);
  unsigned key = ((tab[(xid & 15u) * 8 + (lane & 7)] + (far ? 0x10000u : 0u)) << 6) | (unsigned)lane;
  if ((lane >> 3) >= NXC) key = 0xffffffffu;
  unsigned perm = 0;
#pragma unroll 1
  for (int r = 0; r < NXC; ++r) {
    const unsigned m = wave_min(key);
    const unsigned bx = (m >> 3) & 7u, bc = m & 7u;
    perm |= bc << (3 * bx);
    if ((unsigned)(lane >> 3) == bx || (unsigned)(lane & 7) == bc) key = 0xffffffffu;
  }
  return __builtin_amdgcn_readfirstlane(perm | (s1 < s0 ? 0u : 8192u));
}
__device__ __forceinline__ int numa_far(v2u in) {
  const unsigned c = in[0], x0 = __builtin_amdgcn_readfirstlane(in[1]);
  int d = (int)(c & 0xffffu);
  d = ((c >> 21) & 3u) == ((x0 >> 2) & 3u) ? ((c & 0x20000u) ? -d : d) : 0;
  d += __builtin_amdgcn_mov_dpp(d, 0x121, 0xf, 0xf, true);
  d += __builtin_amdgcn_mov_dpp(d, 0x122, 0xf, 0xf, true);
  d += __builtin_amdgcn_mov_dpp(d, 0x124, 0xf, 0xf, true);
  d += __builtin_amdgcn_mov_dpp(d, 0x128, 0xf, 0xf, true);
  d = __builtin_amdgcn_readlane(d, 0) + __builtin_amdgcn_readlane(d, 16) + __builtin_amdgcn_readlane(d, 32) + __builtin_amdgcn_readlane(d, 48);
  return d > 0 ? 0 : 8192;
}
#define VRING 64
#define VFAR 0
__device__ __forceinline__ int vw_addr(int fr, int tile, int seg, int gq) {
  return (int)OFF_VWX + ((((tile >> 3) * VRING + (seg & (VRING - 1))) * 8 + (tile & 7)) * 8 + gq) * 4096;
}
__device__ __forceinline__ int fqk(int h, int seg) { return F_QK + h * 256 + seg * 4; }
__device__ __forceinline__ int fvw(int pr, int h, int seg) { return F_VW + ((pr * 4 + (h >> 3)) * 2 + (seg & 1)) * 256 + (seg >> 1) * 8 + (h & 7); }
constexpr int QK_RT = 4096;
__device__ __forceinline__ int qk_addr(int mat, int seg, int h) { return (int)OFF_QK + ((mat * 64 + seg) * 32 + h) * 8192; }

__device__ __forceinline__ u64 hs_post(__amdgpu_buffer_rsrc_t rs, int w, int HSW) {
  const u64 n = __builtin_amdgcn_s_memrealtime();
  if (w == HSW) __builtin_amdgcn_raw_buffer_store_b64(v2u{(unsigned)n, (unsigned)(n >> 32)}, rs, olane() == 0 ? 0 : OOB,
                                                    OFF_REQ + (int)BIDX * 8, AUX_SC1);
  return n;
}
__device__ __forceinline__ bool hs_wait(__amdgpu_buffer_rsrc_t rs, u64 n, v2u v) {
  unsigned spins = 0;
  _Pragma("clang loop unroll(disable)") for (; spins < (kSpinCap << 2); ++spins) {
    if (__builtin_expect(__all(v[0] == (unsigned)n && v[1] == (unsigned)(n >> 32)), 1)) break;
    if (spins) __builtin_amdgcn_s_sleep(8);
    int z = 0;
    asm volatile("" : "+v"(z));
    v = __builtin_amdgcn_raw_buffer_load_b64(rs, z, OFF_ACK + (int)BIDX * 8, AUX_SC1);
  }
  return spins == 0;
}
__device__ __forceinline__ void cnt_inc(char* ws, int cnt) {
  if (olane() == 0) __hip_atomic_fetch_add((gu32*)(ws + cnt), 1u, RLX_AGENT);
}
template <int SLP>
__device__ __forceinline__ void hs_lead(__amdgpu_buffer_rsrc_t rs, bool fill, int cnt, unsigned want) {
  const int l16 = olane() * 16;
  if (fill) {
    for (int o = 0; o < (int)ARM_HOST; o += 1024) __builtin_amdgcn_raw_buffer_store_b128(v4u{~0u, ~0u, ~0u, ~0u}, rs, l16, o, AUX_SC1);
    VMCNT(0);
  }
  _Pragma("clang loop unroll(disable)") for (unsigned spins = 0; spins < (kSpinCap << 2); ++spins) {
    int va = l16;
    asm volatile("" : "+v"(va));
    const v4u a = __builtin_amdgcn_raw_buffer_load_b128(rs, va, OFF_REQ, AUX_SC1);
    const v4u b = __builtin_amdgcn_raw_buffer_load_b128(rs, va + 1024, OFF_REQ, AUX_SC1);
    const unsigned n = __builtin_amdgcn_raw_buffer_load_b32(rs, va & 0, cnt, AUX_SC1);
    if (__all(n + 1u >= want)) break;
    __builtin_amdgcn_raw_buffer_store_b128(a, rs, va, OFF_ACK, AUX_SC1);
    __builtin_amdgcn_raw_buffer_store_b128(b, rs, va + 1024, OFF_ACK, AUX_SC1);
    __builtin_amdgcn_s_sleep(SLP);
  }
}

#define PTW(i, wv)
#define PLDECL
#define PL(i)
#define PLDUMP(wv, mask)
#define PT(i) PTW(i, 0)
#define TN(k, d) (d)
#define EVDECL
#define EV(k)
#define EVDUMP(s)
#define EVCLK(s, c)
#define EVOUT
#define VO_A \
  "s_branch .Lgo_%=\n" \
  ".Lp0_%=:\n" \
  "s_mov_b32 %[cnt], 0x40000\n" \
  ".Lp0r_%=:\n" \
  "s_sleep 2\n" \
  "buffer_load_dwordx4 v[236:239], v251, %[rs], %[o0] offen sc1\n" \
  "s_waitcnt vmcnt(0)\n" \
  "ds_write_b128 v250, v[236:239] offset:0\n" \
  "v_max_u32 v241, v236, v237\n" \
  "v_max3_u32 v241, v241, v238, v239\n" \
  "v_cmp_eq_u32 vcc, -1, v241\n" \
  "s_cbranch_vccz .Lp0b_%=\n" \
  "s_sub_u32 %[cnt], %[cnt], 1\n" \
  "s_cbranch_scc0 .Lp0r_%=\n" \
  "s_branch .Lp0b_%=\n" \
  ".Lgo_%=:\n" \
  "ds_read_b128 v[200:203], v243 offset:32768\n" \
  "ds_read_b128 v[204:207], v243 offset:33792\n" \
  "ds_read_b128 v[208:211], v243 offset:49152\n" \
  "ds_read_b128 v[212:215], v243 offset:50176\n" \
  "v_mov_b64 v[192:193], 0\n" \
  "v_mov_b64 v[194:195], 0\n" \
  "v_mov_b64 v[196:197], 0\n" \
  "v_mov_b64 v[198:199], 0\n" \
  "s_waitcnt lgkmcnt(0)\n" \
  "s_barrier\n" \
  "buffer_load_dwordx4 v[236:239], v251, %[rs], %[o0] offen sc1\n" \
  "s_waitcnt vmcnt(0)\n" \
  "ds_write_b128 v250, v[236:239] offset:0\n" \
  "v_max3_u32 v241, v236, v237, v238\n" \
  "v_max_u32 v241, v241, v239\n" \
  "v_cmp_eq_u32 vcc, -1, v241\n" \
  "s_cbranch_vccnz .Lp0_%=\n" \
  ".Lp0b_%=:\n" \
  "s_waitcnt lgkmcnt(0)\n" \
  "s_barrier\n" \
  "ds_read_b128 v[216:219], v251 offset:0\n" \
  "ds_read_b128 v[220:223], v251 offset:1024\n" \
  "ds_read_b128 v[224:227], v251 offset:2048\n" \
  "ds_read_b128 v[228:231], v251 offset:3072\n" \
  "s_waitcnt lgkmcnt(3)\n" \
  "v_smfmac_f32_16x16x64_bf16 v[192:195], v[216:219], v[200:207], v248\n" \
  "v_smfmac_f32_16x16x64_bf16 v[196:199], v[216:219], v[208:215], v248\n" \
  "ds_read_b128 v[232:235], v251 offset:4096\n" \
  "ds_read_b128 v[236:239], v251 offset:5120\n" \
  "ds_read_b128 v[216:219], v251 offset:6144\n" \
  "s_waitcnt lgkmcnt(5)\n" \
  "v_smfmac_f32_16x16x64_bf16 v[192:195], v[220:223], v[0:7], v248\n" \
  "v_smfmac_f32_16x16x64_bf16 v[196:199], v[220:223], v[8:15], v248\n" \
  "ds_read_b128 v[220:223], v251 offset:7168\n" \
  "s_waitcnt lgkmcnt(5)\n" \
  "v_smfmac_f32_16x16x64_bf16 v[192:195], v[224:227], v[16:23], v248\n" \
  "v_smfmac_f32_16x16x64_bf16 v[196:199], v[224:227], v[24:31], v248\n" \
  "s_waitcnt lgkmcnt(4)\n" \
  "v_smfmac_f32_16x16x64_bf16 v[192:195], v[228:231], v[32:39], v248\n" \
  "v_smfmac_f32_16x16x64_bf16 v[196:199], v[228:231], v[40:47], v248\n" \
  "s_waitcnt lgkmcnt(3)\n" \
  "v_smfmac_f32_16x16x64_bf16 v[192:195], v[232:235], v[48:55], v248\n" \
  "v_mov_b64 v[200:201], 0\n" \
  "v_smfmac_f32_16x16x64_bf16 v[196:199], v[232:235], v[56:63], v248\n" \
  "v_mov_b64 v[202:203], 0\n" \
  "s_waitcnt lgkmcnt(2)\n" \
  "v_smfmac_f32_16x16x64_bf16 v[192:195], v[236:239], v[64:71], v248\n" \
  "v_mov_b64 v[204:205], 0\n" \
  "v_smfmac_f32_16x16x64_bf16 v[196:199], v[236:239], v[72:79], v248\n" \
  "v_mov_b64 v[206:207], 0\n" \
  "s_waitcnt lgkmcnt(1)\n" \
  "v_smfmac_f32_16x16x64_bf16 v[192:195], v[216:219], v[80:87], v248\n" \
  "v_mov_b64 v[208:209], 0\n" \
  "s_waitcnt lgkmcnt(0)\n" \
  "v_smfmac_f32_16x16x64_bf16 v[192:195], v[220:223], v[96:103], v248\n" \
  "v_mov_b64 v[210:211], 0\n" \
  "v_smfmac_f32_16x16x64_bf16 v[196:199], v[216:219], v[88:95], v248\n" \
  "v_mov_b64 v[212:213], 0\n" \
  "v_smfmac_f32_16x16x64_bf16 v[196:199], v[220:223], v[104:111], v248\n" \
  "v_mov_b64 v[214:215], 0\n" \
  "ds_write_b128 v250, v[192:195] offset:24576\n" \
  "s_nop 6\n" \
  "ds_write_b128 v250, v[196:199] offset:28672\n" \
  "s_barrier\n" \
  "s_waitcnt lgkmcnt(0)\n" \
  "s_barrier\n" \
  "s_nop 11\n" \
  "ds_read_b128 v[216:219], v249 offset:0\n" \
  "ds_read_b128 v[220:223], v249 offset:1024\n" \
  "ds_read_b128 v[224:227], v249 offset:16384\n" \
  "ds_read_b128 v[228:231], v249 offset:17408\n" \
  "s_barrier\n" \
  "ds_read_b64_tr_b16 v[232:233], v245 offset:17920\n" \
  "ds_read_b64_tr_b16 v[234:235], v245 offset:18112\n" \
  "ds_read_b64_tr_b16 v[236:237], v245 offset:18688\n" \
  "ds_read_b64_tr_b16 v[238:239], v245 offset:18880\n" \
  "ds_read_b64_tr_b16 v[192:193], v245 offset:16384\n" \
  "ds_read_b64_tr_b16 v[194:195], v245 offset:16576\n" \
  "ds_read_b64_tr_b16 v[196:197], v245 offset:17152\n" \
  "ds_read_b64_tr_b16 v[198:199], v245 offset:17344\n" \
  "s_waitcnt lgkmcnt(6)\n" \
  "v_smfmac_f32_16x16x64_bf16 v[200:203], v[232:235], v[216:223], v248 abid:1\n" \
  "ds_read_b128 v[216:219], v249 offset:32768\n" \
  "ds_read_b128 v[220:223], v249 offset:33792\n" \
  "v_smfmac_f32_16x16x64_bf16 v[204:207], v[232:235], v[128:135], v248 abid:1\n" \
  "s_waitcnt lgkmcnt(6)\n" \
  "v_smfmac_f32_16x16x64_bf16 v[200:203], v[236:239], v[224:231], v248 abid:1\n" \
  "ds_read_b128 v[224:227], v249 offset:49152\n" \
  "ds_read_b128 v[228:231], v249 offset:50176\n" \
  "v_smfmac_f32_16x16x64_bf16 v[204:207], v[236:239], v[136:143], v248 abid:1\n" \
  "s_waitcnt lgkmcnt(6)\n" \
  "v_smfmac_f32_16x16x64_bf16 v[200:203], v[192:195], v[176:183], v248 abid:1\n" \
  "v_smfmac_f32_16x16x64_bf16 v[204:207], v[192:195], v[112:119], v248 abid:1\n" \
  "s_waitcnt lgkmcnt(4)\n" \
  "v_smfmac_f32_16x16x64_bf16 v[200:203], v[196:199], v[184:191], v248 abid:1\n" \
  "v_smfmac_f32_16x16x64_bf16 v[204:207], v[196:199], v[120:127], v248 abid:1\n" \
  "s_waitcnt lgkmcnt(2)\n" \
  "v_smfmac_f32_16x16x64_bf16 v[208:211], v[192:195], v[216:223], v248 abid:1\n" \
  "ds_read_b128 v[216:219], v243 offset:0\n" \
  "ds_read_b128 v[220:223], v243 offset:1024\n" \
  "v_smfmac_f32_16x16x64_bf16 v[212:215], v[192:195], v[144:151], v248 abid:1\n" \
  "s_waitcnt lgkmcnt(2)\n" \
  "v_smfmac_f32_16x16x64_bf16 v[208:211], v[196:199], v[224:231], v248 abid:1\n" \
  "ds_read_b128 v[224:227], v243 offset:16384\n" \
  "ds_read_b128 v[228:231], v243 offset:17408\n" \
  "v_cvt_pk_bf16_f32 v200, v200, v201\n" \
  "v_smfmac_f32_16x16x64_bf16 v[212:215], v[196:199], v[152:159], v248 abid:1\n" \
  "v_cvt_pk_bf16_f32 v201, v202, v203\n" \
  "v_smfmac_f32_16x16x64_bf16 v[212:215], v[232:235], v[160:167], v248 abid:1\n" \
  "v_cvt_pk_bf16_f32 v202, v204, v205\n" \
  "v_smfmac_f32_16x16x64_bf16 v[212:215], v[236:239], v[168:175], v248 abid:1\n" \
  "v_cvt_pk_bf16_f32 v203, v206, v207\n" \
  "s_waitcnt lgkmcnt(2)\n" \
  "v_smfmac_f32_16x16x64_bf16 v[208:211], v[232:235], v[216:223], v248 abid:1\n" \
  "v_permlane16_swap_b32 v200, v202\n" \
  "s_waitcnt lgkmcnt(0)\n" \
  "v_smfmac_f32_16x16x64_bf16 v[208:211], v[236:239], v[224:231], v248 abid:1\n" \
  "v_permlane16_swap_b32 v201, v203\n" \
  "buffer_store_dwordx4 v[200:203], v247, %[rs], %[vs] offen sc1\n" \
  "v_cvt_pk_bf16_f32 v212, v212, v213\n" \
  "v_cvt_pk_bf16_f32 v213, v214, v215\n" \
  "s_nop 4\n" \
  "v_cvt_pk_bf16_f32 v211, v210, v211\n" \
  "v_cvt_pk_bf16_f32 v210, v208, v209\n" \
  "s_nop 0\n" \
  "v_permlane16_swap_b32 v211, v213\n" \
  "v_permlane16_swap_b32 v210, v212\n" \
  "buffer_store_dwordx4 v[210:213], v246, %[rs], %[vs] offen sc1\n" \
  "s_nop 1\n"
#define VO_B \
  "s_branch .Lgo_%=\n" \
  ".Lp0_%=:\n" \
  "s_mov_b32 %[cnt], 0x40000\n" \
  ".Lp0r_%=:\n" \
  "s_sleep 2\n" \
  "buffer_load_dwordx4 v[228:231], v251, %[rs], %[o0] offen sc1\n" \
  "buffer_load_dwordx4 v[232:235], v251, %[rs], %[o1] offen sc1\n" \
  "buffer_load_dwordx4 v[236:239], v251, %[rs], %[o2] offen sc1\n" \
  "s_waitcnt vmcnt(0)\n" \
  "ds_write_b128 v250, v[228:231] offset:0\n" \
  "v_max_u32 v241, v228, v229\n" \
  "v_max3_u32 v241, v241, v230, v231\n" \
  "ds_write_b128 v250, v[232:235] offset:4096\n" \
  "v_max3_u32 v241, v241, v232, v233\n" \
  "v_max3_u32 v241, v241, v234, v235\n" \
  "ds_write_b128 v250, v[236:239] offset:8192\n" \
  "v_max3_u32 v241, v241, v236, v237\n" \
  "v_max3_u32 v241, v241, v238, v239\n" \
  "v_cmp_eq_u32 vcc, -1, v241\n" \
  "s_cbranch_vccz .Lp0b_%=\n" \
  "s_sub_u32 %[cnt], %[cnt], 1\n" \
  "s_cbranch_scc0 .Lp0r_%=\n" \
  "s_branch .Lp0b_%=\n" \
  ".Lgo_%=:\n" \
  "ds_read_b128 v[200:203], v243 offset:32768\n" \
  "ds_read_b128 v[204:207], v243 offset:33792\n" \
  "ds_read_b128 v[208:211], v243 offset:49152\n" \
  "ds_read_b128 v[212:215], v243 offset:50176\n" \
  "v_mov_b64 v[192:193], 0\n" \
  "v_mov_b64 v[194:195], 0\n" \
  "v_mov_b64 v[196:197], 0\n" \
  "v_mov_b64 v[198:199], 0\n" \
  "s_waitcnt lgkmcnt(0)\n" \
  "s_cmp_eq_u32 %[tw], 0\n" \
  "s_cbranch_scc1 .Ltq_%=\n" \
  ".Ltl_%=:\n" \
  "s_memtime vcc\n" \
  "s_waitcnt lgkmcnt(0)\n" \
  "s_sub_i32 %[cnt], %[tg], vcc_lo\n" \
  "s_cmp_lt_i32 %[cnt], 260\n" \
  "s_cbranch_scc1 .Ltf_%=\n" \
  "s_sleep 1\n" \
  "s_branch .Ltl_%=\n" \
  ".Ltf_%=:\n" \
  "s_max_i32 %[cnt], %[cnt], 0\n" \
  ".Ltg_%=:\n" \
  "s_sub_u32 %[cnt], %[cnt], 28\n" \
  "s_cbranch_scc0 .Ltg_%=\n" \
  ".Ltq_%=:\n" \
  "s_barrier\n" \
  "buffer_load_dwordx4 v[228:231], v251, %[rs], %[o0] offen sc1\n" \
  "buffer_load_dwordx4 v[232:235], v251, %[rs], %[o1] offen sc1\n" \
  "buffer_load_dwordx4 v[236:239], v251, %[rs], %[o2] offen sc1\n" \
  "s_waitcnt vmcnt(2)\n" \
  "ds_write_b128 v250, v[228:231] offset:0\n" \
  "s_waitcnt vmcnt(1)\n" \
  "ds_write_b128 v250, v[232:235] offset:4096\n" \
  "v_max3_u32 v241, v228, v229, v230\n" \
  "v_max3_u32 v241, v241, v231, v232\n" \
  "v_max3_u32 v241, v241, v233, v234\n" \
  "s_waitcnt vmcnt(0)\n" \
  "v_max3_u32 v241, v241, v235, v236\n" \
  "v_max3_u32 v241, v241, v237, v238\n" \
  "v_max_u32 v241, v241, v239\n" \
  "v_cmp_eq_u32 vcc, -1, v241\n" \
  "s_cbranch_vccnz .Lp0_%=\n" \
  ".Lp0b_%=:\n" \
  "s_waitcnt lgkmcnt(0)\n" \
  "s_barrier\n" \
  "s_memtime vcc\n" \
  "s_waitcnt lgkmcnt(0)\n" \
  "s_sub_u32 %[cnt], vcc_lo, %[ckm]\n" \
  "s_mul_hi_u32 %[cnt], %[cnt], %[inv]\n" \
  "s_add_u32 %[cnt], %[cnt], %[r16]\n" \
  "s_and_b32 %[cnt], %[cnt], -2\n" \
  "v_mov_b32 v241, %[cnt]\n" \
  "buffer_store_dword v241, v242, %[rs], %[sst] offen sc1\n" \
  "s_nop 11\n" \
  "ds_write_b128 v250, v[236:239] offset:8192\n" \
  "ds_read_b128 v[216:219], v251 offset:8192\n" \
  "ds_read_b128 v[220:223], v251 offset:9216\n" \
  "ds_read_b128 v[224:227], v251 offset:10240\n" \
  "ds_read_b128 v[228:231], v251 offset:11264\n" \
  "s_waitcnt lgkmcnt(0)\n" \
  "v_smfmac_f32_16x16x64_bf16 v[192:195], v[216:219], v[200:207], v248\n" \
  "v_smfmac_f32_16x16x64_bf16 v[196:199], v[216:219], v[208:215], v248\n" \
  "v_mov_b64 v[200:201], 0\n" \
  "v_mov_b64 v[202:203], 0\n" \
  "v_mov_b64 v[204:205], 0\n" \
  "v_mov_b64 v[206:207], 0\n" \
  "v_mov_b64 v[208:209], 0\n" \
  "v_mov_b64 v[210:211], 0\n" \
  "v_mov_b64 v[212:213], 0\n" \
  "v_mov_b64 v[214:215], 0\n" \
  "s_barrier\n" \
  "v_smfmac_f32_16x16x64_bf16 v[192:195], v[220:223], v[0:7], v248\n" \
  "v_smfmac_f32_16x16x64_bf16 v[196:199], v[220:223], v[8:15], v248\n" \
  "ds_read_b128 v[232:235], v251 offset:12288\n" \
  "ds_read_b128 v[236:239], v251 offset:13312\n" \
  "ds_read_b128 v[216:219], v251 offset:14336\n" \
  "ds_read_b128 v[220:223], v251 offset:15360\n" \
  "v_smfmac_f32_16x16x64_bf16 v[192:195], v[224:227], v[16:23], v248\n" \
  "v_smfmac_f32_16x16x64_bf16 v[196:199], v[224:227], v[24:31], v248\n" \
  "v_smfmac_f32_16x16x64_bf16 v[192:195], v[228:231], v[32:39], v248\n" \
  "v_smfmac_f32_16x16x64_bf16 v[196:199], v[228:231], v[40:47], v248\n" \
  "s_barrier\n" \
  "ds_read_b128 v[224:227], v250 offset:20480\n" \
  "ds_read_b128 v[228:231], v250 offset:24576\n" \
  "s_waitcnt lgkmcnt(5)\n" \
  "v_smfmac_f32_16x16x64_bf16 v[192:195], v[232:235], v[48:55], v248\n" \
  "v_smfmac_f32_16x16x64_bf16 v[196:199], v[232:235], v[56:63], v248\n" \
  "s_waitcnt lgkmcnt(4)\n" \
  "v_smfmac_f32_16x16x64_bf16 v[192:195], v[236:239], v[64:71], v248\n" \
  "v_smfmac_f32_16x16x64_bf16 v[196:199], v[236:239], v[72:79], v248\n" \
  "s_waitcnt lgkmcnt(3)\n" \
  "v_smfmac_f32_16x16x64_bf16 v[192:195], v[216:219], v[80:87], v248\n" \
  "v_smfmac_f32_16x16x64_bf16 v[196:199], v[216:219], v[88:95], v248\n" \
  "s_waitcnt lgkmcnt(2)\n" \
  "v_smfmac_f32_16x16x64_bf16 v[192:195], v[220:223], v[96:103], v248\n" \
  "v_smfmac_f32_16x16x64_bf16 v[196:199], v[220:223], v[104:111], v248\n" \
  "s_waitcnt lgkmcnt(1)\n" \
  "v_add_f32 v224, v224, v226\n" \
  "v_add_f32 v225, v225, v227\n" \
  "s_waitcnt lgkmcnt(0)\n" \
  "v_add_f32 v228, v228, v230\n" \
  "v_add_f32 v229, v229, v231\n" \
  "v_add_f32 v192, v192, v194\n" \
  "v_add_f32 v193, v193, v195\n" \
  "v_add_f32 v192, v192, v224\n" \
  "v_add_f32 v193, v193, v225\n" \
  "v_cvt_pk_bf16_f32 v192, v192, v193\n" \
  "v_add_f32 v196, v196, v198\n" \
  "v_add_f32 v197, v197, v199\n" \
  "v_add_f32 v196, v196, v228\n" \
  "v_add_f32 v197, v197, v229\n" \
  "v_cvt_pk_bf16_f32 v196, v196, v197\n" \
  "ds_write2_b32 v244, v192, v196 offset1:96\n" \
  "s_waitcnt lgkmcnt(0)\n" \
  "s_barrier\n" \
  "s_nop 7\n" \
  "ds_read_b64_tr_b16 v[232:233], v245 offset:17920\n" \
  "ds_read_b64_tr_b16 v[234:235], v245 offset:18112\n" \
  "ds_read_b64_tr_b16 v[236:237], v245 offset:18688\n" \
  "ds_read_b64_tr_b16 v[238:239], v245 offset:18880\n" \
  "ds_read_b64_tr_b16 v[192:193], v245 offset:16384\n" \
  "ds_read_b64_tr_b16 v[194:195], v245 offset:16576\n" \
  "ds_read_b64_tr_b16 v[196:197], v245 offset:17152\n" \
  "ds_read_b64_tr_b16 v[198:199], v245 offset:17344\n" \
  "ds_read_b128 v[216:219], v249 offset:0\n" \
  "ds_read_b128 v[220:223], v249 offset:1024\n" \
  "ds_read_b128 v[224:227], v249 offset:16384\n" \
  "ds_read_b128 v[228:231], v249 offset:17408\n" \
  "buffer_load_dwordx2 v[240:241], v240, %[rs], %[ss] offen sc1\n" \
  "s_waitcnt lgkmcnt(2)\n" \
  "v_smfmac_f32_16x16x64_bf16 v[200:203], v[232:235], v[216:223], v248 abid:1\n" \
  "ds_read_b128 v[216:219], v249 offset:32768\n" \
  "ds_read_b128 v[220:223], v249 offset:33792\n" \
  "v_smfmac_f32_16x16x64_bf16 v[204:207], v[232:235], v[128:135], v248 abid:1\n" \
  "s_waitcnt lgkmcnt(2)\n" \
  "v_smfmac_f32_16x16x64_bf16 v[200:203], v[236:239], v[224:231], v248 abid:1\n" \
  "ds_read_b128 v[224:227], v249 offset:49152\n" \
  "ds_read_b128 v[228:231], v249 offset:50176\n" \
  "v_smfmac_f32_16x16x64_bf16 v[204:207], v[236:239], v[136:143], v248 abid:1\n" \
  "v_smfmac_f32_16x16x64_bf16 v[200:203], v[192:195], v[176:183], v248 abid:1\n" \
  "v_smfmac_f32_16x16x64_bf16 v[204:207], v[192:195], v[112:119], v248 abid:1\n" \
  "v_smfmac_f32_16x16x64_bf16 v[200:203], v[196:199], v[184:191], v248 abid:1\n" \
  "v_smfmac_f32_16x16x64_bf16 v[204:207], v[196:199], v[120:127], v248 abid:1\n" \
  "s_waitcnt lgkmcnt(2)\n" \
  "v_smfmac_f32_16x16x64_bf16 v[208:211], v[192:195], v[216:223], v248 abid:1\n" \
  "ds_read_b128 v[216:219], v243 offset:0\n" \
  "ds_read_b128 v[220:223], v243 offset:1024\n" \
  "v_smfmac_f32_16x16x64_bf16 v[212:215], v[192:195], v[144:151], v248 abid:1\n" \
  "s_waitcnt lgkmcnt(2)\n" \
  "v_smfmac_f32_16x16x64_bf16 v[208:211], v[196:199], v[224:231], v248 abid:1\n" \
  "ds_read_b128 v[224:227], v243 offset:16384\n" \
  "ds_read_b128 v[228:231], v243 offset:17408\n" \
  "v_cvt_pk_bf16_f32 v200, v200, v201\n" \
  "v_smfmac_f32_16x16x64_bf16 v[212:215], v[196:199], v[152:159], v248 abid:1\n" \
  "v_cvt_pk_bf16_f32 v201, v202, v203\n" \
  "v_smfmac_f32_16x16x64_bf16 v[212:215], v[232:235], v[160:167], v248 abid:1\n" \
  "v_cvt_pk_bf16_f32 v202, v204, v205\n" \
  "v_smfmac_f32_16x16x64_bf16 v[212:215], v[236:239], v[168:175], v248 abid:1\n" \
  "v_cvt_pk_bf16_f32 v203, v206, v207\n" \
  "s_waitcnt lgkmcnt(2)\n" \
  "v_smfmac_f32_16x16x64_bf16 v[208:211], v[232:235], v[216:223], v248 abid:1\n" \
  "v_permlane16_swap_b32 v200, v202\n" \
  "s_waitcnt lgkmcnt(0)\n" \
  "v_smfmac_f32_16x16x64_bf16 v[208:211], v[236:239], v[224:231], v248 abid:1\n" \
  "v_permlane16_swap_b32 v201, v203\n" \
  "buffer_store_dwordx4 v[200:203], v247, %[rs], %[vs] offen sc1\n" \
  "v_cvt_pk_bf16_f32 v212, v212, v213\n" \
  "v_cvt_pk_bf16_f32 v213, v214, v215\n" \
  "s_nop 4\n" \
  "v_cvt_pk_bf16_f32 v211, v210, v211\n" \
  "v_cvt_pk_bf16_f32 v210, v208, v209\n" \
  "s_nop 0\n" \
  "v_permlane16_swap_b32 v211, v213\n" \
  "v_permlane16_swap_b32 v210, v212\n" \
  "buffer_store_dwordx4 v[210:213], v246, %[rs], %[vs] offen sc1\n" \
  "s_nop 1\n" \
  "s_waitcnt vmcnt(2)\n"
#define QK_A \
  "s_branch .Lgo_%=\n" \
  ".Lp0_%=:\n" \
  "s_mov_b32 %[cnt], 0x40000\n" \
  ".Lp0r_%=:\n" \
  "s_sleep 2\n" \
  "buffer_load_dwordx4 v[236:239], v251, %[rs], %[o0] offen sc1\n" \
  "s_waitcnt vmcnt(0)\n" \
  "ds_write_b128 v250, v[236:239] offset:0\n" \
  "v_max_u32 v241, v236, v237\n" \
  "v_max3_u32 v241, v241, v238, v239\n" \
  "v_cmp_eq_u32 vcc, -1, v241\n" \
  "s_cbranch_vccz .Lp0b_%=\n" \
  "s_sub_u32 %[cnt], %[cnt], 1\n" \
  "s_cbranch_scc0 .Lp0r_%=\n" \
  "s_branch .Lp0b_%=\n" \
  ".Lw1_%=:\n" \
  "v_xor_b32 v238, 0x1000, v242\n" \
  "ds_read_b128 v[216:219], v238 offset:0\n" \
  "ds_read_b128 v[228:231], v238 offset:1024\n" \
  "ds_read_b128 v[232:235], v238 offset:2048\n" \
  "ds_read_b128 v[200:203], v238 offset:3072\n" \
  "s_waitcnt lgkmcnt(0)\n" \
  "v_mfma_f32_16x16x32_bf16 v[216:219], v[216:219], v[192:195], 0\n" \
  "v_mfma_f32_16x16x32_bf16 v[216:219], v[228:231], v[196:199], v[216:219]\n" \
  "v_mfma_f32_16x16x32_bf16 v[216:219], v[232:235], v[208:211], v[216:219]\n" \
  "v_mfma_f32_16x16x32_bf16 v[216:219], v[200:203], v[212:215], v[216:219]\n" \
  "s_nop 9\n" \
  "v_cndmask_b32 v238, v216, v218, %[hf]\n" \
  "v_cndmask_b32 v239, v217, v219, %[hf]\n" \
  "v_max3_f32 v222, |v238|, |v239|, v221\n" \
  "v_cmp_nge_f32 vcc, 0x43aa0000, v222\n" \
  "s_cbranch_vccz .Lw1b_%=\n" \
  "s_mov_b64 %[wd], -1\n" \
  "v_cvt_pk_bf16_f32 v238, 0, v238\n" \
  "v_mul_f32 v238, 0x3db504f3, v238\n" \
  "v_cvt_pk_bf16_f32 v238, 0, v238\n" \
  "v_cvt_pk_bf16_f32 v239, 0, v239\n" \
  "v_mul_f32 v239, 0x3db504f3, v239\n" \
  "v_cvt_pk_bf16_f32 v239, 0, v239\n" \
  "v_max3_f32 v222, v206, v207, v238\n" \
  "v_max_f32 v222, v222, v239\n" \
  "s_nop 1\n" \
  "v_max_f32_dpp v222, v222, v222 row_ror:8 row_mask:0xf bank_mask:0xf\n" \
  "v_mov_b32 v221, v222\n" \
  "s_nop 1\n" \
  "v_permlane32_swap_b32 v222, v221\n" \
  "v_max_f32 v222, v222, v221\n" \
  "v_mov_b32 v221, v222\n" \
  "s_nop 1\n" \
  "v_permlane16_swap_b32 v222, v221\n" \
  "v_max_f32 v222, v222, v221\n" \
  "v_mul_f32 v221, 0xbfb8aa3b, v222\n" \
  "v_fmamk_f32 v236, v206, 0x3fb8aa3b, v221\n" \
  "v_fmamk_f32 v237, v207, 0x3fb8aa3b, v221\n" \
  "v_exp_f32 v236, v236\n" \
  "v_exp_f32 v237, v237\n" \
  "s_nop 1\n" \
  "v_cvt_pk_bf16_f32 v220, v236, v237\n" \
  "v_and_b32 v220, 0x7fffffff, v220\n" \
  "s_branch .Lw1b_%=\n" \
  ".Lw2_%=:\n" \
  "v_max3_f32 v204, v206, v207, v222\n" \
  "s_nop 1\n" \
  "v_max_f32_dpp v204, v204, v204 row_ror:8 row_mask:0xf bank_mask:0xf\n" \
  "v_mov_b32 v221, v204\n" \
  "s_nop 1\n" \
  "v_permlane32_swap_b32 v204, v221\n" \
  "v_max_f32 v204, v204, v221\n" \
  "v_mov_b32 v221, v204\n" \
  "s_nop 1\n" \
  "v_permlane16_swap_b32 v204, v221\n" \
  "v_max_f32 v204, v204, v221\n" \
  "v_mul_f32 v221, 0xbfb8aa3b, v204\n" \
  "v_fmamk_f32 v236, v206, 0x3fb8aa3b, v221\n" \
  "v_fmamk_f32 v237, v207, 0x3fb8aa3b, v221\n" \
  "v_fmamk_f32 v239, v222, 0x3fb8aa3b, v221\n" \
  "v_exp_f32 v236, v236\n" \
  "v_exp_f32 v237, v237\n" \
  "v_exp_f32 v239, v239\n" \
  "s_nop 1\n" \
  "v_fma_f32 v238, v223, v239, v236\n" \
  "v_add_f32 v238, v238, v237\n" \
  "s_nop 1\n" \
  "s_branch .Lw2b_%=\n" \
  ".Lgo_%=:\n" \
  "ds_read_b128 v[200:203], v249 offset:0\n" \
  "ds_read_b128 v[204:207], v249 offset:1024\n" \
  "ds_read_b128 v[208:211], v249 offset:16384\n" \
  "ds_read_b128 v[212:215], v249 offset:17408\n" \
  "v_mov_b32 v241, 0x7f800000\n" \
  "ds_write_b32 v250, v241 offset:22528\n" \
  "v_mov_b64 v[192:193], 0\n" \
  "v_mov_b64 v[194:195], 0\n" \
  "v_mov_b64 v[196:197], 0\n" \
  "v_mov_b64 v[198:199], 0\n" \
  "s_waitcnt lgkmcnt(0)\n" \
  "s_barrier\n" \
  "buffer_load_dwordx4 v[236:239], v251, %[rs], %[o0] offen sc1\n" \
  "s_waitcnt vmcnt(0)\n" \
  "ds_write_b128 v250, v[236:239] offset:0\n" \
  "v_max3_u32 v241, v236, v237, v238\n" \
  "v_max_u32 v241, v241, v239\n" \
  "v_cmp_eq_u32 vcc, -1, v241\n" \
  "s_cbranch_vccnz .Lp0_%=\n" \
  ".Lp0b_%=:\n" \
  "s_waitcnt lgkmcnt(0)\n" \
  "s_barrier\n" \
  "ds_read_b128 v[216:219], v251 offset:0\n" \
  "ds_read_b128 v[220:223], v251 offset:1024\n" \
  "ds_read_b128 v[224:227], v251 offset:2048\n" \
  "ds_read_b128 v[228:231], v251 offset:3072\n" \
  "s_waitcnt lgkmcnt(3)\n" \
  "v_smfmac_f32_16x16x64_bf16 v[192:195], v[216:219], v[200:207], v248\n" \
  "ds_read_b128 v[200:203], v249 offset:32768\n" \
  "ds_read_b128 v[204:207], v249 offset:33792\n" \
  "s_waitcnt lgkmcnt(4)\n" \
  "v_smfmac_f32_16x16x64_bf16 v[192:195], v[220:223], v[0:7], v248\n" \
  "ds_read_b128 v[232:235], v251 offset:4096\n" \
  "v_smfmac_f32_16x16x64_bf16 v[196:199], v[216:219], v[128:135], v248\n" \
  "ds_read_b128 v[216:219], v251 offset:5120\n" \
  "s_waitcnt lgkmcnt(5)\n" \
  "v_smfmac_f32_16x16x64_bf16 v[192:195], v[224:227], v[208:215], v248\n" \
  "ds_read_b128 v[208:211], v249 offset:49152\n" \
  "v_smfmac_f32_16x16x64_bf16 v[196:199], v[220:223], v[64:71], v248\n" \
  "ds_read_b128 v[212:215], v249 offset:50176\n" \
  "s_waitcnt lgkmcnt(6)\n" \
  "v_smfmac_f32_16x16x64_bf16 v[192:195], v[228:231], v[8:15], v248\n" \
  "ds_read_b128 v[220:223], v251 offset:6144\n" \
  "v_smfmac_f32_16x16x64_bf16 v[196:199], v[224:227], v[136:143], v248\n" \
  "ds_read_b128 v[224:227], v251 offset:7168\n" \
  "s_waitcnt lgkmcnt(5)\n" \
  "v_smfmac_f32_16x16x64_bf16 v[192:195], v[232:235], v[200:207], v248\n" \
  "ds_read_b128 v[200:203], v243 offset:0\n" \
  "v_smfmac_f32_16x16x64_bf16 v[196:199], v[228:231], v[72:79], v248\n" \
  "ds_read_b128 v[204:207], v243 offset:1024\n" \
  "s_waitcnt lgkmcnt(6)\n" \
  "v_smfmac_f32_16x16x64_bf16 v[192:195], v[216:219], v[16:23], v248\n" \
  "ds_read_b128 v[228:231], v251 offset:8192\n" \
  "v_smfmac_f32_16x16x64_bf16 v[196:199], v[232:235], v[144:151], v248\n" \
  "ds_read_b128 v[232:235], v251 offset:9216\n" \
  "s_waitcnt lgkmcnt(5)\n" \
  "v_smfmac_f32_16x16x64_bf16 v[192:195], v[220:223], v[208:215], v248\n" \
  "ds_read_b128 v[208:211], v243 offset:16384\n" \
  "s_barrier\n" \
  "v_smfmac_f32_16x16x64_bf16 v[196:199], v[216:219], v[80:87], v248\n" \
  "ds_read_b128 v[212:215], v243 offset:17408\n" \
  "s_waitcnt lgkmcnt(6)\n" \
  "v_smfmac_f32_16x16x64_bf16 v[192:195], v[224:227], v[24:31], v248\n" \
  "ds_read_b128 v[216:219], v251 offset:10240\n" \
  "v_smfmac_f32_16x16x64_bf16 v[196:199], v[220:223], v[152:159], v248\n" \
  "ds_read_b128 v[220:223], v251 offset:11264\n" \
  "s_waitcnt lgkmcnt(5)\n" \
  "v_smfmac_f32_16x16x64_bf16 v[192:195], v[228:231], v[200:207], v248\n" \
  "ds_read_b128 v[200:203], v243 offset:32768\n" \
  "v_smfmac_f32_16x16x64_bf16 v[196:199], v[224:227], v[88:95], v248\n" \
  "ds_read_b128 v[204:207], v243 offset:33792\n" \
  "s_waitcnt lgkmcnt(6)\n" \
  "v_smfmac_f32_16x16x64_bf16 v[192:195], v[232:235], v[32:39], v248\n" \
  "ds_read_b128 v[224:227], v251 offset:12288\n" \
  "v_smfmac_f32_16x16x64_bf16 v[196:199], v[228:231], v[160:167], v248\n" \
  "ds_read_b128 v[228:231], v251 offset:13312\n" \
  "s_waitcnt lgkmcnt(5)\n" \
  "v_smfmac_f32_16x16x64_bf16 v[192:195], v[216:219], v[208:215], v248\n" \
  "ds_read_b128 v[208:211], v243 offset:49152\n" \
  "ds_read_b128 v[212:215], v243 offset:50176\n" \
  "v_smfmac_f32_16x16x64_bf16 v[196:199], v[232:235], v[96:103], v248\n" \
  "ds_read_b128 v[236:239], v242 offset:2048\n" \
  "s_waitcnt lgkmcnt(7)\n" \
  "v_smfmac_f32_16x16x64_bf16 v[192:195], v[220:223], v[40:47], v248\n" \
  "ds_read_b128 v[232:235], v251 offset:14336\n" \
  "v_smfmac_f32_16x16x64_bf16 v[196:199], v[216:219], v[168:175], v248\n" \
  "ds_read_b128 v[216:219], v251 offset:15360\n" \
  "s_waitcnt lgkmcnt(6)\n" \
  "v_smfmac_f32_16x16x64_bf16 v[192:195], v[224:227], v[200:207], v248\n" \
  "ds_read_b128 v[200:203], v242 offset:0\n" \
  "v_smfmac_f32_16x16x64_bf16 v[196:199], v[220:223], v[104:111], v248\n" \
  "ds_read_b128 v[204:207], v242 offset:1024\n" \
  "s_waitcnt lgkmcnt(7)\n" \
  "v_smfmac_f32_16x16x64_bf16 v[192:195], v[228:231], v[48:55], v248\n" \
  "ds_read_b128 v[220:223], v242 offset:3072\n" \
  "v_smfmac_f32_16x16x64_bf16 v[196:199], v[224:227], v[176:183], v248\n" \
  "s_waitcnt lgkmcnt(4)\n" \
  "v_smfmac_f32_16x16x64_bf16 v[192:195], v[232:235], v[208:215], v248\n" \
  "v_smfmac_f32_16x16x64_bf16 v[196:199], v[228:231], v[112:119], v248\n" \
  "s_waitcnt lgkmcnt(3)\n" \
  "v_smfmac_f32_16x16x64_bf16 v[192:195], v[216:219], v[56:63], v248\n" \
  "v_smfmac_f32_16x16x64_bf16 v[196:199], v[232:235], v[184:191], v248\n" \
  "v_smfmac_f32_16x16x64_bf16 v[196:199], v[216:219], v[120:127], v248\n" \
  "s_nop 0\n" \
  "v_add_f32 v192, v192, v194\n" \
  "v_add_f32 v193, v193, v195\n" \
  "v_cvt_pk_bf16_f32 v192, v192, v193\n" \
  "s_nop 4\n" \
  "v_add_f32 v196, v196, v198\n" \
  "v_add_f32 v197, v197, v199\n" \
  "v_cvt_pk_bf16_f32 v196, v196, v197\n" \
  "ds_write2_b32 v247, v192, v196 offset1:96\n" \
  "v_xor_b32 v241, 0x400, v250\n" \
  "s_waitcnt lgkmcnt(0)\n" \
  "s_barrier\n" \
  "ds_read_b64_tr_b16 v[192:193], v246 offset:16384\n" \
  "ds_read_b64_tr_b16 v[194:195], v246 offset:16768\n" \
  "ds_read_b64_tr_b16 v[196:197], v246 offset:17152\n" \
  "ds_read_b64_tr_b16 v[198:199], v246 offset:17536\n" \
  "ds_read_b64_tr_b16 v[208:209], v246 offset:17920\n" \
  "ds_read_b64_tr_b16 v[210:211], v246 offset:18304\n" \
  "ds_read_b64_tr_b16 v[212:213], v246 offset:18688\n" \
  "ds_read_b64_tr_b16 v[214:215], v246 offset:19072\n" \
  "s_waitcnt lgkmcnt(6)\n" \
  "v_mfma_f32_16x16x32_bf16 v[200:203], v[200:203], v[192:195], 0\n" \
  "s_waitcnt lgkmcnt(4)\n" \
  "v_mfma_f32_16x16x32_bf16 v[200:203], v[204:207], v[196:199], v[200:203]\n" \
  "s_waitcnt lgkmcnt(2)\n" \
  "v_mfma_f32_16x16x32_bf16 v[200:203], v[236:239], v[208:211], v[200:203]\n" \
  "s_waitcnt lgkmcnt(0)\n" \
  "v_mfma_f32_16x16x32_bf16 v[200:203], v[220:223], v[212:215], v[200:203]\n" \
  "s_nop 8\n" \
  "v_cndmask_b32 v204, v200, v202, %[hf]\n" \
  "v_cndmask_b32 v205, v201, v203, %[hf]\n" \
  "v_max_f32 v221, |v204|, |v205|\n" \
  "ds_write_b32 v250, v221 offset:22528\n" \
  "v_cvt_pk_bf16_f32 v206, 0, v204\n" \
  "v_cvt_pk_bf16_f32 v207, 0, v205\n" \
  "v_mul_f32 v206, 0x3db504f3, v206\n" \
  "v_mul_f32 v207, 0x3db504f3, v207\n" \
  "v_cvt_pk_bf16_f32 v206, 0, v206\n" \
  "v_cvt_pk_bf16_f32 v207, 0, v207\n" \
  "ds_read_b32 v238, v241 offset:22528\n" \
  "v_mul_f32 v236, 0x3fb8aa3b, v206\n" \
  "v_mul_f32 v237, 0x3fb8aa3b, v207\n" \
  "v_exp_f32 v236, v236\n" \
  "v_exp_f32 v237, v237\n" \
  "s_nop 0\n" \
  "v_cvt_pk_bf16_f32 v220, v236, v237\n" \
  "s_mov_b64 %[wd], 0\n" \
  "s_waitcnt lgkmcnt(0)\n" \
  "v_max_f32 v238, v238, v221\n" \
  "v_cmp_nge_f32 vcc, 0x43aa0000, v238\n" \
  "s_cbranch_vccnz .Lw1_%=\n" \
  ".Lw1b_%=:\n" \
  "buffer_store_dword v220, v245, %[rs], %[c0] offen sc1\n" \
  "buffer_store_dword v220, v245, %[rs], %[c1] offen sc1\n" \
  "buffer_store_dword v220, v245, %[rs], %[c2] offen sc1\n" \
  "buffer_store_dword v220, v245, %[rs], %[c3] offen sc1\n" \
  "v_add_f32 v223, v236, v237\n" \
  "ds_write_b32 v250, v223 offset:22532\n" \
  "v_cndmask_b32 v222, 0, v222, %[wd]\n" \
  "v_mov_b32 v239, 1.0\n" \
  "s_waitcnt lgkmcnt(0)\n" \
  "s_barrier\n" \
  "ds_read_b64_tr_b16 v[216:217], v246 offset:19456\n" \
  "ds_read_b64_tr_b16 v[218:219], v246 offset:19840\n" \
  "ds_read_b64_tr_b16 v[228:229], v246 offset:20224\n" \
  "ds_read_b64_tr_b16 v[230:231], v246 offset:20608\n" \
  "ds_read_b64_tr_b16 v[232:233], v246 offset:20992\n" \
  "ds_read_b64_tr_b16 v[234:235], v246 offset:21376\n" \
  "ds_read_b64_tr_b16 v[200:201], v246 offset:21760\n" \
  "ds_read_b64_tr_b16 v[202:203], v246 offset:22144\n" \
  "ds_read_b32 v238, v241 offset:22532\n" \
  "s_waitcnt lgkmcnt(7)\n" \
  "v_mfma_f32_16x16x32_bf16 v[216:219], v[216:219], v[192:195], 0\n" \
  "s_waitcnt lgkmcnt(5)\n" \
  "v_mfma_f32_16x16x32_bf16 v[216:219], v[228:231], v[196:199], v[216:219]\n" \
  "s_waitcnt lgkmcnt(3)\n" \
  "v_mfma_f32_16x16x32_bf16 v[216:219], v[232:235], v[208:211], v[216:219]\n" \
  "s_waitcnt lgkmcnt(1)\n" \
  "v_mfma_f32_16x16x32_bf16 v[216:219], v[200:203], v[212:215], v[216:219]\n" \
  "s_waitcnt lgkmcnt(0)\n" \
  "v_add_f32 v223, v223, v238\n" \
  "v_mov_b32 v238, v223\n" \
  "s_nop 1\n" \
  "v_permlane32_swap_b32 v223, v238\n" \
  "v_add_f32 v223, v223, v238\n" \
  "s_nop 1\n" \
  "v_cndmask_b32 v204, v216, v218, %[hf]\n" \
  "v_cndmask_b32 v205, v217, v219, %[hf]\n" \
  "v_cvt_pk_bf16_f32 v206, 0, v204\n" \
  "v_cvt_pk_bf16_f32 v207, 0, v205\n" \
  "v_mul_f32 v206, 0x3db504f3, v206\n" \
  "v_mul_f32 v207, 0x3db504f3, v207\n" \
  "v_cvt_pk_bf16_f32 v206, 0, v206\n" \
  "v_cvt_pk_bf16_f32 v207, 0, v207\n" \
  "v_mul_f32 v236, 0x3fb8aa3b, v206\n" \
  "v_mul_f32 v237, 0x3fb8aa3b, v207\n" \
  "v_exp_f32 v236, v236\n" \
  "v_exp_f32 v237, v237\n" \
  "v_max_f32 v221, |v204|, |v205|\n" \
  "v_add_f32 v238, v223, v236\n" \
  "v_cmp_nge_f32 vcc, 0x43aa0000, v221\n" \
  "v_add_f32 v238, v238, v237\n" \
  "s_or_b64 vcc, vcc, %[wd]\n" \
  "s_cbranch_scc1 .Lw2_%=\n" \
  ".Lw2b_%=:\n" \
  "v_add_f32_dpp v238, v238, v238 row_ror:8 row_mask:0xf bank_mask:0xf\n" \
  "v_mov_b32 v221, v238\n" \
  "v_cndmask_b32 v236, v239, v236, %[up]\n" \
  "v_cndmask_b32 v237, v239, v237, %[up]\n" \
  "v_permlane16_swap_b32 v238, v221\n" \
  "v_add_f32 v238, v238, v221\n" \
  "v_rcp_f32 v238, v238\n" \
  "s_nop 0\n" \
  "v_mul_f32 v236, v236, v238\n" \
  "v_mul_f32 v237, v237, v238\n" \
  "v_cvt_pk_bf16_f32 v220, v236, v237\n" \
  "v_and_b32 v220, v220, v240\n" \
  "buffer_store_dword v220, v244, %[rs], %[c0] offen sc1\n" \
  "buffer_store_dword v220, v244, %[rs], %[c1] offen sc1\n"
#define QK_X \
  "s_branch .Lgo_%=\n" \
  ".Lp0_%=:\n" \
  "s_mov_b32 %[cnt], 0x40000\n" \
  ".Lp0r_%=:\n" \
  "s_sleep 2\n" \
  "buffer_load_dwordx4 v[236:239], v251, %[rs], %[o0] offen sc1\n" \
  "s_waitcnt vmcnt(0)\n" \
  "ds_write_b128 v250, v[236:239] offset:0\n" \
  "v_max_u32 v241, v236, v237\n" \
  "v_max3_u32 v241, v241, v238, v239\n" \
  "v_cmp_eq_u32 vcc, -1, v241\n" \
  "s_cbranch_vccz .Lp0b_%=\n" \
  "s_sub_u32 %[cnt], %[cnt], 1\n" \
  "s_cbranch_scc0 .Lp0r_%=\n" \
  "s_branch .Lp0b_%=\n" \
  ".Lgo_%=:\n" \
  "ds_read_b128 v[200:203], v249 offset:0\n" \
  "ds_read_b128 v[204:207], v249 offset:1024\n" \
  "ds_read_b128 v[208:211], v249 offset:16384\n" \
  "ds_read_b128 v[212:215], v249 offset:17408\n" \
  "v_mov_b64 v[192:193], 0\n" \
  "v_mov_b64 v[194:195], 0\n" \
  "v_mov_b64 v[196:197], 0\n" \
  "v_mov_b64 v[198:199], 0\n" \
  "s_waitcnt lgkmcnt(0)\n" \
  "s_barrier\n" \
  "buffer_load_dwordx4 v[236:239], v251, %[rs], %[o0] offen sc1\n" \
  "s_waitcnt vmcnt(0)\n" \
  "ds_write_b128 v250, v[236:239] offset:0\n" \
  "v_max3_u32 v241, v236, v237, v238\n" \
  "v_max_u32 v241, v241, v239\n" \
  "v_cmp_eq_u32 vcc, -1, v241\n" \
  "s_cbranch_vccnz .Lp0_%=\n" \
  ".Lp0b_%=:\n" \
  "s_waitcnt lgkmcnt(0)\n" \
  "s_barrier\n" \
  "ds_read_b128 v[216:219], v251 offset:0\n" \
  "ds_read_b128 v[220:223], v251 offset:1024\n" \
  "ds_read_b128 v[224:227], v251 offset:2048\n" \
  "ds_read_b128 v[228:231], v251 offset:3072\n" \
  "s_waitcnt lgkmcnt(3)\n" \
  "v_smfmac_f32_16x16x64_bf16 v[192:195], v[216:219], v[200:207], v248\n" \
  "ds_read_b128 v[200:203], v249 offset:32768\n" \
  "ds_read_b128 v[204:207], v249 offset:33792\n" \
  "s_waitcnt lgkmcnt(4)\n" \
  "v_smfmac_f32_16x16x64_bf16 v[192:195], v[220:223], v[0:7], v248\n" \
  "ds_read_b128 v[232:235], v251 offset:4096\n" \
  "v_smfmac_f32_16x16x64_bf16 v[196:199], v[216:219], v[128:135], v248\n" \
  "ds_read_b128 v[216:219], v251 offset:5120\n" \
  "s_waitcnt lgkmcnt(5)\n" \
  "v_smfmac_f32_16x16x64_bf16 v[192:195], v[224:227], v[208:215], v248\n" \
  "ds_read_b128 v[208:211], v249 offset:49152\n" \
  "v_smfmac_f32_16x16x64_bf16 v[196:199], v[220:223], v[64:71], v248\n" \
  "ds_read_b128 v[212:215], v249 offset:50176\n" \
  "s_waitcnt lgkmcnt(6)\n" \
  "v_smfmac_f32_16x16x64_bf16 v[192:195], v[228:231], v[8:15], v248\n" \
  "ds_read_b128 v[220:223], v251 offset:6144\n" \
  "v_smfmac_f32_16x16x64_bf16 v[196:199], v[224:227], v[136:143], v248\n" \
  "ds_read_b128 v[224:227], v251 offset:7168\n" \
  "s_waitcnt lgkmcnt(5)\n" \
  "v_smfmac_f32_16x16x64_bf16 v[192:195], v[232:235], v[200:207], v248\n" \
  "ds_read_b128 v[200:203], v243 offset:0\n" \
  "v_smfmac_f32_16x16x64_bf16 v[196:199], v[228:231], v[72:79], v248\n" \
  "ds_read_b128 v[204:207], v243 offset:1024\n" \
  "s_waitcnt lgkmcnt(6)\n" \
  "v_smfmac_f32_16x16x64_bf16 v[192:195], v[216:219], v[16:23], v248\n" \
  "ds_read_b128 v[228:231], v251 offset:8192\n" \
  "v_smfmac_f32_16x16x64_bf16 v[196:199], v[232:235], v[144:151], v248\n" \
  "ds_read_b128 v[232:235], v251 offset:9216\n" \
  "s_waitcnt lgkmcnt(5)\n" \
  "v_smfmac_f32_16x16x64_bf16 v[192:195], v[220:223], v[208:215], v248\n" \
  "ds_read_b128 v[208:211], v243 offset:16384\n" \
  "s_barrier\n" \
  "v_smfmac_f32_16x16x64_bf16 v[196:199], v[216:219], v[80:87], v248\n" \
  "ds_read_b128 v[212:215], v243 offset:17408\n" \
  "s_waitcnt lgkmcnt(6)\n" \
  "v_smfmac_f32_16x16x64_bf16 v[192:195], v[224:227], v[24:31], v248\n" \
  "ds_read_b128 v[216:219], v251 offset:10240\n" \
  "v_smfmac_f32_16x16x64_bf16 v[196:199], v[220:223], v[152:159], v248\n" \
  "ds_read_b128 v[220:223], v251 offset:11264\n" \
  "s_waitcnt lgkmcnt(5)\n" \
  "v_smfmac_f32_16x16x64_bf16 v[192:195], v[228:231], v[200:207], v248\n" \
  "ds_read_b128 v[200:203], v243 offset:32768\n" \
  "v_smfmac_f32_16x16x64_bf16 v[196:199], v[224:227], v[88:95], v248\n" \
  "ds_read_b128 v[204:207], v243 offset:33792\n" \
  "s_waitcnt lgkmcnt(6)\n" \
  "v_smfmac_f32_16x16x64_bf16 v[192:195], v[232:235], v[32:39], v248\n" \
  "ds_read_b128 v[224:227], v251 offset:12288\n" \
  "v_smfmac_f32_16x16x64_bf16 v[196:199], v[228:231], v[160:167], v248\n" \
  "ds_read_b128 v[228:231], v251 offset:13312\n" \
  "s_waitcnt lgkmcnt(5)\n" \
  "v_smfmac_f32_16x16x64_bf16 v[192:195], v[216:219], v[208:215], v248\n" \
  "ds_read_b128 v[208:211], v243 offset:49152\n" \
  "v_smfmac_f32_16x16x64_bf16 v[196:199], v[232:235], v[96:103], v248\n" \
  "ds_read_b128 v[212:215], v243 offset:50176\n" \
  "s_waitcnt lgkmcnt(6)\n" \
  "v_smfmac_f32_16x16x64_bf16 v[192:195], v[220:223], v[40:47], v248\n" \
  "ds_read_b128 v[232:235], v251 offset:14336\n" \
  "v_smfmac_f32_16x16x64_bf16 v[196:199], v[216:219], v[168:175], v248\n" \
  "ds_read_b128 v[216:219], v251 offset:15360\n" \
  "s_waitcnt lgkmcnt(5)\n" \
  "v_smfmac_f32_16x16x64_bf16 v[192:195], v[224:227], v[200:207], v248\n" \
  "v_smfmac_f32_16x16x64_bf16 v[196:199], v[220:223], v[104:111], v248\n" \
  "s_waitcnt lgkmcnt(4)\n" \
  "v_smfmac_f32_16x16x64_bf16 v[192:195], v[228:231], v[48:55], v248\n" \
  "v_smfmac_f32_16x16x64_bf16 v[196:199], v[224:227], v[176:183], v248\n" \
  "s_waitcnt lgkmcnt(1)\n" \
  "v_smfmac_f32_16x16x64_bf16 v[192:195], v[232:235], v[208:215], v248\n" \
  "v_smfmac_f32_16x16x64_bf16 v[196:199], v[228:231], v[112:119], v248\n" \
  "s_waitcnt lgkmcnt(0)\n" \
  "v_smfmac_f32_16x16x64_bf16 v[192:195], v[216:219], v[56:63], v248\n" \
  "v_smfmac_f32_16x16x64_bf16 v[196:199], v[232:235], v[184:191], v248\n" \
  "v_smfmac_f32_16x16x64_bf16 v[196:199], v[216:219], v[120:127], v248\n" \
  "s_nop 0\n" \
  "v_add_f32 v192, v192, v194\n" \
  "v_add_f32 v193, v193, v195\n" \
  "v_cvt_pk_bf16_f32 v192, v192, v193\n" \
  "s_nop 4\n" \
  "v_add_f32 v196, v196, v198\n" \
  "v_add_f32 v197, v197, v199\n" \
  "v_cvt_pk_bf16_f32 v196, v196, v197\n" \
  "ds_write2_b32 v247, v192, v196 offset1:96\n" \
  "buffer_load_dwordx4 v[200:203], v251, %[rs], %[qo] offen offset:0 sc1\n" \
  "buffer_load_dwordx4 v[204:207], v251, %[rs], %[qo] offen offset:1024 sc1\n" \
  "buffer_load_dwordx4 v[208:211], v251, %[rs], %[qo] offen offset:2048 sc1\n" \
  "buffer_load_dwordx4 v[212:215], v251, %[rs], %[qo] offen offset:3072 sc1\n" \
  "s_waitcnt lgkmcnt(0)\n" \
  "s_barrier\n" \
  "s_nop 15\n" \
  "s_nop 15\n" \
  "s_nop 15\n" \
  "s_nop 15\n" \
  "s_nop 15\n" \
  "s_nop 9\n" \
  "ds_read_b128 v[192:195], v251 offset:24576\n" \
  "ds_read_b128 v[196:199], v251 offset:28672\n" \
  "ds_read_b128 v[216:219], v251 offset:25600\n" \
  "ds_read_b128 v[220:223], v251 offset:29696\n" \
  "ds_read_b128 v[224:227], v251 offset:26624\n" \
  "ds_read_b128 v[228:231], v251 offset:30720\n" \
  "ds_read_b128 v[232:235], v251 offset:27648\n" \
  "ds_read_b128 v[236:239], v251 offset:31744\n" \
  "s_waitcnt lgkmcnt(0)\n" \
  "s_barrier\n" \
  "s_waitcnt vmcnt(3)\n" \
  "v_mfma_f32_16x16x32_bf16 v[192:195], v[192:195], v[200:203], 0\n" \
  "v_mfma_f32_16x16x32_bf16 v[196:199], v[196:199], v[200:203], 0\n" \
  "s_waitcnt vmcnt(2)\n" \
  "v_mfma_f32_16x16x32_bf16 v[192:195], v[216:219], v[204:207], v[192:195]\n" \
  "v_mfma_f32_16x16x32_bf16 v[196:199], v[220:223], v[204:207], v[196:199]\n" \
  "s_waitcnt vmcnt(1)\n" \
  "v_mfma_f32_16x16x32_bf16 v[192:195], v[224:227], v[208:211], v[192:195]\n" \
  "v_mfma_f32_16x16x32_bf16 v[196:199], v[228:231], v[208:211], v[196:199]\n" \
  "s_waitcnt vmcnt(0)\n" \
  "v_mfma_f32_16x16x32_bf16 v[192:195], v[232:235], v[212:215], v[192:195]\n" \
  "v_mfma_f32_16x16x32_bf16 v[196:199], v[236:239], v[212:215], v[196:199]\n" \
  "s_nop 8\n"
#define QK_K \
  "s_branch .Lgo_%=\n" \
  ".Lp0_%=:\n" \
  "s_mov_b32 %[cnt], 0x40000\n" \
  ".Lp0r_%=:\n" \
  "s_sleep 2\n" \
  "buffer_load_dwordx4 v[228:231], v251, %[rs], %[o0] offen sc1\n" \
  "buffer_load_dwordx4 v[232:235], v251, %[rs], %[o1] offen sc1\n" \
  "buffer_load_dwordx4 v[236:239], v251, %[rs], %[o2] offen sc1\n" \
  "s_waitcnt vmcnt(0)\n" \
  "ds_write_b128 v250, v[228:231] offset:0\n" \
  "v_max_u32 v241, v228, v229\n" \
  "v_max3_u32 v241, v241, v230, v231\n" \
  "ds_write_b128 v250, v[232:235] offset:4096\n" \
  "v_max3_u32 v241, v241, v232, v233\n" \
  "v_max3_u32 v241, v241, v234, v235\n" \
  "ds_write_b128 v250, v[236:239] offset:8192\n" \
  "v_max3_u32 v241, v241, v236, v237\n" \
  "v_max3_u32 v241, v241, v238, v239\n" \
  "v_cmp_eq_u32 vcc, -1, v241\n" \
  "s_cbranch_vccz .Lp0b_%=\n" \
  "s_sub_u32 %[cnt], %[cnt], 1\n" \
  "s_cbranch_scc0 .Lp0r_%=\n" \
  "s_branch .Lp0b_%=\n" \
  ".Lgo_%=:\n" \
  "ds_read_b128 v[200:203], v249 offset:0\n" \
  "ds_read_b128 v[204:207], v249 offset:1024\n" \
  "ds_read_b128 v[208:211], v249 offset:16384\n" \
  "ds_read_b128 v[212:215], v249 offset:17408\n" \
  "v_mov_b64 v[192:193], 0\n" \
  "v_mov_b64 v[194:195], 0\n" \
  "v_mov_b64 v[196:197], 0\n" \
  "v_mov_b64 v[198:199], 0\n" \
  "s_waitcnt lgkmcnt(0)\n" \
  "s_cmp_eq_u32 %[tw], 0\n" \
  "s_cbranch_scc1 .Ltq_%=\n" \
  ".Ltl_%=:\n" \
  "s_memtime vcc\n" \
  "s_waitcnt lgkmcnt(0)\n" \
  "s_sub_i32 %[cnt], %[tg], vcc_lo\n" \
  "s_cmp_lt_i32 %[cnt], 260\n" \
  "s_cbranch_scc1 .Ltf_%=\n" \
  "s_sleep 1\n" \
  "s_branch .Ltl_%=\n" \
  ".Ltf_%=:\n" \
  "s_max_i32 %[cnt], %[cnt], 0\n" \
  ".Ltg_%=:\n" \
  "s_sub_u32 %[cnt], %[cnt], 28\n" \
  "s_cbranch_scc0 .Ltg_%=\n" \
  ".Ltq_%=:\n" \
  "s_barrier\n" \
  "buffer_load_dwordx4 v[228:231], v251, %[rs], %[o0] offen sc1\n" \
  "buffer_load_dwordx4 v[232:235], v251, %[rs], %[o1] offen sc1\n" \
  "buffer_load_dwordx4 v[236:239], v251, %[rs], %[o2] offen sc1\n" \
  "s_waitcnt vmcnt(2)\n" \
  "ds_write_b128 v250, v[228:231] offset:0\n" \
  "s_waitcnt vmcnt(1)\n" \
  "ds_write_b128 v250, v[232:235] offset:4096\n" \
  "v_max3_u32 v241, v228, v229, v230\n" \
  "v_max3_u32 v241, v241, v231, v232\n" \
  "v_max3_u32 v241, v241, v233, v234\n" \
  "s_waitcnt vmcnt(0)\n" \
  "v_max3_u32 v241, v241, v235, v236\n" \
  "v_max3_u32 v241, v241, v237, v238\n" \
  "v_max_u32 v241, v241, v239\n" \
  "v_cmp_eq_u32 vcc, -1, v241\n" \
  "s_cbranch_vccnz .Lp0_%=\n" \
  ".Lp0b_%=:\n" \
  "s_waitcnt lgkmcnt(0)\n" \
  "s_barrier\n" \
  "s_memtime vcc\n" \
  "ds_write_b128 v250, v[236:239] offset:8192\n" \
  "s_waitcnt lgkmcnt(0)\n" \
  "s_sub_u32 %[cnt], vcc_lo, %[ckm]\n" \
  "s_mul_hi_u32 %[cnt], %[cnt], %[inv]\n" \
  "s_add_u32 %[cnt], %[cnt], %[r16]\n" \
  "s_and_b32 %[cnt], %[cnt], -2\n" \
  "v_mov_b32 v241, %[cnt]\n" \
  "buffer_store_dword v241, v245, %[rs], %[sst] offen sc1\n" \
  "s_barrier\n" \
  "ds_read_b128 v[216:219], v251 offset:0\n" \
  "ds_read_b128 v[220:223], v251 offset:1024\n" \
  "ds_read_b128 v[224:227], v251 offset:2048\n" \
  "ds_read_b128 v[228:231], v251 offset:3072\n" \
  "s_waitcnt lgkmcnt(3)\n" \
  "v_smfmac_f32_16x16x64_bf16 v[192:195], v[216:219], v[200:207], v248\n" \
  "ds_read_b128 v[200:203], v249 offset:32768\n" \
  "ds_read_b128 v[204:207], v249 offset:33792\n" \
  "s_waitcnt lgkmcnt(4)\n" \
  "v_smfmac_f32_16x16x64_bf16 v[192:195], v[220:223], v[0:7], v248\n" \
  "ds_read_b128 v[232:235], v251 offset:4096\n" \
  "v_smfmac_f32_16x16x64_bf16 v[196:199], v[216:219], v[128:135], v248\n" \
  "ds_read_b128 v[216:219], v251 offset:5120\n" \
  "s_waitcnt lgkmcnt(5)\n" \
  "v_smfmac_f32_16x16x64_bf16 v[192:195], v[224:227], v[208:215], v248\n" \
  "ds_read_b128 v[208:211], v249 offset:49152\n" \
  "v_smfmac_f32_16x16x64_bf16 v[196:199], v[220:223], v[64:71], v248\n" \
  "ds_read_b128 v[212:215], v249 offset:50176\n" \
  "s_waitcnt lgkmcnt(6)\n" \
  "v_smfmac_f32_16x16x64_bf16 v[192:195], v[228:231], v[8:15], v248\n" \
  "ds_read_b128 v[220:223], v251 offset:6144\n" \
  "v_smfmac_f32_16x16x64_bf16 v[196:199], v[224:227], v[136:143], v248\n" \
  "ds_read_b128 v[224:227], v251 offset:7168\n" \
  "s_waitcnt lgkmcnt(5)\n" \
  "v_smfmac_f32_16x16x64_bf16 v[192:195], v[232:235], v[200:207], v248\n" \
  "ds_read_b128 v[200:203], v243 offset:0\n" \
  "v_smfmac_f32_16x16x64_bf16 v[196:199], v[228:231], v[72:79], v248\n" \
  "buffer_load_dwordx2 v[240:241], v240, %[rs], %[ss] offen sc1\n" \
  "ds_read_b128 v[204:207], v243 offset:1024\n" \
  "s_waitcnt lgkmcnt(6)\n" \
  "v_smfmac_f32_16x16x64_bf16 v[192:195], v[216:219], v[16:23], v248\n" \
  "ds_read_b128 v[228:231], v251 offset:8192\n" \
  "s_barrier\n" \
  "v_smfmac_f32_16x16x64_bf16 v[196:199], v[232:235], v[144:151], v248\n" \
  "ds_read_b128 v[232:235], v251 offset:9216\n" \
  "s_waitcnt lgkmcnt(5)\n" \
  "v_smfmac_f32_16x16x64_bf16 v[192:195], v[220:223], v[208:215], v248\n" \
  "s_cmp_eq_u32 %[tc], 0\n" \
  "s_cbranch_scc1 .Lnt_%=\n" \
  "buffer_load_dword v242, v242, %[rs], %[tc] offen sc1\n" \
  ".Lnt_%=:\n" \
  "ds_read_b128 v[208:211], v243 offset:16384\n" \
  "v_smfmac_f32_16x16x64_bf16 v[196:199], v[216:219], v[80:87], v248\n" \
  "ds_read_b128 v[212:215], v243 offset:17408\n" \
  "s_waitcnt lgkmcnt(6)\n" \
  "v_smfmac_f32_16x16x64_bf16 v[192:195], v[224:227], v[24:31], v248\n" \
  "ds_read_b128 v[216:219], v251 offset:10240\n" \
  "v_smfmac_f32_16x16x64_bf16 v[196:199], v[220:223], v[152:159], v248\n" \
  "ds_read_b128 v[220:223], v251 offset:11264\n" \
  "s_waitcnt lgkmcnt(5)\n" \
  "v_smfmac_f32_16x16x64_bf16 v[192:195], v[228:231], v[200:207], v248\n" \
  "ds_read_b128 v[200:203], v243 offset:32768\n" \
  "v_smfmac_f32_16x16x64_bf16 v[196:199], v[224:227], v[88:95], v248\n" \
  "ds_read_b128 v[204:207], v243 offset:33792\n" \
  "s_waitcnt lgkmcnt(6)\n" \
  "v_smfmac_f32_16x16x64_bf16 v[192:195], v[232:235], v[32:39], v248\n" \
  "ds_read_b128 v[224:227], v251 offset:12288\n" \
  "v_smfmac_f32_16x16x64_bf16 v[196:199], v[228:231], v[160:167], v248\n" \
  "ds_read_b128 v[228:231], v251 offset:13312\n" \
  "s_waitcnt lgkmcnt(5)\n" \
  "v_smfmac_f32_16x16x64_bf16 v[192:195], v[216:219], v[208:215], v248\n" \
  "ds_read_b128 v[208:211], v243 offset:49152\n" \
  "v_smfmac_f32_16x16x64_bf16 v[196:199], v[232:235], v[96:103], v248\n" \
  "ds_read_b128 v[212:215], v243 offset:50176\n" \
  "s_waitcnt lgkmcnt(6)\n" \
  "v_smfmac_f32_16x16x64_bf16 v[192:195], v[220:223], v[40:47], v248\n" \
  "ds_read_b128 v[232:235], v251 offset:14336\n" \
  "v_smfmac_f32_16x16x64_bf16 v[196:199], v[216:219], v[168:175], v248\n" \
  "ds_read_b128 v[216:219], v251 offset:15360\n" \
  "s_waitcnt lgkmcnt(5)\n" \
  "v_smfmac_f32_16x16x64_bf16 v[192:195], v[224:227], v[200:207], v248\n" \
  "v_smfmac_f32_16x16x64_bf16 v[196:199], v[220:223], v[104:111], v248\n" \
  "s_waitcnt lgkmcnt(4)\n" \
  "v_smfmac_f32_16x16x64_bf16 v[192:195], v[228:231], v[48:55], v248\n" \
  "v_smfmac_f32_16x16x64_bf16 v[196:199], v[224:227], v[176:183], v248\n" \
  "s_waitcnt lgkmcnt(1)\n" \
  "v_smfmac_f32_16x16x64_bf16 v[192:195], v[232:235], v[208:215], v248\n" \
  "v_smfmac_f32_16x16x64_bf16 v[196:199], v[228:231], v[112:119], v248\n" \
  "s_waitcnt lgkmcnt(0)\n" \
  "v_smfmac_f32_16x16x64_bf16 v[192:195], v[216:219], v[56:63], v248\n" \
  "v_smfmac_f32_16x16x64_bf16 v[196:199], v[232:235], v[184:191], v248\n" \
  "v_smfmac_f32_16x16x64_bf16 v[196:199], v[216:219], v[120:127], v248\n" \
  "s_setprio 3\n" \
  "v_add_f32 v192, v192, v194\n" \
  "v_add_f32 v193, v193, v195\n" \
  "v_cvt_pk_bf16_f32 v192, v192, v193\n" \
  "s_nop 4\n" \
  "v_add_f32 v196, v196, v198\n" \
  "v_add_f32 v197, v197, v199\n" \
  "v_cvt_pk_bf16_f32 v196, v196, v197\n" \
  "ds_write2_b32 v247, v192, v196 offset1:96\n" \
  "s_waitcnt lgkmcnt(0)\n" \
  "s_barrier\n" \
  "s_setprio 0\n" \
  "s_waitcnt vmcnt(0)\n"
#define OW_F \
  "s_branch .Lgo_%=\n" \
  ".Lbad_%=:\n" \
  "s_mov_b32 %[st], 1\n" \
  "s_branch .Lend_%=\n" \
  ".Lgo_%=:\n" \
  "v_mov_b64 v[84:85], 0\n" \
  "v_mov_b64 v[86:87], 0\n" \
  "v_mov_b64 v[88:89], 0\n" \
  "v_mov_b64 v[90:91], 0\n" \
  "v_mov_b64 v[92:93], 0\n" \
  "v_mov_b64 v[94:95], 0\n" \
  "v_mov_b64 v[96:97], 0\n" \
  "v_mov_b64 v[98:99], 0\n" \
  "v_mov_b64 v[100:101], 0\n" \
  "v_mov_b64 v[102:103], 0\n" \
  "s_mov_b32 %[st], 0\n" \
  "s_barrier\n" \
  "ds_read_b32 v104, v119\n" \
  "s_nop 15\n" \
  "s_nop 3\n" \
  "buffer_load_dwordx4 v[0:3], v112, %[rs], %[sv1] offen offset:0 sc1\n" \
  "buffer_load_dwordx4 v[4:7], v112, %[rs], %[sv1] offen offset:1024 sc1\n" \
  "buffer_load_dwordx4 v[8:11], v112, %[rs], %[sv1] offen offset:2048 sc1\n" \
  "buffer_load_dwordx4 v[12:15], v112, %[rs], %[sv1] offen offset:3072 sc1\n" \
  "s_memtime vcc\n" \
  "s_waitcnt lgkmcnt(0)\n" \
  "v_readfirstlane_b32 %[cnt], v104\n" \
  "s_nop 0\n" \
  "s_sub_i32 %[cnt], %[cnt], vcc_lo\n" \
  "s_max_i32 %[cnt], %[cnt], 0\n" \
  "s_min_i32 %[cnt], %[cnt], 2000\n" \
  ".Ltg_%=:\n" \
  "s_sub_u32 %[cnt], %[cnt], 28\n" \
  "s_cbranch_scc0 .Ltg_%=\n" \
  "buffer_load_dwordx4 v[16:19], v112, %[rs], %[sv2] offen offset:0 sc1\n" \
  "buffer_load_dwordx4 v[20:23], v112, %[rs], %[sv2] offen offset:1024 sc1\n" \
  "buffer_load_dwordx4 v[24:27], v112, %[rs], %[sv2] offen offset:2048 sc1\n" \
  "buffer_load_dwordx4 v[28:31], v112, %[rs], %[sv2] offen offset:3072 sc1\n" \
  "s_nop 3\n" \
  "buffer_load_dwordx4 v[80:83], v113, %[rs], %[sp] offen sc1\n" \
  "buffer_load_dwordx4 v[64:67], v114, %[rs], %[sp1] offen offset:0 sc1\n" \
  "buffer_load_dwordx4 v[68:71], v114, %[rs], %[sp1] offen offset:1024 sc1\n" \
  "buffer_load_dwordx4 v[72:75], v114, %[rs], %[sp2] offen offset:0 sc1\n" \
  "buffer_load_dwordx4 v[76:79], v114, %[rs], %[sp2] offen offset:1024 sc1\n" \
  "s_barrier\n" \
  "ds_read_b128 v[32:35], v115 offset:0\n" \
  "ds_read_b128 v[36:39], v115 offset:1024\n" \
  "ds_read_b128 v[40:43], v115 offset:2048\n" \
  "ds_read_b128 v[44:47], v115 offset:3072\n" \
  "ds_read_b128 v[48:51], v115 offset:4096\n" \
  "ds_read_b128 v[52:55], v115 offset:5120\n" \
  "ds_read_b128 v[56:59], v115 offset:6144\n" \
  "ds_read_b128 v[60:63], v115 offset:7168\n" \
  "s_waitcnt vmcnt(4)\n" \
  "s_mov_b64 exec, %[m0]\n" \
  "v_mov_b64 v[84:85], v[80:81]\n" \
  "v_mov_b64 v[86:87], v[82:83]\n" \
  "s_mov_b64 exec, %[m1]\n" \
  "v_mov_b64 v[88:89], v[80:81]\n" \
  "v_mov_b64 v[90:91], v[82:83]\n" \
  "s_mov_b64 exec, %[m2]\n" \
  "v_mov_b64 v[92:93], v[80:81]\n" \
  "v_mov_b64 v[94:95], v[82:83]\n" \
  "s_mov_b64 exec, %[m3]\n" \
  "v_mov_b64 v[96:97], v[80:81]\n" \
  "v_mov_b64 v[98:99], v[82:83]\n" \
  "s_mov_b64 exec, -1\n" \
  "s_waitcnt lgkmcnt(6)\n" \
  "v_smfmac_f32_16x16x64_bf16 v[100:103], v[84:87], v[32:39], v118\n" \
  "s_waitcnt lgkmcnt(4)\n" \
  "v_smfmac_f32_16x16x64_bf16 v[100:103], v[88:91], v[40:47], v118 abid:1\n" \
  "s_waitcnt lgkmcnt(2)\n" \
  "v_smfmac_f32_16x16x64_bf16 v[100:103], v[92:95], v[48:55], v118\n" \
  "s_waitcnt lgkmcnt(0)\n" \
  "v_smfmac_f32_16x16x64_bf16 v[100:103], v[96:99], v[56:63], v118 abid:1\n" \
  "s_waitcnt vmcnt(3)\n" \
  "v_smfmac_f32_16x16x64_bf16 v[100:103], v[64:67], v[0:7], v118\n" \
  "s_waitcnt vmcnt(2)\n" \
  "v_smfmac_f32_16x16x64_bf16 v[100:103], v[68:71], v[8:15], v118\n" \
  "s_waitcnt vmcnt(1)\n" \
  "v_smfmac_f32_16x16x64_bf16 v[100:103], v[72:75], v[16:23], v118\n" \
  "s_waitcnt vmcnt(0)\n" \
  "v_smfmac_f32_16x16x64_bf16 v[100:103], v[76:79], v[24:31], v118\n" \
  "s_nop 8\n" \
  "v_add_f32 v100, v100, v102\n" \
  "v_add_f32 v101, v101, v103\n" \
  "v_cvt_pk_bf16_f32 v104, v100, v101\n" \
  "v_cmp_u_f32 vcc, v100, v101\n" \
  "s_nop 0\n" \
  "v_mov_b32_dpp v105, v104 quad_perm:[1,0,3,2] row_mask:0xf bank_mask:0xf\n" \
  "v_perm_b32 v104, v105, v104, v117\n" \
  "s_cbranch_vccnz .Lbad_%=\n" \
  "buffer_store_dword v104, v116, %[rs], %[sm0] offen sc1\n" \
  "buffer_store_dword v104, v116, %[rs], %[sm1] offen sc1\n" \
  "buffer_store_dword v104, v116, %[rs], %[sm2] offen sc1\n" \
  "buffer_store_dword v104, v116, %[rs], %[sm3] offen sc1\n" \
  ".Lend_%=:\n"
#define ST_Y \
  "s_mov_b32 %[keep], m0\n" \
  "ds_read_b128 v[240:243], v250 offset:0\n" \
  "ds_read_b128 v[244:247], v250 offset:1024\n" \
  "v_mfma_f32_16x16x32_bf16 v[192:195], v[0:3], v[208:211], 0\n" \
  "v_mfma_f32_16x16x32_bf16 v[196:199], v[64:67], v[208:211], 0\n" \
  "ds_read_b128 v[208:211], v252 offset:8192\n" \
  "v_mfma_f32_16x16x32_bf16 v[200:203], v[0:3], v[212:215], 0\n" \
  "v_mfma_f32_16x16x32_bf16 v[204:207], v[64:67], v[212:215], 0\n" \
  "ds_read_b128 v[212:215], v252 offset:9216\n" \
  "v_mfma_f32_16x16x32_bf16 v[192:195], v[128:131], v[216:219], v[192:195]\n" \
  "v_mfma_f32_16x16x32_bf16 v[196:199], v[132:135], v[216:219], v[196:199]\n" \
  "ds_read_b128 v[216:219], v252 offset:10240\n" \
  "v_mfma_f32_16x16x32_bf16 v[200:203], v[128:131], v[220:223], v[200:203]\n" \
  "v_mfma_f32_16x16x32_bf16 v[204:207], v[132:135], v[220:223], v[204:207]\n" \
  "ds_read_b128 v[220:223], v252 offset:11264\n" \
  "v_mfma_f32_16x16x32_bf16 v[192:195], v[4:7], v[224:227], v[192:195]\n" \
  "v_mfma_f32_16x16x32_bf16 v[196:199], v[68:71], v[224:227], v[196:199]\n" \
  "ds_read_b128 v[224:227], v252 offset:12288\n" \
  "v_mfma_f32_16x16x32_bf16 v[200:203], v[4:7], v[228:231], v[200:203]\n" \
  "v_mfma_f32_16x16x32_bf16 v[204:207], v[68:71], v[228:231], v[204:207]\n" \
  "ds_read_b128 v[228:231], v252 offset:13312\n" \
  "s_waitcnt lgkmcnt(7)\n" \
  "v_mfma_f32_16x16x32_bf16 v[192:195], v[240:243], v[232:235], v[192:195]\n" \
  "s_waitcnt lgkmcnt(6)\n" \
  "v_mfma_f32_16x16x32_bf16 v[196:199], v[244:247], v[232:235], v[196:199]\n" \
  "ds_read_b128 v[232:235], v252 offset:14336\n" \
  "v_mfma_f32_16x16x32_bf16 v[200:203], v[240:243], v[236:239], v[200:203]\n" \
  "v_mfma_f32_16x16x32_bf16 v[204:207], v[244:247], v[236:239], v[204:207]\n" \
  "ds_read_b128 v[236:239], v252 offset:15360\n" \
  "ds_read_b128 v[240:243], v250 offset:16384\n" \
  "ds_read_b128 v[244:247], v250 offset:17408\n" \
  "s_waitcnt lgkmcnt(9)\n" \
  "v_mfma_f32_16x16x32_bf16 v[192:195], v[8:11], v[208:211], v[192:195]\n" \
  "v_mfma_f32_16x16x32_bf16 v[196:199], v[72:75], v[208:211], v[196:199]\n" \
  "s_waitcnt lgkmcnt(8)\n" \
  "v_mfma_f32_16x16x32_bf16 v[200:203], v[8:11], v[212:215], v[200:203]\n" \
  "v_mfma_f32_16x16x32_bf16 v[204:207], v[72:75], v[212:215], v[204:207]\n" \
  "s_waitcnt vmcnt(2)\n" \
  "s_waitcnt lgkmcnt(0)\n" \
  "s_barrier\n" \
  "ds_read_b128 v[208:211], v252 offset:16384\n" \
  "ds_read_b128 v[212:215], v252 offset:17408\n" \
  "s_add_u32 m0, %[mw], 0\n" \
  "s_xor_b32 %[cnt], %[sc], 512\n" \
  "buffer_load_dwordx4 v248, %[rx], %[cnt] offen lds\n" \
  "s_add_u32 m0, %[mw], 8192\n" \
  "s_xor_b32 %[cnt], %[sc], 640\n" \
  "buffer_load_dwordx4 v248, %[rx], %[cnt] offen lds\n" \
  "v_mfma_f32_16x16x32_bf16 v[192:195], v[136:139], v[216:219], v[192:195]\n" \
  "v_mfma_f32_16x16x32_bf16 v[196:199], v[140:143], v[216:219], v[196:199]\n" \
  "ds_read_b128 v[216:219], v252 offset:18432\n" \
  "v_mfma_f32_16x16x32_bf16 v[200:203], v[136:139], v[220:223], v[200:203]\n" \
  "v_mfma_f32_16x16x32_bf16 v[204:207], v[140:143], v[220:223], v[204:207]\n" \
  "ds_read_b128 v[220:223], v252 offset:19456\n" \
  "v_mfma_f32_16x16x32_bf16 v[192:195], v[12:15], v[224:227], v[192:195]\n" \
  "v_mfma_f32_16x16x32_bf16 v[196:199], v[76:79], v[224:227], v[196:199]\n" \
  "ds_read_b128 v[224:227], v252 offset:20480\n" \
  "v_mfma_f32_16x16x32_bf16 v[200:203], v[12:15], v[228:231], v[200:203]\n" \
  "v_mfma_f32_16x16x32_bf16 v[204:207], v[76:79], v[228:231], v[204:207]\n" \
  "ds_read_b128 v[228:231], v252 offset:21504\n" \
  "v_mfma_f32_16x16x32_bf16 v[192:195], v[240:243], v[232:235], v[192:195]\n" \
  "v_mfma_f32_16x16x32_bf16 v[196:199], v[244:247], v[232:235], v[196:199]\n" \
  "ds_read_b128 v[232:235], v252 offset:22528\n" \
  "v_mfma_f32_16x16x32_bf16 v[200:203], v[240:243], v[236:239], v[200:203]\n" \
  "v_mfma_f32_16x16x32_bf16 v[204:207], v[244:247], v[236:239], v[204:207]\n" \
  "ds_read_b128 v[236:239], v252 offset:23552\n" \
  "ds_read_b128 v[240:243], v250 offset:32768\n" \
  "ds_read_b128 v[244:247], v250 offset:33792\n" \
  "s_waitcnt lgkmcnt(9)\n" \
  "v_mfma_f32_16x16x32_bf16 v[192:195], v[16:19], v[208:211], v[192:195]\n" \
  "v_mfma_f32_16x16x32_bf16 v[196:199], v[80:83], v[208:211], v[196:199]\n" \
  "ds_read_b128 v[208:211], v252 offset:24576\n" \
  "s_waitcnt lgkmcnt(9)\n" \
  "v_mfma_f32_16x16x32_bf16 v[200:203], v[16:19], v[212:215], v[200:203]\n" \
  "v_mfma_f32_16x16x32_bf16 v[204:207], v[80:83], v[212:215], v[204:207]\n" \
  "ds_read_b128 v[212:215], v252 offset:25600\n" \
  "s_waitcnt lgkmcnt(9)\n" \
  "v_mfma_f32_16x16x32_bf16 v[192:195], v[144:147], v[216:219], v[192:195]\n" \
  "v_mfma_f32_16x16x32_bf16 v[196:199], v[148:151], v[216:219], v[196:199]\n" \
  "ds_read_b128 v[216:219], v252 offset:26624\n" \
  "s_waitcnt lgkmcnt(9)\n" \
  "v_mfma_f32_16x16x32_bf16 v[200:203], v[144:147], v[220:223], v[200:203]\n" \
  "v_mfma_f32_16x16x32_bf16 v[204:207], v[148:151], v[220:223], v[204:207]\n" \
  "ds_read_b128 v[220:223], v252 offset:27648\n" \
  "s_waitcnt lgkmcnt(9)\n" \
  "v_mfma_f32_16x16x32_bf16 v[192:195], v[20:23], v[224:227], v[192:195]\n" \
  "v_mfma_f32_16x16x32_bf16 v[196:199], v[84:87], v[224:227], v[196:199]\n" \
  "ds_read_b128 v[224:227], v252 offset:28672\n" \
  "s_waitcnt lgkmcnt(9)\n" \
  "v_mfma_f32_16x16x32_bf16 v[200:203], v[20:23], v[228:231], v[200:203]\n" \
  "v_mfma_f32_16x16x32_bf16 v[204:207], v[84:87], v[228:231], v[204:207]\n" \
  "ds_read_b128 v[228:231], v252 offset:29696\n" \
  "s_waitcnt lgkmcnt(7)\n" \
  "v_mfma_f32_16x16x32_bf16 v[192:195], v[240:243], v[232:235], v[192:195]\n" \
  "s_waitcnt lgkmcnt(6)\n" \
  "v_mfma_f32_16x16x32_bf16 v[196:199], v[244:247], v[232:235], v[196:199]\n" \
  "ds_read_b128 v[232:235], v252 offset:30720\n" \
  "v_mfma_f32_16x16x32_bf16 v[200:203], v[240:243], v[236:239], v[200:203]\n" \
  "v_mfma_f32_16x16x32_bf16 v[204:207], v[244:247], v[236:239], v[204:207]\n" \
  "ds_read_b128 v[236:239], v252 offset:31744\n" \
  "ds_read_b128 v[240:243], v250 offset:49152\n" \
  "ds_read_b128 v[244:247], v250 offset:50176\n" \
  "s_waitcnt lgkmcnt(9)\n" \
  "v_mfma_f32_16x16x32_bf16 v[192:195], v[24:27], v[208:211], v[192:195]\n" \
  "v_mfma_f32_16x16x32_bf16 v[196:199], v[88:91], v[208:211], v[196:199]\n" \
  "s_waitcnt lgkmcnt(8)\n" \
  "v_mfma_f32_16x16x32_bf16 v[200:203], v[24:27], v[212:215], v[200:203]\n" \
  "v_mfma_f32_16x16x32_bf16 v[204:207], v[88:91], v[212:215], v[204:207]\n" \
  "s_waitcnt vmcnt(0)\n" \
  "s_waitcnt lgkmcnt(0)\n" \
  "s_barrier\n" \
  "ds_read_b128 v[208:211], v252 offset:0\n" \
  "ds_read_b128 v[212:215], v252 offset:1024\n" \
  "s_add_u32 m0, %[mw], 16384\n" \
  "s_xor_b32 %[cnt], %[sc], 768\n" \
  "buffer_load_dwordx4 v248, %[rx], %[cnt] offen lds\n" \
  "s_add_u32 m0, %[mw], 24576\n" \
  "s_xor_b32 %[cnt], %[sc], 896\n" \
  "buffer_load_dwordx4 v248, %[rx], %[cnt] offen lds\n" \
  "v_lshl_or_b32 v253, v251, 24, %[fm]\n" \
  "buffer_store_dword v253, v253, %[rs], %[fo] offen sc1\n" \
  "v_mfma_f32_16x16x32_bf16 v[192:195], v[152:155], v[216:219], v[192:195]\n" \
  "v_mfma_f32_16x16x32_bf16 v[196:199], v[156:159], v[216:219], v[196:199]\n" \
  "ds_read_b128 v[216:219], v252 offset:2048\n" \
  "v_mfma_f32_16x16x32_bf16 v[200:203], v[152:155], v[220:223], v[200:203]\n" \
  "v_mfma_f32_16x16x32_bf16 v[204:207], v[156:159], v[220:223], v[204:207]\n" \
  "ds_read_b128 v[220:223], v252 offset:3072\n" \
  "v_mfma_f32_16x16x32_bf16 v[192:195], v[28:31], v[224:227], v[192:195]\n" \
  "v_mfma_f32_16x16x32_bf16 v[196:199], v[92:95], v[224:227], v[196:199]\n" \
  "ds_read_b128 v[224:227], v252 offset:4096\n" \
  "v_mfma_f32_16x16x32_bf16 v[200:203], v[28:31], v[228:231], v[200:203]\n" \
  "v_mfma_f32_16x16x32_bf16 v[204:207], v[92:95], v[228:231], v[204:207]\n" \
  "ds_read_b128 v[228:231], v252 offset:5120\n" \
  "v_mfma_f32_16x16x32_bf16 v[192:195], v[240:243], v[232:235], v[192:195]\n" \
  "v_mfma_f32_16x16x32_bf16 v[196:199], v[244:247], v[232:235], v[196:199]\n" \
  "ds_read_b128 v[232:235], v252 offset:6144\n" \
  "v_mfma_f32_16x16x32_bf16 v[200:203], v[240:243], v[236:239], v[200:203]\n" \
  "v_mfma_f32_16x16x32_bf16 v[204:207], v[244:247], v[236:239], v[204:207]\n" \
  "ds_read_b128 v[236:239], v252 offset:7168\n" \
  "ds_read_b128 v[240:243], v249 offset:0\n" \
  "ds_read_b128 v[244:247], v249 offset:1024\n" \
  "s_waitcnt lgkmcnt(9)\n" \
  "v_mfma_f32_16x16x32_bf16 v[192:195], v[32:35], v[208:211], v[192:195]\n" \
  "v_mfma_f32_16x16x32_bf16 v[196:199], v[96:99], v[208:211], v[196:199]\n" \
  "ds_read_b128 v[208:211], v252 offset:8192\n" \
  "s_waitcnt lgkmcnt(9)\n" \
  "v_mfma_f32_16x16x32_bf16 v[200:203], v[32:35], v[212:215], v[200:203]\n" \
  "v_mfma_f32_16x16x32_bf16 v[204:207], v[96:99], v[212:215], v[204:207]\n" \
  "ds_read_b128 v[212:215], v252 offset:9216\n" \
  "s_waitcnt lgkmcnt(9)\n" \
  "v_mfma_f32_16x16x32_bf16 v[192:195], v[160:163], v[216:219], v[192:195]\n" \
  "v_mfma_f32_16x16x32_bf16 v[196:199], v[164:167], v[216:219], v[196:199]\n" \
  "ds_read_b128 v[216:219], v252 offset:10240\n" \
  "s_waitcnt lgkmcnt(9)\n" \
  "v_mfma_f32_16x16x32_bf16 v[200:203], v[160:163], v[220:223], v[200:203]\n" \
  "v_mfma_f32_16x16x32_bf16 v[204:207], v[164:167], v[220:223], v[204:207]\n" \
  "ds_read_b128 v[220:223], v252 offset:11264\n" \
  "s_waitcnt lgkmcnt(9)\n" \
  "v_mfma_f32_16x16x32_bf16 v[192:195], v[36:39], v[224:227], v[192:195]\n" \
  "v_mfma_f32_16x16x32_bf16 v[196:199], v[100:103], v[224:227], v[196:199]\n" \
  "ds_read_b128 v[224:227], v252 offset:12288\n" \
  "s_waitcnt lgkmcnt(9)\n" \
  "v_mfma_f32_16x16x32_bf16 v[200:203], v[36:39], v[228:231], v[200:203]\n" \
  "v_mfma_f32_16x16x32_bf16 v[204:207], v[100:103], v[228:231], v[204:207]\n" \
  "ds_read_b128 v[228:231], v252 offset:13312\n" \
  "s_waitcnt lgkmcnt(7)\n" \
  "v_mfma_f32_16x16x32_bf16 v[192:195], v[240:243], v[232:235], v[192:195]\n" \
  "s_waitcnt lgkmcnt(6)\n" \
  "v_mfma_f32_16x16x32_bf16 v[196:199], v[244:247], v[232:235], v[196:199]\n" \
  "ds_read_b128 v[232:235], v252 offset:14336\n" \
  "v_mfma_f32_16x16x32_bf16 v[200:203], v[240:243], v[236:239], v[200:203]\n" \
  "v_mfma_f32_16x16x32_bf16 v[204:207], v[244:247], v[236:239], v[204:207]\n" \
  "ds_read_b128 v[236:239], v252 offset:15360\n" \
  "ds_read_b128 v[240:243], v249 offset:16384\n" \
  "ds_read_b128 v[244:247], v249 offset:17408\n" \
  "s_waitcnt lgkmcnt(9)\n" \
  "v_mfma_f32_16x16x32_bf16 v[192:195], v[40:43], v[208:211], v[192:195]\n" \
  "v_mfma_f32_16x16x32_bf16 v[196:199], v[104:107], v[208:211], v[196:199]\n" \
  "s_waitcnt lgkmcnt(8)\n" \
  "v_mfma_f32_16x16x32_bf16 v[200:203], v[40:43], v[212:215], v[200:203]\n" \
  "v_mfma_f32_16x16x32_bf16 v[204:207], v[104:107], v[212:215], v[204:207]\n" \
  "s_waitcnt vmcnt(1)\n" \
  "s_waitcnt lgkmcnt(0)\n" \
  "s_barrier\n" \
  "ds_read_b128 v[208:211], v252 offset:16384\n" \
  "ds_read_b128 v[212:215], v252 offset:17408\n" \
  "s_add_u32 m0, %[mw], 0\n" \
  "s_xor_b32 %[cnt], %[sn], 0\n" \
  "buffer_load_dwordx4 v248, %[rx], %[cnt] offen lds\n" \
  "s_add_u32 m0, %[mw], 8192\n" \
  "s_xor_b32 %[cnt], %[sn], 128\n" \
  "buffer_load_dwordx4 v248, %[rx], %[cnt] offen lds\n" \
  "v_lshl_or_b32 v253, v251, 3, %[tm]\n" \
  "buffer_load_dword v253, v253, %[rx], %[tc] offen\n" \
  "v_mfma_f32_16x16x32_bf16 v[192:195], v[168:171], v[216:219], v[192:195]\n" \
  "v_mfma_f32_16x16x32_bf16 v[196:199], v[172:175], v[216:219], v[196:199]\n" \
  "ds_read_b128 v[216:219], v252 offset:18432\n" \
  "v_mfma_f32_16x16x32_bf16 v[200:203], v[168:171], v[220:223], v[200:203]\n" \
  "v_mfma_f32_16x16x32_bf16 v[204:207], v[172:175], v[220:223], v[204:207]\n" \
  "ds_read_b128 v[220:223], v252 offset:19456\n" \
  "v_mfma_f32_16x16x32_bf16 v[192:195], v[44:47], v[224:227], v[192:195]\n" \
  "v_mfma_f32_16x16x32_bf16 v[196:199], v[108:111], v[224:227], v[196:199]\n" \
  "ds_read_b128 v[224:227], v252 offset:20480\n" \
  "v_mfma_f32_16x16x32_bf16 v[200:203], v[44:47], v[228:231], v[200:203]\n" \
  "v_mfma_f32_16x16x32_bf16 v[204:207], v[108:111], v[228:231], v[204:207]\n" \
  "ds_read_b128 v[228:231], v252 offset:21504\n" \
  "v_mfma_f32_16x16x32_bf16 v[192:195], v[240:243], v[232:235], v[192:195]\n" \
  "v_mfma_f32_16x16x32_bf16 v[196:199], v[244:247], v[232:235], v[196:199]\n" \
  "ds_read_b128 v[232:235], v252 offset:22528\n" \
  "v_mfma_f32_16x16x32_bf16 v[200:203], v[240:243], v[236:239], v[200:203]\n" \
  "v_mfma_f32_16x16x32_bf16 v[204:207], v[244:247], v[236:239], v[204:207]\n" \
  "ds_read_b128 v[236:239], v252 offset:23552\n" \
  "ds_read_b128 v[240:243], v249 offset:32768\n" \
  "ds_read_b128 v[244:247], v249 offset:33792\n" \
  "s_waitcnt lgkmcnt(9)\n" \
  "v_mfma_f32_16x16x32_bf16 v[192:195], v[48:51], v[208:211], v[192:195]\n" \
  "v_mfma_f32_16x16x32_bf16 v[196:199], v[112:115], v[208:211], v[196:199]\n" \
  "ds_read_b128 v[208:211], v252 offset:24576\n" \
  "s_waitcnt lgkmcnt(9)\n" \
  "v_mfma_f32_16x16x32_bf16 v[200:203], v[48:51], v[212:215], v[200:203]\n" \
  "v_mfma_f32_16x16x32_bf16 v[204:207], v[112:115], v[212:215], v[204:207]\n" \
  "ds_read_b128 v[212:215], v252 offset:25600\n" \
  "s_waitcnt lgkmcnt(9)\n" \
  "v_mfma_f32_16x16x32_bf16 v[192:195], v[176:179], v[216:219], v[192:195]\n" \
  "v_mfma_f32_16x16x32_bf16 v[196:199], v[180:183], v[216:219], v[196:199]\n" \
  "ds_read_b128 v[216:219], v252 offset:26624\n" \
  "s_waitcnt lgkmcnt(9)\n" \
  "v_mfma_f32_16x16x32_bf16 v[200:203], v[176:179], v[220:223], v[200:203]\n" \
  "v_mfma_f32_16x16x32_bf16 v[204:207], v[180:183], v[220:223], v[204:207]\n" \
  "ds_read_b128 v[220:223], v252 offset:27648\n" \
  "s_waitcnt lgkmcnt(9)\n" \
  "v_mfma_f32_16x16x32_bf16 v[192:195], v[52:55], v[224:227], v[192:195]\n" \
  "v_mfma_f32_16x16x32_bf16 v[196:199], v[116:119], v[224:227], v[196:199]\n" \
  "ds_read_b128 v[224:227], v252 offset:28672\n" \
  "s_waitcnt lgkmcnt(9)\n" \
  "v_mfma_f32_16x16x32_bf16 v[200:203], v[52:55], v[228:231], v[200:203]\n" \
  "v_mfma_f32_16x16x32_bf16 v[204:207], v[116:119], v[228:231], v[204:207]\n" \
  "ds_read_b128 v[228:231], v252 offset:29696\n" \
  "s_waitcnt lgkmcnt(7)\n" \
  "v_mfma_f32_16x16x32_bf16 v[192:195], v[240:243], v[232:235], v[192:195]\n" \
  "s_waitcnt lgkmcnt(6)\n" \
  "v_mfma_f32_16x16x32_bf16 v[196:199], v[244:247], v[232:235], v[196:199]\n" \
  "ds_read_b128 v[232:235], v252 offset:30720\n" \
  "v_mfma_f32_16x16x32_bf16 v[200:203], v[240:243], v[236:239], v[200:203]\n" \
  "v_mfma_f32_16x16x32_bf16 v[204:207], v[244:247], v[236:239], v[204:207]\n" \
  "ds_read_b128 v[236:239], v252 offset:31744\n" \
  "ds_read_b128 v[240:243], v249 offset:49152\n" \
  "ds_read_b128 v[244:247], v249 offset:50176\n" \
  "s_waitcnt lgkmcnt(9)\n" \
  "v_mfma_f32_16x16x32_bf16 v[192:195], v[56:59], v[208:211], v[192:195]\n" \
  "v_mfma_f32_16x16x32_bf16 v[196:199], v[120:123], v[208:211], v[196:199]\n" \
  "s_waitcnt lgkmcnt(8)\n" \
  "v_mfma_f32_16x16x32_bf16 v[200:203], v[56:59], v[212:215], v[200:203]\n" \
  "v_mfma_f32_16x16x32_bf16 v[204:207], v[120:123], v[212:215], v[204:207]\n" \
  "s_waitcnt vmcnt(1)\n" \
  "s_waitcnt lgkmcnt(0)\n" \
  "s_barrier\n" \
  "ds_read_b128 v[208:211], v252 offset:0\n" \
  "ds_read_b128 v[212:215], v252 offset:1024\n" \
  "s_add_u32 m0, %[mw], 16384\n" \
  "s_xor_b32 %[cnt], %[sn], 256\n" \
  "buffer_load_dwordx4 v248, %[rx], %[cnt] offen lds\n" \
  "s_add_u32 m0, %[mw], 24576\n" \
  "s_xor_b32 %[cnt], %[sn], 384\n" \
  "buffer_load_dwordx4 v248, %[rx], %[cnt] offen lds\n" \
  "v_mfma_f32_16x16x32_bf16 v[192:195], v[184:187], v[216:219], v[192:195]\n" \
  "v_mfma_f32_16x16x32_bf16 v[196:199], v[188:191], v[216:219], v[196:199]\n" \
  "ds_read_b128 v[216:219], v252 offset:2048\n" \
  "v_mfma_f32_16x16x32_bf16 v[200:203], v[184:187], v[220:223], v[200:203]\n" \
  "v_mfma_f32_16x16x32_bf16 v[204:207], v[188:191], v[220:223], v[204:207]\n" \
  "ds_read_b128 v[220:223], v252 offset:3072\n" \
  "v_mfma_f32_16x16x32_bf16 v[192:195], v[60:63], v[224:227], v[192:195]\n" \
  "v_mfma_f32_16x16x32_bf16 v[196:199], v[124:127], v[224:227], v[196:199]\n" \
  "ds_read_b128 v[224:227], v252 offset:4096\n" \
  "v_mfma_f32_16x16x32_bf16 v[200:203], v[60:63], v[228:231], v[200:203]\n" \
  "v_mfma_f32_16x16x32_bf16 v[204:207], v[124:127], v[228:231], v[204:207]\n" \
  "ds_read_b128 v[228:231], v252 offset:5120\n" \
  "v_mfma_f32_16x16x32_bf16 v[192:195], v[240:243], v[232:235], v[192:195]\n" \
  "v_mfma_f32_16x16x32_bf16 v[196:199], v[244:247], v[232:235], v[196:199]\n" \
  "ds_read_b128 v[232:235], v252 offset:6144\n" \
  "v_mfma_f32_16x16x32_bf16 v[200:203], v[240:243], v[236:239], v[200:203]\n" \
  "v_mfma_f32_16x16x32_bf16 v[204:207], v[244:247], v[236:239], v[204:207]\n" \
  "ds_read_b128 v[236:239], v252 offset:7168\n" \
  "v_cvt_pk_bf16_f32 v192, v192, v193\n" \
  "v_cvt_pk_bf16_f32 v193, v194, v195\n" \
  "v_cvt_pk_bf16_f32 v194, v196, v197\n" \
  "v_cvt_pk_bf16_f32 v195, v198, v199\n" \
  "buffer_store_dwordx4 v[192:195], v251, %[rs], %[so] offen sc1\n" \
  "v_cvt_pk_bf16_f32 v200, v200, v201\n" \
  "v_cvt_pk_bf16_f32 v201, v202, v203\n" \
  "s_nop 0\n" \
  "v_cvt_pk_bf16_f32 v202, v204, v205\n" \
  "v_cvt_pk_bf16_f32 v203, v206, v207\n" \
  "s_add_u32 %[cnt], %[so], %c[rt]\n" \
  "buffer_store_dwordx4 v[200:203], v251, %[rs], %[cnt] offen sc1\n" \
  "s_mov_b32 m0, %[keep]\n" \
  "s_waitcnt lgkmcnt(0)\n"
#define ST_XA \
  "s_mov_b32 %[keep], m0\n" \
  "ds_read_b128 v[240:243], v250 offset:0\n" \
  "ds_read_b128 v[244:247], v250 offset:1024\n" \
  "v_mfma_f32_16x16x32_bf16 v[192:195], v[0:3], v[208:211], 0\n" \
  "v_mfma_f32_16x16x32_bf16 v[196:199], v[64:67], v[208:211], 0\n" \
  "ds_read_b128 v[208:211], v252 offset:8192\n" \
  "v_mfma_f32_16x16x32_bf16 v[200:203], v[0:3], v[212:215], 0\n" \
  "v_mfma_f32_16x16x32_bf16 v[204:207], v[64:67], v[212:215], 0\n" \
  "ds_read_b128 v[212:215], v252 offset:9216\n" \
  "v_mfma_f32_16x16x32_bf16 v[192:195], v[128:131], v[216:219], v[192:195]\n" \
  "v_mfma_f32_16x16x32_bf16 v[196:199], v[132:135], v[216:219], v[196:199]\n" \
  "ds_read_b128 v[216:219], v252 offset:10240\n" \
  "v_mfma_f32_16x16x32_bf16 v[200:203], v[128:131], v[220:223], v[200:203]\n" \
  "v_mfma_f32_16x16x32_bf16 v[204:207], v[132:135], v[220:223], v[204:207]\n" \
  "ds_read_b128 v[220:223], v252 offset:11264\n" \
  "v_mfma_f32_16x16x32_bf16 v[192:195], v[4:7], v[224:227], v[192:195]\n" \
  "v_mfma_f32_16x16x32_bf16 v[196:199], v[68:71], v[224:227], v[196:199]\n" \
  "v_mfma_f32_16x16x32_bf16 v[200:203], v[4:7], v[228:231], v[200:203]\n" \
  "v_mfma_f32_16x16x32_bf16 v[204:207], v[68:71], v[228:231], v[204:207]\n" \
  "s_waitcnt vmcnt(0)\n" \
  "s_waitcnt lgkmcnt(0)\n" \
  "s_barrier\n" \
  "ds_read_b128 v[224:227], v252 offset:12288\n" \
  "ds_read_b128 v[228:231], v252 offset:13312\n" \
  "s_add_u32 m0, %[mw], 0\n" \
  "s_xor_b32 %[cnt], %[sc], 384\n" \
  "buffer_load_dwordx4 v248, %[rx], %[cnt] offen lds\n" \
  "s_add_u32 m0, %[mw], 4096\n" \
  "s_xor_b32 %[cnt], %[sc], 448\n" \
  "buffer_load_dwordx4 v248, %[rx], %[cnt] offen lds\n" \
  "v_mfma_f32_16x16x32_bf16 v[192:195], v[240:243], v[232:235], v[192:195]\n" \
  "v_mfma_f32_16x16x32_bf16 v[196:199], v[244:247], v[232:235], v[196:199]\n" \
  "ds_read_b128 v[232:235], v252 offset:14336\n" \
  "v_mfma_f32_16x16x32_bf16 v[200:203], v[240:243], v[236:239], v[200:203]\n" \
  "v_mfma_f32_16x16x32_bf16 v[204:207], v[244:247], v[236:239], v[204:207]\n" \
  "ds_read_b128 v[236:239], v252 offset:15360\n" \
  "ds_read_b128 v[240:243], v250 offset:16384\n" \
  "ds_read_b128 v[244:247], v250 offset:17408\n" \
  "v_mfma_f32_16x16x32_bf16 v[192:195], v[8:11], v[208:211], v[192:195]\n" \
  "v_mfma_f32_16x16x32_bf16 v[196:199], v[72:75], v[208:211], v[196:199]\n" \
  "ds_read_b128 v[208:211], v252 offset:16384\n" \
  "v_mfma_f32_16x16x32_bf16 v[200:203], v[8:11], v[212:215], v[200:203]\n" \
  "v_mfma_f32_16x16x32_bf16 v[204:207], v[72:75], v[212:215], v[204:207]\n" \
  "ds_read_b128 v[212:215], v252 offset:17408\n" \
  "v_mfma_f32_16x16x32_bf16 v[192:195], v[136:139], v[216:219], v[192:195]\n" \
  "v_mfma_f32_16x16x32_bf16 v[196:199], v[140:143], v[216:219], v[196:199]\n" \
  "ds_read_b128 v[216:219], v252 offset:18432\n" \
  "v_mfma_f32_16x16x32_bf16 v[200:203], v[136:139], v[220:223], v[200:203]\n" \
  "v_mfma_f32_16x16x32_bf16 v[204:207], v[140:143], v[220:223], v[204:207]\n" \
  "ds_read_b128 v[220:223], v252 offset:19456\n" \
  "s_waitcnt lgkmcnt(9)\n" \
  "v_mfma_f32_16x16x32_bf16 v[192:195], v[12:15], v[224:227], v[192:195]\n" \
  "v_mfma_f32_16x16x32_bf16 v[196:199], v[76:79], v[224:227], v[196:199]\n" \
  "ds_read_b128 v[224:227], v252 offset:20480\n" \
  "s_waitcnt lgkmcnt(9)\n" \
  "v_mfma_f32_16x16x32_bf16 v[200:203], v[12:15], v[228:231], v[200:203]\n" \
  "v_mfma_f32_16x16x32_bf16 v[204:207], v[76:79], v[228:231], v[204:207]\n" \
  "ds_read_b128 v[228:231], v252 offset:21504\n" \
  "s_waitcnt lgkmcnt(7)\n" \
  "v_mfma_f32_16x16x32_bf16 v[192:195], v[240:243], v[232:235], v[192:195]\n" \
  "s_waitcnt lgkmcnt(6)\n" \
  "v_mfma_f32_16x16x32_bf16 v[196:199], v[244:247], v[232:235], v[196:199]\n" \
  "ds_read_b128 v[232:235], v252 offset:22528\n" \
  "v_mfma_f32_16x16x32_bf16 v[200:203], v[240:243], v[236:239], v[200:203]\n" \
  "v_mfma_f32_16x16x32_bf16 v[204:207], v[244:247], v[236:239], v[204:207]\n" \
  "ds_read_b128 v[236:239], v252 offset:23552\n" \
  "ds_read_b128 v[240:243], v250 offset:32768\n" \
  "ds_read_b128 v[244:247], v250 offset:33792\n" \
  "s_waitcnt lgkmcnt(9)\n" \
  "v_mfma_f32_16x16x32_bf16 v[192:195], v[16:19], v[208:211], v[192:195]\n" \
  "v_mfma_f32_16x16x32_bf16 v[196:199], v[80:83], v[208:211], v[196:199]\n" \
  "s_waitcnt lgkmcnt(8)\n" \
  "v_mfma_f32_16x16x32_bf16 v[200:203], v[16:19], v[212:215], v[200:203]\n" \
  "v_mfma_f32_16x16x32_bf16 v[204:207], v[80:83], v[212:215], v[204:207]\n" \
  "s_waitcnt vmcnt(0)\n" \
  "s_waitcnt lgkmcnt(0)\n" \
  "s_barrier\n" \
  "ds_read_b128 v[208:211], v252 offset:0\n" \
  "ds_read_b128 v[212:215], v252 offset:1024\n" \
  "s_add_u32 m0, %[mw], 12288\n" \
  "s_xor_b32 %[cnt], %[sc], 512\n" \
  "buffer_load_dwordx4 v248, %[rx], %[cnt] offen lds\n" \
  "s_add_u32 m0, %[mw], 16384\n" \
  "s_xor_b32 %[cnt], %[sc], 576\n" \
  "buffer_load_dwordx4 v248, %[rx], %[cnt] offen lds\n" \
  "s_add_u32 m0, %[mw], 20480\n" \
  "s_xor_b32 %[cnt], %[sc], 640\n" \
  "buffer_load_dwordx4 v248, %[rx], %[cnt] offen lds\n" \
  "v_mfma_f32_16x16x32_bf16 v[192:195], v[144:147], v[216:219], v[192:195]\n" \
  "v_mfma_f32_16x16x32_bf16 v[196:199], v[148:151], v[216:219], v[196:199]\n" \
  "ds_read_b128 v[216:219], v252 offset:2048\n" \
  "v_mfma_f32_16x16x32_bf16 v[200:203], v[144:147], v[220:223], v[200:203]\n" \
  "v_mfma_f32_16x16x32_bf16 v[204:207], v[148:151], v[220:223], v[204:207]\n" \
  "ds_read_b128 v[220:223], v252 offset:3072\n" \
  "v_mfma_f32_16x16x32_bf16 v[192:195], v[20:23], v[224:227], v[192:195]\n" \
  "v_mfma_f32_16x16x32_bf16 v[196:199], v[84:87], v[224:227], v[196:199]\n" \
  "ds_read_b128 v[224:227], v252 offset:4096\n" \
  "v_mfma_f32_16x16x32_bf16 v[200:203], v[20:23], v[228:231], v[200:203]\n" \
  "v_mfma_f32_16x16x32_bf16 v[204:207], v[84:87], v[228:231], v[204:207]\n" \
  "ds_read_b128 v[228:231], v252 offset:5120\n" \
  "v_mfma_f32_16x16x32_bf16 v[192:195], v[240:243], v[232:235], v[192:195]\n" \
  "v_mfma_f32_16x16x32_bf16 v[196:199], v[244:247], v[232:235], v[196:199]\n" \
  "ds_read_b128 v[232:235], v252 offset:6144\n" \
  "v_mfma_f32_16x16x32_bf16 v[200:203], v[240:243], v[236:239], v[200:203]\n" \
  "v_mfma_f32_16x16x32_bf16 v[204:207], v[244:247], v[236:239], v[204:207]\n" \
  "ds_read_b128 v[236:239], v252 offset:7168\n" \
  "ds_read_b128 v[240:243], v250 offset:49152\n" \
  "ds_read_b128 v[244:247], v250 offset:50176\n" \
  "s_waitcnt lgkmcnt(9)\n" \
  "v_mfma_f32_16x16x32_bf16 v[192:195], v[24:27], v[208:211], v[192:195]\n" \
  "v_mfma_f32_16x16x32_bf16 v[196:199], v[88:91], v[208:211], v[196:199]\n" \
  "s_waitcnt lgkmcnt(8)\n" \
  "v_mfma_f32_16x16x32_bf16 v[200:203], v[24:27], v[212:215], v[200:203]\n" \
  "v_mfma_f32_16x16x32_bf16 v[204:207], v[88:91], v[212:215], v[204:207]\n" \
  "s_waitcnt vmcnt(0)\n" \
  "s_waitcnt lgkmcnt(0)\n" \
  "s_barrier\n" \
  "ds_read_b128 v[208:211], v252 offset:12288\n" \
  "ds_read_b128 v[212:215], v252 offset:13312\n" \
  "s_add_u32 m0, %[mw], 0\n" \
  "s_xor_b32 %[cnt], %[sc], 704\n" \
  "buffer_load_dwordx4 v248, %[rx], %[cnt] offen lds\n" \
  "s_add_u32 m0, %[mw], 4096\n" \
  "s_xor_b32 %[cnt], %[sc], 768\n" \
  "buffer_load_dwordx4 v248, %[rx], %[cnt] offen lds\n" \
  "s_add_u32 m0, %[mw], 8192\n" \
  "s_xor_b32 %[cnt], %[sc], 832\n" \
  "buffer_load_dwordx4 v248, %[rx], %[cnt] offen lds\n" \
  "v_lshl_or_b32 v253, v251, 3, %[tm]\n" \
  "buffer_load_dword v253, v253, %[rx], %[tc] offen\n" \
  "v_mfma_f32_16x16x32_bf16 v[192:195], v[152:155], v[216:219], v[192:195]\n" \
  "v_mfma_f32_16x16x32_bf16 v[196:199], v[156:159], v[216:219], v[196:199]\n" \
  "ds_read_b128 v[216:219], v252 offset:14336\n" \
  "v_mfma_f32_16x16x32_bf16 v[200:203], v[152:155], v[220:223], v[200:203]\n" \
  "v_mfma_f32_16x16x32_bf16 v[204:207], v[156:159], v[220:223], v[204:207]\n" \
  "ds_read_b128 v[220:223], v252 offset:15360\n" \
  "v_mfma_f32_16x16x32_bf16 v[192:195], v[28:31], v[224:227], v[192:195]\n" \
  "v_mfma_f32_16x16x32_bf16 v[196:199], v[92:95], v[224:227], v[196:199]\n" \
  "ds_read_b128 v[224:227], v252 offset:16384\n" \
  "v_mfma_f32_16x16x32_bf16 v[200:203], v[28:31], v[228:231], v[200:203]\n" \
  "v_mfma_f32_16x16x32_bf16 v[204:207], v[92:95], v[228:231], v[204:207]\n" \
  "ds_read_b128 v[228:231], v252 offset:17408\n" \
  "v_mfma_f32_16x16x32_bf16 v[192:195], v[240:243], v[232:235], v[192:195]\n" \
  "v_mfma_f32_16x16x32_bf16 v[196:199], v[244:247], v[232:235], v[196:199]\n" \
  "ds_read_b128 v[232:235], v252 offset:18432\n" \
  "v_mfma_f32_16x16x32_bf16 v[200:203], v[240:243], v[236:239], v[200:203]\n" \
  "v_mfma_f32_16x16x32_bf16 v[204:207], v[244:247], v[236:239], v[204:207]\n" \
  "ds_read_b128 v[236:239], v252 offset:19456\n" \
  "ds_read_b128 v[240:243], v249 offset:0\n" \
  "ds_read_b128 v[244:247], v249 offset:1024\n" \
  "s_waitcnt lgkmcnt(9)\n" \
  "v_mfma_f32_16x16x32_bf16 v[192:195], v[32:35], v[208:211], v[192:195]\n" \
  "v_mfma_f32_16x16x32_bf16 v[196:199], v[96:99], v[208:211], v[196:199]\n" \
  "ds_read_b128 v[208:211], v252 offset:20480\n" \
  "s_waitcnt lgkmcnt(9)\n" \
  "v_mfma_f32_16x16x32_bf16 v[200:203], v[32:35], v[212:215], v[200:203]\n" \
  "v_mfma_f32_16x16x32_bf16 v[204:207], v[96:99], v[212:215], v[204:207]\n" \
  "ds_read_b128 v[212:215], v252 offset:21504\n" \
  "s_waitcnt lgkmcnt(9)\n" \
  "v_mfma_f32_16x16x32_bf16 v[192:195], v[160:163], v[216:219], v[192:195]\n" \
  "v_mfma_f32_16x16x32_bf16 v[196:199], v[164:167], v[216:219], v[196:199]\n" \
  "ds_read_b128 v[216:219], v252 offset:22528\n" \
  "s_waitcnt lgkmcnt(9)\n" \
  "v_mfma_f32_16x16x32_bf16 v[200:203], v[160:163], v[220:223], v[200:203]\n" \
  "v_mfma_f32_16x16x32_bf16 v[204:207], v[164:167], v[220:223], v[204:207]\n" \
  "ds_read_b128 v[220:223], v252 offset:23552\n" \
  "s_waitcnt lgkmcnt(9)\n" \
  "v_mfma_f32_16x16x32_bf16 v[192:195], v[36:39], v[224:227], v[192:195]\n" \
  "v_mfma_f32_16x16x32_bf16 v[196:199], v[100:103], v[224:227], v[196:199]\n" \
  "s_waitcnt lgkmcnt(8)\n" \
  "v_mfma_f32_16x16x32_bf16 v[200:203], v[36:39], v[228:231], v[200:203]\n" \
  "v_mfma_f32_16x16x32_bf16 v[204:207], v[100:103], v[228:231], v[204:207]\n" \
  "s_waitcnt vmcnt(1)\n" \
  "s_waitcnt lgkmcnt(0)\n" \
  "s_barrier\n" \
  "ds_read_b128 v[224:227], v252 offset:0\n" \
  "ds_read_b128 v[228:231], v252 offset:1024\n" \
  "s_add_u32 m0, %[mw], 12288\n" \
  "s_xor_b32 %[cnt], %[sc], 896\n" \
  "buffer_load_dwordx4 v248, %[rx], %[cnt] offen lds\n" \
  "s_add_u32 m0, %[mw], 16384\n" \
  "s_xor_b32 %[cnt], %[sc], 960\n" \
  "buffer_load_dwordx4 v248, %[rx], %[cnt] offen lds\n" \
  "v_mfma_f32_16x16x32_bf16 v[192:195], v[240:243], v[232:235], v[192:195]\n" \
  "v_mfma_f32_16x16x32_bf16 v[196:199], v[244:247], v[232:235], v[196:199]\n" \
  "ds_read_b128 v[232:235], v252 offset:2048\n" \
  "v_mfma_f32_16x16x32_bf16 v[200:203], v[240:243], v[236:239], v[200:203]\n" \
  "v_mfma_f32_16x16x32_bf16 v[204:207], v[244:247], v[236:239], v[204:207]\n" \
  "ds_read_b128 v[236:239], v252 offset:3072\n" \
  "ds_read_b128 v[240:243], v249 offset:16384\n" \
  "ds_read_b128 v[244:247], v249 offset:17408\n" \
  "v_mfma_f32_16x16x32_bf16 v[192:195], v[40:43], v[208:211], v[192:195]\n" \
  "v_mfma_f32_16x16x32_bf16 v[196:199], v[104:107], v[208:211], v[196:199]\n" \
  "ds_read_b128 v[208:211], v252 offset:4096\n" \
  "v_mfma_f32_16x16x32_bf16 v[200:203], v[40:43], v[212:215], v[200:203]\n" \
  "v_mfma_f32_16x16x32_bf16 v[204:207], v[104:107], v[212:215], v[204:207]\n" \
  "ds_read_b128 v[212:215], v252 offset:5120\n" \
  "v_mfma_f32_16x16x32_bf16 v[192:195], v[168:171], v[216:219], v[192:195]\n" \
  "v_mfma_f32_16x16x32_bf16 v[196:199], v[172:175], v[216:219], v[196:199]\n" \
  "ds_read_b128 v[216:219], v252 offset:6144\n" \
  "v_mfma_f32_16x16x32_bf16 v[200:203], v[168:171], v[220:223], v[200:203]\n" \
  "v_mfma_f32_16x16x32_bf16 v[204:207], v[172:175], v[220:223], v[204:207]\n" \
  "ds_read_b128 v[220:223], v252 offset:7168\n" \
  "s_waitcnt lgkmcnt(9)\n" \
  "v_mfma_f32_16x16x32_bf16 v[192:195], v[44:47], v[224:227], v[192:195]\n" \
  "v_mfma_f32_16x16x32_bf16 v[196:199], v[108:111], v[224:227], v[196:199]\n" \
  "ds_read_b128 v[224:227], v252 offset:8192\n" \
  "s_waitcnt lgkmcnt(9)\n" \
  "v_mfma_f32_16x16x32_bf16 v[200:203], v[44:47], v[228:231], v[200:203]\n" \
  "v_mfma_f32_16x16x32_bf16 v[204:207], v[108:111], v[228:231], v[204:207]\n" \
  "ds_read_b128 v[228:231], v252 offset:9216\n" \
  "s_waitcnt lgkmcnt(7)\n" \
  "v_mfma_f32_16x16x32_bf16 v[192:195], v[240:243], v[232:235], v[192:195]\n" \
  "s_waitcnt lgkmcnt(6)\n" \
  "v_mfma_f32_16x16x32_bf16 v[196:199], v[244:247], v[232:235], v[196:199]\n" \
  "ds_read_b128 v[232:235], v252 offset:10240\n" \
  "v_mfma_f32_16x16x32_bf16 v[200:203], v[240:243], v[236:239], v[200:203]\n" \
  "v_mfma_f32_16x16x32_bf16 v[204:207], v[244:247], v[236:239], v[204:207]\n" \
  "ds_read_b128 v[236:239], v252 offset:11264\n" \
  "ds_read_b128 v[240:243], v249 offset:32768\n" \
  "ds_read_b128 v[244:247], v249 offset:33792\n" \
  "s_waitcnt lgkmcnt(9)\n" \
  "v_mfma_f32_16x16x32_bf16 v[192:195], v[48:51], v[208:211], v[192:195]\n" \
  "v_mfma_f32_16x16x32_bf16 v[196:199], v[112:115], v[208:211], v[196:199]\n" \
  "s_waitcnt lgkmcnt(8)\n" \
  "v_mfma_f32_16x16x32_bf16 v[200:203], v[48:51], v[212:215], v[200:203]\n" \
  "v_mfma_f32_16x16x32_bf16 v[204:207], v[112:115], v[212:215], v[204:207]\n" \
  "s_waitcnt vmcnt(0)\n" \
  "s_waitcnt lgkmcnt(0)\n" \
  "s_barrier\n" \
  "ds_read_b128 v[208:211], v252 offset:12288\n" \
  "ds_read_b128 v[212:215], v252 offset:13312\n" \
  "s_add_u32 m0, %[mw], 0\n" \
  "s_xor_b32 %[cnt], %[sn], 0\n" \
  "buffer_load_dwordx4 v248, %[rx], %[cnt] offen lds\n" \
  "s_add_u32 m0, %[mw], 4096\n" \
  "s_xor_b32 %[cnt], %[sn], 64\n" \
  "buffer_load_dwordx4 v248, %[rx], %[cnt] offen lds\n" \
  "s_add_u32 m0, %[mw], 8192\n" \
  "s_xor_b32 %[cnt], %[sn], 128\n" \
  "buffer_load_dwordx4 v248, %[rx], %[cnt] offen lds\n" \
  "v_mfma_f32_16x16x32_bf16 v[192:195], v[176:179], v[216:219], v[192:195]\n" \
  "v_mfma_f32_16x16x32_bf16 v[196:199], v[180:183], v[216:219], v[196:199]\n" \
  "ds_read_b128 v[216:219], v252 offset:14336\n" \
  "v_mfma_f32_16x16x32_bf16 v[200:203], v[176:179], v[220:223], v[200:203]\n" \
  "v_mfma_f32_16x16x32_bf16 v[204:207], v[180:183], v[220:223], v[204:207]\n" \
  "ds_read_b128 v[220:223], v252 offset:15360\n" \
  "v_mfma_f32_16x16x32_bf16 v[192:195], v[52:55], v[224:227], v[192:195]\n" \
  "v_mfma_f32_16x16x32_bf16 v[196:199], v[116:119], v[224:227], v[196:199]\n" \
  "ds_read_b128 v[224:227], v252 offset:16384\n" \
  "v_mfma_f32_16x16x32_bf16 v[200:203], v[52:55], v[228:231], v[200:203]\n" \
  "v_mfma_f32_16x16x32_bf16 v[204:207], v[116:119], v[228:231], v[204:207]\n" \
  "ds_read_b128 v[228:231], v252 offset:17408\n" \
  "v_mfma_f32_16x16x32_bf16 v[192:195], v[240:243], v[232:235], v[192:195]\n" \
  "v_mfma_f32_16x16x32_bf16 v[196:199], v[244:247], v[232:235], v[196:199]\n" \
  "ds_read_b128 v[232:235], v252 offset:18432\n" \
  "v_mfma_f32_16x16x32_bf16 v[200:203], v[240:243], v[236:239], v[200:203]\n" \
  "v_mfma_f32_16x16x32_bf16 v[204:207], v[244:247], v[236:239], v[204:207]\n" \
  "ds_read_b128 v[236:239], v252 offset:19456\n" \
  "ds_read_b128 v[240:243], v249 offset:49152\n" \
  "ds_read_b128 v[244:247], v249 offset:50176\n" \
  "s_waitcnt lgkmcnt(9)\n" \
  "v_mfma_f32_16x16x32_bf16 v[192:195], v[56:59], v[208:211], v[192:195]\n" \
  "v_mfma_f32_16x16x32_bf16 v[196:199], v[120:123], v[208:211], v[196:199]\n" \
  "s_waitcnt lgkmcnt(8)\n" \
  "v_mfma_f32_16x16x32_bf16 v[200:203], v[56:59], v[212:215], v[200:203]\n" \
  "v_mfma_f32_16x16x32_bf16 v[204:207], v[120:123], v[212:215], v[204:207]\n" \
  "s_waitcnt vmcnt(0)\n" \
  "s_waitcnt lgkmcnt(0)\n" \
  "s_barrier\n" \
  "ds_read_b128 v[208:211], v252 offset:0\n" \
  "ds_read_b128 v[212:215], v252 offset:1024\n" \
  "s_add_u32 m0, %[mw], 12288\n" \
  "s_xor_b32 %[cnt], %[sn], 192\n" \
  "buffer_load_dwordx4 v248, %[rx], %[cnt] offen lds\n" \
  "s_add_u32 m0, %[mw], 16384\n" \
  "s_xor_b32 %[cnt], %[sn], 256\n" \
  "buffer_load_dwordx4 v248, %[rx], %[cnt] offen lds\n" \
  "s_add_u32 m0, %[mw], 20480\n" \
  "s_xor_b32 %[cnt], %[sn], 320\n" \
  "buffer_load_dwordx4 v248, %[rx], %[cnt] offen lds\n" \
  "v_mfma_f32_16x16x32_bf16 v[192:195], v[184:187], v[216:219], v[192:195]\n" \
  "v_mfma_f32_16x16x32_bf16 v[196:199], v[188:191], v[216:219], v[196:199]\n" \
  "ds_read_b128 v[216:219], v252 offset:2048\n" \
  "v_mfma_f32_16x16x32_bf16 v[200:203], v[184:187], v[220:223], v[200:203]\n" \
  "v_mfma_f32_16x16x32_bf16 v[204:207], v[188:191], v[220:223], v[204:207]\n" \
  "ds_read_b128 v[220:223], v252 offset:3072\n" \
  "v_mfma_f32_16x16x32_bf16 v[192:195], v[60:63], v[224:227], v[192:195]\n" \
  "v_mfma_f32_16x16x32_bf16 v[196:199], v[124:127], v[224:227], v[196:199]\n" \
  "ds_read_b128 v[224:227], v252 offset:4096\n" \
  "v_mfma_f32_16x16x32_bf16 v[200:203], v[60:63], v[228:231], v[200:203]\n" \
  "v_mfma_f32_16x16x32_bf16 v[204:207], v[124:127], v[228:231], v[204:207]\n" \
  "ds_read_b128 v[228:231], v252 offset:5120\n" \
  "v_mfma_f32_16x16x32_bf16 v[192:195], v[240:243], v[232:235], v[192:195]\n" \
  "v_mfma_f32_16x16x32_bf16 v[196:199], v[244:247], v[232:235], v[196:199]\n" \
  "ds_read_b128 v[232:235], v252 offset:6144\n" \
  "v_mfma_f32_16x16x32_bf16 v[200:203], v[240:243], v[236:239], v[200:203]\n" \
  "v_mfma_f32_16x16x32_bf16 v[204:207], v[244:247], v[236:239], v[204:207]\n" \
  "ds_read_b128 v[236:239], v252 offset:7168\n" \
  "v_cvt_pk_bf16_f32 v192, v192, v193\n" \
  "v_cvt_pk_bf16_f32 v193, v194, v195\n" \
  "v_cvt_pk_bf16_f32 v194, v196, v197\n" \
  "v_cvt_pk_bf16_f32 v195, v198, v199\n" \
  "v_add_u32 v196, %[so], v251\n" \
  "ds_write_b128 v196, v[192:195] offset:24576\n" \
  "v_cvt_pk_bf16_f32 v200, v200, v201\n" \
  "v_cvt_pk_bf16_f32 v201, v202, v203\n" \
  "v_cvt_pk_bf16_f32 v202, v204, v205\n" \
  "v_cvt_pk_bf16_f32 v203, v206, v207\n" \
  "ds_write_b128 v196, v[200:203] offset:28672\n" \
  "s_mov_b32 m0, %[keep]\n" \
  "s_waitcnt lgkmcnt(0)\n"
#define ST_XB \
  "s_barrier\n" \
  "ds_read_b128 v[192:195], v251 offset:24576\n" \
  "ds_read_b128 v[208:211], v251 offset:28672\n" \
  "ds_read_b128 v[196:199], v251 offset:25600\n" \
  "ds_read_b128 v[212:215], v251 offset:29696\n" \
  "ds_read_b128 v[200:203], v251 offset:26624\n" \
  "ds_read_b128 v[216:219], v251 offset:30720\n" \
  "ds_read_b128 v[204:207], v251 offset:27648\n" \
  "ds_read_b128 v[220:223], v251 offset:31744\n" \
  "ds_read_b128 v[240:243], v250 offset:0\n" \
  "ds_read_b128 v[244:247], v250 offset:16384\n" \
  "s_waitcnt lgkmcnt(9)\n" \
  "v_mfma_f32_16x16x32_bf16 v[224:227], v[192:195], v[0:3], 0\n" \
  "s_waitcnt lgkmcnt(8)\n" \
  "v_mfma_f32_16x16x32_bf16 v[228:231], v[208:211], v[0:3], 0\n" \
  "s_waitcnt lgkmcnt(7)\n" \
  "v_mfma_f32_16x16x32_bf16 v[224:227], v[196:199], v[4:7], v[224:227]\n" \
  "s_waitcnt lgkmcnt(6)\n" \
  "v_mfma_f32_16x16x32_bf16 v[228:231], v[212:215], v[4:7], v[228:231]\n" \
  "s_waitcnt lgkmcnt(5)\n" \
  "v_mfma_f32_16x16x32_bf16 v[224:227], v[200:203], v[8:11], v[224:227]\n" \
  "s_waitcnt lgkmcnt(4)\n" \
  "v_mfma_f32_16x16x32_bf16 v[228:231], v[216:219], v[8:11], v[228:231]\n" \
  "s_waitcnt lgkmcnt(3)\n" \
  "v_mfma_f32_16x16x32_bf16 v[224:227], v[204:207], v[12:15], v[224:227]\n" \
  "s_waitcnt lgkmcnt(2)\n" \
  "v_mfma_f32_16x16x32_bf16 v[228:231], v[220:223], v[12:15], v[228:231]\n" \
  "v_mfma_f32_16x16x32_bf16 v[232:235], v[192:195], v[128:131], 0\n" \
  "v_mfma_f32_16x16x32_bf16 v[236:239], v[208:211], v[128:131], 0\n" \
  "v_mfma_f32_16x16x32_bf16 v[232:235], v[200:203], v[136:139], v[232:235]\n" \
  "v_cvt_pk_bf16_f32 v224, v224, v225\n" \
  "v_cvt_pk_bf16_f32 v226, v226, v227\n" \
  "v_mfma_f32_16x16x32_bf16 v[236:239], v[216:219], v[136:139], v[236:239]\n" \
  "v_cvt_pk_bf16_f32 v225, v228, v229\n" \
  "v_cvt_pk_bf16_f32 v227, v230, v231\n" \
  "s_waitcnt lgkmcnt(1)\n" \
  "v_mfma_f32_16x16x32_bf16 v[232:235], v[196:199], v[240:243], v[232:235]\n" \
  "s_add_u32 %[cnt], %[so], 0*%c[js]+0*%c[ss]\n" \
  "buffer_store_dwordx4 v[224:227], v251, %[rs], %[cnt] offen sc1\n" \
  "v_mfma_f32_16x16x32_bf16 v[236:239], v[212:215], v[240:243], v[236:239]\n" \
  "s_waitcnt lgkmcnt(0)\n" \
  "v_mfma_f32_16x16x32_bf16 v[232:235], v[204:207], v[244:247], v[232:235]\n" \
  "v_mfma_f32_16x16x32_bf16 v[236:239], v[220:223], v[244:247], v[236:239]\n" \
  "ds_read_b128 v[240:243], v250 offset:1024\n" \
  "ds_read_b128 v[244:247], v250 offset:17408\n" \
  "v_mfma_f32_16x16x32_bf16 v[224:227], v[192:195], v[64:67], 0\n" \
  "v_mfma_f32_16x16x32_bf16 v[228:231], v[208:211], v[64:67], 0\n" \
  "v_mfma_f32_16x16x32_bf16 v[224:227], v[196:199], v[68:71], v[224:227]\n" \
  "v_cvt_pk_bf16_f32 v232, v232, v233\n" \
  "v_cvt_pk_bf16_f32 v234, v234, v235\n" \
  "v_mfma_f32_16x16x32_bf16 v[228:231], v[212:215], v[68:71], v[228:231]\n" \
  "v_cvt_pk_bf16_f32 v233, v236, v237\n" \
  "v_cvt_pk_bf16_f32 v235, v238, v239\n" \
  "v_mfma_f32_16x16x32_bf16 v[224:227], v[200:203], v[72:75], v[224:227]\n" \
  "s_add_u32 %[cnt], %[so], 4*%c[js]+0*%c[ss]\n" \
  "buffer_store_dwordx4 v[232:235], v251, %[rs], %[cnt] offen sc1\n" \
  "v_mfma_f32_16x16x32_bf16 v[228:231], v[216:219], v[72:75], v[228:231]\n" \
  "v_mfma_f32_16x16x32_bf16 v[224:227], v[204:207], v[76:79], v[224:227]\n" \
  "v_mfma_f32_16x16x32_bf16 v[228:231], v[220:223], v[76:79], v[228:231]\n" \
  "v_lshlrev_b32 v253, 24, v251\n" \
  "s_waitcnt vmcnt(2)\n" \
  "buffer_store_byte v253, v253, %[rs], %[fo] offen sc1\n" \
  "s_barrier\n" \
  "v_mfma_f32_16x16x32_bf16 v[232:235], v[192:195], v[132:135], 0\n" \
  "v_mfma_f32_16x16x32_bf16 v[236:239], v[208:211], v[132:135], 0\n" \
  "v_mfma_f32_16x16x32_bf16 v[232:235], v[200:203], v[140:143], v[232:235]\n" \
  "v_cvt_pk_bf16_f32 v224, v224, v225\n" \
  "v_cvt_pk_bf16_f32 v226, v226, v227\n" \
  "v_mfma_f32_16x16x32_bf16 v[236:239], v[216:219], v[140:143], v[236:239]\n" \
  "v_cvt_pk_bf16_f32 v225, v228, v229\n" \
  "v_cvt_pk_bf16_f32 v227, v230, v231\n" \
  "s_waitcnt lgkmcnt(1)\n" \
  "v_mfma_f32_16x16x32_bf16 v[232:235], v[196:199], v[240:243], v[232:235]\n" \
  "s_add_u32 %[cnt], %[so], 0*%c[js]+1*%c[ss]\n" \
  "buffer_store_dwordx4 v[224:227], v251, %[rs], %[cnt] offen sc1\n" \
  "v_mfma_f32_16x16x32_bf16 v[236:239], v[212:215], v[240:243], v[236:239]\n" \
  "s_waitcnt lgkmcnt(0)\n" \
  "v_mfma_f32_16x16x32_bf16 v[232:235], v[204:207], v[244:247], v[232:235]\n" \
  "v_mfma_f32_16x16x32_bf16 v[236:239], v[220:223], v[244:247], v[236:239]\n" \
  "ds_read_b128 v[240:243], v250 offset:32768\n" \
  "ds_read_b128 v[244:247], v250 offset:49152\n" \
  "v_mfma_f32_16x16x32_bf16 v[224:227], v[192:195], v[16:19], 0\n" \
  "v_mfma_f32_16x16x32_bf16 v[228:231], v[208:211], v[16:19], 0\n" \
  "v_mfma_f32_16x16x32_bf16 v[224:227], v[196:199], v[20:23], v[224:227]\n" \
  "v_cvt_pk_bf16_f32 v232, v232, v233\n" \
  "v_cvt_pk_bf16_f32 v234, v234, v235\n" \
  "v_mfma_f32_16x16x32_bf16 v[228:231], v[212:215], v[20:23], v[228:231]\n" \
  "v_cvt_pk_bf16_f32 v233, v236, v237\n" \
  "v_cvt_pk_bf16_f32 v235, v238, v239\n" \
  "v_mfma_f32_16x16x32_bf16 v[224:227], v[200:203], v[24:27], v[224:227]\n" \
  "s_add_u32 %[cnt], %[so], 4*%c[js]+1*%c[ss]\n" \
  "buffer_store_dwordx4 v[232:235], v251, %[rs], %[cnt] offen sc1\n" \
  "v_mfma_f32_16x16x32_bf16 v[228:231], v[216:219], v[24:27], v[228:231]\n" \
  "v_mfma_f32_16x16x32_bf16 v[224:227], v[204:207], v[28:31], v[224:227]\n" \
  "v_mfma_f32_16x16x32_bf16 v[228:231], v[220:223], v[28:31], v[228:231]\n" \
  "s_barrier\n" \
  "v_mfma_f32_16x16x32_bf16 v[232:235], v[192:195], v[144:147], 0\n" \
  "v_mfma_f32_16x16x32_bf16 v[236:239], v[208:211], v[144:147], 0\n" \
  "v_mfma_f32_16x16x32_bf16 v[232:235], v[200:203], v[152:155], v[232:235]\n" \
  "v_cvt_pk_bf16_f32 v224, v224, v225\n" \
  "v_cvt_pk_bf16_f32 v226, v226, v227\n" \
  "v_mfma_f32_16x16x32_bf16 v[236:239], v[216:219], v[152:155], v[236:239]\n" \
  "v_cvt_pk_bf16_f32 v225, v228, v229\n" \
  "v_cvt_pk_bf16_f32 v227, v230, v231\n" \
  "s_waitcnt lgkmcnt(1)\n" \
  "v_mfma_f32_16x16x32_bf16 v[232:235], v[196:199], v[240:243], v[232:235]\n" \
  "s_add_u32 %[cnt], %[so], 1*%c[js]+0*%c[ss]\n" \
  "buffer_store_dwordx4 v[224:227], v251, %[rs], %[cnt] offen sc1\n" \
  "v_mfma_f32_16x16x32_bf16 v[236:239], v[212:215], v[240:243], v[236:239]\n" \
  "s_waitcnt lgkmcnt(0)\n" \
  "v_mfma_f32_16x16x32_bf16 v[232:235], v[204:207], v[244:247], v[232:235]\n" \
  "v_mfma_f32_16x16x32_bf16 v[236:239], v[220:223], v[244:247], v[236:239]\n" \
  "ds_read_b128 v[240:243], v250 offset:33792\n" \
  "ds_read_b128 v[244:247], v250 offset:50176\n" \
  "v_mfma_f32_16x16x32_bf16 v[224:227], v[192:195], v[80:83], 0\n" \
  "v_mfma_f32_16x16x32_bf16 v[228:231], v[208:211], v[80:83], 0\n" \
  "v_mfma_f32_16x16x32_bf16 v[224:227], v[196:199], v[84:87], v[224:227]\n" \
  "v_cvt_pk_bf16_f32 v232, v232, v233\n" \
  "v_cvt_pk_bf16_f32 v234, v234, v235\n" \
  "v_mfma_f32_16x16x32_bf16 v[228:231], v[212:215], v[84:87], v[228:231]\n" \
  "v_cvt_pk_bf16_f32 v233, v236, v237\n" \
  "v_cvt_pk_bf16_f32 v235, v238, v239\n" \
  "v_mfma_f32_16x16x32_bf16 v[224:227], v[200:203], v[88:91], v[224:227]\n" \
  "s_add_u32 %[cnt], %[so], 5*%c[js]+0*%c[ss]\n" \
  "buffer_store_dwordx4 v[232:235], v251, %[rs], %[cnt] offen sc1\n" \
  "v_mfma_f32_16x16x32_bf16 v[228:231], v[216:219], v[88:91], v[228:231]\n" \
  "v_mfma_f32_16x16x32_bf16 v[224:227], v[204:207], v[92:95], v[224:227]\n" \
  "v_mfma_f32_16x16x32_bf16 v[228:231], v[220:223], v[92:95], v[228:231]\n" \
  "v_mfma_f32_16x16x32_bf16 v[232:235], v[192:195], v[148:151], 0\n" \
  "v_mfma_f32_16x16x32_bf16 v[236:239], v[208:211], v[148:151], 0\n" \
  "v_mfma_f32_16x16x32_bf16 v[232:235], v[200:203], v[156:159], v[232:235]\n" \
  "v_cvt_pk_bf16_f32 v224, v224, v225\n" \
  "v_cvt_pk_bf16_f32 v226, v226, v227\n" \
  "v_mfma_f32_16x16x32_bf16 v[236:239], v[216:219], v[156:159], v[236:239]\n" \
  "v_cvt_pk_bf16_f32 v225, v228, v229\n" \
  "v_cvt_pk_bf16_f32 v227, v230, v231\n" \
  "s_waitcnt lgkmcnt(1)\n" \
  "v_mfma_f32_16x16x32_bf16 v[232:235], v[196:199], v[240:243], v[232:235]\n" \
  "s_add_u32 %[cnt], %[so], 1*%c[js]+1*%c[ss]\n" \
  "buffer_store_dwordx4 v[224:227], v251, %[rs], %[cnt] offen sc1\n" \
  "v_mfma_f32_16x16x32_bf16 v[236:239], v[212:215], v[240:243], v[236:239]\n" \
  "s_waitcnt lgkmcnt(0)\n" \
  "v_mfma_f32_16x16x32_bf16 v[232:235], v[204:207], v[244:247], v[232:235]\n" \
  "v_mfma_f32_16x16x32_bf16 v[236:239], v[220:223], v[244:247], v[236:239]\n" \
  "ds_read_b128 v[240:243], v249 offset:0\n" \
  "ds_read_b128 v[244:247], v249 offset:16384\n" \
  "s_barrier\n" \
  "v_mfma_f32_16x16x32_bf16 v[224:227], v[192:195], v[32:35], 0\n" \
  "v_mfma_f32_16x16x32_bf16 v[228:231], v[208:211], v[32:35], 0\n" \
  "v_mfma_f32_16x16x32_bf16 v[224:227], v[196:199], v[36:39], v[224:227]\n" \
  "v_cvt_pk_bf16_f32 v232, v232, v233\n" \
  "v_cvt_pk_bf16_f32 v234, v234, v235\n" \
  "v_mfma_f32_16x16x32_bf16 v[228:231], v[212:215], v[36:39], v[228:231]\n" \
  "v_cvt_pk_bf16_f32 v233, v236, v237\n" \
  "v_cvt_pk_bf16_f32 v235, v238, v239\n" \
  "v_mfma_f32_16x16x32_bf16 v[224:227], v[200:203], v[40:43], v[224:227]\n" \
  "s_add_u32 %[cnt], %[so], 5*%c[js]+1*%c[ss]\n" \
  "buffer_store_dwordx4 v[232:235], v251, %[rs], %[cnt] offen sc1\n" \
  "v_mfma_f32_16x16x32_bf16 v[228:231], v[216:219], v[40:43], v[228:231]\n" \
  "v_mfma_f32_16x16x32_bf16 v[224:227], v[204:207], v[44:47], v[224:227]\n" \
  "v_mfma_f32_16x16x32_bf16 v[228:231], v[220:223], v[44:47], v[228:231]\n" \
  "v_mfma_f32_16x16x32_bf16 v[232:235], v[192:195], v[160:163], 0\n" \
  "v_mfma_f32_16x16x32_bf16 v[236:239], v[208:211], v[160:163], 0\n" \
  "v_mfma_f32_16x16x32_bf16 v[232:235], v[200:203], v[168:171], v[232:235]\n" \
  "v_cvt_pk_bf16_f32 v224, v224, v225\n" \
  "v_cvt_pk_bf16_f32 v226, v226, v227\n" \
  "v_mfma_f32_16x16x32_bf16 v[236:239], v[216:219], v[168:171], v[236:239]\n" \
  "v_cvt_pk_bf16_f32 v225, v228, v229\n" \
  "v_cvt_pk_bf16_f32 v227, v230, v231\n" \
  "s_waitcnt lgkmcnt(1)\n" \
  "v_mfma_f32_16x16x32_bf16 v[232:235], v[196:199], v[240:243], v[232:235]\n" \
  "s_add_u32 %[cnt], %[so], 2*%c[js]+0*%c[ss]\n" \
  "buffer_store_dwordx4 v[224:227], v251, %[rs], %[cnt] offen sc1\n" \
  "v_mfma_f32_16x16x32_bf16 v[236:239], v[212:215], v[240:243], v[236:239]\n" \
  "s_waitcnt lgkmcnt(0)\n" \
  "v_mfma_f32_16x16x32_bf16 v[232:235], v[204:207], v[244:247], v[232:235]\n" \
  "v_mfma_f32_16x16x32_bf16 v[236:239], v[220:223], v[244:247], v[236:239]\n" \
  "ds_read_b128 v[240:243], v249 offset:1024\n" \
  "ds_read_b128 v[244:247], v249 offset:17408\n" \
  "v_mfma_f32_16x16x32_bf16 v[224:227], v[192:195], v[96:99], 0\n" \
  "v_mfma_f32_16x16x32_bf16 v[228:231], v[208:211], v[96:99], 0\n" \
  "v_mfma_f32_16x16x32_bf16 v[224:227], v[196:199], v[100:103], v[224:227]\n" \
  "v_cvt_pk_bf16_f32 v232, v232, v233\n" \
  "v_cvt_pk_bf16_f32 v234, v234, v235\n" \
  "v_mfma_f32_16x16x32_bf16 v[228:231], v[212:215], v[100:103], v[228:231]\n" \
  "v_cvt_pk_bf16_f32 v233, v236, v237\n" \
  "v_cvt_pk_bf16_f32 v235, v238, v239\n" \
  "v_mfma_f32_16x16x32_bf16 v[224:227], v[200:203], v[104:107], v[224:227]\n" \
  "s_add_u32 %[cnt], %[so], 6*%c[js]+0*%c[ss]\n" \
  "buffer_store_dwordx4 v[232:235], v251, %[rs], %[cnt] offen sc1\n" \
  "v_mfma_f32_16x16x32_bf16 v[228:231], v[216:219], v[104:107], v[228:231]\n" \
  "v_mfma_f32_16x16x32_bf16 v[224:227], v[204:207], v[108:111], v[224:227]\n" \
  "v_mfma_f32_16x16x32_bf16 v[228:231], v[220:223], v[108:111], v[228:231]\n" \
  "s_barrier\n" \
  "v_mfma_f32_16x16x32_bf16 v[232:235], v[192:195], v[164:167], 0\n" \
  "v_mfma_f32_16x16x32_bf16 v[236:239], v[208:211], v[164:167], 0\n" \
  "v_mfma_f32_16x16x32_bf16 v[232:235], v[200:203], v[172:175], v[232:235]\n" \
  "v_cvt_pk_bf16_f32 v224, v224, v225\n" \
  "v_cvt_pk_bf16_f32 v226, v226, v227\n" \
  "v_mfma_f32_16x16x32_bf16 v[236:239], v[216:219], v[172:175], v[236:239]\n" \
  "v_cvt_pk_bf16_f32 v225, v228, v229\n" \
  "v_cvt_pk_bf16_f32 v227, v230, v231\n" \
  "s_waitcnt lgkmcnt(1)\n" \
  "v_mfma_f32_16x16x32_bf16 v[232:235], v[196:199], v[240:243], v[232:235]\n" \
  "s_add_u32 %[cnt], %[so], 2*%c[js]+1*%c[ss]\n" \
  "buffer_store_dwordx4 v[224:227], v251, %[rs], %[cnt] offen sc1\n" \
  "v_mfma_f32_16x16x32_bf16 v[236:239], v[212:215], v[240:243], v[236:239]\n" \
  "s_waitcnt lgkmcnt(0)\n" \
  "v_mfma_f32_16x16x32_bf16 v[232:235], v[204:207], v[244:247], v[232:235]\n" \
  "v_mfma_f32_16x16x32_bf16 v[236:239], v[220:223], v[244:247], v[236:239]\n" \
  "ds_read_b128 v[240:243], v249 offset:32768\n" \
  "ds_read_b128 v[244:247], v249 offset:49152\n" \
  "v_mfma_f32_16x16x32_bf16 v[224:227], v[192:195], v[48:51], 0\n" \
  "v_mfma_f32_16x16x32_bf16 v[228:231], v[208:211], v[48:51], 0\n" \
  "v_mfma_f32_16x16x32_bf16 v[224:227], v[196:199], v[52:55], v[224:227]\n" \
  "v_cvt_pk_bf16_f32 v232, v232, v233\n" \
  "v_cvt_pk_bf16_f32 v234, v234, v235\n" \
  "v_mfma_f32_16x16x32_bf16 v[228:231], v[212:215], v[52:55], v[228:231]\n" \
  "v_cvt_pk_bf16_f32 v233, v236, v237\n" \
  "v_cvt_pk_bf16_f32 v235, v238, v239\n" \
  "v_mfma_f32_16x16x32_bf16 v[224:227], v[200:203], v[56:59], v[224:227]\n" \
  "s_add_u32 %[cnt], %[so], 6*%c[js]+1*%c[ss]\n" \
  "buffer_store_dwordx4 v[232:235], v251, %[rs], %[cnt] offen sc1\n" \
  "v_mfma_f32_16x16x32_bf16 v[228:231], v[216:219], v[56:59], v[228:231]\n" \
  "v_mfma_f32_16x16x32_bf16 v[224:227], v[204:207], v[60:63], v[224:227]\n" \
  "v_mfma_f32_16x16x32_bf16 v[228:231], v[220:223], v[60:63], v[228:231]\n" \
  "s_barrier\n" \
  "v_mfma_f32_16x16x32_bf16 v[232:235], v[192:195], v[176:179], 0\n" \
  "v_mfma_f32_16x16x32_bf16 v[236:239], v[208:211], v[176:179], 0\n" \
  "v_mfma_f32_16x16x32_bf16 v[232:235], v[200:203], v[184:187], v[232:235]\n" \
  "v_cvt_pk_bf16_f32 v224, v224, v225\n" \
  "v_cvt_pk_bf16_f32 v226, v226, v227\n" \
  "v_mfma_f32_16x16x32_bf16 v[236:239], v[216:219], v[184:187], v[236:239]\n" \
  "v_cvt_pk_bf16_f32 v225, v228, v229\n" \
  "v_cvt_pk_bf16_f32 v227, v230, v231\n" \
  "s_waitcnt lgkmcnt(1)\n" \
  "v_mfma_f32_16x16x32_bf16 v[232:235], v[196:199], v[240:243], v[232:235]\n" \
  "s_add_u32 %[cnt], %[so], 3*%c[js]+0*%c[ss]\n" \
  "buffer_store_dwordx4 v[224:227], v251, %[rs], %[cnt] offen sc1\n" \
  "v_mfma_f32_16x16x32_bf16 v[236:239], v[212:215], v[240:243], v[236:239]\n" \
  "s_waitcnt lgkmcnt(0)\n" \
  "v_mfma_f32_16x16x32_bf16 v[232:235], v[204:207], v[244:247], v[232:235]\n" \
  "v_mfma_f32_16x16x32_bf16 v[236:239], v[220:223], v[244:247], v[236:239]\n" \
  "ds_read_b128 v[240:243], v249 offset:33792\n" \
  "ds_read_b128 v[244:247], v249 offset:50176\n" \
  "v_mfma_f32_16x16x32_bf16 v[224:227], v[192:195], v[112:115], 0\n" \
  "v_mfma_f32_16x16x32_bf16 v[228:231], v[208:211], v[112:115], 0\n" \
  "v_mfma_f32_16x16x32_bf16 v[224:227], v[196:199], v[116:119], v[224:227]\n" \
  "v_cvt_pk_bf16_f32 v232, v232, v233\n" \
  "v_cvt_pk_bf16_f32 v234, v234, v235\n" \
  "v_mfma_f32_16x16x32_bf16 v[228:231], v[212:215], v[116:119], v[228:231]\n" \
  "v_cvt_pk_bf16_f32 v233, v236, v237\n" \
  "v_cvt_pk_bf16_f32 v235, v238, v239\n" \
  "v_mfma_f32_16x16x32_bf16 v[224:227], v[200:203], v[120:123], v[224:227]\n" \
  "s_add_u32 %[cnt], %[so], 7*%c[js]+0*%c[ss]\n" \
  "buffer_store_dwordx4 v[232:235], v251, %[rs], %[cnt] offen sc1\n" \
  "v_mfma_f32_16x16x32_bf16 v[228:231], v[216:219], v[120:123], v[228:231]\n" \
  "v_mfma_f32_16x16x32_bf16 v[224:227], v[204:207], v[124:127], v[224:227]\n" \
  "v_mfma_f32_16x16x32_bf16 v[228:231], v[220:223], v[124:127], v[228:231]\n" \
  "v_mfma_f32_16x16x32_bf16 v[232:235], v[192:195], v[180:183], 0\n" \
  "v_mfma_f32_16x16x32_bf16 v[236:239], v[208:211], v[180:183], 0\n" \
  "v_mfma_f32_16x16x32_bf16 v[232:235], v[200:203], v[188:191], v[232:235]\n" \
  "v_cvt_pk_bf16_f32 v224, v224, v225\n" \
  "v_cvt_pk_bf16_f32 v226, v226, v227\n" \
  "v_mfma_f32_16x16x32_bf16 v[236:239], v[216:219], v[188:191], v[236:239]\n" \
  "v_cvt_pk_bf16_f32 v225, v228, v229\n" \
  "v_cvt_pk_bf16_f32 v227, v230, v231\n" \
  "s_waitcnt lgkmcnt(1)\n" \
  "v_mfma_f32_16x16x32_bf16 v[232:235], v[196:199], v[240:243], v[232:235]\n" \
  "s_add_u32 %[cnt], %[so], 3*%c[js]+1*%c[ss]\n" \
  "buffer_store_dwordx4 v[224:227], v251, %[rs], %[cnt] offen sc1\n" \
  "v_mfma_f32_16x16x32_bf16 v[236:239], v[212:215], v[240:243], v[236:239]\n" \
  "s_waitcnt lgkmcnt(0)\n" \
  "v_mfma_f32_16x16x32_bf16 v[232:235], v[204:207], v[244:247], v[232:235]\n" \
  "v_mfma_f32_16x16x32_bf16 v[236:239], v[220:223], v[244:247], v[236:239]\n" \
  "s_nop 4\n" \
  "v_cvt_pk_bf16_f32 v232, v232, v233\n" \
  "v_cvt_pk_bf16_f32 v234, v234, v235\n" \
  "s_nop 1\n" \
  "v_cvt_pk_bf16_f32 v233, v236, v237\n" \
  "v_cvt_pk_bf16_f32 v235, v238, v239\n" \
  "s_add_u32 %[cnt], %[so], 7*%c[js]+1*%c[ss]\n" \
  "buffer_store_dwordx4 v[232:235], v251, %[rs], %[cnt] offen sc1\n" \
  "s_nop 1\n"
#define ST_XBF \
  "s_barrier\n" \
  "ds_read_b128 v[192:195], v251 offset:24576\n" \
  "ds_read_b128 v[208:211], v251 offset:28672\n" \
  "ds_read_b128 v[196:199], v251 offset:25600\n" \
  "ds_read_b128 v[212:215], v251 offset:29696\n" \
  "ds_read_b128 v[200:203], v251 offset:26624\n" \
  "ds_read_b128 v[216:219], v251 offset:30720\n" \
  "ds_read_b128 v[204:207], v251 offset:27648\n" \
  "ds_read_b128 v[220:223], v251 offset:31744\n" \
  "ds_read_b128 v[240:243], v250 offset:0\n" \
  "ds_read_b128 v[244:247], v250 offset:16384\n" \
  "s_waitcnt lgkmcnt(9)\n" \
  "v_mfma_f32_16x16x32_bf16 v[224:227], v[192:195], v[0:3], 0\n" \
  "s_waitcnt lgkmcnt(8)\n" \
  "v_mfma_f32_16x16x32_bf16 v[228:231], v[208:211], v[0:3], 0\n" \
  "s_waitcnt lgkmcnt(7)\n" \
  "v_mfma_f32_16x16x32_bf16 v[224:227], v[196:199], v[4:7], v[224:227]\n" \
  "s_waitcnt lgkmcnt(6)\n" \
  "v_mfma_f32_16x16x32_bf16 v[228:231], v[212:215], v[4:7], v[228:231]\n" \
  "s_waitcnt lgkmcnt(5)\n" \
  "v_mfma_f32_16x16x32_bf16 v[224:227], v[200:203], v[8:11], v[224:227]\n" \
  "s_waitcnt lgkmcnt(4)\n" \
  "v_mfma_f32_16x16x32_bf16 v[228:231], v[216:219], v[8:11], v[228:231]\n" \
  "s_waitcnt lgkmcnt(3)\n" \
  "v_mfma_f32_16x16x32_bf16 v[224:227], v[204:207], v[12:15], v[224:227]\n" \
  "s_waitcnt lgkmcnt(2)\n" \
  "v_mfma_f32_16x16x32_bf16 v[228:231], v[220:223], v[12:15], v[228:231]\n" \
  "v_mfma_f32_16x16x32_bf16 v[232:235], v[192:195], v[128:131], 0\n" \
  "v_mfma_f32_16x16x32_bf16 v[236:239], v[208:211], v[128:131], 0\n" \
  "v_mfma_f32_16x16x32_bf16 v[232:235], v[200:203], v[136:139], v[232:235]\n" \
  "v_cvt_pk_bf16_f32 v224, v224, v225\n" \
  "v_cvt_pk_bf16_f32 v226, v226, v227\n" \
  "v_mfma_f32_16x16x32_bf16 v[236:239], v[216:219], v[136:139], v[236:239]\n" \
  "v_cvt_pk_bf16_f32 v225, v228, v229\n" \
  "v_cvt_pk_bf16_f32 v227, v230, v231\n" \
  "s_waitcnt lgkmcnt(1)\n" \
  "v_mfma_f32_16x16x32_bf16 v[232:235], v[196:199], v[240:243], v[232:235]\n" \
  "s_add_u32 %[cnt], %[so], 0*%c[js]+0*%c[ss]\n" \
  "buffer_store_dwordx4 v[224:227], v251, %[rs], %[cnt] offen sc1\n" \
  "v_mfma_f32_16x16x32_bf16 v[236:239], v[212:215], v[240:243], v[236:239]\n" \
  "s_waitcnt lgkmcnt(0)\n" \
  "v_mfma_f32_16x16x32_bf16 v[232:235], v[204:207], v[244:247], v[232:235]\n" \
  "v_mfma_f32_16x16x32_bf16 v[236:239], v[220:223], v[244:247], v[236:239]\n" \
  "ds_read_b128 v[240:243], v250 offset:1024\n" \
  "ds_read_b128 v[244:247], v250 offset:17408\n" \
  "v_mfma_f32_16x16x32_bf16 v[224:227], v[192:195], v[64:67], 0\n" \
  "v_mfma_f32_16x16x32_bf16 v[228:231], v[208:211], v[64:67], 0\n" \
  "v_mfma_f32_16x16x32_bf16 v[224:227], v[196:199], v[68:71], v[224:227]\n" \
  "v_cvt_pk_bf16_f32 v232, v232, v233\n" \
  "v_cvt_pk_bf16_f32 v234, v234, v235\n" \
  "v_mfma_f32_16x16x32_bf16 v[228:231], v[212:215], v[68:71], v[228:231]\n" \
  "v_cvt_pk_bf16_f32 v233, v236, v237\n" \
  "v_cvt_pk_bf16_f32 v235, v238, v239\n" \
  "v_mfma_f32_16x16x32_bf16 v[224:227], v[200:203], v[72:75], v[224:227]\n" \
  "s_add_u32 %[cnt], %[so], 4*%c[js]+0*%c[ss]\n" \
  "buffer_store_dwordx4 v[232:235], v251, %[rs], %[cnt] offen sc1\n" \
  "v_mfma_f32_16x16x32_bf16 v[228:231], v[216:219], v[72:75], v[228:231]\n" \
  "v_mfma_f32_16x16x32_bf16 v[224:227], v[204:207], v[76:79], v[224:227]\n" \
  "v_mfma_f32_16x16x32_bf16 v[228:231], v[220:223], v[76:79], v[228:231]\n" \
  "v_mfma_f32_16x16x32_bf16 v[232:235], v[192:195], v[132:135], 0\n" \
  "v_mfma_f32_16x16x32_bf16 v[236:239], v[208:211], v[132:135], 0\n" \
  "v_mfma_f32_16x16x32_bf16 v[232:235], v[200:203], v[140:143], v[232:235]\n" \
  "v_cvt_pk_bf16_f32 v224, v224, v225\n" \
  "v_cvt_pk_bf16_f32 v226, v226, v227\n" \
  "v_mfma_f32_16x16x32_bf16 v[236:239], v[216:219], v[140:143], v[236:239]\n" \
  "v_cvt_pk_bf16_f32 v225, v228, v229\n" \
  "v_cvt_pk_bf16_f32 v227, v230, v231\n" \
  "s_waitcnt lgkmcnt(1)\n" \
  "v_mfma_f32_16x16x32_bf16 v[232:235], v[196:199], v[240:243], v[232:235]\n" \
  "s_add_u32 %[cnt], %[so], 0*%c[js]+1*%c[ss]\n" \
  "buffer_store_dwordx4 v[224:227], v251, %[rs], %[cnt] offen sc1\n" \
  "v_mfma_f32_16x16x32_bf16 v[236:239], v[212:215], v[240:243], v[236:239]\n" \
  "s_waitcnt lgkmcnt(0)\n" \
  "v_mfma_f32_16x16x32_bf16 v[232:235], v[204:207], v[244:247], v[232:235]\n" \
  "v_mfma_f32_16x16x32_bf16 v[236:239], v[220:223], v[244:247], v[236:239]\n" \
  "ds_read_b128 v[240:243], v250 offset:32768\n" \
  "ds_read_b128 v[244:247], v250 offset:49152\n" \
  "s_barrier\n" \
  "v_mfma_f32_16x16x32_bf16 v[224:227], v[192:195], v[16:19], 0\n" \
  "v_mfma_f32_16x16x32_bf16 v[228:231], v[208:211], v[16:19], 0\n" \
  "v_mfma_f32_16x16x32_bf16 v[224:227], v[196:199], v[20:23], v[224:227]\n" \
  "v_cvt_pk_bf16_f32 v232, v232, v233\n" \
  "v_cvt_pk_bf16_f32 v234, v234, v235\n" \
  "v_mfma_f32_16x16x32_bf16 v[228:231], v[212:215], v[20:23], v[228:231]\n" \
  "v_cvt_pk_bf16_f32 v233, v236, v237\n" \
  "v_cvt_pk_bf16_f32 v235, v238, v239\n" \
  "v_mfma_f32_16x16x32_bf16 v[224:227], v[200:203], v[24:27], v[224:227]\n" \
  "s_add_u32 %[cnt], %[so], 4*%c[js]+1*%c[ss]\n" \
  "buffer_store_dwordx4 v[232:235], v251, %[rs], %[cnt] offen sc1\n" \
  "v_mfma_f32_16x16x32_bf16 v[228:231], v[216:219], v[24:27], v[228:231]\n" \
  "v_mfma_f32_16x16x32_bf16 v[224:227], v[204:207], v[28:31], v[224:227]\n" \
  "v_mfma_f32_16x16x32_bf16 v[228:231], v[220:223], v[28:31], v[228:231]\n" \
  "v_mfma_f32_16x16x32_bf16 v[232:235], v[192:195], v[144:147], 0\n" \
  "v_mfma_f32_16x16x32_bf16 v[236:239], v[208:211], v[144:147], 0\n" \
  "v_mfma_f32_16x16x32_bf16 v[232:235], v[200:203], v[152:155], v[232:235]\n" \
  "v_cvt_pk_bf16_f32 v224, v224, v225\n" \
  "v_cvt_pk_bf16_f32 v226, v226, v227\n" \
  "v_mfma_f32_16x16x32_bf16 v[236:239], v[216:219], v[152:155], v[236:239]\n" \
  "v_cvt_pk_bf16_f32 v225, v228, v229\n" \
  "v_cvt_pk_bf16_f32 v227, v230, v231\n" \
  "s_waitcnt lgkmcnt(1)\n" \
  "v_mfma_f32_16x16x32_bf16 v[232:235], v[196:199], v[240:243], v[232:235]\n" \
  "s_add_u32 %[cnt], %[so], 1*%c[js]+0*%c[ss]\n" \
  "buffer_store_dwordx4 v[224:227], v251, %[rs], %[cnt] offen sc1\n" \
  "v_mfma_f32_16x16x32_bf16 v[236:239], v[212:215], v[240:243], v[236:239]\n" \
  "s_waitcnt lgkmcnt(0)\n" \
  "v_mfma_f32_16x16x32_bf16 v[232:235], v[204:207], v[244:247], v[232:235]\n" \
  "v_mfma_f32_16x16x32_bf16 v[236:239], v[220:223], v[244:247], v[236:239]\n" \
  "ds_read_b128 v[240:243], v250 offset:33792\n" \
  "ds_read_b128 v[244:247], v250 offset:50176\n" \
  "v_mfma_f32_16x16x32_bf16 v[224:227], v[192:195], v[80:83], 0\n" \
  "v_mfma_f32_16x16x32_bf16 v[228:231], v[208:211], v[80:83], 0\n" \
  "v_mfma_f32_16x16x32_bf16 v[224:227], v[196:199], v[84:87], v[224:227]\n" \
  "v_cvt_pk_bf16_f32 v232, v232, v233\n" \
  "v_cvt_pk_bf16_f32 v234, v234, v235\n" \
  "v_mfma_f32_16x16x32_bf16 v[228:231], v[212:215], v[84:87], v[228:231]\n" \
  "v_cvt_pk_bf16_f32 v233, v236, v237\n" \
  "v_cvt_pk_bf16_f32 v235, v238, v239\n" \
  "v_mfma_f32_16x16x32_bf16 v[224:227], v[200:203], v[88:91], v[224:227]\n" \
  "s_add_u32 %[cnt], %[so], 5*%c[js]+0*%c[ss]\n" \
  "buffer_store_dwordx4 v[232:235], v251, %[rs], %[cnt] offen sc1\n" \
  "v_mfma_f32_16x16x32_bf16 v[228:231], v[216:219], v[88:91], v[228:231]\n" \
  "v_mfma_f32_16x16x32_bf16 v[224:227], v[204:207], v[92:95], v[224:227]\n" \
  "v_mfma_f32_16x16x32_bf16 v[228:231], v[220:223], v[92:95], v[228:231]\n" \
  "v_mfma_f32_16x16x32_bf16 v[232:235], v[192:195], v[148:151], 0\n" \
  "v_mfma_f32_16x16x32_bf16 v[236:239], v[208:211], v[148:151], 0\n" \
  "v_mfma_f32_16x16x32_bf16 v[232:235], v[200:203], v[156:159], v[232:235]\n" \
  "v_cvt_pk_bf16_f32 v224, v224, v225\n" \
  "v_cvt_pk_bf16_f32 v226, v226, v227\n" \
  "v_mfma_f32_16x16x32_bf16 v[236:239], v[216:219], v[156:159], v[236:239]\n" \
  "v_cvt_pk_bf16_f32 v225, v228, v229\n" \
  "v_cvt_pk_bf16_f32 v227, v230, v231\n" \
  "s_waitcnt lgkmcnt(1)\n" \
  "v_mfma_f32_16x16x32_bf16 v[232:235], v[196:199], v[240:243], v[232:235]\n" \
  "s_add_u32 %[cnt], %[so], 1*%c[js]+1*%c[ss]\n" \
  "buffer_store_dwordx4 v[224:227], v251, %[rs], %[cnt] offen sc1\n" \
  "v_mfma_f32_16x16x32_bf16 v[236:239], v[212:215], v[240:243], v[236:239]\n" \
  "s_waitcnt lgkmcnt(0)\n" \
  "v_mfma_f32_16x16x32_bf16 v[232:235], v[204:207], v[244:247], v[232:235]\n" \
  "v_mfma_f32_16x16x32_bf16 v[236:239], v[220:223], v[244:247], v[236:239]\n" \
  "ds_read_b128 v[240:243], v249 offset:0\n" \
  "ds_read_b128 v[244:247], v249 offset:16384\n" \
  "s_barrier\n" \
  "v_mfma_f32_16x16x32_bf16 v[224:227], v[192:195], v[32:35], 0\n" \
  "v_mfma_f32_16x16x32_bf16 v[228:231], v[208:211], v[32:35], 0\n" \
  "v_mfma_f32_16x16x32_bf16 v[224:227], v[196:199], v[36:39], v[224:227]\n" \
  "v_cvt_pk_bf16_f32 v232, v232, v233\n" \
  "v_cvt_pk_bf16_f32 v234, v234, v235\n" \
  "v_mfma_f32_16x16x32_bf16 v[228:231], v[212:215], v[36:39], v[228:231]\n" \
  "v_cvt_pk_bf16_f32 v233, v236, v237\n" \
  "v_cvt_pk_bf16_f32 v235, v238, v239\n" \
  "v_mfma_f32_16x16x32_bf16 v[224:227], v[200:203], v[40:43], v[224:227]\n" \
  "s_add_u32 %[cnt], %[so], 5*%c[js]+1*%c[ss]\n" \
  "buffer_store_dwordx4 v[232:235], v251, %[rs], %[cnt] offen sc1\n" \
  "v_mfma_f32_16x16x32_bf16 v[228:231], v[216:219], v[40:43], v[228:231]\n" \
  "v_mfma_f32_16x16x32_bf16 v[224:227], v[204:207], v[44:47], v[224:227]\n" \
  "v_mfma_f32_16x16x32_bf16 v[228:231], v[220:223], v[44:47], v[228:231]\n" \
  "v_mfma_f32_16x16x32_bf16 v[232:235], v[192:195], v[160:163], 0\n" \
  "v_mfma_f32_16x16x32_bf16 v[236:239], v[208:211], v[160:163], 0\n" \
  "v_mfma_f32_16x16x32_bf16 v[232:235], v[200:203], v[168:171], v[232:235]\n" \
  "v_cvt_pk_bf16_f32 v224, v224, v225\n" \
  "v_cvt_pk_bf16_f32 v226, v226, v227\n" \
  "v_mfma_f32_16x16x32_bf16 v[236:239], v[216:219], v[168:171], v[236:239]\n" \
  "v_cvt_pk_bf16_f32 v225, v228, v229\n" \
  "v_cvt_pk_bf16_f32 v227, v230, v231\n" \
  "s_waitcnt lgkmcnt(1)\n" \
  "v_mfma_f32_16x16x32_bf16 v[232:235], v[196:199], v[240:243], v[232:235]\n" \
  "s_add_u32 %[cnt], %[so], 2*%c[js]+0*%c[ss]\n" \
  "buffer_store_dwordx4 v[224:227], v251, %[rs], %[cnt] offen sc1\n" \
  "v_mfma_f32_16x16x32_bf16 v[236:239], v[212:215], v[240:243], v[236:239]\n" \
  "s_waitcnt lgkmcnt(0)\n" \
  "v_mfma_f32_16x16x32_bf16 v[232:235], v[204:207], v[244:247], v[232:235]\n" \
  "v_mfma_f32_16x16x32_bf16 v[236:239], v[220:223], v[244:247], v[236:239]\n" \
  "ds_read_b128 v[240:243], v249 offset:1024\n" \
  "ds_read_b128 v[244:247], v249 offset:17408\n" \
  "v_mfma_f32_16x16x32_bf16 v[224:227], v[192:195], v[96:99], 0\n" \
  "v_mfma_f32_16x16x32_bf16 v[228:231], v[208:211], v[96:99], 0\n" \
  "v_mfma_f32_16x16x32_bf16 v[224:227], v[196:199], v[100:103], v[224:227]\n" \
  "v_cvt_pk_bf16_f32 v232, v232, v233\n" \
  "v_cvt_pk_bf16_f32 v234, v234, v235\n" \
  "v_mfma_f32_16x16x32_bf16 v[228:231], v[212:215], v[100:103], v[228:231]\n" \
  "v_cvt_pk_bf16_f32 v233, v236, v237\n" \
  "v_cvt_pk_bf16_f32 v235, v238, v239\n" \
  "v_mfma_f32_16x16x32_bf16 v[224:227], v[200:203], v[104:107], v[224:227]\n" \
  "s_add_u32 %[cnt], %[so], 6*%c[js]+0*%c[ss]\n" \
  "buffer_store_dwordx4 v[232:235], v251, %[rs], %[cnt] offen sc1\n" \
  "v_mfma_f32_16x16x32_bf16 v[228:231], v[216:219], v[104:107], v[228:231]\n" \
  "v_mfma_f32_16x16x32_bf16 v[224:227], v[204:207], v[108:111], v[224:227]\n" \
  "v_mfma_f32_16x16x32_bf16 v[228:231], v[220:223], v[108:111], v[228:231]\n" \
  "v_mfma_f32_16x16x32_bf16 v[232:235], v[192:195], v[164:167], 0\n" \
  "v_mfma_f32_16x16x32_bf16 v[236:239], v[208:211], v[164:167], 0\n" \
  "v_mfma_f32_16x16x32_bf16 v[232:235], v[200:203], v[172:175], v[232:235]\n" \
  "v_cvt_pk_bf16_f32 v224, v224, v225\n" \
  "v_cvt_pk_bf16_f32 v226, v226, v227\n" \
  "v_mfma_f32_16x16x32_bf16 v[236:239], v[216:219], v[172:175], v[236:239]\n" \
  "v_cvt_pk_bf16_f32 v225, v228, v229\n" \
  "v_cvt_pk_bf16_f32 v227, v230, v231\n" \
  "s_waitcnt lgkmcnt(1)\n" \
  "v_mfma_f32_16x16x32_bf16 v[232:235], v[196:199], v[240:243], v[232:235]\n" \
  "s_add_u32 %[cnt], %[so], 2*%c[js]+1*%c[ss]\n" \
  "buffer_store_dwordx4 v[224:227], v251, %[rs], %[cnt] offen sc1\n" \
  "v_mfma_f32_16x16x32_bf16 v[236:239], v[212:215], v[240:243], v[236:239]\n" \
  "s_waitcnt lgkmcnt(0)\n" \
  "v_mfma_f32_16x16x32_bf16 v[232:235], v[204:207], v[244:247], v[232:235]\n" \
  "v_mfma_f32_16x16x32_bf16 v[236:239], v[220:223], v[244:247], v[236:239]\n" \
  "ds_read_b128 v[240:243], v249 offset:32768\n" \
  "ds_read_b128 v[244:247], v249 offset:49152\n" \
  "s_barrier\n" \
  "v_mfma_f32_16x16x32_bf16 v[224:227], v[192:195], v[48:51], 0\n" \
  "v_mfma_f32_16x16x32_bf16 v[228:231], v[208:211], v[48:51], 0\n" \
  "v_mfma_f32_16x16x32_bf16 v[224:227], v[196:199], v[52:55], v[224:227]\n" \
  "v_cvt_pk_bf16_f32 v232, v232, v233\n" \
  "v_cvt_pk_bf16_f32 v234, v234, v235\n" \
  "v_mfma_f32_16x16x32_bf16 v[228:231], v[212:215], v[52:55], v[228:231]\n" \
  "v_cvt_pk_bf16_f32 v233, v236, v237\n" \
  "v_cvt_pk_bf16_f32 v235, v238, v239\n" \
  "v_mfma_f32_16x16x32_bf16 v[224:227], v[200:203], v[56:59], v[224:227]\n" \
  "s_add_u32 %[cnt], %[so], 6*%c[js]+1*%c[ss]\n" \
  "buffer_store_dwordx4 v[232:235], v251, %[rs], %[cnt] offen sc1\n" \
  "v_mfma_f32_16x16x32_bf16 v[228:231], v[216:219], v[56:59], v[228:231]\n" \
  "v_mfma_f32_16x16x32_bf16 v[224:227], v[204:207], v[60:63], v[224:227]\n" \
  "v_mfma_f32_16x16x32_bf16 v[228:231], v[220:223], v[60:63], v[228:231]\n" \
  "v_mfma_f32_16x16x32_bf16 v[232:235], v[192:195], v[176:179], 0\n" \
  "v_mfma_f32_16x16x32_bf16 v[236:239], v[208:211], v[176:179], 0\n" \
  "v_mfma_f32_16x16x32_bf16 v[232:235], v[200:203], v[184:187], v[232:235]\n" \
  "v_cvt_pk_bf16_f32 v224, v224, v225\n" \
  "v_cvt_pk_bf16_f32 v226, v226, v227\n" \
  "v_mfma_f32_16x16x32_bf16 v[236:239], v[216:219], v[184:187], v[236:239]\n" \
  "v_cvt_pk_bf16_f32 v225, v228, v229\n" \
  "v_cvt_pk_bf16_f32 v227, v230, v231\n" \
  "s_waitcnt lgkmcnt(1)\n" \
  "v_mfma_f32_16x16x32_bf16 v[232:235], v[196:199], v[240:243], v[232:235]\n" \
  "s_add_u32 %[cnt], %[so], 3*%c[js]+0*%c[ss]\n" \
  "buffer_store_dwordx4 v[224:227], v251, %[rs], %[cnt] offen sc1\n" \
  "v_mfma_f32_16x16x32_bf16 v[236:239], v[212:215], v[240:243], v[236:239]\n" \
  "s_waitcnt lgkmcnt(0)\n" \
  "v_mfma_f32_16x16x32_bf16 v[232:235], v[204:207], v[244:247], v[232:235]\n" \
  "v_mfma_f32_16x16x32_bf16 v[236:239], v[220:223], v[244:247], v[236:239]\n" \
  "ds_read_b128 v[240:243], v249 offset:33792\n" \
  "ds_read_b128 v[244:247], v249 offset:50176\n" \
  "v_mfma_f32_16x16x32_bf16 v[224:227], v[192:195], v[112:115], 0\n" \
  "v_mfma_f32_16x16x32_bf16 v[228:231], v[208:211], v[112:115], 0\n" \
  "v_mfma_f32_16x16x32_bf16 v[224:227], v[196:199], v[116:119], v[224:227]\n" \
  "v_cvt_pk_bf16_f32 v232, v232, v233\n" \
  "v_cvt_pk_bf16_f32 v234, v234, v235\n" \
  "v_mfma_f32_16x16x32_bf16 v[228:231], v[212:215], v[116:119], v[228:231]\n" \
  "v_cvt_pk_bf16_f32 v233, v236, v237\n" \
  "v_cvt_pk_bf16_f32 v235, v238, v239\n" \
  "v_mfma_f32_16x16x32_bf16 v[224:227], v[200:203], v[120:123], v[224:227]\n" \
  "s_add_u32 %[cnt], %[so], 7*%c[js]+0*%c[ss]\n" \
  "buffer_store_dwordx4 v[232:235], v251, %[rs], %[cnt] offen sc1\n" \
  "v_mfma_f32_16x16x32_bf16 v[228:231], v[216:219], v[120:123], v[228:231]\n" \
  "v_mfma_f32_16x16x32_bf16 v[224:227], v[204:207], v[124:127], v[224:227]\n" \
  "v_mfma_f32_16x16x32_bf16 v[228:231], v[220:223], v[124:127], v[228:231]\n" \
  "v_mfma_f32_16x16x32_bf16 v[232:235], v[192:195], v[180:183], 0\n" \
  "v_mfma_f32_16x16x32_bf16 v[236:239], v[208:211], v[180:183], 0\n" \
  "v_mfma_f32_16x16x32_bf16 v[232:235], v[200:203], v[188:191], v[232:235]\n" \
  "v_cvt_pk_bf16_f32 v224, v224, v225\n" \
  "v_cvt_pk_bf16_f32 v226, v226, v227\n" \
  "v_mfma_f32_16x16x32_bf16 v[236:239], v[216:219], v[188:191], v[236:239]\n" \
  "v_cvt_pk_bf16_f32 v225, v228, v229\n" \
  "v_cvt_pk_bf16_f32 v227, v230, v231\n" \
  "s_waitcnt lgkmcnt(1)\n" \
  "v_mfma_f32_16x16x32_bf16 v[232:235], v[196:199], v[240:243], v[232:235]\n" \
  "s_add_u32 %[cnt], %[so], 3*%c[js]+1*%c[ss]\n" \
  "buffer_store_dwordx4 v[224:227], v251, %[rs], %[cnt] offen sc1\n" \
  "v_mfma_f32_16x16x32_bf16 v[236:239], v[212:215], v[240:243], v[236:239]\n" \
  "s_waitcnt lgkmcnt(0)\n" \
  "v_mfma_f32_16x16x32_bf16 v[232:235], v[204:207], v[244:247], v[232:235]\n" \
  "v_mfma_f32_16x16x32_bf16 v[236:239], v[220:223], v[244:247], v[236:239]\n" \
  "s_nop 4\n" \
  "v_cvt_pk_bf16_f32 v232, v232, v233\n" \
  "v_cvt_pk_bf16_f32 v234, v234, v235\n" \
  "s_nop 1\n" \
  "v_cvt_pk_bf16_f32 v233, v236, v237\n" \
  "v_cvt_pk_bf16_f32 v235, v238, v239\n" \
  "s_add_u32 %[cnt], %[so], 7*%c[js]+1*%c[ss]\n" \
  "buffer_store_dwordx4 v[232:235], v251, %[rs], %[cnt] offen sc1\n" \
  "s_barrier\n" \
  "s_nop 1\n"
#define ST_XBS \
  "ds_read_b128 v[192:195], v251 offset:24576\n" \
  "ds_read_b128 v[208:211], v251 offset:28672\n" \
  "ds_read_b128 v[196:199], v251 offset:25600\n" \
  "ds_read_b128 v[212:215], v251 offset:29696\n" \
  "ds_read_b128 v[200:203], v251 offset:26624\n" \
  "ds_read_b128 v[216:219], v251 offset:30720\n" \
  "ds_read_b128 v[204:207], v251 offset:27648\n" \
  "ds_read_b128 v[220:223], v251 offset:31744\n" \
  "ds_read_b128 v[240:243], v250 offset:0\n" \
  "ds_read_b128 v[244:247], v250 offset:16384\n" \
  "s_waitcnt lgkmcnt(9)\n" \
  "v_mfma_f32_16x16x32_bf16 v[224:227], v[192:195], v[0:3], 0\n" \
  "s_waitcnt lgkmcnt(8)\n" \
  "v_mfma_f32_16x16x32_bf16 v[228:231], v[208:211], v[0:3], 0\n" \
  "s_waitcnt lgkmcnt(7)\n" \
  "v_mfma_f32_16x16x32_bf16 v[224:227], v[196:199], v[4:7], v[224:227]\n" \
  "s_waitcnt lgkmcnt(6)\n" \
  "v_mfma_f32_16x16x32_bf16 v[228:231], v[212:215], v[4:7], v[228:231]\n" \
  "s_waitcnt lgkmcnt(5)\n" \
  "v_mfma_f32_16x16x32_bf16 v[224:227], v[200:203], v[8:11], v[224:227]\n" \
  "s_waitcnt lgkmcnt(4)\n" \
  "v_mfma_f32_16x16x32_bf16 v[228:231], v[216:219], v[8:11], v[228:231]\n" \
  "s_waitcnt lgkmcnt(3)\n" \
  "v_mfma_f32_16x16x32_bf16 v[224:227], v[204:207], v[12:15], v[224:227]\n" \
  "s_waitcnt lgkmcnt(2)\n" \
  "v_mfma_f32_16x16x32_bf16 v[228:231], v[220:223], v[12:15], v[228:231]\n" \
  "v_mfma_f32_16x16x32_bf16 v[232:235], v[192:195], v[128:131], 0\n" \
  "v_mfma_f32_16x16x32_bf16 v[236:239], v[208:211], v[128:131], 0\n" \
  "v_mfma_f32_16x16x32_bf16 v[232:235], v[200:203], v[136:139], v[232:235]\n" \
  "v_cvt_pk_bf16_f32 v224, v224, v225\n" \
  "v_cvt_pk_bf16_f32 v226, v226, v227\n" \
  "v_mfma_f32_16x16x32_bf16 v[236:239], v[216:219], v[136:139], v[236:239]\n" \
  "v_cvt_pk_bf16_f32 v225, v228, v229\n" \
  "v_cvt_pk_bf16_f32 v227, v230, v231\n" \
  "s_waitcnt lgkmcnt(1)\n" \
  "v_mfma_f32_16x16x32_bf16 v[232:235], v[196:199], v[240:243], v[232:235]\n" \
  "s_add_u32 %[cnt], %[so], 0*%c[js]+0*%c[ss]\n" \
  "buffer_store_dwordx4 v[224:227], v251, %[rs], %[cnt] offen sc1\n" \
  "v_mfma_f32_16x16x32_bf16 v[236:239], v[212:215], v[240:243], v[236:239]\n" \
  "s_waitcnt lgkmcnt(0)\n" \
  "v_mfma_f32_16x16x32_bf16 v[232:235], v[204:207], v[244:247], v[232:235]\n" \
  "v_mfma_f32_16x16x32_bf16 v[236:239], v[220:223], v[244:247], v[236:239]\n" \
  "ds_read_b128 v[240:243], v250 offset:1024\n" \
  "ds_read_b128 v[244:247], v250 offset:17408\n" \
  "v_mfma_f32_16x16x32_bf16 v[224:227], v[192:195], v[64:67], 0\n" \
  "v_mfma_f32_16x16x32_bf16 v[228:231], v[208:211], v[64:67], 0\n" \
  "v_mfma_f32_16x16x32_bf16 v[224:227], v[196:199], v[68:71], v[224:227]\n" \
  "v_cvt_pk_bf16_f32 v232, v232, v233\n" \
  "v_cvt_pk_bf16_f32 v234, v234, v235\n" \
  "v_mfma_f32_16x16x32_bf16 v[228:231], v[212:215], v[68:71], v[228:231]\n" \
  "v_cvt_pk_bf16_f32 v233, v236, v237\n" \
  "v_cvt_pk_bf16_f32 v235, v238, v239\n" \
  "v_mfma_f32_16x16x32_bf16 v[224:227], v[200:203], v[72:75], v[224:227]\n" \
  "s_add_u32 %[cnt], %[so], 4*%c[js]+0*%c[ss]\n" \
  "buffer_store_dwordx4 v[232:235], v251, %[rs], %[cnt] offen sc1\n" \
  "v_mfma_f32_16x16x32_bf16 v[228:231], v[216:219], v[72:75], v[228:231]\n" \
  "v_mfma_f32_16x16x32_bf16 v[224:227], v[204:207], v[76:79], v[224:227]\n" \
  "v_mfma_f32_16x16x32_bf16 v[228:231], v[220:223], v[76:79], v[228:231]\n" \
  "v_mfma_f32_16x16x32_bf16 v[232:235], v[192:195], v[132:135], 0\n" \
  "v_mfma_f32_16x16x32_bf16 v[236:239], v[208:211], v[132:135], 0\n" \
  "v_mfma_f32_16x16x32_bf16 v[232:235], v[200:203], v[140:143], v[232:235]\n" \
  "v_cvt_pk_bf16_f32 v224, v224, v225\n" \
  "v_cvt_pk_bf16_f32 v226, v226, v227\n" \
  "v_mfma_f32_16x16x32_bf16 v[236:239], v[216:219], v[140:143], v[236:239]\n" \
  "v_cvt_pk_bf16_f32 v225, v228, v229\n" \
  "v_cvt_pk_bf16_f32 v227, v230, v231\n" \
  "s_waitcnt lgkmcnt(1)\n" \
  "v_mfma_f32_16x16x32_bf16 v[232:235], v[196:199], v[240:243], v[232:235]\n" \
  "s_add_u32 %[cnt], %[so], 0*%c[js]+1*%c[ss]\n" \
  "buffer_store_dwordx4 v[224:227], v251, %[rs], %[cnt] offen sc1\n" \
  "v_mfma_f32_16x16x32_bf16 v[236:239], v[212:215], v[240:243], v[236:239]\n" \
  "s_waitcnt lgkmcnt(0)\n" \
  "v_mfma_f32_16x16x32_bf16 v[232:235], v[204:207], v[244:247], v[232:235]\n" \
  "v_mfma_f32_16x16x32_bf16 v[236:239], v[220:223], v[244:247], v[236:239]\n" \
  "ds_read_b128 v[240:243], v250 offset:32768\n" \
  "ds_read_b128 v[244:247], v250 offset:49152\n" \
  "v_mfma_f32_16x16x32_bf16 v[224:227], v[192:195], v[16:19], 0\n" \
  "v_mfma_f32_16x16x32_bf16 v[228:231], v[208:211], v[16:19], 0\n" \
  "v_mfma_f32_16x16x32_bf16 v[224:227], v[196:199], v[20:23], v[224:227]\n" \
  "v_cvt_pk_bf16_f32 v232, v232, v233\n" \
  "v_cvt_pk_bf16_f32 v234, v234, v235\n" \
  "v_mfma_f32_16x16x32_bf16 v[228:231], v[212:215], v[20:23], v[228:231]\n" \
  "v_cvt_pk_bf16_f32 v233, v236, v237\n" \
  "v_cvt_pk_bf16_f32 v235, v238, v239\n" \
  "v_mfma_f32_16x16x32_bf16 v[224:227], v[200:203], v[24:27], v[224:227]\n" \
  "s_add_u32 %[cnt], %[so], 4*%c[js]+1*%c[ss]\n" \
  "buffer_store_dwordx4 v[232:235], v251, %[rs], %[cnt] offen sc1\n" \
  "v_mfma_f32_16x16x32_bf16 v[228:231], v[216:219], v[24:27], v[228:231]\n" \
  "v_mfma_f32_16x16x32_bf16 v[224:227], v[204:207], v[28:31], v[224:227]\n" \
  "v_mfma_f32_16x16x32_bf16 v[228:231], v[220:223], v[28:31], v[228:231]\n" \
  "v_mfma_f32_16x16x32_bf16 v[232:235], v[192:195], v[144:147], 0\n" \
  "v_mfma_f32_16x16x32_bf16 v[236:239], v[208:211], v[144:147], 0\n" \
  "v_mfma_f32_16x16x32_bf16 v[232:235], v[200:203], v[152:155], v[232:235]\n" \
  "v_cvt_pk_bf16_f32 v224, v224, v225\n" \
  "v_cvt_pk_bf16_f32 v226, v226, v227\n" \
  "v_mfma_f32_16x16x32_bf16 v[236:239], v[216:219], v[152:155], v[236:239]\n" \
  "v_cvt_pk_bf16_f32 v225, v228, v229\n" \
  "v_cvt_pk_bf16_f32 v227, v230, v231\n" \
  "s_waitcnt lgkmcnt(1)\n" \
  "v_mfma_f32_16x16x32_bf16 v[232:235], v[196:199], v[240:243], v[232:235]\n" \
  "s_add_u32 %[cnt], %[so], 1*%c[js]+0*%c[ss]\n" \
  "buffer_store_dwordx4 v[224:227], v251, %[rs], %[cnt] offen sc1\n" \
  "v_mfma_f32_16x16x32_bf16 v[236:239], v[212:215], v[240:243], v[236:239]\n" \
  "s_waitcnt lgkmcnt(0)\n" \
  "v_mfma_f32_16x16x32_bf16 v[232:235], v[204:207], v[244:247], v[232:235]\n" \
  "v_mfma_f32_16x16x32_bf16 v[236:239], v[220:223], v[244:247], v[236:239]\n" \
  "ds_read_b128 v[240:243], v250 offset:33792\n" \
  "ds_read_b128 v[244:247], v250 offset:50176\n" \
  "v_mfma_f32_16x16x32_bf16 v[224:227], v[192:195], v[80:83], 0\n" \
  "v_mfma_f32_16x16x32_bf16 v[228:231], v[208:211], v[80:83], 0\n" \
  "v_mfma_f32_16x16x32_bf16 v[224:227], v[196:199], v[84:87], v[224:227]\n" \
  "v_cvt_pk_bf16_f32 v232, v232, v233\n" \
  "v_cvt_pk_bf16_f32 v234, v234, v235\n" \
  "v_mfma_f32_16x16x32_bf16 v[228:231], v[212:215], v[84:87], v[228:231]\n" \
  "v_cvt_pk_bf16_f32 v233, v236, v237\n" \
  "v_cvt_pk_bf16_f32 v235, v238, v239\n" \
  "v_mfma_f32_16x16x32_bf16 v[224:227], v[200:203], v[88:91], v[224:227]\n" \
  "s_add_u32 %[cnt], %[so], 5*%c[js]+0*%c[ss]\n" \
  "buffer_store_dwordx4 v[232:235], v251, %[rs], %[cnt] offen sc1\n" \
  "v_mfma_f32_16x16x32_bf16 v[228:231], v[216:219], v[88:91], v[228:231]\n" \
  "v_mfma_f32_16x16x32_bf16 v[224:227], v[204:207], v[92:95], v[224:227]\n" \
  "v_mfma_f32_16x16x32_bf16 v[228:231], v[220:223], v[92:95], v[228:231]\n" \
  "v_mfma_f32_16x16x32_bf16 v[232:235], v[192:195], v[148:151], 0\n" \
  "v_mfma_f32_16x16x32_bf16 v[236:239], v[208:211], v[148:151], 0\n" \
  "v_mfma_f32_16x16x32_bf16 v[232:235], v[200:203], v[156:159], v[232:235]\n" \
  "v_cvt_pk_bf16_f32 v224, v224, v225\n" \
  "v_cvt_pk_bf16_f32 v226, v226, v227\n" \
  "v_mfma_f32_16x16x32_bf16 v[236:239], v[216:219], v[156:159], v[236:239]\n" \
  "v_cvt_pk_bf16_f32 v225, v228, v229\n" \
  "v_cvt_pk_bf16_f32 v227, v230, v231\n" \
  "s_waitcnt lgkmcnt(1)\n" \
  "v_mfma_f32_16x16x32_bf16 v[232:235], v[196:199], v[240:243], v[232:235]\n" \
  "s_add_u32 %[cnt], %[so], 1*%c[js]+1*%c[ss]\n" \
  "buffer_store_dwordx4 v[224:227], v251, %[rs], %[cnt] offen sc1\n" \
  "v_mfma_f32_16x16x32_bf16 v[236:239], v[212:215], v[240:243], v[236:239]\n" \
  "s_waitcnt lgkmcnt(0)\n" \
  "v_mfma_f32_16x16x32_bf16 v[232:235], v[204:207], v[244:247], v[232:235]\n" \
  "v_mfma_f32_16x16x32_bf16 v[236:239], v[220:223], v[244:247], v[236:239]\n" \
  "ds_read_b128 v[240:243], v249 offset:0\n" \
  "ds_read_b128 v[244:247], v249 offset:16384\n" \
  "s_barrier\n" \
  "v_mfma_f32_16x16x32_bf16 v[224:227], v[192:195], v[32:35], 0\n" \
  "v_mfma_f32_16x16x32_bf16 v[228:231], v[208:211], v[32:35], 0\n" \
  "v_mfma_f32_16x16x32_bf16 v[224:227], v[196:199], v[36:39], v[224:227]\n" \
  "v_cvt_pk_bf16_f32 v232, v232, v233\n" \
  "v_cvt_pk_bf16_f32 v234, v234, v235\n" \
  "v_mfma_f32_16x16x32_bf16 v[228:231], v[212:215], v[36:39], v[228:231]\n" \
  "v_cvt_pk_bf16_f32 v233, v236, v237\n" \
  "v_cvt_pk_bf16_f32 v235, v238, v239\n" \
  "v_mfma_f32_16x16x32_bf16 v[224:227], v[200:203], v[40:43], v[224:227]\n" \
  "s_add_u32 %[cnt], %[so], 5*%c[js]+1*%c[ss]\n" \
  "buffer_store_dwordx4 v[232:235], v251, %[rs], %[cnt] offen sc1\n" \
  "v_mfma_f32_16x16x32_bf16 v[228:231], v[216:219], v[40:43], v[228:231]\n" \
  "v_mfma_f32_16x16x32_bf16 v[224:227], v[204:207], v[44:47], v[224:227]\n" \
  "v_mfma_f32_16x16x32_bf16 v[228:231], v[220:223], v[44:47], v[228:231]\n" \
  "v_mfma_f32_16x16x32_bf16 v[232:235], v[192:195], v[160:163], 0\n" \
  "v_mfma_f32_16x16x32_bf16 v[236:239], v[208:211], v[160:163], 0\n" \
  "v_mfma_f32_16x16x32_bf16 v[232:235], v[200:203], v[168:171], v[232:235]\n" \
  "v_cvt_pk_bf16_f32 v224, v224, v225\n" \
  "v_cvt_pk_bf16_f32 v226, v226, v227\n" \
  "v_mfma_f32_16x16x32_bf16 v[236:239], v[216:219], v[168:171], v[236:239]\n" \
  "v_cvt_pk_bf16_f32 v225, v228, v229\n" \
  "v_cvt_pk_bf16_f32 v227, v230, v231\n" \
  "s_waitcnt lgkmcnt(1)\n" \
  "v_mfma_f32_16x16x32_bf16 v[232:235], v[196:199], v[240:243], v[232:235]\n" \
  "s_add_u32 %[cnt], %[so], 2*%c[js]+0*%c[ss]\n" \
  "buffer_store_dwordx4 v[224:227], v251, %[rs], %[cnt] offen sc1\n" \
  "v_mfma_f32_16x16x32_bf16 v[236:239], v[212:215], v[240:243], v[236:239]\n" \
  "s_waitcnt lgkmcnt(0)\n" \
  "v_mfma_f32_16x16x32_bf16 v[232:235], v[204:207], v[244:247], v[232:235]\n" \
  "v_mfma_f32_16x16x32_bf16 v[236:239], v[220:223], v[244:247], v[236:239]\n" \
  "ds_read_b128 v[240:243], v249 offset:1024\n" \
  "ds_read_b128 v[244:247], v249 offset:17408\n" \
  "v_mfma_f32_16x16x32_bf16 v[224:227], v[192:195], v[96:99], 0\n" \
  "v_mfma_f32_16x16x32_bf16 v[228:231], v[208:211], v[96:99], 0\n" \
  "v_mfma_f32_16x16x32_bf16 v[224:227], v[196:199], v[100:103], v[224:227]\n" \
  "v_cvt_pk_bf16_f32 v232, v232, v233\n" \
  "v_cvt_pk_bf16_f32 v234, v234, v235\n" \
  "v_mfma_f32_16x16x32_bf16 v[228:231], v[212:215], v[100:103], v[228:231]\n" \
  "v_cvt_pk_bf16_f32 v233, v236, v237\n" \
  "v_cvt_pk_bf16_f32 v235, v238, v239\n" \
  "v_mfma_f32_16x16x32_bf16 v[224:227], v[200:203], v[104:107], v[224:227]\n" \
  "s_add_u32 %[cnt], %[so], 6*%c[js]+0*%c[ss]\n" \
  "buffer_store_dwordx4 v[232:235], v251, %[rs], %[cnt] offen sc1\n" \
  "v_mfma_f32_16x16x32_bf16 v[228:231], v[216:219], v[104:107], v[228:231]\n" \
  "v_mfma_f32_16x16x32_bf16 v[224:227], v[204:207], v[108:111], v[224:227]\n" \
  "v_mfma_f32_16x16x32_bf16 v[228:231], v[220:223], v[108:111], v[228:231]\n" \
  "v_mfma_f32_16x16x32_bf16 v[232:235], v[192:195], v[164:167], 0\n" \
  "v_mfma_f32_16x16x32_bf16 v[236:239], v[208:211], v[164:167], 0\n" \
  "v_mfma_f32_16x16x32_bf16 v[232:235], v[200:203], v[172:175], v[232:235]\n" \
  "v_cvt_pk_bf16_f32 v224, v224, v225\n" \
  "v_cvt_pk_bf16_f32 v226, v226, v227\n" \
  "v_mfma_f32_16x16x32_bf16 v[236:239], v[216:219], v[172:175], v[236:239]\n" \
  "v_cvt_pk_bf16_f32 v225, v228, v229\n" \
  "v_cvt_pk_bf16_f32 v227, v230, v231\n" \
  "s_waitcnt lgkmcnt(1)\n" \
  "v_mfma_f32_16x16x32_bf16 v[232:235], v[196:199], v[240:243], v[232:235]\n" \
  "s_add_u32 %[cnt], %[so], 2*%c[js]+1*%c[ss]\n" \
  "buffer_store_dwordx4 v[224:227], v251, %[rs], %[cnt] offen sc1\n" \
  "v_mfma_f32_16x16x32_bf16 v[236:239], v[212:215], v[240:243], v[236:239]\n" \
  "s_waitcnt lgkmcnt(0)\n" \
  "v_mfma_f32_16x16x32_bf16 v[232:235], v[204:207], v[244:247], v[232:235]\n" \
  "v_mfma_f32_16x16x32_bf16 v[236:239], v[220:223], v[244:247], v[236:239]\n" \
  "ds_read_b128 v[240:243], v249 offset:32768\n" \
  "ds_read_b128 v[244:247], v249 offset:49152\n" \
  "s_barrier\n" \
  "v_mfma_f32_16x16x32_bf16 v[224:227], v[192:195], v[48:51], 0\n" \
  "v_mfma_f32_16x16x32_bf16 v[228:231], v[208:211], v[48:51], 0\n" \
  "v_mfma_f32_16x16x32_bf16 v[224:227], v[196:199], v[52:55], v[224:227]\n" \
  "v_cvt_pk_bf16_f32 v232, v232, v233\n" \
  "v_cvt_pk_bf16_f32 v234, v234, v235\n" \
  "v_mfma_f32_16x16x32_bf16 v[228:231], v[212:215], v[52:55], v[228:231]\n" \
  "v_cvt_pk_bf16_f32 v233, v236, v237\n" \
  "v_cvt_pk_bf16_f32 v235, v238, v239\n" \
  "v_mfma_f32_16x16x32_bf16 v[224:227], v[200:203], v[56:59], v[224:227]\n" \
  "s_add_u32 %[cnt], %[so], 6*%c[js]+1*%c[ss]\n" \
  "buffer_store_dwordx4 v[232:235], v251, %[rs], %[cnt] offen sc1\n" \
  "v_mfma_f32_16x16x32_bf16 v[228:231], v[216:219], v[56:59], v[228:231]\n" \
  "v_mfma_f32_16x16x32_bf16 v[224:227], v[204:207], v[60:63], v[224:227]\n" \
  "v_mfma_f32_16x16x32_bf16 v[228:231], v[220:223], v[60:63], v[228:231]\n" \
  "v_mfma_f32_16x16x32_bf16 v[232:235], v[192:195], v[176:179], 0\n" \
  "v_mfma_f32_16x16x32_bf16 v[236:239], v[208:211], v[176:179], 0\n" \
  "v_mfma_f32_16x16x32_bf16 v[232:235], v[200:203], v[184:187], v[232:235]\n" \
  "v_cvt_pk_bf16_f32 v224, v224, v225\n" \
  "v_cvt_pk_bf16_f32 v226, v226, v227\n" \
  "v_mfma_f32_16x16x32_bf16 v[236:239], v[216:219], v[184:187], v[236:239]\n" \
  "v_cvt_pk_bf16_f32 v225, v228, v229\n" \
  "v_cvt_pk_bf16_f32 v227, v230, v231\n" \
  "s_waitcnt lgkmcnt(1)\n" \
  "v_mfma_f32_16x16x32_bf16 v[232:235], v[196:199], v[240:243], v[232:235]\n" \
  "s_add_u32 %[cnt], %[so], 3*%c[js]+0*%c[ss]\n" \
  "buffer_store_dwordx4 v[224:227], v251, %[rs], %[cnt] offen sc1\n" \
  "v_mfma_f32_16x16x32_bf16 v[236:239], v[212:215], v[240:243], v[236:239]\n" \
  "s_waitcnt lgkmcnt(0)\n" \
  "v_mfma_f32_16x16x32_bf16 v[232:235], v[204:207], v[244:247], v[232:235]\n" \
  "v_mfma_f32_16x16x32_bf16 v[236:239], v[220:223], v[244:247], v[236:239]\n" \
  "ds_read_b128 v[240:243], v249 offset:33792\n" \
  "ds_read_b128 v[244:247], v249 offset:50176\n" \
  "v_mfma_f32_16x16x32_bf16 v[224:227], v[192:195], v[112:115], 0\n" \
  "v_mfma_f32_16x16x32_bf16 v[228:231], v[208:211], v[112:115], 0\n" \
  "v_mfma_f32_16x16x32_bf16 v[224:227], v[196:199], v[116:119], v[224:227]\n" \
  "v_cvt_pk_bf16_f32 v232, v232, v233\n" \
  "v_cvt_pk_bf16_f32 v234, v234, v235\n" \
  "v_mfma_f32_16x16x32_bf16 v[228:231], v[212:215], v[116:119], v[228:231]\n" \
  "v_cvt_pk_bf16_f32 v233, v236, v237\n" \
  "v_cvt_pk_bf16_f32 v235, v238, v239\n" \
  "v_mfma_f32_16x16x32_bf16 v[224:227], v[200:203], v[120:123], v[224:227]\n" \
  "s_add_u32 %[cnt], %[so], 7*%c[js]+0*%c[ss]\n" \
  "buffer_store_dwordx4 v[232:235], v251, %[rs], %[cnt] offen sc1\n" \
  "v_mfma_f32_16x16x32_bf16 v[228:231], v[216:219], v[120:123], v[228:231]\n" \
  "v_mfma_f32_16x16x32_bf16 v[224:227], v[204:207], v[124:127], v[224:227]\n" \
  "v_mfma_f32_16x16x32_bf16 v[228:231], v[220:223], v[124:127], v[228:231]\n" \
  "v_mfma_f32_16x16x32_bf16 v[232:235], v[192:195], v[180:183], 0\n" \
  "v_mfma_f32_16x16x32_bf16 v[236:239], v[208:211], v[180:183], 0\n" \
  "v_mfma_f32_16x16x32_bf16 v[232:235], v[200:203], v[188:191], v[232:235]\n" \
  "v_cvt_pk_bf16_f32 v224, v224, v225\n" \
  "v_cvt_pk_bf16_f32 v226, v226, v227\n" \
  "v_mfma_f32_16x16x32_bf16 v[236:239], v[216:219], v[188:191], v[236:239]\n" \
  "v_cvt_pk_bf16_f32 v225, v228, v229\n" \
  "v_cvt_pk_bf16_f32 v227, v230, v231\n" \
  "s_waitcnt lgkmcnt(1)\n" \
  "v_mfma_f32_16x16x32_bf16 v[232:235], v[196:199], v[240:243], v[232:235]\n" \
  "s_add_u32 %[cnt], %[so], 3*%c[js]+1*%c[ss]\n" \
  "buffer_store_dwordx4 v[224:227], v251, %[rs], %[cnt] offen sc1\n" \
  "v_mfma_f32_16x16x32_bf16 v[236:239], v[212:215], v[240:243], v[236:239]\n" \
  "s_waitcnt lgkmcnt(0)\n" \
  "v_mfma_f32_16x16x32_bf16 v[232:235], v[204:207], v[244:247], v[232:235]\n" \
  "v_mfma_f32_16x16x32_bf16 v[236:239], v[220:223], v[244:247], v[236:239]\n" \
  "s_nop 4\n" \
  "v_cvt_pk_bf16_f32 v232, v232, v233\n" \
  "v_cvt_pk_bf16_f32 v234, v234, v235\n" \
  "s_nop 1\n" \
  "v_cvt_pk_bf16_f32 v233, v236, v237\n" \
  "v_cvt_pk_bf16_f32 v235, v238, v239\n" \
  "s_add_u32 %[cnt], %[so], 7*%c[js]+1*%c[ss]\n" \
  "buffer_store_dwordx4 v[232:235], v251, %[rs], %[cnt] offen sc1\n" \
  "s_nop 1\n"
#define ST_XB0 \
  "ds_read_b128 v[192:195], v251 offset:24576\n" \
  "ds_read_b128 v[208:211], v251 offset:28672\n" \
  "ds_read_b128 v[196:199], v251 offset:25600\n" \
  "ds_read_b128 v[212:215], v251 offset:29696\n" \
  "ds_read_b128 v[200:203], v251 offset:26624\n" \
  "ds_read_b128 v[216:219], v251 offset:30720\n" \
  "ds_read_b128 v[204:207], v251 offset:27648\n" \
  "ds_read_b128 v[220:223], v251 offset:31744\n" \
  "ds_read_b128 v[240:243], v250 offset:0\n" \
  "ds_read_b128 v[244:247], v250 offset:16384\n" \
  "s_waitcnt lgkmcnt(9)\n" \
  "v_mfma_f32_16x16x32_bf16 v[224:227], v[192:195], v[0:3], 0\n" \
  "s_waitcnt lgkmcnt(8)\n" \
  "v_mfma_f32_16x16x32_bf16 v[228:231], v[208:211], v[0:3], 0\n" \
  "s_waitcnt lgkmcnt(7)\n" \
  "v_mfma_f32_16x16x32_bf16 v[224:227], v[196:199], v[4:7], v[224:227]\n" \
  "s_waitcnt lgkmcnt(6)\n" \
  "v_mfma_f32_16x16x32_bf16 v[228:231], v[212:215], v[4:7], v[228:231]\n" \
  "s_waitcnt lgkmcnt(5)\n" \
  "v_mfma_f32_16x16x32_bf16 v[224:227], v[200:203], v[8:11], v[224:227]\n" \
  "s_waitcnt lgkmcnt(4)\n" \
  "v_mfma_f32_16x16x32_bf16 v[228:231], v[216:219], v[8:11], v[228:231]\n" \
  "s_waitcnt lgkmcnt(3)\n" \
  "v_mfma_f32_16x16x32_bf16 v[224:227], v[204:207], v[12:15], v[224:227]\n" \
  "s_waitcnt lgkmcnt(2)\n" \
  "v_mfma_f32_16x16x32_bf16 v[228:231], v[220:223], v[12:15], v[228:231]\n" \
  "v_mfma_f32_16x16x32_bf16 v[232:235], v[192:195], v[128:131], 0\n" \
  "v_mfma_f32_16x16x32_bf16 v[236:239], v[208:211], v[128:131], 0\n" \
  "v_mfma_f32_16x16x32_bf16 v[232:235], v[200:203], v[136:139], v[232:235]\n" \
  "v_cvt_pk_bf16_f32 v224, v224, v225\n" \
  "v_cvt_pk_bf16_f32 v226, v226, v227\n" \
  "v_mfma_f32_16x16x32_bf16 v[236:239], v[216:219], v[136:139], v[236:239]\n" \
  "v_cvt_pk_bf16_f32 v225, v228, v229\n" \
  "v_cvt_pk_bf16_f32 v227, v230, v231\n" \
  "s_waitcnt lgkmcnt(1)\n" \
  "v_mfma_f32_16x16x32_bf16 v[232:235], v[196:199], v[240:243], v[232:235]\n" \
  "s_add_u32 %[cnt], %[so], 0*%c[js]+0*%c[ss]\n" \
  "buffer_store_dwordx4 v[224:227], v251, %[rs], %[cnt] offen sc1\n" \
  "v_mfma_f32_16x16x32_bf16 v[236:239], v[212:215], v[240:243], v[236:239]\n" \
  "s_waitcnt lgkmcnt(0)\n" \
  "v_mfma_f32_16x16x32_bf16 v[232:235], v[204:207], v[244:247], v[232:235]\n" \
  "v_mfma_f32_16x16x32_bf16 v[236:239], v[220:223], v[244:247], v[236:239]\n" \
  "ds_read_b128 v[240:243], v250 offset:1024\n" \
  "ds_read_b128 v[244:247], v250 offset:17408\n" \
  "v_mfma_f32_16x16x32_bf16 v[224:227], v[192:195], v[64:67], 0\n" \
  "v_mfma_f32_16x16x32_bf16 v[228:231], v[208:211], v[64:67], 0\n" \
  "v_mfma_f32_16x16x32_bf16 v[224:227], v[196:199], v[68:71], v[224:227]\n" \
  "v_cvt_pk_bf16_f32 v232, v232, v233\n" \
  "v_cvt_pk_bf16_f32 v234, v234, v235\n" \
  "v_mfma_f32_16x16x32_bf16 v[228:231], v[212:215], v[68:71], v[228:231]\n" \
  "v_cvt_pk_bf16_f32 v233, v236, v237\n" \
  "v_cvt_pk_bf16_f32 v235, v238, v239\n" \
  "v_mfma_f32_16x16x32_bf16 v[224:227], v[200:203], v[72:75], v[224:227]\n" \
  "s_add_u32 %[cnt], %[so], 4*%c[js]+0*%c[ss]\n" \
  "buffer_store_dwordx4 v[232:235], v251, %[rs], %[cnt] offen sc1\n" \
  "v_mfma_f32_16x16x32_bf16 v[228:231], v[216:219], v[72:75], v[228:231]\n" \
  "v_mfma_f32_16x16x32_bf16 v[224:227], v[204:207], v[76:79], v[224:227]\n" \
  "v_mfma_f32_16x16x32_bf16 v[228:231], v[220:223], v[76:79], v[228:231]\n" \
  "v_mfma_f32_16x16x32_bf16 v[232:235], v[192:195], v[132:135], 0\n" \
  "v_mfma_f32_16x16x32_bf16 v[236:239], v[208:211], v[132:135], 0\n" \
  "v_mfma_f32_16x16x32_bf16 v[232:235], v[200:203], v[140:143], v[232:235]\n" \
  "v_cvt_pk_bf16_f32 v224, v224, v225\n" \
  "v_cvt_pk_bf16_f32 v226, v226, v227\n" \
  "v_mfma_f32_16x16x32_bf16 v[236:239], v[216:219], v[140:143], v[236:239]\n" \
  "v_cvt_pk_bf16_f32 v225, v228, v229\n" \
  "v_cvt_pk_bf16_f32 v227, v230, v231\n" \
  "s_waitcnt lgkmcnt(1)\n" \
  "v_mfma_f32_16x16x32_bf16 v[232:235], v[196:199], v[240:243], v[232:235]\n" \
  "s_add_u32 %[cnt], %[so], 0*%c[js]+1*%c[ss]\n" \
  "buffer_store_dwordx4 v[224:227], v251, %[rs], %[cnt] offen sc1\n" \
  "v_mfma_f32_16x16x32_bf16 v[236:239], v[212:215], v[240:243], v[236:239]\n" \
  "s_waitcnt lgkmcnt(0)\n" \
  "v_mfma_f32_16x16x32_bf16 v[232:235], v[204:207], v[244:247], v[232:235]\n" \
  "v_mfma_f32_16x16x32_bf16 v[236:239], v[220:223], v[244:247], v[236:239]\n" \
  "ds_read_b128 v[240:243], v250 offset:32768\n" \
  "ds_read_b128 v[244:247], v250 offset:49152\n" \
  "v_mfma_f32_16x16x32_bf16 v[224:227], v[192:195], v[16:19], 0\n" \
  "v_mfma_f32_16x16x32_bf16 v[228:231], v[208:211], v[16:19], 0\n" \
  "v_mfma_f32_16x16x32_bf16 v[224:227], v[196:199], v[20:23], v[224:227]\n" \
  "v_cvt_pk_bf16_f32 v232, v232, v233\n" \
  "v_cvt_pk_bf16_f32 v234, v234, v235\n" \
  "v_mfma_f32_16x16x32_bf16 v[228:231], v[212:215], v[20:23], v[228:231]\n" \
  "v_cvt_pk_bf16_f32 v233, v236, v237\n" \
  "v_cvt_pk_bf16_f32 v235, v238, v239\n" \
  "v_mfma_f32_16x16x32_bf16 v[224:227], v[200:203], v[24:27], v[224:227]\n" \
  "s_add_u32 %[cnt], %[so], 4*%c[js]+1*%c[ss]\n" \
  "buffer_store_dwordx4 v[232:235], v251, %[rs], %[cnt] offen sc1\n" \
  "v_mfma_f32_16x16x32_bf16 v[228:231], v[216:219], v[24:27], v[228:231]\n" \
  "v_mfma_f32_16x16x32_bf16 v[224:227], v[204:207], v[28:31], v[224:227]\n" \
  "v_mfma_f32_16x16x32_bf16 v[228:231], v[220:223], v[28:31], v[228:231]\n" \
  "v_mfma_f32_16x16x32_bf16 v[232:235], v[192:195], v[144:147], 0\n" \
  "v_mfma_f32_16x16x32_bf16 v[236:239], v[208:211], v[144:147], 0\n" \
  "v_mfma_f32_16x16x32_bf16 v[232:235], v[200:203], v[152:155], v[232:235]\n" \
  "v_cvt_pk_bf16_f32 v224, v224, v225\n" \
  "v_cvt_pk_bf16_f32 v226, v226, v227\n" \
  "v_mfma_f32_16x16x32_bf16 v[236:239], v[216:219], v[152:155], v[236:239]\n" \
  "v_cvt_pk_bf16_f32 v225, v228, v229\n" \
  "v_cvt_pk_bf16_f32 v227, v230, v231\n" \
  "s_waitcnt lgkmcnt(1)\n" \
  "v_mfma_f32_16x16x32_bf16 v[232:235], v[196:199], v[240:243], v[232:235]\n" \
  "s_add_u32 %[cnt], %[so], 1*%c[js]+0*%c[ss]\n" \
  "buffer_store_dwordx4 v[224:227], v251, %[rs], %[cnt] offen sc1\n" \
  "v_mfma_f32_16x16x32_bf16 v[236:239], v[212:215], v[240:243], v[236:239]\n" \
  "s_waitcnt lgkmcnt(0)\n" \
  "v_mfma_f32_16x16x32_bf16 v[232:235], v[204:207], v[244:247], v[232:235]\n" \
  "v_mfma_f32_16x16x32_bf16 v[236:239], v[220:223], v[244:247], v[236:239]\n" \
  "ds_read_b128 v[240:243], v250 offset:33792\n" \
  "ds_read_b128 v[244:247], v250 offset:50176\n" \
  "v_mfma_f32_16x16x32_bf16 v[224:227], v[192:195], v[80:83], 0\n" \
  "v_mfma_f32_16x16x32_bf16 v[228:231], v[208:211], v[80:83], 0\n" \
  "v_mfma_f32_16x16x32_bf16 v[224:227], v[196:199], v[84:87], v[224:227]\n" \
  "v_cvt_pk_bf16_f32 v232, v232, v233\n" \
  "v_cvt_pk_bf16_f32 v234, v234, v235\n" \
  "v_mfma_f32_16x16x32_bf16 v[228:231], v[212:215], v[84:87], v[228:231]\n" \
  "v_cvt_pk_bf16_f32 v233, v236, v237\n" \
  "v_cvt_pk_bf16_f32 v235, v238, v239\n" \
  "v_mfma_f32_16x16x32_bf16 v[224:227], v[200:203], v[88:91], v[224:227]\n" \
  "s_add_u32 %[cnt], %[so], 5*%c[js]+0*%c[ss]\n" \
  "buffer_store_dwordx4 v[232:235], v251, %[rs], %[cnt] offen sc1\n" \
  "v_mfma_f32_16x16x32_bf16 v[228:231], v[216:219], v[88:91], v[228:231]\n" \
  "v_mfma_f32_16x16x32_bf16 v[224:227], v[204:207], v[92:95], v[224:227]\n" \
  "v_mfma_f32_16x16x32_bf16 v[228:231], v[220:223], v[92:95], v[228:231]\n" \
  "v_mfma_f32_16x16x32_bf16 v[232:235], v[192:195], v[148:151], 0\n" \
  "v_mfma_f32_16x16x32_bf16 v[236:239], v[208:211], v[148:151], 0\n" \
  "v_mfma_f32_16x16x32_bf16 v[232:235], v[200:203], v[156:159], v[232:235]\n" \
  "v_cvt_pk_bf16_f32 v224, v224, v225\n" \
  "v_cvt_pk_bf16_f32 v226, v226, v227\n" \
  "v_mfma_f32_16x16x32_bf16 v[236:239], v[216:219], v[156:159], v[236:239]\n" \
  "v_cvt_pk_bf16_f32 v225, v228, v229\n" \
  "v_cvt_pk_bf16_f32 v227, v230, v231\n" \
  "s_waitcnt lgkmcnt(1)\n" \
  "v_mfma_f32_16x16x32_bf16 v[232:235], v[196:199], v[240:243], v[232:235]\n" \
  "s_add_u32 %[cnt], %[so], 1*%c[js]+1*%c[ss]\n" \
  "buffer_store_dwordx4 v[224:227], v251, %[rs], %[cnt] offen sc1\n" \
  "v_mfma_f32_16x16x32_bf16 v[236:239], v[212:215], v[240:243], v[236:239]\n" \
  "s_waitcnt lgkmcnt(0)\n" \
  "v_mfma_f32_16x16x32_bf16 v[232:235], v[204:207], v[244:247], v[232:235]\n" \
  "v_mfma_f32_16x16x32_bf16 v[236:239], v[220:223], v[244:247], v[236:239]\n" \
  "ds_read_b128 v[240:243], v249 offset:0\n" \
  "ds_read_b128 v[244:247], v249 offset:16384\n" \
  "v_mfma_f32_16x16x32_bf16 v[224:227], v[192:195], v[32:35], 0\n" \
  "v_mfma_f32_16x16x32_bf16 v[228:231], v[208:211], v[32:35], 0\n" \
  "v_mfma_f32_16x16x32_bf16 v[224:227], v[196:199], v[36:39], v[224:227]\n" \
  "v_cvt_pk_bf16_f32 v232, v232, v233\n" \
  "v_cvt_pk_bf16_f32 v234, v234, v235\n" \
  "v_mfma_f32_16x16x32_bf16 v[228:231], v[212:215], v[36:39], v[228:231]\n" \
  "v_cvt_pk_bf16_f32 v233, v236, v237\n" \
  "v_cvt_pk_bf16_f32 v235, v238, v239\n" \
  "v_mfma_f32_16x16x32_bf16 v[224:227], v[200:203], v[40:43], v[224:227]\n" \
  "s_add_u32 %[cnt], %[so], 5*%c[js]+1*%c[ss]\n" \
  "buffer_store_dwordx4 v[232:235], v251, %[rs], %[cnt] offen sc1\n" \
  "v_mfma_f32_16x16x32_bf16 v[228:231], v[216:219], v[40:43], v[228:231]\n" \
  "v_mfma_f32_16x16x32_bf16 v[224:227], v[204:207], v[44:47], v[224:227]\n" \
  "v_mfma_f32_16x16x32_bf16 v[228:231], v[220:223], v[44:47], v[228:231]\n" \
  "v_mfma_f32_16x16x32_bf16 v[232:235], v[192:195], v[160:163], 0\n" \
  "v_mfma_f32_16x16x32_bf16 v[236:239], v[208:211], v[160:163], 0\n" \
  "v_mfma_f32_16x16x32_bf16 v[232:235], v[200:203], v[168:171], v[232:235]\n" \
  "v_cvt_pk_bf16_f32 v224, v224, v225\n" \
  "v_cvt_pk_bf16_f32 v226, v226, v227\n" \
  "v_mfma_f32_16x16x32_bf16 v[236:239], v[216:219], v[168:171], v[236:239]\n" \
  "v_cvt_pk_bf16_f32 v225, v228, v229\n" \
  "v_cvt_pk_bf16_f32 v227, v230, v231\n" \
  "s_waitcnt lgkmcnt(1)\n" \
  "v_mfma_f32_16x16x32_bf16 v[232:235], v[196:199], v[240:243], v[232:235]\n" \
  "s_add_u32 %[cnt], %[so], 2*%c[js]+0*%c[ss]\n" \
  "buffer_store_dwordx4 v[224:227], v251, %[rs], %[cnt] offen sc1\n" \
  "v_mfma_f32_16x16x32_bf16 v[236:239], v[212:215], v[240:243], v[236:239]\n" \
  "s_waitcnt lgkmcnt(0)\n" \
  "v_mfma_f32_16x16x32_bf16 v[232:235], v[204:207], v[244:247], v[232:235]\n" \
  "v_mfma_f32_16x16x32_bf16 v[236:239], v[220:223], v[244:247], v[236:239]\n" \
  "ds_read_b128 v[240:243], v249 offset:1024\n" \
  "ds_read_b128 v[244:247], v249 offset:17408\n" \
  "v_mfma_f32_16x16x32_bf16 v[224:227], v[192:195], v[96:99], 0\n" \
  "v_mfma_f32_16x16x32_bf16 v[228:231], v[208:211], v[96:99], 0\n" \
  "v_mfma_f32_16x16x32_bf16 v[224:227], v[196:199], v[100:103], v[224:227]\n" \
  "v_cvt_pk_bf16_f32 v232, v232, v233\n" \
  "v_cvt_pk_bf16_f32 v234, v234, v235\n" \
  "v_mfma_f32_16x16x32_bf16 v[228:231], v[212:215], v[100:103], v[228:231]\n" \
  "v_cvt_pk_bf16_f32 v233, v236, v237\n" \
  "v_cvt_pk_bf16_f32 v235, v238, v239\n" \
  "v_mfma_f32_16x16x32_bf16 v[224:227], v[200:203], v[104:107], v[224:227]\n" \
  "s_add_u32 %[cnt], %[so], 6*%c[js]+0*%c[ss]\n" \
  "buffer_store_dwordx4 v[232:235], v251, %[rs], %[cnt] offen sc1\n" \
  "v_mfma_f32_16x16x32_bf16 v[228:231], v[216:219], v[104:107], v[228:231]\n" \
  "v_mfma_f32_16x16x32_bf16 v[224:227], v[204:207], v[108:111], v[224:227]\n" \
  "v_mfma_f32_16x16x32_bf16 v[228:231], v[220:223], v[108:111], v[228:231]\n" \
  "v_mfma_f32_16x16x32_bf16 v[232:235], v[192:195], v[164:167], 0\n" \
  "v_mfma_f32_16x16x32_bf16 v[236:239], v[208:211], v[164:167], 0\n" \
  "v_mfma_f32_16x16x32_bf16 v[232:235], v[200:203], v[172:175], v[232:235]\n" \
  "v_cvt_pk_bf16_f32 v224, v224, v225\n" \
  "v_cvt_pk_bf16_f32 v226, v226, v227\n" \
  "v_mfma_f32_16x16x32_bf16 v[236:239], v[216:219], v[172:175], v[236:239]\n" \
  "v_cvt_pk_bf16_f32 v225, v228, v229\n" \
  "v_cvt_pk_bf16_f32 v227, v230, v231\n" \
  "s_waitcnt lgkmcnt(1)\n" \
  "v_mfma_f32_16x16x32_bf16 v[232:235], v[196:199], v[240:243], v[232:235]\n" \
  "s_add_u32 %[cnt], %[so], 2*%c[js]+1*%c[ss]\n" \
  "buffer_store_dwordx4 v[224:227], v251, %[rs], %[cnt] offen sc1\n" \
  "v_mfma_f32_16x16x32_bf16 v[236:239], v[212:215], v[240:243], v[236:239]\n" \
  "s_waitcnt lgkmcnt(0)\n" \
  "v_mfma_f32_16x16x32_bf16 v[232:235], v[204:207], v[244:247], v[232:235]\n" \
  "v_mfma_f32_16x16x32_bf16 v[236:239], v[220:223], v[244:247], v[236:239]\n" \
  "ds_read_b128 v[240:243], v249 offset:32768\n" \
  "ds_read_b128 v[244:247], v249 offset:49152\n" \
  "v_mfma_f32_16x16x32_bf16 v[224:227], v[192:195], v[48:51], 0\n" \
  "v_mfma_f32_16x16x32_bf16 v[228:231], v[208:211], v[48:51], 0\n" \
  "v_mfma_f32_16x16x32_bf16 v[224:227], v[196:199], v[52:55], v[224:227]\n" \
  "v_cvt_pk_bf16_f32 v232, v232, v233\n" \
  "v_cvt_pk_bf16_f32 v234, v234, v235\n" \
  "v_mfma_f32_16x16x32_bf16 v[228:231], v[212:215], v[52:55], v[228:231]\n" \
  "v_cvt_pk_bf16_f32 v233, v236, v237\n" \
  "v_cvt_pk_bf16_f32 v235, v238, v239\n" \
  "v_mfma_f32_16x16x32_bf16 v[224:227], v[200:203], v[56:59], v[224:227]\n" \
  "s_add_u32 %[cnt], %[so], 6*%c[js]+1*%c[ss]\n" \
  "buffer_store_dwordx4 v[232:235], v251, %[rs], %[cnt] offen sc1\n" \
  "v_mfma_f32_16x16x32_bf16 v[228:231], v[216:219], v[56:59], v[228:231]\n" \
  "v_mfma_f32_16x16x32_bf16 v[224:227], v[204:207], v[60:63], v[224:227]\n" \
  "v_mfma_f32_16x16x32_bf16 v[228:231], v[220:223], v[60:63], v[228:231]\n" \
  "v_mfma_f32_16x16x32_bf16 v[232:235], v[192:195], v[176:179], 0\n" \
  "v_mfma_f32_16x16x32_bf16 v[236:239], v[208:211], v[176:179], 0\n" \
  "v_mfma_f32_16x16x32_bf16 v[232:235], v[200:203], v[184:187], v[232:235]\n" \
  "v_cvt_pk_bf16_f32 v224, v224, v225\n" \
  "v_cvt_pk_bf16_f32 v226, v226, v227\n" \
  "v_mfma_f32_16x16x32_bf16 v[236:239], v[216:219], v[184:187], v[236:239]\n" \
  "v_cvt_pk_bf16_f32 v225, v228, v229\n" \
  "v_cvt_pk_bf16_f32 v227, v230, v231\n" \
  "s_waitcnt lgkmcnt(1)\n" \
  "v_mfma_f32_16x16x32_bf16 v[232:235], v[196:199], v[240:243], v[232:235]\n" \
  "s_add_u32 %[cnt], %[so], 3*%c[js]+0*%c[ss]\n" \
  "buffer_store_dwordx4 v[224:227], v251, %[rs], %[cnt] offen sc1\n" \
  "v_mfma_f32_16x16x32_bf16 v[236:239], v[212:215], v[240:243], v[236:239]\n" \
  "s_waitcnt lgkmcnt(0)\n" \
  "v_mfma_f32_16x16x32_bf16 v[232:235], v[204:207], v[244:247], v[232:235]\n" \
  "v_mfma_f32_16x16x32_bf16 v[236:239], v[220:223], v[244:247], v[236:239]\n" \
  "ds_read_b128 v[240:243], v249 offset:33792\n" \
  "ds_read_b128 v[244:247], v249 offset:50176\n" \
  "v_mfma_f32_16x16x32_bf16 v[224:227], v[192:195], v[112:115], 0\n" \
  "v_mfma_f32_16x16x32_bf16 v[228:231], v[208:211], v[112:115], 0\n" \
  "v_mfma_f32_16x16x32_bf16 v[224:227], v[196:199], v[116:119], v[224:227]\n" \
  "v_cvt_pk_bf16_f32 v232, v232, v233\n" \
  "v_cvt_pk_bf16_f32 v234, v234, v235\n" \
  "v_mfma_f32_16x16x32_bf16 v[228:231], v[212:215], v[116:119], v[228:231]\n" \
  "v_cvt_pk_bf16_f32 v233, v236, v237\n" \
  "v_cvt_pk_bf16_f32 v235, v238, v239\n" \
  "v_mfma_f32_16x16x32_bf16 v[224:227], v[200:203], v[120:123], v[224:227]\n" \
  "s_add_u32 %[cnt], %[so], 7*%c[js]+0*%c[ss]\n" \
  "buffer_store_dwordx4 v[232:235], v251, %[rs], %[cnt] offen sc1\n" \
  "v_mfma_f32_16x16x32_bf16 v[228:231], v[216:219], v[120:123], v[228:231]\n" \
  "v_mfma_f32_16x16x32_bf16 v[224:227], v[204:207], v[124:127], v[224:227]\n" \
  "v_mfma_f32_16x16x32_bf16 v[228:231], v[220:223], v[124:127], v[228:231]\n" \
  "v_mfma_f32_16x16x32_bf16 v[232:235], v[192:195], v[180:183], 0\n" \
  "v_mfma_f32_16x16x32_bf16 v[236:239], v[208:211], v[180:183], 0\n" \
  "v_mfma_f32_16x16x32_bf16 v[232:235], v[200:203], v[188:191], v[232:235]\n" \
  "v_cvt_pk_bf16_f32 v224, v224, v225\n" \
  "v_cvt_pk_bf16_f32 v226, v226, v227\n" \
  "v_mfma_f32_16x16x32_bf16 v[236:239], v[216:219], v[188:191], v[236:239]\n" \
  "v_cvt_pk_bf16_f32 v225, v228, v229\n" \
  "v_cvt_pk_bf16_f32 v227, v230, v231\n" \
  "s_waitcnt lgkmcnt(1)\n" \
  "v_mfma_f32_16x16x32_bf16 v[232:235], v[196:199], v[240:243], v[232:235]\n" \
  "s_add_u32 %[cnt], %[so], 3*%c[js]+1*%c[ss]\n" \
  "buffer_store_dwordx4 v[224:227], v251, %[rs], %[cnt] offen sc1\n" \
  "v_mfma_f32_16x16x32_bf16 v[236:239], v[212:215], v[240:243], v[236:239]\n" \
  "s_waitcnt lgkmcnt(0)\n" \
  "v_mfma_f32_16x16x32_bf16 v[232:235], v[204:207], v[244:247], v[232:235]\n" \
  "v_mfma_f32_16x16x32_bf16 v[236:239], v[220:223], v[244:247], v[236:239]\n" \
  "s_nop 4\n" \
  "v_cvt_pk_bf16_f32 v232, v232, v233\n" \
  "v_cvt_pk_bf16_f32 v234, v234, v235\n" \
  "s_nop 1\n" \
  "v_cvt_pk_bf16_f32 v233, v236, v237\n" \
  "v_cvt_pk_bf16_f32 v235, v238, v239\n" \
  "s_add_u32 %[cnt], %[so], 7*%c[js]+1*%c[ss]\n" \
  "buffer_store_dwordx4 v[232:235], v251, %[rs], %[cnt] offen sc1\n" \
  "s_nop 1\n"
#define XBF_NB 5
#define XBS_NB 2
#define VO_CLOB "v192", "v193", "v194", "v195", "v196", "v197", "v198", "v199", "v200", "v201", "v202", "v203", "v204", "v205", "v206", "v207", "v208", "v209", "v210", "v211", "v212", "v213", "v214", "v215", "v216", "v217", "v218", "v219", "v220", "v221", "v222", "v223", "v224", "v225", "v226", "v227", "v228", "v229", "v230", "v231", "v232", "v233", "v234", "v235", "v236", "v237", "v238", "v239"
#define QK_CLOB "v192", "v193", "v194", "v195", "v196", "v197", "v198", "v199", "v200", "v201", "v202", "v203", "v204", "v205", "v206", "v207", "v208", "v209", "v210", "v211", "v212", "v213", "v214", "v215", "v216", "v217", "v218", "v219", "v220", "v221", "v222", "v223", "v224", "v225", "v226", "v227", "v228", "v229", "v230", "v231", "v232", "v233", "v234", "v235", "v236", "v237", "v238", "v239"
#define QX_CLOB "v216", "v217", "v218", "v219", "v220", "v221", "v222", "v223", "v224", "v225", "v226", "v227", "v228", "v229", "v230", "v231", "v232", "v233", "v234", "v235", "v236", "v237", "v238", "v239", "v241"
#define ST_CLOB "v192", "v193", "v194", "v195", "v196", "v197", "v198", "v199", "v200", "v201", "v202", "v203", "v204", "v205", "v206", "v207", "v240", "v241", "v242", "v243", "v244", "v245", "v246", "v247", "v253"
#define XB_CLOB "v192", "v193", "v194", "v195", "v196", "v197", "v198", "v199", "v200", "v201", "v202", "v203", "v204", "v205", "v206", "v207", "v208", "v209", "v210", "v211", "v212", "v213", "v214", "v215", "v216", "v217", "v218", "v219", "v220", "v221", "v222", "v223", "v224", "v225", "v226", "v227", "v228", "v229", "v230", "v231", "v232", "v233", "v234", "v235", "v236", "v237", "v238", "v239", "v240", "v241", "v242", "v243", "v244", "v245", "v246", "v247", "v253"
#define OF_CLOB "v32", "v33", "v34", "v35", "v36", "v37", "v38", "v39", "v40", "v41", "v42", "v43", "v44", "v45", "v46", "v47", "v48", "v49", "v50", "v51", "v52", "v53", "v54", "v55", "v56", "v57", "v58", "v59", "v60", "v61", "v62", "v63", "v64", "v65", "v66", "v67", "v68", "v69", "v70", "v71", "v72", "v73", "v74", "v75", "v76", "v77", "v78", "v79", "v80", "v81", "v82", "v83", "v84", "v85", "v86", "v87", "v88", "v89", "v90", "v91", "v92", "v93", "v94", "v95", "v96", "v97", "v98", "v99", "v100", "v101", "v102", "v103", "v104", "v105", "v106", "v107", "v108", "v109", "v110", "v111"
constexpr int L_W = 32768;
constexpr int L_HI = 16384;
#define MARG 110
#define ED0 720
#define EA0 1290
#define SDK 505
#define TOL 145
#define XLATE 130
#define TFR 900
#define QT 0
#define HSU 8
#define WNT 3
#define KROT 7
#define KSL2 0
#define KSL 5
#define NCY 1860
#define NCYV NCY
#define NRY 24
#define NRX 30
#define NF1O 2
#define NPUB 2
#define NXF 4
#define RSL 2
#define PFL 4
#define EVT2R
__device__ __forceinline__ int slow_head(const u16* wm) { return (int)(((3u - (unsigned)((uintptr_t)wm >> 7)) & 7u) >> 1); }
template <int role>
__device__ __forceinline__ void wroles(const int h, const int par, const int sl, const int xg, const int w, const u64 nonce, const u16* __restrict__ x,
                                       const u16* __restrict__ Wq, const u16* __restrict__ Wk, const u16* __restrict__ Wv,
                                       const u16* __restrict__ Wo, char* ws) {
  LAS char* const L = (LAS char*)g_lds;
  gu32* tmo = (gu32*)(ws + OFF_TMO);
  const __amdgpu_buffer_rsrc_t rs = __builtin_amdgcn_make_buffer_rsrc(ws, 0, (int)WS_USED, 0x00020000);
  constexpr bool vo = role & 1;
  v8u wa[8], wb[16];
  constexpr bool fuse = role >= 2;
  constexpr int RG = fuse ? 4 : 5, RB = fuse ? 32768 : 0;
  f32x4 fac[2][2];
  v2u nin = {0u, 0u}, hsv;
  unsigned arm = 0;
  v8u t13;
  PLDECL;
  const int krot = h & KROT;
  {
    const int lane = olane();
    const int l15 = lane & 15, g = lane >> 4, trq = l15 >> 2, trp = lane & 3;
    const char* bl = (const char*)((vo ? Wv : Wq) + h * DH);
    const char* br = (const char*)(vo ? Wo + (size_t)h * DH * D : Wk + h * DH);
    const int rot = role == 1 ? (h >> 4) * 4 : 0;
    int vl[2], vr[2];
#pragma unroll
    for (int q = 0; q < 2; ++q) {
      const int row = 4 * (2 * w + q) + g;
      const int cb = ((((l15 >> 1) ^ ((row & 3) | ((row >> 1) & 4))) << 5) | ((l15 & 1) << 4));
      const int i = row & 7;
      const int prow = (row & 32) + ((i & 4) << 2) + ((row >> 1) & 12) + (i & 3);
      vl[q] = row * 8192 + cb;
      vr[q] = vo ? (role == 1 ? row : prow) * 2048 + cb : vl[q];
    }
#define W_ISSUE(u)                                                                                   \
  {                                                                                                  \
    const char* pl = bl + (size_t)((u) ^ krot) * 64 * 8192;                                          \
    const char* pr = br + (role == 1 ? (size_t)(((u) >> 1) & 1) * 64 * 2048 + (2 * ((u) >> 2) + ((u) & 1)) * 256          \
                                     : (vo ? (size_t)((u) & 1) * 64 * 2048 + ((u) >> 1) * 256 : (size_t)((u) ^ krot) * 64 * 8192)); \
    _Pragma("unroll") for (int q = 0; q < 2; ++q) {                                                  \
      glds16<((WNT >> (role >> 1)) & 1) != 0>(pl, vl[q], RB + ((u) % RG) * 32768 + (2 * w + q) * 1024);                                \
      glds16<((WNT >> (role >> 1)) & 1) != 0>(pr, vr[q], RB + ((u) % RG) * 32768 + 16384 + (2 * w + q) * 1024);                        \
    }                                                                                                \
  }
    const int ta = role == 1 ? 8 + (w ^ rot) : 2 * w, tb = role == 1 ? 2 * (w & 3) : 2 * w + 1;
    const int sw = trq | ((g & 1) << 2);
    int pa = (ta >> 3) * 16384 + (8 * g + trq) * 256 + (((ta & 7) ^ sw) << 5) + trp * 8;
    int pb = (tb >> 3) * 16384 + (8 * g + trq) * 256 + (((tb & 7) ^ sw) << 5) + trp * 8;
    const int pw = L_W + w * 2048 + lane * 16;
#define W_FRAG(dst, p, u)                                                                            \
  {                                                                                                  \
    int p_ = p + RB + ((u) % RG) * 32768;                                                             \
    asm volatile("" : "+v"(p_));                                                                     \
    const v2u t0 = trd(L + p_), t1 = trd(L + p_ + 1024), t2 = trd(L + p_ + 8192), t3 = trd(L + p_ + 9216); \
    dst = v8u{t0[0], t0[1], t1[0], t1[1], t2[0], t2[1], t3[0], t3[1]};                               \
  }
#define W_PUT(c)                                           \
  {                                                        \
    int p_ = pw + ((c) - 8) * 16384;                       \
    asm volatile("" : "+v"(p_));                           \
    *(LAS v4u*)(L + p_) = lo4(tmp[(c) - 8]);               \
    *(LAS v4u*)(L + p_ + 1024) = hi4(tmp[(c) - 8]);        \
  }
    PL(0);
    const int fxv = (16 * (w & 1) + (lane >> 2)) * 2048 + (((lane ^ -g) & 3) << 4) + ((w >> 1) & 1) * 64;
    const int fxr = l15 * 64 + (((g ^ -trq) & 3) << 4);
#define X_ISSUE(j) glds16((const char*)x + par * 65536 + ((2 * (j) + (w >> 2)) ^ krot) * 128, fxv, (((j) + 1) & 3) * 8192 + w * 1024)
#define HWU 99
#define W_HELP(u)                                                                                                     \
  if (fuse && w >= 5) {                                                                                               \
    const int h0 = slow_head(Wq), rk = (h - h0 - 1) & 3;                                                              \
    const int q = (((role & 1) * 4 + par * 2 + ((h >> 2) & 1)) * 3 + rk) * 3 + w - 5, m = q / 24;                     \
    const u16* wm = m == 2 ? Wv : (m ? Wk : Wq);                                                                      \
    const int hs = (h & 24) + slow_head(wm) + 4 * ((q / 12) & 1);                                                     \
    if (rk < 3)                                                                                                       \
      touch4((const char*)wm + hs * 256 + (size_t)((15 - (q % 12 >> 1)) ^ (hs & KROT)) * 524288,                      \
             ((q & 1) * 32 + (lane >> 1)) * 8192 + (lane & 1) * 128, RB + ((u) % RG) * 32768 + 2 * w * 1024);          \
  }
    if (HWU == -1) W_HELP(0)
    W_ISSUE(0);
    if (fuse) {
#pragma unroll
      for (int j = 0; j < 4; ++j) X_ISSUE(j);
    }
#pragma unroll
    for (int u = 1; u < RG; ++u) W_ISSUE(u);
    PL(1);
    v8u tmp[8];
#pragma unroll
    for (int u = 0; u < 16; ++u) {
      {
        int young = 4 * (u + RG - 1 < 16 ? RG - 1 : 15 - u);
        if (fuse)
          for (int i = u - RG; i < u; ++i) young += i >= 0 && i < 8 && (i & 1);
        if (u > HSU && u < HSU + RG) young += 1;
        if (role < 2 && u > 13) young += 3;
        if (VFAR && role == 3 && u > 12 && u < 15) young += 2;
        asm volatile("s_waitcnt vmcnt(%0)" ::"n"(young) : "memory");
      }
      RAW_BARRIER();
      if ((u & 3) == 0 || u == 15) PL(2 + (u + 1) / 4);
      if constexpr (role == 1) {
#define WO_(n) ((n) < 8 ? wa[(n) & 7] : ((n) < 10 ? wb[14 + ((n) & 1)] : tmp[(n) >= 10 ? (n) - 10 : 0]))
#define WV_(n) ((n) < 14 ? wb[(n) < 14 ? (n) : 0] : tmp[6 + ((n) & 1)])
        if (u == 12) { W_PUT(8); W_PUT(9); }
        W_FRAG(WO_(u), pa, u)
        if ((w >> 2) == (u >> 3)) {
          W_FRAG(WV_(2 * (u & 7)), pb, u)
          W_FRAG(WV_(2 * (u & 7) + 1), (pb ^ 32), u)
        }
        if (u & 1) {
          v8u& f0 = WO_(u & ~1);
          v8u& f1 = WO_(u);
          const v8u n0 = {f0[0], f1[0], f0[1], f1[1], f0[2], f1[2], f0[3], f1[3]};
          const v8u n1 = {f0[4], f1[4], f0[5], f1[5], f0[6], f1[6], f0[7], f1[7]};
          f0 = n0;
          f1 = n1;
          asm volatile("" : "+v"(f0), "+v"(f1));
        }
        if (u == 13) { W_PUT(10); W_PUT(11); }
        if (u == 15) { W_PUT(12); W_PUT(13); W_PUT(14); W_PUT(15); }
      } else if constexpr (role == 0) {
        if (u == 12) { W_PUT(8); W_PUT(9); }
        if (u == 13) { W_PUT(10); W_PUT(11); }
        if (u == 14) { W_PUT(12); W_PUT(13); }
        if (u == 15) { W_PUT(14); }
        if (u < 8) W_FRAG(wa[u < 8 ? u : 0], pa, u) else W_FRAG(tmp[u >= 8 ? u - 8 : 0], pa, u)
        W_FRAG(wb[u], pb, u)
        if (u == 15) W_PUT(15);
      } else {
        if (u == 15 && role == 2 && w == 0) hs_wait(rs, nonce, hsv);
        if (u >= 13) W_PUT(u >= 13 ? 2 * u - 18 : 8);
        if (u == 13 || u == 14) W_PUT(u >= 13 ? 2 * u - 17 : 8);
        v8u fa, fb;
        W_FRAG(fa, pa, u)
        W_FRAG(fb, pb, u)
        if (u < 8) {
          wa[u & 7] = fa;
          wb[u] = fb;
        } else {
          wb[u] = cat8(lo4(fa), lo4(fb));
          tmp[u & 7] = cat8(hi4(fa), hi4(fb));
        }
        if (role == 2 || w < 4) {
#pragma unroll
          for (int hf = 0; hf < 2; ++hf)
#pragma unroll
            for (int rt = 0; rt < 2; ++rt) {
              const v4u xq = *(const LAS v4u*)(L + fxr + ((u + 2) & 7) * 4096 + hf * 2048 + rt * 1024);
              fac[0][rt] = mma(hf ? hi4(fa) : lo4(fa), xq, u + hf == 0 ? f32x4{0.f, 0.f, 0.f, 0.f} : fac[0][rt]);
              fac[1][rt] = mma(hf ? hi4(fb) : lo4(fb), xq, u + hf == 0 ? f32x4{0.f, 0.f, 0.f, 0.f} : fac[1][rt]);
            }
          asm volatile("" : "+v"(fac[0][0]), "+v"(fac[0][1]), "+v"(fac[1][0]), "+v"(fac[1][1]));
          if (u == 15) {
            PL(16);
            const int l16 = olane() * 16;
#pragma unroll
            for (int rt = 0; rt < 2; ++rt) {
              const v4u o = {pk2(fac[0][rt][0], fac[0][rt][1]), pk2(fac[0][rt][2], fac[0][rt][3]), pk2(fac[1][rt][0], fac[1][rt][1]),
                             pk2(fac[1][rt][2], fac[1][rt][3])};
              if (role == 3) *(LAS v4u*)(L + 24576 + rt * 4096 + w * 1024 + l16) = o;
              else st128(o, rs, l16, qk_addr(w >> 2, par, h) + rt * QK_RT + (w & 3) * 1024);
            }
            if (role == 2) {
              W_PUT(13);
              VMCNT(0);
            }
          }
        } else if (u == 15) {
          W_PUT(13);
          if (w == 7) {
            const bool ok = hs_wait(rs, nonce, hsv);
            if (VFAR) {
              if (__builtin_expect(!ok || !__all((nin[0] != 0xffffffffu) & (nin[1] != 0xffffffffu)), 0)) nin = numa_load(rs, tmo);
              *(volatile LAS int*)(L + L_W + 5 * 16384) = numa_far(nin);
            }
          }
        }
      }
      if (u == HSU) hsv = __builtin_amdgcn_raw_buffer_load_b64(rs, w == (role == 3 ? 7 : 0) ? 0 : OOB, OFF_ACK + (int)BIDX * 8, AUX_SC1);
      if (role < 2 && u == 13) {
        const int a = w == 0 ? olane() * 4 : OOB;
        arm = __builtin_amdgcn_raw_buffer_load_b32(rs, a & OOB, CNT_ARM, AUX_SC1);
        nin[0] = __builtin_amdgcn_raw_buffer_load_b32(rs, a, (int)OFF_CAL, AUX_SC1);
        nin[1] = __builtin_amdgcn_raw_buffer_load_b32(rs, (a & OOB) | (((a >> 5) & (NXC - 1)) * 4), (int)OFF_XID, AUX_SC1);
      }
      if (VFAR && role == 3 && u == 12) {
        const int a = w == 7 ? olane() * 4 : OOB;
        nin[0] = __builtin_amdgcn_raw_buffer_load_b32(rs, a, (int)OFF_CAL, AUX_SC1);
        nin[1] = __builtin_amdgcn_raw_buffer_load_b32(rs, (a & OOB) | (((a >> 5) & (NXC - 1)) * 4), (int)OFF_XID, AUX_SC1);
      }
      if (u + RG < 16) {
        RAW_BARRIER();
        if (HWU == u) W_HELP(u + RG)
        W_ISSUE(u + RG);
        if (fuse && u < 8 && (u & 1)) X_ISSUE(4 + (u >> 1));
      }
    }
    PL(17);
    RAW_BARRIER();
    PL(18);
    if (VFAR && role == 3 && w >= 4) arm = __builtin_amdgcn_readfirstlane(*(volatile LAS int*)(L + L_W + 5 * 16384));
    if (role == 2 && w == 0) __builtin_amdgcn_raw_buffer_store_b32(0u, rs, olane() << 28, fqk(h, par), AUX_SC1);
    if constexpr (fuse) {
      if (role == 2 || w < 4) {
        constexpr int B0 = role == 3 ? 6 : 8, PER = role == 3 ? 2 : 4, SL = role == 3 ? 12288 : 16384;
        const int ln = olane();
        const int xv = (16 * (w & 1) + (ln >> 2)) * 2048 + (((ln ^ -(ln >> 4)) & 3) << 4) + ((w >> 1) & 1) * 1024 + (role == 3 ? 0 : (w >> 2) * 64);
#pragma unroll
        for (int k = 0; k < 2; ++k)
#pragma unroll
          for (int j = 0; j < B0 / PER; ++j)
            glds16((const char*)x + (2 + par) * 65536 + (((k * B0 + j * PER) / 2 * 64) ^ (krot * 128)), xv, k * SL + j * PER * 2048 + w * 1024);
        PL(19);
        if (role == 3) t13 = tmp[5];
      }
      W_PUT(14);
      W_PUT(15);
    }
  }
  const int lane = olane();
  const int l15 = lane & 15, g = lane >> 4, trq = l15 >> 2, trp = lane & 3;
  int pw0 = L_W + w * 2048 + lane * 16, pw1 = pw0 + 65536;
  asm volatile("" : "+v"(pw0), "+v"(pw1));
#define WA_P(c) (L + ((c) < 12 ? pw0 : pw1) + ((c) & 3) * 16384)
#define WA_LDS(c) cat8(*(const LAS v4u*)WA_P(c), *(const LAS v4u*)(WA_P(c) + 1024))
  const int r_a = (int)(((uintptr_t)ws >> 19) & 7);
  const int r_s = r_a + (int)(OFF_STEP / UNIT);

  typedef int v4i __attribute__((ext_vector_type(4)));
#define WPIN "{v[0:31]}"(W0), "{v[32:63]}"(W1), "{v[64:95]}"(W2), "{v[96:127]}"(W3), "{v[128:159]}"(W4), "{v[160:191]}"(W5)
  const v4u rsv = {(unsigned)(uintptr_t)ws, (unsigned)((uintptr_t)ws >> 32) & 0xffffu, (unsigned)WS_USED, 0x00020000u};
  if constexpr (role >= 2) {
    constexpr bool isx = role == 3;
    const v32u W0 = cat32(wa[0], wa[1], wa[2], wa[3]), W1 = cat32(wa[4], wa[5], wa[6], wa[7]), W2 = cat32(wb[0], wb[1], wb[2], wb[3]);
    const v32u W3 = cat32(wb[4], wb[5], wb[6], wb[7]), W4 = cat32(wb[8], wb[9], wb[10], wb[11]), W5 = cat32(wb[12], wb[13], wb[14], wb[15]);
    const v4u rxv = {(unsigned)(uintptr_t)x, (unsigned)((uintptr_t)x >> 32) & 0xffffu, (unsigned)(S * D * 2), 0x00020000u};
    v4i adr;
    adr[3] = lane * 16;
    adr[2] = pw0;
    adr[1] = pw1;
    adr[0] = (16 * (w & 1) + (lane >> 2)) * 2048 + (((lane ^ -g) & 3) << 4) + ((w >> 1) & 1) * 1024 + (isx ? 0 : (w >> 2) * 64);
    const int xr = l15 * 64 + (((g ^ -trq) & 3) << 4);
#define FOF(sg) (isx ? fvw(w & 3, h, sg) : fqk(h, sg))
    const int nrel = isx ? TN(8, NRX) : TN(7, NRY);
    const int nit = par ? nrel : NSEG - nrel;
#define SEGOF(i) ((i) < nrel ? 2 * (i) + par : (i) + nrel)
#define ST_FLAG(sg)                                                                                                   \
  {                                                                                                                   \
    VMCNT(0);                                                                                                         \
    RAW_BARRIER();                                                                                                    \
    if (w == 0) __builtin_amdgcn_raw_buffer_store_b32(0u, rs, olane() << 28, FOF(sg), AUX_SC1);               \
  }
#define XB_OPS(sg, sp)                                                                                                  \
  : [cnt] "=&s"(cnt) EVOUT                                                                                              \
  : WPIN, "{v[248:251]}"(adr), [rs] "s"(rsv), [so] "s"(vw_addr(vfr, 2 * (w - 4), sg, h >> 2) + (h & 3) * 1024),               \
    [js] "i"(VRING * 262144), [ss] "i"(VFAR ? 65536 : 32768), [fo] "s"(FOF(sp))                                                 \
  : XB_CLOB, "scc", "memory"
#define XB_FLAG(sg) __builtin_amdgcn_raw_buffer_store_b8(0, rs, olane() << 28, FOF(sg), AUX_SC1)
    PL(7);
    if (isx && w >= 4) {
      const int vfr = (__builtin_amdgcn_readfirstlane(arm) ^ (VFAR == 2 ? 8192 : 0)) | (r_a << 19);
      {
        EVDECL;
        unsigned cnt;
        PL(20);
        asm volatile(ST_XBS XB_OPS(par, par));
        PL(21);
      }
      int it = 1;
#pragma unroll 1
      for (;;) {
        VMCNT(0);
        XB_FLAG(SEGOF(it - 1));
        if (it == 1) { PL(9); PLDUMP(4, 0x300200); }
        if (it == 2) PTW(14, 4);
        if (it == 3) PTW(15, 4);
        for (int k = it == 1 ? XBS_NB - 1 : XBF_NB; k < 6; ++k) BARE_BARRIER();
        if (++it > NXF) break;
        EVDECL;
        unsigned cnt;
        asm volatile(ST_XBF XB_OPS(SEGOF(it - 1), par));
        EVT2R
      }
#pragma unroll 1
      for (; it < nit; ++it) {
        EVDECL;
        unsigned cnt;
        asm volatile(ST_XB XB_OPS(SEGOF(it - 1), SEGOF(it - 2)));
        EVT2R
      }
      BARE_BARRIER();
      {
        EVDECL;
        unsigned cnt;
        asm volatile(ST_XB0 XB_OPS(SEGOF(nit - 1), par));
      }
      BARE_BARRIER();
      VMCNT(0);
      XB_FLAG(SEGOF(nit - 2));
      XB_FLAG(SEGOF(nit - 1));
      return;
    }
    if (w == 1) cnt_inc(ws, CNT_HS);
    VMCNT(0);
    RAW_BARRIER();
    if (isx) {
      *(LAS v4u*)(L + pw1 + 16384) = lo4(t13);
      *(LAS v4u*)(L + pw1 + 17408) = hi4(t13);
    }
    PL(10);
    PLDUMP(0, 0xf05ff);
    {
      v32u xf;
#pragma unroll
      for (int q = 0; q < 8; ++q) {
        const v4u t = *(const LAS v4u*)(L + xr + q * 1024);
#pragma unroll
        for (int j = 0; j < 4; ++j) xf[4 * q + j] = t[j];
      }
#pragma unroll 1
      for (int it = 1; it < nit; ++it) {
        EVDECL;
        const int seg = SEGOF(it);
        unsigned cnt, keep;
#define ST_OPS                                                                                                                 \
  : [cnt] "=&s"(cnt), [keep] "=&s"(keep), "+{v[208:239]}"(xf) EVOUT                                                            \
  : WPIN, "{v[248:251]}"(adr), "{v252}"(xr), [rs] "s"(rsv), [rx] "s"(rxv), [sc] "s"(seg * 65536 + krot * 128),                    \
    [sn] "s"(SEGOF(min(it + 1, nit - 1)) * 65536 + krot * 128),                                                                  \
    [mw] "s"(w * 1024), [so] "s"(isx ? w * 1024 : qk_addr(w >> 2, seg, h) + (w & 3) * 1024), [rt] "i"(QK_RT), \
    [fo] "s"(FOF(SEGOF(it - 1))), [fm] "s"(w == 0 ? 0 : 1 << 30), [tm] "s"(w == 3 ? 0 : 1 << 30),            \
    [tc] "s"(min(SEGOF(it + 2), NSEG - 1) * 65536 + sl * 8192)                                                                   \
  : ST_CLOB, "scc", "memory"
        if constexpr (isx) asm volatile(ST_XA ST_OPS);
        else asm volatile(ST_Y ST_OPS);
        EVT2R
        if (it < 4) PTW(10 + it, 2);
        if (!isx && it < NPUB) ST_FLAG(seg)
      }
    }
    if (isx) {
      RAW_BARRIER();
      BARE_BARRIER();
    } else ST_FLAG(SEGOF(nit - 1))
    return;
  }

#define KF0 4
  volatile LAS unsigned* sword = (volatile LAS unsigned*)(L + L_HI + 6408);
  if (w == 0) {
    const bool ok = hs_wait(rs, nonce, hsv);
    cnt_inc(ws, CNT_HS);
    if (__builtin_expect(!ok || !__all((arm + 1u >= 64u) & (nin[0] != 0xffffffffu) & (nin[1] != 0xffffffffu)), 0)) {
      arm_wait(rs, tmo);
      nin = numa_load(rs, tmo);
    }
    sword[0] = numa_perm(nin, (LAS unsigned*)(L + L_HI + 6656));
    sword[1] = 0u;
  }
  Clk ck = {0u, 0u, 92500u, 46432u};
  PL(7);
  RAW_BARRIER();
  PL(8);
  PLDUMP(0, 0xf01ff);
  const unsigned perm = __builtin_amdgcn_readfirstlane(sword[0]);
  const int cown = cls_of(perm, xg);
  const int l16 = lane * 16;
  const int sidx = ((l15 & 2) ? 0xeeee : 0x4444) | ((l15 & 8) ? 0xeeee0000 : 0x44440000);
  LAS char* const sm = L + l16;
  constexpr int TWV = 5;
  typedef int v8i __attribute__((ext_vector_type(8)));
  const int mg_ = NS2F(TN(0, MARG)), tol_ = NS2F(TN(13, TOL)), xl_ = NS2F(TN(14, XLATE));
  Pace pc = {0u, NS2F(ED0), NS2F(EA0), 0};
  unsigned tg = 0, tw = 0;
#define MEM_OFF(c) (R_MEM + ((c) >> 2) * 32768 + ((c) & 3) * 1024)
#define TLB_TOUCH                    \
  if (((r_s + s + 2) & 3) == 0)      \
    tch = __builtin_amdgcn_raw_buffer_load_b32(rs, 0, so + 2 * (int)UNIT + R_DUM + (role * 32 + h) * 128, AUX_SC1);
#define TQOUT
#define TQACC
#define ST_ADR (lst * 32 + 32768 + 2048)
#define TIMING_WAVE                                                                                  \
  {                                                                                                  \
    if (__builtin_expect(anysent(st[0]), 0)) _Pragma("clang loop unroll(disable)") for (unsigned spins = 0;;) { \
      if (spins) __builtin_amdgcn_s_sleep(4);                                                        \
      if (spin_fail(spins, tmo, 0x30009)) break;                                                     \
      const int lst = olane();                                                                       \
      OPQ(va, ST_ADR - 2048);                                                                        \
      st = __builtin_amdgcn_raw_buffer_load_b64(rs, va, so + R_ST + ((cown ^ xr) << 12), AUX_SC1);   \
      if (!anysent(st[0])) break;                                                                       \
    }                                                                                                \
    TQACC                                                                                            \
    const int dm = stamp_max(ck, st[0]);                                                             \
    const unsigned rf = (ck.r << 4) + dm;                                                            \
    pace_step(pc, s, rf, st[1], mg_, tol_, xl_);                                                     \
    EV(6);                                                                                           \
    EVDUMP(s);                                                                                       \
    EVCLK(s, ck);                                                                                    \
  }
#define TIMING_GO                                                                                    \
  if (w == TWV && s > 0) {                                                                           \
    wait_cyc(clk_cyc(ck, (int)(pc.rf - (ck.r << 4)) + pc.dq) - TFR);                                 \
    clk_update(ck, s == 1);                                                \
    tg = clk_cyc(ck, (int)(pc.rf - (ck.r << 4)) + pc.dq);                                            \
    tw = (unsigned)max(CMAX - (int)(tg - ck.m), 0);                                                  \
  }

  if constexpr (role == 0) {
    const v32u W0 = cat32(wa[0], wa[1], wa[2], wa[3]), W1 = cat32(wa[4], wa[5], wa[6], wa[7]), W2 = cat32(wb[0], wb[1], wb[2], wb[3]);
    const v32u W3 = cat32(wb[4], wb[5], wb[6], wb[7]), W4 = cat32(wb[8], wb[9], wb[10], wb[11]), W5 = cat32(wb[12], wb[13], wb[14], wb[15]);
    v8i adr;
    adr[7] = l16;
    adr[6] = l16 + w * 1024;
    adr[5] = pw0;
    adr[4] = sidx;
    adr[3] = L_HI + (w >> 2) * 3072 + (32 * (w & 3) + l15) * 24 + 4 * g;
    adr[2] = (4 * g + trq) * 24 + (trp & 1) * 8;
#define CORD(i) ((((i) & 1) ? (i) >> 1 : 8 + ((i) >> 1)) ^ krot)
    const int mo0 = MEM_OFF(CORD(w));
#define K_STAGE(sn)                                                                                                   \
  {                                                                                                                   \
    v4u kn[4];                                                                                                        \
    _Pragma("clang loop unroll(disable)") for (unsigned spins = 0; krdy <= (sn);) {    \
      OPQ(vfl, fqk(h, min((sn) + (olane() & 15), NSEG - 1)));                                           \
      const u64 miss = __builtin_amdgcn_ballot_w64(__builtin_amdgcn_raw_buffer_load_b32(rs, vfl, 0, AUX_SC1) != 0u);  \
      krdy = (sn) + (miss ? __builtin_ctzll(miss) : 16);                                                              \
      if (krdy > (sn)) break;                                                                                         \
      __builtin_amdgcn_s_sleep(8);                                                                                    \
      if (spin_fail(spins, tmo, 0x30000)) break;                                                                      \
    }                                                                                                                 \
    const int lk = olane() * 16;                                                                                      \
    _Pragma("unroll") for (int q = 0; q < 4; ++q) kn[q] = __builtin_amdgcn_raw_buffer_load_b128(                      \
        rs, lk + q * 1024, qk_addr(1, sn, h) + (w & 1) * QK_RT, AUX_SC1);                 \
    _Pragma("unroll") for (int q = 0; q < 4; ++q) *(LAS v4u*)(L + L_HI + 8192 + (w & 1) * 4096 + q * 1024 + lk) = kn[q]; \
  }
    int krdy = 1;
    const bool kst = w >= 4 && ((w ^ TWV) & 2);
    PTW(9, 7);
    if (kst) {
      wait_flag<KF0>(rs, fqk(h, 0), tmo, 0x30001);
      sword[1] = 1u;
      K_STAGE(0)
    }
    PTW(10, 7);
    if (w == TWV) {
      _Pragma("clang loop unroll(disable)") for (unsigned spins = 0; !__builtin_amdgcn_readfirstlane(sword[1]) && spins < kSpinCap; ++spins)
        __builtin_amdgcn_s_sleep(2);
      clk_edge(ck.r, ck.m, 2);
    }
    if (w < 2) {
      const int lane = olane(), l15 = lane & 15, l16 = lane * 16;
      const bool up = lane >= 32;
      adr[1] = R_EX + (h >> 3) * 32768 + (h & 7) * 512 + w * 256 + (lane & 32) * 4 + (lane & 7) * 16 + (lane & 24) / 2;
      adr[0] = up ? R_PM + (h >> 3) * 1024 + (l15 + 16 * ((h & 7) >> 1)) * 16 + (h & 1) * 8 + (lane & 16) / 4
                  : (lane < 8 ? R_SC + (h >> 2) * 128 + lane * 16 + (2 * (h & 1) + ((h >> 1) & 1)) * 4 : OOB);
      const unsigned msk = up ? 0x7fffffffu : ((lane & 1) ? 0x7fff0000u : 0xffffu);
      const int kxo = L_HI + 8192 + w * 4096 + l16;
#pragma unroll 1
      for (int s = 0; s < NSEG; ++s) {
        EVDECL;
        const int so = (int)OFF_STEP + s * (int)UNIT, xr = xreg(r_s + s);
#define CG(r) (so + ((cls_of(perm, (r) ^ (2 * w)) ^ xr) << 12))
        unsigned cnt;
        u64 wd;
        asm volatile(QK_A
                     : [cnt] "=&s"(cnt), [wd] "=&s"(wd) EVOUT
                     : WPIN, "{v[244:251]}"(adr), "{v243}"(pw1), "{v242}"(kxo), "{v240}"(msk), [rs] "s"(rsv),
                       [o0] "s"(so + mo0 + ((cown ^ xr) << 12)), [c0] "s"(CG(0)), [c1] "s"(CG(1)), [c2] "s"(CG(2)), [c3] "s"(CG(3)),
                       [hf] "s"(0xff00ff00ff00ff00ull), [up] "s"(0xffffffff00000000ull)
                     : QK_CLOB, "v241", "vcc", "scc", "memory");
        if (s < 4) PT(11 + s);
        EVDUMP(s);
      }
    } else if (w < 4) {
      const float scale = 0.08838834764831845f, L2E = 1.4426950408889634f;
      adr[0] = adr[1] = 0;
#pragma unroll 1
      for (int s = 0; s < NSEG; ++s) {
        {
          EVDECL;
          const int so = (int)OFF_STEP + s * (int)UNIT, xr = xreg(r_s + s);
          unsigned cnt;
          v16u qv;
          f32x4 sc[3];
          asm volatile(QK_X
                       : [cnt] "=&s"(cnt), "={v[200:215]}"(qv), "={v[192:195]}"(sc[0]), "={v[196:199]}"(sc[1]) EVOUT
                       : WPIN, "{v[244:251]}"(adr), "{v243}"(pw1), [rs] "s"(rsv), [o0] "s"(so + mo0 + ((cown ^ xr) << 12)),
                         [qo] "s"(qk_addr(0, s, h) + (w - 2) * QK_RT)
                       : QX_CLOB, "vcc", "scc", "memory");
          const int ln = olane(), l15 = ln & 15, g = ln >> 4, l16 = ln * 16;
          v4u qx[4];
#pragma unroll
          for (int ks = 0; ks < 4; ++ks) qx[ks] = v4u{qv[4 * ks], qv[4 * ks + 1], qv[4 * ks + 2], qv[4 * ks + 3]};
          LAS char* const q_rd = L + L_HI + adr[2];
#define QFRAG(o, ks) [&] { const v2u tx = trd(q_rd + (o) + (ks) * 768), ty = trd(q_rd + (o) + (ks) * 768 + 384); return v4u{tx[0], tx[1], ty[0], ty[1]}; }()
          sc[2] = f32x4{0.f, 0.f, 0.f, 0.f};
#pragma unroll
          for (int ks = 0; ks < 4; ++ks) sc[2] = mma(QFRAG(3072, ks), qx[ks], sc[2]);
          EV(4);
          const int tok = 16 * (w - 2) + l15 - 4 * g;
#pragma unroll
          for (int t = 0; t < 3; ++t)
#pragma unroll
            for (int j = 0; j < 4; ++j) {
              float v = rbf(mul1(rbf(sc[t][j]), scale));
              if (t < 2 && 16 * t + j > tok) v = -1000000000.0f;
              sc[t][j] = v;
            }
          float mx;
          asm("v_max3_f32 %0, %1, %2, %3\n\tv_max3_f32 %0, %0, %4, %5\n\tv_max3_f32 %0, %0, %6, %7\n\tv_max3_f32 %0, %0, %8, %9\n\t"
              "v_max3_f32 %0, %0, %10, %11\n\tv_max_f32 %0, %0, %12\n\ts_nop 1"
              : "=&v"(mx)
              : "v"(sc[0][0]), "v"(sc[0][1]), "v"(sc[0][2]), "v"(sc[0][3]), "v"(sc[1][0]), "v"(sc[1][1]), "v"(sc[1][2]), "v"(sc[1][3]),
                "v"(sc[2][0]), "v"(sc[2][1]), "v"(sc[2][2]), "v"(sc[2][3]));
          mx = xmax16_32(mx);
          const float nmx = -mx * L2E;
#pragma unroll
          for (int t = 0; t < 3; ++t)
#pragma unroll
            for (int j = 0; j < 4; ++j) sc[t][j] = __builtin_amdgcn_exp2f(__builtin_fmaf(sc[t][j], L2E, nmx));
          const f32x4 s4 = sc[0] + sc[1] + 0.5f * sc[2];
          const float inv = __builtin_amdgcn_rcpf(xsum16_32((s4[0] + s4[1]) + (s4[2] + s4[3])));
#pragma unroll
          for (int t = 0; t < 3; ++t) sc[t] *= inv;
          const v4u o = {pk2(sc[0][0], sc[0][1]), pk2(sc[1][0], sc[1][1]), pk2(sc[0][2], sc[0][3]), pk2(sc[1][2], sc[1][3])};
          const v2u o2 = {pk2(sc[2][0], sc[2][1]), pk2(sc[2][2], sc[2][3])};
          const int pb = so + R_PX + (w - 2) * 40960;
          st128(o, rs, l16, pb + h * 1024);
          __builtin_amdgcn_raw_buffer_store_b64(o2, rs, g >= 2 ? l15 * 16 + (g & 1) * 8 : OOB,
                                                pb + (32 + (h >> 2)) * 1024 + (h & 3) * 256, AUX_SC1);
          EV(6);
          EVDUMP(s);
        }
      }
    } else {
      const int mo1 = MEM_OFF(CORD(w + 4)), mo2 = MEM_OFF(CORD(w + 8));
      adr[0] = 0;
#pragma unroll 1
      for (int s = 0; s < NSEG; ++s) {
        EVDECL;
        const int so = (int)OFF_STEP + s * (int)UNIT, xr = xreg(r_s + s), sc_ = so + ((cown ^ xr) << 12);
        const int lst = olane();
        int a1 = ((cls_of(perm, lst & 3) ^ xr) << 12) | ((lst & 4) << 9) | ((lst & 8) << 12) | (((lst >> 4) | (w != TWV)) << 28);
        const bool fq = kst && krdy <= s + 2;
        v2u st = {(unsigned)(fq ? fqk(h, min(s + 1 + (lst & 15), NSEG - 1)) : ST_ADR | ((w != TWV) << 30)), 0u};
        unsigned cnt, tca = (lst & 31) * 128 + (lst >> 5) * QK_RT;
        int o0 = sc_ + mo0, o1 = sc_ + mo1, o2 = sc_ + mo2, sst = so + R_ST + role * 1024 + h * 32, ss = kst ? 0 : sc_ + R_ST;
        int tc = kst ? qk_addr(w & 1, min(s + 1, NSEG - 1), h) : 0;
        asm volatile("" : "+v"(a1), "+{v[240:241]}"(st), "+{v242}"(tca));
        adr[1] = a1;
        asm volatile("" : "+s"(o0), "+s"(o1), "+s"(o2), "+s"(sst), "+s"(ss), "+s"(tc));
        TIMING_GO
        asm volatile(QK_K
                     : [cnt] "=&s"(cnt), "+{v[240:241]}"(st), "+{v242}"(tca) EVOUT TQOUT
                     : WPIN, "{v[244:251]}"(adr), "{v243}"(pw1), [rs] "s"(rsv), [o0] "s"(o0), [o1] "s"(o1), [o2] "s"(o2),
                       [ckm] "s"(ck.m - NCY), [inv] "s"(ck.inv << 16), [r16] "s"(ck.r << 4), [sst] "s"(sst), [tg] "s"(tg), [tw] "s"(tw),
                       [ss] "s"(ss), [tc] "s"(tc)
                     : QK_CLOB, "vcc", "scc", "memory");
        if (s < NSEG - 1 && w == TWV) {
          __builtin_amdgcn_s_sleep(KSL);
          TIMING_WAVE
        } else if (s < NSEG - 1 && kst) {
          if (fq) {
            const u64 miss = __builtin_amdgcn_ballot_w64(st[0] != 0u);
            krdy = s + 1 + (miss ? __builtin_ctzll(miss) : 16);
          }
          if (KSL2) __builtin_amdgcn_s_sleep(KSL2);
          K_STAGE(s + 1)
        } else {
          __builtin_amdgcn_s_sleep(KSL);
          unsigned tch = 0;
          TLB_TOUCH
          asm volatile("" ::"v"(tch));
        }
        if (w != TWV) EV(6);
        if (w != TWV) EVDUMP(s);
      }
    }
  } else {
    const v32u W0 = cat32(wb[0], wb[1], wb[2], wb[3]), W1 = cat32(wb[4], wb[5], wb[6], wb[7]), W2 = cat32(wb[8], wb[9], wb[10], wb[11]);
    const v32u W3 = cat32(wb[12], wb[13], wa[0], wa[1]), W4 = cat32(wa[2], wa[3], wa[4], wa[5]), W5 = cat32(wa[6], wa[7], wb[14], wb[15]);
    v8i adr;
    adr[7] = l16;
    adr[6] = l16 + w * 1024;
    adr[5] = pw0;
    adr[4] = sidx;
#pragma unroll
    for (int q = 0; q < 2; ++q) {
      const int c = 8 * (2 * q + (g >> 1) + ((g & 1) ? 0 : 4)) + (w ^ ((h >> 4) * 4));
      const int blk = (c % PO) * 2 + (h >> 4);
      adr[3 - q] = (int)OFF_VM + blk * 32768 + ((cls_of(perm, c / PO) ^ xreg(r_a + (int)(OFF_VM / UNIT) + (blk >> 4))) << 12) +
                   (h & 15) * 256 + l15 * 16;
    }
    adr[1] = (16 * (g & 1) + 4 * (g >> 1) + trq) * 24 + (trp & 1) * 8;
    adr[0] = L_HI + (32 * (w & 3) + l15) * 24 + 4 * g;
#define VCH(i) (((i) & 8) | ((((i) - 1) & 7) ^ krot))
    const int mo0 = MEM_OFF(VCH(w)), mo1 = MEM_OFF(VCH(w + 4)), mo2 = MEM_OFF(VCH(w + 8));
    if (w == TWV) clk_edge(ck.r, ck.m, 2);
    if (w < 4) {
#pragma unroll 1
      for (int s = 0; s < NSEG; ++s) {
        EVDECL;
        const int so = (int)OFF_STEP + s * (int)UNIT, xr = xreg(r_s + s);
        unsigned cnt;
        asm volatile(VO_A
                     : [cnt] "=&s"(cnt) EVOUT
                     : WPIN, "{v[244:251]}"(adr), "{v243}"(pw1), [rs] "s"(rsv), [o0] "s"(so + mo0 + ((cown ^ xr) << 12)), [vs] "s"(vm_slot(s))
                     : VO_CLOB, "v241", "vcc", "scc", "memory");
        if (s < 4) PT(11 + s);
        if (w == 0) {
          unsigned tch = 0;
          TLB_TOUCH
          asm volatile("" ::"v"(tch));
        }
        EVDUMP(s);
      }
    } else {
#pragma unroll 1
      for (int s = 0; s < NSEG; ++s) {
        EVDECL;
        const int so = (int)OFF_STEP + s * (int)UNIT, xr = xreg(r_s + s);
        const int lst = olane();
        int vst = ((cls_of(perm, lst & 3) ^ xr) << 12) | ((lst & 4) << 9) | ((lst & 8) << 12) | (((lst >> 4) | (w != TWV)) << 28);
        unsigned cnt;
        v2u st = {(unsigned)(ST_ADR | ((w != TWV) << 30)), 0u};
        int o0 = so + mo0 + ((cown ^ xr) << 12), o1 = o0 - mo0 + mo1, o2 = o0 - mo0 + mo2, vs = vm_slot(s), sst = so + R_ST + role * 1024 + h * 32;
        int ss = so + R_ST + ((cown ^ xr) << 12);
        asm volatile("" : "+{v[240:241]}"(st), "+{v242}"(vst));
        asm volatile("" : "+s"(o0), "+s"(o1), "+s"(o2), "+s"(vs), "+s"(sst), "+s"(ss));
        TIMING_GO
        asm volatile(VO_B
                     : [cnt] "=&s"(cnt), "+{v[240:241]}"(st) EVOUT TQOUT
                     : WPIN, "{v[244:251]}"(adr), "{v243}"(pw1), "{v242}"(vst), [rs] "s"(rsv), [o0] "s"(o0), [o1] "s"(o1), [o2] "s"(o2),
                       [vs] "s"(vs), [ckm] "s"(ck.m - NCYV), [inv] "s"(ck.inv << 16), [r16] "s"(ck.r << 4), [sst] "s"(sst), [tg] "s"(tg),
                       [tw] "s"(tw), [ss] "s"(ss)
                     : VO_CLOB, "vcc", "scc", "memory");
        if (w == TWV && s < NSEG - 1) TIMING_WAVE
        if (w != TWV) EVDUMP(s);
      }
    }
  }
  __builtin_amdgcn_endpgm();
}
typedef f32x2 c2;
#define PKOP(name, txt)                                   \
  __device__ __forceinline__ c2 name(c2 a, c2 b) {        \
    c2 d;                                                 \
    asm("v_pk_add_f32 %0, %1, %2 " txt : "=v"(d) : "v"(a), "v"(b)); \
    return d;                                             \
  }
PKOP(c_addi, "op_sel:[0,1] op_sel_hi:[1,0] neg_lo:[0,1]")
PKOP(c_addc, "neg_hi:[0,1]")
PKOP(c_sepb, "op_sel:[1,1] op_sel_hi:[0,0] neg_hi:[1,0]")
PKOP(c_mixc, "op_sel:[0,1] op_sel_hi:[1,0] neg_hi:[1,0]")
#define T_ADD(d, a, b) "v_pk_add_f32 %" #d ", %" #a ", %" #b "\n\t"
#define T_SUB(d, a, b) "v_pk_add_f32 %" #d ", %" #a ", %" #b " neg_lo:[0,1] neg_hi:[0,1]\n\t"
#define T_ROT(d, a, b, n) "v_pk_add_f32 %" #d ", %" #a ", %" #b " op_sel:[0,1] op_sel_hi:[1,0] " n ":[0,1]\n\t"
#define T_MUL(d, a, w) "v_pk_mul_f32 %" #d ", %" #a ", %" #w " op_sel_hi:[1,0]\n\t"
#define T_FRE(d, a, w, c) "v_pk_fma_f32 %" #d ", %" #a ", %" #w ", %" #c " op_sel_hi:[1,0,1]\n\t"
#define T_FIM(d, a, w, c, n) "v_pk_fma_f32 %" #d ", %" #a ", %" #w ", %" #c " op_sel:[1,1,0] op_sel_hi:[0,1,1] " n ":[1,0,0]\n\t"
#define T_2MN(d, a, t) "v_pk_fma_f32 %" #d ", %" #a ", 2.0, %" #t " op_sel_hi:[1,0,1] neg_lo:[0,0,1] neg_hi:[0,0,1]\n\t"
#define T_TAIL(n, m) T_ADD(0, 4, 5) T_ROT(1, 2, 3, m) T_SUB(4, 4, 5) T_ROT(3, 2, 3, n)
#define T_D0(n, m) T_ADD(4, 0, 2) T_ADD(5, 1, 3) T_SUB(2, 0, 2) T_SUB(3, 1, 3) T_TAIL(n, m)
#define T_D1(n, m) T_MUL(5, 1, 7) T_ROT(4, 0, 2, m) T_FIM(1, 1, 7, 5, n) T_ROT(2, 0, 2, n) T_FRE(5, 3, 9, 1) T_FIM(5, 3, 9, 5, n) T_2MN(3, 1, 5) T_TAIL(n, m)
#define T_D2(n, m) T_MUL(5, 1, 7) T_FRE(4, 2, 8, 0) T_FIM(1, 1, 7, 5, n) T_FIM(4, 2, 8, 4, n) T_FRE(5, 3, 9, 1) T_2MN(2, 0, 4) T_FIM(5, 3, 9, 5, n) \
    T_ADD(0, 4, 5) T_2MN(3, 1, 5) T_SUB(4, 4, 5) T_ROT(1, 2, 3, m) T_ROT(3, 2, 3, n)
#define T_D3(n, m) T_MUL(4, 0, 6) T_MUL(5, 1, 7) T_FIM(0, 0, 6, 4, n) T_FIM(1, 1, 7, 5, n) T_FRE(4, 2, 8, 0) T_FRE(5, 3, 9, 1) \
    T_FIM(4, 2, 8, 4, n) T_FIM(5, 3, 9, 5, n) T_2MN(2, 0, 4) T_2MN(3, 1, 5) T_TAIL(n, m)
#define D4_ASM(T, C)                                                                                                       \
  {                                                                                                                        \
    if constexpr (INV) asm(T("neg_hi", "neg_lo") : "+v"(x0), "+v"(x1), "+v"(x2), "+v"(x3), "=&v"(t0), "=&v"(t1) : C(w0), C(w1), C(w2), C(w3)); \
    else asm(T("neg_lo", "neg_hi") : "+v"(x0), "+v"(x1), "+v"(x2), "+v"(x3), "=&v"(t0), "=&v"(t1) : C(w0), C(w1), C(w2), C(w3));               \
  }
__device__ __forceinline__ constexpr float cos32(int m) {
  constexpr float C[9] = {1.f, 0.98078528040323044f, 0.92387953251128674f, 0.83146961230254524f, 0.70710678118654752f,
                          0.55557023301960218f, 0.38268343236508977f, 0.19509032201612825f, 0.f};
  m &= 31;
  if (m > 16) m = 32 - m;
  return m > 8 ? -C[16 - m] : C[m];
}
#define W32(m) c2{cos32(m), -cos32((m) - 8)}
template <bool INV, int M0, int M1, int M2, int M3>
__device__ __forceinline__ void dft4k(c2& x0, c2& x1, c2& x2, c2& x3) {
  c2 t0, t1;
  if constexpr (M3 == 0) {
    const int w0 = 0, w1 = 0, w2 = 0, w3 = 0;
    D4_ASM(T_D0, "n")
  } else {
    const c2 w1 = W32(M1), w3 = W32(M3), w0 = M0 ? W32(M0) : w1, w2 = M0 == 0 && M2 == 8 ? w1 : W32(M2);
    if constexpr (M0 != 0) D4_ASM(T_D3, "s")
    else if constexpr (M2 == 8) D4_ASM(T_D1, "s")
    else D4_ASM(T_D2, "s")
  }
  x2 = t0;
}
template <bool INV, bool W0>
__device__ __forceinline__ void dft4w(c2& x0, c2& x1, c2& x2, c2& x3, c2 w0, c2 w1, c2 w2, c2 w3) {
  c2 t0, t1;
  if constexpr (W0) D4_ASM(T_D3, "v")
  else D4_ASM(T_D2, "v")
  x2 = t0;
}
#define T_B2(n, m) T_FRE(4, 1, 6, 0) T_FRE(5, 3, 7, 2) T_FIM(4, 1, 6, 4, n) T_FIM(5, 3, 7, 5, n) T_2MN(1, 0, 4) T_2MN(3, 2, 5)
#define T_B0(n, m) T_ADD(4, 0, 1) T_ROT(5, 2, 3, m) T_SUB(1, 0, 1) T_ROT(3, 2, 3, n)
template <bool INV, int K>
__device__ __forceinline__ void bf2(c2& x0, c2& x1, c2& x2, c2& x3, c2 w0, c2 w1) {
  c2 t0, t1;
  const c2 w2 = w0, w3 = w0;
  if constexpr (K == 0) D4_ASM(T_B2, "v")
  else if constexpr (K == 1) D4_ASM(T_B2, "s")
  else D4_ASM(T_B0, "s")
  x0 = t0;
  x2 = t1;
}
__device__ __forceinline__ void mul2(c2& d0, c2& d1, c2 a0, c2 w0, c2 a1, c2 w1) {
  asm(T_MUL(0, 2, 3) T_MUL(1, 4, 5) T_FIM(0, 2, 3, 0, "neg_lo") T_FIM(1, 4, 5, 1, "neg_lo") : "=&v"(d0), "=&v"(d1) : "v"(a0), "v"(w0), "v"(a1), "v"(w1));
}
template <int N, bool INV, int TW>
__device__ __forceinline__ void dftn(c2 (&v)[N], const c2* w) {
  constexpr int Q = N / 4;
#pragma unroll
  for (int n = 0; n < Q; ++n) {
    if (TW == 0) dft4k<INV, 0, 0, 0, 0>(v[n], v[Q + n], v[2 * Q + n], v[3 * Q + n]);
    else if (TW == 1 && n == 0) dft4w<INV, false>(v[n], v[Q + n], v[2 * Q + n], v[3 * Q + n], w[Q], w[Q + n], w[2 * Q + n], w[3 * Q + n]);
    else dft4w<INV, true>(v[n], v[Q + n], v[2 * Q + n], v[3 * Q + n], w[n], w[Q + n], w[2 * Q + n], w[3 * Q + n]);
  }
  c2 o[N];
  if constexpr (N == 32) {
#define D8A(K1)                                                                                          \
  dft4k<INV, 0, 2 * K1, 4 * K1, 6 * K1>(v[8 * K1], v[8 * K1 + 2], v[8 * K1 + 4], v[8 * K1 + 6]);          \
  dft4k<INV, K1, 3 * K1, 5 * K1, 7 * K1>(v[8 * K1 + 1], v[8 * K1 + 3], v[8 * K1 + 5], v[8 * K1 + 7]);
    D8A(0) D8A(1) D8A(2) D8A(3)
#pragma unroll
    for (int k = 0; k < 32; k += 8) {
      bf2<INV, 2>(v[k], v[k + 1], v[k + 4], v[k + 5], W32(4), W32(12));
      bf2<INV, 1>(v[k + 2], v[k + 3], v[k + 6], v[k + 7], W32(4), W32(12));
    }
#pragma unroll
    for (int i = 0; i < 32; ++i) o[(i >> 3) + 2 * (i & 6) + 16 * (i & 1)] = v[i];
  } else {
    dft4k<INV, 0, 0, 0, 0>(v[0], v[1], v[2], v[3]);
    dft4k<INV, 0, 2, 4, 6>(v[4], v[5], v[6], v[7]);
    dft4k<INV, 0, 4, 8, 12>(v[8], v[9], v[10], v[11]);
    dft4k<INV, 0, 6, 12, 18>(v[12], v[13], v[14], v[15]);
#pragma unroll
    for (int i = 0; i < 16; ++i) o[(i >> 2) + 4 * (i & 3)] = v[i];
  }
#pragma unroll
  for (int i = 0; i < N; ++i) v[i] = o[i];
}
template <bool H32>
__device__ __forceinline__ void c_swap(c2& a, c2& b) {
#pragma unroll
  for (int j = 0; j < 2; ++j) {
    const unsigned x = __float_as_uint(a[j]), y = __float_as_uint(b[j]);
    const auto r = H32 ? __builtin_amdgcn_permlane32_swap(x, y, false, false) : __builtin_amdgcn_permlane16_swap(x, y, false, false);
    a[j] = __uint_as_float(r[0]);
    b[j] = __uint_as_float(r[1]);
  }
}
#define LANE_NOP()                  \
  do {                              \
    SB();                           \
    asm volatile("s_nop 1");        \
    SB();                           \
  } while (0)
__device__ __forceinline__ c2 lds_c2(int a) { return *(volatile LAS c2*)((LAS char*)g_lds + a); }
template <int N>
__device__ __forceinline__ void lds_put(const c2 (&v)[N], int a) {
#pragma unroll
  for (int i = 0; i < N / 2; ++i) *(volatile LAS f32x4*)((LAS char*)g_lds + a + 16 * i) = f32x4{v[2 * i][0], v[2 * i][1], v[2 * i + 1][0], v[2 * i + 1][1]};
}
constexpr int XS = 32768, WS_ = 16384, WT_OFF = 18 << 20;
__device__ __forceinline__ int side_adr(int hi, int lo, int sd) { return hi + ((lo >> 13) << 14) + (lo & 8191) + (sd ^ ((hi >> 8) & 8192)); }
constexpr int LDS_TW = 0;
constexpr int LDS_EB = 4096;
constexpr int LDS_STG = 4096 + 8 * 9216;
__device__ __forceinline__ void tw2048(c2 (&w)[32], int lane) {
#pragma unroll
  for (int k = 1; k <= 8; ++k) w[k] = lds_c2(LDS_TW + lane * k * 8);
  const c2 h = lds_c2(LDS_TW + (lane & 31) * 128);
  mul2(w[0], w[16], h, h, w[8], w[8]);
#pragma unroll
  for (int k = 9; k < 15; k += 2) mul2(w[k], w[k + 1], w[8], w[k - 8], w[8], w[k - 7]);
  mul2(w[15], w[24], w[8], w[7], w[16], w[8]);
#pragma unroll
  for (int k = 1; k < 8; ++k) mul2(w[16 + k], w[24 + k], w[16], w[k], w[24], w[k]);
}
template <bool INV>
__device__ __forceinline__ void fft2048(c2 (&v)[32], const c2 (&w)[32], int eb, int lane) {
  dftn<32, INV, 0>(v, w);
  lds_put<32>(v, eb + lane * 272);
  const int ra = eb + (lane >> 5) * 4352 + (lane & 31) * 8;
#pragma unroll
  for (int k = 0; k < 16; ++k) {
    v[k] = lds_c2(ra + k * 272);
    v[k + 16] = lds_c2(ra + k * 272 + 8704);
  }
#pragma unroll
  for (int k = 0; k < 16; k += 2) bf2<INV, 0>(v[k], v[k + 16], v[k + 1], v[k + 17], w[0], w[0]);
  LANE_NOP();
#pragma unroll
  for (int k = 0; k < 16; ++k) c_swap<true>(v[k], v[k + 16]);
  dftn<32, INV, 1>(v, w);
}
template <bool SC>
__device__ __forceinline__ void tw1024(c2 (&w)[16], c2 (&u)[4], int lane, float s) {
#pragma unroll
  for (int k = 1; k <= 4; ++k) w[k] = lds_c2(LDS_TW + lane * k * 16);
  u[1] = lds_c2(LDS_TW + (lane & 15) * 256);
  u[0] = u[1];
  mul2(u[2], w[8], u[1], u[1], w[4], w[4]);
  mul2(u[3], w[12], u[2], u[1], w[8], w[4]);
  w[0] = c2{s, 0.f};
  if (SC) {
#pragma unroll
    for (int k = 1; k < 4; ++k) w[k] *= s;
  }
  c2 z;
  mul2(w[5], w[6], w[4], w[1], w[4], w[2]);
  mul2(w[7], z, w[4], w[3], w[4], w[3]);
#pragma unroll
  for (int k = 1; k < 4; ++k) mul2(w[8 + k], w[12 + k], w[8], w[k], w[12], w[k]);
  if (SC) {
#pragma unroll
    for (int k = 4; k < 16; k += 4) w[k] *= s;
  }
}
template <bool INV>
__device__ __forceinline__ void fft1024(c2 (&v)[16], const c2 (&w)[16], const c2 (&u)[4], int eb, int lane) {
  dftn<16, INV, 0>(v, w);
  lds_put<16>(v, eb + lane * 144);
  const int ra = eb + ((lane >> 1) & 8) * 144 + (lane >> 5) * 576 + (lane & 15) * 8;
  c2 t[4][4];
#pragma unroll
  for (int a = 0; a < 4; ++a)
#pragma unroll
    for (int r = 0; r < 4; ++r) t[a][r] = lds_c2(ra + (a + 16 * r) * 144);
#pragma unroll
  for (int a = 0; a < 4; ++a) {
    dft4w<INV, false>(t[a][0], t[a][1], t[a][2], t[a][3], u[0], u[1], u[2], u[3]);
#pragma unroll
    for (int q = 0; q < 4; ++q) v[a + 8 * (q & 1) + 4 * (q >> 1)] = t[a][q];
  }
  LANE_NOP();
#pragma unroll
  for (int i = 0; i < 8; ++i) c_swap<false>(v[i], v[i + 8]);
#pragma unroll
  for (int i = 0; i < 8; ++i) c_swap<true>(v[(i & 3) + 2 * (i & 4)], v[(i & 3) + 2 * (i & 4) + 4]);
  dftn<16, INV, INV ? 2 : 1>(v, w);
}

__device__ __forceinline__ void fft_table(int w) {
  const int m = w * 64 + olane();
  float sn, cs;
  sincospif(-(float)m * (1.f / 1024.f), &sn, &cs);
  *(LAS c2*)((LAS char*)g_lds + LDS_TW + m * 8) = c2{cs, sn};
  RAW_BARRIER();
}
__device__ __forceinline__ void count_done(char* ws, int cnt_off, int w) {
  VMCNT(0);
  RAW_BARRIER();
  if (w == 0) cnt_inc(ws, cnt_off);
}
__device__ __forceinline__ void stg_adr(int lane, int w, int& up, int& mid, int& dn) {
  up = LDS_STG + lane * 128 + ((w ^ (lane >> 1)) & 7) * 16;
  dn = LDS_STG + (64 - lane) * 128 + ((w ^ ((64 - lane) >> 1)) & 7) * 16 + 8;
  mid = dn + 57344 - (lane == 0 ? 8 : 0);
}

#define FT(i)
#define FTV(i, x)
__device__ __forceinline__ void fft_pass1(char* ws, char* x1, int sd, const u16* __restrict__ x, const u16* __restrict__ wr,
                                          const u16* __restrict__ wi, int task, int w) {
  LAS char* const L = (LAS char*)g_lds;
  const __amdgpu_buffer_rsrc_t rx = __builtin_amdgcn_make_buffer_rsrc(x1, 0, 513 * XS, 0x00020000);
  const __amdgpu_buffer_rsrc_t rt = __builtin_amdgcn_make_buffer_rsrc(x1 + WT_OFF, 0, 513 * WS_, 0x00020000);
  const __amdgpu_buffer_rsrc_t ri = __builtin_amdgcn_make_buffer_rsrc((void*)x, 0, S * D * 2, 0x00020000);
  const __amdgpu_buffer_rsrc_t rr = __builtin_amdgcn_make_buffer_rsrc((void*)wr, 0, S * DF * 2, 0x00020000);
  const __amdgpu_buffer_rsrc_t rm = __builtin_amdgcn_make_buffer_rsrc((void*)wi, 0, S * DF * 2, 0x00020000);
  const int lane = olane();
  const int p = task * 8 + w, r0 = (p & 63) + 128 * (p >> 6);
  unsigned xa[16], xb[16], fr[2][9], fi[2][9];
  FT(0);
#pragma unroll
  for (int t = 0; t < 16; ++t) {
    xa[t] = __builtin_amdgcn_raw_buffer_load_b16(ri, lane * 2 + t * 128, r0 * 2048, 0);
    xb[t] = __builtin_amdgcn_raw_buffer_load_b16(ri, lane * 2 + t * 128, r0 * 2048 + 131072, 0);
  }
  const int l8 = lane == 0 ? 1024 : OOB;
#pragma unroll
  for (int r = 0; r < 2; ++r)
#pragma unroll
    for (int t = 0; t < 9; ++t) {
      fr[r][t] = __builtin_amdgcn_raw_buffer_load_b16(rr, t < 8 ? lane * 2 + t * 128 : l8, (r0 + 64 * r) * (DF * 2), 0);
      fi[r][t] = __builtin_amdgcn_raw_buffer_load_b16(rm, t < 8 ? lane * 2 + t * 128 : l8, (r0 + 64 * r) * (DF * 2), 0);
    }
  FT(1);
  c2 tw[16], tu[4], v[16];
  tw1024<false>(tw, tu, lane, 1.f);
  FTV(2, xb[15]);
#pragma unroll
  for (int t = 0; t < 16; ++t) v[t] = c2{__uint_as_float(xa[t] << 16), __uint_as_float(xb[t] << 16)};
  fft1024<false>(v, tw, tu, LDS_EB + w * 9216, lane);
  int up, mid, dn;
  stg_adr(lane, w, up, mid, dn);
#pragma unroll
  for (int j = 0; j < 16; ++j) *(volatile LAS c2*)(L + (j < 8 ? up + 8192 * j : (j == 8 ? mid : dn + 8192 * (15 - j)))) = v[j];
  FT(3);
  RAW_BARRIER();
  FTV(4, fi[1][8]);
  {
    const int fa = LDS_EB + lane * 64 + ((w ^ lane) & 6) * 8 + (w & 1) * 8;
#pragma unroll
    for (int t = 0; t < 9; ++t)
      if (t < 8 || lane == 0) *(LAS v2u*)(L + fa + t * 4096) = v2u{fr[0][t] | (fi[0][t] << 16), fr[1][t] | (fi[1][t] << 16)};
  }
  const int kq = w * 8 + (lane >> 3);
  {
    const int ra = LDS_STG + kq * 128 + ((lane ^ (kq >> 1)) & 7) * 16, ga = kq * XS + (lane & 7) * 16;
    v4u d[9];
#pragma unroll
    for (int i = 0; i < 9; ++i) d[i] = *(const LAS v4u*)(L + ra + i * 8192);
#pragma unroll
    for (int i = 0; i < 9; ++i)
      if (i < 8 || w == 0)
        st128(d[i], rx, ga + (i == 8 ? 512 * XS : 0), side_adr((i & 7) * (64 * XS), task * 128, sd));
  }
  FT(5);
  RAW_BARRIER();
  {
    const int k4 = w * 16 + (lane >> 2);
    const int ra = LDS_EB + k4 * 64 + (((2 * lane) ^ k4) & 6) * 8, ga = k4 * WS_ + (lane & 3) * 16;
    v4u d[5];
#pragma unroll
    for (int i = 0; i < 5; ++i) d[i] = *(const LAS v4u*)(L + ra + i * 8192);
#pragma unroll
    for (int i = 0; i < 5; ++i)
      if (i < 4 || w == 0)
        st128(d[i], rt, ga + (i == 4 ? 512 * WS_ : 0), side_adr((i & 3) * (128 * WS_), task * 64, sd ^ 8192));
  }
  FT(6);
  FT(7);
}
__device__ __forceinline__ void fft_pass2(char* ws, char* x1, int sd, int task, int w) {
  const __amdgpu_buffer_rsrc_t rx = __builtin_amdgcn_make_buffer_rsrc(x1, 0, 513 * XS, 0x00020000);
  const __amdgpu_buffer_rsrc_t rt = __builtin_amdgcn_make_buffer_rsrc(x1 + WT_OFF, 0, 513 * WS_, 0x00020000);
  const int lane = olane();
  const int k = task * 8 + w;
  if (k > 512) return;
  f32x4 d[16];
  FT(0);
#pragma unroll
  for (int t = 0; t < 16; ++t)
    d[t] = __builtin_bit_cast(f32x4, __builtin_amdgcn_raw_buffer_load_b128(rx, lane * 16 + (t & 3) * 1024, side_adr(k * XS, (t >> 2) * 4096, sd), AUX_SC1));
  c2 tw[32], v[32];
  tw2048(tw, lane);
  FT(1);
  const bool edge = (k & 511) == 0;
  FTV(2, d[15]);
  if (edge) {
#pragma unroll
    for (int t = 0; t < 16; ++t) {
      d[t][2] = d[t][0];
      d[t][3] = d[t][1];
    }
  }
#pragma unroll
  for (int t = 0; t < 16; ++t) {
    v[2 * t] = c_addc(c2{d[t][0], d[t][1]}, c2{d[t][2], d[t][3]});
    v[2 * t + 1] = c_sepb(c2{d[t][0], d[t][1]}, c2{d[t][2], d[t][3]});
  }
  SB();
  v2u f[16];
#pragma unroll
  for (int t = 0; t < 16; ++t) f[t] = __builtin_amdgcn_raw_buffer_load_b64(rt, lane * 8 + (t & 7) * 512, side_adr(k * WS_, (t >> 3) * 4096, sd ^ 8192), AUX_SC1);
  SB();
  const int eb = LDS_EB + w * 17408;
  fft2048<false>(v, tw, eb, lane);
  FTV(3, v[31]);
  FTV(4, f[15]);
#pragma unroll
  for (int t = 0; t < 16; ++t)
    mul2(v[2 * t], v[2 * t + 1], v[2 * t], c2{__uint_as_float(f[t][0] << 16), __uint_as_float(f[t][0] & 0xffff0000u)}, v[2 * t + 1],
         c2{__uint_as_float(f[t][1] << 16), __uint_as_float(f[t][1] & 0xffff0000u)});
  fft2048<true>(v, tw, eb, lane);
  FTV(5, v[31]);
  if (edge) {
#pragma unroll
    for (int t = 0; t < 32; ++t) v[t][1] = 0.f;
  }
#pragma unroll
  for (int t = 0; t < 16; ++t) {
    const c2 a = c_addi(v[2 * t], v[2 * t + 1]), b = c_mixc(v[2 * t], v[2 * t + 1]);
    st128(__builtin_bit_cast(v4u, f32x4{a[0], a[1], b[0], b[1]}), rx, lane * 16 + (t & 3) * 1024, side_adr(k * XS, (t >> 2) * 4096, sd));
  }
  FT(6);
  FT(7);
}
__device__ __forceinline__ void fft_pass3(char* ws, char* x1, int sd, u16* __restrict__ out, int task, int w) {
  LAS char* const L = (LAS char*)g_lds;
  const __amdgpu_buffer_rsrc_t rx = __builtin_amdgcn_make_buffer_rsrc(x1, 0, 513 * XS, 0x00020000);
  const __amdgpu_buffer_rsrc_t ro = __builtin_amdgcn_make_buffer_rsrc(out, 0, S * 4096, 0x00020000);
  const int lane = olane();
  const int kq = w * 8 + (lane >> 3);
  const int ra = LDS_STG + kq * 128 + ((lane ^ (kq >> 1)) & 7) * 16, ga = kq * XS + (lane & 7) * 16;
  v4u d[9];
  FT(0);
#pragma unroll
  for (int i = 0; i < 9; ++i) d[i] = __builtin_amdgcn_raw_buffer_load_b128(rx, ga + (i == 8 ? 512 * XS : 0), side_adr((i & 7) * (64 * XS), task * 128, sd), AUX_SC1);
  c2 tw[16], tu[4], v[16];
  tw1024<true>(tw, tu, lane, 1.f / (2.f * 2048.f * 1024.f));
  FT(1);
  FTV(2, d[8]);
#pragma unroll
  for (int i = 0; i < 9; ++i)
    if (i < 8 || w == 0) *(LAS v4u*)(L + ra + i * 8192) = d[i];
  int up, mid, dn;
  stg_adr(lane, w, up, mid, dn);
  FT(3);
  RAW_BARRIER();
  FT(4);
#pragma unroll
  for (int j = 0; j < 16; ++j) v[j] = lds_c2(j < 8 ? up + 8192 * j : (j == 8 ? mid : dn + 8192 * (15 - j)));
  fft1024<true>(v, tw, tu, LDS_EB + w * 9216, lane);
  FTV(5, v[15]);
  const int p = task * 8 + w, r0 = (p & 63) + 128 * (p >> 6);
#pragma unroll
  for (int t = 0; t < 16; ++t) {
    const unsigned o = pk2(v[t][0], v[t][1]);
    __builtin_amdgcn_raw_buffer_store_b16((u16)o, ro, lane * 2 + t * 128, r0 * 4096, 0);
    __builtin_amdgcn_raw_buffer_store_b16((u16)(o >> 16), ro, lane * 2 + t * 128, r0 * 4096 + 262144, 0);
  }
  FT(6);
  FT(7);
}

#define RSO 5
#define NCAL 6
#define DOX2 300
#define DTC 100
#define NX1 4
#define NXG 2
#define NXK 5
#define OFD 1250
#define STI 600
#define TNX 0
#define NXP 0
#define PACE 150
#define PFB 4
#define CV1 -45
#define CV2 125
#define EDL 0
constexpr int L_D = 16384, L_R = 32768, L_F = 122880;
__device__ __forceinline__ volatile LAS unsigned* flw(int i, int m) {
  return (volatile LAS unsigned*)((LAS char*)g_lds + L_F + i * 4 + (olane() & m) * 4);
}
template <int SLP = 0>
__device__ __forceinline__ void lds_wait_ge(int i, int m, unsigned v, gu32* tmo, unsigned code) {
  volatile LAS unsigned* p = flw(i, m);
  _Pragma("clang loop unroll(disable)") for (unsigned spins = 0;;) {
    if (__all(*p >= v)) break;
    if (SLP) __builtin_amdgcn_s_sleep(SLP);
    if (++spins > (kSpinCap << 5)) {
      tmo_report(tmo, code);
      break;
    }
  }
  asm volatile("" ::: "memory");
}
#define HLP 6
#define HLT 0
__device__ __forceinline__ void help(int c, int w, const u16* Wq, const u16* Wk, const u16* Wv) {
  const int j = c & 7, m = j >> 1, xg = c / PO, ln = olane();
  if ((c & 8) && w >= 1 && w <= HLP) {
    const u16* wm = m == 2 ? Wv : ((m == 3 ? j : m) & 1 ? Wk : Wq);
    const int hs = m == 3 ? 31 : PQ * xg + slow_head(wm) + 4 * (j & 1);
    if (m < 3 || xg == NXC - 1)
      for (int q = 0; q < 2; ++q)
        touch4((const char*)wm + hs * 256 + (size_t)((16 - w) ^ (hs & KROT)) * 524288, (q * 32 + (ln >> 1)) * 8192 + (ln & 1) * 128, 65536 + w * 256);
  }
}
template <bool PRE>
__device__ __forceinline__ unsigned owner(const int c, const int w, const u64 nonce, const u16* __restrict__ mem0, char* ws,
                                          u16* __restrict__ out, unsigned perm, const u16* Wq, const u16* Wk, const u16* Wv) {
  LAS char* const L = (LAS char*)g_lds;
  gu32* tmo = (gu32*)(ws + OFF_TMO);
  const int lane = olane();
  const int l15 = lane & 15, g = lane >> 4;
  const __amdgpu_buffer_rsrc_t rs = __builtin_amdgcn_make_buffer_rsrc(ws, 0, (int)WS_USED, 0x00020000);
  const int xg = c / PO, li = c % PO;
  const int r_a = (int)(((uintptr_t)ws >> 19) & 7);
  const int r_s = r_a + (int)(OFF_STEP / UNIT);
  volatile LAS unsigned* fl = (volatile LAS unsigned*)(L + L_F);
  unsigned m0v = 0;
  if constexpr (PRE) {
  if (w == 7) m0v = *(const unsigned*)(mem0 + (lane >> 3) * D + 16 * c + 2 * (lane & 7));
  if (c == 0 && w == 6) hs_lead<4>(rs, true, CNT_HSO, 64u);
  unsigned cal = 0;
  if (w == 0)
    st128(v4u{(unsigned)lane, 0u, 0u, 0u}, rs, (lane & 31) * 16 + (lane >> 5) * 32768, (int)OFF_SCR + (((li & 7) ^ xreg(r_a)) << 12) + (c >> 3) * 512);
  if (w == 0 && lane == 0) {
    const int cl = li & 7;
    int off = (int)OFF_SCR + ((cl ^ xreg(r_a)) << 12) + (c >> 3) * 512;
    unsigned best = 0xfffeu;
#pragma unroll 1
    for (int r = 0; r < NCAL; ++r) {
      VMCNT(0);
      const u64 t1 = __builtin_amdgcn_s_memtime();
      asm volatile("s_waitcnt lgkmcnt(0)" : "+v"(off)::"memory");
      const unsigned v = __builtin_amdgcn_raw_buffer_load_b32(rs, off, (r & 3) * 128 + (r >> 2) * 32768, AUX_SC1);
      asm volatile("s_waitcnt vmcnt(0)" ::"v"(v) : "memory");
      const u64 t2 = __builtin_amdgcn_s_memtime();
      best = min(best, (unsigned)(t2 - t1));
    }
    if (HLT == 0 && li >= 8) best = 0xfffeu;
    cal = ((unsigned)cl << 16) | best;
  }
  if (w == 0) {
    PT(0);
    hs_wait(rs, nonce, v2u{~(unsigned)nonce, 0u});
    cnt_inc(ws, CNT_HSO);
    PT(1);
    unsigned id;
    asm volatile("s_getreg_b32 %0, hwreg(HW_REG_XCC_ID)" : "=s"(id));
    if (lane == 0) {
      __builtin_amdgcn_raw_buffer_store_b32(((id & 15u) << 19) | cal, rs, c * 4, (int)OFF_CAL, AUX_SC1);
      if (li == 0) __builtin_amdgcn_raw_buffer_store_b32(id & 15u, rs, xg * 4, (int)OFF_XID, AUX_SC1);
    }
  }
  static_assert(NF1O == 64 || NF1O == 2, "one task per owner but the first two, or none");
  __syncthreads();
  if (HLT == 1) help(c, w, Wq, Wk, Wv);
  if (w == 7) {
    const unsigned pm = numa_perm(numa_load(rs, tmo), (LAS unsigned*)(L + L_F + 512));
    fl[10] = pm;
  }
  __syncthreads();
  perm = __builtin_amdgcn_readfirstlane(fl[10]);
  PT(2);
  if (HLT == 2) help(c, w, Wq, Wk, Wv);
  }
  const int xr0 = xreg(r_s + (PRE ? 0 : NARM + (li & 1)));
  const v4u sent = {0xffffffffu, 0xffffffffu, 0xffffffffu, 0xffffffffu};
  const int ibs = (int)OFF_VM + li * 65536 + ((cls_of(perm, xg) ^ xreg(r_a + (int)(OFF_VM / UNIT) + (li >> 3))) << 12);

  int am = (int)OFF_STEP + R_MEM + (c >> 4) * 32768 + ((cls_of(perm, g) ^ xr0) << 12) + (c & 15) * 256 + l15 * 16;
  int ap[2];
#pragma unroll
  for (int q = 0; q < 2; ++q) {
    const int o = c * 384 + (lane & 31) * 16;
    ap[q] = (lane & 31) < 24 && c < 56 ? (int)OFF_STEP + R_EX + (o >> 12) * 32768 + ((cls_of(perm, 2 * q + (lane >> 5)) ^ xr0) << 12) + (o & 4095)
                             : OOB;
  }
  int as = lane < 8 * NXC ? (int)OFF_STEP + R_ST + ((cls_of(perm, lane >> 3) ^ xr0) << 12) + (c >> 5) * 32768 + (c & 31) * 128 + (lane & 7) * 16
                          : OOB;
  const int ax = (int)OFF_STEP + R_PX + c * 1280 + lane * 16;
  const int ax2 = lane < 16 ? ax + 1024 : (lane < 24 ? (int)OFF_STEP + R_DUM + c * 128 + (lane - 16) * 16 : OOB);
#define ARM_REC(r)                                                                                                   \
  {                                                                                                                  \
    const int so_ = (r) * (int)UNIT, dx_ = (xreg(r_s + (r)) ^ xreg(r_s + (r) + 1)) << 12;                            \
    __builtin_amdgcn_raw_buffer_store_b128(sent, rs, am, so_, AUX_SC1);                                              \
    am ^= dx_;                                                                                                       \
    _Pragma("unroll") for (int q = 0; q < 2; ++q) {                                                                  \
      __builtin_amdgcn_raw_buffer_store_b128(sent, rs, ap[q], so_, AUX_SC1);                                         \
      ap[q] ^= dx_;                                                                                                  \
    }                                                                                                                \
    __builtin_amdgcn_raw_buffer_store_b128(sent, rs, as, so_, AUX_SC1);                                              \
    as ^= dx_;                                                                                                       \
    __builtin_amdgcn_raw_buffer_store_b128(sent, rs, ax, so_, AUX_SC1);                                              \
    __builtin_amdgcn_raw_buffer_store_b128(sent, rs, ax2, so_, AUX_SC1);                                             \
  }
  if (PRE && w == 7) {
    {
      const int row = lane >> 3, np = lane & 7;
#pragma unroll
      for (int r = 0; r < NXC; ++r)
        __builtin_amdgcn_raw_buffer_store_b32(min(m0v, 0xfffffffeu), rs,
                                              (int)OFF_STEP + R_MEM + (c >> 4) * 32768 + ((cls_of(perm, r) ^ xreg(r_s)) << 12) +
                                                  (c & 15) * 256 + ((row & 1) + 2 * (np & 1) + 4 * (row >> 1)) * 16 + (np >> 1) * 4,
                                              0, AUX_SC1);
    }
    am |= 1 << 30;
#pragma unroll 1
    for (int r = 0; r < NARM + (li & 1); ++r) {
      ARM_REC(r)
      am &= ~(1 << 30);
    }
#pragma unroll
    for (int k = 0; k < 32; ++k)
      __builtin_amdgcn_raw_buffer_store_b128(sent, rs, lane * 16 + (k & 3) * 1024, ibs + vm_slot(k >> 3) + ((k >> 2) & 1) * 32768, AUX_SC1);
    VMCNT(0);
    PTW(3, 7);
    cnt_inc(ws, CNT_ARM);
  }
  if constexpr (PRE) return perm;
  PT(5);
  __syncthreads();
  if (w == 7 && lane < 32) fl[lane] = 0;
  __syncthreads();

  const int team = li & 1, c0 = c & ~1;
  const int l16 = lane * 16;
  const int cown = cls_of(perm, xg);
  if (w < 2) {
    const int r = (l15 & 1) + 2 * (l15 >> 2), kh = (l15 >> 1) & 1;
    const int nl = (kh ^ ((r >> 1) & 1)) + 2 * ((g & 1) ^ (r >> 2));
    typedef int v8i __attribute__((ext_vector_type(8)));
    v8i adr;
    adr[0] = l16;
    adr[1] = 32768 + (2 * nl + (g >> 1)) * 128 + r * 16;
    adr[2] = (r + 8 * kh + 16 * g) * 16;
    adr[3] = L_D + w * 8192 + l16;
    adr[4] = ((l15 & 3) + 4 * g) * 16 + (l15 >> 2) * 4;
    adr[5] = (l15 & 1) ? 0x03020706 : 0x05040100;
    adr[6] = (l15 & 2) ? 0x4444eeee : 0xeeee4444;
    adr[7] = L_F + 48;
    u64 mk[4];
#pragma unroll
    for (int k = 0; k < 4; ++k) mk[k] = __builtin_amdgcn_ballot_w64(nl == k);
    const v4u rsv = {(unsigned)(uintptr_t)ws, (unsigned)((uintptr_t)ws >> 32) & 0xffffu, (unsigned)WS_USED, 0x00020000u};
    const int dtc_ = TN(12, DTC);
    const int eg = (c0 >> 2) & 1;
#pragma unroll 1
    for (int s = team; s < NSEG; s += 2) {
      EVDECL;
      const int so = (int)OFF_STEP + s * (int)UNIT;
      int sp = so + R_PM + ((cown ^ xreg(r_s + s)) << 12);
      int sv = ibs + (w - team) * 65536 + vm_slot(s);
      int sv1 = sv + eg * 32768, sv2 = sv + (1 - eg) * 32768, sp1 = sp + eg * 2048, sp2 = sp + (1 - eg) * 2048;
      const int xn = xreg(r_s + s + 1);
      int sm[NXC];
#pragma unroll
      for (int q = 0; q < NXC; ++q) {
        sm[q] = so + (int)UNIT + R_MEM + (c0 >> 4) * 32768 + ((cls_of(perm, q) ^ xn) << 12) + ((c0 + w) & 15) * 256;
        asm volatile("" : "+s"(sm[q]));
      }
      asm volatile("" : "+s"(sp), "+s"(sv1), "+s"(sv2), "+s"(sp1), "+s"(sp2));
      if (s >= 2) lds_wait_ge<4>(20, 3, s - 1, tmo, 0x30017);
      v4u bl[8];
      if (s < NSEG - 1) {
        v32u blv;
        unsigned cnt, bad;
        asm volatile(OW_F
                     : [cnt] "=&s"(cnt), [st] "=&s"(bad), "={v[0:31]}"(blv) EVOUT
                     : "{v[112:119]}"(adr), [rs] "s"(rsv), [sv1] "s"(sv1), [sv2] "s"(sv2), [sp] "s"(sp), [sp1] "s"(sp1), [sp2] "s"(sp2),
                       [sm0] "s"(sm[0]), [sm1] "s"(sm[1]), [sm2] "s"(sm[2]), [sm3] "s"(sm[3]), [m0] "s"(mk[0]), [m1] "s"(mk[1]),
                       [m2] "s"(mk[2]), [m3] "s"(mk[3])
                     : OF_CLOB, "vcc", "scc", "memory");
#pragma unroll
        for (int k = 0; k < 8; ++k) bl[k] = v4u{blv[4 * k], blv[4 * k + 1], blv[4 * k + 2], blv[4 * k + 3]};
        if (__builtin_expect(bad != 0u, 0)) {
          v4u xs, pv[4];
          _Pragma("clang loop unroll(disable)") for (unsigned spins = 0;;) {
            OPQ(va, l16);
            OPQ(v1, adr[1]);
            OPQ(v2, adr[2]);
#pragma unroll
            for (int k = 0; k < 8; ++k) bl[k] = __builtin_amdgcn_raw_buffer_load_b128(rs, va + (k & 3) * 1024, k < 4 ? sv1 : sv2, AUX_SC1);
            xs = __builtin_amdgcn_raw_buffer_load_b128(rs, v1, sp, AUX_SC1);
#pragma unroll
            for (int k = 0; k < 4; ++k) pv[k] = __builtin_amdgcn_raw_buffer_load_b128(rs, v2 + (k & 1) * 1024, k < 2 ? sp1 : sp2, AUX_SC1);
            unsigned m = mx4(0u, xs);
#pragma unroll
            for (int k = 0; k < 4; ++k) m = mx4(m, pv[k]);
#pragma unroll
            for (int k = 0; k < 8; ++k) m = mx4(m, bl[k]);
            if (!anysent(m)) break;
            __builtin_amdgcn_s_sleep(RSO);
            if (spin_fail(spins, tmo, 0x20000 + s)) break;
          }
          f32x4 a0 = {0.f, 0.f, 0.f, 0.f};
#pragma unroll
          for (int k = 0; k < 4; ++k) {
            const v4u z = v4u{nl == k ? xs[0] : 0u, nl == k ? xs[1] : 0u, nl == k ? xs[2] : 0u, nl == k ? xs[3] : 0u};
            const v8u db = cat8(*(const LAS v4u*)(L + adr[3] + k * 2048), *(const LAS v4u*)(L + adr[3] + k * 2048 + 1024));
            a0 = (k & 1) ? smf1(z, db, a0, adr[6]) : smf(z, db, a0, adr[6]);
          }
#pragma unroll
          for (int k = 0; k < 4; ++k) a0 = smf(pv[k], cat8(bl[2 * k], bl[2 * k + 1]), a0, adr[6]);
          const unsigned own = pk2(a0[0] + a0[2], a0[1] + a0[3]);
          const unsigned nb = (unsigned)__builtin_amdgcn_mov_dpp((int)own, 0xb1, 0xf, 0xf, true);
          const unsigned o = __builtin_amdgcn_perm(nb, own, (unsigned)adr[5]);
#pragma unroll
          for (int q = 0; q < NXC; ++q) __builtin_amdgcn_raw_buffer_store_b32(o, rs, adr[4], sm[q], AUX_SC1);
          EV(3);
        }
      } else {
        BARE_BARRIER();
        wait_cyc(*flw(12, 0));
        _Pragma("clang loop unroll(disable)") for (unsigned spins = 0;;) {
          OPQ(va, l16);
          unsigned m = 0;
#pragma unroll
          for (int k = 0; k < 8; ++k) bl[k] = __builtin_amdgcn_raw_buffer_load_b128(rs, va + (k & 3) * 1024, k < 4 ? sv1 : sv2, AUX_SC1);
#pragma unroll
          for (int k = 0; k < 8; ++k) m = mx4(m, bl[k]);
          if (!anysent(m)) break;
          __builtin_amdgcn_s_sleep(RSO);
          if (spin_fail(spins, tmo, 0x20000 + s)) break;
        }
      }
      const unsigned tp = cyc32();
      if (s < 4) PT(9 + (s >> 1));
      *flw(2 + w, 0) = s + 1;
      if (s < NSEG - 1) {
        const v4u tv = *(volatile LAS v4u*)flw(12, 0);
        const int lr = olane();
        __builtin_amdgcn_raw_buffer_store_b32(min(((tp - tv[1]) * tv[2]) >> 16, 60000u), rs,
                                              ((cls_of(perm, lr & 3) ^ xn) << 12) | ((lr & 4) << 9) | ((lr & 8) << 12) | ((lr >> 4) << 28),
                                              so + (int)UNIT + R_ST + (c0 + w) * 32 + 4, AUX_SC1);
      }
      OPQ(vs, l16);
#pragma unroll
      for (int k = 0; k < 8; ++k) *(LAS v4u*)(L + w * 8192 + ((k ^ (4 * eg)) * 1024) + vs) = bl[k];
      asm volatile("s_waitcnt lgkmcnt(0)" ::: "memory");
      *flw(4 + w, 0) = s + 1;
      if (s < NSEG - 4) {
        lds_wait_ge(2, 1, s + 1, tmo, 0x3001a);
        v4u se;
        asm volatile("v_mov_b32 %0, -1" : "=v"(se[0]));
        se[1] = se[2] = se[3] = se[0];
#pragma unroll
        for (int k = 0; k < 8; ++k) __builtin_amdgcn_raw_buffer_store_b128(se, rs, vs + (k & 3) * 1024, sv + (k >> 2) * 32768, AUX_SC1);
        asm volatile("s_nop 1" ::"v"(se));
      }
      if (s < NSEG - 2) {
        wait_cyc(tp + dtc_);
        const int lt = olane();
        const unsigned tc = __builtin_amdgcn_raw_buffer_load_b32(rs, lt < 40 ? lt * 128 : OOB, so + R_PX + ((li & ~1) + w) * 5120, AUX_SC1);
        asm volatile("" ::"v"(tc));
      }
      EVDUMP(s);
    }
  } else if (w < 6) {
    const int n = w - 2;
    const int vfr = (int)((perm & 8192u) ^ (VFAR == 2 ? 8192u : 0u)) | (r_a << 19);
    const int fv0 = fvw((c0 >> 1) & 3, 8 * n, team);
    const int dox_ = TN(10, DOX2), pace_ = TN(11, PACE);
    v4u raw[2][8];
#define RAW_LD(dst, sg)                                                                                       \
  _Pragma("unroll") for (int t = 0; t < 2; ++t) _Pragma("unroll") for (int k = 0; k < 8; ++k) dst[t][k] =     \
      __builtin_amdgcn_raw_buffer_load_b128(rs, l16 + (k & 3) * 1024, vw_addr(vfr, c0 + t, sg, 2 * n + (k >> 2)), AUX_SC1);
    wait_flag<KF0>(rs, fv0 + (lane & 7), tmo, 0x30018);
    PTW(6, 2);
    RAW_LD(raw, team)
    int nrdy = team;
#pragma unroll 1
    for (int s = team; s < NSEG; s += 2) {
      EVDECL;
      const int so = (int)OFF_STEP + s * (int)UNIT;
      if (s < NSEG - 1) {
        const int ln = olane();
        OPQ(eidx, (ln & 4) ? 0xeeee : 0x4444);
        OPQ(vea, (ln & 4) * 128 + (ln >> 4) * 128 + ((ln & 3) + 4 * (((ln >> 3) & 1) ^ (n >> 1))) * 16);
        int se = so + R_EX + n * 32768 + ((cown ^ xreg(r_s + s)) << 12);
        v8u bx[2][4];
#pragma unroll
        for (int t = 0; t < 2; ++t)
#pragma unroll
          for (int k = 0; k < 4; ++k) {
            const v4u u = raw[t][2 * k], v = raw[t][2 * k + 1];
            bx[t][k] = v8u{u[0], v[0], u[2], v[2], u[1], v[1], u[3], v[3]};
            asm volatile("" : "+v"(bx[t][k]));
          }
        OPQ(vd, L_D + n * 2048 + ln * 16);
        f32x4 d[2][4];
#pragma unroll
        for (int t = 0; t < 2; ++t)
#pragma unroll
          for (int k = 0; k < 4; ++k) d[t][k] = zero4();
        asm volatile("" : "+s"(se));
        EV(3);
        if (s < 4) PTW(7 + (s >> 1), 2);
        BARE_BARRIER();
        if (EDL) asm volatile("s_nop %0" ::"n"(EDL > 0 ? EDL - 1 : 0));
        EV(0);
#define E_PACK(t, k) v2u{pk2(d[t][k][0], d[t][k][1]), pk2(d[t][k][2], d[t][k][3])}
        _Pragma("clang loop unroll(disable)") for (unsigned spins = 0;;) {
          v4u pv[4];
          asm volatile("" : "+v"(vea));
#pragma unroll
          for (int k = 0; k < 4; ++k) pv[k] = __builtin_amdgcn_raw_buffer_load_b128(rs, vea + k * 1024, se, AUX_SC1);
          unsigned m = 0;
          v2u cv[2][4];
          SB();
#pragma unroll
          for (int k = 0; k < 4; ++k) {
            d[0][k] = smf(pv[k], bx[0][k], d[0][k], eidx);
            m = mx4(m, pv[k]);
            asm volatile("" : "+v"(m), "+v"(d[0][k]));
            d[1][k] = smf(pv[k], bx[1][k], d[1][k], eidx);
            asm volatile("" : "+v"(d[1][k]));
            if (k > 0) {
              const int j = k > 0 ? k - 1 : 0;
              asm volatile("" : "+v"(d[0][j]), "+v"(d[1][j]));
              cv[0][j] = E_PACK(0, j);
              cv[1][j] = E_PACK(1, j);
              asm volatile("" : "+v"(cv[0][j]), "+v"(cv[1][j]));
            }
            if (k == 2) {
              *(LAS v4u*)(L + vd) = v4u{cv[0][0][0], cv[0][0][1], cv[0][1][0], cv[0][1][1]};
              *(LAS v4u*)(L + vd + 8192) = v4u{cv[1][0][0], cv[1][0][1], cv[1][1][0], cv[1][1][1]};
            }
            SB();
          }
          EV(1);
          cv[0][3] = E_PACK(0, 3);
          *(LAS v4u*)(L + vd + 1024) = v4u{cv[0][2][0], cv[0][2][1], cv[0][3][0], cv[0][3][1]};
          cv[1][3] = E_PACK(1, 3);
          *(LAS v4u*)(L + vd + 9216) = v4u{cv[1][2][0], cv[1][2][1], cv[1][3][0], cv[1][3][1]};
          if (__builtin_expect(!anysent(m), 1)) break;
          __builtin_amdgcn_s_sleep(RSO);
          if (spin_fail(spins, tmo, 0x10000 + s)) break;
#pragma unroll
          for (int t = 0; t < 2; ++t)
#pragma unroll
            for (int k = 0; k < 4; ++k) d[t][k] = zero4();
        }
        EV(2);
        RAW_BARRIER();
      } else BARE_BARRIER();
      v4u nx[2][8];
      const int sn = min(s + 2, NSEG - 1);
      unsigned tpb = 0;
#define VW_FLAGS                                                                                                      \
  {                                                                                                                   \
    const int lf = olane();                                                                                           \
    OPQ(vfl, fv0 + min((s >> 1) + 1 + (lf >> 3), NSEG / 2 - 1) * 8 + (lf & 7));                                        \
    const u64 miss = __builtin_amdgcn_ballot_w64(__builtin_amdgcn_raw_buffer_load_b8(rs, vfl, 0, AUX_SC1) != 0);      \
    nrdy = s + 2 * (miss ? __builtin_ctzll(miss) >> 3 : 8);                                                           \
  }
      if (s < NSEG - 2) {
        __builtin_amdgcn_s_sleep(5);
        lds_wait_ge(2, 1, s + 1, tmo, 0x30014);
        tpb = cyc32();
        const unsigned tw = tpb + dox_;
        if (nrdy < s + 2) VW_FLAGS
        wait_cyc(tw);
      }
      EV(6);
      OPQ(va, s < NSEG - 2 && nrdy >= s + 2 ? l16 : OOB);
#define NX_LD(dst, q0, q1)                                                                              \
  _Pragma("unroll") for (int q = q0; q < q1; ++q) dst[q >> 3][q & 7] = __builtin_amdgcn_raw_buffer_load_b128( \
      rs, va + (q & 3) * 1024, vw_addr(vfr, c0 + (q >> 3), sn, 2 * n + ((q >> 2) & 1)), AUX_SC1);
      NX_LD(nx, 0, NX1)
      SB();
      if (s < NSEG - 2) wait_cyc(cyc32() + ((n + (li >> 1)) & 3) * pace_);
      else lds_wait_ge(4, 1, s + 1, tmo, 0x30014);
      EV(4);
      f32x4 ac[2][2];
      const int sx = so + R_PX + n * 8192, sy = so + R_PX + 32768 + n * 2048;
      v4u pv[2][10], vm[2][2];
      OPQ(vb, l16);
#define P_LD                                                                                         \
  _Pragma("unroll") for (int k = 0; k < 10; ++k) _Pragma("unroll") for (int rt = 0; rt < 2; ++rt) pv[rt][k] = \
      __builtin_amdgcn_raw_buffer_load_b128(rs, vb + (k & 3) * 1024, (k < 8 ? sx + (k >> 2) * 4096 : sy) + rt * 40960, AUX_SC1);
#define P_MM(k0, k1)                                                                                 \
  _Pragma("unroll") for (int k = k0; k < k1; ++k) _Pragma("unroll") for (int rt = 0; rt < 2; ++rt) {  \
    _Pragma("unroll") for (int t = 0; t < 2; ++t)                                                    \
      ac[rt][t] = mma(k < 8 ? raw[t][k & 7] : vm[t][k & 1], pv[rt][k], k == 0 ? f32x4{0.f, 0.f, 0.f, 0.f} : ac[rt][t]); \
    asm volatile("" : "+v"(ac[rt][0]), "+v"(ac[rt][1]));                                             \
  }
      P_LD
      SB();
      lds_wait_ge(4, 1, s + 1, tmo, 0x30014);
#pragma unroll
      for (int t = 0; t < 2; ++t)
#pragma unroll
        for (int k = 0; k < 2; ++k) vm[t][k] = *(const LAS v4u*)(L + vb + t * 8192 + n * 2048 + k * 1024);
      SB();
      P_MM(0, 10)
      if (__builtin_expect(__builtin_amdgcn_ballot_w64(__builtin_isunordered(ac[0][0][0], ac[1][0][0])) != 0ull, 0)) {
        _Pragma("clang loop unroll(disable)") for (unsigned spins = 0;;) {
          asm volatile("" : "+v"(vb));
          P_LD
          unsigned m = 0;
#pragma unroll
          for (int k = 0; k < 10; ++k) m = mx4(mx4(m, pv[0][k]), pv[1][k]);
          if (!anysent(m)) break;
          __builtin_amdgcn_s_sleep(8);
          if (spin_fail(spins, tmo, 0x30015)) break;
        }
        P_MM(0, 10)
      }
      EV(7);
      if (__builtin_expect(s < NSEG - 2 && nrdy < s + 2, 0)) {
        _Pragma("clang loop unroll(disable)") for (unsigned spins = 0;;) {
          VW_FLAGS
          if (nrdy >= s + 2) break;
          __builtin_amdgcn_s_sleep(8);
          if (spin_fail(spins, tmo, 0x30018)) break;
        }
        va = vb;
        NX_LD(nx, 0, NX1)
      }
      NX_LD(raw, NX1, 16)
      SB();
      OPQ(vr, L_R + (s & 2) * 8192 + l16);
#pragma unroll
      for (int q = 0; q < 4; ++q) *(LAS f32x4*)(L + vr + q * 4096 + n * 1024) = ac[q >> 1][q & 1];
      asm volatile("s_waitcnt lgkmcnt(0)" ::: "memory");
      fl[20 + n] = s + 1;
      lds_wait_ge(20, 3, s + 1, tmo, 0x30016);
      {
        const LAS f32x4* pr = (const LAS f32x4*)(L + vr + n * 4096);
        const f32x4 o = (pr[0] + pr[64]) + (pr[128] + pr[192]);
        const int lo = olane();
        *(v2u*)(out + (size_t)(s * 32 + 16 * (n >> 1) + (lo & 15)) * 2048 + 1024 + 16 * (c0 + (n & 1)) + 4 * (lo >> 4)) =
            v2u{pk2(o[0], o[1]), pk2(o[2], o[3])};
      }
      EV(5);
#pragma unroll
      for (int q = 0; q < NX1; ++q) raw[q >> 3][q & 7] = nx[q >> 3][q & 7];
      EVDUMP(s);
    }
  } else if (w == 6) {
    if (c == 1) hs_lead<64>(rs, false, CNT_HS, 192u);
  } else {
    int narm = NARM + team;
    const int sdk_ = NS2F(TN(2, SDK)), cv1_ = NS2F(TN(3, CV1)), cv2_ = NS2F(TN(4, CV2));
    arm_wait(rs, tmo);
    PTW(4, 7);
    Clk ck;
    clk_init(ck);
    unsigned t1 = cyc32();
    const int mg_ = NS2F(TN(0, MARG)), tol_ = NS2F(TN(13, TOL)), xl_ = NS2F(TN(14, XLATE));
    Pace pc = {0u, NS2F(ED0), NS2F(EA0), 0};
    const int sti_ = TN(15, STI), ofd_ = NS2F(TN(9, OFD));
#pragma unroll 1
    for (int s = 0; s < NSEG; ++s) {
      EVDECL;
      const bool on = (s & 1) == team;
      if (!on || s < 3) clk_update(ck, s < 3);
      int k1 = (cv1_ * (int)ck.cq) >> 16, k2 = (cv2_ * (int)ck.cq) >> 16;
      int sst = (int)OFF_STEP + s * (int)UNIT + R_ST + ((cown ^ xreg(r_s + s)) << 12);
      asm volatile("" : "+s"(k1), "+s"(k2), "+s"(sst));
      OPQ(va, lane * 32);
      OPQ(vt, L_F + 48);
      wait_cyc(t1);
      EV(0);
      v2u st = __builtin_amdgcn_raw_buffer_load_b64(rs, va, sst, AUX_SC1);
      clk_fresh(ck);
      _Pragma("clang loop unroll(disable)") for (unsigned spins = 0; anysent(st[0]);) {
        if (spins >= 2) __builtin_amdgcn_s_sleep(4);
        if (spin_fail(spins, tmo, 0x3000b)) break;
        asm volatile("" : "+v"(va));
        wait_cyc(t1 + spins * sti_);
        st = __builtin_amdgcn_raw_buffer_load_b64(rs, va, sst + (spins == 1 ? 2048 : (spins == 2 ? 32768 : 0)), AUX_SC1);
        clk_fresh(ck);
      }
      if (s < 4) PTW(11 + s, 7);
      const int dm = stamp_max(ck, st[0]);
      const unsigned tr = clk_cyc(ck, dm);
      if (on) {
        *(volatile LAS v4u*)(L + vt) = v4u{tr + k2, tr, ck.inv, 0u};
        EV(1);
        wait_cyc(tr + k1);
        RAW_BARRIER();
        if (s < NSEG - 1) BARE_BARRIER();
        if (narm < NSEG) {
          __builtin_amdgcn_s_sleep(5);
          lds_wait_ge(2, 1, s + 1, tmo, 0x3001b);
#pragma unroll 1
          for (int q = 0; q < 2; ++q) {
            if (narm < NSEG) ARM_REC(narm)
            ++narm;
          }
        }
      }
      pace_step(pc, s, (ck.r << 4) + dm, st[1], mg_, tol_, xl_);
      t1 = clk_cyc(ck, dm + pc.dq + pc.ea - sdk_ + (on ? ofd_ : 0));
      EVDUMP(s);
      EVCLK(s, ck);
    }
  }
  return 0u;
}
__global__ __launch_bounds__(512, 2) void mega_kernel(const u16* __restrict__ x, const u16* __restrict__ mem0,
                                                      const u16* __restrict__ Wq, const u16* __restrict__ Wk,
                                                      const u16* __restrict__ Wv, const u16* __restrict__ Wo,
                                                      char* __restrict__ ws, u16* __restrict__ out,
                                                      const u16* __restrict__ ax, const u16* __restrict__ awr,
                                                      const u16* __restrict__ awi) {
  ws += (size_t)(-(uintptr_t)ws) & (UNIT - 1);
  const int bx = BIDX;
  const int w = __builtin_amdgcn_readfirstlane(threadIdx.x >> 6);
  const int xg = bx & 7, idx = bx >> 3;
#define TEND
#define TMID(k)
#define CODEPF 16
  if (w == 7 && idx < CODEPF) {
    u64 pc;
    unsigned len, keep;
    asm volatile("s_getpc_b64 %0\n.Lcode_here:\n\ts_mov_b32 %1, .Lcode_end-.Lcode_here\n\t.subsection 1\n.Lcode_end:\n\t.subsection 0" : "=s"(pc), "=s"(len));
    const v4u rc = {(unsigned)pc, (unsigned)(pc >> 32) & 0xffffu, len, 0x00020000u};
    asm volatile("s_mov_b32 %0, m0\n\ts_mov_b32 m0, %3\n\ts_nop 0\n\tbuffer_load_dword %1, %2, 0 offen lds\n\ts_mov_b32 m0, %0"
                 : "=&s"(keep)
                 : "v"(olane() * 128 + idx * 8192), "s"(rc), "s"(32768 + 7 * 2048)
                 : "memory");
  }
  const u64 nonce = hs_post(__builtin_amdgcn_make_buffer_rsrc(ws, 0, 65536, 0x00020000), w, xg >= NXC && (idx & PQ) ? 7 : 0);
  TMID(0);
  int role, h, par = 0;
  if (xg < NXC) {
    role = idx >= 2 * PQ ? 4 : idx >= PQ;
    h = xg * PQ + idx % PQ;
  } else {
    role = 2 + (idx / PQ & 1);
    par = idx / (2 * PQ);
    h = (xg - NXC) * PQ + idx % PQ;
  }
  if (role == 0) wroles<0>(h, par, idx & 7, xg, w, nonce, x, Wq, Wk, Wv, Wo, ws);
  if (role == 1) wroles<1>(h, par, idx & 7, xg, w, nonce, x, Wq, Wk, Wv, Wo, ws);
  if (role == 2) wroles<2>(h, par, idx & 7, xg, w, nonce, x, Wq, Wk, Wv, Wo, ws);
  if (role == 3) wroles<3>(h, par, idx & 7, xg, w, nonce, x, Wq, Wk, Wv, Wo, ws);
  const int oc = xg * PO + idx - 2 * PQ;
  unsigned perm = 0;
  if (role == 4 && HLT == 0) help(oc, w, Wq, Wk, Wv);
  if (role == 4) {
    perm = owner<true>(oc, w, nonce, mem0, ws, out, 0u, Wq, Wk, Wv);
    VMCNT(0);
  }
  TMID(2);
  const __amdgpu_buffer_rsrc_t rs = __builtin_amdgcn_make_buffer_rsrc(ws, 0, 65536, 0x00020000);
  gu32* tmo = (gu32*)(ws + OFF_TMO);
  volatile LAS int* sact = (volatile LAS int*)(g_lds + 163584);
  unsigned idle = 0;
  bool tab = false;
#define FSIDE 0
  char* const x1 = ws + OFF_X1 + ((size_t)(-(uintptr_t)(ws + OFF_X1)) & ((2u << 20) - 1));
  int sd = 0;
#define TLOG(id)
  auto pick = [&]() __attribute__((always_inline)) {
    int off = CNT_FFT1 + (olane() & 7) * 128;
    asm volatile("" : "+v"(off));
    const unsigned v = __builtin_amdgcn_raw_buffer_load_b32(rs, off, 0, AUX_SC1) + 1u;
    const unsigned d1 = __builtin_amdgcn_readlane(v, 0), d2 = __builtin_amdgcn_readlane(v, 1);
    const unsigned q1 = __builtin_amdgcn_readlane(v, 4), q2 = __builtin_amdgcn_readlane(v, 5), q3 = __builtin_amdgcn_readlane(v, 6);
    int kind = -1, qoff = 0;
    unsigned lim = 0;
    if (q1 < 130u - NF1O) kind = 1, qoff = Q_FFT1, lim = 130u - NF1O;
    else if (d1 >= 128u && q2 < 65u) kind = 2, qoff = Q_FFT2, lim = 65u;
    else if (d2 >= 65u && q3 < 128u) kind = 3, qoff = Q_FFT3, lim = 128u;
    else if (q3 >= 128u) kind = -2;
    int act = kind;
    if (kind >= 0) {
      unsigned t = 0;
      if (olane() == 0) t = __hip_atomic_fetch_add((gu32*)(ws + qoff), 1u, RLX_AGENT) + 1u;
      t = __builtin_amdgcn_readfirstlane(t);
      act = t < lim ? (kind << 16) | (int)t : -3;
    }
    return act;
  };
  for (;;) {
    if (w == 0) *sact = role == 4 ? (oc > 1 && oc < NF1O ? 0x18000 | oc : -2) : pick();
    RAW_BARRIER();
    const int act = __builtin_amdgcn_readfirstlane(*sact);
    RAW_BARRIER();
    if (act == -2) break;
    if (act >= 0) {
      const int kind = act >> 16, t = act & 0xffff;
      TLOG(1000 + kind);
      if (!tab) {
        if (w == 7) sact[1] = numa_far(numa_load(rs, tmo));
        fft_table(w);
        sd = (__builtin_amdgcn_readfirstlane(sact[1]) ^ (int)((uintptr_t)x1 >> 8) ^ FSIDE) & 8192;
      }
      tab = true;
      if (kind == 1) {
        fft_pass1(ws, x1, sd, ax, awr, awi, (t & 0x8000) ? t & 0xff : (t < 2 ? t : NF1O - 2 + t), w);
        count_done(ws, CNT_FFT1, w);
      } else if (kind == 2) {
        fft_pass2(ws, x1, sd, t == 0 ? 64 : t - 1, w);
        count_done(ws, CNT_FFT2, w);
      } else fft_pass3(ws, x1, sd, out, t, w);
      TLOG(999);
      if (role == 4) break;
    } else if (act == -1) {
      __builtin_amdgcn_s_sleep(64);
      if (spin_fail(idle, tmo, 0x30022)) break;
    }
  }
  if (role == 4) {
    owner<false>(oc, w, nonce, mem0, ws, out, perm, Wq, Wk, Wv);
    return;
  }
  TEND;
}

extern "C" void kernel_launch(void* const* d_in, const int* in_sizes, int n_in, void* d_out, int out_size, void* d_ws,
                              size_t ws_size, hipStream_t stream) {
  const u16* A_x = (const u16*)d_in[0];
  const u16* A_wr = (const u16*)d_in[1];
  const u16* A_wi = (const u16*)d_in[2];
  const u16* B_x = (const u16*)d_in[3];
  const u16* B_mem0 = (const u16*)d_in[4];
  const u16* Wq = (const u16*)d_in[5];
  const u16* Wk = (const u16*)d_in[6];
  const u16* Wv = (const u16*)d_in[7];
  const u16* Wo = (const u16*)d_in[8];
  u16* out = (u16*)d_out;
  char* ws = (char*)d_ws;

  mega_kernel<<<256, 512, 0, stream>>>(B_x, B_mem0, Wq, Wk, Wv, Wo, ws, out, A_x, A_wr, A_wi);
}
```

```cpp
#include <hip/hip_runtime.h>
#include <hip/hip_bf16.h>
#include <stdint.h>
#include <type_traits>

typedef unsigned short u16;
typedef unsigned long long u64;
using bf16x8 = __attribute__((ext_vector_type(8))) __bf16;
using bf16x16 = __attribute__((ext_vector_type(16))) __bf16;
using f32x4 = __attribute__((ext_vector_type(4))) float;
typedef unsigned v32u __attribute__((ext_vector_type(32)));
typedef unsigned v16u __attribute__((ext_vector_type(16)));
typedef unsigned v8u __attribute__((ext_vector_type(8)));
typedef unsigned v4u __attribute__((ext_vector_type(4)));
typedef unsigned v2u __attribute__((ext_vector_type(2)));
typedef short s16x4 __attribute__((ext_vector_type(4)));
typedef float f32x2 __attribute__((ext_vector_type(2)));
#define GAS __attribute__((address_space(1)))
#define LAS __attribute__((address_space(3)))
typedef GAS unsigned gu32;
#define RLX_AGENT __ATOMIC_RELAXED, __HIP_MEMORY_SCOPE_AGENT

constexpr int S = 2048, D = 1024, H = 32, DH = 128, SEG = 32, NSEG = 64;
constexpr int DF = 513;

#define XSW 4
#define BIDX (blockIdx.x ^ XSW)
constexpr int NXC = 4;
constexpr int PQ = 32 / NXC, PO = 64 / NXC;

__shared__ __attribute__((aligned(16))) char g_lds[163840];
#define SPINCAP (1u << 18)
constexpr unsigned kSpinCap = SPINCAP;
constexpr int AUX_SC1 = 16;
constexpr int OOB = 0x7ff00000;
constexpr size_t UNIT = 524288;
constexpr int F_QK = 0, F_VW = 8192, CNT_FFT1 = 16384, CNT_FFT2 = 16512, CNT_ARM = 16640;
constexpr int Q_FFT1 = 16896, Q_FFT2 = 17024, Q_FFT3 = 17152;
constexpr size_t OFF_TMO = 20480;
constexpr size_t OFF_CAL = 24576, OFF_XID = 25088;
constexpr size_t ARM_HOST = 28672;
constexpr int OFF_REQ = 49152, OFF_ACK = 53248, CNT_HS = 17280, CNT_HSO = 17408;
constexpr size_t OFF_SCR = 65536;
constexpr size_t OFF_VM = UNIT;
constexpr int VM_SLOT = 4 << 20, VM_SLOT2 = 8 << 20;
constexpr size_t OFF_DBG = 3 * UNIT;
constexpr size_t OFF_STEP = 27 * UNIT;
constexpr int R_MEM = 0, R_EX = 131072, R_PM = 262144, R_SC = 294912, R_ST = 327680, R_PX = 393216, R_DUM = 475136;
constexpr size_t OFF_QK = (size_t)48 << 20;
constexpr size_t OFF_X1 = (size_t)217 << 20;
#define GST 64
constexpr size_t OFF_VWX = (size_t)(GST == 64 ? 89 : 80) << 20;
constexpr size_t WS_USED = (size_t)217 << 20;
constexpr int NARM = 4;
#define SCL 1
#define SC_POS(q, r) ((q) * 256 + ((r) & 4) * 32 + ((r) & 3) * 16)

__device__ __forceinline__ int olane() {
  int t;
  asm volatile("v_mbcnt_lo_u32_b32 %0, -1, 0\n\tv_mbcnt_hi_u32_b32 %0, -1, %0" : "=v"(t));
  __builtin_assume(t >= 0 && t < 64);
  return t;
}
__device__ __forceinline__ float bf2f(u16 v) { return __uint_as_float((unsigned)v << 16); }
__device__ __forceinline__ unsigned pk2(float a, float b) {
  typedef __bf16 b2 __attribute__((ext_vector_type(2)));
  f32x2 f = {a, b};
  b2 r = __builtin_convertvector(f, b2);
  return __builtin_bit_cast(unsigned, r);
}
__device__ __forceinline__ unsigned pk2s(float a, float b) { return min(pk2(a, b), 0xfffffffeu); }
__device__ __forceinline__ u16 f2bf(float f) { return (u16)(pk2(f, 0.f) & 0xffffu); }
__device__ __forceinline__ float rbf(float f) { return __uint_as_float(pk2(0.f, f)); }
__device__ __forceinline__ float opq_one() {
  float o = 1.f;
  asm("" : "+s"(o));
  return o;
}
__device__ __forceinline__ unsigned fold_pk(f32x4 a, float one) { return pk2(a[0] + a[2], __builtin_fmaf(a[1], one, a[3])); }
__device__ __forceinline__ float mul1(float a, float c) {
  float r;
  asm("v_mul_f32 %0, %2, %1" : "=v"(r) : "v"(a), "s"(c));
  return r;
}
__device__ __forceinline__ float fma1(float a, float c, float b) {
  float r;
  asm("v_fma_f32 %0, %1, %2, %3" : "=v"(r) : "v"(a), "s"(c), "v"(b));
  return r;
}
__device__ __forceinline__ int xreg(int r) { return ((r & 1) << 2) | ((r >> 1) & 1) | ((r >> 1) & 2); }

__device__ __forceinline__ void tmo_report(gu32* tmo, unsigned code) {
  unsigned z = 0, c = code;
  asm volatile("" : "+v"(z), "+v"(c));
  asm volatile("global_store_dword %0, %1, %2 sc1" ::"v"(z), "v"(c), "s"(tmo) : "memory");
}
__device__ __forceinline__ bool spin_fail(unsigned& spins, gu32* tmo, unsigned code) {
  asm volatile("" ::: "memory");
  if (__builtin_expect(++spins > kSpinCap, 0)) {
    tmo_report(tmo, code);
    return true;
  }
  return false;
}
__device__ __forceinline__ unsigned wave_min(unsigned v) {
  v = min(v, (unsigned)__builtin_amdgcn_mov_dpp((int)v, 0x121, 0xf, 0xf, true));
  v = min(v, (unsigned)__builtin_amdgcn_mov_dpp((int)v, 0x122, 0xf, 0xf, true));
  v = min(v, (unsigned)__builtin_amdgcn_mov_dpp((int)v, 0x124, 0xf, 0xf, true));
  v = min(v, (unsigned)__builtin_amdgcn_mov_dpp((int)v, 0x128, 0xf, 0xf, true));
  auto r = __builtin_amdgcn_permlane32_swap(v, v, false, false);
  v = min(r[0], r[1]);
  auto q = __builtin_amdgcn_permlane16_swap(v, v, false, false);
  return min(q[0], q[1]);
}
__device__ __forceinline__ float xmax16_32(float x) {
  auto r = __builtin_amdgcn_permlane32_swap(__float_as_uint(x), __float_as_uint(x), false, false);
  x = __builtin_amdgcn_fmed3f(__uint_as_float(r[0]), __uint_as_float(r[1]), __builtin_inff());
  auto q = __builtin_amdgcn_permlane16_swap(__float_as_uint(x), __float_as_uint(x), false, false);
  return __builtin_amdgcn_fmed3f(__uint_as_float(q[0]), __uint_as_float(q[1]), __builtin_inff());
}
__device__ __forceinline__ float xsum16(float x) {
  auto q = __builtin_amdgcn_permlane16_swap(__float_as_uint(x), __float_as_uint(x), false, false);
  return __uint_as_float(q[0]) + __uint_as_float(q[1]);
}
__device__ __forceinline__ float xsum16_32(float x) {
  auto r = __builtin_amdgcn_permlane32_swap(__float_as_uint(x), __float_as_uint(x), false, false);
  return xsum16(__uint_as_float(r[0]) + __uint_as_float(r[1]));
}
__device__ __forceinline__ unsigned mx4(unsigned m, v4u v) { return max(max(max(max(m, v[0]), v[1]), v[2]), v[3]); }
__device__ __forceinline__ bool anysent(unsigned m) { return __builtin_amdgcn_ballot_w64(m == 0xffffffffu) != 0ull; }

__device__ __forceinline__ unsigned now32() { return (unsigned)__builtin_amdgcn_s_memrealtime(); }
__device__ __forceinline__ unsigned cyc32() { return (unsigned)__builtin_amdgcn_s_memtime(); }
#define CMAX 14000
__device__ __forceinline__ void wait_cyc(unsigned t) {
  t = __builtin_amdgcn_readfirstlane(t);
  int d = (int)(t - cyc32());
  if (d > CMAX) return;
  while (d > 260) {
    __builtin_amdgcn_s_sleep(1);
    d = (int)(t - cyc32());
  }
  d = __builtin_amdgcn_readfirstlane(d - 70);
  if (d > 0) asm volatile("1: s_sub_u32 %0, %0, 28\n\ts_cbranch_scc0 1b" : "+s"(d)::"scc", "memory");
}
#define CLKSPAN 124u
struct Clk {
  unsigned r, m;
  unsigned cq, inv;
};
__device__ __forceinline__ bool clk_edge(unsigned& r, unsigned& m, int tries) {
  constexpr int NS = 14;
#pragma unroll 1
  for (int k = 0; k < tries; ++k) {
    u64 t[NS], m0, m1;
    asm volatile("s_memtime %0" : "=s"(m0));
#pragma unroll
    for (int i = 0; i < NS; ++i) {
      asm volatile("s_memrealtime %0\n\ts_nop 0" : "=s"(t[i]));
      if (i == NS - 2) asm volatile("s_memtime %0" : "=s"(m1));
    }
    asm volatile("s_waitcnt lgkmcnt(0)" : "+s"(m0), "+s"(m1), "+s"(t[0]), "+s"(t[1]), "+s"(t[2]), "+s"(t[3]), "+s"(t[4]), "+s"(t[5]),
                 "+s"(t[6]), "+s"(t[7]), "+s"(t[8]), "+s"(t[9]), "+s"(t[10]), "+s"(t[11]), "+s"(t[12]), "+s"(t[13]));
    unsigned n = 0;
#pragma unroll
    for (int i = 1; i < NS; ++i) asm("s_cmp_eq_u32 %1, %2\n\ts_addc_u32 %0, %0, 0" : "+s"(n) : "s"((unsigned)t[i]), "s"((unsigned)t[0]) : "scc");
    r = (unsigned)t[0] + 4;
    m = (unsigned)m0 + n * 8;
    asm volatile("" : "+s"(m));
    if ((unsigned)m1 - (unsigned)m0 <= CLKSPAN) return true;
  }
  return false;
}
__device__ __forceinline__ unsigned sdiv(unsigned a, unsigned b) {
  unsigned r;
  float t, u;
  asm volatile("v_cvt_f32_u32 %1, %4\n\tv_cvt_f32_u32 %2, %3\n\tv_rcp_f32 %1, %1\n\ts_nop 0\n\tv_mul_f32 %1, %1, %2\n\tv_cvt_u32_f32 %1, %1\n\t"
               "s_nop 0\n\tv_readfirstlane_b32 %0, %1\n\ts_nop 3"
               : "=s"(r), "=&v"(t), "=&v"(u)
               : "s"(a), "s"(b));
  return r;
}
__device__ __forceinline__ void clk_init(Clk& c) {
  clk_edge(c.r, c.m, 4);
  c.cq = 92500u;
  c.inv = 46432u;
}
__device__ __forceinline__ void clk_update(Clk& c, bool first = false) {
  unsigned r, m;
  if (!clk_edge(r, m, first ? 2 : 1) && r - c.r < 1500u) return;
  if (r - c.r >= 100u && r - c.r < 40000u) {
    unsigned q = sdiv((m - c.m) << 12, r - c.r);
    asm("s_max_u32 %0, %0, 0x101d0\n\ts_min_u32 %0, %0, 0x1b580" : "+s"(q)::"scc");
    c.cq = first ? q : (3 * c.cq + q) >> 2;
    c.inv = sdiv(0xffffffffu, c.cq);
  }
  c.r = r;
  c.m = m;
}
__device__ __forceinline__ void clk_fresh(Clk& c) {
  unsigned r, m;
  if (clk_edge(r, m, 1)) {
    c.r = r;
    c.m = m;
  }
}
__device__ __forceinline__ unsigned clk_fine(const Clk& c, unsigned m) {
  return (c.r << 4) + (unsigned)(((u64)(m - c.m) * c.inv) >> 16);
}
__device__ __forceinline__ int stamp_max(const Clk& c, unsigned st) {
  const unsigned tn = (c.r << 4) + 65536u;
  return 65536 - __builtin_amdgcn_readfirstlane((int)wave_min(tn - st));
}
__device__ __forceinline__ unsigned clk_cyc(const Clk& c, int d) { return c.m + (unsigned)(((long long)d * (int)c.cq) >> 16); }
#define NS2F(ns) (((ns) * 8) / 5)
struct Pace {
  unsigned rf;
  int ed, ea, dq;
};
__device__ __forceinline__ void pace_step(Pace& p, int s, unsigned rf, unsigned rep, int marg, int tol, int xlate) {
  const int arr = (int)(rf - p.rf) - p.dq;
  const bool late = s == 0 || arr > p.ea + tol;
  if (s > 0) {
    const int md = (int)~__builtin_amdgcn_readfirstlane(wave_min(~rep));
    p.ea = min(arr, p.ea + 5);
    p.ed = (int)min((unsigned)md, (unsigned)p.ed + 5u);
  }
  p.rf = rf;
  p.dq = p.ed + marg + (late ? xlate : 0);
}

template <int SLP>
__device__ __forceinline__ void wait_flag(__amdgpu_buffer_rsrc_t rs, int off, gu32* tmo, unsigned code) {
  _Pragma("clang loop unroll(disable)") for (unsigned spins = 0;;) {
    asm volatile("" : "+v"(off));
    unsigned v = __builtin_amdgcn_raw_buffer_load_b8(rs, off, 0, AUX_SC1);
    if (__all(v == 0u)) break;
    __builtin_amdgcn_s_sleep(SLP);
    if (spin_fail(spins, tmo, code)) break;
  }
}
#define RAW_BARRIER()                                    \
  do {                                                   \
    asm volatile("" ::: "memory");                       \
    __builtin_amdgcn_s_waitcnt(0xc07f);                  \
    __builtin_amdgcn_s_barrier();                        \
    asm volatile("" ::: "memory");                       \
  } while (0)
#define BARE_BARRIER()                 \
  do {                                 \
    asm volatile("" ::: "memory");     \
    __builtin_amdgcn_s_barrier();      \
    asm volatile("" ::: "memory");     \
  } while (0)
#define VMCNT(n) asm volatile("s_waitcnt vmcnt(" #n ")" ::: "memory")
#define SB() __builtin_amdgcn_sched_barrier(0)
#define OPQ(v, e) int v = (e); asm volatile("" : "+v"(v))
template <bool NT = false>
__device__ __forceinline__ void glds16(const void* base, int voff, unsigned dst) {
  unsigned keep;
  if constexpr (NT)
    asm volatile("s_mov_b32 %0, m0\n\ts_mov_b32 m0, %3\n\ts_nop 0\n\tglobal_load_lds_dwordx4 %1, %2 nt\n\ts_mov_b32 m0, %0"
                 : "=&s"(keep)
                 : "v"(voff), "s"(base), "s"(dst)
                 : "memory");
  else
  asm volatile("s_mov_b32 %0, m0\n\ts_mov_b32 m0, %3\n\ts_nop 0\n\tglobal_load_lds_dwordx4 %1, %2\n\ts_mov_b32 m0, %0"
               : "=&s"(keep)
               : "v"(voff), "s"(base), "s"(dst)
               : "memory");
}
__device__ __forceinline__ void touch4(const void* base, int voff, unsigned dst) {
  unsigned keep;
  asm volatile("s_mov_b32 %0, m0\n\ts_mov_b32 m0, %3\n\ts_nop 0\n\tglobal_load_lds_dword %1, %2\n\ts_mov_b32 m0, %0"
               : "=&s"(keep)
               : "v"(voff), "s"(base), "s"(dst)
               : "memory");
}
__device__ __forceinline__ f32x4 smf(v4u a, v8u b, f32x4 c, int idx) {
  return __builtin_amdgcn_smfmac_f32_16x16x64_bf16(__builtin_bit_cast(bf16x8, a), __builtin_bit_cast(bf16x16, b), c, idx, 0, 0);
}
__device__ __forceinline__ f32x4 smf1(v4u a, v8u b, f32x4 c, int idx) {
  return __builtin_amdgcn_smfmac_f32_16x16x64_bf16(__builtin_bit_cast(bf16x8, a), __builtin_bit_cast(bf16x16, b), c, idx, 0, 1);
}
__device__ __forceinline__ f32x4 mma(v4u a, v4u b, f32x4 c) {
  return __builtin_amdgcn_mfma_f32_16x16x32_bf16(__builtin_bit_cast(bf16x8, a), __builtin_bit_cast(bf16x8, b), c, 0, 0, 0);
}
__device__ __forceinline__ void st128(v4u d, __amdgpu_buffer_rsrc_t rs, int voff, int soff) {
  __builtin_amdgcn_raw_buffer_store_b128(d, rs, voff, soff, AUX_SC1);
  asm volatile("s_nop 1" ::"v"(d));
}
__device__ __forceinline__ f32x4 zero4() {
  f32x2 a, b;
  asm volatile("v_mov_b64 %0, 0" : "=v"(a));
  asm volatile("v_mov_b64 %0, 0" : "=v"(b));
  return f32x4{a[0], a[1], b[0], b[1]};
}
__device__ __forceinline__ f32x2 zero2() {
  f32x2 a;
  asm volatile("v_mov_b64 %0, 0" : "=v"(a));
  return a;
}
__device__ __forceinline__ v4u lo4(v8u v) { return v4u{v[0], v[1], v[2], v[3]}; }
__device__ __forceinline__ v4u hi4(v8u v) { return v4u{v[4], v[5], v[6], v[7]}; }
__device__ __forceinline__ v8u cat8(v4u a, v4u b) { return v8u{a[0], a[1], a[2], a[3], b[0], b[1], b[2], b[3]}; }
__device__ __forceinline__ v32u cat32(v8u a, v8u b, v8u c, v8u d) {
  const v16u x = __builtin_shufflevector(a, b, 0, 1, 2, 3, 4, 5, 6, 7, 8, 9, 10, 11, 12, 13, 14, 15);
  const v16u y = __builtin_shufflevector(c, d, 0, 1, 2, 3, 4, 5, 6, 7, 8, 9, 10, 11, 12, 13, 14, 15);
  return __builtin_shufflevector(x, y, 0, 1, 2, 3, 4, 5, 6, 7, 8, 9, 10, 11, 12, 13, 14, 15, 16, 17, 18, 19, 20, 21, 22, 23, 24, 25, 26,
                                 27, 28, 29, 30, 31);
}
__device__ __forceinline__ v2u trd(LAS char* p) {
  return __builtin_bit_cast(v2u, __builtin_amdgcn_ds_read_tr16_b64_v4i16((LAS s16x4*)p));
}
__device__ __forceinline__ int vm_slot(int s) { return (s & 1) * VM_SLOT + ((s >> 1) & 1) * VM_SLOT2; }
__device__ __forceinline__ int cls_of(unsigned perm, int x) { return (int)((perm >> (3 * x)) & 7u); }

__device__ __forceinline__ void arm_wait(__amdgpu_buffer_rsrc_t rs, gu32* tmo) {
  _Pragma("clang loop unroll(disable)") for (unsigned spins = 0;;) {
    OPQ(off, CNT_ARM);
    if (__all(__builtin_amdgcn_raw_buffer_load_b32(rs, off, 0, AUX_SC1) + 1u >= 64u)) break;
    __builtin_amdgcn_s_sleep(8);
    if (spin_fail(spins, tmo, 0x30007)) break;
  }
}
__device__ __forceinline__ v2u numa_load(__amdgpu_buffer_rsrc_t rs, gu32* tmo) {
  unsigned c, xid;
  _Pragma("clang loop unroll(disable)") for (unsigned spins = 0;;) {
    OPQ(a, olane() * 4);
    c = __builtin_amdgcn_raw_buffer_load_b32(rs, a, (int)OFF_CAL, AUX_SC1);
    xid = __builtin_amdgcn_raw_buffer_load_b32(rs, ((a >> 5) & (NXC - 1)) * 4, (int)OFF_XID, AUX_SC1);
    if (__all((c != 0xffffffffu) & (xid != 0xffffffffu))) break;
    __builtin_amdgcn_s_sleep(16);
    if (spin_fail(spins, tmo, 0x30008)) break;
  }
  return v2u{c, xid};
}
__device__ __forceinline__ unsigned numa_perm(v2u in, LAS unsigned* tab) {
  const int lane = olane();
  const unsigned c = in[0], xid = in[1];
  tab[lane] = 0xffffu;
  tab[64 + lane] = 0xffffu;
  tab[128 + lane] = 0u;
  __builtin_amdgcn_s_waitcnt(0xc07f);
  __hip_atomic_fetch_min(tab + ((c >> 16) & 127u), c & 0xffffu, __ATOMIC_RELAXED, __HIP_MEMORY_SCOPE_WORKGROUP);
  __hip_atomic_fetch_add(tab + 128 + ((c >> 21) & 3u) * 2 + ((c >> 17) & 1u), c & 0xffffu, __ATOMIC_RELAXED, __HIP_MEMORY_SCOPE_WORKGROUP);
  __builtin_amdgcn_s_waitcnt(0xc07f);
  asm volatile("" ::: "memory");
  const unsigned s0 = tab[128 + ((xid >> 2) & 3u) * 2], s1 = tab[129 + ((xid >> 2) & 3u) * 2];
  const bool far = ((lane >> 1) & 1) != (s1 < s0 ? 1 : 0);
  unsigned key = ((tab[(xid & 15u) * 8 + (lane & 7)] + (far ? 0x10000u : 0u)) << 6) | (unsigned)lane;
  if ((lane >> 3) >= NXC) key = 0xffffffffu;
  unsigned perm = 0;
#pragma unroll 1
  for (int r = 0; r < NXC; ++r) {
    const unsigned m = wave_min(key);
    const unsigned bx = (m >> 3) & 7u, bc = m & 7u;
    perm |= bc << (3 * bx);
    if ((unsigned)(lane >> 3) == bx || (unsigned)(lane & 7) == bc) key = 0xffffffffu;
  }
  return __builtin_amdgcn_readfirstlane(perm | (s1 < s0 ? 0u : 8192u));
}
__device__ __forceinline__ int numa_far(v2u in) {
  const unsigned c = in[0], x0 = __builtin_amdgcn_readfirstlane(in[1]);
  int d = (int)(c & 0xffffu);
  d = ((c >> 21) & 3u) == ((x0 >> 2) & 3u) ? ((c & 0x20000u) ? -d : d) : 0;
  d += __builtin_amdgcn_mov_dpp(d, 0x121, 0xf, 0xf, true);
  d += __builtin_amdgcn_mov_dpp(d, 0x122, 0xf, 0xf, true);
  d += __builtin_amdgcn_mov_dpp(d, 0x124, 0xf, 0xf, true);
  d += __builtin_amdgcn_mov_dpp(d, 0x128, 0xf, 0xf, true);
  d = __builtin_amdgcn_readlane(d, 0) + __builtin_amdgcn_readlane(d, 16) + __builtin_amdgcn_readlane(d, 32) + __builtin_amdgcn_readlane(d, 48);
  return d > 0 ? 0 : 8192;
}
#define VRING 64
#define QRING 64
#define VFAR 0
__device__ __forceinline__ int vw_addr(int fr, int tile, int seg, int gq) {
  return (int)OFF_VWX + ((((tile >> 3) * GST + (seg & (VRING - 1))) * 8 + (tile & 7)) * 8 + gq) * 4096;
}
__device__ __forceinline__ int fqk(int h, int seg) { return F_QK + h * 256 + seg * 4; }
__device__ __forceinline__ int fvw(int pr, int h, int seg) { return F_VW + ((pr * 4 + (h >> 3)) * 2 + (seg & 1)) * 256 + (seg >> 1) * 8 + (h & 7); }
constexpr int QK_RT = 4096;
__device__ __forceinline__ int qk_addr(int mat, int seg, int h) {
  return (int)OFF_QK + ((mat * 64 + (seg & (QRING - 1))) * 32 + h) * 8192;
}

__device__ __forceinline__ u64 hs_post(__amdgpu_buffer_rsrc_t rs, int w, int HSW) {
  const u64 n = __builtin_amdgcn_s_memrealtime();
  if (w == HSW) __builtin_amdgcn_raw_buffer_store_b64(v2u{(unsigned)n, (unsigned)(n >> 32)}, rs, olane() == 0 ? 0 : OOB,
                                                    OFF_REQ + (int)BIDX * 8, AUX_SC1);
  return n;
}
__device__ __forceinline__ bool hs_wait(__amdgpu_buffer_rsrc_t rs, u64 n, v2u v) {
  unsigned spins = 0;
  _Pragma("clang loop unroll(disable)") for (; spins < (kSpinCap << 2); ++spins) {
    if (__builtin_expect(__all(v[0] == (unsigned)n && v[1] == (unsigned)(n >> 32)), 1)) break;
    if (spins) __builtin_amdgcn_s_sleep(8);
    int z = 0;
    asm volatile("" : "+v"(z));
    v = __builtin_amdgcn_raw_buffer_load_b64(rs, z, OFF_ACK + (int)BIDX * 8, AUX_SC1);
  }
  return spins == 0;
}
__device__ __forceinline__ void cnt_inc(char* ws, int cnt) {
  if (olane() == 0) __hip_atomic_fetch_add((gu32*)(ws + cnt), 1u, RLX_AGENT);
}
template <int SLP>
__device__ __forceinline__ void hs_lead(__amdgpu_buffer_rsrc_t rs, bool fill, int cnt, unsigned want) {
  const int l16 = olane() * 16;
  if (fill) {
    for (int o = 0; o < (int)ARM_HOST; o += 1024) __builtin_amdgcn_raw_buffer_store_b128(v4u{~0u, ~0u, ~0u, ~0u}, rs, l16, o, AUX_SC1);
    VMCNT(0);
  }
  _Pragma("clang loop unroll(disable)") for (unsigned spins = 0; spins < (kSpinCap << 2); ++spins) {
    int va = l16;
    asm volatile("" : "+v"(va));
    const v4u a = __builtin_amdgcn_raw_buffer_load_b128(rs, va, OFF_REQ, AUX_SC1);
    const v4u b = __builtin_amdgcn_raw_buffer_load_b128(rs, va + 1024, OFF_REQ, AUX_SC1);
    const unsigned n = __builtin_amdgcn_raw_buffer_load_b32(rs, va & 0, cnt, AUX_SC1);
    if (__all(n + 1u >= want)) break;
    __builtin_amdgcn_raw_buffer_store_b128(a, rs, va, OFF_ACK, AUX_SC1);
    __builtin_amdgcn_raw_buffer_store_b128(b, rs, va + 1024, OFF_ACK, AUX_SC1);
    __builtin_amdgcn_s_sleep(SLP);
  }
}

#define PTW(i, wv)
#define PLDECL
#define PL(i)
#define PLDUMP(wv, mask)
#define PT(i) PTW(i, 0)
#define TN(k, d) (d)
#define EVDECL
#define EV(k)
#define EVDUMP(s)
#define EVCLK(s, c)
#define EVOUT
#define VO_A \
  "s_branch .Lgo_%=\n" \
  ".Lp0_%=:\n" \
  "s_mov_b32 %[cnt], 0x40000\n" \
  ".Lp0r_%=:\n" \
  "s_sleep 2\n" \
  "buffer_load_dwordx4 v[236:239], v251, %[rs], %[o0] offen sc1\n" \
  "s_waitcnt vmcnt(0)\n" \
  "ds_write_b128 v250, v[236:239] offset:0\n" \
  "v_max_u32 v241, v236, v237\n" \
  "v_max3_u32 v241, v241, v238, v239\n" \
  "v_cmp_eq_u32 vcc, -1, v241\n" \
  "s_cbranch_vccz .Lp0b_%=\n" \
  "s_sub_u32 %[cnt], %[cnt], 1\n" \
  "s_cbranch_scc0 .Lp0r_%=\n" \
  "s_branch .Lp0b_%=\n" \
  ".Lgo_%=:\n" \
  "ds_read_b128 v[200:203], v243 offset:32768\n" \
  "ds_read_b128 v[204:207], v243 offset:33792\n" \
  "ds_read_b128 v[208:211], v243 offset:49152\n" \
  "ds_read_b128 v[212:215], v243 offset:50176\n" \
  "v_mov_b64 v[192:193], 0\n" \
  "v_mov_b64 v[194:195], 0\n" \
  "v_mov_b64 v[196:197], 0\n" \
  "v_mov_b64 v[198:199], 0\n" \
  "s_waitcnt lgkmcnt(0)\n" \
  "s_barrier\n" \
  "buffer_load_dwordx4 v[236:239], v251, %[rs], %[o0] offen sc1\n" \
  "s_waitcnt vmcnt(0)\n" \
  "ds_write_b128 v250, v[236:239] offset:0\n" \
  "v_max3_u32 v241, v236, v237, v238\n" \
  "v_max_u32 v241, v241, v239\n" \
  "v_cmp_eq_u32 vcc, -1, v241\n" \
  "s_cbranch_vccnz .Lp0_%=\n" \
  ".Lp0b_%=:\n" \
  "s_waitcnt lgkmcnt(0)\n" \
  "s_barrier\n" \
  "ds_read_b128 v[216:219], v251 offset:0\n" \
  "ds_read_b128 v[220:223], v251 offset:1024\n" \
  "ds_read_b128 v[224:227], v251 offset:2048\n" \
  "ds_read_b128 v[228:231], v251 offset:3072\n" \
  "s_waitcnt lgkmcnt(3)\n" \
  "v_smfmac_f32_16x16x64_bf16 v[192:195], v[216:219], v[200:207], v248\n" \
  "v_smfmac_f32_16x16x64_bf16 v[196:199], v[216:219], v[208:215], v248\n" \
  "ds_read_b128 v[232:235], v251 offset:4096\n" \
  "ds_read_b128 v[236:239], v251 offset:5120\n" \
  "ds_read_b128 v[216:219], v251 offset:6144\n" \
  "s_waitcnt lgkmcnt(5)\n" \
  "v_smfmac_f32_16x16x64_bf16 v[192:195], v[220:223], v[0:7], v248\n" \
  "v_smfmac_f32_16x16x64_bf16 v[196:199], v[220:223], v[8:15], v248\n" \
  "ds_read_b128 v[220:223], v251 offset:7168\n" \
  "s_waitcnt lgkmcnt(5)\n" \
  "v_smfmac_f32_16x16x64_bf16 v[192:195], v[224:227], v[16:23], v248\n" \
  "v_smfmac_f32_16x16x64_bf16 v[196:199], v[224:227], v[24:31], v248\n" \
  "s_waitcnt lgkmcnt(4)\n" \
  "v_smfmac_f32_16x16x64_bf16 v[192:195], v[228:231], v[32:39], v248\n" \
  "v_smfmac_f32_16x16x64_bf16 v[196:199], v[228:231], v[40:47], v248\n" \
  "s_waitcnt lgkmcnt(3)\n" \
  "v_smfmac_f32_16x16x64_bf16 v[192:195], v[232:235], v[48:55], v248\n" \
  "v_mov_b64 v[200:201], 0\n" \
  "v_smfmac_f32_16x16x64_bf16 v[196:199], v[232:235], v[56:63], v248\n" \
  "v_mov_b64 v[202:203], 0\n" \
  "s_waitcnt lgkmcnt(2)\n" \
  "v_smfmac_f32_16x16x64_bf16 v[192:195], v[236:239], v[64:71], v248\n" \
  "v_mov_b64 v[204:205], 0\n" \
  "v_smfmac_f32_16x16x64_bf16 v[196:199], v[236:239], v[72:79], v248\n" \
  "v_mov_b64 v[206:207], 0\n" \
  "s_waitcnt lgkmcnt(1)\n" \
  "v_smfmac_f32_16x16x64_bf16 v[192:195], v[216:219], v[80:87], v248\n" \
  "v_mov_b64 v[208:209], 0\n" \
  "s_waitcnt lgkmcnt(0)\n" \
  "v_smfmac_f32_16x16x64_bf16 v[192:195], v[220:223], v[96:103], v248\n" \
  "v_mov_b64 v[210:211], 0\n" \
  "v_smfmac_f32_16x16x64_bf16 v[196:199], v[216:219], v[88:95], v248\n" \
  "v_mov_b64 v[212:213], 0\n" \
  "v_smfmac_f32_16x16x64_bf16 v[196:199], v[220:223], v[104:111], v248\n" \
  "v_mov_b64 v[214:215], 0\n" \
  "ds_write_b128 v250, v[192:195] offset:24576\n" \
  "s_nop 6\n" \
  "ds_write_b128 v250, v[196:199] offset:28672\n" \
  "s_barrier\n" \
  "s_waitcnt lgkmcnt(0)\n" \
  "s_barrier\n" \
  "s_nop 11\n" \
  "ds_read_b128 v[216:219], v249 offset:0\n" \
  "ds_read_b128 v[220:223], v249 offset:1024\n" \
  "ds_read_b128 v[224:227], v249 offset:16384\n" \
  "ds_read_b128 v[228:231], v249 offset:17408\n" \
  "s_barrier\n" \
  "ds_read_b64_tr_b16 v[232:233], v245 offset:17920\n" \
  "ds_read_b64_tr_b16 v[234:235], v245 offset:18112\n" \
  "ds_read_b64_tr_b16 v[236:237], v245 offset:18688\n" \
  "ds_read_b64_tr_b16 v[238:239], v245 offset:18880\n" \
  "ds_read_b64_tr_b16 v[192:193], v245 offset:16384\n" \
  "ds_read_b64_tr_b16 v[194:195], v245 offset:16576\n" \
  "ds_read_b64_tr_b16 v[196:197], v245 offset:17152\n" \
  "ds_read_b64_tr_b16 v[198:199], v245 offset:17344\n" \
  "s_waitcnt lgkmcnt(6)\n" \
  "v_smfmac_f32_16x16x64_bf16 v[200:203], v[232:235], v[216:223], v248 abid:1\n" \
  "ds_read_b128 v[216:219], v249 offset:32768\n" \
  "ds_read_b128 v[220:223], v249 offset:33792\n" \
  "v_smfmac_f32_16x16x64_bf16 v[204:207], v[232:235], v[128:135], v248 abid:1\n" \
  "s_waitcnt lgkmcnt(6)\n" \
  "v_smfmac_f32_16x16x64_bf16 v[200:203], v[236:239], v[224:231], v248 abid:1\n" \
  "ds_read_b128 v[224:227], v249 offset:49152\n" \
  "ds_read_b128 v[228:231], v249 offset:50176\n" \
  "v_smfmac_f32_16x16x64_bf16 v[204:207], v[236:239], v[136:143], v248 abid:1\n" \
  "s_waitcnt lgkmcnt(6)\n" \
  "v_smfmac_f32_16x16x64_bf16 v[200:203], v[192:195], v[176:183], v248 abid:1\n" \
  "v_smfmac_f32_16x16x64_bf16 v[204:207], v[192:195], v[112:119], v248 abid:1\n" \
  "s_waitcnt lgkmcnt(4)\n" \
  "v_smfmac_f32_16x16x64_bf16 v[200:203], v[196:199], v[184:191], v248 abid:1\n" \
  "v_smfmac_f32_16x16x64_bf16 v[204:207], v[196:199], v[120:127], v248 abid:1\n" \
  "s_waitcnt lgkmcnt(2)\n" \
  "v_smfmac_f32_16x16x64_bf16 v[208:211], v[192:195], v[216:223], v248 abid:1\n" \
  "ds_read_b128 v[216:219], v243 offset:0\n" \
  "ds_read_b128 v[220:223], v243 offset:1024\n" \
  "v_smfmac_f32_16x16x64_bf16 v[212:215], v[192:195], v[144:151], v248 abid:1\n" \
  "s_waitcnt lgkmcnt(2)\n" \
  "v_smfmac_f32_16x16x64_bf16 v[208:211], v[196:199], v[224:231], v248 abid:1\n" \
  "ds_read_b128 v[224:227], v243 offset:16384\n" \
  "ds_read_b128 v[228:231], v243 offset:17408\n" \
  "v_cvt_pk_bf16_f32 v200, v200, v201\n" \
  "v_smfmac_f32_16x16x64_bf16 v[212:215], v[196:199], v[152:159], v248 abid:1\n" \
  "v_cvt_pk_bf16_f32 v201, v202, v203\n" \
  "v_smfmac_f32_16x16x64_bf16 v[212:215], v[232:235], v[160:167], v248 abid:1\n" \
  "v_cvt_pk_bf16_f32 v202, v204, v205\n" \
  "v_smfmac_f32_16x16x64_bf16 v[212:215], v[236:239], v[168:175], v248 abid:1\n" \
  "v_cvt_pk_bf16_f32 v203, v206, v207\n" \
  "s_waitcnt lgkmcnt(2)\n" \
  "v_smfmac_f32_16x16x64_bf16 v[208:211], v[232:235], v[216:223], v248 abid:1\n" \
  "v_permlane16_swap_b32 v200, v202\n" \
  "s_waitcnt lgkmcnt(0)\n" \
  "v_smfmac_f32_16x16x64_bf16 v[208:211], v[236:239], v[224:231], v248 abid:1\n" \
  "v_permlane16_swap_b32 v201, v203\n" \
  "buffer_store_dwordx4 v[200:203], v247, %[rs], %[vs] offen sc1\n" \
  "v_cvt_pk_bf16_f32 v212, v212, v213\n" \
  "v_cvt_pk_bf16_f32 v213, v214, v215\n" \
  "s_nop 4\n" \
  "v_cvt_pk_bf16_f32 v211, v210, v211\n" \
  "v_cvt_pk_bf16_f32 v210, v208, v209\n" \
  "s_nop 0\n" \
  "v_permlane16_swap_b32 v211, v213\n" \
  "v_permlane16_swap_b32 v210, v212\n" \
  "buffer_store_dwordx4 v[210:213], v246, %[rs], %[vs] offen sc1\n" \
  "s_nop 1\n"
#define VO_B \
  "s_branch .Lgo_%=\n" \
  ".Lp0_%=:\n" \
  "s_mov_b32 %[cnt], 0x40000\n" \
  ".Lp0r_%=:\n" \
  "s_sleep 2\n" \
  "buffer_load_dwordx4 v[228:231], v251, %[rs], %[o0] offen sc1\n" \
  "buffer_load_dwordx4 v[232:235], v251, %[rs], %[o1] offen sc1\n" \
  "buffer_load_dwordx4 v[236:239], v251, %[rs], %[o2] offen sc1\n" \
  "s_waitcnt vmcnt(0)\n" \
  "ds_write_b128 v250, v[228:231] offset:0\n" \
  "v_max_u32 v241, v228, v229\n" \
  "v_max3_u32 v241, v241, v230, v231\n" \
  "ds_write_b128 v250, v[232:235] offset:4096\n" \
  "v_max3_u32 v241, v241, v232, v233\n" \
  "v_max3_u32 v241, v241, v234, v235\n" \
  "ds_write_b128 v250, v[236:239] offset:8192\n" \
  "v_max3_u32 v241, v241, v236, v237\n" \
  "v_max3_u32 v241, v241, v238, v239\n" \
  "v_cmp_eq_u32 vcc, -1, v241\n" \
  "s_cbranch_vccz .Lp0b_%=\n" \
  "s_sub_u32 %[cnt], %[cnt], 1\n" \
  "s_cbranch_scc0 .Lp0r_%=\n" \
  "s_branch .Lp0b_%=\n" \
  ".Lgo_%=:\n" \
  "ds_read_b128 v[200:203], v243 offset:32768\n" \
  "ds_read_b128 v[204:207], v243 offset:33792\n" \
  "ds_read_b128 v[208:211], v243 offset:49152\n" \
  "ds_read_b128 v[212:215], v243 offset:50176\n" \
  "v_mov_b64 v[192:193], 0\n" \
  "v_mov_b64 v[194:195], 0\n" \
  "v_mov_b64 v[196:197], 0\n" \
  "v_mov_b64 v[198:199], 0\n" \
  "s_waitcnt lgkmcnt(0)\n" \
  "s_cmp_eq_u32 %[tw], 0\n" \
  "s_cbranch_scc1 .Ltq_%=\n" \
  ".Ltl_%=:\n" \
  "s_memtime vcc\n" \
  "s_waitcnt lgkmcnt(0)\n" \
  "s_sub_i32 %[cnt], %[tg], vcc_lo\n" \
  "s_cmp_lt_i32 %[cnt], 260\n" \
  "s_cbranch_scc1 .Ltf_%=\n" \
  "s_sleep 1\n" \
  "s_branch .Ltl_%=\n" \
  ".Ltf_%=:\n" \
  "s_max_i32 %[cnt], %[cnt], 0\n" \
  ".Ltg_%=:\n" \
  "s_sub_u32 %[cnt], %[cnt], 28\n" \
  "s_cbranch_scc0 .Ltg_%=\n" \
  ".Ltq_%=:\n" \
  "s_barrier\n" \
  "buffer_load_dwordx4 v[228:231], v251, %[rs], %[o0] offen sc1\n" \
  "buffer_load_dwordx4 v[232:235], v251, %[rs], %[o1] offen sc1\n" \
  "buffer_load_dwordx4 v[236:239], v251, %[rs], %[o2] offen sc1\n" \
  "s_waitcnt vmcnt(2)\n" \
  "ds_write_b128 v250, v[228:231] offset:0\n" \
  "s_waitcnt vmcnt(1)\n" \
  "ds_write_b128 v250, v[232:235] offset:4096\n" \
  "v_max3_u32 v241, v228, v229, v230\n" \
  "v_max3_u32 v241, v241, v231, v232\n" \
  "v_max3_u32 v241, v241, v233, v234\n" \
  "s_waitcnt vmcnt(0)\n" \
  "v_max3_u32 v241, v241, v235, v236\n" \
  "v_max3_u32 v241, v241, v237, v238\n" \
  "v_max_u32 v241, v241, v239\n" \
  "v_cmp_eq_u32 vcc, -1, v241\n" \
  "s_cbranch_vccnz .Lp0_%=\n" \
  ".Lp0b_%=:\n" \
  "s_waitcnt lgkmcnt(0)\n" \
  "s_barrier\n" \
  "s_memtime vcc\n" \
  "s_waitcnt lgkmcnt(0)\n" \
  "s_sub_u32 %[cnt], vcc_lo, %[ckm]\n" \
  "s_mul_hi_u32 %[cnt], %[cnt], %[inv]\n" \
  "s_add_u32 %[cnt], %[cnt], %[r16]\n" \
  "s_and_b32 %[cnt], %[cnt], -2\n" \
  "v_mov_b32 v241, %[cnt]\n" \
  "buffer_store_dword v241, v242, %[rs], %[sst] offen sc1\n" \
  "s_nop 11\n" \
  "ds_write_b128 v250, v[236:239] offset:8192\n" \
  "ds_read_b128 v[216:219], v251 offset:8192\n" \
  "ds_read_b128 v[220:223], v251 offset:9216\n" \
  "ds_read_b128 v[224:227], v251 offset:10240\n" \
  "ds_read_b128 v[228:231], v251 offset:11264\n" \
  "s_waitcnt lgkmcnt(0)\n" \
  "v_smfmac_f32_16x16x64_bf16 v[192:195], v[216:219], v[200:207], v248\n" \
  "v_smfmac_f32_16x16x64_bf16 v[196:199], v[216:219], v[208:215], v248\n" \
  "v_mov_b64 v[200:201], 0\n" \
  "v_mov_b64 v[202:203], 0\n" \
  "v_mov_b64 v[204:205], 0\n" \
  "v_mov_b64 v[206:207], 0\n" \
  "v_mov_b64 v[208:209], 0\n" \
  "v_mov_b64 v[210:211], 0\n" \
  "v_mov_b64 v[212:213], 0\n" \
  "v_mov_b64 v[214:215], 0\n" \
  "s_barrier\n" \
  "v_smfmac_f32_16x16x64_bf16 v[192:195], v[220:223], v[0:7], v248\n" \
  "v_smfmac_f32_16x16x64_bf16 v[196:199], v[220:223], v[8:15], v248\n" \
  "ds_read_b128 v[232:235], v251 offset:12288\n" \
  "ds_read_b128 v[236:239], v251 offset:13312\n" \
  "ds_read_b128 v[216:219], v251 offset:14336\n" \
  "ds_read_b128 v[220:223], v251 offset:15360\n" \
  "v_smfmac_f32_16x16x64_bf16 v[192:195], v[224:227], v[16:23], v248\n" \
  "v_smfmac_f32_16x16x64_bf16 v[196:199], v[224:227], v[24:31], v248\n" \
  "v_smfmac_f32_16x16x64_bf16 v[192:195], v[228:231], v[32:39], v248\n" \
  "v_smfmac_f32_16x16x64_bf16 v[196:199], v[228:231], v[40:47], v248\n" \
  "s_barrier\n" \
  "ds_read_b128 v[224:227], v250 offset:20480\n" \
  "ds_read_b128 v[228:231], v250 offset:24576\n" \
  "s_waitcnt lgkmcnt(5)\n" \
  "v_smfmac_f32_16x16x64_bf16 v[192:195], v[232:235], v[48:55], v248\n" \
  "v_smfmac_f32_16x16x64_bf16 v[196:199], v[232:235], v[56:63], v248\n" \
  "s_waitcnt lgkmcnt(4)\n" \
  "v_smfmac_f32_16x16x64_bf16 v[192:195], v[236:239], v[64:71], v248\n" \
  "v_smfmac_f32_16x16x64_bf16 v[196:199], v[236:239], v[72:79], v248\n" \
  "s_waitcnt lgkmcnt(3)\n" \
  "v_smfmac_f32_16x16x64_bf16 v[192:195], v[216:219], v[80:87], v248\n" \
  "v_smfmac_f32_16x16x64_bf16 v[196:199], v[216:219], v[88:95], v248\n" \
  "s_waitcnt lgkmcnt(2)\n" \
  "v_smfmac_f32_16x16x64_bf16 v[192:195], v[220:223], v[96:103], v248\n" \
  "v_smfmac_f32_16x16x64_bf16 v[196:199], v[220:223], v[104:111], v248\n" \
  "s_waitcnt lgkmcnt(1)\n" \
  "v_add_f32 v224, v224, v226\n" \
  "v_add_f32 v225, v225, v227\n" \
  "s_waitcnt lgkmcnt(0)\n" \
  "v_add_f32 v228, v228, v230\n" \
  "v_add_f32 v229, v229, v231\n" \
  "v_add_f32 v192, v192, v194\n" \
  "v_add_f32 v193, v193, v195\n" \
  "v_add_f32 v192, v192, v224\n" \
  "v_add_f32 v193, v193, v225\n" \
  "v_cvt_pk_bf16_f32 v192, v192, v193\n" \
  "v_add_f32 v196, v196, v198\n" \
  "v_add_f32 v197, v197, v199\n" \
  "v_add_f32 v196, v196, v228\n" \
  "v_add_f32 v197, v197, v229\n" \
  "v_cvt_pk_bf16_f32 v196, v196, v197\n" \
  "ds_write2_b32 v244, v192, v196 offset1:96\n" \
  "s_waitcnt lgkmcnt(0)\n" \
  "s_barrier\n" \
  "s_nop 7\n" \
  "ds_read_b64_tr_b16 v[232:233], v245 offset:17920\n" \
  "ds_read_b64_tr_b16 v[234:235], v245 offset:18112\n" \
  "ds_read_b64_tr_b16 v[236:237], v245 offset:18688\n" \
  "ds_read_b64_tr_b16 v[238:239], v245 offset:18880\n" \
  "ds_read_b64_tr_b16 v[192:193], v245 offset:16384\n" \
  "ds_read_b64_tr_b16 v[194:195], v245 offset:16576\n" \
  "ds_read_b64_tr_b16 v[196:197], v245 offset:17152\n" \
  "ds_read_b64_tr_b16 v[198:199], v245 offset:17344\n" \
  "ds_read_b128 v[216:219], v249 offset:0\n" \
  "ds_read_b128 v[220:223], v249 offset:1024\n" \
  "ds_read_b128 v[224:227], v249 offset:16384\n" \
  "ds_read_b128 v[228:231], v249 offset:17408\n" \
  "buffer_load_dwordx2 v[240:241], v240, %[rs], %[ss] offen sc1\n" \
  "s_waitcnt lgkmcnt(2)\n" \
  "v_smfmac_f32_16x16x64_bf16 v[200:203], v[232:235], v[216:223], v248 abid:1\n" \
  "ds_read_b128 v[216:219], v249 offset:32768\n" \
  "ds_read_b128 v[220:223], v249 offset:33792\n" \
  "v_smfmac_f32_16x16x64_bf16 v[204:207], v[232:235], v[128:135], v248 abid:1\n" \
  "s_waitcnt lgkmcnt(2)\n" \
  "v_smfmac_f32_16x16x64_bf16 v[200:203], v[236:239], v[224:231], v248 abid:1\n" \
  "ds_read_b128 v[224:227], v249 offset:49152\n" \
  "ds_read_b128 v[228:231], v249 offset:50176\n" \
  "v_smfmac_f32_16x16x64_bf16 v[204:207], v[236:239], v[136:143], v248 abid:1\n" \
  "v_smfmac_f32_16x16x64_bf16 v[200:203], v[192:195], v[176:183], v248 abid:1\n" \
  "v_smfmac_f32_16x16x64_bf16 v[204:207], v[192:195], v[112:119], v248 abid:1\n" \
  "v_smfmac_f32_16x16x64_bf16 v[200:203], v[196:199], v[184:191], v248 abid:1\n" \
  "v_smfmac_f32_16x16x64_bf16 v[204:207], v[196:199], v[120:127], v248 abid:1\n" \
  "s_waitcnt lgkmcnt(2)\n" \
  "v_smfmac_f32_16x16x64_bf16 v[208:211], v[192:195], v[216:223], v248 abid:1\n" \
  "ds_read_b128 v[216:219], v243 offset:0\n" \
  "ds_read_b128 v[220:223], v243 offset:1024\n" \
  "v_smfmac_f32_16x16x64_bf16 v[212:215], v[192:195], v[144:151], v248 abid:1\n" \
  "s_waitcnt lgkmcnt(2)\n" \
  "v_smfmac_f32_16x16x64_bf16 v[208:211], v[196:199], v[224:231], v248 abid:1\n" \
  "ds_read_b128 v[224:227], v243 offset:16384\n" \
  "ds_read_b128 v[228:231], v243 offset:17408\n" \
  "v_cvt_pk_bf16_f32 v200, v200, v201\n" \
  "v_smfmac_f32_16x16x64_bf16 v[212:215], v[196:199], v[152:159], v248 abid:1\n" \
  "v_cvt_pk_bf16_f32 v201, v202, v203\n" \
  "v_smfmac_f32_16x16x64_bf16 v[212:215], v[232:235], v[160:167], v248 abid:1\n" \
  "v_cvt_pk_bf16_f32 v202, v204, v205\n" \
  "v_smfmac_f32_16x16x64_bf16 v[212:215], v[236:239], v[168:175], v248 abid:1\n" \
  "v_cvt_pk_bf16_f32 v203, v206, v207\n" \
  "s_waitcnt lgkmcnt(2)\n" \
  "v_smfmac_f32_16x16x64_bf16 v[208:211], v[232:235], v[216:223], v248 abid:1\n" \
  "v_permlane16_swap_b32 v200, v202\n" \
  "s_waitcnt lgkmcnt(0)\n" \
  "v_smfmac_f32_16x16x64_bf16 v[208:211], v[236:239], v[224:231], v248 abid:1\n" \
  "v_permlane16_swap_b32 v201, v203\n" \
  "buffer_store_dwordx4 v[200:203], v247, %[rs], %[vs] offen sc1\n" \
  "v_cvt_pk_bf16_f32 v212, v212, v213\n" \
  "v_cvt_pk_bf16_f32 v213, v214, v215\n" \
  "s_nop 4\n" \
  "v_cvt_pk_bf16_f32 v211, v210, v211\n" \
  "v_cvt_pk_bf16_f32 v210, v208, v209\n" \
  "s_nop 0\n" \
  "v_permlane16_swap_b32 v211, v213\n" \
  "v_permlane16_swap_b32 v210, v212\n" \
  "buffer_store_dwordx4 v[210:213], v246, %[rs], %[vs] offen sc1\n" \
  "s_nop 1\n" \
  "s_waitcnt vmcnt(2)\n"
#define QK_A \
  "s_branch .Lgo_%=\n" \
  ".Lp0_%=:\n" \
  "s_mov_b32 %[cnt], 0x40000\n" \
  ".Lp0r_%=:\n" \
  "s_sleep 2\n" \
  "buffer_load_dwordx4 v[236:239], v251, %[rs], %[o0] offen sc1\n" \
  "s_waitcnt vmcnt(0)\n" \
  "ds_write_b128 v250, v[236:239] offset:0\n" \
  "v_max_u32 v241, v236, v237\n" \
  "v_max3_u32 v241, v241, v238, v239\n" \
  "v_cmp_eq_u32 vcc, -1, v241\n" \
  "s_cbranch_vccz .Lp0b_%=\n" \
  "s_sub_u32 %[cnt], %[cnt], 1\n" \
  "s_cbranch_scc0 .Lp0r_%=\n" \
  "s_branch .Lp0b_%=\n" \
  ".Lw1_%=:\n" \
  "v_xor_b32 v238, 0x1000, v242\n" \
  "ds_read_b128 v[216:219], v238 offset:0\n" \
  "ds_read_b128 v[228:231], v238 offset:1024\n" \
  "ds_read_b128 v[232:235], v238 offset:2048\n" \
  "ds_read_b128 v[200:203], v238 offset:3072\n" \
  "s_waitcnt lgkmcnt(0)\n" \
  "v_mfma_f32_16x16x32_bf16 v[216:219], v[216:219], v[192:195], 0\n" \
  "v_mfma_f32_16x16x32_bf16 v[216:219], v[228:231], v[196:199], v[216:219]\n" \
  "v_mfma_f32_16x16x32_bf16 v[216:219], v[232:235], v[208:211], v[216:219]\n" \
  "v_mfma_f32_16x16x32_bf16 v[216:219], v[200:203], v[212:215], v[216:219]\n" \
  "s_nop 9\n" \
  "v_cndmask_b32 v238, v216, v218, %[hf]\n" \
  "v_cndmask_b32 v239, v217, v219, %[hf]\n" \
  "v_max3_f32 v222, |v238|, |v239|, v221\n" \
  "v_cmp_nge_f32 vcc, 0x43aa0000, v222\n" \
  "s_cbranch_vccz .Lw1b_%=\n" \
  "s_mov_b64 %[wd], -1\n" \
  "v_max3_f32 v222, v204, v205, v238\n" \
  "v_max_f32 v222, v222, v239\n" \
  "s_nop 1\n" \
  "v_max_f32_dpp v222, v222, v222 row_ror:8 row_mask:0xf bank_mask:0xf\n" \
  "v_mov_b32 v221, v222\n" \
  "s_nop 1\n" \
  "v_permlane32_swap_b32 v222, v221\n" \
  "v_max_f32 v222, v222, v221\n" \
  "v_mov_b32 v221, v222\n" \
  "s_nop 1\n" \
  "v_permlane16_swap_b32 v222, v221\n" \
  "v_max_f32 v222, v222, v221\n" \
  "v_mul_f32 v221, 0xbe0293ee, v222\n" \
  "v_fmamk_f32 v236, v204, 0x3e0293ee, v221\n" \
  "v_fmamk_f32 v237, v205, 0x3e0293ee, v221\n" \
  "v_exp_f32 v236, v236\n" \
  "v_exp_f32 v237, v237\n" \
  "s_nop 1\n" \
  "v_cvt_pk_bf16_f32 v220, v236, v237\n" \
  "v_and_b32 v220, 0x7fffffff, v220\n" \
  "s_branch .Lw1b_%=\n" \
  ".Lw2_%=:\n" \
  "v_max3_f32 v206, v204, v205, v222\n" \
  "s_nop 1\n" \
  "v_max_f32_dpp v206, v206, v206 row_ror:8 row_mask:0xf bank_mask:0xf\n" \
  "v_mov_b32 v221, v206\n" \
  "s_nop 1\n" \
  "v_permlane32_swap_b32 v206, v221\n" \
  "v_max_f32 v206, v206, v221\n" \
  "v_mov_b32 v221, v206\n" \
  "s_nop 1\n" \
  "v_permlane16_swap_b32 v206, v221\n" \
  "v_max_f32 v206, v206, v221\n" \
  "v_mul_f32 v221, 0xbe0293ee, v206\n" \
  "v_fmamk_f32 v236, v204, 0x3e0293ee, v221\n" \
  "v_fmamk_f32 v237, v205, 0x3e0293ee, v221\n" \
  "v_fmamk_f32 v239, v222, 0x3e0293ee, v221\n" \
  "v_exp_f32 v236, v236\n" \
  "v_exp_f32 v237, v237\n" \
  "v_exp_f32 v239, v239\n" \
  "s_nop 1\n" \
  "v_fma_f32 v238, v223, v239, v236\n" \
  "v_add_f32 v238, v238, v237\n" \
  "s_nop 1\n" \
  "s_branch .Lw2b_%=\n" \
  ".Lgo_%=:\n" \
  "ds_read_b128 v[200:203], v249 offset:0\n" \
  "ds_read_b128 v[204:207], v249 offset:1024\n" \
  "ds_read_b128 v[208:211], v249 offset:16384\n" \
  "ds_read_b128 v[212:215], v249 offset:17408\n" \
  "v_mov_b32 v241, 0x7f800000\n" \
  "ds_write_b32 v250, v241 offset:22528\n" \
  "v_mov_b64 v[192:193], 0\n" \
  "v_mov_b64 v[194:195], 0\n" \
  "v_mov_b64 v[196:197], 0\n" \
  "v_mov_b64 v[198:199], 0\n" \
  "s_waitcnt lgkmcnt(0)\n" \
  "s_barrier\n" \
  "buffer_load_dwordx4 v[236:239], v251, %[rs], %[o0] offen sc1\n" \
  "s_waitcnt vmcnt(0)\n" \
  "ds_write_b128 v250, v[236:239] offset:0\n" \
  "v_max3_u32 v241, v236, v237, v238\n" \
  "v_max_u32 v241, v241, v239\n" \
  "v_cmp_eq_u32 vcc, -1, v241\n" \
  "s_cbranch_vccnz .Lp0_%=\n" \
  ".Lp0b_%=:\n" \
  "s_waitcnt lgkmcnt(0)\n" \
  "s_barrier\n" \
  "ds_read_b128 v[216:219], v251 offset:0\n" \
  "ds_read_b128 v[220:223], v251 offset:1024\n" \
  "ds_read_b128 v[224:227], v251 offset:2048\n" \
  "ds_read_b128 v[228:231], v251 offset:3072\n" \
  "s_waitcnt lgkmcnt(3)\n" \
  "v_smfmac_f32_16x16x64_bf16 v[192:195], v[216:219], v[200:207], v248\n" \
  "ds_read_b128 v[200:203], v249 offset:32768\n" \
  "ds_read_b128 v[204:207], v249 offset:33792\n" \
  "s_waitcnt lgkmcnt(4)\n" \
  "v_smfmac_f32_16x16x64_bf16 v[192:195], v[220:223], v[0:7], v248\n" \
  "ds_read_b128 v[232:235], v251 offset:4096\n" \
  "v_smfmac_f32_16x16x64_bf16 v[196:199], v[216:219], v[128:135], v248\n" \
  "ds_read_b128 v[216:219], v251 offset:5120\n" \
  "s_waitcnt lgkmcnt(5)\n" \
  "v_smfmac_f32_16x16x64_bf16 v[192:195], v[224:227], v[208:215], v248\n" \
  "ds_read_b128 v[208:211], v249 offset:49152\n" \
  "v_smfmac_f32_16x16x64_bf16 v[196:199], v[220:223], v[64:71], v248\n" \
  "ds_read_b128 v[212:215], v249 offset:50176\n" \
  "s_waitcnt lgkmcnt(6)\n" \
  "v_smfmac_f32_16x16x64_bf16 v[192:195], v[228:231], v[8:15], v248\n" \
  "ds_read_b128 v[220:223], v251 offset:6144\n" \
  "v_smfmac_f32_16x16x64_bf16 v[196:199], v[224:227], v[136:143], v248\n" \
  "ds_read_b128 v[224:227], v251 offset:7168\n" \
  "s_waitcnt lgkmcnt(5)\n" \
  "v_smfmac_f32_16x16x64_bf16 v[192:195], v[232:235], v[200:207], v248\n" \
  "ds_read_b128 v[200:203], v243 offset:0\n" \
  "v_smfmac_f32_16x16x64_bf16 v[196:199], v[228:231], v[72:79], v248\n" \
  "ds_read_b128 v[204:207], v243 offset:1024\n" \
  "s_waitcnt lgkmcnt(6)\n" \
  "v_smfmac_f32_16x16x64_bf16 v[192:195], v[216:219], v[16:23], v248\n" \
  "ds_read_b128 v[228:231], v251 offset:8192\n" \
  "v_smfmac_f32_16x16x64_bf16 v[196:199], v[232:235], v[144:151], v248\n" \
  "ds_read_b128 v[232:235], v251 offset:9216\n" \
  "s_waitcnt lgkmcnt(5)\n" \
  "v_smfmac_f32_16x16x64_bf16 v[192:195], v[220:223], v[208:215], v248\n" \
  "ds_read_b128 v[208:211], v243 offset:16384\n" \
  "s_barrier\n" \
  "v_smfmac_f32_16x16x64_bf16 v[196:199], v[216:219], v[80:87], v248\n" \
  "ds_read_b128 v[212:215], v243 offset:17408\n" \
  "s_waitcnt lgkmcnt(6)\n" \
  "v_smfmac_f32_16x16x64_bf16 v[192:195], v[224:227], v[24:31], v248\n" \
  "ds_read_b128 v[216:219], v251 offset:10240\n" \
  "v_smfmac_f32_16x16x64_bf16 v[196:199], v[220:223], v[152:159], v248\n" \
  "ds_read_b128 v[220:223], v251 offset:11264\n" \
  "s_waitcnt lgkmcnt(5)\n" \
  "v_smfmac_f32_16x16x64_bf16 v[192:195], v[228:231], v[200:207], v248\n" \
  "ds_read_b128 v[200:203], v243 offset:32768\n" \
  "v_smfmac_f32_16x16x64_bf16 v[196:199], v[224:227], v[88:95], v248\n" \
  "ds_read_b128 v[204:207], v243 offset:33792\n" \
  "s_waitcnt lgkmcnt(6)\n" \
  "v_smfmac_f32_16x16x64_bf16 v[192:195], v[232:235], v[32:39], v248\n" \
  "ds_read_b128 v[224:227], v251 offset:12288\n" \
  "v_smfmac_f32_16x16x64_bf16 v[196:199], v[228:231], v[160:167], v248\n" \
  "ds_read_b128 v[228:231], v251 offset:13312\n" \
  "s_waitcnt lgkmcnt(5)\n" \
  "v_smfmac_f32_16x16x64_bf16 v[192:195], v[216:219], v[208:215], v248\n" \
  "ds_read_b128 v[208:211], v243 offset:49152\n" \
  "ds_read_b128 v[212:215], v243 offset:50176\n" \
  "v_smfmac_f32_16x16x64_bf16 v[196:199], v[232:235], v[96:103], v248\n" \
  "ds_read_b128 v[236:239], v242 offset:2048\n" \
  "s_waitcnt lgkmcnt(7)\n" \
  "v_smfmac_f32_16x16x64_bf16 v[192:195], v[220:223], v[40:47], v248\n" \
  "ds_read_b128 v[232:235], v251 offset:14336\n" \
  "v_smfmac_f32_16x16x64_bf16 v[196:199], v[216:219], v[168:175], v248\n" \
  "ds_read_b128 v[216:219], v251 offset:15360\n" \
  "s_waitcnt lgkmcnt(6)\n" \
  "v_smfmac_f32_16x16x64_bf16 v[192:195], v[224:227], v[200:207], v248\n" \
  "ds_read_b128 v[200:203], v242 offset:0\n" \
  "v_smfmac_f32_16x16x64_bf16 v[196:199], v[220:223], v[104:111], v248\n" \
  "ds_read_b128 v[204:207], v242 offset:1024\n" \
  "s_waitcnt lgkmcnt(7)\n" \
  "v_smfmac_f32_16x16x64_bf16 v[192:195], v[228:231], v[48:55], v248\n" \
  "ds_read_b128 v[220:223], v242 offset:3072\n" \
  "v_smfmac_f32_16x16x64_bf16 v[196:199], v[224:227], v[176:183], v248\n" \
  "s_waitcnt lgkmcnt(4)\n" \
  "v_smfmac_f32_16x16x64_bf16 v[192:195], v[232:235], v[208:215], v248\n" \
  "v_smfmac_f32_16x16x64_bf16 v[196:199], v[228:231], v[112:119], v248\n" \
  "s_waitcnt lgkmcnt(3)\n" \
  "v_smfmac_f32_16x16x64_bf16 v[192:195], v[216:219], v[56:63], v248\n" \
  "v_smfmac_f32_16x16x64_bf16 v[196:199], v[232:235], v[184:191], v248\n" \
  "v_smfmac_f32_16x16x64_bf16 v[196:199], v[216:219], v[120:127], v248\n" \
  "s_nop 0\n" \
  "v_add_f32 v192, v192, v194\n" \
  "v_add_f32 v193, v193, v195\n" \
  "v_cvt_pk_bf16_f32 v192, v192, v193\n" \
  "s_nop 4\n" \
  "v_add_f32 v196, v196, v198\n" \
  "v_add_f32 v197, v197, v199\n" \
  "v_cvt_pk_bf16_f32 v196, v196, v197\n" \
  "ds_write2_b32 v247, v192, v196 offset1:96\n" \
  "v_xor_b32 v241, 0x400, v250\n" \
  "s_waitcnt lgkmcnt(0)\n" \
  "s_barrier\n" \
  "ds_read_b64_tr_b16 v[192:193], v246 offset:16384\n" \
  "ds_read_b64_tr_b16 v[194:195], v246 offset:16768\n" \
  "ds_read_b64_tr_b16 v[196:197], v246 offset:17152\n" \
  "ds_read_b64_tr_b16 v[198:199], v246 offset:17536\n" \
  "ds_read_b64_tr_b16 v[208:209], v246 offset:17920\n" \
  "ds_read_b64_tr_b16 v[210:211], v246 offset:18304\n" \
  "ds_read_b64_tr_b16 v[212:213], v246 offset:18688\n" \
  "ds_read_b64_tr_b16 v[214:215], v246 offset:19072\n" \
  "s_waitcnt lgkmcnt(6)\n" \
  "v_mfma_f32_16x16x32_bf16 v[200:203], v[200:203], v[192:195], 0\n" \
  "s_waitcnt lgkmcnt(4)\n" \
  "v_mfma_f32_16x16x32_bf16 v[200:203], v[204:207], v[196:199], v[200:203]\n" \
  "s_waitcnt lgkmcnt(2)\n" \
  "v_mfma_f32_16x16x32_bf16 v[200:203], v[236:239], v[208:211], v[200:203]\n" \
  "s_waitcnt lgkmcnt(0)\n" \
  "v_mfma_f32_16x16x32_bf16 v[200:203], v[220:223], v[212:215], v[200:203]\n" \
  "s_nop 8\n" \
  "v_cndmask_b32 v204, v200, v202, %[hf]\n" \
  "v_cndmask_b32 v205, v201, v203, %[hf]\n" \
  "v_max_f32 v221, |v204|, |v205|\n" \
  "ds_write_b32 v250, v221 offset:22528\n" \
  "v_mul_f32 v236, 0x3e0293ee, v204\n" \
  "v_mul_f32 v237, 0x3e0293ee, v205\n" \
  "v_exp_f32 v236, v236\n" \
  "v_exp_f32 v237, v237\n" \
  "ds_read_b32 v238, v241 offset:22528\n" \
  "v_cvt_pk_bf16_f32 v220, v236, v237\n" \
  "s_mov_b64 %[wd], 0\n" \
  "s_waitcnt lgkmcnt(0)\n" \
  "v_max_f32 v238, v238, v221\n" \
  "v_cmp_nge_f32 vcc, 0x43aa0000, v238\n" \
  "s_cbranch_vccnz .Lw1_%=\n" \
  ".Lw1b_%=:\n" \
  "buffer_store_dword v220, v245, %[rs], %[c0] offen sc1\n" \
  "buffer_store_dword v220, v245, %[rs], %[c1] offen sc1\n" \
  "buffer_store_dword v220, v245, %[rs], %[c2] offen sc1\n" \
  "buffer_store_dword v220, v245, %[rs], %[c3] offen sc1\n" \
  "v_add_f32 v223, v236, v237\n" \
  "ds_write_b32 v250, v223 offset:22532\n" \
  "v_cndmask_b32 v222, 0, v222, %[wd]\n" \
  "v_mov_b32 v239, 1.0\n" \
  "s_waitcnt lgkmcnt(0)\n" \
  "s_barrier\n" \
  "ds_read_b64_tr_b16 v[216:217], v246 offset:19456\n" \
  "ds_read_b64_tr_b16 v[218:219], v246 offset:19840\n" \
  "ds_read_b64_tr_b16 v[228:229], v246 offset:20224\n" \
  "ds_read_b64_tr_b16 v[230:231], v246 offset:20608\n" \
  "ds_read_b64_tr_b16 v[232:233], v246 offset:20992\n" \
  "ds_read_b64_tr_b16 v[234:235], v246 offset:21376\n" \
  "ds_read_b64_tr_b16 v[200:201], v246 offset:21760\n" \
  "ds_read_b64_tr_b16 v[202:203], v246 offset:22144\n" \
  "ds_read_b32 v238, v241 offset:22532\n" \
  "s_waitcnt lgkmcnt(7)\n" \
  "v_mfma_f32_16x16x32_bf16 v[216:219], v[216:219], v[192:195], 0\n" \
  "s_waitcnt lgkmcnt(5)\n" \
  "v_mfma_f32_16x16x32_bf16 v[216:219], v[228:231], v[196:199], v[216:219]\n" \
  "s_waitcnt lgkmcnt(3)\n" \
  "v_mfma_f32_16x16x32_bf16 v[216:219], v[232:235], v[208:211], v[216:219]\n" \
  "s_waitcnt lgkmcnt(1)\n" \
  "v_mfma_f32_16x16x32_bf16 v[216:219], v[200:203], v[212:215], v[216:219]\n" \
  "s_waitcnt lgkmcnt(0)\n" \
  "v_add_f32 v223, v223, v238\n" \
  "v_mov_b32 v238, v223\n" \
  "s_nop 1\n" \
  "v_permlane32_swap_b32 v223, v238\n" \
  "v_add_f32 v223, v223, v238\n" \
  "s_nop 1\n" \
  "v_cndmask_b32 v204, v216, v218, %[hf]\n" \
  "v_cndmask_b32 v205, v217, v219, %[hf]\n" \
  "v_mul_f32 v236, 0x3e0293ee, v204\n" \
  "v_mul_f32 v237, 0x3e0293ee, v205\n" \
  "v_exp_f32 v236, v236\n" \
  "v_exp_f32 v237, v237\n" \
  "v_max_f32 v221, |v204|, |v205|\n" \
  "v_add_f32 v238, v223, v236\n" \
  "v_cmp_nge_f32 vcc, 0x43aa0000, v221\n" \
  "v_add_f32 v238, v238, v237\n" \
  "s_or_b64 vcc, vcc, %[wd]\n" \
  "s_cbranch_scc1 .Lw2_%=\n" \
  ".Lw2b_%=:\n" \
  "v_add_f32_dpp v238, v238, v238 row_ror:8 row_mask:0xf bank_mask:0xf\n" \
  "v_mov_b32 v221, v238\n" \
  "v_cndmask_b32 v236, v239, v236, %[up]\n" \
  "v_cndmask_b32 v237, v239, v237, %[up]\n" \
  "v_permlane16_swap_b32 v238, v221\n" \
  "v_add_f32 v238, v238, v221\n" \
  "v_rcp_f32 v238, v238\n" \
  "s_nop 0\n" \
  "v_mul_f32 v236, v236, v238\n" \
  "v_mul_f32 v237, v237, v238\n" \
  "v_cvt_pk_bf16_f32 v220, v236, v237\n" \
  "v_and_b32 v220, v220, v240\n" \
  "buffer_store_dword v220, v244, %[rs], %[c0] offen sc1\n" \
  "buffer_store_dword v220, v244, %[rs], %[c1] offen sc1\n"
#define QK_X \
  "s_branch .Lgo_%=\n" \
  ".Lp0_%=:\n" \
  "s_mov_b32 %[cnt], 0x40000\n" \
  ".Lp0r_%=:\n" \
  "s_sleep 2\n" \
  "buffer_load_dwordx4 v[236:239], v251, %[rs], %[o0] offen sc1\n" \
  "s_waitcnt vmcnt(0)\n" \
  "ds_write_b128 v250, v[236:239] offset:0\n" \
  "v_max_u32 v241, v236, v237\n" \
  "v_max3_u32 v241, v241, v238, v239\n" \
  "v_cmp_eq_u32 vcc, -1, v241\n" \
  "s_cbranch_vccz .Lp0b_%=\n" \
  "s_sub_u32 %[cnt], %[cnt], 1\n" \
  "s_cbranch_scc0 .Lp0r_%=\n" \
  "s_branch .Lp0b_%=\n" \
  ".Lgo_%=:\n" \
  "ds_read_b128 v[200:203], v249 offset:0\n" \
  "ds_read_b128 v[204:207], v249 offset:1024\n" \
  "ds_read_b128 v[208:211], v249 offset:16384\n" \
  "ds_read_b128 v[212:215], v249 offset:17408\n" \
  "v_mov_b64 v[192:193], 0\n" \
  "v_mov_b64 v[194:195], 0\n" \
  "v_mov_b64 v[196:197], 0\n" \
  "v_mov_b64 v[198:199], 0\n" \
  "s_waitcnt lgkmcnt(0)\n" \
  "s_barrier\n" \
  "buffer_load_dwordx4 v[236:239], v251, %[rs], %[o0] offen sc1\n" \
  "s_waitcnt vmcnt(0)\n" \
  "ds_write_b128 v250, v[236:239] offset:0\n" \
  "v_max3_u32 v241, v236, v237, v238\n" \
  "v_max_u32 v241, v241, v239\n" \
  "v_cmp_eq_u32 vcc, -1, v241\n" \
  "s_cbranch_vccnz .Lp0_%=\n" \
  ".Lp0b_%=:\n" \
  "s_waitcnt lgkmcnt(0)\n" \
  "s_barrier\n" \
  "ds_read_b128 v[216:219], v251 offset:0\n" \
  "ds_read_b128 v[220:223], v251 offset:1024\n" \
  "ds_read_b128 v[224:227], v251 offset:2048\n" \
  "ds_read_b128 v[228:231], v251 offset:3072\n" \
  "s_waitcnt lgkmcnt(3)\n" \
  "v_smfmac_f32_16x16x64_bf16 v[192:195], v[216:219], v[200:207], v248\n" \
  "ds_read_b128 v[200:203], v249 offset:32768\n" \
  "ds_read_b128 v[204:207], v249 offset:33792\n" \
  "s_waitcnt lgkmcnt(4)\n" \
  "v_smfmac_f32_16x16x64_bf16 v[192:195], v[220:223], v[0:7], v248\n" \
  "ds_read_b128 v[232:235], v251 offset:4096\n" \
  "v_smfmac_f32_16x16x64_bf16 v[196:199], v[216:219], v[128:135], v248\n" \
  "ds_read_b128 v[216:219], v251 offset:5120\n" \
  "s_waitcnt lgkmcnt(5)\n" \
  "v_smfmac_f32_16x16x64_bf16 v[192:195], v[224:227], v[208:215], v248\n" \
  "ds_read_b128 v[208:211], v249 offset:49152\n" \
  "v_smfmac_f32_16x16x64_bf16 v[196:199], v[220:223], v[64:71], v248\n" \
  "ds_read_b128 v[212:215], v249 offset:50176\n" \
  "s_waitcnt lgkmcnt(6)\n" \
  "v_smfmac_f32_16x16x64_bf16 v[192:195], v[228:231], v[8:15], v248\n" \
  "ds_read_b128 v[220:223], v251 offset:6144\n" \
  "v_smfmac_f32_16x16x64_bf16 v[196:199], v[224:227], v[136:143], v248\n" \
  "ds_read_b128 v[224:227], v251 offset:7168\n" \
  "s_waitcnt lgkmcnt(5)\n" \
  "v_smfmac_f32_16x16x64_bf16 v[192:195], v[232:235], v[200:207], v248\n" \
  "ds_read_b128 v[200:203], v243 offset:0\n" \
  "v_smfmac_f32_16x16x64_bf16 v[196:199], v[228:231], v[72:79], v248\n" \
  "ds_read_b128 v[204:207], v243 offset:1024\n" \
  "s_waitcnt lgkmcnt(6)\n" \
  "v_smfmac_f32_16x16x64_bf16 v[192:195], v[216:219], v[16:23], v248\n" \
  "ds_read_b128 v[228:231], v251 offset:8192\n" \
  "v_smfmac_f32_16x16x64_bf16 v[196:199], v[232:235], v[144:151], v248\n" \
  "ds_read_b128 v[232:235], v251 offset:9216\n" \
  "s_waitcnt lgkmcnt(5)\n" \
  "v_smfmac_f32_16x16x64_bf16 v[192:195], v[220:223], v[208:215], v248\n" \
  "ds_read_b128 v[208:211], v243 offset:16384\n" \
  "s_barrier\n" \
  "v_smfmac_f32_16x16x64_bf16 v[196:199], v[216:219], v[80:87], v248\n" \
  "ds_read_b128 v[212:215], v243 offset:17408\n" \
  "s_waitcnt lgkmcnt(6)\n" \
  "v_smfmac_f32_16x16x64_bf16 v[192:195], v[224:227], v[24:31], v248\n" \
  "ds_read_b128 v[216:219], v251 offset:10240\n" \
  "v_smfmac_f32_16x16x64_bf16 v[196:199], v[220:223], v[152:159], v248\n" \
  "ds_read_b128 v[220:223], v251 offset:11264\n" \
  "s_waitcnt lgkmcnt(5)\n" \
  "v_smfmac_f32_16x16x64_bf16 v[192:195], v[228:231], v[200:207], v248\n" \
  "ds_read_b128 v[200:203], v243 offset:32768\n" \
  "v_smfmac_f32_16x16x64_bf16 v[196:199], v[224:227], v[88:95], v248\n" \
  "ds_read_b128 v[204:207], v243 offset:33792\n" \
  "s_waitcnt lgkmcnt(6)\n" \
  "v_smfmac_f32_16x16x64_bf16 v[192:195], v[232:235], v[32:39], v248\n" \
  "ds_read_b128 v[224:227], v251 offset:12288\n" \
  "v_smfmac_f32_16x16x64_bf16 v[196:199], v[228:231], v[160:167], v248\n" \
  "ds_read_b128 v[228:231], v251 offset:13312\n" \
  "s_waitcnt lgkmcnt(5)\n" \
  "v_smfmac_f32_16x16x64_bf16 v[192:195], v[216:219], v[208:215], v248\n" \
  "ds_read_b128 v[208:211], v243 offset:49152\n" \
  "v_smfmac_f32_16x16x64_bf16 v[196:199], v[232:235], v[96:103], v248\n" \
  "ds_read_b128 v[212:215], v243 offset:50176\n" \
  "s_waitcnt lgkmcnt(6)\n" \
  "v_smfmac_f32_16x16x64_bf16 v[192:195], v[220:223], v[40:47], v248\n" \
  "ds_read_b128 v[232:235], v251 offset:14336\n" \
  "v_smfmac_f32_16x16x64_bf16 v[196:199], v[216:219], v[168:175], v248\n" \
  "ds_read_b128 v[216:219], v251 offset:15360\n" \
  "s_waitcnt lgkmcnt(5)\n" \
  "v_smfmac_f32_16x16x64_bf16 v[192:195], v[224:227], v[200:207], v248\n" \
  "v_smfmac_f32_16x16x64_bf16 v[196:199], v[220:223], v[104:111], v248\n" \
  "s_waitcnt lgkmcnt(4)\n" \
  "v_smfmac_f32_16x16x64_bf16 v[192:195], v[228:231], v[48:55], v248\n" \
  "v_smfmac_f32_16x16x64_bf16 v[196:199], v[224:227], v[176:183], v248\n" \
  "s_waitcnt lgkmcnt(1)\n" \
  "v_smfmac_f32_16x16x64_bf16 v[192:195], v[232:235], v[208:215], v248\n" \
  "v_smfmac_f32_16x16x64_bf16 v[196:199], v[228:231], v[112:119], v248\n" \
  "s_waitcnt lgkmcnt(0)\n" \
  "v_smfmac_f32_16x16x64_bf16 v[192:195], v[216:219], v[56:63], v248\n" \
  "v_smfmac_f32_16x16x64_bf16 v[196:199], v[232:235], v[184:191], v248\n" \
  "v_smfmac_f32_16x16x64_bf16 v[196:199], v[216:219], v[120:127], v248\n" \
  "s_nop 0\n" \
  "v_add_f32 v192, v192, v194\n" \
  "v_add_f32 v193, v193, v195\n" \
  "v_cvt_pk_bf16_f32 v192, v192, v193\n" \
  "s_nop 4\n" \
  "v_add_f32 v196, v196, v198\n" \
  "v_add_f32 v197, v197, v199\n" \
  "v_cvt_pk_bf16_f32 v196, v196, v197\n" \
  "ds_write2_b32 v247, v192, v196 offset1:96\n" \
  "buffer_load_dwordx4 v[200:203], v251, %[rs], %[qo] offen offset:0 sc1\n" \
  "buffer_load_dwordx4 v[204:207], v251, %[rs], %[qo] offen offset:1024 sc1\n" \
  "buffer_load_dwordx4 v[208:211], v251, %[rs], %[qo] offen offset:2048 sc1\n" \
  "buffer_load_dwordx4 v[212:215], v251, %[rs], %[qo] offen offset:3072 sc1\n" \
  "s_waitcnt lgkmcnt(0)\n" \
  "s_barrier\n" \
  "s_nop 15\n" \
  "s_nop 15\n" \
  "s_nop 15\n" \
  "s_nop 15\n" \
  "s_nop 15\n" \
  "s_nop 9\n" \
  "ds_read_b128 v[192:195], v251 offset:24576\n" \
  "ds_read_b128 v[196:199], v251 offset:28672\n" \
  "ds_read_b128 v[216:219], v251 offset:25600\n" \
  "ds_read_b128 v[220:223], v251 offset:29696\n" \
  "ds_read_b128 v[224:227], v251 offset:26624\n" \
  "ds_read_b128 v[228:231], v251 offset:30720\n" \
  "ds_read_b128 v[232:235], v251 offset:27648\n" \
  "ds_read_b128 v[236:239], v251 offset:31744\n" \
  "s_waitcnt lgkmcnt(0)\n" \
  "s_barrier\n" \
  "s_waitcnt vmcnt(3)\n" \
  "v_mfma_f32_16x16x32_bf16 v[192:195], v[192:195], v[200:203], 0\n" \
  "v_mfma_f32_16x16x32_bf16 v[196:199], v[196:199], v[200:203], 0\n" \
  "s_waitcnt vmcnt(2)\n" \
  "v_mfma_f32_16x16x32_bf16 v[192:195], v[216:219], v[204:207], v[192:195]\n" \
  "v_mfma_f32_16x16x32_bf16 v[196:199], v[220:223], v[204:207], v[196:199]\n" \
  "s_waitcnt vmcnt(1)\n" \
  "v_mfma_f32_16x16x32_bf16 v[192:195], v[224:227], v[208:211], v[192:195]\n" \
  "v_mfma_f32_16x16x32_bf16 v[196:199], v[228:231], v[208:211], v[196:199]\n" \
  "s_waitcnt vmcnt(0)\n" \
  "v_mfma_f32_16x16x32_bf16 v[192:195], v[232:235], v[212:215], v[192:195]\n" \
  "v_mfma_f32_16x16x32_bf16 v[196:199], v[236:239], v[212:215], v[196:199]\n" \
  "s_nop 8\n"
#define QK_K \
  "s_branch .Lgo_%=\n" \
  ".Lp0_%=:\n" \
  "s_mov_b32 %[cnt], 0x40000\n" \
  ".Lp0r_%=:\n" \
  "s_sleep 2\n" \
  "buffer_load_dwordx4 v[228:231], v251, %[rs], %[o0] offen sc1\n" \
  "buffer_load_dwordx4 v[232:235], v251, %[rs], %[o1] offen sc1\n" \
  "buffer_load_dwordx4 v[236:239], v251, %[rs], %[o2] offen sc1\n" \
  "s_waitcnt vmcnt(0)\n" \
  "ds_write_b128 v250, v[228:231] offset:0\n" \
  "v_max_u32 v241, v228, v229\n" \
  "v_max3_u32 v241, v241, v230, v231\n" \
  "ds_write_b128 v250, v[232:235] offset:4096\n" \
  "v_max3_u32 v241, v241, v232, v233\n" \
  "v_max3_u32 v241, v241, v234, v235\n" \
  "ds_write_b128 v250, v[236:239] offset:8192\n" \
  "v_max3_u32 v241, v241, v236, v237\n" \
  "v_max3_u32 v241, v241, v238, v239\n" \
  "v_cmp_eq_u32 vcc, -1, v241\n" \
  "s_cbranch_vccz .Lp0b_%=\n" \
  "s_sub_u32 %[cnt], %[cnt], 1\n" \
  "s_cbranch_scc0 .Lp0r_%=\n" \
  "s_branch .Lp0b_%=\n" \
  ".Lgo_%=:\n" \
  "ds_read_b128 v[200:203], v249 offset:0\n" \
  "ds_read_b128 v[204:207], v249 offset:1024\n" \
  "ds_read_b128 v[208:211], v249 offset:16384\n" \
  "ds_read_b128 v[212:215], v249 offset:17408\n" \
  "v_mov_b64 v[192:193], 0\n" \
  "v_mov_b64 v[194:195], 0\n" \
  "v_mov_b64 v[196:197], 0\n" \
  "v_mov_b64 v[198:199], 0\n" \
  "s_waitcnt lgkmcnt(0)\n" \
  "s_cmp_eq_u32 %[tw], 0\n" \
  "s_cbranch_scc1 .Ltq_%=\n" \
  ".Ltl_%=:\n" \
  "s_memtime vcc\n" \
  "s_waitcnt lgkmcnt(0)\n" \
  "s_sub_i32 %[cnt], %[tg], vcc_lo\n" \
  "s_cmp_lt_i32 %[cnt], 260\n" \
  "s_cbranch_scc1 .Ltf_%=\n" \
  "s_sleep 1\n" \
  "s_branch .Ltl_%=\n" \
  ".Ltf_%=:\n" \
  "s_max_i32 %[cnt], %[cnt], 0\n" \
  ".Ltg_%=:\n" \
  "s_sub_u32 %[cnt], %[cnt], 28\n" \
  "s_cbranch_scc0 .Ltg_%=\n" \
  ".Ltq_%=:\n" \
  "s_barrier\n" \
  "buffer_load_dwordx4 v[228:231], v251, %[rs], %[o0] offen sc1\n" \
  "buffer_load_dwordx4 v[232:235], v251, %[rs], %[o1] offen sc1\n" \
  "buffer_load_dwordx4 v[236:239], v251, %[rs], %[o2] offen sc1\n" \
  "s_waitcnt vmcnt(2)\n" \
  "ds_write_b128 v250, v[228:231] offset:0\n" \
  "s_waitcnt vmcnt(1)\n" \
  "ds_write_b128 v250, v[232:235] offset:4096\n" \
  "v_max3_u32 v241, v228, v229, v230\n" \
  "v_max3_u32 v241, v241, v231, v232\n" \
  "v_max3_u32 v241, v241, v233, v234\n" \
  "s_waitcnt vmcnt(0)\n" \
  "v_max3_u32 v241, v241, v235, v236\n" \
  "v_max3_u32 v241, v241, v237, v238\n" \
  "v_max_u32 v241, v241, v239\n" \
  "v_cmp_eq_u32 vcc, -1, v241\n" \
  "s_cbranch_vccnz .Lp0_%=\n" \
  ".Lp0b_%=:\n" \
  "s_waitcnt lgkmcnt(0)\n" \
  "s_barrier\n" \
  "s_memtime vcc\n" \
  "ds_write_b128 v250, v[236:239] offset:8192\n" \
  "s_waitcnt lgkmcnt(0)\n" \
  "s_sub_u32 %[cnt], vcc_lo, %[ckm]\n" \
  "s_mul_hi_u32 %[cnt], %[cnt], %[inv]\n" \
  "s_add_u32 %[cnt], %[cnt], %[r16]\n" \
  "s_and_b32 %[cnt], %[cnt], -2\n" \
  "v_mov_b32 v241, %[cnt]\n" \
  "buffer_store_dword v241, v245, %[rs], %[sst] offen sc1\n" \
  "s_barrier\n" \
  "ds_read_b128 v[216:219], v251 offset:0\n" \
  "ds_read_b128 v[220:223], v251 offset:1024\n" \
  "ds_read_b128 v[224:227], v251 offset:2048\n" \
  "ds_read_b128 v[228:231], v251 offset:3072\n" \
  "s_waitcnt lgkmcnt(3)\n" \
  "v_smfmac_f32_16x16x64_bf16 v[192:195], v[216:219], v[200:207], v248\n" \
  "ds_read_b128 v[200:203], v249 offset:32768\n" \
  "ds_read_b128 v[204:207], v249 offset:33792\n" \
  "s_waitcnt lgkmcnt(4)\n" \
  "v_smfmac_f32_16x16x64_bf16 v[192:195], v[220:223], v[0:7], v248\n" \
  "ds_read_b128 v[232:235], v251 offset:4096\n" \
  "v_smfmac_f32_16x16x64_bf16 v[196:199], v[216:219], v[128:135], v248\n" \
  "ds_read_b128 v[216:219], v251 offset:5120\n" \
  "s_waitcnt lgkmcnt(5)\n" \
  "v_smfmac_f32_16x16x64_bf16 v[192:195], v[224:227], v[208:215], v248\n" \
  "ds_read_b128 v[208:211], v249 offset:49152\n" \
  "v_smfmac_f32_16x16x64_bf16 v[196:199], v[220:223], v[64:71], v248\n" \
  "ds_read_b128 v[212:215], v249 offset:50176\n" \
  "s_waitcnt lgkmcnt(6)\n" \
  "v_smfmac_f32_16x16x64_bf16 v[192:195], v[228:231], v[8:15], v248\n" \
  "ds_read_b128 v[220:223], v251 offset:6144\n" \
  "v_smfmac_f32_16x16x64_bf16 v[196:199], v[224:227], v[136:143], v248\n" \
  "ds_read_b128 v[224:227], v251 offset:7168\n" \
  "s_waitcnt lgkmcnt(5)\n" \
  "v_smfmac_f32_16x16x64_bf16 v[192:195], v[232:235], v[200:207], v248\n" \
  "ds_read_b128 v[200:203], v243 offset:0\n" \
  "v_smfmac_f32_16x16x64_bf16 v[196:199], v[228:231], v[72:79], v248\n" \
  "buffer_load_dwordx2 v[240:241], v240, %[rs], %[ss] offen sc1\n" \
  "ds_read_b128 v[204:207], v243 offset:1024\n" \
  "s_waitcnt lgkmcnt(6)\n" \
  "v_smfmac_f32_16x16x64_bf16 v[192:195], v[216:219], v[16:23], v248\n" \
  "ds_read_b128 v[228:231], v251 offset:8192\n" \
  "s_barrier\n" \
  "v_smfmac_f32_16x16x64_bf16 v[196:199], v[232:235], v[144:151], v248\n" \
  "ds_read_b128 v[232:235], v251 offset:9216\n" \
  "s_waitcnt lgkmcnt(5)\n" \
  "v_smfmac_f32_16x16x64_bf16 v[192:195], v[220:223], v[208:215], v248\n" \
  "s_cmp_eq_u32 %[tc], 0\n" \
  "s_cbranch_scc1 .Lnt_%=\n" \
  "buffer_load_dword v242, v242, %[rs], %[tc] offen sc1\n" \
  ".Lnt_%=:\n" \
  "ds_read_b128 v[208:211], v243 offset:16384\n" \
  "v_smfmac_f32_16x16x64_bf16 v[196:199], v[216:219], v[80:87], v248\n" \
  "ds_read_b128 v[212:215], v243 offset:17408\n" \
  "s_waitcnt lgkmcnt(6)\n" \
  "v_smfmac_f32_16x16x64_bf16 v[192:195], v[224:227], v[24:31], v248\n" \
  "ds_read_b128 v[216:219], v251 offset:10240\n" \
  "v_smfmac_f32_16x16x64_bf16 v[196:199], v[220:223], v[152:159], v248\n" \
  "ds_read_b128 v[220:223], v251 offset:11264\n" \
  "s_waitcnt lgkmcnt(5)\n" \
  "v_smfmac_f32_16x16x64_bf16 v[192:195], v[228:231], v[200:207], v248\n" \
  "ds_read_b128 v[200:203], v243 offset:32768\n" \
  "v_smfmac_f32_16x16x64_bf16 v[196:199], v[224:227], v[88:95], v248\n" \
  "ds_read_b128 v[204:207], v243 offset:33792\n" \
  "s_waitcnt lgkmcnt(6)\n" \
  "v_smfmac_f32_16x16x64_bf16 v[192:195], v[232:235], v[32:39], v248\n" \
  "ds_read_b128 v[224:227], v251 offset:12288\n" \
  "v_smfmac_f32_16x16x64_bf16 v[196:199], v[228:231], v[160:167], v248\n" \
  "ds_read_b128 v[228:231], v251 offset:13312\n" \
  "s_waitcnt lgkmcnt(5)\n" \
  "v_smfmac_f32_16x16x64_bf16 v[192:195], v[216:219], v[208:215], v248\n" \
  "ds_read_b128 v[208:211], v243 offset:49152\n" \
  "v_smfmac_f32_16x16x64_bf16 v[196:199], v[232:235], v[96:103], v248\n" \
  "ds_read_b128 v[212:215], v243 offset:50176\n" \
  "s_waitcnt lgkmcnt(6)\n" \
  "v_smfmac_f32_16x16x64_bf16 v[192:195], v[220:223], v[40:47], v248\n" \
  "ds_read_b128 v[232:235], v251 offset:14336\n" \
  "v_smfmac_f32_16x16x64_bf16 v[196:199], v[216:219], v[168:175], v248\n" \
  "ds_read_b128 v[216:219], v251 offset:15360\n" \
  "s_waitcnt lgkmcnt(5)\n" \
  "v_smfmac_f32_16x16x64_bf16 v[192:195], v[224:227], v[200:207], v248\n" \
  "v_smfmac_f32_16x16x64_bf16 v[196:199], v[220:223], v[104:111], v248\n" \
  "s_waitcnt lgkmcnt(4)\n" \
  "v_smfmac_f32_16x16x64_bf16 v[192:195], v[228:231], v[48:55], v248\n" \
  "v_smfmac_f32_16x16x64_bf16 v[196:199], v[224:227], v[176:183], v248\n" \
  "s_waitcnt lgkmcnt(1)\n" \
  "v_smfmac_f32_16x16x64_bf16 v[192:195], v[232:235], v[208:215], v248\n" \
  "v_smfmac_f32_16x16x64_bf16 v[196:199], v[228:231], v[112:119], v248\n" \
  "s_waitcnt lgkmcnt(0)\n" \
  "v_smfmac_f32_16x16x64_bf16 v[192:195], v[216:219], v[56:63], v248\n" \
  "v_smfmac_f32_16x16x64_bf16 v[196:199], v[232:235], v[184:191], v248\n" \
  "v_smfmac_f32_16x16x64_bf16 v[196:199], v[216:219], v[120:127], v248\n" \
  "s_setprio 3\n" \
  "v_add_f32 v192, v192, v194\n" \
  "v_add_f32 v193, v193, v195\n" \
  "v_cvt_pk_bf16_f32 v192, v192, v193\n" \
  "s_nop 4\n" \
  "v_add_f32 v196, v196, v198\n" \
  "v_add_f32 v197, v197, v199\n" \
  "v_cvt_pk_bf16_f32 v196, v196, v197\n" \
  "ds_write2_b32 v247, v192, v196 offset1:96\n" \
  "s_waitcnt lgkmcnt(0)\n" \
  "s_barrier\n" \
  "s_setprio 0\n" \
  "s_waitcnt vmcnt(0)\n"
#define OW_F \
  "s_branch .Lgo_%=\n" \
  ".Lbad_%=:\n" \
  "s_mov_b32 %[st], 1\n" \
  "s_branch .Lend_%=\n" \
  ".Lgo_%=:\n" \
  "v_mov_b64 v[84:85], 0\n" \
  "v_mov_b64 v[86:87], 0\n" \
  "v_mov_b64 v[88:89], 0\n" \
  "v_mov_b64 v[90:91], 0\n" \
  "v_mov_b64 v[92:93], 0\n" \
  "v_mov_b64 v[94:95], 0\n" \
  "v_mov_b64 v[96:97], 0\n" \
  "v_mov_b64 v[98:99], 0\n" \
  "v_mov_b64 v[100:101], 0\n" \
  "v_mov_b64 v[102:103], 0\n" \
  "s_mov_b32 %[st], 0\n" \
  "s_barrier\n" \
  "ds_read_b32 v104, v119\n" \
  "s_nop 15\n" \
  "s_nop 15\n" \
  "s_nop 15\n" \
  "s_nop 11\n" \
  "buffer_load_dwordx4 v[0:3], v112, %[rs], %[sv1] offen offset:0 sc1\n" \
  "buffer_load_dwordx4 v[4:7], v112, %[rs], %[sv1] offen offset:1024 sc1\n" \
  "buffer_load_dwordx4 v[8:11], v112, %[rs], %[sv1] offen offset:2048 sc1\n" \
  "buffer_load_dwordx4 v[12:15], v112, %[rs], %[sv1] offen offset:3072 sc1\n" \
  "s_waitcnt lgkmcnt(0)\n" \
  "v_readfirstlane_b32 %[st], v104\n" \
  ".Ltl_%=:\n" \
  "s_memtime vcc\n" \
  "s_waitcnt lgkmcnt(0)\n" \
  "s_sub_i32 %[cnt], %[st], vcc_lo\n" \
  "s_sub_i32 vcc_hi, %[cnt], 120\n" \
  "s_cmp_lt_u32 vcc_hi, 2000\n" \
  "s_cbranch_scc1 .Ltl_%=\n" \
  "s_mov_b32 %[st], 0\n" \
  "s_max_i32 %[cnt], %[cnt], 0\n" \
  "s_min_i32 %[cnt], %[cnt], 2000\n" \
  ".Ltg_%=:\n" \
  "s_sub_u32 %[cnt], %[cnt], 28\n" \
  "s_cbranch_scc0 .Ltg_%=\n" \
  "buffer_load_dwordx4 v[16:19], v112, %[rs], %[sv2] offen offset:0 sc1\n" \
  "buffer_load_dwordx4 v[20:23], v112, %[rs], %[sv2] offen offset:1024 sc1\n" \
  "s_nop 3\n" \
  "buffer_load_dwordx4 v[80:83], v113, %[rs], %[sp] offen sc1\n" \
  "s_nop 5\n" \
  "buffer_load_dwordx4 v[64:67], v114, %[rs], %[sp1] offen offset:0 sc1\n" \
  "buffer_load_dwordx4 v[68:71], v114, %[rs], %[sp1] offen offset:1024 sc1\n" \
  "buffer_load_dwordx4 v[72:75], v114, %[rs], %[sp2] offen offset:0 sc1\n" \
  "buffer_load_dwordx4 v[24:27], v112, %[rs], %[sv2] offen offset:2048 sc1\n" \
  "buffer_load_dwordx4 v[28:31], v112, %[rs], %[sv2] offen offset:3072 sc1\n" \
  "buffer_load_dwordx4 v[76:79], v114, %[rs], %[sp2] offen offset:1024 sc1\n" \
  "s_barrier\n" \
  "ds_read_b128 v[32:35], v115 offset:0\n" \
  "ds_read_b128 v[36:39], v115 offset:1024\n" \
  "ds_read_b128 v[40:43], v115 offset:2048\n" \
  "ds_read_b128 v[44:47], v115 offset:3072\n" \
  "ds_read_b128 v[48:51], v115 offset:4096\n" \
  "ds_read_b128 v[52:55], v115 offset:5120\n" \
  "ds_read_b128 v[56:59], v115 offset:6144\n" \
  "ds_read_b128 v[60:63], v115 offset:7168\n" \
  "s_waitcnt vmcnt(6)\n" \
  "s_mov_b64 exec, %[m0]\n" \
  "v_mov_b64 v[84:85], v[80:81]\n" \
  "v_mov_b64 v[86:87], v[82:83]\n" \
  "s_mov_b64 exec, %[m1]\n" \
  "v_mov_b64 v[88:89], v[80:81]\n" \
  "v_mov_b64 v[90:91], v[82:83]\n" \
  "s_mov_b64 exec, %[m2]\n" \
  "v_mov_b64 v[92:93], v[80:81]\n" \
  "v_mov_b64 v[94:95], v[82:83]\n" \
  "s_mov_b64 exec, %[m3]\n" \
  "v_mov_b64 v[96:97], v[80:81]\n" \
  "v_mov_b64 v[98:99], v[82:83]\n" \
  "s_mov_b64 exec, -1\n" \
  "s_waitcnt vmcnt(5)\n" \
  "v_smfmac_f32_16x16x64_bf16 v[100:103], v[64:67], v[0:7], v118\n" \
  "s_waitcnt vmcnt(4)\n" \
  "v_smfmac_f32_16x16x64_bf16 v[100:103], v[68:71], v[8:15], v118\n" \
  "s_waitcnt lgkmcnt(6)\n" \
  "v_smfmac_f32_16x16x64_bf16 v[100:103], v[84:87], v[32:39], v118\n" \
  "s_waitcnt lgkmcnt(4)\n" \
  "v_smfmac_f32_16x16x64_bf16 v[100:103], v[88:91], v[40:47], v118 abid:1\n" \
  "s_waitcnt vmcnt(3)\n" \
  "v_smfmac_f32_16x16x64_bf16 v[100:103], v[72:75], v[16:23], v118\n" \
  "s_waitcnt lgkmcnt(2)\n" \
  "v_smfmac_f32_16x16x64_bf16 v[100:103], v[92:95], v[48:55], v118\n" \
  "s_waitcnt lgkmcnt(0)\n" \
  "v_smfmac_f32_16x16x64_bf16 v[100:103], v[96:99], v[56:63], v118 abid:1\n" \
  "s_waitcnt vmcnt(0)\n" \
  "v_smfmac_f32_16x16x64_bf16 v[100:103], v[76:79], v[24:31], v118\n" \
  "s_nop 8\n" \
  "v_add_f32 v100, v100, v102\n" \
  "v_add_f32 v101, v101, v103\n" \
  "v_cvt_pk_bf16_f32 v104, v100, v101\n" \
  "v_cmp_u_f32 vcc, v100, v101\n" \
  "s_nop 0\n" \
  "v_mov_b32_dpp v105, v104 quad_perm:[1,0,3,2] row_mask:0xf bank_mask:0xf\n" \
  "v_perm_b32 v104, v105, v104, v117\n" \
  "s_cbranch_vccnz .Lbad_%=\n" \
  "buffer_store_dword v104, v116, %[rs], %[sm0] offen sc1\n" \
  "buffer_store_dword v104, v116, %[rs], %[sm1] offen sc1\n" \
  "buffer_store_dword v104, v116, %[rs], %[sm2] offen sc1\n" \
  "buffer_store_dword v104, v116, %[rs], %[sm3] offen sc1\n" \
  ".Lend_%=:\n"
#define OW_E \
  "s_branch .Lgo_%=\n" \
  ".Lbad_%=:\n" \
  "s_mov_b32 %[st], 1\n" \
  "s_branch .Lend_%=\n" \
  ".Lgo_%=:\n" \
  "v_mov_b64 v[208:209], 0\n" \
  "v_mov_b64 v[210:211], 0\n" \
  "v_mov_b64 v[212:213], 0\n" \
  "v_mov_b64 v[214:215], 0\n" \
  "v_mov_b64 v[216:217], 0\n" \
  "v_mov_b64 v[218:219], 0\n" \
  "v_mov_b64 v[220:221], 0\n" \
  "v_mov_b64 v[222:223], 0\n" \
  "v_mov_b64 v[224:225], 0\n" \
  "v_mov_b64 v[226:227], 0\n" \
  "v_mov_b64 v[228:229], 0\n" \
  "v_mov_b64 v[230:231], 0\n" \
  "v_mov_b64 v[232:233], 0\n" \
  "v_mov_b64 v[234:235], 0\n" \
  "v_mov_b64 v[236:237], 0\n" \
  "v_mov_b64 v[238:239], 0\n" \
  "s_mov_b32 %[st], 0\n" \
  "s_barrier\n" \
  "buffer_load_dwordx4 v[192:195], v241, %[rs], %[se] offen offset:0 sc1\n" \
  "s_nop 11\n" \
  "buffer_load_dwordx4 v[196:199], v241, %[rs], %[se] offen offset:1024 sc1\n" \
  "s_nop 11\n" \
  "buffer_load_dwordx4 v[200:203], v241, %[rs], %[se] offen offset:2048 sc1\n" \
  "buffer_load_dwordx4 v[204:207], v241, %[rs], %[se] offen offset:3072 sc1\n" \
  "s_waitcnt vmcnt(3)\n" \
  "v_smfmac_f32_16x16x64_bf16 v[208:211], v[192:195], %[b00], v243\n" \
  "v_smfmac_f32_16x16x64_bf16 v[224:227], v[192:195], %[b10], v243\n" \
  "v_max3_u32 v240, v192, v192, v193\n" \
  "v_max3_u32 v240, v240, v194, v195\n" \
  "s_waitcnt vmcnt(2)\n" \
  "v_smfmac_f32_16x16x64_bf16 v[212:215], v[196:199], %[b01], v243\n" \
  "v_smfmac_f32_16x16x64_bf16 v[228:231], v[196:199], %[b11], v243\n" \
  "v_max3_u32 v240, v240, v196, v197\n" \
  "v_max3_u32 v240, v240, v198, v199\n" \
  "v_cvt_pk_bf16_f32 v208, v208, v209\n" \
  "v_cvt_pk_bf16_f32 v209, v210, v211\n" \
  "v_cvt_pk_bf16_f32 v224, v224, v225\n" \
  "v_cvt_pk_bf16_f32 v225, v226, v227\n" \
  "v_cvt_pk_bf16_f32 v210, v212, v213\n" \
  "v_cvt_pk_bf16_f32 v211, v214, v215\n" \
  "ds_write_b128 v242, v[208:211] offset:0\n" \
  "v_cvt_pk_bf16_f32 v226, v228, v229\n" \
  "v_cvt_pk_bf16_f32 v227, v230, v231\n" \
  "ds_write_b128 v242, v[224:227] offset:8192\n" \
  "s_waitcnt vmcnt(1)\n" \
  "v_smfmac_f32_16x16x64_bf16 v[216:219], v[200:203], %[b02], v243\n" \
  "v_smfmac_f32_16x16x64_bf16 v[232:235], v[200:203], %[b12], v243\n" \
  "s_waitcnt vmcnt(0)\n" \
  "v_smfmac_f32_16x16x64_bf16 v[220:223], v[204:207], %[b03], v243\n" \
  "v_smfmac_f32_16x16x64_bf16 v[236:239], v[204:207], %[b13], v243\n" \
  "v_cvt_pk_bf16_f32 v216, v216, v217\n" \
  "v_cvt_pk_bf16_f32 v217, v218, v219\n" \
  "v_cvt_pk_bf16_f32 v232, v232, v233\n" \
  "v_cvt_pk_bf16_f32 v233, v234, v235\n" \
  "s_nop 0\n" \
  "v_cvt_pk_bf16_f32 v218, v220, v221\n" \
  "v_cvt_pk_bf16_f32 v219, v222, v223\n" \
  "ds_write_b128 v242, v[216:219] offset:1024\n" \
  "s_nop 0\n" \
  "v_cvt_pk_bf16_f32 v234, v236, v237\n" \
  "v_cvt_pk_bf16_f32 v235, v238, v239\n" \
  "ds_write_b128 v242, v[232:235] offset:9216\n" \
  "v_max3_u32 v240, v240, v200, v201\n" \
  "v_max3_u32 v240, v240, v202, v203\n" \
  "v_max3_u32 v240, v240, v204, v205\n" \
  "v_max3_u32 v240, v240, v206, v207\n" \
  "v_cmp_eq_u32 vcc, -1, v240\n" \
  "s_cbranch_vccnz .Lbad_%=\n" \
  "s_waitcnt lgkmcnt(0)\n" \
  "s_barrier\n" \
  ".Lend_%=:\n"
#define ST_Y \
  "s_mov_b32 %[keep], m0\n" \
  "ds_read_b128 v[240:243], v250 offset:0\n" \
  "ds_read_b128 v[244:247], v250 offset:1024\n" \
  "v_mfma_f32_16x16x32_bf16 v[192:195], v[0:3], v[208:211], 0\n" \
  "v_mfma_f32_16x16x32_bf16 v[196:199], v[64:67], v[208:211], 0\n" \
  "ds_read_b128 v[208:211], v252 offset:8192\n" \
  "v_mfma_f32_16x16x32_bf16 v[200:203], v[0:3], v[212:215], 0\n" \
  "v_mfma_f32_16x16x32_bf16 v[204:207], v[64:67], v[212:215], 0\n" \
  "ds_read_b128 v[212:215], v252 offset:9216\n" \
  "v_mfma_f32_16x16x32_bf16 v[192:195], v[128:131], v[216:219], v[192:195]\n" \
  "v_mfma_f32_16x16x32_bf16 v[196:199], v[132:135], v[216:219], v[196:199]\n" \
  "ds_read_b128 v[216:219], v252 offset:10240\n" \
  "v_mfma_f32_16x16x32_bf16 v[200:203], v[128:131], v[220:223], v[200:203]\n" \
  "v_mfma_f32_16x16x32_bf16 v[204:207], v[132:135], v[220:223], v[204:207]\n" \
  "ds_read_b128 v[220:223], v252 offset:11264\n" \
  "v_mfma_f32_16x16x32_bf16 v[192:195], v[4:7], v[224:227], v[192:195]\n" \
  "v_mfma_f32_16x16x32_bf16 v[196:199], v[68:71], v[224:227], v[196:199]\n" \
  "ds_read_b128 v[224:227], v252 offset:12288\n" \
  "v_mfma_f32_16x16x32_bf16 v[200:203], v[4:7], v[228:231], v[200:203]\n" \
  "v_mfma_f32_16x16x32_bf16 v[204:207], v[68:71], v[228:231], v[204:207]\n" \
  "ds_read_b128 v[228:231], v252 offset:13312\n" \
  "s_waitcnt lgkmcnt(7)\n" \
  "v_mfma_f32_16x16x32_bf16 v[192:195], v[240:243], v[232:235], v[192:195]\n" \
  "s_waitcnt lgkmcnt(6)\n" \
  "v_mfma_f32_16x16x32_bf16 v[196:199], v[244:247], v[232:235], v[196:199]\n" \
  "ds_read_b128 v[232:235], v252 offset:14336\n" \
  "v_mfma_f32_16x16x32_bf16 v[200:203], v[240:243], v[236:239], v[200:203]\n" \
  "v_mfma_f32_16x16x32_bf16 v[204:207], v[244:247], v[236:239], v[204:207]\n" \
  "ds_read_b128 v[236:239], v252 offset:15360\n" \
  "ds_read_b128 v[240:243], v250 offset:16384\n" \
  "ds_read_b128 v[244:247], v250 offset:17408\n" \
  "s_waitcnt lgkmcnt(9)\n" \
  "v_mfma_f32_16x16x32_bf16 v[192:195], v[8:11], v[208:211], v[192:195]\n" \
  "v_mfma_f32_16x16x32_bf16 v[196:199], v[72:75], v[208:211], v[196:199]\n" \
  "s_waitcnt lgkmcnt(8)\n" \
  "v_mfma_f32_16x16x32_bf16 v[200:203], v[8:11], v[212:215], v[200:203]\n" \
  "v_mfma_f32_16x16x32_bf16 v[204:207], v[72:75], v[212:215], v[204:207]\n" \
  "s_waitcnt vmcnt(2)\n" \
  "s_waitcnt lgkmcnt(0)\n" \
  "s_barrier\n" \
  "ds_read_b128 v[208:211], v252 offset:16384\n" \
  "ds_read_b128 v[212:215], v252 offset:17408\n" \
  "s_add_u32 m0, %[mw], 0\n" \
  "s_xor_b32 %[cnt], %[sc], 512\n" \
  "buffer_load_dwordx4 v248, %[rx], %[cnt] offen lds\n" \
  "s_add_u32 m0, %[mw], 8192\n" \
  "s_xor_b32 %[cnt], %[sc], 640\n" \
  "buffer_load_dwordx4 v248, %[rx], %[cnt] offen lds\n" \
  "v_mfma_f32_16x16x32_bf16 v[192:195], v[136:139], v[216:219], v[192:195]\n" \
  "v_mfma_f32_16x16x32_bf16 v[196:199], v[140:143], v[216:219], v[196:199]\n" \
  "ds_read_b128 v[216:219], v252 offset:18432\n" \
  "v_mfma_f32_16x16x32_bf16 v[200:203], v[136:139], v[220:223], v[200:203]\n" \
  "v_mfma_f32_16x16x32_bf16 v[204:207], v[140:143], v[220:223], v[204:207]\n" \
  "ds_read_b128 v[220:223], v252 offset:19456\n" \
  "v_mfma_f32_16x16x32_bf16 v[192:195], v[12:15], v[224:227], v[192:195]\n" \
  "v_mfma_f32_16x16x32_bf16 v[196:199], v[76:79], v[224:227], v[196:199]\n" \
  "ds_read_b128 v[224:227], v252 offset:20480\n" \
  "v_mfma_f32_16x16x32_bf16 v[200:203], v[12:15], v[228:231], v[200:203]\n" \
  "v_mfma_f32_16x16x32_bf16 v[204:207], v[76:79], v[228:231], v[204:207]\n" \
  "ds_read_b128 v[228:231], v252 offset:21504\n" \
  "v_mfma_f32_16x16x32_bf16 v[192:195], v[240:243], v[232:235], v[192:195]\n" \
  "v_mfma_f32_16x16x32_bf16 v[196:199], v[244:247], v[232:235], v[196:199]\n" \
  "ds_read_b128 v[232:235], v252 offset:22528\n" \
  "v_mfma_f32_16x16x32_bf16 v[200:203], v[240:243], v[236:239], v[200:203]\n" \
  "v_mfma_f32_16x16x32_bf16 v[204:207], v[244:247], v[236:239], v[204:207]\n" \
  "ds_read_b128 v[236:239], v252 offset:23552\n" \
  "ds_read_b128 v[240:243], v250 offset:32768\n" \
  "ds_read_b128 v[244:247], v250 offset:33792\n" \
  "s_waitcnt lgkmcnt(9)\n" \
  "v_mfma_f32_16x16x32_bf16 v[192:195], v[16:19], v[208:211], v[192:195]\n" \
  "v_mfma_f32_16x16x32_bf16 v[196:199], v[80:83], v[208:211], v[196:199]\n" \
  "ds_read_b128 v[208:211], v252 offset:24576\n" \
  "s_waitcnt lgkmcnt(9)\n" \
  "v_mfma_f32_16x16x32_bf16 v[200:203], v[16:19], v[212:215], v[200:203]\n" \
  "v_mfma_f32_16x16x32_bf16 v[204:207], v[80:83], v[212:215], v[204:207]\n" \
  "ds_read_b128 v[212:215], v252 offset:25600\n" \
  "s_waitcnt lgkmcnt(9)\n" \
  "v_mfma_f32_16x16x32_bf16 v[192:195], v[144:147], v[216:219], v[192:195]\n" \
  "v_mfma_f32_16x16x32_bf16 v[196:199], v[148:151], v[216:219], v[196:199]\n" \
  "ds_read_b128 v[216:219], v252 offset:26624\n" \
  "s_waitcnt lgkmcnt(9)\n" \
  "v_mfma_f32_16x16x32_bf16 v[200:203], v[144:147], v[220:223], v[200:203]\n" \
  "v_mfma_f32_16x16x32_bf16 v[204:207], v[148:151], v[220:223], v[204:207]\n" \
  "ds_read_b128 v[220:223], v252 offset:27648\n" \
  "s_waitcnt lgkmcnt(9)\n" \
  "v_mfma_f32_16x16x32_bf16 v[192:195], v[20:23], v[224:227], v[192:195]\n" \
  "v_mfma_f32_16x16x32_bf16 v[196:199], v[84:87], v[224:227], v[196:199]\n" \
  "ds_read_b128 v[224:227], v252 offset:28672\n" \
  "s_waitcnt lgkmcnt(9)\n" \
  "v_mfma_f32_16x16x32_bf16 v[200:203], v[20:23], v[228:231], v[200:203]\n" \
  "v_mfma_f32_16x16x32_bf16 v[204:207], v[84:87], v[228:231], v[204:207]\n" \
  "ds_read_b128 v[228:231], v252 offset:29696\n" \
  "s_waitcnt lgkmcnt(7)\n" \
  "v_mfma_f32_16x16x32_bf16 v[192:195], v[240:243], v[232:235], v[192:195]\n" \
  "s_waitcnt lgkmcnt(6)\n" \
  "v_mfma_f32_16x16x32_bf16 v[196:199], v[244:247], v[232:235], v[196:199]\n" \
  "ds_read_b128 v[232:235], v252 offset:30720\n" \
  "v_mfma_f32_16x16x32_bf16 v[200:203], v[240:243], v[236:239], v[200:203]\n" \
  "v_mfma_f32_16x16x32_bf16 v[204:207], v[244:247], v[236:239], v[204:207]\n" \
  "ds_read_b128 v[236:239], v252 offset:31744\n" \
  "ds_read_b128 v[240:243], v250 offset:49152\n" \
  "ds_read_b128 v[244:247], v250 offset:50176\n" \
  "s_waitcnt lgkmcnt(9)\n" \
  "v_mfma_f32_16x16x32_bf16 v[192:195], v[24:27], v[208:211], v[192:195]\n" \
  "v_mfma_f32_16x16x32_bf16 v[196:199], v[88:91], v[208:211], v[196:199]\n" \
  "s_waitcnt lgkmcnt(8)\n" \
  "v_mfma_f32_16x16x32_bf16 v[200:203], v[24:27], v[212:215], v[200:203]\n" \
  "v_mfma_f32_16x16x32_bf16 v[204:207], v[88:91], v[212:215], v[204:207]\n" \
  "s_waitcnt vmcnt(0)\n" \
  "s_waitcnt lgkmcnt(0)\n" \
  "s_barrier\n" \
  "ds_read_b128 v[208:211], v252 offset:0\n" \
  "ds_read_b128 v[212:215], v252 offset:1024\n" \
  "s_add_u32 m0, %[mw], 16384\n" \
  "s_xor_b32 %[cnt], %[sc], 768\n" \
  "buffer_load_dwordx4 v248, %[rx], %[cnt] offen lds\n" \
  "s_add_u32 m0, %[mw], 24576\n" \
  "s_xor_b32 %[cnt], %[sc], 896\n" \
  "buffer_load_dwordx4 v248, %[rx], %[cnt] offen lds\n" \
  "v_lshl_or_b32 v253, v251, 24, %[fm]\n" \
  "buffer_store_dword v253, v253, %[rs], %[fo] offen sc1\n" \
  "v_mfma_f32_16x16x32_bf16 v[192:195], v[152:155], v[216:219], v[192:195]\n" \
  "v_mfma_f32_16x16x32_bf16 v[196:199], v[156:159], v[216:219], v[196:199]\n" \
  "ds_read_b128 v[216:219], v252 offset:2048\n" \
  "v_mfma_f32_16x16x32_bf16 v[200:203], v[152:155], v[220:223], v[200:203]\n" \
  "v_mfma_f32_16x16x32_bf16 v[204:207], v[156:159], v[220:223], v[204:207]\n" \
  "ds_read_b128 v[220:223], v252 offset:3072\n" \
  "v_mfma_f32_16x16x32_bf16 v[192:195], v[28:31], v[224:227], v[192:195]\n" \
  "v_mfma_f32_16x16x32_bf16 v[196:199], v[92:95], v[224:227], v[196:199]\n" \
  "ds_read_b128 v[224:227], v252 offset:4096\n" \
  "v_mfma_f32_16x16x32_bf16 v[200:203], v[28:31], v[228:231], v[200:203]\n" \
  "v_mfma_f32_16x16x32_bf16 v[204:207], v[92:95], v[228:231], v[204:207]\n" \
  "ds_read_b128 v[228:231], v252 offset:5120\n" \
  "v_mfma_f32_16x16x32_bf16 v[192:195], v[240:243], v[232:235], v[192:195]\n" \
  "v_mfma_f32_16x16x32_bf16 v[196:199], v[244:247], v[232:235], v[196:199]\n" \
  "ds_read_b128 v[232:235], v252 offset:6144\n" \
  "v_mfma_f32_16x16x32_bf16 v[200:203], v[240:243], v[236:239], v[200:203]\n" \
  "v_mfma_f32_16x16x32_bf16 v[204:207], v[244:247], v[236:239], v[204:207]\n" \
  "ds_read_b128 v[236:239], v252 offset:7168\n" \
  "ds_read_b128 v[240:243], v249 offset:0\n" \
  "ds_read_b128 v[244:247], v249 offset:1024\n" \
  "s_waitcnt lgkmcnt(9)\n" \
  "v_mfma_f32_16x16x32_bf16 v[192:195], v[32:35], v[208:211], v[192:195]\n" \
  "v_mfma_f32_16x16x32_bf16 v[196:199], v[96:99], v[208:211], v[196:199]\n" \
  "ds_read_b128 v[208:211], v252 offset:8192\n" \
  "s_waitcnt lgkmcnt(9)\n" \
  "v_mfma_f32_16x16x32_bf16 v[200:203], v[32:35], v[212:215], v[200:203]\n" \
  "v_mfma_f32_16x16x32_bf16 v[204:207], v[96:99], v[212:215], v[204:207]\n" \
  "ds_read_b128 v[212:215], v252 offset:9216\n" \
  "s_waitcnt lgkmcnt(9)\n" \
  "v_mfma_f32_16x16x32_bf16 v[192:195], v[160:163], v[216:219], v[192:195]\n" \
  "v_mfma_f32_16x16x32_bf16 v[196:199], v[164:167], v[216:219], v[196:199]\n" \
  "ds_read_b128 v[216:219], v252 offset:10240\n" \
  "s_waitcnt lgkmcnt(9)\n" \
  "v_mfma_f32_16x16x32_bf16 v[200:203], v[160:163], v[220:223], v[200:203]\n" \
  "v_mfma_f32_16x16x32_bf16 v[204:207], v[164:167], v[220:223], v[204:207]\n" \
  "ds_read_b128 v[220:223], v252 offset:11264\n" \
  "s_waitcnt lgkmcnt(9)\n" \
  "v_mfma_f32_16x16x32_bf16 v[192:195], v[36:39], v[224:227], v[192:195]\n" \
  "v_mfma_f32_16x16x32_bf16 v[196:199], v[100:103], v[224:227], v[196:199]\n" \
  "ds_read_b128 v[224:227], v252 offset:12288\n" \
  "s_waitcnt lgkmcnt(9)\n" \
  "v_mfma_f32_16x16x32_bf16 v[200:203], v[36:39], v[228:231], v[200:203]\n" \
  "v_mfma_f32_16x16x32_bf16 v[204:207], v[100:103], v[228:231], v[204:207]\n" \
  "ds_read_b128 v[228:231], v252 offset:13312\n" \
  "s_waitcnt lgkmcnt(7)\n" \
  "v_mfma_f32_16x16x32_bf16 v[192:195], v[240:243], v[232:235], v[192:195]\n" \
  "s_waitcnt lgkmcnt(6)\n" \
  "v_mfma_f32_16x16x32_bf16 v[196:199], v[244:247], v[232:235], v[196:199]\n" \
  "ds_read_b128 v[232:235], v252 offset:14336\n" \
  "v_mfma_f32_16x16x32_bf16 v[200:203], v[240:243], v[236:239], v[200:203]\n" \
  "v_mfma_f32_16x16x32_bf16 v[204:207], v[244:247], v[236:239], v[204:207]\n" \
  "ds_read_b128 v[236:239], v252 offset:15360\n" \
  "ds_read_b128 v[240:243], v249 offset:16384\n" \
  "ds_read_b128 v[244:247], v249 offset:17408\n" \
  "s_waitcnt lgkmcnt(9)\n" \
  "v_mfma_f32_16x16x32_bf16 v[192:195], v[40:43], v[208:211], v[192:195]\n" \
  "v_mfma_f32_16x16x32_bf16 v[196:199], v[104:107], v[208:211], v[196:199]\n" \
  "s_waitcnt lgkmcnt(8)\n" \
  "v_mfma_f32_16x16x32_bf16 v[200:203], v[40:43], v[212:215], v[200:203]\n" \
  "v_mfma_f32_16x16x32_bf16 v[204:207], v[104:107], v[212:215], v[204:207]\n" \
  "s_waitcnt vmcnt(1)\n" \
  "s_waitcnt lgkmcnt(0)\n" \
  "s_barrier\n" \
  "ds_read_b128 v[208:211], v252 offset:16384\n" \
  "ds_read_b128 v[212:215], v252 offset:17408\n" \
  "s_add_u32 m0, %[mw], 0\n" \
  "s_xor_b32 %[cnt], %[sn], 0\n" \
  "buffer_load_dwordx4 v248, %[rx], %[cnt] offen lds\n" \
  "s_add_u32 m0, %[mw], 8192\n" \
  "s_xor_b32 %[cnt], %[sn], 128\n" \
  "buffer_load_dwordx4 v248, %[rx], %[cnt] offen lds\n" \
  "v_lshl_or_b32 v253, v251, 3, %[tm]\n" \
  "buffer_load_dword v253, v253, %[rx], %[tc] offen\n" \
  "v_mfma_f32_16x16x32_bf16 v[192:195], v[168:171], v[216:219], v[192:195]\n" \
  "v_mfma_f32_16x16x32_bf16 v[196:199], v[172:175], v[216:219], v[196:199]\n" \
  "ds_read_b128 v[216:219], v252 offset:18432\n" \
  "v_mfma_f32_16x16x32_bf16 v[200:203], v[168:171], v[220:223], v[200:203]\n" \
  "v_mfma_f32_16x16x32_bf16 v[204:207], v[172:175], v[220:223], v[204:207]\n" \
  "ds_read_b128 v[220:223], v252 offset:19456\n" \
  "v_mfma_f32_16x16x32_bf16 v[192:195], v[44:47], v[224:227], v[192:195]\n" \
  "v_mfma_f32_16x16x32_bf16 v[196:199], v[108:111], v[224:227], v[196:199]\n" \
  "ds_read_b128 v[224:227], v252 offset:20480\n" \
  "v_mfma_f32_16x16x32_bf16 v[200:203], v[44:47], v[228:231], v[200:203]\n" \
  "v_mfma_f32_16x16x32_bf16 v[204:207], v[108:111], v[228:231], v[204:207]\n" \
  "ds_read_b128 v[228:231], v252 offset:21504\n" \
  "v_mfma_f32_16x16x32_bf16 v[192:195], v[240:243], v[232:235], v[192:195]\n" \
  "v_mfma_f32_16x16x32_bf16 v[196:199], v[244:247], v[232:235], v[196:199]\n" \
  "ds_read_b128 v[232:235], v252 offset:22528\n" \
  "v_mfma_f32_16x16x32_bf16 v[200:203], v[240:243], v[236:239], v[200:203]\n" \
  "v_mfma_f32_16x16x32_bf16 v[204:207], v[244:247], v[236:239], v[204:207]\n" \
  "ds_read_b128 v[236:239], v252 offset:23552\n" \
  "ds_read_b128 v[240:243], v249 offset:32768\n" \
  "ds_read_b128 v[244:247], v249 offset:33792\n" \
  "s_waitcnt lgkmcnt(9)\n" \
  "v_mfma_f32_16x16x32_bf16 v[192:195], v[48:51], v[208:211], v[192:195]\n" \
  "v_mfma_f32_16x16x32_bf16 v[196:199], v[112:115], v[208:211], v[196:199]\n" \
  "ds_read_b128 v[208:211], v252 offset:24576\n" \
  "s_waitcnt lgkmcnt(9)\n" \
  "v_mfma_f32_16x16x32_bf16 v[200:203], v[48:51], v[212:215], v[200:203]\n" \
  "v_mfma_f32_16x16x32_bf16 v[204:207], v[112:115], v[212:215], v[204:207]\n" \
  "ds_read_b128 v[212:215], v252 offset:25600\n" \
  "s_waitcnt lgkmcnt(9)\n" \
  "v_mfma_f32_16x16x32_bf16 v[192:195], v[176:179], v[216:219], v[192:195]\n" \
  "v_mfma_f32_16x16x32_bf16 v[196:199], v[180:183], v[216:219], v[196:199]\n" \
  "ds_read_b128 v[216:219], v252 offset:26624\n" \
  "s_waitcnt lgkmcnt(9)\n" \
  "v_mfma_f32_16x16x32_bf16 v[200:203], v[176:179], v[220:223], v[200:203]\n" \
  "v_mfma_f32_16x16x32_bf16 v[204:207], v[180:183], v[220:223], v[204:207]\n" \
  "ds_read_b128 v[220:223], v252 offset:27648\n" \
  "s_waitcnt lgkmcnt(9)\n" \
  "v_mfma_f32_16x16x32_bf16 v[192:195], v[52:55], v[224:227], v[192:195]\n" \
  "v_mfma_f32_16x16x32_bf16 v[196:199], v[116:119], v[224:227], v[196:199]\n" \
  "ds_read_b128 v[224:227], v252 offset:28672\n" \
  "s_waitcnt lgkmcnt(9)\n" \
  "v_mfma_f32_16x16x32_bf16 v[200:203], v[52:55], v[228:231], v[200:203]\n" \
  "v_mfma_f32_16x16x32_bf16 v[204:207], v[116:119], v[228:231], v[204:207]\n" \
  "ds_read_b128 v[228:231], v252 offset:29696\n" \
  "s_waitcnt lgkmcnt(7)\n" \
  "v_mfma_f32_16x16x32_bf16 v[192:195], v[240:243], v[232:235], v[192:195]\n" \
  "s_waitcnt lgkmcnt(6)\n" \
  "v_mfma_f32_16x16x32_bf16 v[196:199], v[244:247], v[232:235], v[196:199]\n" \
  "ds_read_b128 v[232:235], v252 offset:30720\n" \
  "v_mfma_f32_16x16x32_bf16 v[200:203], v[240:243], v[236:239], v[200:203]\n" \
  "v_mfma_f32_16x16x32_bf16 v[204:207], v[244:247], v[236:239], v[204:207]\n" \
  "ds_read_b128 v[236:239], v252 offset:31744\n" \
  "ds_read_b128 v[240:243], v249 offset:49152\n" \
  "ds_read_b128 v[244:247], v249 offset:50176\n" \
  "s_waitcnt lgkmcnt(9)\n" \
  "v_mfma_f32_16x16x32_bf16 v[192:195], v[56:59], v[208:211], v[192:195]\n" \
  "v_mfma_f32_16x16x32_bf16 v[196:199], v[120:123], v[208:211], v[196:199]\n" \
  "s_waitcnt lgkmcnt(8)\n" \
  "v_mfma_f32_16x16x32_bf16 v[200:203], v[56:59], v[212:215], v[200:203]\n" \
  "v_mfma_f32_16x16x32_bf16 v[204:207], v[120:123], v[212:215], v[204:207]\n" \
  "s_waitcnt vmcnt(1)\n" \
  "s_waitcnt lgkmcnt(0)\n" \
  "s_barrier\n" \
  "ds_read_b128 v[208:211], v252 offset:0\n" \
  "ds_read_b128 v[212:215], v252 offset:1024\n" \
  "s_add_u32 m0, %[mw], 16384\n" \
  "s_xor_b32 %[cnt], %[sn], 256\n" \
  "buffer_load_dwordx4 v248, %[rx], %[cnt] offen lds\n" \
  "s_add_u32 m0, %[mw], 24576\n" \
  "s_xor_b32 %[cnt], %[sn], 384\n" \
  "buffer_load_dwordx4 v248, %[rx], %[cnt] offen lds\n" \
  "v_mfma_f32_16x16x32_bf16 v[192:195], v[184:187], v[216:219], v[192:195]\n" \
  "v_mfma_f32_16x16x32_bf16 v[196:199], v[188:191], v[216:219], v[196:199]\n" \
  "ds_read_b128 v[216:219], v252 offset:2048\n" \
  "v_mfma_f32_16x16x32_bf16 v[200:203], v[184:187], v[220:223], v[200:203]\n" \
  "v_mfma_f32_16x16x32_bf16 v[204:207], v[188:191], v[220:223], v[204:207]\n" \
  "ds_read_b128 v[220:223], v252 offset:3072\n" \
  "v_mfma_f32_16x16x32_bf16 v[192:195], v[60:63], v[224:227], v[192:195]\n" \
  "v_mfma_f32_16x16x32_bf16 v[196:199], v[124:127], v[224:227], v[196:199]\n" \
  "ds_read_b128 v[224:227], v252 offset:4096\n" \
  "v_mfma_f32_16x16x32_bf16 v[200:203], v[60:63], v[228:231], v[200:203]\n" \
  "v_mfma_f32_16x16x32_bf16 v[204:207], v[124:127], v[228:231], v[204:207]\n" \
  "ds_read_b128 v[228:231], v252 offset:5120\n" \
  "v_mfma_f32_16x16x32_bf16 v[192:195], v[240:243], v[232:235], v[192:195]\n" \
  "v_mfma_f32_16x16x32_bf16 v[196:199], v[244:247], v[232:235], v[196:199]\n" \
  "ds_read_b128 v[232:235], v252 offset:6144\n" \
  "v_mfma_f32_16x16x32_bf16 v[200:203], v[240:243], v[236:239], v[200:203]\n" \
  "v_mfma_f32_16x16x32_bf16 v[204:207], v[244:247], v[236:239], v[204:207]\n" \
  "ds_read_b128 v[236:239], v252 offset:7168\n" \
  "v_cvt_pk_bf16_f32 v192, v192, v193\n" \
  "v_cvt_pk_bf16_f32 v193, v194, v195\n" \
  "v_cvt_pk_bf16_f32 v194, v196, v197\n" \
  "v_cvt_pk_bf16_f32 v195, v198, v199\n" \
  "buffer_store_dwordx4 v[192:195], v251, %[rs], %[so] offen sc1\n" \
  "v_cvt_pk_bf16_f32 v200, v200, v201\n" \
  "v_cvt_pk_bf16_f32 v201, v202, v203\n" \
  "s_nop 0\n" \
  "v_cvt_pk_bf16_f32 v202, v204, v205\n" \
  "v_cvt_pk_bf16_f32 v203, v206, v207\n" \
  "s_add_u32 %[cnt], %[so], %c[rt]\n" \
  "buffer_store_dwordx4 v[200:203], v251, %[rs], %[cnt] offen sc1\n" \
  "s_mov_b32 m0, %[keep]\n" \
  "s_waitcnt lgkmcnt(0)\n"
#define ST_XA \
  "s_mov_b32 %[keep], m0\n" \
  "ds_read_b128 v[240:243], v250 offset:0\n" \
  "ds_read_b128 v[244:247], v250 offset:1024\n" \
  "v_mfma_f32_16x16x32_bf16 v[192:195], v[0:3], v[208:211], 0\n" \
  "v_mfma_f32_16x16x32_bf16 v[196:199], v[64:67], v[208:211], 0\n" \
  "ds_read_b128 v[208:211], v252 offset:8192\n" \
  "v_mfma_f32_16x16x32_bf16 v[200:203], v[0:3], v[212:215], 0\n" \
  "v_mfma_f32_16x16x32_bf16 v[204:207], v[64:67], v[212:215], 0\n" \
  "ds_read_b128 v[212:215], v252 offset:9216\n" \
  "v_mfma_f32_16x16x32_bf16 v[192:195], v[128:131], v[216:219], v[192:195]\n" \
  "v_mfma_f32_16x16x32_bf16 v[196:199], v[132:135], v[216:219], v[196:199]\n" \
  "ds_read_b128 v[216:219], v252 offset:10240\n" \
  "v_mfma_f32_16x16x32_bf16 v[200:203], v[128:131], v[220:223], v[200:203]\n" \
  "v_mfma_f32_16x16x32_bf16 v[204:207], v[132:135], v[220:223], v[204:207]\n" \
  "ds_read_b128 v[220:223], v252 offset:11264\n" \
  "v_mfma_f32_16x16x32_bf16 v[192:195], v[4:7], v[224:227], v[192:195]\n" \
  "v_mfma_f32_16x16x32_bf16 v[196:199], v[68:71], v[224:227], v[196:199]\n" \
  "v_mfma_f32_16x16x32_bf16 v[200:203], v[4:7], v[228:231], v[200:203]\n" \
  "v_mfma_f32_16x16x32_bf16 v[204:207], v[68:71], v[228:231], v[204:207]\n" \
  "s_waitcnt vmcnt(0)\n" \
  "s_waitcnt lgkmcnt(0)\n" \
  "s_barrier\n" \
  "ds_read_b128 v[224:227], v252 offset:12288\n" \
  "ds_read_b128 v[228:231], v252 offset:13312\n" \
  "s_add_u32 m0, %[mw], 0\n" \
  "s_xor_b32 %[cnt], %[sc], 384\n" \
  "buffer_load_dwordx4 v248, %[rx], %[cnt] offen lds\n" \
  "s_add_u32 m0, %[mw], 4096\n" \
  "s_xor_b32 %[cnt], %[sc], 448\n" \
  "buffer_load_dwordx4 v248, %[rx], %[cnt] offen lds\n" \
  "v_mfma_f32_16x16x32_bf16 v[192:195], v[240:243], v[232:235], v[192:195]\n" \
  "v_mfma_f32_16x16x32_bf16 v[196:199], v[244:247], v[232:235], v[196:199]\n" \
  "ds_read_b128 v[232:235], v252 offset:14336\n" \
  "v_mfma_f32_16x16x32_bf16 v[200:203], v[240:243], v[236:239], v[200:203]\n" \
  "v_mfma_f32_16x16x32_bf16 v[204:207], v[244:247], v[236:239], v[204:207]\n" \
  "ds_read_b128 v[236:239], v252 offset:15360\n" \
  "ds_read_b128 v[240:243], v250 offset:16384\n" \
  "ds_read_b128 v[244:247], v250 offset:17408\n" \
  "v_mfma_f32_16x16x32_bf16 v[192:195], v[8:11], v[208:211], v[192:195]\n" \
  "v_mfma_f32_16x16x32_bf16 v[196:199], v[72:75], v[208:211], v[196:199]\n" \
  "ds_read_b128 v[208:211], v252 offset:16384\n" \
  "v_mfma_f32_16x16x32_bf16 v[200:203], v[8:11], v[212:215], v[200:203]\n" \
  "v_mfma_f32_16x16x32_bf16 v[204:207], v[72:75], v[212:215], v[204:207]\n" \
  "ds_read_b128 v[212:215], v252 offset:17408\n" \
  "v_mfma_f32_16x16x32_bf16 v[192:195], v[136:139], v[216:219], v[192:195]\n" \
  "v_mfma_f32_16x16x32_bf16 v[196:199], v[140:143], v[216:219], v[196:199]\n" \
  "ds_read_b128 v[216:219], v252 offset:18432\n" \
  "v_mfma_f32_16x16x32_bf16 v[200:203], v[136:139], v[220:223], v[200:203]\n" \
  "v_mfma_f32_16x16x32_bf16 v[204:207], v[140:143], v[220:223], v[204:207]\n" \
  "ds_read_b128 v[220:223], v252 offset:19456\n" \
  "s_waitcnt lgkmcnt(9)\n" \
  "v_mfma_f32_16x16x32_bf16 v[192:195], v[12:15], v[224:227], v[192:195]\n" \
  "v_mfma_f32_16x16x32_bf16 v[196:199], v[76:79], v[224:227], v[196:199]\n" \
  "ds_read_b128 v[224:227], v252 offset:20480\n" \
  "s_waitcnt lgkmcnt(9)\n" \
  "v_mfma_f32_16x16x32_bf16 v[200:203], v[12:15], v[228:231], v[200:203]\n" \
  "v_mfma_f32_16x16x32_bf16 v[204:207], v[76:79], v[228:231], v[204:207]\n" \
  "ds_read_b128 v[228:231], v252 offset:21504\n" \
  "s_waitcnt lgkmcnt(7)\n" \
  "v_mfma_f32_16x16x32_bf16 v[192:195], v[240:243], v[232:235], v[192:195]\n" \
  "s_waitcnt lgkmcnt(6)\n" \
  "v_mfma_f32_16x16x32_bf16 v[196:199], v[244:247], v[232:235], v[196:199]\n" \
  "ds_read_b128 v[232:235], v252 offset:22528\n" \
  "v_mfma_f32_16x16x32_bf16 v[200:203], v[240:243], v[236:239], v[200:203]\n" \
  "v_mfma_f32_16x16x32_bf16 v[204:207], v[244:247], v[236:239], v[204:207]\n" \
  "ds_read_b128 v[236:239], v252 offset:23552\n" \
  "ds_read_b128 v[240:243], v250 offset:32768\n" \
  "ds_read_b128 v[244:247], v250 offset:33792\n" \
  "s_waitcnt lgkmcnt(9)\n" \
  "v_mfma_f32_16x16x32_bf16 v[192:195], v[16:19], v[208:211], v[192:195]\n" \
  "v_mfma_f32_16x16x32_bf16 v[196:199], v[80:83], v[208:211], v[196:199]\n" \
  "s_waitcnt lgkmcnt(8)\n" \
  "v_mfma_f32_16x16x32_bf16 v[200:203], v[16:19], v[212:215], v[200:203]\n" \
  "v_mfma_f32_16x16x32_bf16 v[204:207], v[80:83], v[212:215], v[204:207]\n" \
  "s_waitcnt vmcnt(0)\n" \
  "s_waitcnt lgkmcnt(0)\n" \
  "s_barrier\n" \
  "ds_read_b128 v[208:211], v252 offset:0\n" \
  "ds_read_b128 v[212:215], v252 offset:1024\n" \
  "s_add_u32 m0, %[mw], 12288\n" \
  "s_xor_b32 %[cnt], %[sc], 512\n" \
  "buffer_load_dwordx4 v248, %[rx], %[cnt] offen lds\n" \
  "s_add_u32 m0, %[mw], 16384\n" \
  "s_xor_b32 %[cnt], %[sc], 576\n" \
  "buffer_load_dwordx4 v248, %[rx], %[cnt] offen lds\n" \
  "s_add_u32 m0, %[mw], 20480\n" \
  "s_xor_b32 %[cnt], %[sc], 640\n" \
  "buffer_load_dwordx4 v248, %[rx], %[cnt] offen lds\n" \
  "v_mfma_f32_16x16x32_bf16 v[192:195], v[144:147], v[216:219], v[192:195]\n" \
  "v_mfma_f32_16x16x32_bf16 v[196:199], v[148:151], v[216:219], v[196:199]\n" \
  "ds_read_b128 v[216:219], v252 offset:2048\n" \
  "v_mfma_f32_16x16x32_bf16 v[200:203], v[144:147], v[220:223], v[200:203]\n" \
  "v_mfma_f32_16x16x32_bf16 v[204:207], v[148:151], v[220:223], v[204:207]\n" \
  "ds_read_b128 v[220:223], v252 offset:3072\n" \
  "v_mfma_f32_16x16x32_bf16 v[192:195], v[20:23], v[224:227], v[192:195]\n" \
  "v_mfma_f32_16x16x32_bf16 v[196:199], v[84:87], v[224:227], v[196:199]\n" \
  "ds_read_b128 v[224:227], v252 offset:4096\n" \
  "v_mfma_f32_16x16x32_bf16 v[200:203], v[20:23], v[228:231], v[200:203]\n" \
  "v_mfma_f32_16x16x32_bf16 v[204:207], v[84:87], v[228:231], v[204:207]\n" \
  "ds_read_b128 v[228:231], v252 offset:5120\n" \
  "v_mfma_f32_16x16x32_bf16 v[192:195], v[240:243], v[232:235], v[192:195]\n" \
  "v_mfma_f32_16x16x32_bf16 v[196:199], v[244:247], v[232:235], v[196:199]\n" \
  "ds_read_b128 v[232:235], v252 offset:6144\n" \
  "v_mfma_f32_16x16x32_bf16 v[200:203], v[240:243], v[236:239], v[200:203]\n" \
  "v_mfma_f32_16x16x32_bf16 v[204:207], v[244:247], v[236:239], v[204:207]\n" \
  "ds_read_b128 v[236:239], v252 offset:7168\n" \
  "ds_read_b128 v[240:243], v250 offset:49152\n" \
  "ds_read_b128 v[244:247], v250 offset:50176\n" \
  "s_waitcnt lgkmcnt(9)\n" \
  "v_mfma_f32_16x16x32_bf16 v[192:195], v[24:27], v[208:211], v[192:195]\n" \
  "v_mfma_f32_16x16x32_bf16 v[196:199], v[88:91], v[208:211], v[196:199]\n" \
  "s_waitcnt lgkmcnt(8)\n" \
  "v_mfma_f32_16x16x32_bf16 v[200:203], v[24:27], v[212:215], v[200:203]\n" \
  "v_mfma_f32_16x16x32_bf16 v[204:207], v[88:91], v[212:215], v[204:207]\n" \
  "s_waitcnt vmcnt(0)\n" \
  "s_waitcnt lgkmcnt(0)\n" \
  "s_barrier\n" \
  "ds_read_b128 v[208:211], v252 offset:12288\n" \
  "ds_read_b128 v[212:215], v252 offset:13312\n" \
  "s_add_u32 m0, %[mw], 0\n" \
  "s_xor_b32 %[cnt], %[sc], 704\n" \
  "buffer_load_dwordx4 v248, %[rx], %[cnt] offen lds\n" \
  "s_add_u32 m0, %[mw], 4096\n" \
  "s_xor_b32 %[cnt], %[sc], 768\n" \
  "buffer_load_dwordx4 v248, %[rx], %[cnt] offen lds\n" \
  "s_add_u32 m0, %[mw], 8192\n" \
  "s_xor_b32 %[cnt], %[sc], 832\n" \
  "buffer_load_dwordx4 v248, %[rx], %[cnt] offen lds\n" \
  "v_lshl_or_b32 v253, v251, 3, %[tm]\n" \
  "buffer_load_dword v253, v253, %[rx], %[tc] offen\n" \
  "v_mfma_f32_16x16x32_bf16 v[192:195], v[152:155], v[216:219], v[192:195]\n" \
  "v_mfma_f32_16x16x32_bf16 v[196:199], v[156:159], v[216:219], v[196:199]\n" \
  "ds_read_b128 v[216:219], v252 offset:14336\n" \
  "v_mfma_f32_16x16x32_bf16 v[200:203], v[152:155], v[220:223], v[200:203]\n" \
  "v_mfma_f32_16x16x32_bf16 v[204:207], v[156:159], v[220:223], v[204:207]\n" \
  "ds_read_b128 v[220:223], v252 offset:15360\n" \
  "v_mfma_f32_16x16x32_bf16 v[192:195], v[28:31], v[224:227], v[192:195]\n" \
  "v_mfma_f32_16x16x32_bf16 v[196:199], v[92:95], v[224:227], v[196:199]\n" \
  "ds_read_b128 v[224:227], v252 offset:16384\n" \
  "v_mfma_f32_16x16x32_bf16 v[200:203], v[28:31], v[228:231], v[200:203]\n" \
  "v_mfma_f32_16x16x32_bf16 v[204:207], v[92:95], v[228:231], v[204:207]\n" \
  "ds_read_b128 v[228:231], v252 offset:17408\n" \
  "v_mfma_f32_16x16x32_bf16 v[192:195], v[240:243], v[232:235], v[192:195]\n" \
  "v_mfma_f32_16x16x32_bf16 v[196:199], v[244:247], v[232:235], v[196:199]\n" \
  "ds_read_b128 v[232:235], v252 offset:18432\n" \
  "v_mfma_f32_16x16x32_bf16 v[200:203], v[240:243], v[236:239], v[200:203]\n" \
  "v_mfma_f32_16x16x32_bf16 v[204:207], v[244:247], v[236:239], v[204:207]\n" \
  "ds_read_b128 v[236:239], v252 offset:19456\n" \
  "ds_read_b128 v[240:243], v249 offset:0\n" \
  "ds_read_b128 v[244:247], v249 offset:1024\n" \
  "s_waitcnt lgkmcnt(9)\n" \
  "v_mfma_f32_16x16x32_bf16 v[192:195], v[32:35], v[208:211], v[192:195]\n" \
  "v_mfma_f32_16x16x32_bf16 v[196:199], v[96:99], v[208:211], v[196:199]\n" \
  "ds_read_b128 v[208:211], v252 offset:20480\n" \
  "s_waitcnt lgkmcnt(9)\n" \
  "v_mfma_f32_16x16x32_bf16 v[200:203], v[32:35], v[212:215], v[200:203]\n" \
  "v_mfma_f32_16x16x32_bf16 v[204:207], v[96:99], v[212:215], v[204:207]\n" \
  "ds_read_b128 v[212:215], v252 offset:21504\n" \
  "s_waitcnt lgkmcnt(9)\n" \
  "v_mfma_f32_16x16x32_bf16 v[192:195], v[160:163], v[216:219], v[192:195]\n" \
  "v_mfma_f32_16x16x32_bf16 v[196:199], v[164:167], v[216:219], v[196:199]\n" \
  "ds_read_b128 v[216:219], v252 offset:22528\n" \
  "s_waitcnt lgkmcnt(9)\n" \
  "v_mfma_f32_16x16x32_bf16 v[200:203], v[160:163], v[220:223], v[200:203]\n" \
  "v_mfma_f32_16x16x32_bf16 v[204:207], v[164:167], v[220:223], v[204:207]\n" \
  "ds_read_b128 v[220:223], v252 offset:23552\n" \
  "s_waitcnt lgkmcnt(9)\n" \
  "v_mfma_f32_16x16x32_bf16 v[192:195], v[36:39], v[224:227], v[192:195]\n" \
  "v_mfma_f32_16x16x32_bf16 v[196:199], v[100:103], v[224:227], v[196:199]\n" \
  "s_waitcnt lgkmcnt(8)\n" \
  "v_mfma_f32_16x16x32_bf16 v[200:203], v[36:39], v[228:231], v[200:203]\n" \
  "v_mfma_f32_16x16x32_bf16 v[204:207], v[100:103], v[228:231], v[204:207]\n" \
  "s_waitcnt vmcnt(1)\n" \
  "s_waitcnt lgkmcnt(0)\n" \
  "s_barrier\n" \
  "ds_read_b128 v[224:227], v252 offset:0\n" \
  "ds_read_b128 v[228:231], v252 offset:1024\n" \
  "s_add_u32 m0, %[mw], 12288\n" \
  "s_xor_b32 %[cnt], %[sc], 896\n" \
  "buffer_load_dwordx4 v248, %[rx], %[cnt] offen lds\n" \
  "s_add_u32 m0, %[mw], 16384\n" \
  "s_xor_b32 %[cnt], %[sc], 960\n" \
  "buffer_load_dwordx4 v248, %[rx], %[cnt] offen lds\n" \
  "v_mfma_f32_16x16x32_bf16 v[192:195], v[240:243], v[232:235], v[192:195]\n" \
  "v_mfma_f32_16x16x32_bf16 v[196:199], v[244:247], v[232:235], v[196:199]\n" \
  "ds_read_b128 v[232:235], v252 offset:2048\n" \
  "v_mfma_f32_16x16x32_bf16 v[200:203], v[240:243], v[236:239], v[200:203]\n" \
  "v_mfma_f32_16x16x32_bf16 v[204:207], v[244:247], v[236:239], v[204:207]\n" \
  "ds_read_b128 v[236:239], v252 offset:3072\n" \
  "ds_read_b128 v[240:243], v249 offset:16384\n" \
  "ds_read_b128 v[244:247], v249 offset:17408\n" \
  "v_mfma_f32_16x16x32_bf16 v[192:195], v[40:43], v[208:211], v[192:195]\n" \
  "v_mfma_f32_16x16x32_bf16 v[196:199], v[104:107], v[208:211], v[196:199]\n" \
  "ds_read_b128 v[208:211], v252 offset:4096\n" \
  "v_mfma_f32_16x16x32_bf16 v[200:203], v[40:43], v[212:215], v[200:203]\n" \
  "v_mfma_f32_16x16x32_bf16 v[204:207], v[104:107], v[212:215], v[204:207]\n" \
  "ds_read_b128 v[212:215], v252 offset:5120\n" \
  "v_mfma_f32_16x16x32_bf16 v[192:195], v[168:171], v[216:219], v[192:195]\n" \
  "v_mfma_f32_16x16x32_bf16 v[196:199], v[172:175], v[216:219], v[196:199]\n" \
  "ds_read_b128 v[216:219], v252 offset:6144\n" \
  "v_mfma_f32_16x16x32_bf16 v[200:203], v[168:171], v[220:223], v[200:203]\n" \
  "v_mfma_f32_16x16x32_bf16 v[204:207], v[172:175], v[220:223], v[204:207]\n" \
  "ds_read_b128 v[220:223], v252 offset:7168\n" \
  "s_waitcnt lgkmcnt(9)\n" \
  "v_mfma_f32_16x16x32_bf16 v[192:195], v[44:47], v[224:227], v[192:195]\n" \
  "v_mfma_f32_16x16x32_bf16 v[196:199], v[108:111], v[224:227], v[196:199]\n" \
  "ds_read_b128 v[224:227], v252 offset:8192\n" \
  "s_waitcnt lgkmcnt(9)\n" \
  "v_mfma_f32_16x16x32_bf16 v[200:203], v[44:47], v[228:231], v[200:203]\n" \
  "v_mfma_f32_16x16x32_bf16 v[204:207], v[108:111], v[228:231], v[204:207]\n" \
  "ds_read_b128 v[228:231], v252 offset:9216\n" \
  "s_waitcnt lgkmcnt(7)\n" \
  "v_mfma_f32_16x16x32_bf16 v[192:195], v[240:243], v[232:235], v[192:195]\n" \
  "s_waitcnt lgkmcnt(6)\n" \
  "v_mfma_f32_16x16x32_bf16 v[196:199], v[244:247], v[232:235], v[196:199]\n" \
  "ds_read_b128 v[232:235], v252 offset:10240\n" \
  "v_mfma_f32_16x16x32_bf16 v[200:203], v[240:243], v[236:239], v[200:203]\n" \
  "v_mfma_f32_16x16x32_bf16 v[204:207], v[244:247], v[236:239], v[204:207]\n" \
  "ds_read_b128 v[236:239], v252 offset:11264\n" \
  "ds_read_b128 v[240:243], v249 offset:32768\n" \
  "ds_read_b128 v[244:247], v249 offset:33792\n" \
  "s_waitcnt lgkmcnt(9)\n" \
  "v_mfma_f32_16x16x32_bf16 v[192:195], v[48:51], v[208:211], v[192:195]\n" \
  "v_mfma_f32_16x16x32_bf16 v[196:199], v[112:115], v[208:211], v[196:199]\n" \
  "s_waitcnt lgkmcnt(8)\n" \
  "v_mfma_f32_16x16x32_bf16 v[200:203], v[48:51], v[212:215], v[200:203]\n" \
  "v_mfma_f32_16x16x32_bf16 v[204:207], v[112:115], v[212:215], v[204:207]\n" \
  "s_waitcnt vmcnt(0)\n" \
  "s_waitcnt lgkmcnt(0)\n" \
  "s_barrier\n" \
  "ds_read_b128 v[208:211], v252 offset:12288\n" \
  "ds_read_b128 v[212:215], v252 offset:13312\n" \
  "s_add_u32 m0, %[mw], 0\n" \
  "s_xor_b32 %[cnt], %[sn], 0\n" \
  "buffer_load_dwordx4 v248, %[rx], %[cnt] offen lds\n" \
  "s_add_u32 m0, %[mw], 4096\n" \
  "s_xor_b32 %[cnt], %[sn], 64\n" \
  "buffer_load_dwordx4 v248, %[rx], %[cnt] offen lds\n" \
  "s_add_u32 m0, %[mw], 8192\n" \
  "s_xor_b32 %[cnt], %[sn], 128\n" \
  "buffer_load_dwordx4 v248, %[rx], %[cnt] offen lds\n" \
  "v_mfma_f32_16x16x32_bf16 v[192:195], v[176:179], v[216:219], v[192:195]\n" \
  "v_mfma_f32_16x16x32_bf16 v[196:199], v[180:183], v[216:219], v[196:199]\n" \
  "ds_read_b128 v[216:219], v252 offset:14336\n" \
  "v_mfma_f32_16x16x32_bf16 v[200:203], v[176:179], v[220:223], v[200:203]\n" \
  "v_mfma_f32_16x16x32_bf16 v[204:207], v[180:183], v[220:223], v[204:207]\n" \
  "ds_read_b128 v[220:223], v252 offset:15360\n" \
  "v_mfma_f32_16x16x32_bf16 v[192:195], v[52:55], v[224:227], v[192:195]\n" \
  "v_mfma_f32_16x16x32_bf16 v[196:199], v[116:119], v[224:227], v[196:199]\n" \
  "ds_read_b128 v[224:227], v252 offset:16384\n" \
  "v_mfma_f32_16x16x32_bf16 v[200:203], v[52:55], v[228:231], v[200:203]\n" \
  "v_mfma_f32_16x16x32_bf16 v[204:207], v[116:119], v[228:231], v[204:207]\n" \
  "ds_read_b128 v[228:231], v252 offset:17408\n" \
  "v_mfma_f32_16x16x32_bf16 v[192:195], v[240:243], v[232:235], v[192:195]\n" \
  "v_mfma_f32_16x16x32_bf16 v[196:199], v[244:247], v[232:235], v[196:199]\n" \
  "ds_read_b128 v[232:235], v252 offset:18432\n" \
  "v_mfma_f32_16x16x32_bf16 v[200:203], v[240:243], v[236:239], v[200:203]\n" \
  "v_mfma_f32_16x16x32_bf16 v[204:207], v[244:247], v[236:239], v[204:207]\n" \
  "ds_read_b128 v[236:239], v252 offset:19456\n" \
  "ds_read_b128 v[240:243], v249 offset:49152\n" \
  "ds_read_b128 v[244:247], v249 offset:50176\n" \
  "s_waitcnt lgkmcnt(9)\n" \
  "v_mfma_f32_16x16x32_bf16 v[192:195], v[56:59], v[208:211], v[192:195]\n" \
  "v_mfma_f32_16x16x32_bf16 v[196:199], v[120:123], v[208:211], v[196:199]\n" \
  "s_waitcnt lgkmcnt(8)\n" \
  "v_mfma_f32_16x16x32_bf16 v[200:203], v[56:59], v[212:215], v[200:203]\n" \
  "v_mfma_f32_16x16x32_bf16 v[204:207], v[120:123], v[212:215], v[204:207]\n" \
  "s_waitcnt vmcnt(0)\n" \
  "s_waitcnt lgkmcnt(0)\n" \
  "s_barrier\n" \
  "ds_read_b128 v[208:211], v252 offset:0\n" \
  "ds_read_b128 v[212:215], v252 offset:1024\n" \
  "s_add_u32 m0, %[mw], 12288\n" \
  "s_xor_b32 %[cnt], %[sn], 192\n" \
  "buffer_load_dwordx4 v248, %[rx], %[cnt] offen lds\n" \
  "s_add_u32 m0, %[mw], 16384\n" \
  "s_xor_b32 %[cnt], %[sn], 256\n" \
  "buffer_load_dwordx4 v248, %[rx], %[cnt] offen lds\n" \
  "s_add_u32 m0, %[mw], 20480\n" \
  "s_xor_b32 %[cnt], %[sn], 320\n" \
  "buffer_load_dwordx4 v248, %[rx], %[cnt] offen lds\n" \
  "v_mfma_f32_16x16x32_bf16 v[192:195], v[184:187], v[216:219], v[192:195]\n" \
  "v_mfma_f32_16x16x32_bf16 v[196:199], v[188:191], v[216:219], v[196:199]\n" \
  "ds_read_b128 v[216:219], v252 offset:2048\n" \
  "v_mfma_f32_16x16x32_bf16 v[200:203], v[184:187], v[220:223], v[200:203]\n" \
  "v_mfma_f32_16x16x32_bf16 v[204:207], v[188:191], v[220:223], v[204:207]\n" \
  "ds_read_b128 v[220:223], v252 offset:3072\n" \
  "v_mfma_f32_16x16x32_bf16 v[192:195], v[60:63], v[224:227], v[192:195]\n" \
  "v_mfma_f32_16x16x32_bf16 v[196:199], v[124:127], v[224:227], v[196:199]\n" \
  "ds_read_b128 v[224:227], v252 offset:4096\n" \
  "v_mfma_f32_16x16x32_bf16 v[200:203], v[60:63], v[228:231], v[200:203]\n" \
  "v_mfma_f32_16x16x32_bf16 v[204:207], v[124:127], v[228:231], v[204:207]\n" \
  "ds_read_b128 v[228:231], v252 offset:5120\n" \
  "v_mfma_f32_16x16x32_bf16 v[192:195], v[240:243], v[232:235], v[192:195]\n" \
  "v_mfma_f32_16x16x32_bf16 v[196:199], v[244:247], v[232:235], v[196:199]\n" \
  "ds_read_b128 v[232:235], v252 offset:6144\n" \
  "v_mfma_f32_16x16x32_bf16 v[200:203], v[240:243], v[236:239], v[200:203]\n" \
  "v_mfma_f32_16x16x32_bf16 v[204:207], v[244:247], v[236:239], v[204:207]\n" \
  "ds_read_b128 v[236:239], v252 offset:7168\n" \
  "v_cvt_pk_bf16_f32 v192, v192, v193\n" \
  "v_cvt_pk_bf16_f32 v193, v194, v195\n" \
  "v_cvt_pk_bf16_f32 v194, v196, v197\n" \
  "v_cvt_pk_bf16_f32 v195, v198, v199\n" \
  "v_add_u32 v196, %[so], v251\n" \
  "ds_write_b128 v196, v[192:195] offset:24576\n" \
  "v_cvt_pk_bf16_f32 v200, v200, v201\n" \
  "v_cvt_pk_bf16_f32 v201, v202, v203\n" \
  "v_cvt_pk_bf16_f32 v202, v204, v205\n" \
  "v_cvt_pk_bf16_f32 v203, v206, v207\n" \
  "ds_write_b128 v196, v[200:203] offset:28672\n" \
  "s_mov_b32 m0, %[keep]\n" \
  "s_waitcnt lgkmcnt(0)\n"
#define ST_XB \
  "s_barrier\n" \
  "ds_read_b128 v[192:195], v251 offset:24576\n" \
  "ds_read_b128 v[208:211], v251 offset:28672\n" \
  "ds_read_b128 v[196:199], v251 offset:25600\n" \
  "ds_read_b128 v[212:215], v251 offset:29696\n" \
  "ds_read_b128 v[200:203], v251 offset:26624\n" \
  "ds_read_b128 v[216:219], v251 offset:30720\n" \
  "ds_read_b128 v[204:207], v251 offset:27648\n" \
  "ds_read_b128 v[220:223], v251 offset:31744\n" \
  "ds_read_b128 v[240:243], v250 offset:0\n" \
  "ds_read_b128 v[244:247], v250 offset:16384\n" \
  "s_waitcnt lgkmcnt(9)\n" \
  "v_mfma_f32_16x16x32_bf16 v[224:227], v[192:195], v[0:3], 0\n" \
  "s_waitcnt lgkmcnt(8)\n" \
  "v_mfma_f32_16x16x32_bf16 v[228:231], v[208:211], v[0:3], 0\n" \
  "s_waitcnt lgkmcnt(7)\n" \
  "v_mfma_f32_16x16x32_bf16 v[224:227], v[196:199], v[4:7], v[224:227]\n" \
  "s_waitcnt lgkmcnt(6)\n" \
  "v_mfma_f32_16x16x32_bf16 v[228:231], v[212:215], v[4:7], v[228:231]\n" \
  "s_waitcnt lgkmcnt(5)\n" \
  "v_mfma_f32_16x16x32_bf16 v[224:227], v[200:203], v[8:11], v[224:227]\n" \
  "s_waitcnt lgkmcnt(4)\n" \
  "v_mfma_f32_16x16x32_bf16 v[228:231], v[216:219], v[8:11], v[228:231]\n" \
  "s_waitcnt lgkmcnt(3)\n" \
  "v_mfma_f32_16x16x32_bf16 v[224:227], v[204:207], v[12:15], v[224:227]\n" \
  "s_waitcnt lgkmcnt(2)\n" \
  "v_mfma_f32_16x16x32_bf16 v[228:231], v[220:223], v[12:15], v[228:231]\n" \
  "v_mfma_f32_16x16x32_bf16 v[232:235], v[192:195], v[128:131], 0\n" \
  "v_mfma_f32_16x16x32_bf16 v[236:239], v[208:211], v[128:131], 0\n" \
  "v_mfma_f32_16x16x32_bf16 v[232:235], v[200:203], v[136:139], v[232:235]\n" \
  "v_cvt_pk_bf16_f32 v224, v224, v225\n" \
  "v_cvt_pk_bf16_f32 v226, v226, v227\n" \
  "v_mfma_f32_16x16x32_bf16 v[236:239], v[216:219], v[136:139], v[236:239]\n" \
  "v_cvt_pk_bf16_f32 v225, v228, v229\n" \
  "v_cvt_pk_bf16_f32 v227, v230, v231\n" \
  "s_waitcnt lgkmcnt(1)\n" \
  "v_mfma_f32_16x16x32_bf16 v[232:235], v[196:199], v[240:243], v[232:235]\n" \
  "s_add_u32 %[cnt], %[so], 0*%c[js]+0*%c[ss]\n" \
  "buffer_store_dwordx4 v[224:227], v251, %[rs], %[cnt] offen sc1\n" \
  "v_mfma_f32_16x16x32_bf16 v[236:239], v[212:215], v[240:243], v[236:239]\n" \
  "s_waitcnt lgkmcnt(0)\n" \
  "v_mfma_f32_16x16x32_bf16 v[232:235], v[204:207], v[244:247], v[232:235]\n" \
  "v_mfma_f32_16x16x32_bf16 v[236:239], v[220:223], v[244:247], v[236:239]\n" \
  "ds_read_b128 v[240:243], v250 offset:1024\n" \
  "ds_read_b128 v[244:247], v250 offset:17408\n" \
  "v_mfma_f32_16x16x32_bf16 v[224:227], v[192:195], v[64:67], 0\n" \
  "v_mfma_f32_16x16x32_bf16 v[228:231], v[208:211], v[64:67], 0\n" \
  "v_mfma_f32_16x16x32_bf16 v[224:227], v[196:199], v[68:71], v[224:227]\n" \
  "v_cvt_pk_bf16_f32 v232, v232, v233\n" \
  "v_cvt_pk_bf16_f32 v234, v234, v235\n" \
  "v_mfma_f32_16x16x32_bf16 v[228:231], v[212:215], v[68:71], v[228:231]\n" \
  "v_cvt_pk_bf16_f32 v233, v236, v237\n" \
  "v_cvt_pk_bf16_f32 v235, v238, v239\n" \
  "v_mfma_f32_16x16x32_bf16 v[224:227], v[200:203], v[72:75], v[224:227]\n" \
  "s_add_u32 %[cnt], %[so], 4*%c[js]+0*%c[ss]\n" \
  "buffer_store_dwordx4 v[232:235], v251, %[rs], %[cnt] offen sc1\n" \
  "v_mfma_f32_16x16x32_bf16 v[228:231], v[216:219], v[72:75], v[228:231]\n" \
  "v_mfma_f32_16x16x32_bf16 v[224:227], v[204:207], v[76:79], v[224:227]\n" \
  "v_mfma_f32_16x16x32_bf16 v[228:231], v[220:223], v[76:79], v[228:231]\n" \
  "v_lshlrev_b32 v253, 24, v251\n" \
  "s_waitcnt vmcnt(2)\n" \
  "buffer_store_byte v253, v253, %[rs], %[fo] offen sc1\n" \
  "s_barrier\n" \
  "v_mfma_f32_16x16x32_bf16 v[232:235], v[192:195], v[132:135], 0\n" \
  "v_mfma_f32_16x16x32_bf16 v[236:239], v[208:211], v[132:135], 0\n" \
  "v_mfma_f32_16x16x32_bf16 v[232:235], v[200:203], v[140:143], v[232:235]\n" \
  "v_cvt_pk_bf16_f32 v224, v224, v225\n" \
  "v_cvt_pk_bf16_f32 v226, v226, v227\n" \
  "v_mfma_f32_16x16x32_bf16 v[236:239], v[216:219], v[140:143], v[236:239]\n" \
  "v_cvt_pk_bf16_f32 v225, v228, v229\n" \
  "v_cvt_pk_bf16_f32 v227, v230, v231\n" \
  "s_waitcnt lgkmcnt(1)\n" \
  "v_mfma_f32_16x16x32_bf16 v[232:235], v[196:199], v[240:243], v[232:235]\n" \
  "s_add_u32 %[cnt], %[so], 0*%c[js]+1*%c[ss]\n" \
  "buffer_store_dwordx4 v[224:227], v251, %[rs], %[cnt] offen sc1\n" \
  "v_mfma_f32_16x16x32_bf16 v[236:239], v[212:215], v[240:243], v[236:239]\n" \
  "s_waitcnt lgkmcnt(0)\n" \
  "v_mfma_f32_16x16x32_bf16 v[232:235], v[204:207], v[244:247], v[232:235]\n" \
  "v_mfma_f32_16x16x32_bf16 v[236:239], v[220:223], v[244:247], v[236:239]\n" \
  "ds_read_b128 v[240:243], v250 offset:32768\n" \
  "ds_read_b128 v[244:247], v250 offset:49152\n" \
  "v_mfma_f32_16x16x32_bf16 v[224:227], v[192:195], v[16:19], 0\n" \
  "v_mfma_f32_16x16x32_bf16 v[228:231], v[208:211], v[16:19], 0\n" \
  "v_mfma_f32_16x16x32_bf16 v[224:227], v[196:199], v[20:23], v[224:227]\n" \
  "v_cvt_pk_bf16_f32 v232, v232, v233\n" \
  "v_cvt_pk_bf16_f32 v234, v234, v235\n" \
  "v_mfma_f32_16x16x32_bf16 v[228:231], v[212:215], v[20:23], v[228:231]\n" \
  "v_cvt_pk_bf16_f32 v233, v236, v237\n" \
  "v_cvt_pk_bf16_f32 v235, v238, v239\n" \
  "v_mfma_f32_16x16x32_bf16 v[224:227], v[200:203], v[24:27], v[224:227]\n" \
  "s_add_u32 %[cnt], %[so], 4*%c[js]+1*%c[ss]\n" \
  "buffer_store_dwordx4 v[232:235], v251, %[rs], %[cnt] offen sc1\n" \
  "v_mfma_f32_16x16x32_bf16 v[228:231], v[216:219], v[24:27], v[228:231]\n" \
  "v_mfma_f32_16x16x32_bf16 v[224:227], v[204:207], v[28:31], v[224:227]\n" \
  "v_mfma_f32_16x16x32_bf16 v[228:231], v[220:223], v[28:31], v[228:231]\n" \
  "s_barrier\n" \
  "v_mfma_f32_16x16x32_bf16 v[232:235], v[192:195], v[144:147], 0\n" \
  "v_mfma_f32_16x16x32_bf16 v[236:239], v[208:211], v[144:147], 0\n" \
  "v_mfma_f32_16x16x32_bf16 v[232:235], v[200:203], v[152:155], v[232:235]\n" \
  "v_cvt_pk_bf16_f32 v224, v224, v225\n" \
  "v_cvt_pk_bf16_f32 v226, v226, v227\n" \
  "v_mfma_f32_16x16x32_bf16 v[236:239], v[216:219], v[152:155], v[236:239]\n" \
  "v_cvt_pk_bf16_f32 v225, v228, v229\n" \
  "v_cvt_pk_bf16_f32 v227, v230, v231\n" \
  "s_waitcnt lgkmcnt(1)\n" \
  "v_mfma_f32_16x16x32_bf16 v[232:235], v[196:199], v[240:243], v[232:235]\n" \
  "s_add_u32 %[cnt], %[so], 1*%c[js]+0*%c[ss]\n" \
  "buffer_store_dwordx4 v[224:227], v251, %[rs], %[cnt] offen sc1\n" \
  "v_mfma_f32_16x16x32_bf16 v[236:239], v[212:215], v[240:243], v[236:239]\n" \
  "s_waitcnt lgkmcnt(0)\n" \
  "v_mfma_f32_16x16x32_bf16 v[232:235], v[204:207], v[244:247], v[232:235]\n" \
  "v_mfma_f32_16x16x32_bf16 v[236:239], v[220:223], v[244:247], v[236:239]\n" \
  "ds_read_b128 v[240:243], v250 offset:33792\n" \
  "ds_read_b128 v[244:247], v250 offset:50176\n" \
  "v_mfma_f32_16x16x32_bf16 v[224:227], v[192:195], v[80:83], 0\n" \
  "v_mfma_f32_16x16x32_bf16 v[228:231], v[208:211], v[80:83], 0\n" \
  "v_mfma_f32_16x16x32_bf16 v[224:227], v[196:199], v[84:87], v[224:227]\n" \
  "v_cvt_pk_bf16_f32 v232, v232, v233\n" \
  "v_cvt_pk_bf16_f32 v234, v234, v235\n" \
  "v_mfma_f32_16x16x32_bf16 v[228:231], v[212:215], v[84:87], v[228:231]\n" \
  "v_cvt_pk_bf16_f32 v233, v236, v237\n" \
  "v_cvt_pk_bf16_f32 v235, v238, v239\n" \
  "v_mfma_f32_16x16x32_bf16 v[224:227], v[200:203], v[88:91], v[224:227]\n" \
  "s_add_u32 %[cnt], %[so], 5*%c[js]+0*%c[ss]\n" \
  "buffer_store_dwordx4 v[232:235], v251, %[rs], %[cnt] offen sc1\n" \
  "v_mfma_f32_16x16x32_bf16 v[228:231], v[216:219], v[88:91], v[228:231]\n" \
  "v_mfma_f32_16x16x32_bf16 v[224:227], v[204:207], v[92:95], v[224:227]\n" \
  "v_mfma_f32_16x16x32_bf16 v[228:231], v[220:223], v[92:95], v[228:231]\n" \
  "v_mfma_f32_16x16x32_bf16 v[232:235], v[192:195], v[148:151], 0\n" \
  "v_mfma_f32_16x16x32_bf16 v[236:239], v[208:211], v[148:151], 0\n" \
  "v_mfma_f32_16x16x32_bf16 v[232:235], v[200:203], v[156:159], v[232:235]\n" \
  "v_cvt_pk_bf16_f32 v224, v224, v225\n" \
  "v_cvt_pk_bf16_f32 v226, v226, v227\n" \
  "v_mfma_f32_16x16x32_bf16 v[236:239], v[216:219], v[156:159], v[236:239]\n" \
  "v_cvt_pk_bf16_f32 v225, v228, v229\n" \
  "v_cvt_pk_bf16_f32 v227, v230, v231\n" \
  "s_waitcnt lgkmcnt(1)\n" \
  "v_mfma_f32_16x16x32_bf16 v[232:235], v[196:199], v[240:243], v[232:235]\n" \
  "s_add_u32 %[cnt], %[so], 1*%c[js]+1*%c[ss]\n" \
  "buffer_store_dwordx4 v[224:227], v251, %[rs], %[cnt] offen sc1\n" \
  "v_mfma_f32_16x16x32_bf16 v[236:239], v[212:215], v[240:243], v[236:239]\n" \
  "s_waitcnt lgkmcnt(0)\n" \
  "v_mfma_f32_16x16x32_bf16 v[232:235], v[204:207], v[244:247], v[232:235]\n" \
  "v_mfma_f32_16x16x32_bf16 v[236:239], v[220:223], v[244:247], v[236:239]\n" \
  "ds_read_b128 v[240:243], v249 offset:0\n" \
  "ds_read_b128 v[244:247], v249 offset:16384\n" \
  "s_barrier\n" \
  "v_mfma_f32_16x16x32_bf16 v[224:227], v[192:195], v[32:35], 0\n" \
  "v_mfma_f32_16x16x32_bf16 v[228:231], v[208:211], v[32:35], 0\n" \
  "v_mfma_f32_16x16x32_bf16 v[224:227], v[196:199], v[36:39], v[224:227]\n" \
  "v_cvt_pk_bf16_f32 v232, v232, v233\n" \
  "v_cvt_pk_bf16_f32 v234, v234, v235\n" \
  "v_mfma_f32_16x16x32_bf16 v[228:231], v[212:215], v[36:39], v[228:231]\n" \
  "v_cvt_pk_bf16_f32 v233, v236, v237\n" \
  "v_cvt_pk_bf16_f32 v235, v238, v239\n" \
  "v_mfma_f32_16x16x32_bf16 v[224:227], v[200:203], v[40:43], v[224:227]\n" \
  "s_add_u32 %[cnt], %[so], 5*%c[js]+1*%c[ss]\n" \
  "buffer_store_dwordx4 v[232:235], v251, %[rs], %[cnt] offen sc1\n" \
  "v_mfma_f32_16x16x32_bf16 v[228:231], v[216:219], v[40:43], v[228:231]\n" \
  "v_mfma_f32_16x16x32_bf16 v[224:227], v[204:207], v[44:47], v[224:227]\n" \
  "v_mfma_f32_16x16x32_bf16 v[228:231], v[220:223], v[44:47], v[228:231]\n" \
  "v_mfma_f32_16x16x32_bf16 v[232:235], v[192:195], v[160:163], 0\n" \
  "v_mfma_f32_16x16x32_bf16 v[236:239], v[208:211], v[160:163], 0\n" \
  "v_mfma_f32_16x16x32_bf16 v[232:235], v[200:203], v[168:171], v[232:235]\n" \
  "v_cvt_pk_bf16_f32 v224, v224, v225\n" \
  "v_cvt_pk_bf16_f32 v226, v226, v227\n" \
  "v_mfma_f32_16x16x32_bf16 v[236:239], v[216:219], v[168:171], v[236:239]\n" \
  "v_cvt_pk_bf16_f32 v225, v228, v229\n" \
  "v_cvt_pk_bf16_f32 v227, v230, v231\n" \
  "s_waitcnt lgkmcnt(1)\n" \
  "v_mfma_f32_16x16x32_bf16 v[232:235], v[196:199], v[240:243], v[232:235]\n" \
  "s_add_u32 %[cnt], %[so], 2*%c[js]+0*%c[ss]\n" \
  "buffer_store_dwordx4 v[224:227], v251, %[rs], %[cnt] offen sc1\n" \
  "v_mfma_f32_16x16x32_bf16 v[236:239], v[212:215], v[240:243], v[236:239]\n" \
  "s_waitcnt lgkmcnt(0)\n" \
  "v_mfma_f32_16x16x32_bf16 v[232:235], v[204:207], v[244:247], v[232:235]\n" \
  "v_mfma_f32_16x16x32_bf16 v[236:239], v[220:223], v[244:247], v[236:239]\n" \
  "ds_read_b128 v[240:243], v249 offset:1024\n" \
  "ds_read_b128 v[244:247], v249 offset:17408\n" \
  "v_mfma_f32_16x16x32_bf16 v[224:227], v[192:195], v[96:99], 0\n" \
  "v_mfma_f32_16x16x32_bf16 v[228:231], v[208:211], v[96:99], 0\n" \
  "v_mfma_f32_16x16x32_bf16 v[224:227], v[196:199], v[100:103], v[224:227]\n" \
  "v_cvt_pk_bf16_f32 v232, v232, v233\n" \
  "v_cvt_pk_bf16_f32 v234, v234, v235\n" \
  "v_mfma_f32_16x16x32_bf16 v[228:231], v[212:215], v[100:103], v[228:231]\n" \
  "v_cvt_pk_bf16_f32 v233, v236, v237\n" \
  "v_cvt_pk_bf16_f32 v235, v238, v239\n" \
  "v_mfma_f32_16x16x32_bf16 v[224:227], v[200:203], v[104:107], v[224:227]\n" \
  "s_add_u32 %[cnt], %[so], 6*%c[js]+0*%c[ss]\n" \
  "buffer_store_dwordx4 v[232:235], v251, %[rs], %[cnt] offen sc1\n" \
  "v_mfma_f32_16x16x32_bf16 v[228:231], v[216:219], v[104:107], v[228:231]\n" \
  "v_mfma_f32_16x16x32_bf16 v[224:227], v[204:207], v[108:111], v[224:227]\n" \
  "v_mfma_f32_16x16x32_bf16 v[228:231], v[220:223], v[108:111], v[228:231]\n" \
  "s_barrier\n" \
  "v_mfma_f32_16x16x32_bf16 v[232:235], v[192:195], v[164:167], 0\n" \
  "v_mfma_f32_16x16x32_bf16 v[236:239], v[208:211], v[164:167], 0\n" \
  "v_mfma_f32_16x16x32_bf16 v[232:235], v[200:203], v[172:175], v[232:235]\n" \
  "v_cvt_pk_bf16_f32 v224, v224, v225\n" \
  "v_cvt_pk_bf16_f32 v226, v226, v227\n" \
  "v_mfma_f32_16x16x32_bf16 v[236:239], v[216:219], v[172:175], v[236:239]\n" \
  "v_cvt_pk_bf16_f32 v225, v228, v229\n" \
  "v_cvt_pk_bf16_f32 v227, v230, v231\n" \
  "s_waitcnt lgkmcnt(1)\n" \
  "v_mfma_f32_16x16x32_bf16 v[232:235], v[196:199], v[240:243], v[232:235]\n" \
  "s_add_u32 %[cnt], %[so], 2*%c[js]+1*%c[ss]\n" \
  "buffer_store_dwordx4 v[224:227], v251, %[rs], %[cnt] offen sc1\n" \
  "v_mfma_f32_16x16x32_bf16 v[236:239], v[212:215], v[240:243], v[236:239]\n" \
  "s_waitcnt lgkmcnt(0)\n" \
  "v_mfma_f32_16x16x32_bf16 v[232:235], v[204:207], v[244:247], v[232:235]\n" \
  "v_mfma_f32_16x16x32_bf16 v[236:239], v[220:223], v[244:247], v[236:239]\n" \
  "ds_read_b128 v[240:243], v249 offset:32768\n" \
  "ds_read_b128 v[244:247], v249 offset:49152\n" \
  "v_mfma_f32_16x16x32_bf16 v[224:227], v[192:195], v[48:51], 0\n" \
  "v_mfma_f32_16x16x32_bf16 v[228:231], v[208:211], v[48:51], 0\n" \
  "v_mfma_f32_16x16x32_bf16 v[224:227], v[196:199], v[52:55], v[224:227]\n" \
  "v_cvt_pk_bf16_f32 v232, v232, v233\n" \
  "v_cvt_pk_bf16_f32 v234, v234, v235\n" \
  "v_mfma_f32_16x16x32_bf16 v[228:231], v[212:215], v[52:55], v[228:231]\n" \
  "v_cvt_pk_bf16_f32 v233, v236, v237\n" \
  "v_cvt_pk_bf16_f32 v235, v238, v239\n" \
  "v_mfma_f32_16x16x32_bf16 v[224:227], v[200:203], v[56:59], v[224:227]\n" \
  "s_add_u32 %[cnt], %[so], 6*%c[js]+1*%c[ss]\n" \
  "buffer_store_dwordx4 v[232:235], v251, %[rs], %[cnt] offen sc1\n" \
  "v_mfma_f32_16x16x32_bf16 v[228:231], v[216:219], v[56:59], v[228:231]\n" \
  "v_mfma_f32_16x16x32_bf16 v[224:227], v[204:207], v[60:63], v[224:227]\n" \
  "v_mfma_f32_16x16x32_bf16 v[228:231], v[220:223], v[60:63], v[228:231]\n" \
  "s_barrier\n" \
  "v_mfma_f32_16x16x32_bf16 v[232:235], v[192:195], v[176:179], 0\n" \
  "v_mfma_f32_16x16x32_bf16 v[236:239], v[208:211], v[176:179], 0\n" \
  "v_mfma_f32_16x16x32_bf16 v[232:235], v[200:203], v[184:187], v[232:235]\n" \
  "v_cvt_pk_bf16_f32 v224, v224, v225\n" \
  "v_cvt_pk_bf16_f32 v226, v226, v227\n" \
  "v_mfma_f32_16x16x32_bf16 v[236:239], v[216:219], v[184:187], v[236:239]\n" \
  "v_cvt_pk_bf16_f32 v225, v228, v229\n" \
  "v_cvt_pk_bf16_f32 v227, v230, v231\n" \
  "s_waitcnt lgkmcnt(1)\n" \
  "v_mfma_f32_16x16x32_bf16 v[232:235], v[196:199], v[240:243], v[232:235]\n" \
  "s_add_u32 %[cnt], %[so], 3*%c[js]+0*%c[ss]\n" \
  "buffer_store_dwordx4 v[224:227], v251, %[rs], %[cnt] offen sc1\n" \
  "v_mfma_f32_16x16x32_bf16 v[236:239], v[212:215], v[240:243], v[236:239]\n" \
  "s_waitcnt lgkmcnt(0)\n" \
  "v_mfma_f32_16x16x32_bf16 v[232:235], v[204:207], v[244:247], v[232:235]\n" \
  "v_mfma_f32_16x16x32_bf16 v[236:239], v[220:223], v[244:247], v[236:239]\n" \
  "ds_read_b128 v[240:243], v249 offset:33792\n" \
  "ds_read_b128 v[244:247], v249 offset:50176\n" \
  "v_mfma_f32_16x16x32_bf16 v[224:227], v[192:195], v[112:115], 0\n" \
  "v_mfma_f32_16x16x32_bf16 v[228:231], v[208:211], v[112:115], 0\n" \
  "v_mfma_f32_16x16x32_bf16 v[224:227], v[196:199], v[116:119], v[224:227]\n" \
  "v_cvt_pk_bf16_f32 v232, v232, v233\n" \
  "v_cvt_pk_bf16_f32 v234, v234, v235\n" \
  "v_mfma_f32_16x16x32_bf16 v[228:231], v[212:215], v[116:119], v[228:231]\n" \
  "v_cvt_pk_bf16_f32 v233, v236, v237\n" \
  "v_cvt_pk_bf16_f32 v235, v238, v239\n" \
  "v_mfma_f32_16x16x32_bf16 v[224:227], v[200:203], v[120:123], v[224:227]\n" \
  "s_add_u32 %[cnt], %[so], 7*%c[js]+0*%c[ss]\n" \
  "buffer_store_dwordx4 v[232:235], v251, %[rs], %[cnt] offen sc1\n" \
  "v_mfma_f32_16x16x32_bf16 v[228:231], v[216:219], v[120:123], v[228:231]\n" \
  "v_mfma_f32_16x16x32_bf16 v[224:227], v[204:207], v[124:127], v[224:227]\n" \
  "v_mfma_f32_16x16x32_bf16 v[228:231], v[220:223], v[124:127], v[228:231]\n" \
  "v_mfma_f32_16x16x32_bf16 v[232:235], v[192:195], v[180:183], 0\n" \
  "v_mfma_f32_16x16x32_bf16 v[236:239], v[208:211], v[180:183], 0\n" \
  "v_mfma_f32_16x16x32_bf16 v[232:235], v[200:203], v[188:191], v[232:235]\n" \
  "v_cvt_pk_bf16_f32 v224, v224, v225\n" \
  "v_cvt_pk_bf16_f32 v226, v226, v227\n" \
  "v_mfma_f32_16x16x32_bf16 v[236:239], v[216:219], v[188:191], v[236:239]\n" \
  "v_cvt_pk_bf16_f32 v225, v228, v229\n" \
  "v_cvt_pk_bf16_f32 v227, v230, v231\n" \
  "s_waitcnt lgkmcnt(1)\n" \
  "v_mfma_f32_16x16x32_bf16 v[232:235], v[196:199], v[240:243], v[232:235]\n" \
  "s_add_u32 %[cnt], %[so], 3*%c[js]+1*%c[ss]\n" \
  "buffer_store_dwordx4 v[224:227], v251, %[rs], %[cnt] offen sc1\n" \
  "v_mfma_f32_16x16x32_bf16 v[236:239], v[212:215], v[240:243], v[236:239]\n" \
  "s_waitcnt lgkmcnt(0)\n" \
  "v_mfma_f32_16x16x32_bf16 v[232:235], v[204:207], v[244:247], v[232:235]\n" \
  "v_mfma_f32_16x16x32_bf16 v[236:239], v[220:223], v[244:247], v[236:239]\n" \
  "s_nop 4\n" \
  "v_cvt_pk_bf16_f32 v232, v232, v233\n" \
  "v_cvt_pk_bf16_f32 v234, v234, v235\n" \
  "s_nop 1\n" \
  "v_cvt_pk_bf16_f32 v233, v236, v237\n" \
  "v_cvt_pk_bf16_f32 v235, v238, v239\n" \
  "s_add_u32 %[cnt], %[so], 7*%c[js]+1*%c[ss]\n" \
  "buffer_store_dwordx4 v[232:235], v251, %[rs], %[cnt] offen sc1\n" \
  "s_nop 1\n"
#define ST_XBF \
  "s_barrier\n" \
  "ds_read_b128 v[192:195], v251 offset:24576\n" \
  "ds_read_b128 v[208:211], v251 offset:28672\n" \
  "ds_read_b128 v[196:199], v251 offset:25600\n" \
  "ds_read_b128 v[212:215], v251 offset:29696\n" \
  "ds_read_b128 v[200:203], v251 offset:26624\n" \
  "ds_read_b128 v[216:219], v251 offset:30720\n" \
  "ds_read_b128 v[204:207], v251 offset:27648\n" \
  "ds_read_b128 v[220:223], v251 offset:31744\n" \
  "ds_read_b128 v[240:243], v250 offset:0\n" \
  "ds_read_b128 v[244:247], v250 offset:16384\n" \
  "s_waitcnt lgkmcnt(9)\n" \
  "v_mfma_f32_16x16x32_bf16 v[224:227], v[192:195], v[0:3], 0\n" \
  "s_waitcnt lgkmcnt(8)\n" \
  "v_mfma_f32_16x16x32_bf16 v[228:231], v[208:211], v[0:3], 0\n" \
  "s_waitcnt lgkmcnt(7)\n" \
  "v_mfma_f32_16x16x32_bf16 v[224:227], v[196:199], v[4:7], v[224:227]\n" \
  "s_waitcnt lgkmcnt(6)\n" \
  "v_mfma_f32_16x16x32_bf16 v[228:231], v[212:215], v[4:7], v[228:231]\n" \
  "s_waitcnt lgkmcnt(5)\n" \
  "v_mfma_f32_16x16x32_bf16 v[224:227], v[200:203], v[8:11], v[224:227]\n" \
  "s_waitcnt lgkmcnt(4)\n" \
  "v_mfma_f32_16x16x32_bf16 v[228:231], v[216:219], v[8:11], v[228:231]\n" \
  "s_waitcnt lgkmcnt(3)\n" \
  "v_mfma_f32_16x16x32_bf16 v[224:227], v[204:207], v[12:15], v[224:227]\n" \
  "s_waitcnt lgkmcnt(2)\n" \
  "v_mfma_f32_16x16x32_bf16 v[228:231], v[220:223], v[12:15], v[228:231]\n" \
  "v_mfma_f32_16x16x32_bf16 v[232:235], v[192:195], v[128:131], 0\n" \
  "v_mfma_f32_16x16x32_bf16 v[236:239], v[208:211], v[128:131], 0\n" \
  "v_mfma_f32_16x16x32_bf16 v[232:235], v[200:203], v[136:139], v[232:235]\n" \
  "v_cvt_pk_bf16_f32 v224, v224, v225\n" \
  "v_cvt_pk_bf16_f32 v226, v226, v227\n" \
  "v_mfma_f32_16x16x32_bf16 v[236:239], v[216:219], v[136:139], v[236:239]\n" \
  "v_cvt_pk_bf16_f32 v225, v228, v229\n" \
  "v_cvt_pk_bf16_f32 v227, v230, v231\n" \
  "s_waitcnt lgkmcnt(1)\n" \
  "v_mfma_f32_16x16x32_bf16 v[232:235], v[196:199], v[240:243], v[232:235]\n" \
  "s_add_u32 %[cnt], %[so], 0*%c[js]+0*%c[ss]\n" \
  "buffer_store_dwordx4 v[224:227], v251, %[rs], %[cnt] offen sc1\n" \
  "v_mfma_f32_16x16x32_bf16 v[236:239], v[212:215], v[240:243], v[236:239]\n" \
  "s_waitcnt lgkmcnt(0)\n" \
  "v_mfma_f32_16x16x32_bf16 v[232:235], v[204:207], v[244:247], v[232:235]\n" \
  "v_mfma_f32_16x16x32_bf16 v[236:239], v[220:223], v[244:247], v[236:239]\n" \
  "ds_read_b128 v[240:243], v250 offset:1024\n" \
  "ds_read_b128 v[244:247], v250 offset:17408\n" \
  "v_mfma_f32_16x16x32_bf16 v[224:227], v[192:195], v[64:67], 0\n" \
  "v_mfma_f32_16x16x32_bf16 v[228:231], v[208:211], v[64:67], 0\n" \
  "v_mfma_f32_16x16x32_bf16 v[224:227], v[196:199], v[68:71], v[224:227]\n" \
  "v_cvt_pk_bf16_f32 v232, v232, v233\n" \
  "v_cvt_pk_bf16_f32 v234, v234, v235\n" \
  "v_mfma_f32_16x16x32_bf16 v[228:231], v[212:215], v[68:71], v[228:231]\n" \
  "v_cvt_pk_bf16_f32 v233, v236, v237\n" \
  "v_cvt_pk_bf16_f32 v235, v238, v239\n" \
  "v_mfma_f32_16x16x32_bf16 v[224:227], v[200:203], v[72:75], v[224:227]\n" \
  "s_add_u32 %[cnt], %[so], 4*%c[js]+0*%c[ss]\n" \
  "buffer_store_dwordx4 v[232:235], v251, %[rs], %[cnt] offen sc1\n" \
  "v_mfma_f32_16x16x32_bf16 v[228:231], v[216:219], v[72:75], v[228:231]\n" \
  "v_mfma_f32_16x16x32_bf16 v[224:227], v[204:207], v[76:79], v[224:227]\n" \
  "v_mfma_f32_16x16x32_bf16 v[228:231], v[220:223], v[76:79], v[228:231]\n" \
  "v_mfma_f32_16x16x32_bf16 v[232:235], v[192:195], v[132:135], 0\n" \
  "v_mfma_f32_16x16x32_bf16 v[236:239], v[208:211], v[132:135], 0\n" \
  "v_mfma_f32_16x16x32_bf16 v[232:235], v[200:203], v[140:143], v[232:235]\n" \
  "v_cvt_pk_bf16_f32 v224, v224, v225\n" \
  "v_cvt_pk_bf16_f32 v226, v226, v227\n" \
  "v_mfma_f32_16x16x32_bf16 v[236:239], v[216:219], v[140:143], v[236:239]\n" \
  "v_cvt_pk_bf16_f32 v225, v228, v229\n" \
  "v_cvt_pk_bf16_f32 v227, v230, v231\n" \
  "s_waitcnt lgkmcnt(1)\n" \
  "v_mfma_f32_16x16x32_bf16 v[232:235], v[196:199], v[240:243], v[232:235]\n" \
  "s_add_u32 %[cnt], %[so], 0*%c[js]+1*%c[ss]\n" \
  "buffer_store_dwordx4 v[224:227], v251, %[rs], %[cnt] offen sc1\n" \
  "v_mfma_f32_16x16x32_bf16 v[236:239], v[212:215], v[240:243], v[236:239]\n" \
  "s_waitcnt lgkmcnt(0)\n" \
  "v_mfma_f32_16x16x32_bf16 v[232:235], v[204:207], v[244:247], v[232:235]\n" \
  "v_mfma_f32_16x16x32_bf16 v[236:239], v[220:223], v[244:247], v[236:239]\n" \
  "ds_read_b128 v[240:243], v250 offset:32768\n" \
  "ds_read_b128 v[244:247], v250 offset:49152\n" \
  "s_barrier\n" \
  "v_mfma_f32_16x16x32_bf16 v[224:227], v[192:195], v[16:19], 0\n" \
  "v_mfma_f32_16x16x32_bf16 v[228:231], v[208:211], v[16:19], 0\n" \
  "v_mfma_f32_16x16x32_bf16 v[224:227], v[196:199], v[20:23], v[224:227]\n" \
  "v_cvt_pk_bf16_f32 v232, v232, v233\n" \
  "v_cvt_pk_bf16_f32 v234, v234, v235\n" \
  "v_mfma_f32_16x16x32_bf16 v[228:231], v[212:215], v[20:23], v[228:231]\n" \
  "v_cvt_pk_bf16_f32 v233, v236, v237\n" \
  "v_cvt_pk_bf16_f32 v235, v238, v239\n" \
  "v_mfma_f32_16x16x32_bf16 v[224:227], v[200:203], v[24:27], v[224:227]\n" \
  "s_add_u32 %[cnt], %[so], 4*%c[js]+1*%c[ss]\n" \
  "buffer_store_dwordx4 v[232:235], v251, %[rs], %[cnt] offen sc1\n" \
  "v_mfma_f32_16x16x32_bf16 v[228:231], v[216:219], v[24:27], v[228:231]\n" \
  "v_mfma_f32_16x16x32_bf16 v[224:227], v[204:207], v[28:31], v[224:227]\n" \
  "v_mfma_f32_16x16x32_bf16 v[228:231], v[220:223], v[28:31], v[228:231]\n" \
  "v_mfma_f32_16x16x32_bf16 v[232:235], v[192:195], v[144:147], 0\n" \
  "v_mfma_f32_16x16x32_bf16 v[236:239], v[208:211], v[144:147], 0\n" \
  "v_mfma_f32_16x16x32_bf16 v[232:235], v[200:203], v[152:155], v[232:235]\n" \
  "v_cvt_pk_bf16_f32 v224, v224, v225\n" \
  "v_cvt_pk_bf16_f32 v226, v226, v227\n" \
  "v_mfma_f32_16x16x32_bf16 v[236:239], v[216:219], v[152:155], v[236:239]\n" \
  "v_cvt_pk_bf16_f32 v225, v228, v229\n" \
  "v_cvt_pk_bf16_f32 v227, v230, v231\n" \
  "s_waitcnt lgkmcnt(1)\n" \
  "v_mfma_f32_16x16x32_bf16 v[232:235], v[196:199], v[240:243], v[232:235]\n" \
  "s_add_u32 %[cnt], %[so], 1*%c[js]+0*%c[ss]\n" \
  "buffer_store_dwordx4 v[224:227], v251, %[rs], %[cnt] offen sc1\n" \
  "v_mfma_f32_16x16x32_bf16 v[236:239], v[212:215], v[240:243], v[236:239]\n" \
  "s_waitcnt lgkmcnt(0)\n" \
  "v_mfma_f32_16x16x32_bf16 v[232:235], v[204:207], v[244:247], v[232:235]\n" \
  "v_mfma_f32_16x16x32_bf16 v[236:239], v[220:223], v[244:247], v[236:239]\n" \
  "ds_read_b128 v[240:243], v250 offset:33792\n" \
  "ds_read_b128 v[244:247], v250 offset:50176\n" \
  "v_mfma_f32_16x16x32_bf16 v[224:227], v[192:195], v[80:83], 0\n" \
  "v_mfma_f32_16x16x32_bf16 v[228:231], v[208:211], v[80:83], 0\n" \
  "v_mfma_f32_16x16x32_bf16 v[224:227], v[196:199], v[84:87], v[224:227]\n" \
  "v_cvt_pk_bf16_f32 v232, v232, v233\n" \
  "v_cvt_pk_bf16_f32 v234, v234, v235\n" \
  "v_mfma_f32_16x16x32_bf16 v[228:231], v[212:215], v[84:87], v[228:231]\n" \
  "v_cvt_pk_bf16_f32 v233, v236, v237\n" \
  "v_cvt_pk_bf16_f32 v235, v238, v239\n" \
  "v_mfma_f32_16x16x32_bf16 v[224:227], v[200:203], v[88:91], v[224:227]\n" \
  "s_add_u32 %[cnt], %[so], 5*%c[js]+0*%c[ss]\n" \
  "buffer_store_dwordx4 v[232:235], v251, %[rs], %[cnt] offen sc1\n" \
  "v_mfma_f32_16x16x32_bf16 v[228:231], v[216:219], v[88:91], v[228:231]\n" \
  "v_mfma_f32_16x16x32_bf16 v[224:227], v[204:207], v[92:95], v[224:227]\n" \
  "v_mfma_f32_16x16x32_bf16 v[228:231], v[220:223], v[92:95], v[228:231]\n" \
  "v_mfma_f32_16x16x32_bf16 v[232:235], v[192:195], v[148:151], 0\n" \
  "v_mfma_f32_16x16x32_bf16 v[236:239], v[208:211], v[148:151], 0\n" \
  "v_mfma_f32_16x16x32_bf16 v[232:235], v[200:203], v[156:159], v[232:235]\n" \
  "v_cvt_pk_bf16_f32 v224, v224, v225\n" \
  "v_cvt_pk_bf16_f32 v226, v226, v227\n" \
  "v_mfma_f32_16x16x32_bf16 v[236:239], v[216:219], v[156:159], v[236:239]\n" \
  "v_cvt_pk_bf16_f32 v225, v228, v229\n" \
  "v_cvt_pk_bf16_f32 v227, v230, v231\n" \
  "s_waitcnt lgkmcnt(1)\n" \
  "v_mfma_f32_16x16x32_bf16 v[232:235], v[196:199], v[240:243], v[232:235]\n" \
  "s_add_u32 %[cnt], %[so], 1*%c[js]+1*%c[ss]\n" \
  "buffer_store_dwordx4 v[224:227], v251, %[rs], %[cnt] offen sc1\n" \
  "v_mfma_f32_16x16x32_bf16 v[236:239], v[212:215], v[240:243], v[236:239]\n" \
  "s_waitcnt lgkmcnt(0)\n" \
  "v_mfma_f32_16x16x32_bf16 v[232:235], v[204:207], v[244:247], v[232:235]\n" \
  "v_mfma_f32_16x16x32_bf16 v[236:239], v[220:223], v[244:247], v[236:239]\n" \
  "ds_read_b128 v[240:243], v249 offset:0\n" \
  "ds_read_b128 v[244:247], v249 offset:16384\n" \
  "s_barrier\n" \
  "v_mfma_f32_16x16x32_bf16 v[224:227], v[192:195], v[32:35], 0\n" \
  "v_mfma_f32_16x16x32_bf16 v[228:231], v[208:211], v[32:35], 0\n" \
  "v_mfma_f32_16x16x32_bf16 v[224:227], v[196:199], v[36:39], v[224:227]\n" \
  "v_cvt_pk_bf16_f32 v232, v232, v233\n" \
  "v_cvt_pk_bf16_f32 v234, v234, v235\n" \
  "v_mfma_f32_16x16x32_bf16 v[228:231], v[212:215], v[36:39], v[228:231]\n" \
  "v_cvt_pk_bf16_f32 v233, v236, v237\n" \
  "v_cvt_pk_bf16_f32 v235, v238, v239\n" \
  "v_mfma_f32_16x16x32_bf16 v[224:227], v[200:203], v[40:43], v[224:227]\n" \
  "s_add_u32 %[cnt], %[so], 5*%c[js]+1*%c[ss]\n" \
  "buffer_store_dwordx4 v[232:235], v251, %[rs], %[cnt] offen sc1\n" \
  "v_mfma_f32_16x16x32_bf16 v[228:231], v[216:219], v[40:43], v[228:231]\n" \
  "v_mfma_f32_16x16x32_bf16 v[224:227], v[204:207], v[44:47], v[224:227]\n" \
  "v_mfma_f32_16x16x32_bf16 v[228:231], v[220:223], v[44:47], v[228:231]\n" \
  "v_mfma_f32_16x16x32_bf16 v[232:235], v[192:195], v[160:163], 0\n" \
  "v_mfma_f32_16x16x32_bf16 v[236:239], v[208:211], v[160:163], 0\n" \
  "v_mfma_f32_16x16x32_bf16 v[232:235], v[200:203], v[168:171], v[232:235]\n" \
  "v_cvt_pk_bf16_f32 v224, v224, v225\n" \
  "v_cvt_pk_bf16_f32 v226, v226, v227\n" \
  "v_mfma_f32_16x16x32_bf16 v[236:239], v[216:219], v[168:171], v[236:239]\n" \
  "v_cvt_pk_bf16_f32 v225, v228, v229\n" \
  "v_cvt_pk_bf16_f32 v227, v230, v231\n" \
  "s_waitcnt lgkmcnt(1)\n" \
  "v_mfma_f32_16x16x32_bf16 v[232:235], v[196:199], v[240:243], v[232:235]\n" \
  "s_add_u32 %[cnt], %[so], 2*%c[js]+0*%c[ss]\n" \
  "buffer_store_dwordx4 v[224:227], v251, %[rs], %[cnt] offen sc1\n" \
  "v_mfma_f32_16x16x32_bf16 v[236:239], v[212:215], v[240:243], v[236:239]\n" \
  "s_waitcnt lgkmcnt(0)\n" \
  "v_mfma_f32_16x16x32_bf16 v[232:235], v[204:207], v[244:247], v[232:235]\n" \
  "v_mfma_f32_16x16x32_bf16 v[236:239], v[220:223], v[244:247], v[236:239]\n" \
  "ds_read_b128 v[240:243], v249 offset:1024\n" \
  "ds_read_b128 v[244:247], v249 offset:17408\n" \
  "v_mfma_f32_16x16x32_bf16 v[224:227], v[192:195], v[96:99], 0\n" \
  "v_mfma_f32_16x16x32_bf16 v[228:231], v[208:211], v[96:99], 0\n" \
  "v_mfma_f32_16x16x32_bf16 v[224:227], v[196:199], v[100:103], v[224:227]\n" \
  "v_cvt_pk_bf16_f32 v232, v232, v233\n" \
  "v_cvt_pk_bf16_f32 v234, v234, v235\n" \
  "v_mfma_f32_16x16x32_bf16 v[228:231], v[212:215], v[100:103], v[228:231]\n" \
  "v_cvt_pk_bf16_f32 v233, v236, v237\n" \
  "v_cvt_pk_bf16_f32 v235, v238, v239\n" \
  "v_mfma_f32_16x16x32_bf16 v[224:227], v[200:203], v[104:107], v[224:227]\n" \
  "s_add_u32 %[cnt], %[so], 6*%c[js]+0*%c[ss]\n" \
  "buffer_store_dwordx4 v[232:235], v251, %[rs], %[cnt] offen sc1\n" \
  "v_mfma_f32_16x16x32_bf16 v[228:231], v[216:219], v[104:107], v[228:231]\n" \
  "v_mfma_f32_16x16x32_bf16 v[224:227], v[204:207], v[108:111], v[224:227]\n" \
  "v_mfma_f32_16x16x32_bf16 v[228:231], v[220:223], v[108:111], v[228:231]\n" \
  "v_mfma_f32_16x16x32_bf16 v[232:235], v[192:195], v[164:167], 0\n" \
  "v_mfma_f32_16x16x32_bf16 v[236:239], v[208:211], v[164:167], 0\n" \
  "v_mfma_f32_16x16x32_bf16 v[232:235], v[200:203], v[172:175], v[232:235]\n" \
  "v_cvt_pk_bf16_f32 v224, v224, v225\n" \
  "v_cvt_pk_bf16_f32 v226, v226, v227\n" \
  "v_mfma_f32_16x16x32_bf16 v[236:239], v[216:219], v[172:175], v[236:239]\n" \
  "v_cvt_pk_bf16_f32 v225, v228, v229\n" \
  "v_cvt_pk_bf16_f32 v227, v230, v231\n" \
  "s_waitcnt lgkmcnt(1)\n" \
  "v_mfma_f32_16x16x32_bf16 v[232:235], v[196:199], v[240:243], v[232:235]\n" \
  "s_add_u32 %[cnt], %[so], 2*%c[js]+1*%c[ss]\n" \
  "buffer_store_dwordx4 v[224:227], v251, %[rs], %[cnt] offen sc1\n" \
  "v_mfma_f32_16x16x32_bf16 v[236:239], v[212:215], v[240:243], v[236:239]\n" \
  "s_waitcnt lgkmcnt(0)\n" \
  "v_mfma_f32_16x16x32_bf16 v[232:235], v[204:207], v[244:247], v[232:235]\n" \
  "v_mfma_f32_16x16x32_bf16 v[236:239], v[220:223], v[244:247], v[236:239]\n" \
  "ds_read_b128 v[240:243], v249 offset:32768\n" \
  "ds_read_b128 v[244:247], v249 offset:49152\n" \
  "s_barrier\n" \
  "v_mfma_f32_16x16x32_bf16 v[224:227], v[192:195], v[48:51], 0\n" \
  "v_mfma_f32_16x16x32_bf16 v[228:231], v[208:211], v[48:51], 0\n" \
  "v_mfma_f32_16x16x32_bf16 v[224:227], v[196:199], v[52:55], v[224:227]\n" \
  "v_cvt_pk_bf16_f32 v232, v232, v233\n" \
  "v_cvt_pk_bf16_f32 v234, v234, v235\n" \
  "v_mfma_f32_16x16x32_bf16 v[228:231], v[212:215], v[52:55], v[228:231]\n" \
  "v_cvt_pk_bf16_f32 v233, v236, v237\n" \
  "v_cvt_pk_bf16_f32 v235, v238, v239\n" \
  "v_mfma_f32_16x16x32_bf16 v[224:227], v[200:203], v[56:59], v[224:227]\n" \
  "s_add_u32 %[cnt], %[so], 6*%c[js]+1*%c[ss]\n" \
  "buffer_store_dwordx4 v[232:235], v251, %[rs], %[cnt] offen sc1\n" \
  "v_mfma_f32_16x16x32_bf16 v[228:231], v[216:219], v[56:59], v[228:231]\n" \
  "v_mfma_f32_16x16x32_bf16 v[224:227], v[204:207], v[60:63], v[224:227]\n" \
  "v_mfma_f32_16x16x32_bf16 v[228:231], v[220:223], v[60:63], v[228:231]\n" \
  "v_mfma_f32_16x16x32_bf16 v[232:235], v[192:195], v[176:179], 0\n" \
  "v_mfma_f32_16x16x32_bf16 v[236:239], v[208:211], v[176:179], 0\n" \
  "v_mfma_f32_16x16x32_bf16 v[232:235], v[200:203], v[184:187], v[232:235]\n" \
  "v_cvt_pk_bf16_f32 v224, v224, v225\n" \
  "v_cvt_pk_bf16_f32 v226, v226, v227\n" \
  "v_mfma_f32_16x16x32_bf16 v[236:239], v[216:219], v[184:187], v[236:239]\n" \
  "v_cvt_pk_bf16_f32 v225, v228, v229\n" \
  "v_cvt_pk_bf16_f32 v227, v230, v231\n" \
  "s_waitcnt lgkmcnt(1)\n" \
  "v_mfma_f32_16x16x32_bf16 v[232:235], v[196:199], v[240:243], v[232:235]\n" \
  "s_add_u32 %[cnt], %[so], 3*%c[js]+0*%c[ss]\n" \
  "buffer_store_dwordx4 v[224:227], v251, %[rs], %[cnt] offen sc1\n" \
  "v_mfma_f32_16x16x32_bf16 v[236:239], v[212:215], v[240:243], v[236:239]\n" \
  "s_waitcnt lgkmcnt(0)\n" \
  "v_mfma_f32_16x16x32_bf16 v[232:235], v[204:207], v[244:247], v[232:235]\n" \
  "v_mfma_f32_16x16x32_bf16 v[236:239], v[220:223], v[244:247], v[236:239]\n" \
  "ds_read_b128 v[240:243], v249 offset:33792\n" \
  "ds_read_b128 v[244:247], v249 offset:50176\n" \
  "v_mfma_f32_16x16x32_bf16 v[224:227], v[192:195], v[112:115], 0\n" \
  "v_mfma_f32_16x16x32_bf16 v[228:231], v[208:211], v[112:115], 0\n" \
  "v_mfma_f32_16x16x32_bf16 v[224:227], v[196:199], v[116:119], v[224:227]\n" \
  "v_cvt_pk_bf16_f32 v232, v232, v233\n" \
  "v_cvt_pk_bf16_f32 v234, v234, v235\n" \
  "v_mfma_f32_16x16x32_bf16 v[228:231], v[212:215], v[116:119], v[228:231]\n" \
  "v_cvt_pk_bf16_f32 v233, v236, v237\n" \
  "v_cvt_pk_bf16_f32 v235, v238, v239\n" \
  "v_mfma_f32_16x16x32_bf16 v[224:227], v[200:203], v[120:123], v[224:227]\n" \
  "s_add_u32 %[cnt], %[so], 7*%c[js]+0*%c[ss]\n" \
  "buffer_store_dwordx4 v[232:235], v251, %[rs], %[cnt] offen sc1\n" \
  "v_mfma_f32_16x16x32_bf16 v[228:231], v[216:219], v[120:123], v[228:231]\n" \
  "v_mfma_f32_16x16x32_bf16 v[224:227], v[204:207], v[124:127], v[224:227]\n" \
  "v_mfma_f32_16x16x32_bf16 v[228:231], v[220:223], v[124:127], v[228:231]\n" \
  "v_mfma_f32_16x16x32_bf16 v[232:235], v[192:195], v[180:183], 0\n" \
  "v_mfma_f32_16x16x32_bf16 v[236:239], v[208:211], v[180:183], 0\n" \
  "v_mfma_f32_16x16x32_bf16 v[232:235], v[200:203], v[188:191], v[232:235]\n" \
  "v_cvt_pk_bf16_f32 v224, v224, v225\n" \
  "v_cvt_pk_bf16_f32 v226, v226, v227\n" \
  "v_mfma_f32_16x16x32_bf16 v[236:239], v[216:219], v[188:191], v[236:239]\n" \
  "v_cvt_pk_bf16_f32 v225, v228, v229\n" \
  "v_cvt_pk_bf16_f32 v227, v230, v231\n" \
  "s_waitcnt lgkmcnt(1)\n" \
  "v_mfma_f32_16x16x32_bf16 v[232:235], v[196:199], v[240:243], v[232:235]\n" \
  "s_add_u32 %[cnt], %[so], 3*%c[js]+1*%c[ss]\n" \
  "buffer_store_dwordx4 v[224:227], v251, %[rs], %[cnt] offen sc1\n" \
  "v_mfma_f32_16x16x32_bf16 v[236:239], v[212:215], v[240:243], v[236:239]\n" \
  "s_waitcnt lgkmcnt(0)\n" \
  "v_mfma_f32_16x16x32_bf16 v[232:235], v[204:207], v[244:247], v[232:235]\n" \
  "v_mfma_f32_16x16x32_bf16 v[236:239], v[220:223], v[244:247], v[236:239]\n" \
  "s_nop 4\n" \
  "v_cvt_pk_bf16_f32 v232, v232, v233\n" \
  "v_cvt_pk_bf16_f32 v234, v234, v235\n" \
  "s_nop 1\n" \
  "v_cvt_pk_bf16_f32 v233, v236, v237\n" \
  "v_cvt_pk_bf16_f32 v235, v238, v239\n" \
  "s_add_u32 %[cnt], %[so], 7*%c[js]+1*%c[ss]\n" \
  "buffer_store_dwordx4 v[232:235], v251, %[rs], %[cnt] offen sc1\n" \
  "s_barrier\n" \
  "s_nop 1\n"
#define ST_XBS \
  "ds_read_b128 v[192:195], v251 offset:24576\n" \
  "ds_read_b128 v[208:211], v251 offset:28672\n" \
  "ds_read_b128 v[196:199], v251 offset:25600\n" \
  "ds_read_b128 v[212:215], v251 offset:29696\n" \
  "ds_read_b128 v[200:203], v251 offset:26624\n" \
  "ds_read_b128 v[216:219], v251 offset:30720\n" \
  "ds_read_b128 v[204:207], v251 offset:27648\n" \
  "ds_read_b128 v[220:223], v251 offset:31744\n" \
  "ds_read_b128 v[240:243], v250 offset:0\n" \
  "ds_read_b128 v[244:247], v250 offset:16384\n" \
  "s_waitcnt lgkmcnt(9)\n" \
  "v_mfma_f32_16x16x32_bf16 v[224:227], v[192:195], v[0:3], 0\n" \
  "s_waitcnt lgkmcnt(8)\n" \
  "v_mfma_f32_16x16x32_bf16 v[228:231], v[208:211], v[0:3], 0\n" \
  "s_waitcnt lgkmcnt(7)\n" \
  "v_mfma_f32_16x16x32_bf16 v[224:227], v[196:199], v[4:7], v[224:227]\n" \
  "s_waitcnt lgkmcnt(6)\n" \
  "v_mfma_f32_16x16x32_bf16 v[228:231], v[212:215], v[4:7], v[228:231]\n" \
  "s_waitcnt lgkmcnt(5)\n" \
  "v_mfma_f32_16x16x32_bf16 v[224:227], v[200:203], v[8:11], v[224:227]\n" \
  "s_waitcnt lgkmcnt(4)\n" \
  "v_mfma_f32_16x16x32_bf16 v[228:231], v[216:219], v[8:11], v[228:231]\n" \
  "s_waitcnt lgkmcnt(3)\n" \
  "v_mfma_f32_16x16x32_bf16 v[224:227], v[204:207], v[12:15], v[224:227]\n" \
  "s_waitcnt lgkmcnt(2)\n" \
  "v_mfma_f32_16x16x32_bf16 v[228:231], v[220:223], v[12:15], v[228:231]\n" \
  "v_mfma_f32_16x16x32_bf16 v[232:235], v[192:195], v[128:131], 0\n" \
  "v_mfma_f32_16x16x32_bf16 v[236:239], v[208:211], v[128:131], 0\n" \
  "v_mfma_f32_16x16x32_bf16 v[232:235], v[200:203], v[136:139], v[232:235]\n" \
  "v_cvt_pk_bf16_f32 v224, v224, v225\n" \
  "v_cvt_pk_bf16_f32 v226, v226, v227\n" \
  "v_mfma_f32_16x16x32_bf16 v[236:239], v[216:219], v[136:139], v[236:239]\n" \
  "v_cvt_pk_bf16_f32 v225, v228, v229\n" \
  "v_cvt_pk_bf16_f32 v227, v230, v231\n" \
  "s_waitcnt lgkmcnt(1)\n" \
  "v_mfma_f32_16x16x32_bf16 v[232:235], v[196:199], v[240:243], v[232:235]\n" \
  "s_add_u32 %[cnt], %[so], 0*%c[js]+0*%c[ss]\n" \
  "buffer_store_dwordx4 v[224:227], v251, %[rs], %[cnt] offen sc1\n" \
  "v_mfma_f32_16x16x32_bf16 v[236:239], v[212:215], v[240:243], v[236:239]\n" \
  "s_waitcnt lgkmcnt(0)\n" \
  "v_mfma_f32_16x16x32_bf16 v[232:235], v[204:207], v[244:247], v[232:235]\n" \
  "v_mfma_f32_16x16x32_bf16 v[236:239], v[220:223], v[244:247], v[236:239]\n" \
  "ds_read_b128 v[240:243], v250 offset:1024\n" \
  "ds_read_b128 v[244:247], v250 offset:17408\n" \
  "v_mfma_f32_16x16x32_bf16 v[224:227], v[192:195], v[64:67], 0\n" \
  "v_mfma_f32_16x16x32_bf16 v[228:231], v[208:211], v[64:67], 0\n" \
  "v_mfma_f32_16x16x32_bf16 v[224:227], v[196:199], v[68:71], v[224:227]\n" \
  "v_cvt_pk_bf16_f32 v232, v232, v233\n" \
  "v_cvt_pk_bf16_f32 v234, v234, v235\n" \
  "v_mfma_f32_16x16x32_bf16 v[228:231], v[212:215], v[68:71], v[228:231]\n" \
  "v_cvt_pk_bf16_f32 v233, v236, v237\n" \
  "v_cvt_pk_bf16_f32 v235, v238, v239\n" \
  "v_mfma_f32_16x16x32_bf16 v[224:227], v[200:203], v[72:75], v[224:227]\n" \
  "s_add_u32 %[cnt], %[so], 4*%c[js]+0*%c[ss]\n" \
  "buffer_store_dwordx4 v[232:235], v251, %[rs], %[cnt] offen sc1\n" \
  "v_mfma_f32_16x16x32_bf16 v[228:231], v[216:219], v[72:75], v[228:231]\n" \
  "v_mfma_f32_16x16x32_bf16 v[224:227], v[204:207], v[76:79], v[224:227]\n" \
  "v_mfma_f32_16x16x32_bf16 v[228:231], v[220:223], v[76:79], v[228:231]\n" \
  "v_mfma_f32_16x16x32_bf16 v[232:235], v[192:195], v[132:135], 0\n" \
  "v_mfma_f32_16x16x32_bf16 v[236:239], v[208:211], v[132:135], 0\n" \
  "v_mfma_f32_16x16x32_bf16 v[232:235], v[200:203], v[140:143], v[232:235]\n" \
  "v_cvt_pk_bf16_f32 v224, v224, v225\n" \
  "v_cvt_pk_bf16_f32 v226, v226, v227\n" \
  "v_mfma_f32_16x16x32_bf16 v[236:239], v[216:219], v[140:143], v[236:239]\n" \
  "v_cvt_pk_bf16_f32 v225, v228, v229\n" \
  "v_cvt_pk_bf16_f32 v227, v230, v231\n" \
  "s_waitcnt lgkmcnt(1)\n" \
  "v_mfma_f32_16x16x32_bf16 v[232:235], v[196:199], v[240:243], v[232:235]\n" \
  "s_add_u32 %[cnt], %[so], 0*%c[js]+1*%c[ss]\n" \
  "buffer_store_dwordx4 v[224:227], v251, %[rs], %[cnt] offen sc1\n" \
  "v_mfma_f32_16x16x32_bf16 v[236:239], v[212:215], v[240:243], v[236:239]\n" \
  "s_waitcnt lgkmcnt(0)\n" \
  "v_mfma_f32_16x16x32_bf16 v[232:235], v[204:207], v[244:247], v[232:235]\n" \
  "v_mfma_f32_16x16x32_bf16 v[236:239], v[220:223], v[244:247], v[236:239]\n" \
  "ds_read_b128 v[240:243], v250 offset:32768\n" \
  "ds_read_b128 v[244:247], v250 offset:49152\n" \
  "v_mfma_f32_16x16x32_bf16 v[224:227], v[192:195], v[16:19], 0\n" \
  "v_mfma_f32_16x16x32_bf16 v[228:231], v[208:211], v[16:19], 0\n" \
  "v_mfma_f32_16x16x32_bf16 v[224:227], v[196:199], v[20:23], v[224:227]\n" \
  "v_cvt_pk_bf16_f32 v232, v232, v233\n" \
  "v_cvt_pk_bf16_f32 v234, v234, v235\n" \
  "v_mfma_f32_16x16x32_bf16 v[228:231], v[212:215], v[20:23], v[228:231]\n" \
  "v_cvt_pk_bf16_f32 v233, v236, v237\n" \
  "v_cvt_pk_bf16_f32 v235, v238, v239\n" \
  "v_mfma_f32_16x16x32_bf16 v[224:227], v[200:203], v[24:27], v[224:227]\n" \
  "s_add_u32 %[cnt], %[so], 4*%c[js]+1*%c[ss]\n" \
  "buffer_store_dwordx4 v[232:235], v251, %[rs], %[cnt] offen sc1\n" \
  "v_mfma_f32_16x16x32_bf16 v[228:231], v[216:219], v[24:27], v[228:231]\n" \
  "v_mfma_f32_16x16x32_bf16 v[224:227], v[204:207], v[28:31], v[224:227]\n" \
  "v_mfma_f32_16x16x32_bf16 v[228:231], v[220:223], v[28:31], v[228:231]\n" \
  "v_mfma_f32_16x16x32_bf16 v[232:235], v[192:195], v[144:147], 0\n" \
  "v_mfma_f32_16x16x32_bf16 v[236:239], v[208:211], v[144:147], 0\n" \
  "v_mfma_f32_16x16x32_bf16 v[232:235], v[200:203], v[152:155], v[232:235]\n" \
  "v_cvt_pk_bf16_f32 v224, v224, v225\n" \
  "v_cvt_pk_bf16_f32 v226, v226, v227\n" \
  "v_mfma_f32_16x16x32_bf16 v[236:239], v[216:219], v[152:155], v[236:239]\n" \
  "v_cvt_pk_bf16_f32 v225, v228, v229\n" \
  "v_cvt_pk_bf16_f32 v227, v230, v231\n" \
  "s_waitcnt lgkmcnt(1)\n" \
  "v_mfma_f32_16x16x32_bf16 v[232:235], v[196:199], v[240:243], v[232:235]\n" \
  "s_add_u32 %[cnt], %[so], 1*%c[js]+0*%c[ss]\n" \
  "buffer_store_dwordx4 v[224:227], v251, %[rs], %[cnt] offen sc1\n" \
  "v_mfma_f32_16x16x32_bf16 v[236:239], v[212:215], v[240:243], v[236:239]\n" \
  "s_waitcnt lgkmcnt(0)\n" \
  "v_mfma_f32_16x16x32_bf16 v[232:235], v[204:207], v[244:247], v[232:235]\n" \
  "v_mfma_f32_16x16x32_bf16 v[236:239], v[220:223], v[244:247], v[236:239]\n" \
  "ds_read_b128 v[240:243], v250 offset:33792\n" \
  "ds_read_b128 v[244:247], v250 offset:50176\n" \
  "v_mfma_f32_16x16x32_bf16 v[224:227], v[192:195], v[80:83], 0\n" \
  "v_mfma_f32_16x16x32_bf16 v[228:231], v[208:211], v[80:83], 0\n" \
  "v_mfma_f32_16x16x32_bf16 v[224:227], v[196:199], v[84:87], v[224:227]\n" \
  "v_cvt_pk_bf16_f32 v232, v232, v233\n" \
  "v_cvt_pk_bf16_f32 v234, v234, v235\n" \
  "v_mfma_f32_16x16x32_bf16 v[228:231], v[212:215], v[84:87], v[228:231]\n" \
  "v_cvt_pk_bf16_f32 v233, v236, v237\n" \
  "v_cvt_pk_bf16_f32 v235, v238, v239\n" \
  "v_mfma_f32_16x16x32_bf16 v[224:227], v[200:203], v[88:91], v[224:227]\n" \
  "s_add_u32 %[cnt], %[so], 5*%c[js]+0*%c[ss]\n" \
  "buffer_store_dwordx4 v[232:235], v251, %[rs], %[cnt] offen sc1\n" \
  "v_mfma_f32_16x16x32_bf16 v[228:231], v[216:219], v[88:91], v[228:231]\n" \
  "v_mfma_f32_16x16x32_bf16 v[224:227], v[204:207], v[92:95], v[224:227]\n" \
  "v_mfma_f32_16x16x32_bf16 v[228:231], v[220:223], v[92:95], v[228:231]\n" \
  "v_mfma_f32_16x16x32_bf16 v[232:235], v[192:195], v[148:151], 0\n" \
  "v_mfma_f32_16x16x32_bf16 v[236:239], v[208:211], v[148:151], 0\n" \
  "v_mfma_f32_16x16x32_bf16 v[232:235], v[200:203], v[156:159], v[232:235]\n" \
  "v_cvt_pk_bf16_f32 v224, v224, v225\n" \
  "v_cvt_pk_bf16_f32 v226, v226, v227\n" \
  "v_mfma_f32_16x16x32_bf16 v[236:239], v[216:219], v[156:159], v[236:239]\n" \
  "v_cvt_pk_bf16_f32 v225, v228, v229\n" \
  "v_cvt_pk_bf16_f32 v227, v230, v231\n" \
  "s_waitcnt lgkmcnt(1)\n" \
  "v_mfma_f32_16x16x32_bf16 v[232:235], v[196:199], v[240:243], v[232:235]\n" \
  "s_add_u32 %[cnt], %[so], 1*%c[js]+1*%c[ss]\n" \
  "buffer_store_dwordx4 v[224:227], v251, %[rs], %[cnt] offen sc1\n" \
  "v_mfma_f32_16x16x32_bf16 v[236:239], v[212:215], v[240:243], v[236:239]\n" \
  "s_waitcnt lgkmcnt(0)\n" \
  "v_mfma_f32_16x16x32_bf16 v[232:235], v[204:207], v[244:247], v[232:235]\n" \
  "v_mfma_f32_16x16x32_bf16 v[236:239], v[220:223], v[244:247], v[236:239]\n" \
  "ds_read_b128 v[240:243], v249 offset:0\n" \
  "ds_read_b128 v[244:247], v249 offset:16384\n" \
  "s_barrier\n" \
  "v_mfma_f32_16x16x32_bf16 v[224:227], v[192:195], v[32:35], 0\n" \
  "v_mfma_f32_16x16x32_bf16 v[228:231], v[208:211], v[32:35], 0\n" \
  "v_mfma_f32_16x16x32_bf16 v[224:227], v[196:199], v[36:39], v[224:227]\n" \
  "v_cvt_pk_bf16_f32 v232, v232, v233\n" \
  "v_cvt_pk_bf16_f32 v234, v234, v235\n" \
  "v_mfma_f32_16x16x32_bf16 v[228:231], v[212:215], v[36:39], v[228:231]\n" \
  "v_cvt_pk_bf16_f32 v233, v236, v237\n" \
  "v_cvt_pk_bf16_f32 v235, v238, v239\n" \
  "v_mfma_f32_16x16x32_bf16 v[224:227], v[200:203], v[40:43], v[224:227]\n" \
  "s_add_u32 %[cnt], %[so], 5*%c[js]+1*%c[ss]\n" \
  "buffer_store_dwordx4 v[232:235], v251, %[rs], %[cnt] offen sc1\n" \
  "v_mfma_f32_16x16x32_bf16 v[228:231], v[216:219], v[40:43], v[228:231]\n" \
  "v_mfma_f32_16x16x32_bf16 v[224:227], v[204:207], v[44:47], v[224:227]\n" \
  "v_mfma_f32_16x16x32_bf16 v[228:231], v[220:223], v[44:47], v[228:231]\n" \
  "v_mfma_f32_16x16x32_bf16 v[232:235], v[192:195], v[160:163], 0\n" \
  "v_mfma_f32_16x16x32_bf16 v[236:239], v[208:211], v[160:163], 0\n" \
  "v_mfma_f32_16x16x32_bf16 v[232:235], v[200:203], v[168:171], v[232:235]\n" \
  "v_cvt_pk_bf16_f32 v224, v224, v225\n" \
  "v_cvt_pk_bf16_f32 v226, v226, v227\n" \
  "v_mfma_f32_16x16x32_bf16 v[236:239], v[216:219], v[168:171], v[236:239]\n" \
  "v_cvt_pk_bf16_f32 v225, v228, v229\n" \
  "v_cvt_pk_bf16_f32 v227, v230, v231\n" \
  "s_waitcnt lgkmcnt(1)\n" \
  "v_mfma_f32_16x16x32_bf16 v[232:235], v[196:199], v[240:243], v[232:235]\n" \
  "s_add_u32 %[cnt], %[so], 2*%c[js]+0*%c[ss]\n" \
  "buffer_store_dwordx4 v[224:227], v251, %[rs], %[cnt] offen sc1\n" \
  "v_mfma_f32_16x16x32_bf16 v[236:239], v[212:215], v[240:243], v[236:239]\n" \
  "s_waitcnt lgkmcnt(0)\n" \
  "v_mfma_f32_16x16x32_bf16 v[232:235], v[204:207], v[244:247], v[232:235]\n" \
  "v_mfma_f32_16x16x32_bf16 v[236:239], v[220:223], v[244:247], v[236:239]\n" \
  "ds_read_b128 v[240:243], v249 offset:1024\n" \
  "ds_read_b128 v[244:247], v249 offset:17408\n" \
  "v_mfma_f32_16x16x32_bf16 v[224:227], v[192:195], v[96:99], 0\n" \
  "v_mfma_f32_16x16x32_bf16 v[228:231], v[208:211], v[96:99], 0\n" \
  "v_mfma_f32_16x16x32_bf16 v[224:227], v[196:199], v[100:103], v[224:227]\n" \
  "v_cvt_pk_bf16_f32 v232, v232, v233\n" \
  "v_cvt_pk_bf16_f32 v234, v234, v235\n" \
  "v_mfma_f32_16x16x32_bf16 v[228:231], v[212:215], v[100:103], v[228:231]\n" \
  "v_cvt_pk_bf16_f32 v233, v236, v237\n" \
  "v_cvt_pk_bf16_f32 v235, v238, v239\n" \
  "v_mfma_f32_16x16x32_bf16 v[224:227], v[200:203], v[104:107], v[224:227]\n" \
  "s_add_u32 %[cnt], %[so], 6*%c[js]+0*%c[ss]\n" \
  "buffer_store_dwordx4 v[232:235], v251, %[rs], %[cnt] offen sc1\n" \
  "v_mfma_f32_16x16x32_bf16 v[228:231], v[216:219], v[104:107], v[228:231]\n" \
  "v_mfma_f32_16x16x32_bf16 v[224:227], v[204:207], v[108:111], v[224:227]\n" \
  "v_mfma_f32_16x16x32_bf16 v[228:231], v[220:223], v[108:111], v[228:231]\n" \
  "v_mfma_f32_16x16x32_bf16 v[232:235], v[192:195], v[164:167], 0\n" \
  "v_mfma_f32_16x16x32_bf16 v[236:239], v[208:211], v[164:167], 0\n" \
  "v_mfma_f32_16x16x32_bf16 v[232:235], v[200:203], v[172:175], v[232:235]\n" \
  "v_cvt_pk_bf16_f32 v224, v224, v225\n" \
  "v_cvt_pk_bf16_f32 v226, v226, v227\n" \
  "v_mfma_f32_16x16x32_bf16 v[236:239], v[216:219], v[172:175], v[236:239]\n" \
  "v_cvt_pk_bf16_f32 v225, v228, v229\n" \
  "v_cvt_pk_bf16_f32 v227, v230, v231\n" \
  "s_waitcnt lgkmcnt(1)\n" \
  "v_mfma_f32_16x16x32_bf16 v[232:235], v[196:199], v[240:243], v[232:235]\n" \
  "s_add_u32 %[cnt], %[so], 2*%c[js]+1*%c[ss]\n" \
  "buffer_store_dwordx4 v[224:227], v251, %[rs], %[cnt] offen sc1\n" \
  "v_mfma_f32_16x16x32_bf16 v[236:239], v[212:215], v[240:243], v[236:239]\n" \
  "s_waitcnt lgkmcnt(0)\n" \
  "v_mfma_f32_16x16x32_bf16 v[232:235], v[204:207], v[244:247], v[232:235]\n" \
  "v_mfma_f32_16x16x32_bf16 v[236:239], v[220:223], v[244:247], v[236:239]\n" \
  "ds_read_b128 v[240:243], v249 offset:32768\n" \
  "ds_read_b128 v[244:247], v249 offset:49152\n" \
  "s_barrier\n" \
  "v_mfma_f32_16x16x32_bf16 v[224:227], v[192:195], v[48:51], 0\n" \
  "v_mfma_f32_16x16x32_bf16 v[228:231], v[208:211], v[48:51], 0\n" \
  "v_mfma_f32_16x16x32_bf16 v[224:227], v[196:199], v[52:55], v[224:227]\n" \
  "v_cvt_pk_bf16_f32 v232, v232, v233\n" \
  "v_cvt_pk_bf16_f32 v234, v234, v235\n" \
  "v_mfma_f32_16x16x32_bf16 v[228:231], v[212:215], v[52:55], v[228:231]\n" \
  "v_cvt_pk_bf16_f32 v233, v236, v237\n" \
  "v_cvt_pk_bf16_f32 v235, v238, v239\n" \
  "v_mfma_f32_16x16x32_bf16 v[224:227], v[200:203], v[56:59], v[224:227]\n" \
  "s_add_u32 %[cnt], %[so], 6*%c[js]+1*%c[ss]\n" \
  "buffer_store_dwordx4 v[232:235], v251, %[rs], %[cnt] offen sc1\n" \
  "v_mfma_f32_16x16x32_bf16 v[228:231], v[216:219], v[56:59], v[228:231]\n" \
  "v_mfma_f32_16x16x32_bf16 v[224:227], v[204:207], v[60:63], v[224:227]\n" \
  "v_mfma_f32_16x16x32_bf16 v[228:231], v[220:223], v[60:63], v[228:231]\n" \
  "v_mfma_f32_16x16x32_bf16 v[232:235], v[192:195], v[176:179], 0\n" \
  "v_mfma_f32_16x16x32_bf16 v[236:239], v[208:211], v[176:179], 0\n" \
  "v_mfma_f32_16x16x32_bf16 v[232:235], v[200:203], v[184:187], v[232:235]\n" \
  "v_cvt_pk_bf16_f32 v224, v224, v225\n" \
  "v_cvt_pk_bf16_f32 v226, v226, v227\n" \
  "v_mfma_f32_16x16x32_bf16 v[236:239], v[216:219], v[184:187], v[236:239]\n" \
  "v_cvt_pk_bf16_f32 v225, v228, v229\n" \
  "v_cvt_pk_bf16_f32 v227, v230, v231\n" \
  "s_waitcnt lgkmcnt(1)\n" \
  "v_mfma_f32_16x16x32_bf16 v[232:235], v[196:199], v[240:243], v[232:235]\n" \
  "s_add_u32 %[cnt], %[so], 3*%c[js]+0*%c[ss]\n" \
  "buffer_store_dwordx4 v[224:227], v251, %[rs], %[cnt] offen sc1\n" \
  "v_mfma_f32_16x16x32_bf16 v[236:239], v[212:215], v[240:243], v[236:239]\n" \
  "s_waitcnt lgkmcnt(0)\n" \
  "v_mfma_f32_16x16x32_bf16 v[232:235], v[204:207], v[244:247], v[232:235]\n" \
  "v_mfma_f32_16x16x32_bf16 v[236:239], v[220:223], v[244:247], v[236:239]\n" \
  "ds_read_b128 v[240:243], v249 offset:33792\n" \
  "ds_read_b128 v[244:247], v249 offset:50176\n" \
  "v_mfma_f32_16x16x32_bf16 v[224:227], v[192:195], v[112:115], 0\n" \
  "v_mfma_f32_16x16x32_bf16 v[228:231], v[208:211], v[112:115], 0\n" \
  "v_mfma_f32_16x16x32_bf16 v[224:227], v[196:199], v[116:119], v[224:227]\n" \
  "v_cvt_pk_bf16_f32 v232, v232, v233\n" \
  "v_cvt_pk_bf16_f32 v234, v234, v235\n" \
  "v_mfma_f32_16x16x32_bf16 v[228:231], v[212:215], v[116:119], v[228:231]\n" \
  "v_cvt_pk_bf16_f32 v233, v236, v237\n" \
  "v_cvt_pk_bf16_f32 v235, v238, v239\n" \
  "v_mfma_f32_16x16x32_bf16 v[224:227], v[200:203], v[120:123], v[224:227]\n" \
  "s_add_u32 %[cnt], %[so], 7*%c[js]+0*%c[ss]\n" \
  "buffer_store_dwordx4 v[232:235], v251, %[rs], %[cnt] offen sc1\n" \
  "v_mfma_f32_16x16x32_bf16 v[228:231], v[216:219], v[120:123], v[228:231]\n" \
  "v_mfma_f32_16x16x32_bf16 v[224:227], v[204:207], v[124:127], v[224:227]\n" \
  "v_mfma_f32_16x16x32_bf16 v[228:231], v[220:223], v[124:127], v[228:231]\n" \
  "v_mfma_f32_16x16x32_bf16 v[232:235], v[192:195], v[180:183], 0\n" \
  "v_mfma_f32_16x16x32_bf16 v[236:239], v[208:211], v[180:183], 0\n" \
  "v_mfma_f32_16x16x32_bf16 v[232:235], v[200:203], v[188:191], v[232:235]\n" \
  "v_cvt_pk_bf16_f32 v224, v224, v225\n" \
  "v_cvt_pk_bf16_f32 v226, v226, v227\n" \
  "v_mfma_f32_16x16x32_bf16 v[236:239], v[216:219], v[188:191], v[236:239]\n" \
  "v_cvt_pk_bf16_f32 v225, v228, v229\n" \
  "v_cvt_pk_bf16_f32 v227, v230, v231\n" \
  "s_waitcnt lgkmcnt(1)\n" \
  "v_mfma_f32_16x16x32_bf16 v[232:235], v[196:199], v[240:243], v[232:235]\n" \
  "s_add_u32 %[cnt], %[so], 3*%c[js]+1*%c[ss]\n" \
  "buffer_store_dwordx4 v[224:227], v251, %[rs], %[cnt] offen sc1\n" \
  "v_mfma_f32_16x16x32_bf16 v[236:239], v[212:215], v[240:243], v[236:239]\n" \
  "s_waitcnt lgkmcnt(0)\n" \
  "v_mfma_f32_16x16x32_bf16 v[232:235], v[204:207], v[244:247], v[232:235]\n" \
  "v_mfma_f32_16x16x32_bf16 v[236:239], v[220:223], v[244:247], v[236:239]\n" \
  "s_nop 4\n" \
  "v_cvt_pk_bf16_f32 v232, v232, v233\n" \
  "v_cvt_pk_bf16_f32 v234, v234, v235\n" \
  "s_nop 1\n" \
  "v_cvt_pk_bf16_f32 v233, v236, v237\n" \
  "v_cvt_pk_bf16_f32 v235, v238, v239\n" \
  "s_add_u32 %[cnt], %[so], 7*%c[js]+1*%c[ss]\n" \
  "buffer_store_dwordx4 v[232:235], v251, %[rs], %[cnt] offen sc1\n" \
  "s_nop 1\n"
#define ST_XB0 \
  "ds_read_b128 v[192:195], v251 offset:24576\n" \
  "ds_read_b128 v[208:211], v251 offset:28672\n" \
  "ds_read_b128 v[196:199], v251 offset:25600\n" \
  "ds_read_b128 v[212:215], v251 offset:29696\n" \
  "ds_read_b128 v[200:203], v251 offset:26624\n" \
  "ds_read_b128 v[216:219], v251 offset:30720\n" \
  "ds_read_b128 v[204:207], v251 offset:27648\n" \
  "ds_read_b128 v[220:223], v251 offset:31744\n" \
  "ds_read_b128 v[240:243], v250 offset:0\n" \
  "ds_read_b128 v[244:247], v250 offset:16384\n" \
  "s_waitcnt lgkmcnt(9)\n" \
  "v_mfma_f32_16x16x32_bf16 v[224:227], v[192:195], v[0:3], 0\n" \
  "s_waitcnt lgkmcnt(8)\n" \
  "v_mfma_f32_16x16x32_bf16 v[228:231], v[208:211], v[0:3], 0\n" \
  "s_waitcnt lgkmcnt(7)\n" \
  "v_mfma_f32_16x16x32_bf16 v[224:227], v[196:199], v[4:7], v[224:227]\n" \
  "s_waitcnt lgkmcnt(6)\n" \
  "v_mfma_f32_16x16x32_bf16 v[228:231], v[212:215], v[4:7], v[228:231]\n" \
  "s_waitcnt lgkmcnt(5)\n" \
  "v_mfma_f32_16x16x32_bf16 v[224:227], v[200:203], v[8:11], v[224:227]\n" \
  "s_waitcnt lgkmcnt(4)\n" \
  "v_mfma_f32_16x16x32_bf16 v[228:231], v[216:219], v[8:11], v[228:231]\n" \
  "s_waitcnt lgkmcnt(3)\n" \
  "v_mfma_f32_16x16x32_bf16 v[224:227], v[204:207], v[12:15], v[224:227]\n" \
  "s_waitcnt lgkmcnt(2)\n" \
  "v_mfma_f32_16x16x32_bf16 v[228:231], v[220:223], v[12:15], v[228:231]\n" \
  "v_mfma_f32_16x16x32_bf16 v[232:235], v[192:195], v[128:131], 0\n" \
  "v_mfma_f32_16x16x32_bf16 v[236:239], v[208:211], v[128:131], 0\n" \
  "v_mfma_f32_16x16x32_bf16 v[232:235], v[200:203], v[136:139], v[232:235]\n" \
  "v_cvt_pk_bf16_f32 v224, v224, v225\n" \
  "v_cvt_pk_bf16_f32 v226, v226, v227\n" \
  "v_mfma_f32_16x16x32_bf16 v[236:239], v[216:219], v[136:139], v[236:239]\n" \
  "v_cvt_pk_bf16_f32 v225, v228, v229\n" \
  "v_cvt_pk_bf16_f32 v227, v230, v231\n" \
  "s_waitcnt lgkmcnt(1)\n" \
  "v_mfma_f32_16x16x32_bf16 v[232:235], v[196:199], v[240:243], v[232:235]\n" \
  "s_add_u32 %[cnt], %[so], 0*%c[js]+0*%c[ss]\n" \
  "buffer_store_dwordx4 v[224:227], v251, %[rs], %[cnt] offen sc1\n" \
  "v_mfma_f32_16x16x32_bf16 v[236:239], v[212:215], v[240:243], v[236:239]\n" \
  "s_waitcnt lgkmcnt(0)\n" \
  "v_mfma_f32_16x16x32_bf16 v[232:235], v[204:207], v[244:247], v[232:235]\n" \
  "v_mfma_f32_16x16x32_bf16 v[236:239], v[220:223], v[244:247], v[236:239]\n" \
  "ds_read_b128 v[240:243], v250 offset:1024\n" \
  "ds_read_b128 v[244:247], v250 offset:17408\n" \
  "v_mfma_f32_16x16x32_bf16 v[224:227], v[192:195], v[64:67], 0\n" \
  "v_mfma_f32_16x16x32_bf16 v[228:231], v[208:211], v[64:67], 0\n" \
  "v_mfma_f32_16x16x32_bf16 v[224:227], v[196:199], v[68:71], v[224:227]\n" \
  "v_cvt_pk_bf16_f32 v232, v232, v233\n" \
  "v_cvt_pk_bf16_f32 v234, v234, v235\n" \
  "v_mfma_f32_16x16x32_bf16 v[228:231], v[212:215], v[68:71], v[228:231]\n" \
  "v_cvt_pk_bf16_f32 v233, v236, v237\n" \
  "v_cvt_pk_bf16_f32 v235, v238, v239\n" \
  "v_mfma_f32_16x16x32_bf16 v[224:227], v[200:203], v[72:75], v[224:227]\n" \
  "s_add_u32 %[cnt], %[so], 4*%c[js]+0*%c[ss]\n" \
  "buffer_store_dwordx4 v[232:235], v251, %[rs], %[cnt] offen sc1\n" \
  "v_mfma_f32_16x16x32_bf16 v[228:231], v[216:219], v[72:75], v[228:231]\n" \
  "v_mfma_f32_16x16x32_bf16 v[224:227], v[204:207], v[76:79], v[224:227]\n" \
  "v_mfma_f32_16x16x32_bf16 v[228:231], v[220:223], v[76:79], v[228:231]\n" \
  "v_mfma_f32_16x16x32_bf16 v[232:235], v[192:195], v[132:135], 0\n" \
  "v_mfma_f32_16x16x32_bf16 v[236:239], v[208:211], v[132:135], 0\n" \
  "v_mfma_f32_16x16x32_bf16 v[232:235], v[200:203], v[140:143], v[232:235]\n" \
  "v_cvt_pk_bf16_f32 v224, v224, v225\n" \
  "v_cvt_pk_bf16_f32 v226, v226, v227\n" \
  "v_mfma_f32_16x16x32_bf16 v[236:239], v[216:219], v[140:143], v[236:239]\n" \
  "v_cvt_pk_bf16_f32 v225, v228, v229\n" \
  "v_cvt_pk_bf16_f32 v227, v230, v231\n" \
  "s_waitcnt lgkmcnt(1)\n" \
  "v_mfma_f32_16x16x32_bf16 v[232:235], v[196:199], v[240:243], v[232:235]\n" \
  "s_add_u32 %[cnt], %[so], 0*%c[js]+1*%c[ss]\n" \
  "buffer_store_dwordx4 v[224:227], v251, %[rs], %[cnt] offen sc1\n" \
  "v_mfma_f32_16x16x32_bf16 v[236:239], v[212:215], v[240:243], v[236:239]\n" \
  "s_waitcnt lgkmcnt(0)\n" \
  "v_mfma_f32_16x16x32_bf16 v[232:235], v[204:207], v[244:247], v[232:235]\n" \
  "v_mfma_f32_16x16x32_bf16 v[236:239], v[220:223], v[244:247], v[236:239]\n" \
  "ds_read_b128 v[240:243], v250 offset:32768\n" \
  "ds_read_b128 v[244:247], v250 offset:49152\n" \
  "v_mfma_f32_16x16x32_bf16 v[224:227], v[192:195], v[16:19], 0\n" \
  "v_mfma_f32_16x16x32_bf16 v[228:231], v[208:211], v[16:19], 0\n" \
  "v_mfma_f32_16x16x32_bf16 v[224:227], v[196:199], v[20:23], v[224:227]\n" \
  "v_cvt_pk_bf16_f32 v232, v232, v233\n" \
  "v_cvt_pk_bf16_f32 v234, v234, v235\n" \
  "v_mfma_f32_16x16x32_bf16 v[228:231], v[212:215], v[20:23], v[228:231]\n" \
  "v_cvt_pk_bf16_f32 v233, v236, v237\n" \
  "v_cvt_pk_bf16_f32 v235, v238, v239\n" \
  "v_mfma_f32_16x16x32_bf16 v[224:227], v[200:203], v[24:27], v[224:227]\n" \
  "s_add_u32 %[cnt], %[so], 4*%c[js]+1*%c[ss]\n" \
  "buffer_store_dwordx4 v[232:235], v251, %[rs], %[cnt] offen sc1\n" \
  "v_mfma_f32_16x16x32_bf16 v[228:231], v[216:219], v[24:27], v[228:231]\n" \
  "v_mfma_f32_16x16x32_bf16 v[224:227], v[204:207], v[28:31], v[224:227]\n" \
  "v_mfma_f32_16x16x32_bf16 v[228:231], v[220:223], v[28:31], v[228:231]\n" \
  "v_mfma_f32_16x16x32_bf16 v[232:235], v[192:195], v[144:147], 0\n" \
  "v_mfma_f32_16x16x32_bf16 v[236:239], v[208:211], v[144:147], 0\n" \
  "v_mfma_f32_16x16x32_bf16 v[232:235], v[200:203], v[152:155], v[232:235]\n" \
  "v_cvt_pk_bf16_f32 v224, v224, v225\n" \
  "v_cvt_pk_bf16_f32 v226, v226, v227\n" \
  "v_mfma_f32_16x16x32_bf16 v[236:239], v[216:219], v[152:155], v[236:239]\n" \
  "v_cvt_pk_bf16_f32 v225, v228, v229\n" \
  "v_cvt_pk_bf16_f32 v227, v230, v231\n" \
  "s_waitcnt lgkmcnt(1)\n" \
  "v_mfma_f32_16x16x32_bf16 v[232:235], v[196:199], v[240:243], v[232:235]\n" \
  "s_add_u32 %[cnt], %[so], 1*%c[js]+0*%c[ss]\n" \
  "buffer_store_dwordx4 v[224:227], v251, %[rs], %[cnt] offen sc1\n" \
  "v_mfma_f32_16x16x32_bf16 v[236:239], v[212:215], v[240:243], v[236:239]\n" \
  "s_waitcnt lgkmcnt(0)\n" \
  "v_mfma_f32_16x16x32_bf16 v[232:235], v[204:207], v[244:247], v[232:235]\n" \
  "v_mfma_f32_16x16x32_bf16 v[236:239], v[220:223], v[244:247], v[236:239]\n" \
  "ds_read_b128 v[240:243], v250 offset:33792\n" \
  "ds_read_b128 v[244:247], v250 offset:50176\n" \
  "v_mfma_f32_16x16x32_bf16 v[224:227], v[192:195], v[80:83], 0\n" \
  "v_mfma_f32_16x16x32_bf16 v[228:231], v[208:211], v[80:83], 0\n" \
  "v_mfma_f32_16x16x32_bf16 v[224:227], v[196:199], v[84:87], v[224:227]\n" \
  "v_cvt_pk_bf16_f32 v232, v232, v233\n" \
  "v_cvt_pk_bf16_f32 v234, v234, v235\n" \
  "v_mfma_f32_16x16x32_bf16 v[228:231], v[212:215], v[84:87], v[228:231]\n" \
  "v_cvt_pk_bf16_f32 v233, v236, v237\n" \
  "v_cvt_pk_bf16_f32 v235, v238, v239\n" \
  "v_mfma_f32_16x16x32_bf16 v[224:227], v[200:203], v[88:91], v[224:227]\n" \
  "s_add_u32 %[cnt], %[so], 5*%c[js]+0*%c[ss]\n" \
  "buffer_store_dwordx4 v[232:235], v251, %[rs], %[cnt] offen sc1\n" \
  "v_mfma_f32_16x16x32_bf16 v[228:231], v[216:219], v[88:91], v[228:231]\n" \
  "v_mfma_f32_16x16x32_bf16 v[224:227], v[204:207], v[92:95], v[224:227]\n" \
  "v_mfma_f32_16x16x32_bf16 v[228:231], v[220:223], v[92:95], v[228:231]\n" \
  "v_mfma_f32_16x16x32_bf16 v[232:235], v[192:195], v[148:151], 0\n" \
  "v_mfma_f32_16x16x32_bf16 v[236:239], v[208:211], v[148:151], 0\n" \
  "v_mfma_f32_16x16x32_bf16 v[232:235], v[200:203], v[156:159], v[232:235]\n" \
  "v_cvt_pk_bf16_f32 v224, v224, v225\n" \
  "v_cvt_pk_bf16_f32 v226, v226, v227\n" \
  "v_mfma_f32_16x16x32_bf16 v[236:239], v[216:219], v[156:159], v[236:239]\n" \
  "v_cvt_pk_bf16_f32 v225, v228, v229\n" \
  "v_cvt_pk_bf16_f32 v227, v230, v231\n" \
  "s_waitcnt lgkmcnt(1)\n" \
  "v_mfma_f32_16x16x32_bf16 v[232:235], v[196:199], v[240:243], v[232:235]\n" \
  "s_add_u32 %[cnt], %[so], 1*%c[js]+1*%c[ss]\n" \
  "buffer_store_dwordx4 v[224:227], v251, %[rs], %[cnt] offen sc1\n" \
  "v_mfma_f32_16x16x32_bf16 v[236:239], v[212:215], v[240:243], v[236:239]\n" \
  "s_waitcnt lgkmcnt(0)\n" \
  "v_mfma_f32_16x16x32_bf16 v[232:235], v[204:207], v[244:247], v[232:235]\n" \
  "v_mfma_f32_16x16x32_bf16 v[236:239], v[220:223], v[244:247], v[236:239]\n" \
  "ds_read_b128 v[240:243], v249 offset:0\n" \
  "ds_read_b128 v[244:247], v249 offset:16384\n" \
  "v_mfma_f32_16x16x32_bf16 v[224:227], v[192:195], v[32:35], 0\n" \
  "v_mfma_f32_16x16x32_bf16 v[228:231], v[208:211], v[32:35], 0\n" \
  "v_mfma_f32_16x16x32_bf16 v[224:227], v[196:199], v[36:39], v[224:227]\n" \
  "v_cvt_pk_bf16_f32 v232, v232, v233\n" \
  "v_cvt_pk_bf16_f32 v234, v234, v235\n" \
  "v_mfma_f32_16x16x32_bf16 v[228:231], v[212:215], v[36:39], v[228:231]\n" \
  "v_cvt_pk_bf16_f32 v233, v236, v237\n" \
  "v_cvt_pk_bf16_f32 v235, v238, v239\n" \
  "v_mfma_f32_16x16x32_bf16 v[224:227], v[200:203], v[40:43], v[224:227]\n" \
  "s_add_u32 %[cnt], %[so], 5*%c[js]+1*%c[ss]\n" \
  "buffer_store_dwordx4 v[232:235], v251, %[rs], %[cnt] offen sc1\n" \
  "v_mfma_f32_16x16x32_bf16 v[228:231], v[216:219], v[40:43], v[228:231]\n" \
  "v_mfma_f32_16x16x32_bf16 v[224:227], v[204:207], v[44:47], v[224:227]\n" \
  "v_mfma_f32_16x16x32_bf16 v[228:231], v[220:223], v[44:47], v[228:231]\n" \
  "v_mfma_f32_16x16x32_bf16 v[232:235], v[192:195], v[160:163], 0\n" \
  "v_mfma_f32_16x16x32_bf16 v[236:239], v[208:211], v[160:163], 0\n" \
  "v_mfma_f32_16x16x32_bf16 v[232:235], v[200:203], v[168:171], v[232:235]\n" \
  "v_cvt_pk_bf16_f32 v224, v224, v225\n" \
  "v_cvt_pk_bf16_f32 v226, v226, v227\n" \
  "v_mfma_f32_16x16x32_bf16 v[236:239], v[216:219], v[168:171], v[236:239]\n" \
  "v_cvt_pk_bf16_f32 v225, v228, v229\n" \
  "v_cvt_pk_bf16_f32 v227, v230, v231\n" \
  "s_waitcnt lgkmcnt(1)\n" \
  "v_mfma_f32_16x16x32_bf16 v[232:235], v[196:199], v[240:243], v[232:235]\n" \
  "s_add_u32 %[cnt], %[so], 2*%c[js]+0*%c[ss]\n" \
  "buffer_store_dwordx4 v[224:227], v251, %[rs], %[cnt] offen sc1\n" \
  "v_mfma_f32_16x16x32_bf16 v[236:239], v[212:215], v[240:243], v[236:239]\n" \
  "s_waitcnt lgkmcnt(0)\n" \
  "v_mfma_f32_16x16x32_bf16 v[232:235], v[204:207], v[244:247], v[232:235]\n" \
  "v_mfma_f32_16x16x32_bf16 v[236:239], v[220:223], v[244:247], v[236:239]\n" \
  "ds_read_b128 v[240:243], v249 offset:1024\n" \
  "ds_read_b128 v[244:247], v249 offset:17408\n" \
  "v_mfma_f32_16x16x32_bf16 v[224:227], v[192:195], v[96:99], 0\n" \
  "v_mfma_f32_16x16x32_bf16 v[228:231], v[208:211], v[96:99], 0\n" \
  "v_mfma_f32_16x16x32_bf16 v[224:227], v[196:199], v[100:103], v[224:227]\n" \
  "v_cvt_pk_bf16_f32 v232, v232, v233\n" \
  "v_cvt_pk_bf16_f32 v234, v234, v235\n" \
  "v_mfma_f32_16x16x32_bf16 v[228:231], v[212:215], v[100:103], v[228:231]\n" \
  "v_cvt_pk_bf16_f32 v233, v236, v237\n" \
  "v_cvt_pk_bf16_f32 v235, v238, v239\n" \
  "v_mfma_f32_16x16x32_bf16 v[224:227], v[200:203], v[104:107], v[224:227]\n" \
  "s_add_u32 %[cnt], %[so], 6*%c[js]+0*%c[ss]\n" \
  "buffer_store_dwordx4 v[232:235], v251, %[rs], %[cnt] offen sc1\n" \
  "v_mfma_f32_16x16x32_bf16 v[228:231], v[216:219], v[104:107], v[228:231]\n" \
  "v_mfma_f32_16x16x32_bf16 v[224:227], v[204:207], v[108:111], v[224:227]\n" \
  "v_mfma_f32_16x16x32_bf16 v[228:231], v[220:223], v[108:111], v[228:231]\n" \
  "v_mfma_f32_16x16x32_bf16 v[232:235], v[192:195], v[164:167], 0\n" \
  "v_mfma_f32_16x16x32_bf16 v[236:239], v[208:211], v[164:167], 0\n" \
  "v_mfma_f32_16x16x32_bf16 v[232:235], v[200:203], v[172:175], v[232:235]\n" \
  "v_cvt_pk_bf16_f32 v224, v224, v225\n" \
  "v_cvt_pk_bf16_f32 v226, v226, v227\n" \
  "v_mfma_f32_16x16x32_bf16 v[236:239], v[216:219], v[172:175], v[236:239]\n" \
  "v_cvt_pk_bf16_f32 v225, v228, v229\n" \
  "v_cvt_pk_bf16_f32 v227, v230, v231\n" \
  "s_waitcnt lgkmcnt(1)\n" \
  "v_mfma_f32_16x16x32_bf16 v[232:235], v[196:199], v[240:243], v[232:235]\n" \
  "s_add_u32 %[cnt], %[so], 2*%c[js]+1*%c[ss]\n" \
  "buffer_store_dwordx4 v[224:227], v251, %[rs], %[cnt] offen sc1\n" \
  "v_mfma_f32_16x16x32_bf16 v[236:239], v[212:215], v[240:243], v[236:239]\n" \
  "s_waitcnt lgkmcnt(0)\n" \
  "v_mfma_f32_16x16x32_bf16 v[232:235], v[204:207], v[244:247], v[232:235]\n" \
  "v_mfma_f32_16x16x32_bf16 v[236:239], v[220:223], v[244:247], v[236:239]\n" \
  "ds_read_b128 v[240:243], v249 offset:32768\n" \
  "ds_read_b128 v[244:247], v249 offset:49152\n" \
  "v_mfma_f32_16x16x32_bf16 v[224:227], v[192:195], v[48:51], 0\n" \
  "v_mfma_f32_16x16x32_bf16 v[228:231], v[208:211], v[48:51], 0\n" \
  "v_mfma_f32_16x16x32_bf16 v[224:227], v[196:199], v[52:55], v[224:227]\n" \
  "v_cvt_pk_bf16_f32 v232, v232, v233\n" \
  "v_cvt_pk_bf16_f32 v234, v234, v235\n" \
  "v_mfma_f32_16x16x32_bf16 v[228:231], v[212:215], v[52:55], v[228:231]\n" \
  "v_cvt_pk_bf16_f32 v233, v236, v237\n" \
  "v_cvt_pk_bf16_f32 v235, v238, v239\n" \
  "v_mfma_f32_16x16x32_bf16 v[224:227], v[200:203], v[56:59], v[224:227]\n" \
  "s_add_u32 %[cnt], %[so], 6*%c[js]+1*%c[ss]\n" \
  "buffer_store_dwordx4 v[232:235], v251, %[rs], %[cnt] offen sc1\n" \
  "v_mfma_f32_16x16x32_bf16 v[228:231], v[216:219], v[56:59], v[228:231]\n" \
  "v_mfma_f32_16x16x32_bf16 v[224:227], v[204:207], v[60:63], v[224:227]\n" \
  "v_mfma_f32_16x16x32_bf16 v[228:231], v[220:223], v[60:63], v[228:231]\n" \
  "v_mfma_f32_16x16x32_bf16 v[232:235], v[192:195], v[176:179], 0\n" \
  "v_mfma_f32_16x16x32_bf16 v[236:239], v[208:211], v[176:179], 0\n" \
  "v_mfma_f32_16x16x32_bf16 v[232:235], v[200:203], v[184:187], v[232:235]\n" \
  "v_cvt_pk_bf16_f32 v224, v224, v225\n" \
  "v_cvt_pk_bf16_f32 v226, v226, v227\n" \
  "v_mfma_f32_16x16x32_bf16 v[236:239], v[216:219], v[184:187], v[236:239]\n" \
  "v_cvt_pk_bf16_f32 v225, v228, v229\n" \
  "v_cvt_pk_bf16_f32 v227, v230, v231\n" \
  "s_waitcnt lgkmcnt(1)\n" \
  "v_mfma_f32_16x16x32_bf16 v[232:235], v[196:199], v[240:243], v[232:235]\n" \
  "s_add_u32 %[cnt], %[so], 3*%c[js]+0*%c[ss]\n" \
  "buffer_store_dwordx4 v[224:227], v251, %[rs], %[cnt] offen sc1\n" \
  "v_mfma_f32_16x16x32_bf16 v[236:239], v[212:215], v[240:243], v[236:239]\n" \
  "s_waitcnt lgkmcnt(0)\n" \
  "v_mfma_f32_16x16x32_bf16 v[232:235], v[204:207], v[244:247], v[232:235]\n" \
  "v_mfma_f32_16x16x32_bf16 v[236:239], v[220:223], v[244:247], v[236:239]\n" \
  "ds_read_b128 v[240:243], v249 offset:33792\n" \
  "ds_read_b128 v[244:247], v249 offset:50176\n" \
  "v_mfma_f32_16x16x32_bf16 v[224:227], v[192:195], v[112:115], 0\n" \
  "v_mfma_f32_16x16x32_bf16 v[228:231], v[208:211], v[112:115], 0\n" \
  "v_mfma_f32_16x16x32_bf16 v[224:227], v[196:199], v[116:119], v[224:227]\n" \
  "v_cvt_pk_bf16_f32 v232, v232, v233\n" \
  "v_cvt_pk_bf16_f32 v234, v234, v235\n" \
  "v_mfma_f32_16x16x32_bf16 v[228:231], v[212:215], v[116:119], v[228:231]\n" \
  "v_cvt_pk_bf16_f32 v233, v236, v237\n" \
  "v_cvt_pk_bf16_f32 v235, v238, v239\n" \
  "v_mfma_f32_16x16x32_bf16 v[224:227], v[200:203], v[120:123], v[224:227]\n" \
  "s_add_u32 %[cnt], %[so], 7*%c[js]+0*%c[ss]\n" \
  "buffer_store_dwordx4 v[232:235], v251, %[rs], %[cnt] offen sc1\n" \
  "v_mfma_f32_16x16x32_bf16 v[228:231], v[216:219], v[120:123], v[228:231]\n" \
  "v_mfma_f32_16x16x32_bf16 v[224:227], v[204:207], v[124:127], v[224:227]\n" \
  "v_mfma_f32_16x16x32_bf16 v[228:231], v[220:223], v[124:127], v[228:231]\n" \
  "v_mfma_f32_16x16x32_bf16 v[232:235], v[192:195], v[180:183], 0\n" \
  "v_mfma_f32_16x16x32_bf16 v[236:239], v[208:211], v[180:183], 0\n" \
  "v_mfma_f32_16x16x32_bf16 v[232:235], v[200:203], v[188:191], v[232:235]\n" \
  "v_cvt_pk_bf16_f32 v224, v224, v225\n" \
  "v_cvt_pk_bf16_f32 v226, v226, v227\n" \
  "v_mfma_f32_16x16x32_bf16 v[236:239], v[216:219], v[188:191], v[236:239]\n" \
  "v_cvt_pk_bf16_f32 v225, v228, v229\n" \
  "v_cvt_pk_bf16_f32 v227, v230, v231\n" \
  "s_waitcnt lgkmcnt(1)\n" \
  "v_mfma_f32_16x16x32_bf16 v[232:235], v[196:199], v[240:243], v[232:235]\n" \
  "s_add_u32 %[cnt], %[so], 3*%c[js]+1*%c[ss]\n" \
  "buffer_store_dwordx4 v[224:227], v251, %[rs], %[cnt] offen sc1\n" \
  "v_mfma_f32_16x16x32_bf16 v[236:239], v[212:215], v[240:243], v[236:239]\n" \
  "s_waitcnt lgkmcnt(0)\n" \
  "v_mfma_f32_16x16x32_bf16 v[232:235], v[204:207], v[244:247], v[232:235]\n" \
  "v_mfma_f32_16x16x32_bf16 v[236:239], v[220:223], v[244:247], v[236:239]\n" \
  "s_nop 4\n" \
  "v_cvt_pk_bf16_f32 v232, v232, v233\n" \
  "v_cvt_pk_bf16_f32 v234, v234, v235\n" \
  "s_nop 1\n" \
  "v_cvt_pk_bf16_f32 v233, v236, v237\n" \
  "v_cvt_pk_bf16_f32 v235, v238, v239\n" \
  "s_add_u32 %[cnt], %[so], 7*%c[js]+1*%c[ss]\n" \
  "buffer_store_dwordx4 v[232:235], v251, %[rs], %[cnt] offen sc1\n" \
  "s_nop 1\n"
#define XBF_NB 5
#define XBS_NB 2
#define F_ORD "01ab2cd3"
#define VO_CLOB "v192", "v193", "v194", "v195", "v196", "v197", "v198", "v199", "v200", "v201", "v202", "v203", "v204", "v205", "v206", "v207", "v208", "v209", "v210", "v211", "v212", "v213", "v214", "v215", "v216", "v217", "v218", "v219", "v220", "v221", "v222", "v223", "v224", "v225", "v226", "v227", "v228", "v229", "v230", "v231", "v232", "v233", "v234", "v235", "v236", "v237", "v238", "v239"
#define QK_CLOB "v192", "v193", "v194", "v195", "v196", "v197", "v198", "v199", "v200", "v201", "v202", "v203", "v204", "v205", "v206", "v207", "v208", "v209", "v210", "v211", "v212", "v213", "v214", "v215", "v216", "v217", "v218", "v219", "v220", "v221", "v222", "v223", "v224", "v225", "v226", "v227", "v228", "v229", "v230", "v231", "v232", "v233", "v234", "v235", "v236", "v237", "v238", "v239"
#define QX_CLOB "v216", "v217", "v218", "v219", "v220", "v221", "v222", "v223", "v224", "v225", "v226", "v227", "v228", "v229", "v230", "v231", "v232", "v233", "v234", "v235", "v236", "v237", "v238", "v239", "v241"
#define ST_CLOB "v192", "v193", "v194", "v195", "v196", "v197", "v198", "v199", "v200", "v201", "v202", "v203", "v204", "v205", "v206", "v207", "v240", "v241", "v242", "v243", "v244", "v245", "v246", "v247", "v253"
#define XB_CLOB "v192", "v193", "v194", "v195", "v196", "v197", "v198", "v199", "v200", "v201", "v202", "v203", "v204", "v205", "v206", "v207", "v208", "v209", "v210", "v211", "v212", "v213", "v214", "v215", "v216", "v217", "v218", "v219", "v220", "v221", "v222", "v223", "v224", "v225", "v226", "v227", "v228", "v229", "v230", "v231", "v232", "v233", "v234", "v235", "v236", "v237", "v238", "v239", "v240", "v241", "v242", "v243", "v244", "v245", "v246", "v247", "v253"
#define OF_CLOB "v32", "v33", "v34", "v35", "v36", "v37", "v38", "v39", "v40", "v41", "v42", "v43", "v44", "v45", "v46", "v47", "v48", "v49", "v50", "v51", "v52", "v53", "v54", "v55", "v56", "v57", "v58", "v59", "v60", "v61", "v62", "v63", "v64", "v65", "v66", "v67", "v68", "v69", "v70", "v71", "v72", "v73", "v74", "v75", "v76", "v77", "v78", "v79", "v80", "v81", "v82", "v83", "v84", "v85", "v86", "v87", "v88", "v89", "v90", "v91", "v92", "v93", "v94", "v95", "v96", "v97", "v98", "v99", "v100", "v101", "v102", "v103", "v104", "v105", "v106", "v107", "v108", "v109", "v110", "v111"
#define OE_CLOB "v192", "v193", "v194", "v195", "v196", "v197", "v198", "v199", "v200", "v201", "v202", "v203", "v204", "v205", "v206", "v207", "v208", "v209", "v210", "v211", "v212", "v213", "v214", "v215", "v216", "v217", "v218", "v219", "v220", "v221", "v222", "v223", "v224", "v225", "v226", "v227", "v228", "v229", "v230", "v231", "v232", "v233", "v234", "v235", "v236", "v237", "v238", "v239", "v240"
constexpr int L_W = 32768;
constexpr int L_HI = 16384;
#define MARG 110
#define ED0 720
#define EA0 1290
#define SDK 515
#define TOL 145
#define XLATE 130
#define TFR 900
#define QT 0
#define HSU 8
#define WNT 3
#define KROT 7
#define KSL2 0
#define KSL 5
#define NCY 1860
#define NCYV NCY
#define NRY 24
#define NRX 30
#define NF1O 2
#define NPUB 2
#define NXF 4
#define RSL 2
#define PFL 4
#define EVT2R
__device__ __forceinline__ int slow_head(const u16* wm) { return (int)(((3u - (unsigned)((uintptr_t)wm >> 7)) & 7u) >> 1); }
template <int role>
__device__ __forceinline__ void wroles(const int h, const int par, const int sl, const int xg, const int w, const u64 nonce, const u16* __restrict__ x,
                                       const u16* __restrict__ Wq, const u16* __restrict__ Wk, const u16* __restrict__ Wv,
                                       const u16* __restrict__ Wo, char* ws) {
  LAS char* const L = (LAS char*)g_lds;
  gu32* tmo = (gu32*)(ws + OFF_TMO);
  const __amdgpu_buffer_rsrc_t rs = __builtin_amdgcn_make_buffer_rsrc(ws, 0, (int)WS_USED, 0x00020000);
  constexpr bool vo = role & 1;
  v8u wa[8], wb[16];
  constexpr bool fuse = role >= 2;
  constexpr int RG = fuse ? 4 : 5, RB = fuse ? 32768 : 0;
  f32x4 fac[2][2];
  v2u nin = {0u, 0u}, hsv;
  unsigned arm = 0;
  v8u t13;
  PLDECL;
  const int krot = h & KROT;
  {
    const int lane = olane();
    const int l15 = lane & 15, g = lane >> 4, trq = l15 >> 2, trp = lane & 3;
    const char* bl = (const char*)((vo ? Wv : Wq) + h * DH);
    const char* br = (const char*)(vo ? Wo + (size_t)h * DH * D : Wk + h * DH);
    const int rot = role == 1 ? (h >> 4) * 4 : 0;
    int vl[2], vr[2];
#pragma unroll
    for (int q = 0; q < 2; ++q) {
      const int row = 4 * (2 * w + q) + g;
      const int cb = ((((l15 >> 1) ^ ((row & 3) | ((row >> 1) & 4))) << 5) | ((l15 & 1) << 4));
      const int i = row & 7;
      const int prow = (row & 32) + ((i & 4) << 2) + ((row >> 1) & 12) + (i & 3);
      vl[q] = row * 8192 + cb;
      vr[q] = vo ? (role == 1 ? row : prow) * 2048 + cb : vl[q];
    }
#define W_ISSUE(u)                                                                                   \
  {                                                                                                  \
    const char* pl = bl + (size_t)((u) ^ krot) * 64 * 8192;                                          \
    const char* pr = br + (role == 1 ? (size_t)(((u) >> 1) & 1) * 64 * 2048 + (2 * ((u) >> 2) + ((u) & 1)) * 256          \
                                     : (vo ? (size_t)((u) & 1) * 64 * 2048 + ((u) >> 1) * 256 : (size_t)((u) ^ krot) * 64 * 8192)); \
    _Pragma("unroll") for (int q = 0; q < 2; ++q) {                                                  \
      glds16<((WNT >> (role >> 1)) & 1) != 0>(pl, vl[q], RB + ((u) % RG) * 32768 + (2 * w + q) * 1024);                                \
      glds16<((WNT >> (role >> 1)) & 1) != 0>(pr, vr[q], RB + ((u) % RG) * 32768 + 16384 + (2 * w + q) * 1024);                        \
    }                                                                                                \
  }
    const int ta = role == 1 ? 8 + (w ^ rot) : 2 * w, tb = role == 1 ? 2 * (w & 3) : 2 * w + 1;
    const int sw = trq | ((g & 1) << 2);
    int pa = (ta >> 3) * 16384 + (8 * g + trq) * 256 + (((ta & 7) ^ sw) << 5) + trp * 8;
    int pb = (tb >> 3) * 16384 + (8 * g + trq) * 256 + (((tb & 7) ^ sw) << 5) + trp * 8;
    const int pw = L_W + w * 2048 + lane * 16;
#define W_FRAG(dst, p, u)                                                                            \
  {                                                                                                  \
    int p_ = p + RB + ((u) % RG) * 32768;                                                             \
    asm volatile("" : "+v"(p_));                                                                     \
    const v2u t0 = trd(L + p_), t1 = trd(L + p_ + 1024), t2 = trd(L + p_ + 8192), t3 = trd(L + p_ + 9216); \
    dst = v8u{t0[0], t0[1], t1[0], t1[1], t2[0], t2[1], t3[0], t3[1]};                               \
  }
#define W_PUT(c)                                           \
  {                                                        \
    int p_ = pw + ((c) - 8) * 16384;                       \
    asm volatile("" : "+v"(p_));                           \
    *(LAS v4u*)(L + p_) = lo4(tmp[(c) - 8]);               \
    *(LAS v4u*)(L + p_ + 1024) = hi4(tmp[(c) - 8]);        \
  }
    PL(0);
    const int fxv = (16 * (w & 1) + (lane >> 2)) * 2048 + (((lane ^ -g) & 3) << 4) + ((w >> 1) & 1) * 64;
    const int fxr = l15 * 64 + (((g ^ -trq) & 3) << 4);
#define X_ISSUE(j) glds16((const char*)x + par * 65536 + ((2 * (j) + (w >> 2)) ^ krot) * 128, fxv, (((j) + 1) & 3) * 8192 + w * 1024)
#define HWU 99
#define W_HELP(u)                                                                                                     \
  if (fuse && w >= 5) {                                                                                               \
    const int h0 = slow_head(Wq), rk = (h - h0 - 1) & 3;                                                              \
    const int q = (((role & 1) * 4 + par * 2 + ((h >> 2) & 1)) * 3 + rk) * 3 + w - 5, m = q / 24;                     \
    const u16* wm = m == 2 ? Wv : (m ? Wk : Wq);                                                                      \
    const int hs = (h & 24) + slow_head(wm) + 4 * ((q / 12) & 1);                                                     \
    if (rk < 3)                                                                                                       \
      touch4((const char*)wm + hs * 256 + (size_t)((15 - (q % 12 >> 1)) ^ (hs & KROT)) * 524288,                      \
             ((q & 1) * 32 + (lane >> 1)) * 8192 + (lane & 1) * 128, RB + ((u) % RG) * 32768 + 2 * w * 1024);          \
  }
    if (HWU == -1) W_HELP(0)
    W_ISSUE(0);
    if (fuse) {
#pragma unroll
      for (int j = 0; j < 4; ++j) X_ISSUE(j);
    }
#pragma unroll
    for (int u = 1; u < RG; ++u) W_ISSUE(u);
    PL(1);
    v8u tmp[8];
#pragma unroll
    for (int u = 0; u < 16; ++u) {
      {
        int young = 4 * (u + RG - 1 < 16 ? RG - 1 : 15 - u);
        if (fuse)
          for (int i = u - RG; i < u; ++i) young += i >= 0 && i < 8 && (i & 1);
        if (u > HSU && u < HSU + RG) young += 1;
        if (role < 2 && u > 13) young += 3;
        if (VFAR && role == 3 && u > 12 && u < 15) young += 2;
        asm volatile("s_waitcnt vmcnt(%0)" ::"n"(young) : "memory");
      }
      RAW_BARRIER();
      if ((u & 3) == 0 || u == 15) PL(2 + (u + 1) / 4);
      if constexpr (role == 1) {
#define WO_(n) ((n) < 8 ? wa[(n) & 7] : ((n) < 10 ? wb[14 + ((n) & 1)] : tmp[(n) >= 10 ? (n) - 10 : 0]))
#define WV_(n) ((n) < 14 ? wb[(n) < 14 ? (n) : 0] : tmp[6 + ((n) & 1)])
        if (u == 12) { W_PUT(8); W_PUT(9); }
        W_FRAG(WO_(u), pa, u)
        if ((w >> 2) == (u >> 3)) {
          W_FRAG(WV_(2 * (u & 7)), pb, u)
          W_FRAG(WV_(2 * (u & 7) + 1), (pb ^ 32), u)
        }
        if (u & 1) {
          v8u& f0 = WO_(u & ~1);
          v8u& f1 = WO_(u);
          const v8u n0 = {f0[0], f1[0], f0[1], f1[1], f0[2], f1[2], f0[3], f1[3]};
          const v8u n1 = {f0[4], f1[4], f0[5], f1[5], f0[6], f1[6], f0[7], f1[7]};
          f0 = n0;
          f1 = n1;
          asm volatile("" : "+v"(f0), "+v"(f1));
        }
        if (u == 13) { W_PUT(10); W_PUT(11); }
        if (u == 15) { W_PUT(12); W_PUT(13); W_PUT(14); W_PUT(15); }
      } else if constexpr (role == 0) {
        if (u == 12) { W_PUT(8); W_PUT(9); }
        if (u == 13) { W_PUT(10); W_PUT(11); }
        if (u == 14) { W_PUT(12); W_PUT(13); }
        if (u == 15) { W_PUT(14); }
        if (u < 8) W_FRAG(wa[u < 8 ? u : 0], pa, u) else W_FRAG(tmp[u >= 8 ? u - 8 : 0], pa, u)
        W_FRAG(wb[u], pb, u)
        if (u == 15) W_PUT(15);
      } else {
        if (u == 15 && role == 2 && w == 0) hs_wait(rs, nonce, hsv);
        if (u >= 13) W_PUT(u >= 13 ? 2 * u - 18 : 8);
        if (u == 13 || u == 14) W_PUT(u >= 13 ? 2 * u - 17 : 8);
        v8u fa, fb;
        W_FRAG(fa, pa, u)
        W_FRAG(fb, pb, u)
        if (u < 8) {
          wa[u & 7] = fa;
          wb[u] = fb;
        } else {
          wb[u] = cat8(lo4(fa), lo4(fb));
          tmp[u & 7] = cat8(hi4(fa), hi4(fb));
        }
        if (role == 2 || w < 4) {
#pragma unroll
          for (int hf = 0; hf < 2; ++hf)
#pragma unroll
            for (int rt = 0; rt < 2; ++rt) {
              const v4u xq = *(const LAS v4u*)(L + fxr + ((u + 2) & 7) * 4096 + hf * 2048 + rt * 1024);
              fac[0][rt] = mma(hf ? hi4(fa) : lo4(fa), xq, u + hf == 0 ? f32x4{0.f, 0.f, 0.f, 0.f} : fac[0][rt]);
              fac[1][rt] = mma(hf ? hi4(fb) : lo4(fb), xq, u + hf == 0 ? f32x4{0.f, 0.f, 0.f, 0.f} : fac[1][rt]);
            }
          asm volatile("" : "+v"(fac[0][0]), "+v"(fac[0][1]), "+v"(fac[1][0]), "+v"(fac[1][1]));
          if (u == 15) {
            PL(16);
            const int l16 = olane() * 16;
#pragma unroll
            for (int rt = 0; rt < 2; ++rt) {
              const v4u o = {pk2(fac[0][rt][0], fac[0][rt][1]), pk2(fac[0][rt][2], fac[0][rt][3]), pk2(fac[1][rt][0], fac[1][rt][1]),
                             pk2(fac[1][rt][2], fac[1][rt][3])};
              if (role == 3) *(LAS v4u*)(L + 24576 + rt * 4096 + w * 1024 + l16) = o;
              else st128(o, rs, l16, qk_addr(w >> 2, par, h) + rt * QK_RT + (w & 3) * 1024);
            }
            if (role == 2) {
              W_PUT(13);
              VMCNT(0);
            }
          }
        } else if (u == 15) {
          W_PUT(13);
          if (w == 7) {
            const bool ok = hs_wait(rs, nonce, hsv);
            if (VFAR) {
              if (__builtin_expect(!ok || !__all((nin[0] != 0xffffffffu) & (nin[1] != 0xffffffffu)), 0)) nin = numa_load(rs, tmo);
              *(volatile LAS int*)(L + L_W + 5 * 16384) = numa_far(nin);
            }
          }
        }
      }
      if (u == HSU) hsv = __builtin_amdgcn_raw_buffer_load_b64(rs, w == (role == 3 ? 7 : 0) ? 0 : OOB, OFF_ACK + (int)BIDX * 8, AUX_SC1);
      if (role < 2 && u == 13) {
        const int a = w == 0 ? olane() * 4 : OOB;
        arm = __builtin_amdgcn_raw_buffer_load_b32(rs, a & OOB, CNT_ARM, AUX_SC1);
        nin[0] = __builtin_amdgcn_raw_buffer_load_b32(rs, a, (int)OFF_CAL, AUX_SC1);
        nin[1] = __builtin_amdgcn_raw_buffer_load_b32(rs, (a & OOB) | (((a >> 5) & (NXC - 1)) * 4), (int)OFF_XID, AUX_SC1);
      }
      if (VFAR && role == 3 && u == 12) {
        const int a = w == 7 ? olane() * 4 : OOB;
        nin[0] = __builtin_amdgcn_raw_buffer_load_b32(rs, a, (int)OFF_CAL, AUX_SC1);
        nin[1] = __builtin_amdgcn_raw_buffer_load_b32(rs, (a & OOB) | (((a >> 5) & (NXC - 1)) * 4), (int)OFF_XID, AUX_SC1);
      }
      if (u + RG < 16) {
        RAW_BARRIER();
        if (HWU == u) W_HELP(u + RG)
        W_ISSUE(u + RG);
        if (fuse && u < 8 && (u & 1)) X_ISSUE(4 + (u >> 1));
      }
    }
    PL(17);
    RAW_BARRIER();
    PL(18);
    if (VFAR && role == 3 && w >= 4) arm = __builtin_amdgcn_readfirstlane(*(volatile LAS int*)(L + L_W + 5 * 16384));
    if (role == 2 && w == 0) __builtin_amdgcn_raw_buffer_store_b32(0u, rs, olane() << 28, fqk(h, par), AUX_SC1);
    if constexpr (fuse) {
      if (role == 2 || w < 4) {
        constexpr int B0 = role == 3 ? 6 : 8, PER = role == 3 ? 2 : 4, SL = role == 3 ? 12288 : 16384;
        const int ln = olane();
        const int xv = (16 * (w & 1) + (ln >> 2)) * 2048 + (((ln ^ -(ln >> 4)) & 3) << 4) + ((w >> 1) & 1) * 1024 + (role == 3 ? 0 : (w >> 2) * 64);
#pragma unroll
        for (int k = 0; k < 2; ++k)
#pragma unroll
          for (int j = 0; j < B0 / PER; ++j)
            glds16((const char*)x + (2 + par) * 65536 + (((k * B0 + j * PER) / 2 * 64) ^ (krot * 128)), xv, k * SL + j * PER * 2048 + w * 1024);
        PL(19);
        if (role == 3) t13 = tmp[5];
      }
      W_PUT(14);
      W_PUT(15);
    }
  }
  const int lane = olane();
  const int l15 = lane & 15, g = lane >> 4, trq = l15 >> 2, trp = lane & 3;
  int pw0 = L_W + w * 2048 + lane * 16, pw1 = pw0 + 65536;
  asm volatile("" : "+v"(pw0), "+v"(pw1));
#define WA_P(c) (L + ((c) < 12 ? pw0 : pw1) + ((c) & 3) * 16384)
#define WA_LDS(c) cat8(*(const LAS v4u*)WA_P(c), *(const LAS v4u*)(WA_P(c) + 1024))
  const int r_a = (int)(((uintptr_t)ws >> 19) & 7);
  const int r_s = r_a + (int)(OFF_STEP / UNIT);

  typedef int v4i __attribute__((ext_vector_type(4)));
#define WPIN "{v[0:31]}"(W0), "{v[32:63]}"(W1), "{v[64:95]}"(W2), "{v[96:127]}"(W3), "{v[128:159]}"(W4), "{v[160:191]}"(W5)
  const v4u rsv = {(unsigned)(uintptr_t)ws, (unsigned)((uintptr_t)ws >> 32) & 0xffffu, (unsigned)WS_USED, 0x00020000u};
  if constexpr (role >= 2) {
    constexpr bool isx = role == 3;
    const v32u W0 = cat32(wa[0], wa[1], wa[2], wa[3]), W1 = cat32(wa[4], wa[5], wa[6], wa[7]), W2 = cat32(wb[0], wb[1], wb[2], wb[3]);
    const v32u W3 = cat32(wb[4], wb[5], wb[6], wb[7]), W4 = cat32(wb[8], wb[9], wb[10], wb[11]), W5 = cat32(wb[12], wb[13], wb[14], wb[15]);
    const v4u rxv = {(unsigned)(uintptr_t)x, (unsigned)((uintptr_t)x >> 32) & 0xffffu, (unsigned)(S * D * 2), 0x00020000u};
    v4i adr;
    adr[3] = lane * 16;
    adr[2] = pw0;
    adr[1] = pw1;
    adr[0] = (16 * (w & 1) + (lane >> 2)) * 2048 + (((lane ^ -g) & 3) << 4) + ((w >> 1) & 1) * 1024 + (isx ? 0 : (w >> 2) * 64);
    const int xr = l15 * 64 + (((g ^ -trq) & 3) << 4);
#define FOF(sg) (isx ? fvw(w & 3, h, sg) : fqk(h, sg))
    const int nrel = isx ? TN(8, NRX) : TN(7, NRY);
    const int nit = par ? nrel : NSEG - nrel;
#define SEGOF(i) ((i) < nrel ? 2 * (i) + par : (i) + nrel)
#define ST_FLAG(sg)                                                                                                   \
  {                                                                                                                   \
    VMCNT(0);                                                                                                         \
    RAW_BARRIER();                                                                                                    \
    if (w == 0) __builtin_amdgcn_raw_buffer_store_b32(0u, rs, olane() << 28, FOF(sg), AUX_SC1);               \
  }
#define XB_OPS(sg, sp)                                                                                                  \
  : [cnt] "=&s"(cnt) EVOUT                                                                                              \
  : WPIN, "{v[248:251]}"(adr), [rs] "s"(rsv), [so] "s"(vw_addr(vfr, 2 * (w - 4), sg, h >> 2) + (h & 3) * 1024),               \
    [js] "i"((VFAR ? 64 : GST) * 262144), [ss] "i"(VFAR ? 65536 : 32768), [fo] "s"(FOF(sp))                                                 \
  : XB_CLOB, "scc", "memory"
#define XB_FLAG(sg) __builtin_amdgcn_raw_buffer_store_b8(0, rs, olane() << 28, FOF(sg), AUX_SC1)
    PL(7);
    if (isx && w >= 4) {
      const int vfr = (__builtin_amdgcn_readfirstlane(arm) ^ (VFAR == 2 ? 8192 : 0)) | (r_a << 19);
      {
        EVDECL;
        unsigned cnt;
        PL(20);
        asm volatile(ST_XBS XB_OPS(par, par));
        PL(21);
      }
      int it = 1;
#pragma unroll 1
      for (;;) {
        VMCNT(0);
        XB_FLAG(SEGOF(it - 1));
        if (it == 1) { PL(9); PLDUMP(4, 0x300200); }
        if (it == 2) PTW(14, 4);
        if (it == 3) PTW(15, 4);
        for (int k = it == 1 ? XBS_NB - 1 : XBF_NB; k < 6; ++k) BARE_BARRIER();
        if (++it > NXF) break;
        EVDECL;
        unsigned cnt;
        asm volatile(ST_XBF XB_OPS(SEGOF(it - 1), par));
        EVT2R
      }
#pragma unroll 1
      for (; it < nit; ++it) {
        EVDECL;
        unsigned cnt;
        asm volatile(ST_XB XB_OPS(SEGOF(it - 1), SEGOF(it - 2)));
        EVT2R
      }
      BARE_BARRIER();
      {
        EVDECL;
        unsigned cnt;
        asm volatile(ST_XB0 XB_OPS(SEGOF(nit - 1), par));
      }
      BARE_BARRIER();
      VMCNT(0);
      XB_FLAG(SEGOF(nit - 2));
      XB_FLAG(SEGOF(nit - 1));
      return;
    }
    if (w == 1) cnt_inc(ws, CNT_HS);
    VMCNT(0);
    RAW_BARRIER();
    if (isx) {
      *(LAS v4u*)(L + pw1 + 16384) = lo4(t13);
      *(LAS v4u*)(L + pw1 + 17408) = hi4(t13);
    }
    PL(10);
    PLDUMP(0, 0xf05ff);
    {
      v32u xf;
#pragma unroll
      for (int q = 0; q < 8; ++q) {
        const v4u t = *(const LAS v4u*)(L + xr + q * 1024);
#pragma unroll
        for (int j = 0; j < 4; ++j) xf[4 * q + j] = t[j];
      }
#pragma unroll 1
      for (int it = 1; it < nit; ++it) {
        EVDECL;
        const int seg = SEGOF(it);
        unsigned cnt, keep;
#define ST_OPS                                                                                                                 \
  : [cnt] "=&s"(cnt), [keep] "=&s"(keep), "+{v[208:239]}"(xf) EVOUT                                                            \
  : WPIN, "{v[248:251]}"(adr), "{v252}"(xr), [rs] "s"(rsv), [rx] "s"(rxv), [sc] "s"(seg * 65536 + krot * 128),                    \
    [sn] "s"(SEGOF(min(it + 1, nit - 1)) * 65536 + krot * 128),                                                                  \
    [mw] "s"(w * 1024), [so] "s"(isx ? w * 1024 : qk_addr(w >> 2, seg, h) + (w & 3) * 1024), [rt] "i"(QK_RT), \
    [fo] "s"(FOF(SEGOF(it - 1))), [fm] "s"(w == 0 ? 0 : 1 << 30), [tm] "s"(w == 3 ? 0 : 1 << 30),            \
    [tc] "s"(min(SEGOF(it + 2), NSEG - 1) * 65536 + sl * 8192)                                                                   \
  : ST_CLOB, "scc", "memory"
        if constexpr (isx) asm volatile(ST_XA ST_OPS);
        else asm volatile(ST_Y ST_OPS);
        EVT2R
        if (it < 4) PTW(10 + it, 2);
        if (!isx && it < NPUB) ST_FLAG(seg)
      }
    }
    if (isx) {
      RAW_BARRIER();
      BARE_BARRIER();
    } else ST_FLAG(SEGOF(nit - 1))
    return;
  }

#define KF0 4
  volatile LAS unsigned* sword = (volatile LAS unsigned*)(L + L_HI + 6408);
  if (w == 0) {
    const bool ok = hs_wait(rs, nonce, hsv);
    cnt_inc(ws, CNT_HS);
    if (__builtin_expect(!ok || !__all((arm + 1u >= 64u) & (nin[0] != 0xffffffffu) & (nin[1] != 0xffffffffu)), 0)) {
      arm_wait(rs, tmo);
      nin = numa_load(rs, tmo);
    }
    sword[0] = numa_perm(nin, (LAS unsigned*)(L + L_HI + 6656));
    sword[1] = 0u;
  }
  Clk ck = {0u, 0u, 92500u, 46432u};
  PL(7);
  RAW_BARRIER();
  PL(8);
  PLDUMP(0, 0xf01ff);
  const unsigned perm = __builtin_amdgcn_readfirstlane(sword[0]);
  const int cown = cls_of(perm, xg);
  const int l16 = lane * 16;
  const int sidx = ((l15 & 2) ? 0xeeee : 0x4444) | ((l15 & 8) ? 0xeeee0000 : 0x44440000);
  LAS char* const sm = L + l16;
  constexpr int TWV = 5;
  typedef int v8i __attribute__((ext_vector_type(8)));
  const int mg_ = NS2F(TN(0, MARG)), tol_ = NS2F(TN(13, TOL)), xl_ = NS2F(TN(14, XLATE));
  Pace pc = {0u, NS2F(ED0), NS2F(EA0), 0};
  unsigned tg = 0, tw = 0;
#define MEM_OFF(c) (R_MEM + ((c) >> 2) * 32768 + ((c) & 3) * 1024)
#define TLB_TOUCH                    \
  if (((r_s + s + 2) & 3) == 0)      \
    tch = __builtin_amdgcn_raw_buffer_load_b32(rs, 0, so + 2 * (int)UNIT + R_DUM + (role * 32 + h) * 128, AUX_SC1);
#define TQOUT
#define TQACC
#define ST_ADR (lst * 32 + 32768 + 2048)
#define TIMING_WAVE                                                                                  \
  {                                                                                                  \
    if (__builtin_expect(anysent(st[0]), 0)) _Pragma("clang loop unroll(disable)") for (unsigned spins = 0;;) { \
      if (spins) __builtin_amdgcn_s_sleep(4);                                                        \
      if (spin_fail(spins, tmo, 0x30009)) break;                                                     \
      const int lst = olane();                                                                       \
      OPQ(va, ST_ADR - 2048);                                                                        \
      st = __builtin_amdgcn_raw_buffer_load_b64(rs, va, so + R_ST + ((cown ^ xr) << 12), AUX_SC1);   \
      if (!anysent(st[0])) break;                                                                       \
    }                                                                                                \
    TQACC                                                                                            \
    const int dm = stamp_max(ck, st[0]);                                                             \
    const unsigned rf = (ck.r << 4) + dm;                                                            \
    pace_step(pc, s, rf, st[1], mg_, tol_, xl_);                                                     \
    EV(6);                                                                                           \
    EVDUMP(s);                                                                                       \
    EVCLK(s, ck);                                                                                    \
  }
#define TIMING_GO                                                                                    \
  if (w == TWV && s > 0) {                                                                           \
    wait_cyc(clk_cyc(ck, (int)(pc.rf - (ck.r << 4)) + pc.dq) - TFR);                                 \
    clk_update(ck, s == 1);                                                \
    tg = clk_cyc(ck, (int)(pc.rf - (ck.r << 4)) + pc.dq);                                            \
    tw = (unsigned)max(CMAX - (int)(tg - ck.m), 0);                                                  \
  }

  if constexpr (role == 0) {
    const v32u W0 = cat32(wa[0], wa[1], wa[2], wa[3]), W1 = cat32(wa[4], wa[5], wa[6], wa[7]), W2 = cat32(wb[0], wb[1], wb[2], wb[3]);
    const v32u W3 = cat32(wb[4], wb[5], wb[6], wb[7]), W4 = cat32(wb[8], wb[9], wb[10], wb[11]), W5 = cat32(wb[12], wb[13], wb[14], wb[15]);
    v8i adr;
    adr[7] = l16;
    adr[6] = l16 + w * 1024;
    adr[5] = pw0;
    adr[4] = sidx;
    adr[3] = L_HI + (w >> 2) * 3072 + (32 * (w & 3) + l15) * 24 + 4 * g;
    adr[2] = (4 * g + trq) * 24 + (trp & 1) * 8;
#define CORD(i) ((((i) & 1) ? (i) >> 1 : 8 + ((i) >> 1)) ^ krot)
    const int mo0 = MEM_OFF(CORD(w));
#define K_STAGE(sn)                                                                                                   \
  {                                                                                                                   \
    v4u kn[4];                                                                                                        \
    _Pragma("clang loop unroll(disable)") for (unsigned spins = 0; krdy <= (sn);) {    \
      OPQ(vfl, fqk(h, min((sn) + (olane() & 15), NSEG - 1)));                                           \
      const u64 miss = __builtin_amdgcn_ballot_w64(__builtin_amdgcn_raw_buffer_load_b32(rs, vfl, 0, AUX_SC1) != 0u);  \
      krdy = (sn) + (miss ? __builtin_ctzll(miss) : 16);                                                              \
      if (krdy > (sn)) break;                                                                                         \
      __builtin_amdgcn_s_sleep(8);                                                                                    \
      if (spin_fail(spins, tmo, 0x30000)) break;                                                                      \
    }                                                                                                                 \
    const int lk = olane() * 16;                                                                                      \
    _Pragma("unroll") for (int q = 0; q < 4; ++q) kn[q] = __builtin_amdgcn_raw_buffer_load_b128(                      \
        rs, lk + q * 1024, qk_addr(1, sn, h) + (w & 1) * QK_RT, AUX_SC1);                 \
    _Pragma("unroll") for (int q = 0; q < 4; ++q) *(LAS v4u*)(L + L_HI + 8192 + (w & 1) * 4096 + q * 1024 + lk) = kn[q]; \
  }
    int krdy = 1;
    const bool kst = w >= 4 && ((w ^ TWV) & 2);
    PTW(9, 7);
    if (kst) {
      wait_flag<KF0>(rs, fqk(h, 0), tmo, 0x30001);
      sword[1] = 1u;
      K_STAGE(0)
    }
    PTW(10, 7);
    if (w == TWV) {
      _Pragma("clang loop unroll(disable)") for (unsigned spins = 0; !__builtin_amdgcn_readfirstlane(sword[1]) && spins < kSpinCap; ++spins)
        __builtin_amdgcn_s_sleep(2);
      clk_edge(ck.r, ck.m, 2);
    }
    if (w < 2) {
      const int lane = olane(), l15 = lane & 15, l16 = lane * 16;
      const bool up = lane >= 32;
      adr[1] = R_EX + (h >> 3) * 32768 + (h & 7) * 512 + w * 256 + (lane & 32) * 4 + (lane & 7) * 16 + (lane & 24) / 2;
      const int rq = (lane & 1) + 2 * (lane & 6);
      adr[0] = up ? R_PM + (h >> 3) * 1024 + (rq + (l15 >> 3) * 2 + 16 * ((h & 7) >> 1)) * 16 + (h & 1) * 8 + (lane & 16) / 4
                  : (lane < 8 ? R_SC + SC_POS(h >> 2, lane) + (2 * (h & 1) + ((h >> 1) & 1)) * 4 : OOB);
      const unsigned msk = up ? 0x7fffffffu : ((lane & 1) ? 0x7fff0000u : 0xffffu);
      const int kxo = L_HI + 8192 + w * 4096 + l16;
#pragma unroll 1
      for (int s = 0; s < NSEG; ++s) {
        EVDECL;
        const int so = (int)OFF_STEP + s * (int)UNIT, xr = xreg(r_s + s);
#define CG(r) (so + ((cls_of(perm, (r) ^ (2 * w)) ^ xr) << 12))
        unsigned cnt;
        u64 wd;
        asm volatile(QK_A
                     : [cnt] "=&s"(cnt), [wd] "=&s"(wd) EVOUT
                     : WPIN, "{v[244:251]}"(adr), "{v243}"(pw1), "{v242}"(kxo), "{v240}"(msk), [rs] "s"(rsv),
                       [o0] "s"(so + mo0 + ((cown ^ xr) << 12)), [c0] "s"(CG(0)), [c1] "s"(CG(1)), [c2] "s"(CG(2)), [c3] "s"(CG(3)),
                       [hf] "s"(0xff00ff00ff00ff00ull), [up] "s"(0xffffffff00000000ull)
                     : QK_CLOB, "v241", "vcc", "scc", "memory");
        if (s < 4) PT(11 + s);
        EVDUMP(s);
      }
    } else if (w < 4) {
      const float scale = 0.08838834764831845f, L2E = 1.4426950408889634f;
      adr[0] = adr[1] = 0;
#pragma unroll 1
      for (int s = 0; s < NSEG; ++s) {
        {
          EVDECL;
          const int so = (int)OFF_STEP + s * (int)UNIT, xr = xreg(r_s + s);
          unsigned cnt;
          v16u qv;
          f32x4 sc[3];
          asm volatile(QK_X
                       : [cnt] "=&s"(cnt), "={v[200:215]}"(qv), "={v[192:195]}"(sc[0]), "={v[196:199]}"(sc[1]) EVOUT
                       : WPIN, "{v[244:251]}"(adr), "{v243}"(pw1), [rs] "s"(rsv), [o0] "s"(so + mo0 + ((cown ^ xr) << 12)),
                         [qo] "s"(qk_addr(0, s, h) + (w - 2) * QK_RT)
                       : QX_CLOB, "vcc", "scc", "memory");
          const int ln = olane(), l15 = ln & 15, g = ln >> 4, l16 = ln * 16;
          v4u qx[4];
#pragma unroll
          for (int ks = 0; ks < 4; ++ks) qx[ks] = v4u{qv[4 * ks], qv[4 * ks + 1], qv[4 * ks + 2], qv[4 * ks + 3]};
          LAS char* const q_rd = L + L_HI + adr[2];
#define QFRAG(o, ks) [&] { const v2u tx = trd(q_rd + (o) + (ks) * 768), ty = trd(q_rd + (o) + (ks) * 768 + 384); return v4u{tx[0], tx[1], ty[0], ty[1]}; }()
          sc[2] = f32x4{0.f, 0.f, 0.f, 0.f};
#pragma unroll
          for (int ks = 0; ks < 4; ++ks) sc[2] = mma(QFRAG(3072, ks), qx[ks], sc[2]);
          EV(4);
          const int tok = 16 * (w - 2) + l15 - 4 * g;
#pragma unroll
          for (int t = 0; t < 3; ++t)
#pragma unroll
            for (int j = 0; j < 4; ++j) {
              float v = rbf(mul1(rbf(sc[t][j]), scale));
              if (t < 2 && 16 * t + j > tok) v = -1000000000.0f;
              sc[t][j] = v;
            }
          float mx;
          asm("v_max3_f32 %0, %1, %2, %3\n\tv_max3_f32 %0, %0, %4, %5\n\tv_max3_f32 %0, %0, %6, %7\n\tv_max3_f32 %0, %0, %8, %9\n\t"
              "v_max3_f32 %0, %0, %10, %11\n\tv_max_f32 %0, %0, %12\n\ts_nop 1"
              : "=&v"(mx)
              : "v"(sc[0][0]), "v"(sc[0][1]), "v"(sc[0][2]), "v"(sc[0][3]), "v"(sc[1][0]), "v"(sc[1][1]), "v"(sc[1][2]), "v"(sc[1][3]),
                "v"(sc[2][0]), "v"(sc[2][1]), "v"(sc[2][2]), "v"(sc[2][3]));
          mx = xmax16_32(mx);
          const float nmx = -mx * L2E;
#pragma unroll
          for (int t = 0; t < 3; ++t)
#pragma unroll
            for (int j = 0; j < 4; ++j) sc[t][j] = __builtin_amdgcn_exp2f(__builtin_fmaf(sc[t][j], L2E, nmx));
          const f32x4 s4 = sc[0] + sc[1] + 0.5f * sc[2];
          const float inv = __builtin_amdgcn_rcpf(xsum16_32((s4[0] + s4[1]) + (s4[2] + s4[3])));
#pragma unroll
          for (int t = 0; t < 3; ++t) sc[t] *= inv;
          const v4u o = {pk2(sc[0][0], sc[0][1]), pk2(sc[1][0], sc[1][1]), pk2(sc[0][2], sc[0][3]), pk2(sc[1][2], sc[1][3])};
          const v2u o2 = {pk2(sc[2][0], sc[2][1]), pk2(sc[2][2], sc[2][3])};
          const int pb = so + R_PX + (w - 2) * 40960;
          st128(o, rs, l16, pb + h * 1024);
          __builtin_amdgcn_raw_buffer_store_b64(o2, rs, g >= 2 ? l15 * 16 + (g & 1) * 8 : OOB,
                                                pb + (32 + (h >> 2)) * 1024 + (h & 3) * 256, AUX_SC1);
          EV(6);
          EVDUMP(s);
        }
      }
    } else {
      const int mo1 = MEM_OFF(CORD(w + 4)), mo2 = MEM_OFF(CORD(w + 8));
      adr[0] = 0;
#pragma unroll 1
      for (int s = 0; s < NSEG; ++s) {
        EVDECL;
        const int so = (int)OFF_STEP + s * (int)UNIT, xr = xreg(r_s + s), sc_ = so + ((cown ^ xr) << 12);
        const int lst = olane();
        int a1 = ((cls_of(perm, lst & 3) ^ xr) << 12) | ((lst & 4) << 9) | ((lst & 8) << 12) | (((lst >> 4) | (w != TWV)) << 28);
        const bool fq = kst && krdy <= s + 2;
        v2u st = {(unsigned)(fq ? fqk(h, min(s + 1 + (lst & 15), NSEG - 1)) : ST_ADR | ((w != TWV) << 30)), 0u};
        unsigned cnt, tca = (lst & 31) * 128 + (lst >> 5) * QK_RT;
        int o0 = sc_ + mo0, o1 = sc_ + mo1, o2 = sc_ + mo2, sst = so + R_ST + role * 1024 + h * 32, ss = kst ? 0 : sc_ + R_ST;
        int tc = kst ? qk_addr(w & 1, min(s + 1, NSEG - 1), h) : 0;
        asm volatile("" : "+v"(a1), "+{v[240:241]}"(st), "+{v242}"(tca));
        adr[1] = a1;
        asm volatile("" : "+s"(o0), "+s"(o1), "+s"(o2), "+s"(sst), "+s"(ss), "+s"(tc));
        TIMING_GO
        asm volatile(QK_K
                     : [cnt] "=&s"(cnt), "+{v[240:241]}"(st), "+{v242}"(tca) EVOUT TQOUT
                     : WPIN, "{v[244:251]}"(adr), "{v243}"(pw1), [rs] "s"(rsv), [o0] "s"(o0), [o1] "s"(o1), [o2] "s"(o2),
                       [ckm] "s"(ck.m - NCY), [inv] "s"(ck.inv << 16), [r16] "s"(ck.r << 4), [sst] "s"(sst), [tg] "s"(tg), [tw] "s"(tw),
                       [ss] "s"(ss), [tc] "s"(tc)
                     : QK_CLOB, "vcc", "scc", "memory");
        if (s < NSEG - 1 && w == TWV) {
          __builtin_amdgcn_s_sleep(KSL);
          TIMING_WAVE
        } else if (s < NSEG - 1 && kst) {
          if (fq) {
            const u64 miss = __builtin_amdgcn_ballot_w64(st[0] != 0u);
            krdy = s + 1 + (miss ? __builtin_ctzll(miss) : 16);
          }
          if (KSL2) __builtin_amdgcn_s_sleep(KSL2);
          K_STAGE(s + 1)
        } else {
          __builtin_amdgcn_s_sleep(KSL);
          unsigned tch = 0;
          TLB_TOUCH
          asm volatile("" ::"v"(tch));
        }
        if (w != TWV) EV(6);
        if (w != TWV) EVDUMP(s);
      }
    }
  } else {
    const v32u W0 = cat32(wb[0], wb[1], wb[2], wb[3]), W1 = cat32(wb[4], wb[5], wb[6], wb[7]), W2 = cat32(wb[8], wb[9], wb[10], wb[11]);
    const v32u W3 = cat32(wb[12], wb[13], wa[0], wa[1]), W4 = cat32(wa[2], wa[3], wa[4], wa[5]), W5 = cat32(wa[6], wa[7], wb[14], wb[15]);
    v8i adr;
    adr[7] = l16;
    adr[6] = l16 + w * 1024;
    adr[5] = pw0;
    adr[4] = sidx;
#pragma unroll
    for (int q = 0; q < 2; ++q) {
      const int c = 8 * (2 * q + (g >> 1) + ((g & 1) ? 0 : 4)) + (w ^ ((h >> 4) * 4));
      const int blk = (c % PO) * 2 + (h >> 4);
      adr[3 - q] = (int)OFF_VM + blk * 32768 + ((cls_of(perm, c / PO) ^ xreg(r_a + (int)(OFF_VM / UNIT) + (blk >> 4))) << 12) +
                   (h & 15) * 256 + l15 * 16;
    }
    adr[1] = (16 * (g & 1) + 4 * (g >> 1) + trq) * 24 + (trp & 1) * 8;
    adr[0] = L_HI + (32 * (w & 3) + l15) * 24 + 4 * g;
#define VCH(i) (((i) & 8) | ((((i) - 1) & 7) ^ krot))
    const int mo0 = MEM_OFF(VCH(w)), mo1 = MEM_OFF(VCH(w + 4)), mo2 = MEM_OFF(VCH(w + 8));
    if (w == TWV) clk_edge(ck.r, ck.m, 2);
    if (w < 4) {
#pragma unroll 1
      for (int s = 0; s < NSEG; ++s) {
        EVDECL;
        const int so = (int)OFF_STEP + s * (int)UNIT, xr = xreg(r_s + s);
        unsigned cnt;
        asm volatile(VO_A
                     : [cnt] "=&s"(cnt) EVOUT
                     : WPIN, "{v[244:251]}"(adr), "{v243}"(pw1), [rs] "s"(rsv), [o0] "s"(so + mo0 + ((cown ^ xr) << 12)), [vs] "s"(vm_slot(s))
                     : VO_CLOB, "v241", "vcc", "scc", "memory");
        if (s < 4) PT(11 + s);
        if (w == 0) {
          unsigned tch = 0;
          TLB_TOUCH
          asm volatile("" ::"v"(tch));
        }
        EVDUMP(s);
      }
    } else {
#pragma unroll 1
      for (int s = 0; s < NSEG; ++s) {
        EVDECL;
        const int so = (int)OFF_STEP + s * (int)UNIT, xr = xreg(r_s + s);
        const int lst = olane();
        int vst = ((cls_of(perm, lst & 3) ^ xr) << 12) | ((lst & 4) << 9) | ((lst & 8) << 12) | (((lst >> 4) | (w != TWV)) << 28);
        unsigned cnt;
        v2u st = {(unsigned)(ST_ADR | ((w != TWV) << 30)), 0u};
        int o0 = so + mo0 + ((cown ^ xr) << 12), o1 = o0 - mo0 + mo1, o2 = o0 - mo0 + mo2, vs = vm_slot(s), sst = so + R_ST + role * 1024 + h * 32;
        int ss = so + R_ST + ((cown ^ xr) << 12);
        asm volatile("" : "+{v[240:241]}"(st), "+{v242}"(vst));
        asm volatile("" : "+s"(o0), "+s"(o1), "+s"(o2), "+s"(vs), "+s"(sst), "+s"(ss));
        TIMING_GO
        asm volatile(VO_B
                     : [cnt] "=&s"(cnt), "+{v[240:241]}"(st) EVOUT TQOUT
                     : WPIN, "{v[244:251]}"(adr), "{v243}"(pw1), "{v242}"(vst), [rs] "s"(rsv), [o0] "s"(o0), [o1] "s"(o1), [o2] "s"(o2),
                       [vs] "s"(vs), [ckm] "s"(ck.m - NCYV), [inv] "s"(ck.inv << 16), [r16] "s"(ck.r << 4), [sst] "s"(sst), [tg] "s"(tg),
                       [tw] "s"(tw), [ss] "s"(ss)
                     : VO_CLOB, "vcc", "scc", "memory");
        if (w == TWV && s < NSEG - 1) TIMING_WAVE
        if (w != TWV) EVDUMP(s);
      }
    }
  }
  __builtin_amdgcn_endpgm();
}
typedef f32x2 c2;
#define PKOP(name, txt)                                   \
  __device__ __forceinline__ c2 name(c2 a, c2 b) {        \
    c2 d;                                                 \
    asm("v_pk_add_f32 %0, %1, %2 " txt : "=v"(d) : "v"(a), "v"(b)); \
    return d;                                             \
  }
PKOP(c_addi, "op_sel:[0,1] op_sel_hi:[1,0] neg_lo:[0,1]")
PKOP(c_addc, "neg_hi:[0,1]")
PKOP(c_sepb, "op_sel:[1,1] op_sel_hi:[0,0] neg_hi:[1,0]")
PKOP(c_mixc, "op_sel:[0,1] op_sel_hi:[1,0] neg_hi:[1,0]")
#define T_ADD(d, a, b) "v_pk_add_f32 %" #d ", %" #a ", %" #b "\n\t"
#define T_SUB(d, a, b) "v_pk_add_f32 %" #d ", %" #a ", %" #b " neg_lo:[0,1] neg_hi:[0,1]\n\t"
#define T_ROT(d, a, b, n) "v_pk_add_f32 %" #d ", %" #a ", %" #b " op_sel:[0,1] op_sel_hi:[1,0] " n ":[0,1]\n\t"
#define T_MUL(d, a, w) "v_pk_mul_f32 %" #d ", %" #a ", %" #w " op_sel_hi:[1,0]\n\t"
#define T_FRE(d, a, w, c) "v_pk_fma_f32 %" #d ", %" #a ", %" #w ", %" #c " op_sel_hi:[1,0,1]\n\t"
#define T_FIM(d, a, w, c, n) "v_pk_fma_f32 %" #d ", %" #a ", %" #w ", %" #c " op_sel:[1,1,0] op_sel_hi:[0,1,1] " n ":[1,0,0]\n\t"
#define T_2MN(d, a, t) "v_pk_fma_f32 %" #d ", %" #a ", 2.0, %" #t " op_sel_hi:[1,0,1] neg_lo:[0,0,1] neg_hi:[0,0,1]\n\t"
#define T_TAIL(n, m) T_ADD(0, 4, 5) T_ROT(1, 2, 3, m) T_SUB(4, 4, 5) T_ROT(3, 2, 3, n)
#define T_D0(n, m) T_ADD(4, 0, 2) T_ADD(5, 1, 3) T_SUB(2, 0, 2) T_SUB(3, 1, 3) T_TAIL(n, m)
#define T_D1(n, m) T_MUL(5, 1, 7) T_ROT(4, 0, 2, m) T_FIM(1, 1, 7, 5, n) T_ROT(2, 0, 2, n) T_FRE(5, 3, 9, 1) T_FIM(5, 3, 9, 5, n) T_2MN(3, 1, 5) T_TAIL(n, m)
#define T_D2(n, m) T_MUL(5, 1, 7) T_FRE(4, 2, 8, 0) T_FIM(1, 1, 7, 5, n) T_FIM(4, 2, 8, 4, n) T_FRE(5, 3, 9, 1) T_2MN(2, 0, 4) T_FIM(5, 3, 9, 5, n) \
    T_ADD(0, 4, 5) T_2MN(3, 1, 5) T_SUB(4, 4, 5) T_ROT(1, 2, 3, m) T_ROT(3, 2, 3, n)
#define T_D3(n, m) T_MUL(4, 0, 6) T_MUL(5, 1, 7) T_FIM(0, 0, 6, 4, n) T_FIM(1, 1, 7, 5, n) T_FRE(4, 2, 8, 0) T_FRE(5, 3, 9, 1) \
    T_FIM(4, 2, 8, 4, n) T_FIM(5, 3, 9, 5, n) T_2MN(2, 0, 4) T_2MN(3, 1, 5) T_TAIL(n, m)
#define D4_ASM(T, C)                                                                                                       \
  {                                                                                                                        \
    if constexpr (INV) asm(T("neg_hi", "neg_lo") : "+v"(x0), "+v"(x1), "+v"(x2), "+v"(x3), "=&v"(t0), "=&v"(t1) : C(w0), C(w1), C(w2), C(w3)); \
    else asm(T("neg_lo", "neg_hi") : "+v"(x0), "+v"(x1), "+v"(x2), "+v"(x3), "=&v"(t0), "=&v"(t1) : C(w0), C(w1), C(w2), C(w3));               \
  }
__device__ __forceinline__ constexpr float cos32(int m) {
  constexpr float C[9] = {1.f, 0.98078528040323044f, 0.92387953251128674f, 0.83146961230254524f, 0.70710678118654752f,
                          0.55557023301960218f, 0.38268343236508977f, 0.19509032201612825f, 0.f};
  m &= 31;
  if (m > 16) m = 32 - m;
  return m > 8 ? -C[16 - m] : C[m];
}
#define W32(m) c2{cos32(m), -cos32((m) - 8)}
template <bool INV, int M0, int M1, int M2, int M3>
__device__ __forceinline__ void dft4k(c2& x0, c2& x1, c2& x2, c2& x3) {
  c2 t0, t1;
  if constexpr (M3 == 0) {
    const int w0 = 0, w1 = 0, w2 = 0, w3 = 0;
    D4_ASM(T_D0, "n")
  } else {
    const c2 w1 = W32(M1), w3 = W32(M3), w0 = M0 ? W32(M0) : w1, w2 = M0 == 0 && M2 == 8 ? w1 : W32(M2);
    if constexpr (M0 != 0) D4_ASM(T_D3, "s")
    else if constexpr (M2 == 8) D4_ASM(T_D1, "s")
    else D4_ASM(T_D2, "s")
  }
  x2 = t0;
}
template <bool INV, bool W0>
__device__ __forceinline__ void dft4w(c2& x0, c2& x1, c2& x2, c2& x3, c2 w0, c2 w1, c2 w2, c2 w3) {
  c2 t0, t1;
  if constexpr (W0) D4_ASM(T_D3, "v")
  else D4_ASM(T_D2, "v")
  x2 = t0;
}
#define T_B2(n, m) T_FRE(4, 1, 6, 0) T_FRE(5, 3, 7, 2) T_FIM(4, 1, 6, 4, n) T_FIM(5, 3, 7, 5, n) T_2MN(1, 0, 4) T_2MN(3, 2, 5)
#define T_B0(n, m) T_ADD(4, 0, 1) T_ROT(5, 2, 3, m) T_SUB(1, 0, 1) T_ROT(3, 2, 3, n)
template <bool INV, int K>
__device__ __forceinline__ void bf2(c2& x0, c2& x1, c2& x2, c2& x3, c2 w0, c2 w1) {
  c2 t0, t1;
  const c2 w2 = w0, w3 = w0;
  if constexpr (K == 0) D4_ASM(T_B2, "v")
  else if constexpr (K == 1) D4_ASM(T_B2, "s")
  else D4_ASM(T_B0, "s")
  x0 = t0;
  x2 = t1;
}
__device__ __forceinline__ void mul2(c2& d0, c2& d1, c2 a0, c2 w0, c2 a1, c2 w1) {
  asm(T_MUL(0, 2, 3) T_MUL(1, 4, 5) T_FIM(0, 2, 3, 0, "neg_lo") T_FIM(1, 4, 5, 1, "neg_lo") : "=&v"(d0), "=&v"(d1) : "v"(a0), "v"(w0), "v"(a1), "v"(w1));
}
template <int N, bool INV, int TW>
__device__ __forceinline__ void dftn(c2 (&v)[N], const c2* w) {
  constexpr int Q = N / 4;
#pragma unroll
  for (int n = 0; n < Q; ++n) {
    if (TW == 0) dft4k<INV, 0, 0, 0, 0>(v[n], v[Q + n], v[2 * Q + n], v[3 * Q + n]);
    else if (TW == 1 && n == 0) dft4w<INV, false>(v[n], v[Q + n], v[2 * Q + n], v[3 * Q + n], w[Q], w[Q + n], w[2 * Q + n], w[3 * Q + n]);
    else dft4w<INV, true>(v[n], v[Q + n], v[2 * Q + n], v[3 * Q + n], w[n], w[Q + n], w[2 * Q + n], w[3 * Q + n]);
  }
  c2 o[N];
  if constexpr (N == 32) {
#define D8A(K1)                                                                                          \
  dft4k<INV, 0, 2 * K1, 4 * K1, 6 * K1>(v[8 * K1], v[8 * K1 + 2], v[8 * K1 + 4], v[8 * K1 + 6]);          \
  dft4k<INV, K1, 3 * K1, 5 * K1, 7 * K1>(v[8 * K1 + 1], v[8 * K1 + 3], v[8 * K1 + 5], v[8 * K1 + 7]);
    D8A(0) D8A(1) D8A(2) D8A(3)
#pragma unroll
    for (int k = 0; k < 32; k += 8) {
      bf2<INV, 2>(v[k], v[k + 1], v[k + 4], v[k + 5], W32(4), W32(12));
      bf2<INV, 1>(v[k + 2], v[k + 3], v[k + 6], v[k + 7], W32(4), W32(12));
    }
#pragma unroll
    for (int i = 0; i < 32; ++i) o[(i >> 3) + 2 * (i & 6) + 16 * (i & 1)] = v[i];
  } else {
    dft4k<INV, 0, 0, 0, 0>(v[0], v[1], v[2], v[3]);
    dft4k<INV, 0, 2, 4, 6>(v[4], v[5], v[6], v[7]);
    dft4k<INV, 0, 4, 8, 12>(v[8], v[9], v[10], v[11]);
    dft4k<INV, 0, 6, 12, 18>(v[12], v[13], v[14], v[15]);
#pragma unroll
    for (int i = 0; i < 16; ++i) o[(i >> 2) + 4 * (i & 3)] = v[i];
  }
#pragma unroll
  for (int i = 0; i < N; ++i) v[i] = o[i];
}
template <bool H32>
__device__ __forceinline__ void c_swap(c2& a, c2& b) {
#pragma unroll
  for (int j = 0; j < 2; ++j) {
    const unsigned x = __float_as_uint(a[j]), y = __float_as_uint(b[j]);
    const auto r = H32 ? __builtin_amdgcn_permlane32_swap(x, y, false, false) : __builtin_amdgcn_permlane16_swap(x, y, false, false);
    a[j] = __uint_as_float(r[0]);
    b[j] = __uint_as_float(r[1]);
  }
}
#define LANE_NOP()                  \
  do {                              \
    SB();                           \
    asm volatile("s_nop 1");        \
    SB();                           \
  } while (0)
__device__ __forceinline__ c2 lds_c2(int a) { return *(volatile LAS c2*)((LAS char*)g_lds + a); }
template <int N>
__device__ __forceinline__ void lds_put(const c2 (&v)[N], int a) {
#pragma unroll
  for (int i = 0; i < N / 2; ++i) *(volatile LAS f32x4*)((LAS char*)g_lds + a + 16 * i) = f32x4{v[2 * i][0], v[2 * i][1], v[2 * i + 1][0], v[2 * i + 1][1]};
}
constexpr int XS = 32768, WS_ = 16384, WT_OFF = 18 << 20;
__device__ __forceinline__ int side_adr(int hi, int lo, int sd) { return hi + ((lo >> 13) << 14) + (lo & 8191) + (sd ^ ((hi >> 8) & 8192)); }
constexpr int LDS_TW = 0;
constexpr int LDS_EB = 4096;
constexpr int LDS_STG = 4096 + 8 * 9216;
__device__ __forceinline__ void tw2048(c2 (&w)[32], int lane) {
#pragma unroll
  for (int k = 1; k <= 8; ++k) w[k] = lds_c2(LDS_TW + lane * k * 8);
  const c2 h = lds_c2(LDS_TW + (lane & 31) * 128);
  mul2(w[0], w[16], h, h, w[8], w[8]);
#pragma unroll
  for (int k = 9; k < 15; k += 2) mul2(w[k], w[k + 1], w[8], w[k - 8], w[8], w[k - 7]);
  mul2(w[15], w[24], w[8], w[7], w[16], w[8]);
#pragma unroll
  for (int k = 1; k < 8; ++k) mul2(w[16 + k], w[24 + k], w[16], w[k], w[24], w[k]);
}
template <bool INV>
__device__ __forceinline__ void fft2048(c2 (&v)[32], const c2 (&w)[32], int eb, int lane) {
  dftn<32, INV, 0>(v, w);
  lds_put<32>(v, eb + lane * 272);
  const int ra = eb + (lane >> 5) * 4352 + (lane & 31) * 8;
#pragma unroll
  for (int k = 0; k < 16; ++k) {
    v[k] = lds_c2(ra + k * 272);
    v[k + 16] = lds_c2(ra + k * 272 + 8704);
  }
#pragma unroll
  for (int k = 0; k < 16; k += 2) bf2<INV, 0>(v[k], v[k + 16], v[k + 1], v[k + 17], w[0], w[0]);
  LANE_NOP();
#pragma unroll
  for (int k = 0; k < 16; ++k) c_swap<true>(v[k], v[k + 16]);
  dftn<32, INV, 1>(v, w);
}
template <bool SC>
__device__ __forceinline__ void tw1024(c2 (&w)[16], c2 (&u)[4], int lane, float s) {
#pragma unroll
  for (int k = 1; k <= 4; ++k) w[k] = lds_c2(LDS_TW + lane * k * 16);
  u[1] = lds_c2(LDS_TW + (lane & 15) * 256);
  u[0] = u[1];
  mul2(u[2], w[8], u[1], u[1], w[4], w[4]);
  mul2(u[3], w[12], u[2], u[1], w[8], w[4]);
  w[0] = c2{s, 0.f};
  if (SC) {
#pragma unroll
    for (int k = 1; k < 4; ++k) w[k] *= s;
  }
  c2 z;
  mul2(w[5], w[6], w[4], w[1], w[4], w[2]);
  mul2(w[7], z, w[4], w[3], w[4], w[3]);
#pragma unroll
  for (int k = 1; k < 4; ++k) mul2(w[8 + k], w[12 + k], w[8], w[k], w[12], w[k]);
  if (SC) {
#pragma unroll
    for (int k = 4; k < 16; k += 4) w[k] *= s;
  }
}
template <bool INV>
__device__ __forceinline__ void fft1024(c2 (&v)[16], const c2 (&w)[16], const c2 (&u)[4], int eb, int lane) {
  dftn<16, INV, 0>(v, w);
  lds_put<16>(v, eb + lane * 144);
  const int ra = eb + ((lane >> 1) & 8) * 144 + (lane >> 5) * 576 + (lane & 15) * 8;
  c2 t[4][4];
#pragma unroll
  for (int a = 0; a < 4; ++a)
#pragma unroll
    for (int r = 0; r < 4; ++r) t[a][r] = lds_c2(ra + (a + 16 * r) * 144);
#pragma unroll
  for (int a = 0; a < 4; ++a) {
    dft4w<INV, false>(t[a][0], t[a][1], t[a][2], t[a][3], u[0], u[1], u[2], u[3]);
#pragma unroll
    for (int q = 0; q < 4; ++q) v[a + 8 * (q & 1) + 4 * (q >> 1)] = t[a][q];
  }
  LANE_NOP();
#pragma unroll
  for (int i = 0; i < 8; ++i) c_swap<false>(v[i], v[i + 8]);
#pragma unroll
  for (int i = 0; i < 8; ++i) c_swap<true>(v[(i & 3) + 2 * (i & 4)], v[(i & 3) + 2 * (i & 4) + 4]);
  dftn<16, INV, INV ? 2 : 1>(v, w);
}

__device__ __forceinline__ void fft_table(int w) {
  const int m = w * 64 + olane();
  float sn, cs;
  sincospif(-(float)m * (1.f / 1024.f), &sn, &cs);
  *(LAS c2*)((LAS char*)g_lds + LDS_TW + m * 8) = c2{cs, sn};
  RAW_BARRIER();
}
__device__ __forceinline__ void count_done(char* ws, int cnt_off, int w) {
  VMCNT(0);
  RAW_BARRIER();
  if (w == 0) cnt_inc(ws, cnt_off);
}
__device__ __forceinline__ void stg_adr(int lane, int w, int& up, int& mid, int& dn) {
  up = LDS_STG + lane * 128 + ((w ^ (lane >> 1)) & 7) * 16;
  dn = LDS_STG + (64 - lane) * 128 + ((w ^ ((64 - lane) >> 1)) & 7) * 16 + 8;
  mid = dn + 57344 - (lane == 0 ? 8 : 0);
}

#define FT(i)
#define FTV(i, x)
__device__ __forceinline__ void fft_pass1(char* ws, char* x1, int sd, const u16* __restrict__ x, const u16* __restrict__ wr,
                                          const u16* __restrict__ wi, int task, int w) {
  LAS char* const L = (LAS char*)g_lds;
  const __amdgpu_buffer_rsrc_t rx = __builtin_amdgcn_make_buffer_rsrc(x1, 0, 513 * XS, 0x00020000);
  const __amdgpu_buffer_rsrc_t rt = __builtin_amdgcn_make_buffer_rsrc(x1 + WT_OFF, 0, 513 * WS_, 0x00020000);
  const __amdgpu_buffer_rsrc_t ri = __builtin_amdgcn_make_buffer_rsrc((void*)x, 0, S * D * 2, 0x00020000);
  const __amdgpu_buffer_rsrc_t rr = __builtin_amdgcn_make_buffer_rsrc((void*)wr, 0, S * DF * 2, 0x00020000);
  const __amdgpu_buffer_rsrc_t rm = __builtin_amdgcn_make_buffer_rsrc((void*)wi, 0, S * DF * 2, 0x00020000);
  const int lane = olane();
  const int p = task * 8 + w, r0 = (p & 63) + 128 * (p >> 6);
  unsigned xa[16], xb[16], fr[2][9], fi[2][9];
  FT(0);
#pragma unroll
  for (int t = 0; t < 16; ++t) {
    xa[t] = __builtin_amdgcn_raw_buffer_load_b16(ri, lane * 2 + t * 128, r0 * 2048, 0);
    xb[t] = __builtin_amdgcn_raw_buffer_load_b16(ri, lane * 2 + t * 128, r0 * 2048 + 131072, 0);
  }
  const int l8 = lane == 0 ? 1024 : OOB;
#pragma unroll
  for (int r = 0; r < 2; ++r)
#pragma unroll
    for (int t = 0; t < 9; ++t) {
      fr[r][t] = __builtin_amdgcn_raw_buffer_load_b16(rr, t < 8 ? lane * 2 + t * 128 : l8, (r0 + 64 * r) * (DF * 2), 0);
      fi[r][t] = __builtin_amdgcn_raw_buffer_load_b16(rm, t < 8 ? lane * 2 + t * 128 : l8, (r0 + 64 * r) * (DF * 2), 0);
    }
  FT(1);
  c2 tw[16], tu[4], v[16];
  tw1024<false>(tw, tu, lane, 1.f);
  FTV(2, xb[15]);
#pragma unroll
  for (int t = 0; t < 16; ++t) v[t] = c2{__uint_as_float(xa[t] << 16), __uint_as_float(xb[t] << 16)};
  fft1024<false>(v, tw, tu, LDS_EB + w * 9216, lane);
  int up, mid, dn;
  stg_adr(lane, w, up, mid, dn);
#pragma unroll
  for (int j = 0; j < 16; ++j) *(volatile LAS c2*)(L + (j < 8 ? up + 8192 * j : (j == 8 ? mid : dn + 8192 * (15 - j)))) = v[j];
  FT(3);
  RAW_BARRIER();
  FTV(4, fi[1][8]);
  {
    const int fa = LDS_EB + lane * 64 + ((w ^ lane) & 6) * 8 + (w & 1) * 8;
#pragma unroll
    for (int t = 0; t < 9; ++t)
      if (t < 8 || lane == 0) *(LAS v2u*)(L + fa + t * 4096) = v2u{fr[0][t] | (fi[0][t] << 16), fr[1][t] | (fi[1][t] << 16)};
  }
  const int kq = w * 8 + (lane >> 3);
  {
    const int ra = LDS_STG + kq * 128 + ((lane ^ (kq >> 1)) & 7) * 16, ga = kq * XS + (lane & 7) * 16;
    v4u d[9];
#pragma unroll
    for (int i = 0; i < 9; ++i) d[i] = *(const LAS v4u*)(L + ra + i * 8192);
#pragma unroll
    for (int i = 0; i < 9; ++i)
      if (i < 8 || w == 0)
        st128(d[i], rx, ga + (i == 8 ? 512 * XS : 0), side_adr((i & 7) * (64 * XS), task * 128, sd));
  }
  FT(5);
  RAW_BARRIER();
  {
    const int k4 = w * 16 + (lane >> 2);
    const int ra = LDS_EB + k4 * 64 + (((2 * lane) ^ k4) & 6) * 8, ga = k4 * WS_ + (lane & 3) * 16;
    v4u d[5];
#pragma unroll
    for (int i = 0; i < 5; ++i) d[i] = *(const LAS v4u*)(L + ra + i * 8192);
#pragma unroll
    for (int i = 0; i < 5; ++i)
      if (i < 4 || w == 0)
        st128(d[i], rt, ga + (i == 4 ? 512 * WS_ : 0), side_adr((i & 3) * (128 * WS_), task * 64, sd ^ 8192));
  }
  FT(6);
  FT(7);
}
__device__ __forceinline__ void fft_pass2(char* ws, char* x1, int sd, int task, int w) {
  const __amdgpu_buffer_rsrc_t rx = __builtin_amdgcn_make_buffer_rsrc(x1, 0, 513 * XS, 0x00020000);
  const __amdgpu_buffer_rsrc_t rt = __builtin_amdgcn_make_buffer_rsrc(x1 + WT_OFF, 0, 513 * WS_, 0x00020000);
  const int lane = olane();
  const int k = task * 8 + w;
  if (k > 512) return;
  f32x4 d[16];
  FT(0);
#pragma unroll
  for (int t = 0; t < 16; ++t)
    d[t] = __builtin_bit_cast(f32x4, __builtin_amdgcn_raw_buffer_load_b128(rx, lane * 16 + (t & 3) * 1024, side_adr(k * XS, (t >> 2) * 4096, sd), AUX_SC1));
  c2 tw[32], v[32];
  tw2048(tw, lane);
  FT(1);
  const bool edge = (k & 511) == 0;
  FTV(2, d[15]);
  if (edge) {
#pragma unroll
    for (int t = 0; t < 16; ++t) {
      d[t][2] = d[t][0];
      d[t][3] = d[t][1];
    }
  }
#pragma unroll
  for (int t = 0; t < 16; ++t) {
    v[2 * t] = c_addc(c2{d[t][0], d[t][1]}, c2{d[t][2], d[t][3]});
    v[2 * t + 1] = c_sepb(c2{d[t][0], d[t][1]}, c2{d[t][2], d[t][3]});
  }
  SB();
  v2u f[16];
#pragma unroll
  for (int t = 0; t < 16; ++t) f[t] = __builtin_amdgcn_raw_buffer_load_b64(rt, lane * 8 + (t & 7) * 512, side_adr(k * WS_, (t >> 3) * 4096, sd ^ 8192), AUX_SC1);
  SB();
  const int eb = LDS_EB + w * 17408;
  fft2048<false>(v, tw, eb, lane);
  FTV(3, v[31]);
  FTV(4, f[15]);
#pragma unroll
  for (int t = 0; t < 16; ++t)
    mul2(v[2 * t], v[2 * t + 1], v[2 * t], c2{__uint_as_float(f[t][0] << 16), __uint_as_float(f[t][0] & 0xffff0000u)}, v[2 * t + 1],
         c2{__uint_as_float(f[t][1] << 16), __uint_as_float(f[t][1] & 0xffff0000u)});
  fft2048<true>(v, tw, eb, lane);
  FTV(5, v[31]);
  if (edge) {
#pragma unroll
    for (int t = 0; t < 32; ++t) v[t][1] = 0.f;
  }
#pragma unroll
  for (int t = 0; t < 16; ++t) {
    const c2 a = c_addi(v[2 * t], v[2 * t + 1]), b = c_mixc(v[2 * t], v[2 * t + 1]);
    st128(__builtin_bit_cast(v4u, f32x4{a[0], a[1], b[0], b[1]}), rx, lane * 16 + (t & 3) * 1024, side_adr(k * XS, (t >> 2) * 4096, sd));
  }
  FT(6);
  FT(7);
}
__device__ __forceinline__ void fft_pass3(char* ws, char* x1, int sd, u16* __restrict__ out, int task, int w) {
  LAS char* const L = (LAS char*)g_lds;
  const __amdgpu_buffer_rsrc_t rx = __builtin_amdgcn_make_buffer_rsrc(x1, 0, 513 * XS, 0x00020000);
  const __amdgpu_buffer_rsrc_t ro = __builtin_amdgcn_make_buffer_rsrc(out, 0, S * 4096, 0x00020000);
  const int lane = olane();
  const int kq = w * 8 + (lane >> 3);
  const int ra = LDS_STG + kq * 128 + ((lane ^ (kq >> 1)) & 7) * 16, ga = kq * XS + (lane & 7) * 16;
  v4u d[9];
  FT(0);
#pragma unroll
  for (int i = 0; i < 9; ++i) d[i] = __builtin_amdgcn_raw_buffer_load_b128(rx, ga + (i == 8 ? 512 * XS : 0), side_adr((i & 7) * (64 * XS), task * 128, sd), AUX_SC1);
  c2 tw[16], tu[4], v[16];
  tw1024<true>(tw, tu, lane, 1.f / (2.f * 2048.f * 1024.f));
  FT(1);
  FTV(2, d[8]);
#pragma unroll
  for (int i = 0; i < 9; ++i)
    if (i < 8 || w == 0) *(LAS v4u*)(L + ra + i * 8192) = d[i];
  int up, mid, dn;
  stg_adr(lane, w, up, mid, dn);
  FT(3);
  RAW_BARRIER();
  FT(4);
#pragma unroll
  for (int j = 0; j < 16; ++j) v[j] = lds_c2(j < 8 ? up + 8192 * j : (j == 8 ? mid : dn + 8192 * (15 - j)));
  fft1024<true>(v, tw, tu, LDS_EB + w * 9216, lane);
  FTV(5, v[15]);
  const int p = task * 8 + w, r0 = (p & 63) + 128 * (p >> 6);
#pragma unroll
  for (int t = 0; t < 16; ++t) {
    const unsigned o = pk2(v[t][0], v[t][1]);
    __builtin_amdgcn_raw_buffer_store_b16((u16)o, ro, lane * 2 + t * 128, r0 * 4096, 0);
    __builtin_amdgcn_raw_buffer_store_b16((u16)(o >> 16), ro, lane * 2 + t * 128, r0 * 4096 + 262144, 0);
  }
  FT(6);
  FT(7);
}

#define W7P 3
#define VTC 0
#define NXA 0
#define ARA 0
#define RSO 5
#define NCAL 6
#define DOX2 300
#define DTC 100
#define NX1 4
#define NXG 2
#define NXK 5
#define OFD 400
#define STI 600
#define TNX 0
#define NXP 0
#define PACE 150
#define PFB 4
#define CV1 -70
#define CV2 110
#define EDL 0
#define EVB(k) EV(k)
constexpr int L_D = 16384, L_R = 32768, L_F = 122880;
__device__ __forceinline__ volatile LAS unsigned* flw(int i, int m) {
  return (volatile LAS unsigned*)((LAS char*)g_lds + L_F + i * 4 + (olane() & m) * 4);
}
template <int SLP = 0>
__device__ __forceinline__ void lds_wait_ge(int i, int m, unsigned v, gu32* tmo, unsigned code) {
  volatile LAS unsigned* p = flw(i, m);
  _Pragma("clang loop unroll(disable)") for (unsigned spins = 0;;) {
    if (__all(*p >= v)) break;
    if (SLP) __builtin_amdgcn_s_sleep(SLP);
    if (++spins > (kSpinCap << 5)) {
      tmo_report(tmo, code);
      break;
    }
  }
  asm volatile("" ::: "memory");
}
#define HLP 6
#define HLT 0
__device__ __forceinline__ void help(int c, int w, const u16* Wq, const u16* Wk, const u16* Wv) {
  const int j = c & 7, m = j >> 1, xg = c / PO, ln = olane();
  if ((c & 8) && w >= 1 && w <= HLP) {
    const u16* wm = m == 2 ? Wv : ((m == 3 ? j : m) & 1 ? Wk : Wq);
    const int hs = m == 3 ? 31 : PQ * xg + slow_head(wm) + 4 * (j & 1);
    if (m < 3 || xg == NXC - 1)
      for (int q = 0; q < 2; ++q)
        touch4((const char*)wm + hs * 256 + (size_t)((16 - w) ^ (hs & KROT)) * 524288, (q * 32 + (ln >> 1)) * 8192 + (ln & 1) * 128, 65536 + w * 256);
  }
}
template <bool PRE>
__device__ __forceinline__ unsigned owner(const int c, const int w, const u64 nonce, const u16* __restrict__ mem0, char* ws,
                                          u16* __restrict__ out, unsigned perm, const u16* Wq, const u16* Wk, const u16* Wv) {
  LAS char* const L = (LAS char*)g_lds;
  gu32* tmo = (gu32*)(ws + OFF_TMO);
  const int lane = olane();
  const int l15 = lane & 15, g = lane >> 4;
  const __amdgpu_buffer_rsrc_t rs = __builtin_amdgcn_make_buffer_rsrc(ws, 0, (int)WS_USED, 0x00020000);
  const int xg = c / PO, li = c % PO;
  const int r_a = (int)(((uintptr_t)ws >> 19) & 7);
  const int r_s = r_a + (int)(OFF_STEP / UNIT);
  volatile LAS unsigned* fl = (volatile LAS unsigned*)(L + L_F);
  unsigned m0v = 0;
  if constexpr (PRE) {
  if (w == 7) m0v = *(const unsigned*)(mem0 + (lane >> 3) * D + 16 * c + 2 * (lane & 7));
  if (c == 0 && w == 6) hs_lead<4>(rs, true, CNT_HSO, 64u);
  unsigned cal = 0;
  if (w == 0)
    st128(v4u{(unsigned)lane, 0u, 0u, 0u}, rs, (lane & 31) * 16 + (lane >> 5) * 32768, (int)OFF_SCR + (((li & 7) ^ xreg(r_a)) << 12) + (c >> 3) * 512);
  if (w == 0 && lane == 0) {
    const int cl = li & 7;
    int off = (int)OFF_SCR + ((cl ^ xreg(r_a)) << 12) + (c >> 3) * 512;
    unsigned best = 0xfffeu;
#pragma unroll 1
    for (int r = 0; r < NCAL; ++r) {
      VMCNT(0);
      const u64 t1 = __builtin_amdgcn_s_memtime();
      asm volatile("s_waitcnt lgkmcnt(0)" : "+v"(off)::"memory");
      const unsigned v = __builtin_amdgcn_raw_buffer_load_b32(rs, off, (r & 3) * 128 + (r >> 2) * 32768, AUX_SC1);
      asm volatile("s_waitcnt vmcnt(0)" ::"v"(v) : "memory");
      const u64 t2 = __builtin_amdgcn_s_memtime();
      best = min(best, (unsigned)(t2 - t1));
    }
    if (HLT == 0 && li >= 8) best = 0xfffeu;
    cal = ((unsigned)cl << 16) | best;
  }
  if (w == 0) {
    PT(0);
    hs_wait(rs, nonce, v2u{~(unsigned)nonce, 0u});
    cnt_inc(ws, CNT_HSO);
    PT(1);
    unsigned id;
    asm volatile("s_getreg_b32 %0, hwreg(HW_REG_XCC_ID)" : "=s"(id));
    if (lane == 0) {
      __builtin_amdgcn_raw_buffer_store_b32(((id & 15u) << 19) | cal, rs, c * 4, (int)OFF_CAL, AUX_SC1);
      if (li == 0) __builtin_amdgcn_raw_buffer_store_b32(id & 15u, rs, xg * 4, (int)OFF_XID, AUX_SC1);
    }
  }
  static_assert(NF1O == 64 || NF1O == 2, "one task per owner but the first two, or none");
  __syncthreads();
  if (HLT == 1) help(c, w, Wq, Wk, Wv);
  if (w == 7) {
    const unsigned pm = numa_perm(numa_load(rs, tmo), (LAS unsigned*)(L + L_F + 512));
    fl[10] = pm;
  }
  __syncthreads();
  perm = __builtin_amdgcn_readfirstlane(fl[10]);
  PT(2);
  if (HLT == 2) help(c, w, Wq, Wk, Wv);
  }
  const int xr0 = xreg(r_s + (PRE ? 0 : NARM + (li & 1)));
  const v4u sent = {0xffffffffu, 0xffffffffu, 0xffffffffu, 0xffffffffu};
  const int ibs = (int)OFF_VM + li * 65536 + ((cls_of(perm, xg) ^ xreg(r_a + (int)(OFF_VM / UNIT) + (li >> 3))) << 12);

  int am = (int)OFF_STEP + R_MEM + (c >> 4) * 32768 + ((cls_of(perm, g) ^ xr0) << 12) + (c & 15) * 256 + l15 * 16;
  int ap[2];
#pragma unroll
  for (int q = 0; q < 2; ++q) {
    const int o = c * 384 + (lane & 31) * 16;
    ap[q] = (lane & 31) < 24 && c < 59 ? (int)OFF_STEP + R_EX + (o >> 12) * 32768 + ((cls_of(perm, 2 * q + (lane >> 5)) ^ xr0) << 12) + (o & 4095)
                             : OOB;
  }
  int as = lane < 8 * NXC ? (int)OFF_STEP + R_ST + ((cls_of(perm, lane >> 3) ^ xr0) << 12) + (c >> 5) * 32768 + (c & 31) * 128 + (lane & 7) * 16
                          : OOB;
  const int ax = (int)OFF_STEP + R_PX + c * 1280 + lane * 16;
  const int ax2 = lane < 16 ? ax + 1024 : (lane < 24 ? (int)OFF_STEP + R_DUM + c * 128 + (lane - 16) * 16 : OOB);
#define ARM_REC(r)                                                                                                   \
  {                                                                                                                  \
    const int so_ = (r) * (int)UNIT, dx_ = (xreg(r_s + (r)) ^ xreg(r_s + (r) + 1)) << 12;                            \
    __builtin_amdgcn_raw_buffer_store_b128(sent, rs, am, so_, AUX_SC1);                                              \
    am ^= dx_;                                                                                                       \
    _Pragma("unroll") for (int q = 0; q < 2; ++q) {                                                                  \
      __builtin_amdgcn_raw_buffer_store_b128(sent, rs, ap[q], so_, AUX_SC1);                                         \
      ap[q] ^= dx_;                                                                                                  \
    }                                                                                                                \
    __builtin_amdgcn_raw_buffer_store_b128(sent, rs, as, so_, AUX_SC1);                                              \
    as ^= dx_;                                                                                                       \
    __builtin_amdgcn_raw_buffer_store_b128(sent, rs, ax, so_, AUX_SC1);                                              \
    __builtin_amdgcn_raw_buffer_store_b128(sent, rs, ax2, so_, AUX_SC1);                                             \
  }
  if (PRE && w == 7) {
    {
      const int row = lane >> 3, np = lane & 7;
#pragma unroll
      for (int r = 0; r < NXC; ++r)
        __builtin_amdgcn_raw_buffer_store_b32(min(m0v, 0xfffffffeu), rs,
                                              (int)OFF_STEP + R_MEM + (c >> 4) * 32768 + ((cls_of(perm, r) ^ xreg(r_s)) << 12) +
                                                  (c & 15) * 256 + ((row & 1) + 2 * (np & 1) + 4 * (row >> 1)) * 16 + (np >> 1) * 4,
                                              0, AUX_SC1);
    }
    am |= 1 << 30;
#pragma unroll 1
    for (int r = 0; r < NARM + (li & 1); ++r) {
      ARM_REC(r)
      am &= ~(1 << 30);
    }
#pragma unroll
    for (int k = 0; k < 32; ++k)
      __builtin_amdgcn_raw_buffer_store_b128(sent, rs, lane * 16 + (k & 3) * 1024, ibs + vm_slot(k >> 3) + ((k >> 2) & 1) * 32768, AUX_SC1);
    VMCNT(0);
    PTW(3, 7);
    cnt_inc(ws, CNT_ARM);
  }
  if constexpr (PRE) return perm;
  PT(5);
  __syncthreads();
  if (w == 7 && lane < 32) fl[lane] = 0;
  __syncthreads();

  const int team = li & 1, c0 = c & ~1;
  const int l16 = lane * 16;
  const int cown = cls_of(perm, xg);
  if (w < 2) {
    const int r = (l15 & 1) + 2 * (l15 >> 2), kh = (l15 >> 1) & 1;
    const int nl = (kh ^ ((r >> 1) & 1)) + 2 * ((g & 1) ^ (r >> 2));
    typedef int v8i __attribute__((ext_vector_type(8)));
    v8i adr;
    adr[0] = l16;
    adr[1] = 32768 + SC_POS(2 * nl + (g >> 1), r);
    adr[2] = l16;
    adr[3] = L_D + w * 8192 + l16;
    adr[4] = ((l15 & 3) + 4 * g) * 16 + (l15 >> 2) * 4;
    adr[5] = (l15 & 1) ? 0x03020706 : 0x05040100;
    adr[6] = (l15 & 2) ? 0x4444eeee : 0xeeee4444;
    adr[7] = L_F + 48;
    u64 mk[4];
#pragma unroll
    for (int k = 0; k < 4; ++k) mk[k] = __builtin_amdgcn_ballot_w64(nl == k);
    const v4u rsv = {(unsigned)(uintptr_t)ws, (unsigned)((uintptr_t)ws >> 32) & 0xffffu, (unsigned)WS_USED, 0x00020000u};
    const int dtc_ = TN(12, DTC);
    const int eg = (c0 >> 2) & 1;
#pragma unroll 1
    for (int s = team; s < NSEG; s += 2) {
      EVDECL;
      const int so = (int)OFF_STEP + s * (int)UNIT;
      int sp = so + R_PM + ((cown ^ xreg(r_s + s)) << 12);
      int sv = ibs + (w - team) * 65536 + vm_slot(s);
      int sv1 = sv + eg * 32768, sv2 = sv + (1 - eg) * 32768, sp1 = sp + eg * 2048, sp2 = sp + (1 - eg) * 2048;
      const int xn = xreg(r_s + s + 1);
      int sm[NXC];
#pragma unroll
      for (int q = 0; q < NXC; ++q) {
        sm[q] = so + (int)UNIT + R_MEM + (c0 >> 4) * 32768 + ((cls_of(perm, q) ^ xn) << 12) + ((c0 + w) & 15) * 256;
        asm volatile("" : "+s"(sm[q]));
      }
      asm volatile("" : "+s"(sp), "+s"(sv1), "+s"(sv2), "+s"(sp1), "+s"(sp2));
      if (s >= 2) lds_wait_ge<4>(20, 3, s - 1, tmo, 0x30017);
      if (VTC && w == 0) {
        const unsigned tc = __builtin_amdgcn_raw_buffer_load_b32(rs, 0, sv ^ 8192, AUX_SC1);
        asm volatile("" ::"v"(tc));
      }
      v4u bl[8];
      if (s < NSEG - 1) {
        v32u blv;
        unsigned cnt, bad;
        asm volatile(OW_F
                     : [cnt] "=&s"(cnt), [st] "=&s"(bad), "={v[0:31]}"(blv) EVOUT
                     : "{v[112:119]}"(adr), [rs] "s"(rsv), [sv1] "s"(sv1), [sv2] "s"(sv2), [sp] "s"(sp), [sp1] "s"(sp1), [sp2] "s"(sp2),
                       [sm0] "s"(sm[0]), [sm1] "s"(sm[1]), [sm2] "s"(sm[2]), [sm3] "s"(sm[3]), [m0] "s"(mk[0]), [m1] "s"(mk[1]),
                       [m2] "s"(mk[2]), [m3] "s"(mk[3])
                     : OF_CLOB, "vcc", "scc", "memory");
#pragma unroll
        for (int k = 0; k < 8; ++k) bl[k] = v4u{blv[4 * k], blv[4 * k + 1], blv[4 * k + 2], blv[4 * k + 3]};
        if (__builtin_expect(bad != 0u, 0)) {
          v4u xs, pv[4];
          _Pragma("clang loop unroll(disable)") for (unsigned spins = 0;;) {
            OPQ(va, l16);
            OPQ(v1, adr[1]);
            OPQ(v2, adr[2]);
#pragma unroll
            for (int k = 0; k < 8; ++k) bl[k] = __builtin_amdgcn_raw_buffer_load_b128(rs, va + (k & 3) * 1024, k < 4 ? sv1 : sv2, AUX_SC1);
            xs = __builtin_amdgcn_raw_buffer_load_b128(rs, v1, sp, AUX_SC1);
#pragma unroll
            for (int k = 0; k < 4; ++k) pv[k] = __builtin_amdgcn_raw_buffer_load_b128(rs, v2 + (k & 1) * 1024, k < 2 ? sp1 : sp2, AUX_SC1);
            unsigned m = mx4(0u, xs);
#pragma unroll
            for (int k = 0; k < 4; ++k) m = mx4(m, pv[k]);
#pragma unroll
            for (int k = 0; k < 8; ++k) m = mx4(m, bl[k]);
            if (!anysent(m)) break;
            __builtin_amdgcn_s_sleep(RSO);
            if (spin_fail(spins, tmo, 0x20000 + s)) break;
          }
          f32x4 a0 = {0.f, 0.f, 0.f, 0.f};
#pragma unroll
          for (int i = 0; i < 8; ++i) {
            const int k = F_ORD[i] >= 'a' ? F_ORD[i] - 'a' : F_ORD[i] - '0';
            if (F_ORD[i] >= 'a') {
              const v4u z = v4u{nl == k ? xs[0] : 0u, nl == k ? xs[1] : 0u, nl == k ? xs[2] : 0u, nl == k ? xs[3] : 0u};
              const v8u db = cat8(*(const LAS v4u*)(L + adr[3] + k * 2048), *(const LAS v4u*)(L + adr[3] + k * 2048 + 1024));
              a0 = (k & 1) ? smf1(z, db, a0, adr[6]) : smf(z, db, a0, adr[6]);
            } else
              a0 = smf(pv[k], cat8(bl[2 * k], bl[2 * k + 1]), a0, adr[6]);
          }
          const unsigned own = pk2(a0[0] + a0[2], a0[1] + a0[3]);
          const unsigned nb = (unsigned)__builtin_amdgcn_mov_dpp((int)own, 0xb1, 0xf, 0xf, true);
          const unsigned o = __builtin_amdgcn_perm(nb, own, (unsigned)adr[5]);
#pragma unroll
          for (int q = 0; q < NXC; ++q) __builtin_amdgcn_raw_buffer_store_b32(o, rs, adr[4], sm[q], AUX_SC1);
          EV(3);
        }
      } else {
        BARE_BARRIER();
        wait_cyc(*flw(12, 0));
        _Pragma("clang loop unroll(disable)") for (unsigned spins = 0;;) {
          OPQ(va, l16);
          unsigned m = 0;
#pragma unroll
          for (int k = 0; k < 8; ++k) bl[k] = __builtin_amdgcn_raw_buffer_load_b128(rs, va + (k & 3) * 1024, k < 4 ? sv1 : sv2, AUX_SC1);
#pragma unroll
          for (int k = 0; k < 8; ++k) m = mx4(m, bl[k]);
          if (!anysent(m)) break;
          __builtin_amdgcn_s_sleep(RSO);
          if (spin_fail(spins, tmo, 0x20000 + s)) break;
        }
      }
      const unsigned tp = cyc32();
      if (s < 4) PT(9 + (s >> 1));
      *flw(2 + w, 0) = s + 1;
      if (s < NSEG - 1) {
        const v4u tv = *(volatile LAS v4u*)flw(12, 0);
        const int lr = olane();
        __builtin_amdgcn_raw_buffer_store_b32(min(((tp - tv[1]) * tv[2]) >> 16, 60000u), rs,
                                              ((cls_of(perm, lr & 3) ^ xn) << 12) | ((lr & 4) << 9) | ((lr & 8) << 12) | ((lr >> 4) << 28),
                                              so + (int)UNIT + R_ST + (c0 + w) * 32 + 4, AUX_SC1);
      }
      OPQ(vs, l16);
#pragma unroll
      for (int k = 0; k < 8; ++k) *(LAS v4u*)(L + w * 8192 + ((k ^ (4 * eg)) * 1024) + vs) = bl[k];
      asm volatile("s_waitcnt lgkmcnt(0)" ::: "memory");
      *flw(4 + w, 0) = s + 1;
      if (s < NSEG - 4) {
        lds_wait_ge(2, 1, s + 1, tmo, 0x3001a);
        v4u se;
        asm volatile("v_mov_b32 %0, -1" : "=v"(se[0]));
        se[1] = se[2] = se[3] = se[0];
#pragma unroll
        for (int k = 0; k < 8; ++k) __builtin_amdgcn_raw_buffer_store_b128(se, rs, vs + (k & 3) * 1024, sv + (k >> 2) * 32768, AUX_SC1 | ARA);
        asm volatile("s_nop 1" ::"v"(se));
      }
      if (s < NSEG - 2) {
        wait_cyc(tp + dtc_);
        const int lt = olane();
        const unsigned tc = __builtin_amdgcn_raw_buffer_load_b32(rs, lt < 40 ? lt * 128 : OOB, so + R_PX + ((li & ~1) + w) * 5120, AUX_SC1);
        asm volatile("" ::"v"(tc));
      }
      EVDUMP(s);
    }
  } else if (w < 6) {
    const int n = w - 2;
    const v4u rsv = {(unsigned)(uintptr_t)ws, (unsigned)((uintptr_t)ws >> 32) & 0xffffu, (unsigned)WS_USED, 0x00020000u};
    const int vfr = (int)((perm & 8192u) ^ (VFAR == 2 ? 8192u : 0u)) | (r_a << 19);
    const int fv0 = fvw((c0 >> 1) & 3, 8 * n, team);
    const int dox_ = TN(10, DOX2), pace_ = TN(11, PACE);
    v4u raw[2][8];
#define RAW_LD(dst, sg)                                                                                       \
  _Pragma("unroll") for (int t = 0; t < 2; ++t) _Pragma("unroll") for (int k = 0; k < 8; ++k) dst[t][k] =     \
      __builtin_amdgcn_raw_buffer_load_b128(rs, l16 + (k & 3) * 1024, vw_addr(vfr, c0 + t, sg, 2 * n + (k >> 2)), AUX_SC1);
    wait_flag<KF0>(rs, fv0 + (lane & 7), tmo, 0x30018);
    PTW(6, 2);
    RAW_LD(raw, team)
    int nrdy = team;
#pragma unroll 1
    for (int s = team; s < NSEG; s += 2) {
      EVDECL;
      const int so = (int)OFF_STEP + s * (int)UNIT;
      if (s < NSEG - 1) {
        const int ln = olane();
        OPQ(eidx, (ln & 4) ? 0xeeee : 0x4444);
        OPQ(vea, (ln & 4) * 128 + (ln >> 4) * 128 + ((ln & 3) + 4 * (((ln >> 3) & 1) ^ (n >> 1))) * 16);
        int se = so + R_EX + n * 32768 + ((cown ^ xreg(r_s + s)) << 12);
        v8u bx[2][4];
#pragma unroll
        for (int t = 0; t < 2; ++t)
#pragma unroll
          for (int k = 0; k < 4; ++k) {
            const v4u u = raw[t][2 * k], v = raw[t][2 * k + 1];
            bx[t][k] = v8u{u[0], v[0], u[2], v[2], u[1], v[1], u[3], v[3]};
            asm volatile("" : "+v"(bx[t][k]));
          }
        OPQ(vd, L_D + n * 2048 + ln * 16);
        asm volatile("" : "+s"(se));
        EV(3);
        if (s < 4) PTW(7 + (s >> 1), 2);
        unsigned bad;
        asm volatile(OW_E
                     : [st] "=&s"(bad) EVOUT
                     : "{v241}"(vea), "{v242}"(vd), "{v243}"(eidx), [rs] "s"(rsv), [se] "s"(se), [b00] "v"(bx[0][0]), [b01] "v"(bx[0][1]),
                       [b02] "v"(bx[0][2]), [b03] "v"(bx[0][3]), [b10] "v"(bx[1][0]), [b11] "v"(bx[1][1]), [b12] "v"(bx[1][2]),
                       [b13] "v"(bx[1][3])
                     : OE_CLOB, "vcc", "scc", "memory");
        if (__builtin_expect(bad != 0u, 0)) {
          v4u pv[4];
          _Pragma("clang loop unroll(disable)") for (unsigned spins = 0;;) {
            __builtin_amdgcn_s_sleep(RSO);
            asm volatile("" : "+v"(vea));
            unsigned m = 0;
#pragma unroll
            for (int k = 0; k < 4; ++k) pv[k] = __builtin_amdgcn_raw_buffer_load_b128(rs, vea + k * 1024, se, AUX_SC1);
#pragma unroll
            for (int k = 0; k < 4; ++k) m = mx4(m, pv[k]);
            if (!anysent(m) || spin_fail(spins, tmo, 0x10000 + s)) break;
          }
#pragma unroll
          for (int q = 0; q < 4; ++q) {
            const f32x4 z = {0.f, 0.f, 0.f, 0.f};
            const f32x4 d = smf(pv[2 * (q & 1)], bx[q >> 1][2 * (q & 1)], z, eidx), e = smf(pv[2 * (q & 1) + 1], bx[q >> 1][2 * (q & 1) + 1], z, eidx);
            *(LAS v4u*)(L + vd + (q >> 1) * 8192 + (q & 1) * 1024) = v4u{pk2(d[0], d[1]), pk2(d[2], d[3]), pk2(e[0], e[1]), pk2(e[2], e[3])};
          }
          EV(2);
          RAW_BARRIER();
        }
      } else BARE_BARRIER();
      v4u nx[2][8];
      const int sn = min(s + 2, NSEG - 1);
      unsigned tpb = 0;
#define VW_FLAGS                                                                                                      \
  {                                                                                                                   \
    const int lf = olane();                                                                                           \
    OPQ(vfl, fv0 + min((s >> 1) + 1 + (lf >> 3), NSEG / 2 - 1) * 8 + (lf & 7));                                        \
    const u64 miss = __builtin_amdgcn_ballot_w64(__builtin_amdgcn_raw_buffer_load_b8(rs, vfl, 0, AUX_SC1) != 0);      \
    nrdy = s + 2 * (miss ? __builtin_ctzll(miss) >> 3 : 8);                                                           \
  }
      if (s < NSEG - 2) {
        __builtin_amdgcn_s_sleep(5);
        lds_wait_ge(2, 1, s + 1, tmo, 0x30014);
        tpb = cyc32();
        const unsigned tw = tpb + dox_;
        if (nrdy < s + 2) VW_FLAGS
        wait_cyc(tw);
      }
      EVB(6);
      OPQ(va, s < NSEG - 2 && nrdy >= s + 2 ? l16 : OOB);
#define NX_LD(dst, q0, q1)                                                                              \
  _Pragma("unroll") for (int q = q0; q < q1; ++q) dst[q >> 3][q & 7] = __builtin_amdgcn_raw_buffer_load_b128( \
      rs, va + (q & 3) * 1024, vw_addr(vfr, c0 + (q >> 3), sn, 2 * n + ((q >> 2) & 1)), AUX_SC1 | NXA);
      NX_LD(nx, 0, NX1)
      SB();
      if (s < NSEG - 2) wait_cyc(cyc32() + ((n + (li >> 1)) & 3) * pace_);
      else lds_wait_ge(4, 1, s + 1, tmo, 0x30014);
      EVB(4);
      f32x4 ac[2][2];
      const int sx = so + R_PX + n * 8192, sy = so + R_PX + 32768 + n * 2048;
      v4u pv[2][10], vm[2][2];
      OPQ(vb, l16);
#define P_LD                                                                                         \
  _Pragma("unroll") for (int k = 0; k < 10; ++k) _Pragma("unroll") for (int rt = 0; rt < 2; ++rt) pv[rt][k] = \
      __builtin_amdgcn_raw_buffer_load_b128(rs, vb + (k & 3) * 1024, (k < 8 ? sx + (k >> 2) * 4096 : sy) + rt * 40960, AUX_SC1);
#define P_MM(k0, k1)                                                                                 \
  _Pragma("unroll") for (int k = k0; k < k1; ++k) _Pragma("unroll") for (int rt = 0; rt < 2; ++rt) {  \
    _Pragma("unroll") for (int t = 0; t < 2; ++t)                                                    \
      ac[rt][t] = mma(k < 8 ? raw[t][k & 7] : vm[t][k & 1], pv[rt][k], k == 0 ? f32x4{0.f, 0.f, 0.f, 0.f} : ac[rt][t]); \
    asm volatile("" : "+v"(ac[rt][0]), "+v"(ac[rt][1]));                                             \
  }
      P_LD
      SB();
      lds_wait_ge(4, 1, s + 1, tmo, 0x30014);
#pragma unroll
      for (int t = 0; t < 2; ++t)
#pragma unroll
        for (int k = 0; k < 2; ++k) vm[t][k] = *(const LAS v4u*)(L + vb + t * 8192 + n * 2048 + k * 1024);
      SB();
      P_MM(0, 10)
      if (__builtin_expect(__builtin_amdgcn_ballot_w64(__builtin_isunordered(ac[0][0][0], ac[1][0][0])) != 0ull, 0)) {
        _Pragma("clang loop unroll(disable)") for (unsigned spins = 0;;) {
          asm volatile("" : "+v"(vb));
          P_LD
          unsigned m = 0;
#pragma unroll
          for (int k = 0; k < 10; ++k) m = mx4(mx4(m, pv[0][k]), pv[1][k]);
          if (!anysent(m)) break;
          __builtin_amdgcn_s_sleep(8);
          if (spin_fail(spins, tmo, 0x30015)) break;
        }
        P_MM(0, 10)
      }
      EVB(7);
      if (__builtin_expect(s < NSEG - 2 && nrdy < s + 2, 0)) {
        _Pragma("clang loop unroll(disable)") for (unsigned spins = 0;;) {
          VW_FLAGS
          if (nrdy >= s + 2) break;
          __builtin_amdgcn_s_sleep(8);
          if (spin_fail(spins, tmo, 0x30018)) break;
        }
        va = vb;
        NX_LD(nx, 0, NX1)
      }
      NX_LD(raw, NX1, 16)
      SB();
      OPQ(vr, L_R + (s & 2) * 8192 + l16);
#pragma unroll
      for (int q = 0; q < 4; ++q) *(LAS f32x4*)(L + vr + q * 4096 + n * 1024) = ac[q >> 1][q & 1];
      asm volatile("s_waitcnt lgkmcnt(0)" ::: "memory");
      fl[20 + n] = s + 1;
      lds_wait_ge(20, 3, s + 1, tmo, 0x30016);
      {
        const LAS f32x4* pr = (const LAS f32x4*)(L + vr + n * 4096);
        const f32x4 o = (pr[0] + pr[64]) + (pr[128] + pr[192]);
        const int lo = olane();
        *(v2u*)(out + (size_t)(s * 32 + 16 * (n >> 1) + (lo & 15)) * 2048 + 1024 + 16 * (c0 + (n & 1)) + 4 * (lo >> 4)) =
            v2u{pk2(o[0], o[1]), pk2(o[2], o[3])};
      }
      EVB(5);
#pragma unroll
      for (int q = 0; q < NX1; ++q) raw[q >> 3][q & 7] = nx[q >> 3][q & 7];
      EVDUMP(s);
    }
  } else if (w == 6) {
    if (c == 1) hs_lead<64>(rs, false, CNT_HS, 192u);
  } else {
    int narm = NARM + team;
    if (W7P) __builtin_amdgcn_s_setprio(W7P);
    const int sdk_ = NS2F(TN(2, SDK)), cv1_ = NS2F(TN(3, CV1)), cv2_ = NS2F(TN(4, CV2));
    arm_wait(rs, tmo);
    PTW(4, 7);
    Clk ck;
    clk_init(ck);
    unsigned t1 = cyc32();
    const int mg_ = NS2F(TN(0, MARG)), tol_ = NS2F(TN(13, TOL)), xl_ = NS2F(TN(14, XLATE));
    Pace pc = {0u, NS2F(ED0), NS2F(EA0), 0};
    const int sti_ = TN(15, STI), ofd_ = NS2F(TN(9, OFD));
#pragma unroll 1
    for (int s = 0; s < NSEG; ++s) {
      EVDECL;
      const bool on = (s & 1) == team;
      if (!on || s < 3) clk_update(ck, s < 3);
      int k1 = (cv1_ * (int)ck.cq) >> 16, k2 = (cv2_ * (int)ck.cq) >> 16;
      int sst = (int)OFF_STEP + s * (int)UNIT + R_ST + ((cown ^ xreg(r_s + s)) << 12);
      asm volatile("" : "+s"(k1), "+s"(k2), "+s"(sst));
      OPQ(va, lane * 32);
      OPQ(vt, L_F + 48);
      wait_cyc(t1);
      EV(0);
      v2u st = __builtin_amdgcn_raw_buffer_load_b64(rs, va, sst, AUX_SC1);
      clk_fresh(ck);
      _Pragma("clang loop unroll(disable)") for (unsigned spins = 0; anysent(st[0]);) {
        if (spins >= 2) __builtin_amdgcn_s_sleep(4);
        if (spin_fail(spins, tmo, 0x3000b)) break;
        asm volatile("" : "+v"(va));
        wait_cyc(t1 + spins * sti_);
        st = __builtin_amdgcn_raw_buffer_load_b64(rs, va, sst + (spins == 1 ? 2048 : (spins == 2 ? 32768 : 0)), AUX_SC1);
        clk_fresh(ck);
      }
      if (s < 4) PTW(11 + s, 7);
      const int dm = stamp_max(ck, st[0]);
      const unsigned tr = clk_cyc(ck, dm);
      if (on) {
        *(volatile LAS v4u*)(L + vt) = v4u{tr + k2, tr, ck.inv, 0u};
        EV(1);
        wait_cyc(tr + k1);
        RAW_BARRIER();
        if (s < NSEG - 1) BARE_BARRIER();
        if (narm < NSEG) {
          __builtin_amdgcn_s_sleep(5);
          lds_wait_ge(2, 1, s + 1, tmo, 0x3001b);
#pragma unroll 1
          for (int q = 0; q < 2; ++q) {
            if (narm < NSEG) ARM_REC(narm)
            ++narm;
          }
        }
      }
      pace_step(pc, s, (ck.r << 4) + dm, st[1], mg_, tol_, xl_);
      t1 = clk_cyc(ck, dm + pc.dq + pc.ea - sdk_ + (on ? ofd_ : 0));
      EVDUMP(s);
      EVCLK(s, ck);
    }
  }
  return 0u;
}
__global__ __launch_bounds__(512, 2) void mega_kernel(const u16* __restrict__ x, const u16* __restrict__ mem0,
                                                      const u16* __restrict__ Wq, const u16* __restrict__ Wk,
                                                      const u16* __restrict__ Wv, const u16* __restrict__ Wo,
                                                      char* __restrict__ ws, u16* __restrict__ out,
                                                      const u16* __restrict__ ax, const u16* __restrict__ awr,
                                                      const u16* __restrict__ awi) {
  ws += (size_t)(-(uintptr_t)ws) & (UNIT - 1);
  const int bx = BIDX;
  const int w = __builtin_amdgcn_readfirstlane(threadIdx.x >> 6);
  const int xg = bx & 7, idx = bx >> 3;
#define TEND
#define TMID(k)
#define CODEPF 16
  if (w == 7 && idx < CODEPF) {
    u64 pc;
    unsigned len, keep;
    asm volatile("s_getpc_b64 %0\n.Lcode_here:\n\ts_mov_b32 %1, .Lcode_end-.Lcode_here\n\t.subsection 1\n.Lcode_end:\n\t.subsection 0" : "=s"(pc), "=s"(len));
    const v4u rc = {(unsigned)pc, (unsigned)(pc >> 32) & 0xffffu, len, 0x00020000u};
    asm volatile("s_mov_b32 %0, m0\n\ts_mov_b32 m0, %3\n\ts_nop 0\n\tbuffer_load_dword %1, %2, 0 offen lds\n\ts_mov_b32 m0, %0"
                 : "=&s"(keep)
                 : "v"(olane() * 128 + idx * 8192), "s"(rc), "s"(32768 + 7 * 2048)
                 : "memory");
  }
  const u64 nonce = hs_post(__builtin_amdgcn_make_buffer_rsrc(ws, 0, 65536, 0x00020000), w, xg >= NXC && (idx & PQ) ? 7 : 0);
  TMID(0);
  int role, h, par = 0;
  if (xg < NXC) {
    role = idx >= 2 * PQ ? 4 : idx >= PQ;
    h = xg * PQ + idx % PQ;
  } else {
    role = 2 + (idx / PQ & 1);
    par = idx / (2 * PQ);
    h = (xg - NXC) * PQ + idx % PQ;
  }
  if (role == 0) wroles<0>(h, par, idx & 7, xg, w, nonce, x, Wq, Wk, Wv, Wo, ws);
  if (role == 1) wroles<1>(h, par, idx & 7, xg, w, nonce, x, Wq, Wk, Wv, Wo, ws);
  if (role == 2) wroles<2>(h, par, idx & 7, xg, w, nonce, x, Wq, Wk, Wv, Wo, ws);
  if (role == 3) wroles<3>(h, par, idx & 7, xg, w, nonce, x, Wq, Wk, Wv, Wo, ws);
  const int oc = xg * PO + idx - 2 * PQ;
  unsigned perm = 0;
  if (role == 4 && HLT == 0) help(oc, w, Wq, Wk, Wv);
  if (role == 4) {
    perm = owner<true>(oc, w, nonce, mem0, ws, out, 0u, Wq, Wk, Wv);
    VMCNT(0);
  }
  TMID(2);
  const __amdgpu_buffer_rsrc_t rs = __builtin_amdgcn_make_buffer_rsrc(ws, 0, 65536, 0x00020000);
  gu32* tmo = (gu32*)(ws + OFF_TMO);
  volatile LAS int* sact = (volatile LAS int*)(g_lds + 163584);
  unsigned idle = 0;
  bool tab = false;
#define FSIDE 0
  char* const x1 = ws + OFF_X1 + ((size_t)(-(uintptr_t)(ws + OFF_X1)) & ((2u << 20) - 1));
  int sd = 0;
#define TLOG(id)
  auto pick = [&]() __attribute__((always_inline)) {
    int off = CNT_FFT1 + (olane() & 7) * 128;
    asm volatile("" : "+v"(off));
    const unsigned v = __builtin_amdgcn_raw_buffer_load_b32(rs, off, 0, AUX_SC1) + 1u;
    const unsigned d1 = __builtin_amdgcn_readlane(v, 0), d2 = __builtin_amdgcn_readlane(v, 1);
    const unsigned q1 = __builtin_amdgcn_readlane(v, 4), q2 = __builtin_amdgcn_readlane(v, 5), q3 = __builtin_amdgcn_readlane(v, 6);
    int kind = -1, qoff = 0;
    unsigned lim = 0;
    if (q1 < 130u - NF1O) kind = 1, qoff = Q_FFT1, lim = 130u - NF1O;
    else if (d1 >= 128u && q2 < 65u) kind = 2, qoff = Q_FFT2, lim = 65u;
    else if (d2 >= 65u && q3 < 128u) kind = 3, qoff = Q_FFT3, lim = 128u;
    else if (q3 >= 128u) kind = -2;
    int act = kind;
    if (kind >= 0) {
      unsigned t = 0;
      if (olane() == 0) t = __hip_atomic_fetch_add((gu32*)(ws + qoff), 1u, RLX_AGENT) + 1u;
      t = __builtin_amdgcn_readfirstlane(t);
      act = t < lim ? (kind << 16) | (int)t : -3;
    }
    return act;
  };
  for (;;) {
    if (w == 0) *sact = role == 4 ? (oc > 1 && oc < NF1O ? 0x18000 | oc : -2) : pick();
    RAW_BARRIER();
    const int act = __builtin_amdgcn_readfirstlane(*sact);
    RAW_BARRIER();
    if (act == -2) break;
    if (act >= 0) {
      const int kind = act >> 16, t = act & 0xffff;
      TLOG(1000 + kind);
      if (!tab) {
        if (w == 7) sact[1] = numa_far(numa_load(rs, tmo));
        fft_table(w);
        sd = (__builtin_amdgcn_readfirstlane(sact[1]) ^ (int)((uintptr_t)x1 >> 8) ^ FSIDE) & 8192;
      }
      tab = true;
      if (kind == 1) {
        fft_pass1(ws, x1, sd, ax, awr, awi, (t & 0x8000) ? t & 0xff : (t < 2 ? t : NF1O - 2 + t), w);
        count_done(ws, CNT_FFT1, w);
      } else if (kind == 2) {
        fft_pass2(ws, x1, sd, t == 0 ? 64 : t - 1, w);
        count_done(ws, CNT_FFT2, w);
      } else fft_pass3(ws, x1, sd, out, t, w);
      TLOG(999);
      if (role == 4) break;
    } else if (act == -1) {
      __builtin_amdgcn_s_sleep(64);
      if (spin_fail(idle, tmo, 0x30022)) break;
    }
  }
  if (role == 4) {
    owner<false>(oc, w, nonce, mem0, ws, out, perm, Wq, Wk, Wv);
    return;
  }
  TEND;
}

extern "C" void kernel_launch(void* const* d_in, const int* in_sizes, int n_in, void* d_out, int out_size, void* d_ws,
                              size_t ws_size, hipStream_t stream) {
  const u16* A_x = (const u16*)d_in[0];
  const u16* A_wr = (const u16*)d_in[1];
  const u16* A_wi = (const u16*)d_in[2];
  const u16* B_x = (const u16*)d_in[3];
  const u16* B_mem0 = (const u16*)d_in[4];
  const u16* Wq = (const u16*)d_in[5];
  const u16* Wk = (const u16*)d_in[6];
  const u16* Wv = (const u16*)d_in[7];
  const u16* Wo = (const u16*)d_in[8];
  u16* out = (u16*)d_out;
  char* ws = (char*)d_ws;

  mega_kernel<<<256, 512, 0, stream>>>(B_x, B_mem0, Wq, Wk, Wv, Wo, ws, out, A_x, A_wr, A_wi);
}
```

```cpp
#include <hip/hip_runtime.h>
#include <hip/hip_bf16.h>
#include <stdint.h>
#include <type_traits>

typedef unsigned short u16;
typedef unsigned long long u64;
using bf16x8 = __attribute__((ext_vector_type(8))) __bf16;
using bf16x16 = __attribute__((ext_vector_type(16))) __bf16;
using f32x4 = __attribute__((ext_vector_type(4))) float;
typedef unsigned v32u __attribute__((ext_vector_type(32)));
typedef unsigned v16u __attribute__((ext_vector_type(16)));
typedef unsigned v8u __attribute__((ext_vector_type(8)));
typedef unsigned v4u __attribute__((ext_vector_type(4)));
typedef unsigned v2u __attribute__((ext_vector_type(2)));
typedef short s16x4 __attribute__((ext_vector_type(4)));
typedef float f32x2 __attribute__((ext_vector_type(2)));
#define GAS __attribute__((address_space(1)))
#define LAS __attribute__((address_space(3)))
typedef GAS unsigned gu32;
#define RLX_AGENT __ATOMIC_RELAXED, __HIP_MEMORY_SCOPE_AGENT

constexpr int S = 2048, D = 1024, H = 32, DH = 128, SEG = 32, NSEG = 64;
constexpr int DF = 513;

#define XSW 4
#define BIDX (blockIdx.x ^ XSW)
constexpr int NXC = 4;
constexpr int PQ = 32 / NXC, PO = 64 / NXC;

__shared__ __attribute__((aligned(16))) char g_lds[163840];
#define SPINCAP (1u << 18)
constexpr unsigned kSpinCap = SPINCAP;
constexpr int AUX_SC1 = 16;
constexpr int OOB = 0x7ff00000;
constexpr size_t UNIT = 524288;
constexpr int F_QK = 0, F_VW = 8192, CNT_FFT1 = 16384, CNT_FFT2 = 16512, CNT_ARM = 16640;
constexpr int Q_FFT1 = 16896, Q_FFT2 = 17024, Q_FFT3 = 17152;
constexpr size_t OFF_TMO = 20480;
constexpr size_t OFF_CAL = 24576, OFF_XID = 25088;
constexpr size_t ARM_HOST = 28672;
constexpr int OFF_REQ = 49152, OFF_ACK = 53248, CNT_HS = 17280, CNT_HSO = 17408;
constexpr size_t OFF_SCR = 65536;
constexpr size_t OFF_VM = UNIT;
constexpr int VM_SLOT = 4 << 20, VM_SLOT2 = 8 << 20;
constexpr size_t OFF_DBG = 3 * UNIT;
constexpr size_t OFF_STEP = 27 * UNIT;
constexpr int R_MEM = 0, R_EX = 131072, R_PM = 262144, R_SC = 294912, R_ST = 327680, R_PX = 393216, R_DUM = 475136;
constexpr size_t OFF_QK = (size_t)48 << 20;
constexpr size_t OFF_X1 = (size_t)217 << 20;
#define GST 64
constexpr size_t OFF_VWX = (size_t)(GST == 64 ? 89 : 80) << 20;
constexpr size_t WS_USED = (size_t)217 << 20;
constexpr int NARM = 4;
#define SCL 1
#define SC_POS(q, r) ((q) * 256 + ((r) & 4) * 32 + ((r) & 3) * 16)

__device__ __forceinline__ int olane() {
  int t;
  asm volatile("v_mbcnt_lo_u32_b32 %0, -1, 0\n\tv_mbcnt_hi_u32_b32 %0, -1, %0" : "=v"(t));
  __builtin_assume(t >= 0 && t < 64);
  return t;
}
__device__ __forceinline__ float bf2f(u16 v) { return __uint_as_float((unsigned)v << 16); }
__device__ __forceinline__ unsigned pk2(float a, float b) {
  typedef __bf16 b2 __attribute__((ext_vector_type(2)));
  f32x2 f = {a, b};
  b2 r = __builtin_convertvector(f, b2);
  return __builtin_bit_cast(unsigned, r);
}
__device__ __forceinline__ unsigned pk2s(float a, float b) { return min(pk2(a, b), 0xfffffffeu); }
__device__ __forceinline__ u16 f2bf(float f) { return (u16)(pk2(f, 0.f) & 0xffffu); }
__device__ __forceinline__ float rbf(float f) { return __uint_as_float(pk2(0.f, f)); }
__device__ __forceinline__ float opq_one() {
  float o = 1.f;
  asm("" : "+s"(o));
  return o;
}
__device__ __forceinline__ unsigned fold_pk(f32x4 a, float one) { return pk2(a[0] + a[2], __builtin_fmaf(a[1], one, a[3])); }
__device__ __forceinline__ float mul1(float a, float c) {
  float r;
  asm("v_mul_f32 %0, %2, %1" : "=v"(r) : "v"(a), "s"(c));
  return r;
}
__device__ __forceinline__ float fma1(float a, float c, float b) {
  float r;
  asm("v_fma_f32 %0, %1, %2, %3" : "=v"(r) : "v"(a), "s"(c), "v"(b));
  return r;
}
__device__ __forceinline__ int xreg(int r) { return ((r & 1) << 2) | ((r >> 1) & 1) | ((r >> 1) & 2); }

__device__ __forceinline__ void tmo_report(gu32* tmo, unsigned code) {
  unsigned z = 0, c = code;
  asm volatile("" : "+v"(z), "+v"(c));
  asm volatile("global_store_dword %0, %1, %2 sc1" ::"v"(z), "v"(c), "s"(tmo) : "memory");
}
__device__ __forceinline__ bool spin_fail(unsigned& spins, gu32* tmo, unsigned code) {
  asm volatile("" ::: "memory");
  if (__builtin_expect(++spins > kSpinCap, 0)) {
    tmo_report(tmo, code);
    return true;
  }
  return false;
}
__device__ __forceinline__ unsigned wave_min(unsigned v) {
  v = min(v, (unsigned)__builtin_amdgcn_mov_dpp((int)v, 0x121, 0xf, 0xf, true));
  v = min(v, (unsigned)__builtin_amdgcn_mov_dpp((int)v, 0x122, 0xf, 0xf, true));
  v = min(v, (unsigned)__builtin_amdgcn_mov_dpp((int)v, 0x124, 0xf, 0xf, true));
  v = min(v, (unsigned)__builtin_amdgcn_mov_dpp((int)v, 0x128, 0xf, 0xf, true));
  auto r = __builtin_amdgcn_permlane32_swap(v, v, false, false);
  v = min(r[0], r[1]);
  auto q = __builtin_amdgcn_permlane16_swap(v, v, false, false);
  return min(q[0], q[1]);
}
__device__ __forceinline__ float xmax16_32(float x) {
  auto r = __builtin_amdgcn_permlane32_swap(__float_as_uint(x), __float_as_uint(x), false, false);
  x = __builtin_amdgcn_fmed3f(__uint_as_float(r[0]), __uint_as_float(r[1]), __builtin_inff());
  auto q = __builtin_amdgcn_permlane16_swap(__float_as_uint(x), __float_as_uint(x), false, false);
  return __builtin_amdgcn_fmed3f(__uint_as_float(q[0]), __uint_as_float(q[1]), __builtin_inff());
}
__device__ __forceinline__ float xsum16(float x) {
  auto q = __builtin_amdgcn_permlane16_swap(__float_as_uint(x), __float_as_uint(x), false, false);
  return __uint_as_float(q[0]) + __uint_as_float(q[1]);
}
__device__ __forceinline__ float xsum16_32(float x) {
  auto r = __builtin_amdgcn_permlane32_swap(__float_as_uint(x), __float_as_uint(x), false, false);
  return xsum16(__uint_as_float(r[0]) + __uint_as_float(r[1]));
}
__device__ __forceinline__ unsigned mx4(unsigned m, v4u v) { return max(max(max(max(m, v[0]), v[1]), v[2]), v[3]); }
__device__ __forceinline__ bool anysent(unsigned m) { return __builtin_amdgcn_ballot_w64(m == 0xffffffffu) != 0ull; }

__device__ __forceinline__ unsigned now32() { return (unsigned)__builtin_amdgcn_s_memrealtime(); }
__device__ __forceinline__ unsigned cyc32() { return (unsigned)__builtin_amdgcn_s_memtime(); }
#define CMAX 14000
__device__ __forceinline__ void wait_cyc(unsigned t) {
  t = __builtin_amdgcn_readfirstlane(t);
  int d = (int)(t - cyc32());
  if (d > CMAX) return;
  while (d > 260) {
    __builtin_amdgcn_s_sleep(1);
    d = (int)(t - cyc32());
  }
  d = __builtin_amdgcn_readfirstlane(d - 70);
  if (d > 0) asm volatile("1: s_sub_u32 %0, %0, 28\n\ts_cbranch_scc0 1b" : "+s"(d)::"scc", "memory");
}
#define CLKSPAN 124u
struct Clk {
  unsigned r, m;
  unsigned cq, inv;
};
__device__ __forceinline__ bool clk_edge(unsigned& r, unsigned& m, int tries) {
  constexpr int NS = 14;
#pragma unroll 1
  for (int k = 0; k < tries; ++k) {
    u64 t[NS], m0, m1;
    asm volatile("s_memtime %0" : "=s"(m0));
#pragma unroll
    for (int i = 0; i < NS; ++i) {
      asm volatile("s_memrealtime %0\n\ts_nop 0" : "=s"(t[i]));
      if (i == NS - 2) asm volatile("s_memtime %0" : "=s"(m1));
    }
    asm volatile("s_waitcnt lgkmcnt(0)" : "+s"(m0), "+s"(m1), "+s"(t[0]), "+s"(t[1]), "+s"(t[2]), "+s"(t[3]), "+s"(t[4]), "+s"(t[5]),
                 "+s"(t[6]), "+s"(t[7]), "+s"(t[8]), "+s"(t[9]), "+s"(t[10]), "+s"(t[11]), "+s"(t[12]), "+s"(t[13]));
    unsigned n = 0;
#pragma unroll
    for (int i = 1; i < NS; ++i) asm("s_cmp_eq_u32 %1, %2\n\ts_addc_u32 %0, %0, 0" : "+s"(n) : "s"((unsigned)t[i]), "s"((unsigned)t[0]) : "scc");
    r = (unsigned)t[0] + 4;
    m = (unsigned)m0 + n * 8;
    asm volatile("" : "+s"(m));
    if ((unsigned)m1 - (unsigned)m0 <= CLKSPAN) return true;
  }
  return false;
}
__device__ __forceinline__ unsigned sdiv(unsigned a, unsigned b) {
  unsigned r;
  float t, u;
  asm volatile("v_cvt_f32_u32 %1, %4\n\tv_cvt_f32_u32 %2, %3\n\tv_rcp_f32 %1, %1\n\ts_nop 0\n\tv_mul_f32 %1, %1, %2\n\tv_cvt_u32_f32 %1, %1\n\t"
               "s_nop 0\n\tv_readfirstlane_b32 %0, %1\n\ts_nop 3"
               : "=s"(r), "=&v"(t), "=&v"(u)
               : "s"(a), "s"(b));
  return r;
}
__device__ __forceinline__ void clk_init(Clk& c) {
  clk_edge(c.r, c.m, 4);
  c.cq = 92500u;
  c.inv = 46432u;
}
__device__ __forceinline__ void clk_update(Clk& c, bool first = false) {
  unsigned r, m;
  if (!clk_edge(r, m, first ? 2 : 1) && r - c.r < 1500u) return;
  if (r - c.r >= 100u && r - c.r < 40000u) {
    unsigned q = sdiv((m - c.m) << 12, r - c.r);
    asm("s_max_u32 %0, %0, 0x101d0\n\ts_min_u32 %0, %0, 0x1b580" : "+s"(q)::"scc");
    c.cq = first ? q : (3 * c.cq + q) >> 2;
    c.inv = sdiv(0xffffffffu, c.cq);
  }
  c.r = r;
  c.m = m;
}
__device__ __forceinline__ void clk_fresh(Clk& c) {
  unsigned r, m;
  if (clk_edge(r, m, 1)) {
    c.r = r;
    c.m = m;
  }
}
__device__ __forceinline__ unsigned clk_fine(const Clk& c, unsigned m) {
  return (c.r << 4) + (unsigned)(((u64)(m - c.m) * c.inv) >> 16);
}
__device__ __forceinline__ int stamp_max(const Clk& c, unsigned st) {
  const unsigned tn = (c.r << 4) + 65536u;
  return 65536 - __builtin_amdgcn_readfirstlane((int)wave_min(tn - st));
}
__device__ __forceinline__ unsigned clk_cyc(const Clk& c, int d) { return c.m + (unsigned)(((long long)d * (int)c.cq) >> 16); }
#define NS2F(ns) (((ns) * 8) / 5)
struct Pace {
  unsigned rf;
  int ed, ea, dq;
};
__device__ __forceinline__ void pace_step(Pace& p, int s, unsigned rf, unsigned rep, int marg, int tol, int xlate) {
  const int arr = (int)(rf - p.rf) - p.dq;
  const bool late = s == 0 || arr > p.ea + tol;
  if (s > 0) {
    const int md = (int)~__builtin_amdgcn_readfirstlane(wave_min(~rep));
    p.ea = min(arr, p.ea + 5);
    p.ed = (int)min((unsigned)md, (unsigned)p.ed + 5u);
  }
  p.rf = rf;
  p.dq = p.ed + marg + (late ? xlate : 0);
}

template <int SLP>
__device__ __forceinline__ void wait_flag(__amdgpu_buffer_rsrc_t rs, int off, gu32* tmo, unsigned code) {
  _Pragma("clang loop unroll(disable)") for (unsigned spins = 0;;) {
    asm volatile("" : "+v"(off));
    unsigned v = __builtin_amdgcn_raw_buffer_load_b8(rs, off, 0, AUX_SC1);
    if (__all(v == 0u)) break;
    __builtin_amdgcn_s_sleep(SLP);
    if (spin_fail(spins, tmo, code)) break;
  }
}
#define RAW_BARRIER()                                    \
  do {                                                   \
    asm volatile("" ::: "memory");                       \
    __builtin_amdgcn_s_waitcnt(0xc07f);                  \
    __builtin_amdgcn_s_barrier();                        \
    asm volatile("" ::: "memory");                       \
  } while (0)
#define BARE_BARRIER()                 \
  do {                                 \
    asm volatile("" ::: "memory");     \
    __builtin_amdgcn_s_barrier();      \
    asm volatile("" ::: "memory");     \
  } while (0)
#define VMCNT(n) asm volatile("s_waitcnt vmcnt(" #n ")" ::: "memory")
#define SB() __builtin_amdgcn_sched_barrier(0)
#define OPQ(v, e) int v = (e); asm volatile("" : "+v"(v))
template <bool NT = false>
__device__ __forceinline__ void glds16(const void* base, int voff, unsigned dst) {
  unsigned keep;
  if constexpr (NT)
    asm volatile("s_mov_b32 %0, m0\n\ts_mov_b32 m0, %3\n\ts_nop 0\n\tglobal_load_lds_dwordx4 %1, %2 nt\n\ts_mov_b32 m0, %0"
                 : "=&s"(keep)
                 : "v"(voff), "s"(base), "s"(dst)
                 : "memory");
  else
  asm volatile("s_mov_b32 %0, m0\n\ts_mov_b32 m0, %3\n\ts_nop 0\n\tglobal_load_lds_dwordx4 %1, %2\n\ts_mov_b32 m0, %0"
               : "=&s"(keep)
               : "v"(voff), "s"(base), "s"(dst)
               : "memory");
}
__device__ __forceinline__ void touch4(const void* base, int voff, unsigned dst) {
  unsigned keep;
  asm volatile("s_mov_b32 %0, m0\n\ts_mov_b32 m0, %3\n\ts_nop 0\n\tglobal_load_lds_dword %1, %2\n\ts_mov_b32 m0, %0"
               : "=&s"(keep)
               : "v"(voff), "s"(base), "s"(dst)
               : "memory");
}
__device__ __forceinline__ f32x4 smf(v4u a, v8u b, f32x4 c, int idx) {
  return __builtin_amdgcn_smfmac_f32_16x16x64_bf16(__builtin_bit_cast(bf16x8, a), __builtin_bit_cast(bf16x16, b), c, idx, 0, 0);
}
__device__ __forceinline__ f32x4 smf1(v4u a, v8u b, f32x4 c, int idx) {
  return __builtin_amdgcn_smfmac_f32_16x16x64_bf16(__builtin_bit_cast(bf16x8, a), __builtin_bit_cast(bf16x16, b), c, idx, 0, 1);
}
__device__ __forceinline__ f32x4 mma(v4u a, v4u b, f32x4 c) {
  return __builtin_amdgcn_mfma_f32_16x16x32_bf16(__builtin_bit_cast(bf16x8, a), __builtin_bit_cast(bf16x8, b), c, 0, 0, 0);
}
__device__ __forceinline__ void st128(v4u d, __amdgpu_buffer_rsrc_t rs, int voff, int soff) {
  __builtin_amdgcn_raw_buffer_store_b128(d, rs, voff, soff, AUX_SC1);
  asm volatile("s_nop 1" ::"v"(d));
}
__device__ __forceinline__ f32x4 zero4() {
  f32x2 a, b;
  asm volatile("v_mov_b64 %0, 0" : "=v"(a));
  asm volatile("v_mov_b64 %0, 0" : "=v"(b));
  return f32x4{a[0], a[1], b[0], b[1]};
}
__device__ __forceinline__ f32x2 zero2() {
  f32x2 a;
  asm volatile("v_mov_b64 %0, 0" : "=v"(a));
  return a;
}
__device__ __forceinline__ v4u lo4(v8u v) { return v4u{v[0], v[1], v[2], v[3]}; }
__device__ __forceinline__ v4u hi4(v8u v) { return v4u{v[4], v[5], v[6], v[7]}; }
__device__ __forceinline__ v8u cat8(v4u a, v4u b) { return v8u{a[0], a[1], a[2], a[3], b[0], b[1], b[2], b[3]}; }
__device__ __forceinline__ v32u cat32(v8u a, v8u b, v8u c, v8u d) {
  const v16u x = __builtin_shufflevector(a, b, 0, 1, 2, 3, 4, 5, 6, 7, 8, 9, 10, 11, 12, 13, 14, 15);
  const v16u y = __builtin_shufflevector(c, d, 0, 1, 2, 3, 4, 5, 6, 7, 8, 9, 10, 11, 12, 13, 14, 15);
  return __builtin_shufflevector(x, y, 0, 1, 2, 3, 4, 5, 6, 7, 8, 9, 10, 11, 12, 13, 14, 15, 16, 17, 18, 19, 20, 21, 22, 23, 24, 25, 26,
                                 27, 28, 29, 30, 31);
}
__device__ __forceinline__ v2u trd(LAS char* p) {
  return __builtin_bit_cast(v2u, __builtin_amdgcn_ds_read_tr16_b64_v4i16((LAS s16x4*)p));
}
__device__ __forceinline__ int vm_slot(int s) { return (s & 1) * VM_SLOT + ((s >> 1) & 1) * VM_SLOT2; }
__device__ __forceinline__ int cls_of(unsigned perm, int x) { return (int)((perm >> (3 * x)) & 7u); }

__device__ __forceinline__ void arm_wait(__amdgpu_buffer_rsrc_t rs, gu32* tmo) {
  _Pragma("clang loop unroll(disable)") for (unsigned spins = 0;;) {
    OPQ(off, CNT_ARM);
    if (__all(__builtin_amdgcn_raw_buffer_load_b32(rs, off, 0, AUX_SC1) + 1u >= 64u)) break;
    __builtin_amdgcn_s_sleep(8);
    if (spin_fail(spins, tmo, 0x30007)) break;
  }
}
__device__ __forceinline__ v2u numa_load(__amdgpu_buffer_rsrc_t rs, gu32* tmo) {
  unsigned c, xid;
  _Pragma("clang loop unroll(disable)") for (unsigned spins = 0;;) {
    OPQ(a, olane() * 4);
    c = __builtin_amdgcn_raw_buffer_load_b32(rs, a, (int)OFF_CAL, AUX_SC1);
    xid = __builtin_amdgcn_raw_buffer_load_b32(rs, ((a >> 5) & (NXC - 1)) * 4, (int)OFF_XID, AUX_SC1);
    if (__all((c != 0xffffffffu) & (xid != 0xffffffffu))) break;
    __builtin_amdgcn_s_sleep(16);
    if (spin_fail(spins, tmo, 0x30008)) break;
  }
  return v2u{c, xid};
}
__device__ __forceinline__ unsigned numa_perm(v2u in, LAS unsigned* tab) {
  const int lane = olane();
  const unsigned c = in[0], xid = in[1];
  tab[lane] = 0xffffu;
  tab[64 + lane] = 0xffffu;
  tab[128 + lane] = 0u;
  __builtin_amdgcn_s_waitcnt(0xc07f);
  __hip_atomic_fetch_min(tab + ((c >> 16) & 127u), c & 0xffffu, __ATOMIC_RELAXED, __HIP_MEMORY_SCOPE_WORKGROUP);
  __hip_atomic_fetch_add(tab + 128 + ((c >> 21) & 3u) * 2 + ((c >> 17) & 1u), c & 0xffffu, __ATOMIC_RELAXED, __HIP_MEMORY_SCOPE_WORKGROUP);
  __builtin_amdgcn_s_waitcnt(0xc07f);
  asm volatile("" ::: "memory");
  const unsigned s0 = tab[128 + ((xid >> 2) & 3u) * 2], s1 = tab[129 + ((xid >> 2) & 3u) * 2];
  const bool far = ((lane >> 1) & 1) != (s1 < s0 ? 1 : 0);
  unsigned key = ((tab[(xid & 15u) * 8 + (lane & 7)] + (far ? 0x10000u : 0u)) << 6) | (unsigned)lane;
  if ((lane >> 3) >= NXC) key = 0xffffffffu;
  unsigned perm = 0;
#pragma unroll 1
  for (int r = 0; r < NXC; ++r) {
    const unsigned m = wave_min(key);
    const unsigned bx = (m >> 3) & 7u, bc = m & 7u;
    perm |= bc << (3 * bx);
    if ((unsigned)(lane >> 3) == bx || (unsigned)(lane & 7) == bc) key = 0xffffffffu;
  }
  return __builtin_amdgcn_readfirstlane(perm | (s1 < s0 ? 0u : 8192u));
}
__device__ __forceinline__ int numa_far(v2u in) {
  const unsigned c = in[0], x0 = __builtin_amdgcn_readfirstlane(in[1]);
  int d = (int)(c & 0xffffu);
  d = ((c >> 21) & 3u) == ((x0 >> 2) & 3u) ? ((c & 0x20000u) ? -d : d) : 0;
  d += __builtin_amdgcn_mov_dpp(d, 0x121, 0xf, 0xf, true);
  d += __builtin_amdgcn_mov_dpp(d, 0x122, 0xf, 0xf, true);
  d += __builtin_amdgcn_mov_dpp(d, 0x124, 0xf, 0xf, true);
  d += __builtin_amdgcn_mov_dpp(d, 0x128, 0xf, 0xf, true);
  d = __builtin_amdgcn_readlane(d, 0) + __builtin_amdgcn_readlane(d, 16) + __builtin_amdgcn_readlane(d, 32) + __builtin_amdgcn_readlane(d, 48);
  return d > 0 ? 0 : 8192;
}
#define VRING 64
#define QRING 64
#define VFAR 0
__device__ __forceinline__ int vw_addr(int fr, int tile, int seg, int gq) {
  return (int)OFF_VWX + ((((tile >> 3) * GST + (seg & (VRING - 1))) * 8 + (tile & 7)) * 8 + gq) * 4096;
}
__device__ __forceinline__ int fqk(int h, int seg) { return F_QK + h * 256 + seg * 4; }
__device__ __forceinline__ int fvw(int pr, int h, int seg) { return F_VW + ((pr * 4 + (h >> 3)) * 2 + (seg & 1)) * 256 + (seg >> 1) * 8 + (h & 7); }
constexpr int QK_RT = 4096;
__device__ __forceinline__ int qk_addr(int mat, int seg, int h) {
  return (int)OFF_QK + ((mat * 64 + (seg & (QRING - 1))) * 32 + h) * 8192;
}

__device__ __forceinline__ u64 hs_post(__amdgpu_buffer_rsrc_t rs, int w, int HSW) {
  const u64 n = __builtin_amdgcn_s_memrealtime();
  if (w == HSW) __builtin_amdgcn_raw_buffer_store_b64(v2u{(unsigned)n, (unsigned)(n >> 32)}, rs, olane() == 0 ? 0 : OOB,
                                                    OFF_REQ + (int)BIDX * 8, AUX_SC1);
  return n;
}
__device__ __forceinline__ bool hs_wait(__amdgpu_buffer_rsrc_t rs, u64 n, v2u v) {
  unsigned spins = 0;
  _Pragma("clang loop unroll(disable)") for (; spins < (kSpinCap << 2); ++spins) {
    if (__builtin_expect(__all(v[0] == (unsigned)n && v[1] == (unsigned)(n >> 32)), 1)) break;
    if (spins) __builtin_amdgcn_s_sleep(8);
    int z = 0;
    asm volatile("" : "+v"(z));
    v = __builtin_amdgcn_raw_buffer_load_b64(rs, z, OFF_ACK + (int)BIDX * 8, AUX_SC1);
  }
  return spins == 0;
}
__device__ __forceinline__ void cnt_inc(char* ws, int cnt) {
  if (olane() == 0) __hip_atomic_fetch_add((gu32*)(ws + cnt), 1u, RLX_AGENT);
}
template <int SLP>
__device__ __forceinline__ void hs_lead(__amdgpu_buffer_rsrc_t rs, bool fill, int cnt, unsigned want) {
  const int l16 = olane() * 16;
  if (fill) {
    for (int o = 0; o < (int)ARM_HOST; o += 1024) __builtin_amdgcn_raw_buffer_store_b128(v4u{~0u, ~0u, ~0u, ~0u}, rs, l16, o, AUX_SC1);
    VMCNT(0);
  }
  _Pragma("clang loop unroll(disable)") for (unsigned spins = 0; spins < (kSpinCap << 2); ++spins) {
    int va = l16;
    asm volatile("" : "+v"(va));
    const v4u a = __builtin_amdgcn_raw_buffer_load_b128(rs, va, OFF_REQ, AUX_SC1);
    const v4u b = __builtin_amdgcn_raw_buffer_load_b128(rs, va + 1024, OFF_REQ, AUX_SC1);
    const unsigned n = __builtin_amdgcn_raw_buffer_load_b32(rs, va & 0, cnt, AUX_SC1);
    if (__all(n + 1u >= want)) break;
    __builtin_amdgcn_raw_buffer_store_b128(a, rs, va, OFF_ACK, AUX_SC1);
    __builtin_amdgcn_raw_buffer_store_b128(b, rs, va + 1024, OFF_ACK, AUX_SC1);
    __builtin_amdgcn_s_sleep(SLP);
  }
}

#define PTW(i, wv)
#define PLDECL
#define PL(i)
#define PLDUMP(wv, mask)
#define PT(i) PTW(i, 0)
#define TN(k, d) (d)
#define EVDECL
#define EV(k)
#define EVDUMP(s)
#define EVCLK(s, c)
#define EVOUT
#define VO_A \
  "s_branch .Lgo_%=\n" \
  ".Lp0_%=:\n" \
  "s_mov_b32 %[cnt], 0x40000\n" \
  ".Lp0r_%=:\n" \
  "s_sleep 2\n" \
  "buffer_load_dwordx4 v[236:239], v251, %[rs], %[o0] offen sc1\n" \
  "s_waitcnt vmcnt(0)\n" \
  "ds_write_b128 v250, v[236:239] offset:0\n" \
  "v_max_u32 v241, v236, v237\n" \
  "v_max3_u32 v241, v241, v238, v239\n" \
  "v_cmp_eq_u32 vcc, -1, v241\n" \
  "s_cbranch_vccz .Lp0b_%=\n" \
  "s_sub_u32 %[cnt], %[cnt], 1\n" \
  "s_cbranch_scc0 .Lp0r_%=\n" \
  "s_branch .Lp0b_%=\n" \
  ".Lgo_%=:\n" \
  "ds_read_b128 v[200:203], v243 offset:32768\n" \
  "ds_read_b128 v[204:207], v243 offset:33792\n" \
  "ds_read_b128 v[208:211], v243 offset:49152\n" \
  "ds_read_b128 v[212:215], v243 offset:50176\n" \
  "v_mov_b64 v[192:193], 0\n" \
  "v_mov_b64 v[194:195], 0\n" \
  "v_mov_b64 v[196:197], 0\n" \
  "v_mov_b64 v[198:199], 0\n" \
  "s_waitcnt lgkmcnt(0)\n" \
  "s_barrier\n" \
  "buffer_load_dwordx4 v[236:239], v251, %[rs], %[o0] offen sc1\n" \
  "s_waitcnt vmcnt(0)\n" \
  "ds_write_b128 v250, v[236:239] offset:0\n" \
  "v_max3_u32 v241, v236, v237, v238\n" \
  "v_max_u32 v241, v241, v239\n" \
  "v_cmp_eq_u32 vcc, -1, v241\n" \
  "s_cbranch_vccnz .Lp0_%=\n" \
  ".Lp0b_%=:\n" \
  "s_waitcnt lgkmcnt(0)\n" \
  "s_barrier\n" \
  "ds_read_b128 v[216:219], v251 offset:0\n" \
  "ds_read_b128 v[220:223], v251 offset:1024\n" \
  "ds_read_b128 v[224:227], v251 offset:2048\n" \
  "ds_read_b128 v[228:231], v251 offset:3072\n" \
  "s_waitcnt lgkmcnt(3)\n" \
  "v_smfmac_f32_16x16x64_bf16 v[192:195], v[216:219], v[200:207], v248\n" \
  "v_smfmac_f32_16x16x64_bf16 v[196:199], v[216:219], v[208:215], v248\n" \
  "ds_read_b128 v[232:235], v251 offset:4096\n" \
  "ds_read_b128 v[236:239], v251 offset:5120\n" \
  "ds_read_b128 v[216:219], v251 offset:6144\n" \
  "s_waitcnt lgkmcnt(5)\n" \
  "v_smfmac_f32_16x16x64_bf16 v[192:195], v[220:223], v[0:7], v248\n" \
  "v_smfmac_f32_16x16x64_bf16 v[196:199], v[220:223], v[8:15], v248\n" \
  "ds_read_b128 v[220:223], v251 offset:7168\n" \
  "s_barrier\n" \
  "s_waitcnt lgkmcnt(5)\n" \
  "v_smfmac_f32_16x16x64_bf16 v[192:195], v[224:227], v[16:23], v248\n" \
  "v_smfmac_f32_16x16x64_bf16 v[196:199], v[224:227], v[24:31], v248\n" \
  "s_waitcnt lgkmcnt(4)\n" \
  "v_smfmac_f32_16x16x64_bf16 v[192:195], v[228:231], v[32:39], v248\n" \
  "v_smfmac_f32_16x16x64_bf16 v[196:199], v[228:231], v[40:47], v248\n" \
  "s_waitcnt lgkmcnt(3)\n" \
  "v_smfmac_f32_16x16x64_bf16 v[192:195], v[232:235], v[48:55], v248\n" \
  "v_smfmac_f32_16x16x64_bf16 v[196:199], v[232:235], v[56:63], v248\n" \
  "s_waitcnt lgkmcnt(2)\n" \
  "v_smfmac_f32_16x16x64_bf16 v[192:195], v[236:239], v[64:71], v248\n" \
  "v_smfmac_f32_16x16x64_bf16 v[196:199], v[236:239], v[72:79], v248\n" \
  "s_waitcnt lgkmcnt(1)\n" \
  "v_smfmac_f32_16x16x64_bf16 v[192:195], v[216:219], v[80:87], v248\n" \
  "s_waitcnt lgkmcnt(0)\n" \
  "v_smfmac_f32_16x16x64_bf16 v[192:195], v[220:223], v[96:103], v248\n" \
  "v_smfmac_f32_16x16x64_bf16 v[196:199], v[216:219], v[88:95], v248\n" \
  "v_smfmac_f32_16x16x64_bf16 v[196:199], v[220:223], v[104:111], v248\n" \
  "s_setprio 3\n" \
  "ds_write_b128 v250, v[192:195] offset:24576\n" \
  "s_nop 6\n" \
  "ds_write_b128 v250, v[196:199] offset:28672\n" \
  "v_mov_b64 v[200:201], 0\n" \
  "v_mov_b64 v[202:203], 0\n" \
  "v_mov_b64 v[204:205], 0\n" \
  "v_mov_b64 v[206:207], 0\n" \
  "v_mov_b64 v[208:209], 0\n" \
  "v_mov_b64 v[210:211], 0\n" \
  "v_mov_b64 v[212:213], 0\n" \
  "v_mov_b64 v[214:215], 0\n" \
  "s_waitcnt lgkmcnt(0)\n" \
  "s_setprio 0\n" \
  "s_barrier\n" \
  "s_nop 11\n" \
  "ds_read_b128 v[216:219], v249 offset:0\n" \
  "ds_read_b128 v[220:223], v249 offset:1024\n" \
  "ds_read_b128 v[224:227], v249 offset:16384\n" \
  "ds_read_b128 v[228:231], v249 offset:17408\n" \
  "s_barrier\n" \
  "ds_read_b64_tr_b16 v[232:233], v245 offset:17920\n" \
  "ds_read_b64_tr_b16 v[234:235], v245 offset:18112\n" \
  "ds_read_b64_tr_b16 v[236:237], v245 offset:18688\n" \
  "ds_read_b64_tr_b16 v[238:239], v245 offset:18880\n" \
  "ds_read_b64_tr_b16 v[192:193], v245 offset:16384\n" \
  "ds_read_b64_tr_b16 v[194:195], v245 offset:16576\n" \
  "ds_read_b64_tr_b16 v[196:197], v245 offset:17152\n" \
  "ds_read_b64_tr_b16 v[198:199], v245 offset:17344\n" \
  "s_waitcnt lgkmcnt(6)\n" \
  "v_smfmac_f32_16x16x64_bf16 v[200:203], v[232:235], v[216:223], v248 abid:1\n" \
  "ds_read_b128 v[216:219], v249 offset:32768\n" \
  "ds_read_b128 v[220:223], v249 offset:33792\n" \
  "v_smfmac_f32_16x16x64_bf16 v[204:207], v[232:235], v[128:135], v248 abid:1\n" \
  "s_waitcnt lgkmcnt(6)\n" \
  "v_smfmac_f32_16x16x64_bf16 v[200:203], v[236:239], v[224:231], v248 abid:1\n" \
  "ds_read_b128 v[224:227], v249 offset:49152\n" \
  "ds_read_b128 v[228:231], v249 offset:50176\n" \
  "v_smfmac_f32_16x16x64_bf16 v[204:207], v[236:239], v[136:143], v248 abid:1\n" \
  "s_waitcnt lgkmcnt(6)\n" \
  "v_smfmac_f32_16x16x64_bf16 v[200:203], v[192:195], v[176:183], v248 abid:1\n" \
  "v_smfmac_f32_16x16x64_bf16 v[204:207], v[192:195], v[112:119], v248 abid:1\n" \
  "s_waitcnt lgkmcnt(4)\n" \
  "v_smfmac_f32_16x16x64_bf16 v[200:203], v[196:199], v[184:191], v248 abid:1\n" \
  "v_smfmac_f32_16x16x64_bf16 v[204:207], v[196:199], v[120:127], v248 abid:1\n" \
  "s_waitcnt lgkmcnt(2)\n" \
  "v_smfmac_f32_16x16x64_bf16 v[208:211], v[192:195], v[216:223], v248 abid:1\n" \
  "ds_read_b128 v[216:219], v243 offset:0\n" \
  "ds_read_b128 v[220:223], v243 offset:1024\n" \
  "v_smfmac_f32_16x16x64_bf16 v[212:215], v[192:195], v[144:151], v248 abid:1\n" \
  "s_waitcnt lgkmcnt(2)\n" \
  "v_smfmac_f32_16x16x64_bf16 v[208:211], v[196:199], v[224:231], v248 abid:1\n" \
  "ds_read_b128 v[224:227], v243 offset:16384\n" \
  "ds_read_b128 v[228:231], v243 offset:17408\n" \
  "v_cvt_pk_bf16_f32 v200, v200, v201\n" \
  "v_smfmac_f32_16x16x64_bf16 v[212:215], v[196:199], v[152:159], v248 abid:1\n" \
  "v_cvt_pk_bf16_f32 v201, v202, v203\n" \
  "v_smfmac_f32_16x16x64_bf16 v[212:215], v[232:235], v[160:167], v248 abid:1\n" \
  "v_cvt_pk_bf16_f32 v202, v204, v205\n" \
  "v_smfmac_f32_16x16x64_bf16 v[212:215], v[236:239], v[168:175], v248 abid:1\n" \
  "v_cvt_pk_bf16_f32 v203, v206, v207\n" \
  "s_waitcnt lgkmcnt(2)\n" \
  "v_smfmac_f32_16x16x64_bf16 v[208:211], v[232:235], v[216:223], v248 abid:1\n" \
  "v_permlane16_swap_b32 v200, v202\n" \
  "s_waitcnt lgkmcnt(0)\n" \
  "v_smfmac_f32_16x16x64_bf16 v[208:211], v[236:239], v[224:231], v248 abid:1\n" \
  "v_permlane16_swap_b32 v201, v203\n" \
  "buffer_store_dwordx4 v[200:203], v247, %[rs], %[vs] offen sc1\n" \
  "v_cvt_pk_bf16_f32 v212, v212, v213\n" \
  "v_cvt_pk_bf16_f32 v213, v214, v215\n" \
  "s_nop 4\n" \
  "v_cvt_pk_bf16_f32 v211, v210, v211\n" \
  "v_cvt_pk_bf16_f32 v210, v208, v209\n" \
  "s_nop 0\n" \
  "v_permlane16_swap_b32 v211, v213\n" \
  "v_permlane16_swap_b32 v210, v212\n" \
  "buffer_store_dwordx4 v[210:213], v246, %[rs], %[vs] offen sc1\n" \
  "s_nop 1\n"
#define VO_B \
  "s_branch .Lgo_%=\n" \
  ".Lp0_%=:\n" \
  "s_mov_b32 %[cnt], 0x40000\n" \
  ".Lp0r_%=:\n" \
  "s_sleep 2\n" \
  "buffer_load_dwordx4 v[228:231], v251, %[rs], %[o0] offen sc1\n" \
  "buffer_load_dwordx4 v[232:235], v251, %[rs], %[o1] offen sc1\n" \
  "buffer_load_dwordx4 v[236:239], v251, %[rs], %[o2] offen sc1\n" \
  "s_waitcnt vmcnt(0)\n" \
  "ds_write_b128 v250, v[228:231] offset:0\n" \
  "v_max_u32 v241, v228, v229\n" \
  "v_max3_u32 v241, v241, v230, v231\n" \
  "v_cmp_eq_u32 vcc, -1, v241\n" \
  "s_cbranch_vccz .Lp0b_%=\n" \
  "s_sub_u32 %[cnt], %[cnt], 1\n" \
  "s_cbranch_scc0 .Lp0r_%=\n" \
  "s_branch .Lp0b_%=\n" \
  ".Lp1_%=:\n" \
  "s_mov_b32 %[cnt], 0x40000\n" \
  "s_branch .Lp1s_%=\n" \
  ".Lp1r_%=:\n" \
  "s_sleep 2\n" \
  ".Lp1s_%=:\n" \
  "buffer_load_dwordx4 v[232:235], v251, %[rs], %[o1] offen sc1\n" \
  "buffer_load_dwordx4 v[236:239], v251, %[rs], %[o2] offen sc1\n" \
  "s_waitcnt vmcnt(0)\n" \
  "ds_write_b128 v250, v[232:235] offset:4096\n" \
  "v_max_u32 v241, v232, v233\n" \
  "v_max3_u32 v241, v241, v234, v235\n" \
  "ds_write_b128 v250, v[236:239] offset:8192\n" \
  "v_max3_u32 v241, v241, v236, v237\n" \
  "v_max3_u32 v241, v241, v238, v239\n" \
  "v_cmp_eq_u32 vcc, -1, v241\n" \
  "s_cbranch_vccz .Lp1b_%=\n" \
  "s_sub_u32 %[cnt], %[cnt], 1\n" \
  "s_cbranch_scc0 .Lp1r_%=\n" \
  "s_branch .Lp1b_%=\n" \
  ".Lgo_%=:\n" \
  "ds_read_b128 v[200:203], v243 offset:32768\n" \
  "ds_read_b128 v[204:207], v243 offset:33792\n" \
  "ds_read_b128 v[208:211], v243 offset:49152\n" \
  "ds_read_b128 v[212:215], v243 offset:50176\n" \
  "v_mov_b64 v[192:193], 0\n" \
  "v_mov_b64 v[194:195], 0\n" \
  "v_mov_b64 v[196:197], 0\n" \
  "v_mov_b64 v[198:199], 0\n" \
  "s_waitcnt lgkmcnt(0)\n" \
  "s_cmp_eq_u32 %[tw], 0\n" \
  "s_cbranch_scc1 .Ltq_%=\n" \
  ".Ltl_%=:\n" \
  "s_memtime vcc\n" \
  "s_waitcnt lgkmcnt(0)\n" \
  "s_sub_i32 %[cnt], %[tg], vcc_lo\n" \
  "s_cmp_lt_i32 %[cnt], 260\n" \
  "s_cbranch_scc1 .Ltf_%=\n" \
  "s_sleep 1\n" \
  "s_branch .Ltl_%=\n" \
  ".Ltf_%=:\n" \
  "s_max_i32 %[cnt], %[cnt], 0\n" \
  ".Ltg_%=:\n" \
  "s_sub_u32 %[cnt], %[cnt], 28\n" \
  "s_cbranch_scc0 .Ltg_%=\n" \
  ".Ltq_%=:\n" \
  "s_barrier\n" \
  "buffer_load_dwordx4 v[228:231], v251, %[rs], %[o0] offen sc1\n" \
  "s_nop 11\n" \
  "buffer_load_dwordx4 v[232:235], v251, %[rs], %[o1] offen sc1\n" \
  "s_nop 11\n" \
  "buffer_load_dwordx4 v[236:239], v251, %[rs], %[o2] offen sc1\n" \
  "s_waitcnt vmcnt(2)\n" \
  "ds_write_b128 v250, v[228:231] offset:0\n" \
  "v_max3_u32 v241, v228, v229, v230\n" \
  "v_max_u32 v241, v241, v231\n" \
  "v_cmp_eq_u32 vcc, -1, v241\n" \
  "s_cbranch_vccnz .Lp0_%=\n" \
  ".Lp0b_%=:\n" \
  "s_waitcnt lgkmcnt(0)\n" \
  "s_barrier\n" \
  "s_setprio 3\n" \
  "s_waitcnt vmcnt(1)\n" \
  "ds_write_b128 v250, v[232:235] offset:4096\n" \
  "s_waitcnt vmcnt(0)\n" \
  "ds_write_b128 v250, v[236:239] offset:8192\n" \
  "v_max3_u32 v241, v232, v233, v234\n" \
  "v_max3_u32 v241, v241, v235, v236\n" \
  "v_max3_u32 v241, v241, v237, v238\n" \
  "v_max_u32 v241, v241, v239\n" \
  "v_cmp_eq_u32 vcc, -1, v241\n" \
  "s_cbranch_vccnz .Lp1_%=\n" \
  ".Lp1b_%=:\n" \
  "s_waitcnt lgkmcnt(0)\n" \
  "s_barrier\n" \
  "s_memtime vcc\n" \
  "s_waitcnt lgkmcnt(0)\n" \
  "ds_read_b128 v[216:219], v251 offset:8192\n" \
  "ds_read_b128 v[220:223], v251 offset:9216\n" \
  "ds_read_b128 v[224:227], v251 offset:10240\n" \
  "ds_read_b128 v[228:231], v251 offset:11264\n" \
  "ds_read_b128 v[232:235], v251 offset:12288\n" \
  "ds_read_b128 v[236:239], v251 offset:13312\n" \
  "s_sub_u32 %[cnt], vcc_lo, %[ckm]\n" \
  "s_mul_hi_u32 %[cnt], %[cnt], %[inv]\n" \
  "s_add_u32 %[cnt], %[cnt], %[r16]\n" \
  "s_and_b32 %[cnt], %[cnt], -2\n" \
  "v_mov_b32 v241, %[cnt]\n" \
  "buffer_store_dword v241, v242, %[rs], %[sst] offen sc1\n" \
  "s_setprio 0\n" \
  "s_waitcnt lgkmcnt(5)\n" \
  "v_smfmac_f32_16x16x64_bf16 v[192:195], v[216:219], v[200:207], v248\n" \
  "v_smfmac_f32_16x16x64_bf16 v[196:199], v[216:219], v[208:215], v248\n" \
  "s_waitcnt lgkmcnt(4)\n" \
  "v_smfmac_f32_16x16x64_bf16 v[192:195], v[220:223], v[0:7], v248\n" \
  "v_smfmac_f32_16x16x64_bf16 v[196:199], v[220:223], v[8:15], v248\n" \
  "ds_read_b128 v[216:219], v251 offset:14336\n" \
  "ds_read_b128 v[220:223], v251 offset:15360\n" \
  "s_waitcnt lgkmcnt(5)\n" \
  "v_smfmac_f32_16x16x64_bf16 v[192:195], v[224:227], v[16:23], v248\n" \
  "v_smfmac_f32_16x16x64_bf16 v[196:199], v[224:227], v[24:31], v248\n" \
  "s_waitcnt lgkmcnt(4)\n" \
  "v_smfmac_f32_16x16x64_bf16 v[192:195], v[228:231], v[32:39], v248\n" \
  "v_smfmac_f32_16x16x64_bf16 v[196:199], v[228:231], v[40:47], v248\n" \
  "s_waitcnt lgkmcnt(3)\n" \
  "v_smfmac_f32_16x16x64_bf16 v[192:195], v[232:235], v[48:55], v248\n" \
  "v_smfmac_f32_16x16x64_bf16 v[196:199], v[232:235], v[56:63], v248\n" \
  "s_waitcnt lgkmcnt(2)\n" \
  "v_smfmac_f32_16x16x64_bf16 v[192:195], v[236:239], v[64:71], v248\n" \
  "v_smfmac_f32_16x16x64_bf16 v[196:199], v[236:239], v[72:79], v248\n" \
  "s_barrier\n" \
  "ds_read_b128 v[224:227], v250 offset:20480\n" \
  "ds_read_b128 v[228:231], v250 offset:24576\n" \
  "s_waitcnt lgkmcnt(3)\n" \
  "v_smfmac_f32_16x16x64_bf16 v[192:195], v[216:219], v[80:87], v248\n" \
  "v_smfmac_f32_16x16x64_bf16 v[196:199], v[216:219], v[88:95], v248\n" \
  "s_waitcnt lgkmcnt(2)\n" \
  "v_smfmac_f32_16x16x64_bf16 v[192:195], v[220:223], v[96:103], v248\n" \
  "v_smfmac_f32_16x16x64_bf16 v[196:199], v[220:223], v[104:111], v248\n" \
  "s_waitcnt lgkmcnt(1)\n" \
  "v_add_f32 v224, v224, v226\n" \
  "v_add_f32 v225, v225, v227\n" \
  "s_waitcnt lgkmcnt(0)\n" \
  "v_add_f32 v228, v228, v230\n" \
  "v_add_f32 v229, v229, v231\n" \
  "v_add_f32 v192, v192, v194\n" \
  "v_add_f32 v193, v193, v195\n" \
  "v_add_f32 v192, v192, v224\n" \
  "v_add_f32 v193, v193, v225\n" \
  "v_cvt_pk_bf16_f32 v192, v192, v193\n" \
  "v_add_f32 v196, v196, v198\n" \
  "v_add_f32 v197, v197, v199\n" \
  "v_add_f32 v196, v196, v228\n" \
  "v_add_f32 v197, v197, v229\n" \
  "v_cvt_pk_bf16_f32 v196, v196, v197\n" \
  "ds_write2_b32 v244, v192, v196 offset1:96\n" \
  "s_waitcnt lgkmcnt(0)\n" \
  "s_barrier\n" \
  "v_mov_b64 v[200:201], 0\n" \
  "v_mov_b64 v[202:203], 0\n" \
  "v_mov_b64 v[204:205], 0\n" \
  "v_mov_b64 v[206:207], 0\n" \
  "v_mov_b64 v[208:209], 0\n" \
  "v_mov_b64 v[210:211], 0\n" \
  "v_mov_b64 v[212:213], 0\n" \
  "v_mov_b64 v[214:215], 0\n" \
  "ds_read_b64_tr_b16 v[232:233], v245 offset:17920\n" \
  "ds_read_b64_tr_b16 v[234:235], v245 offset:18112\n" \
  "ds_read_b64_tr_b16 v[236:237], v245 offset:18688\n" \
  "ds_read_b64_tr_b16 v[238:239], v245 offset:18880\n" \
  "ds_read_b64_tr_b16 v[192:193], v245 offset:16384\n" \
  "ds_read_b64_tr_b16 v[194:195], v245 offset:16576\n" \
  "ds_read_b64_tr_b16 v[196:197], v245 offset:17152\n" \
  "ds_read_b64_tr_b16 v[198:199], v245 offset:17344\n" \
  "ds_read_b128 v[216:219], v249 offset:0\n" \
  "ds_read_b128 v[220:223], v249 offset:1024\n" \
  "ds_read_b128 v[224:227], v249 offset:16384\n" \
  "ds_read_b128 v[228:231], v249 offset:17408\n" \
  "buffer_load_dwordx2 v[240:241], v240, %[rs], %[ss] offen sc1\n" \
  "s_waitcnt lgkmcnt(2)\n" \
  "v_smfmac_f32_16x16x64_bf16 v[200:203], v[232:235], v[216:223], v248 abid:1\n" \
  "ds_read_b128 v[216:219], v249 offset:32768\n" \
  "ds_read_b128 v[220:223], v249 offset:33792\n" \
  "v_smfmac_f32_16x16x64_bf16 v[204:207], v[232:235], v[128:135], v248 abid:1\n" \
  "s_waitcnt lgkmcnt(2)\n" \
  "v_smfmac_f32_16x16x64_bf16 v[200:203], v[236:239], v[224:231], v248 abid:1\n" \
  "ds_read_b128 v[224:227], v249 offset:49152\n" \
  "ds_read_b128 v[228:231], v249 offset:50176\n" \
  "v_smfmac_f32_16x16x64_bf16 v[204:207], v[236:239], v[136:143], v248 abid:1\n" \
  "v_smfmac_f32_16x16x64_bf16 v[200:203], v[192:195], v[176:183], v248 abid:1\n" \
  "v_smfmac_f32_16x16x64_bf16 v[204:207], v[192:195], v[112:119], v248 abid:1\n" \
  "v_smfmac_f32_16x16x64_bf16 v[200:203], v[196:199], v[184:191], v248 abid:1\n" \
  "v_smfmac_f32_16x16x64_bf16 v[204:207], v[196:199], v[120:127], v248 abid:1\n" \
  "s_waitcnt lgkmcnt(2)\n" \
  "v_smfmac_f32_16x16x64_bf16 v[208:211], v[192:195], v[216:223], v248 abid:1\n" \
  "ds_read_b128 v[216:219], v243 offset:0\n" \
  "ds_read_b128 v[220:223], v243 offset:1024\n" \
  "v_smfmac_f32_16x16x64_bf16 v[212:215], v[192:195], v[144:151], v248 abid:1\n" \
  "s_waitcnt lgkmcnt(2)\n" \
  "v_smfmac_f32_16x16x64_bf16 v[208:211], v[196:199], v[224:231], v248 abid:1\n" \
  "ds_read_b128 v[224:227], v243 offset:16384\n" \
  "ds_read_b128 v[228:231], v243 offset:17408\n" \
  "v_cvt_pk_bf16_f32 v200, v200, v201\n" \
  "v_smfmac_f32_16x16x64_bf16 v[212:215], v[196:199], v[152:159], v248 abid:1\n" \
  "v_cvt_pk_bf16_f32 v201, v202, v203\n" \
  "v_smfmac_f32_16x16x64_bf16 v[212:215], v[232:235], v[160:167], v248 abid:1\n" \
  "v_cvt_pk_bf16_f32 v202, v204, v205\n" \
  "v_smfmac_f32_16x16x64_bf16 v[212:215], v[236:239], v[168:175], v248 abid:1\n" \
  "v_cvt_pk_bf16_f32 v203, v206, v207\n" \
  "s_waitcnt lgkmcnt(2)\n" \
  "v_smfmac_f32_16x16x64_bf16 v[208:211], v[232:235], v[216:223], v248 abid:1\n" \
  "v_permlane16_swap_b32 v200, v202\n" \
  "s_waitcnt lgkmcnt(0)\n" \
  "v_smfmac_f32_16x16x64_bf16 v[208:211], v[236:239], v[224:231], v248 abid:1\n" \
  "v_permlane16_swap_b32 v201, v203\n" \
  "buffer_store_dwordx4 v[200:203], v247, %[rs], %[vs] offen sc1\n" \
  "v_cvt_pk_bf16_f32 v212, v212, v213\n" \
  "v_cvt_pk_bf16_f32 v213, v214, v215\n" \
  "s_nop 4\n" \
  "v_cvt_pk_bf16_f32 v211, v210, v211\n" \
  "v_cvt_pk_bf16_f32 v210, v208, v209\n" \
  "s_nop 0\n" \
  "v_permlane16_swap_b32 v211, v213\n" \
  "v_permlane16_swap_b32 v210, v212\n" \
  "buffer_store_dwordx4 v[210:213], v246, %[rs], %[vs] offen sc1\n" \
  "s_nop 1\n" \
  "s_waitcnt vmcnt(2)\n"
#define QK_A \
  "s_branch .Lgo_%=\n" \
  ".Lp0_%=:\n" \
  "s_mov_b32 %[cnt], 0x40000\n" \
  ".Lp0r_%=:\n" \
  "s_sleep 2\n" \
  "buffer_load_dwordx4 v[236:239], v251, %[rs], %[o0] offen sc1\n" \
  "s_waitcnt vmcnt(0)\n" \
  "ds_write_b128 v250, v[236:239] offset:0\n" \
  "v_max_u32 v241, v236, v237\n" \
  "v_max3_u32 v241, v241, v238, v239\n" \
  "v_cmp_eq_u32 vcc, -1, v241\n" \
  "s_cbranch_vccz .Lp0b_%=\n" \
  "s_sub_u32 %[cnt], %[cnt], 1\n" \
  "s_cbranch_scc0 .Lp0r_%=\n" \
  "s_branch .Lp0b_%=\n" \
  ".Lw1_%=:\n" \
  "v_xor_b32 v238, 0x1000, v242\n" \
  "ds_read_b128 v[216:219], v238 offset:0\n" \
  "ds_read_b128 v[228:231], v238 offset:1024\n" \
  "ds_read_b128 v[232:235], v238 offset:2048\n" \
  "ds_read_b128 v[200:203], v238 offset:3072\n" \
  "s_waitcnt lgkmcnt(0)\n" \
  "v_mfma_f32_16x16x32_bf16 v[216:219], v[216:219], v[192:195], 0\n" \
  "v_mfma_f32_16x16x32_bf16 v[216:219], v[228:231], v[196:199], v[216:219]\n" \
  "v_mfma_f32_16x16x32_bf16 v[216:219], v[232:235], v[208:211], v[216:219]\n" \
  "v_mfma_f32_16x16x32_bf16 v[216:219], v[200:203], v[212:215], v[216:219]\n" \
  "s_nop 9\n" \
  "v_cndmask_b32 v238, v216, v218, %[hf]\n" \
  "v_cndmask_b32 v239, v217, v219, %[hf]\n" \
  "v_max3_f32 v222, |v238|, |v239|, v221\n" \
  "v_cmp_nge_f32 vcc, 0x43aa0000, v222\n" \
  "s_cbranch_vccz .Lw1b_%=\n" \
  "s_mov_b64 %[wd], -1\n" \
  "v_max3_f32 v222, v204, v205, v238\n" \
  "v_max_f32 v222, v222, v239\n" \
  "s_nop 1\n" \
  "v_max_f32_dpp v222, v222, v222 row_ror:8 row_mask:0xf bank_mask:0xf\n" \
  "v_mov_b32 v221, v222\n" \
  "s_nop 1\n" \
  "v_permlane32_swap_b32 v222, v221\n" \
  "v_max_f32 v222, v222, v221\n" \
  "v_mov_b32 v221, v222\n" \
  "s_nop 1\n" \
  "v_permlane16_swap_b32 v222, v221\n" \
  "v_max_f32 v222, v222, v221\n" \
  "v_mul_f32 v221, 0xbe0293ee, v222\n" \
  "v_fmamk_f32 v236, v204, 0x3e0293ee, v221\n" \
  "v_fmamk_f32 v237, v205, 0x3e0293ee, v221\n" \
  "v_exp_f32 v236, v236\n" \
  "v_exp_f32 v237, v237\n" \
  "s_nop 1\n" \
  "v_cvt_pk_bf16_f32 v220, v236, v237\n" \
  "v_and_b32 v220, 0x7fffffff, v220\n" \
  "s_branch .Lw1b_%=\n" \
  ".Lw2_%=:\n" \
  "v_max3_f32 v206, v204, v205, v222\n" \
  "s_nop 1\n" \
  "v_max_f32_dpp v206, v206, v206 row_ror:8 row_mask:0xf bank_mask:0xf\n" \
  "v_mov_b32 v221, v206\n" \
  "s_nop 1\n" \
  "v_permlane32_swap_b32 v206, v221\n" \
  "v_max_f32 v206, v206, v221\n" \
  "v_mov_b32 v221, v206\n" \
  "s_nop 1\n" \
  "v_permlane16_swap_b32 v206, v221\n" \
  "v_max_f32 v206, v206, v221\n" \
  "v_mul_f32 v221, 0xbe0293ee, v206\n" \
  "v_fmamk_f32 v236, v204, 0x3e0293ee, v221\n" \
  "v_fmamk_f32 v237, v205, 0x3e0293ee, v221\n" \
  "v_fmamk_f32 v239, v222, 0x3e0293ee, v221\n" \
  "v_exp_f32 v236, v236\n" \
  "v_exp_f32 v237, v237\n" \
  "v_exp_f32 v239, v239\n" \
  "s_nop 1\n" \
  "v_fma_f32 v238, v223, v239, v236\n" \
  "v_add_f32 v238, v238, v237\n" \
  "s_nop 1\n" \
  "s_branch .Lw2b_%=\n" \
  ".Lgo_%=:\n" \
  "ds_read_b128 v[200:203], v249 offset:0\n" \
  "ds_read_b128 v[204:207], v249 offset:1024\n" \
  "ds_read_b128 v[208:211], v249 offset:16384\n" \
  "ds_read_b128 v[212:215], v249 offset:17408\n" \
  "v_mov_b32 v241, 0x7f800000\n" \
  "ds_write_b32 v250, v241 offset:22528\n" \
  "v_mov_b64 v[192:193], 0\n" \
  "v_mov_b64 v[194:195], 0\n" \
  "v_mov_b64 v[196:197], 0\n" \
  "v_mov_b64 v[198:199], 0\n" \
  "s_waitcnt lgkmcnt(0)\n" \
  "s_barrier\n" \
  "buffer_load_dwordx4 v[236:239], v251, %[rs], %[o0] offen sc1\n" \
  "s_waitcnt vmcnt(0)\n" \
  "ds_write_b128 v250, v[236:239] offset:0\n" \
  "v_max3_u32 v241, v236, v237, v238\n" \
  "v_max_u32 v241, v241, v239\n" \
  "v_cmp_eq_u32 vcc, -1, v241\n" \
  "s_cbranch_vccnz .Lp0_%=\n" \
  ".Lp0b_%=:\n" \
  "s_waitcnt lgkmcnt(0)\n" \
  "s_barrier\n" \
  "ds_read_b128 v[216:219], v251 offset:0\n" \
  "ds_read_b128 v[220:223], v251 offset:1024\n" \
  "ds_read_b128 v[224:227], v251 offset:2048\n" \
  "ds_read_b128 v[228:231], v251 offset:3072\n" \
  "s_waitcnt lgkmcnt(3)\n" \
  "v_smfmac_f32_16x16x64_bf16 v[192:195], v[216:219], v[200:207], v248\n" \
  "ds_read_b128 v[200:203], v249 offset:32768\n" \
  "ds_read_b128 v[204:207], v249 offset:33792\n" \
  "s_waitcnt lgkmcnt(4)\n" \
  "v_smfmac_f32_16x16x64_bf16 v[192:195], v[220:223], v[0:7], v248\n" \
  "ds_read_b128 v[232:235], v251 offset:4096\n" \
  "v_smfmac_f32_16x16x64_bf16 v[196:199], v[216:219], v[128:135], v248\n" \
  "ds_read_b128 v[216:219], v251 offset:5120\n" \
  "s_waitcnt lgkmcnt(5)\n" \
  "v_smfmac_f32_16x16x64_bf16 v[192:195], v[224:227], v[208:215], v248\n" \
  "ds_read_b128 v[208:211], v249 offset:49152\n" \
  "v_smfmac_f32_16x16x64_bf16 v[196:199], v[220:223], v[64:71], v248\n" \
  "ds_read_b128 v[212:215], v249 offset:50176\n" \
  "s_waitcnt lgkmcnt(6)\n" \
  "v_smfmac_f32_16x16x64_bf16 v[192:195], v[228:231], v[8:15], v248\n" \
  "ds_read_b128 v[220:223], v251 offset:6144\n" \
  "v_smfmac_f32_16x16x64_bf16 v[196:199], v[224:227], v[136:143], v248\n" \
  "ds_read_b128 v[224:227], v251 offset:7168\n" \
  "s_waitcnt lgkmcnt(5)\n" \
  "v_smfmac_f32_16x16x64_bf16 v[192:195], v[232:235], v[200:207], v248\n" \
  "ds_read_b128 v[200:203], v243 offset:0\n" \
  "s_barrier\n" \
  "v_smfmac_f32_16x16x64_bf16 v[196:199], v[228:231], v[72:79], v248\n" \
  "ds_read_b128 v[204:207], v243 offset:1024\n" \
  "s_waitcnt lgkmcnt(6)\n" \
  "v_smfmac_f32_16x16x64_bf16 v[192:195], v[216:219], v[16:23], v248\n" \
  "ds_read_b128 v[228:231], v251 offset:8192\n" \
  "v_smfmac_f32_16x16x64_bf16 v[196:199], v[232:235], v[144:151], v248\n" \
  "ds_read_b128 v[232:235], v251 offset:9216\n" \
  "s_waitcnt lgkmcnt(5)\n" \
  "v_smfmac_f32_16x16x64_bf16 v[192:195], v[220:223], v[208:215], v248\n" \
  "ds_read_b128 v[208:211], v243 offset:16384\n" \
  "v_smfmac_f32_16x16x64_bf16 v[196:199], v[216:219], v[80:87], v248\n" \
  "ds_read_b128 v[212:215], v243 offset:17408\n" \
  "s_waitcnt lgkmcnt(6)\n" \
  "v_smfmac_f32_16x16x64_bf16 v[192:195], v[224:227], v[24:31], v248\n" \
  "ds_read_b128 v[216:219], v251 offset:10240\n" \
  "v_smfmac_f32_16x16x64_bf16 v[196:199], v[220:223], v[152:159], v248\n" \
  "ds_read_b128 v[220:223], v251 offset:11264\n" \
  "s_waitcnt lgkmcnt(5)\n" \
  "v_smfmac_f32_16x16x64_bf16 v[192:195], v[228:231], v[200:207], v248\n" \
  "ds_read_b128 v[200:203], v243 offset:32768\n" \
  "v_smfmac_f32_16x16x64_bf16 v[196:199], v[224:227], v[88:95], v248\n" \
  "ds_read_b128 v[204:207], v243 offset:33792\n" \
  "s_waitcnt lgkmcnt(6)\n" \
  "v_smfmac_f32_16x16x64_bf16 v[192:195], v[232:235], v[32:39], v248\n" \
  "ds_read_b128 v[224:227], v251 offset:12288\n" \
  "v_smfmac_f32_16x16x64_bf16 v[196:199], v[228:231], v[160:167], v248\n" \
  "ds_read_b128 v[228:231], v251 offset:13312\n" \
  "s_waitcnt lgkmcnt(5)\n" \
  "v_smfmac_f32_16x16x64_bf16 v[192:195], v[216:219], v[208:215], v248\n" \
  "ds_read_b128 v[208:211], v243 offset:49152\n" \
  "ds_read_b128 v[212:215], v243 offset:50176\n" \
  "v_smfmac_f32_16x16x64_bf16 v[196:199], v[232:235], v[96:103], v248\n" \
  "ds_read_b128 v[236:239], v242 offset:2048\n" \
  "s_waitcnt lgkmcnt(7)\n" \
  "v_smfmac_f32_16x16x64_bf16 v[192:195], v[220:223], v[40:47], v248\n" \
  "ds_read_b128 v[232:235], v251 offset:14336\n" \
  "v_smfmac_f32_16x16x64_bf16 v[196:199], v[216:219], v[168:175], v248\n" \
  "ds_read_b128 v[216:219], v251 offset:15360\n" \
  "s_waitcnt lgkmcnt(6)\n" \
  "v_smfmac_f32_16x16x64_bf16 v[192:195], v[224:227], v[200:207], v248\n" \
  "ds_read_b128 v[200:203], v242 offset:0\n" \
  "v_smfmac_f32_16x16x64_bf16 v[196:199], v[220:223], v[104:111], v248\n" \
  "ds_read_b128 v[204:207], v242 offset:1024\n" \
  "s_waitcnt lgkmcnt(7)\n" \
  "v_smfmac_f32_16x16x64_bf16 v[192:195], v[228:231], v[48:55], v248\n" \
  "ds_read_b128 v[220:223], v242 offset:3072\n" \
  "v_smfmac_f32_16x16x64_bf16 v[196:199], v[224:227], v[176:183], v248\n" \
  "s_waitcnt lgkmcnt(4)\n" \
  "v_smfmac_f32_16x16x64_bf16 v[192:195], v[232:235], v[208:215], v248\n" \
  "v_smfmac_f32_16x16x64_bf16 v[196:199], v[228:231], v[112:119], v248\n" \
  "s_waitcnt lgkmcnt(3)\n" \
  "v_smfmac_f32_16x16x64_bf16 v[192:195], v[216:219], v[56:63], v248\n" \
  "v_smfmac_f32_16x16x64_bf16 v[196:199], v[232:235], v[184:191], v248\n" \
  "v_smfmac_f32_16x16x64_bf16 v[196:199], v[216:219], v[120:127], v248\n" \
  "s_nop 0\n" \
  "v_add_f32 v192, v192, v194\n" \
  "v_add_f32 v193, v193, v195\n" \
  "v_cvt_pk_bf16_f32 v192, v192, v193\n" \
  "s_nop 4\n" \
  "v_add_f32 v196, v196, v198\n" \
  "v_add_f32 v197, v197, v199\n" \
  "v_cvt_pk_bf16_f32 v196, v196, v197\n" \
  "ds_write2_b32 v247, v192, v196 offset1:96\n" \
  "v_xor_b32 v241, 0x400, v250\n" \
  "s_waitcnt lgkmcnt(0)\n" \
  "s_barrier\n" \
  "ds_read_b64_tr_b16 v[192:193], v246 offset:16384\n" \
  "ds_read_b64_tr_b16 v[194:195], v246 offset:16768\n" \
  "ds_read_b64_tr_b16 v[196:197], v246 offset:17152\n" \
  "ds_read_b64_tr_b16 v[198:199], v246 offset:17536\n" \
  "ds_read_b64_tr_b16 v[208:209], v246 offset:17920\n" \
  "ds_read_b64_tr_b16 v[210:211], v246 offset:18304\n" \
  "ds_read_b64_tr_b16 v[212:213], v246 offset:18688\n" \
  "ds_read_b64_tr_b16 v[214:215], v246 offset:19072\n" \
  "s_waitcnt lgkmcnt(6)\n" \
  "v_mfma_f32_16x16x32_bf16 v[200:203], v[200:203], v[192:195], 0\n" \
  "s_waitcnt lgkmcnt(4)\n" \
  "v_mfma_f32_16x16x32_bf16 v[200:203], v[204:207], v[196:199], v[200:203]\n" \
  "s_waitcnt lgkmcnt(2)\n" \
  "v_mfma_f32_16x16x32_bf16 v[200:203], v[236:239], v[208:211], v[200:203]\n" \
  "s_waitcnt lgkmcnt(0)\n" \
  "v_mfma_f32_16x16x32_bf16 v[200:203], v[220:223], v[212:215], v[200:203]\n" \
  "s_nop 8\n" \
  "v_cndmask_b32 v204, v200, v202, %[hf]\n" \
  "v_cndmask_b32 v205, v201, v203, %[hf]\n" \
  "v_max_f32 v221, |v204|, |v205|\n" \
  "ds_write_b32 v250, v221 offset:22528\n" \
  "v_mul_f32 v236, 0x3e0293ee, v204\n" \
  "v_mul_f32 v237, 0x3e0293ee, v205\n" \
  "v_exp_f32 v236, v236\n" \
  "v_exp_f32 v237, v237\n" \
  "ds_read_b32 v238, v241 offset:22528\n" \
  "v_cvt_pk_bf16_f32 v220, v236, v237\n" \
  "s_mov_b64 %[wd], 0\n" \
  "s_waitcnt lgkmcnt(0)\n" \
  "v_max_f32 v238, v238, v221\n" \
  "v_cmp_nge_f32 vcc, 0x43aa0000, v238\n" \
  "s_cbranch_vccnz .Lw1_%=\n" \
  ".Lw1b_%=:\n" \
  "buffer_store_dword v220, v245, %[rs], %[c0] offen sc1\n" \
  "buffer_store_dword v220, v245, %[rs], %[c1] offen sc1\n" \
  "buffer_store_dword v220, v245, %[rs], %[c2] offen sc1\n" \
  "buffer_store_dword v220, v245, %[rs], %[c3] offen sc1\n" \
  "v_add_f32 v223, v236, v237\n" \
  "ds_write_b32 v250, v223 offset:22532\n" \
  "v_cndmask_b32 v222, 0, v222, %[wd]\n" \
  "v_mov_b32 v239, 1.0\n" \
  "s_waitcnt lgkmcnt(0)\n" \
  "s_barrier\n" \
  "ds_read_b64_tr_b16 v[216:217], v246 offset:19456\n" \
  "ds_read_b64_tr_b16 v[218:219], v246 offset:19840\n" \
  "ds_read_b64_tr_b16 v[228:229], v246 offset:20224\n" \
  "ds_read_b64_tr_b16 v[230:231], v246 offset:20608\n" \
  "ds_read_b64_tr_b16 v[232:233], v246 offset:20992\n" \
  "ds_read_b64_tr_b16 v[234:235], v246 offset:21376\n" \
  "ds_read_b64_tr_b16 v[200:201], v246 offset:21760\n" \
  "ds_read_b64_tr_b16 v[202:203], v246 offset:22144\n" \
  "ds_read_b32 v238, v241 offset:22532\n" \
  "s_waitcnt lgkmcnt(7)\n" \
  "v_mfma_f32_16x16x32_bf16 v[216:219], v[216:219], v[192:195], 0\n" \
  "s_waitcnt lgkmcnt(5)\n" \
  "v_mfma_f32_16x16x32_bf16 v[216:219], v[228:231], v[196:199], v[216:219]\n" \
  "s_waitcnt lgkmcnt(3)\n" \
  "v_mfma_f32_16x16x32_bf16 v[216:219], v[232:235], v[208:211], v[216:219]\n" \
  "s_waitcnt lgkmcnt(1)\n" \
  "v_mfma_f32_16x16x32_bf16 v[216:219], v[200:203], v[212:215], v[216:219]\n" \
  "s_waitcnt lgkmcnt(0)\n" \
  "v_add_f32 v223, v223, v238\n" \
  "v_mov_b32 v238, v223\n" \
  "s_nop 1\n" \
  "v_permlane32_swap_b32 v223, v238\n" \
  "v_add_f32 v223, v223, v238\n" \
  "s_nop 1\n" \
  "v_cndmask_b32 v204, v216, v218, %[hf]\n" \
  "v_cndmask_b32 v205, v217, v219, %[hf]\n" \
  "v_mul_f32 v236, 0x3e0293ee, v204\n" \
  "v_mul_f32 v237, 0x3e0293ee, v205\n" \
  "v_exp_f32 v236, v236\n" \
  "v_exp_f32 v237, v237\n" \
  "v_max_f32 v221, |v204|, |v205|\n" \
  "v_add_f32 v238, v223, v236\n" \
  "v_cmp_nge_f32 vcc, 0x43aa0000, v221\n" \
  "v_add_f32 v238, v238, v237\n" \
  "s_or_b64 vcc, vcc, %[wd]\n" \
  "s_cbranch_scc1 .Lw2_%=\n" \
  ".Lw2b_%=:\n" \
  "v_add_f32_dpp v238, v238, v238 row_ror:8 row_mask:0xf bank_mask:0xf\n" \
  "v_mov_b32 v221, v238\n" \
  "v_cndmask_b32 v236, v239, v236, %[up]\n" \
  "v_cndmask_b32 v237, v239, v237, %[up]\n" \
  "v_permlane16_swap_b32 v238, v221\n" \
  "v_add_f32 v238, v238, v221\n" \
  "v_rcp_f32 v238, v238\n" \
  "s_nop 0\n" \
  "v_mul_f32 v236, v236, v238\n" \
  "v_mul_f32 v237, v237, v238\n" \
  "v_cvt_pk_bf16_f32 v220, v236, v237\n" \
  "v_and_b32 v220, v220, v240\n" \
  "buffer_store_dword v220, v244, %[rs], %[c0] offen sc1\n" \
  "buffer_store_dword v220, v244, %[rs], %[c1] offen sc1\n"
#define QK_X \
  "s_branch .Lgo_%=\n" \
  ".Lp0_%=:\n" \
  "s_mov_b32 %[cnt], 0x40000\n" \
  ".Lp0r_%=:\n" \
  "s_sleep 2\n" \
  "buffer_load_dwordx4 v[236:239], v251, %[rs], %[o0] offen sc1\n" \
  "s_waitcnt vmcnt(0)\n" \
  "ds_write_b128 v250, v[236:239] offset:0\n" \
  "v_max_u32 v241, v236, v237\n" \
  "v_max3_u32 v241, v241, v238, v239\n" \
  "v_cmp_eq_u32 vcc, -1, v241\n" \
  "s_cbranch_vccz .Lp0b_%=\n" \
  "s_sub_u32 %[cnt], %[cnt], 1\n" \
  "s_cbranch_scc0 .Lp0r_%=\n" \
  "s_branch .Lp0b_%=\n" \
  ".Lgo_%=:\n" \
  "ds_read_b128 v[200:203], v249 offset:0\n" \
  "ds_read_b128 v[204:207], v249 offset:1024\n" \
  "ds_read_b128 v[208:211], v249 offset:16384\n" \
  "ds_read_b128 v[212:215], v249 offset:17408\n" \
  "v_mov_b64 v[192:193], 0\n" \
  "v_mov_b64 v[194:195], 0\n" \
  "v_mov_b64 v[196:197], 0\n" \
  "v_mov_b64 v[198:199], 0\n" \
  "s_waitcnt lgkmcnt(0)\n" \
  "s_barrier\n" \
  "buffer_load_dwordx4 v[236:239], v251, %[rs], %[o0] offen sc1\n" \
  "s_waitcnt vmcnt(0)\n" \
  "ds_write_b128 v250, v[236:239] offset:0\n" \
  "v_max3_u32 v241, v236, v237, v238\n" \
  "v_max_u32 v241, v241, v239\n" \
  "v_cmp_eq_u32 vcc, -1, v241\n" \
  "s_cbranch_vccnz .Lp0_%=\n" \
  ".Lp0b_%=:\n" \
  "s_waitcnt lgkmcnt(0)\n" \
  "s_barrier\n" \
  "ds_read_b128 v[216:219], v251 offset:0\n" \
  "ds_read_b128 v[220:223], v251 offset:1024\n" \
  "ds_read_b128 v[224:227], v251 offset:2048\n" \
  "ds_read_b128 v[228:231], v251 offset:3072\n" \
  "s_waitcnt lgkmcnt(3)\n" \
  "v_smfmac_f32_16x16x64_bf16 v[192:195], v[216:219], v[200:207], v248\n" \
  "ds_read_b128 v[200:203], v249 offset:32768\n" \
  "ds_read_b128 v[204:207], v249 offset:33792\n" \
  "s_waitcnt lgkmcnt(4)\n" \
  "v_smfmac_f32_16x16x64_bf16 v[192:195], v[220:223], v[0:7], v248\n" \
  "ds_read_b128 v[232:235], v251 offset:4096\n" \
  "v_smfmac_f32_16x16x64_bf16 v[196:199], v[216:219], v[128:135], v248\n" \
  "ds_read_b128 v[216:219], v251 offset:5120\n" \
  "s_waitcnt lgkmcnt(5)\n" \
  "v_smfmac_f32_16x16x64_bf16 v[192:195], v[224:227], v[208:215], v248\n" \
  "ds_read_b128 v[208:211], v249 offset:49152\n" \
  "v_smfmac_f32_16x16x64_bf16 v[196:199], v[220:223], v[64:71], v248\n" \
  "ds_read_b128 v[212:215], v249 offset:50176\n" \
  "s_waitcnt lgkmcnt(6)\n" \
  "v_smfmac_f32_16x16x64_bf16 v[192:195], v[228:231], v[8:15], v248\n" \
  "ds_read_b128 v[220:223], v251 offset:6144\n" \
  "v_smfmac_f32_16x16x64_bf16 v[196:199], v[224:227], v[136:143], v248\n" \
  "ds_read_b128 v[224:227], v251 offset:7168\n" \
  "s_waitcnt lgkmcnt(5)\n" \
  "v_smfmac_f32_16x16x64_bf16 v[192:195], v[232:235], v[200:207], v248\n" \
  "ds_read_b128 v[200:203], v243 offset:0\n" \
  "s_barrier\n" \
  "v_smfmac_f32_16x16x64_bf16 v[196:199], v[228:231], v[72:79], v248\n" \
  "ds_read_b128 v[204:207], v243 offset:1024\n" \
  "s_waitcnt lgkmcnt(6)\n" \
  "v_smfmac_f32_16x16x64_bf16 v[192:195], v[216:219], v[16:23], v248\n" \
  "ds_read_b128 v[228:231], v251 offset:8192\n" \
  "v_smfmac_f32_16x16x64_bf16 v[196:199], v[232:235], v[144:151], v248\n" \
  "ds_read_b128 v[232:235], v251 offset:9216\n" \
  "s_waitcnt lgkmcnt(5)\n" \
  "v_smfmac_f32_16x16x64_bf16 v[192:195], v[220:223], v[208:215], v248\n" \
  "ds_read_b128 v[208:211], v243 offset:16384\n" \
  "v_smfmac_f32_16x16x64_bf16 v[196:199], v[216:219], v[80:87], v248\n" \
  "ds_read_b128 v[212:215], v243 offset:17408\n" \
  "s_waitcnt lgkmcnt(6)\n" \
  "v_smfmac_f32_16x16x64_bf16 v[192:195], v[224:227], v[24:31], v248\n" \
  "ds_read_b128 v[216:219], v251 offset:10240\n" \
  "v_smfmac_f32_16x16x64_bf16 v[196:199], v[220:223], v[152:159], v248\n" \
  "ds_read_b128 v[220:223], v251 offset:11264\n" \
  "s_waitcnt lgkmcnt(5)\n" \
  "v_smfmac_f32_16x16x64_bf16 v[192:195], v[228:231], v[200:207], v248\n" \
  "ds_read_b128 v[200:203], v243 offset:32768\n" \
  "v_smfmac_f32_16x16x64_bf16 v[196:199], v[224:227], v[88:95], v248\n" \
  "ds_read_b128 v[204:207], v243 offset:33792\n" \
  "s_waitcnt lgkmcnt(6)\n" \
  "v_smfmac_f32_16x16x64_bf16 v[192:195], v[232:235], v[32:39], v248\n" \
  "ds_read_b128 v[224:227], v251 offset:12288\n" \
  "v_smfmac_f32_16x16x64_bf16 v[196:199], v[228:231], v[160:167], v248\n" \
  "ds_read_b128 v[228:231], v251 offset:13312\n" \
  "s_waitcnt lgkmcnt(5)\n" \
  "v_smfmac_f32_16x16x64_bf16 v[192:195], v[216:219], v[208:215], v248\n" \
  "ds_read_b128 v[208:211], v243 offset:49152\n" \
  "v_smfmac_f32_16x16x64_bf16 v[196:199], v[232:235], v[96:103], v248\n" \
  "ds_read_b128 v[212:215], v243 offset:50176\n" \
  "s_waitcnt lgkmcnt(6)\n" \
  "v_smfmac_f32_16x16x64_bf16 v[192:195], v[220:223], v[40:47], v248\n" \
  "ds_read_b128 v[232:235], v251 offset:14336\n" \
  "v_smfmac_f32_16x16x64_bf16 v[196:199], v[216:219], v[168:175], v248\n" \
  "ds_read_b128 v[216:219], v251 offset:15360\n" \
  "s_waitcnt lgkmcnt(5)\n" \
  "v_smfmac_f32_16x16x64_bf16 v[192:195], v[224:227], v[200:207], v248\n" \
  "v_smfmac_f32_16x16x64_bf16 v[196:199], v[220:223], v[104:111], v248\n" \
  "s_waitcnt lgkmcnt(4)\n" \
  "v_smfmac_f32_16x16x64_bf16 v[192:195], v[228:231], v[48:55], v248\n" \
  "v_smfmac_f32_16x16x64_bf16 v[196:199], v[224:227], v[176:183], v248\n" \
  "s_waitcnt lgkmcnt(1)\n" \
  "v_smfmac_f32_16x16x64_bf16 v[192:195], v[232:235], v[208:215], v248\n" \
  "v_smfmac_f32_16x16x64_bf16 v[196:199], v[228:231], v[112:119], v248\n" \
  "s_waitcnt lgkmcnt(0)\n" \
  "v_smfmac_f32_16x16x64_bf16 v[192:195], v[216:219], v[56:63], v248\n" \
  "v_smfmac_f32_16x16x64_bf16 v[196:199], v[232:235], v[184:191], v248\n" \
  "v_smfmac_f32_16x16x64_bf16 v[196:199], v[216:219], v[120:127], v248\n" \
  "s_nop 0\n" \
  "v_add_f32 v192, v192, v194\n" \
  "v_add_f32 v193, v193, v195\n" \
  "v_cvt_pk_bf16_f32 v192, v192, v193\n" \
  "s_nop 4\n" \
  "v_add_f32 v196, v196, v198\n" \
  "v_add_f32 v197, v197, v199\n" \
  "v_cvt_pk_bf16_f32 v196, v196, v197\n" \
  "ds_write2_b32 v247, v192, v196 offset1:96\n" \
  "buffer_load_dwordx4 v[200:203], v251, %[rs], %[qo] offen offset:0 sc1\n" \
  "buffer_load_dwordx4 v[204:207], v251, %[rs], %[qo] offen offset:1024 sc1\n" \
  "buffer_load_dwordx4 v[208:211], v251, %[rs], %[qo] offen offset:2048 sc1\n" \
  "buffer_load_dwordx4 v[212:215], v251, %[rs], %[qo] offen offset:3072 sc1\n" \
  "s_waitcnt lgkmcnt(0)\n" \
  "s_barrier\n" \
  "s_nop 15\n" \
  "s_nop 15\n" \
  "s_nop 15\n" \
  "s_nop 15\n" \
  "s_nop 15\n" \
  "s_nop 9\n" \
  "ds_read_b128 v[192:195], v251 offset:24576\n" \
  "ds_read_b128 v[196:199], v251 offset:28672\n" \
  "ds_read_b128 v[216:219], v251 offset:25600\n" \
  "ds_read_b128 v[220:223], v251 offset:29696\n" \
  "ds_read_b128 v[224:227], v251 offset:26624\n" \
  "ds_read_b128 v[228:231], v251 offset:30720\n" \
  "ds_read_b128 v[232:235], v251 offset:27648\n" \
  "ds_read_b128 v[236:239], v251 offset:31744\n" \
  "s_waitcnt lgkmcnt(0)\n" \
  "s_barrier\n" \
  "s_waitcnt vmcnt(3)\n" \
  "v_mfma_f32_16x16x32_bf16 v[192:195], v[192:195], v[200:203], 0\n" \
  "v_mfma_f32_16x16x32_bf16 v[196:199], v[196:199], v[200:203], 0\n" \
  "s_waitcnt vmcnt(2)\n" \
  "v_mfma_f32_16x16x32_bf16 v[192:195], v[216:219], v[204:207], v[192:195]\n" \
  "v_mfma_f32_16x16x32_bf16 v[196:199], v[220:223], v[204:207], v[196:199]\n" \
  "s_waitcnt vmcnt(1)\n" \
  "v_mfma_f32_16x16x32_bf16 v[192:195], v[224:227], v[208:211], v[192:195]\n" \
  "v_mfma_f32_16x16x32_bf16 v[196:199], v[228:231], v[208:211], v[196:199]\n" \
  "s_waitcnt vmcnt(0)\n" \
  "v_mfma_f32_16x16x32_bf16 v[192:195], v[232:235], v[212:215], v[192:195]\n" \
  "v_mfma_f32_16x16x32_bf16 v[196:199], v[236:239], v[212:215], v[196:199]\n" \
  "s_nop 8\n"
#define QK_K \
  "s_branch .Lgo_%=\n" \
  ".Lp0_%=:\n" \
  "s_mov_b32 %[cnt], 0x40000\n" \
  ".Lp0r_%=:\n" \
  "s_sleep 2\n" \
  "buffer_load_dwordx4 v[228:231], v251, %[rs], %[o0] offen sc1\n" \
  "buffer_load_dwordx4 v[232:235], v251, %[rs], %[o1] offen sc1\n" \
  "buffer_load_dwordx4 v[236:239], v251, %[rs], %[o2] offen sc1\n" \
  "s_waitcnt vmcnt(0)\n" \
  "ds_write_b128 v250, v[228:231] offset:0\n" \
  "v_max_u32 v241, v228, v229\n" \
  "v_max3_u32 v241, v241, v230, v231\n" \
  "v_cmp_eq_u32 vcc, -1, v241\n" \
  "s_cbranch_vccz .Lp0b_%=\n" \
  "s_sub_u32 %[cnt], %[cnt], 1\n" \
  "s_cbranch_scc0 .Lp0r_%=\n" \
  "s_branch .Lp0b_%=\n" \
  ".Lp1_%=:\n" \
  "s_mov_b32 %[cnt], 0x40000\n" \
  "s_branch .Lp1s_%=\n" \
  ".Lp1r_%=:\n" \
  "s_sleep 2\n" \
  ".Lp1s_%=:\n" \
  "buffer_load_dwordx4 v[232:235], v251, %[rs], %[o1] offen sc1\n" \
  "buffer_load_dwordx4 v[236:239], v251, %[rs], %[o2] offen sc1\n" \
  "s_waitcnt vmcnt(0)\n" \
  "ds_write_b128 v250, v[232:235] offset:4096\n" \
  "v_max_u32 v241, v232, v233\n" \
  "v_max3_u32 v241, v241, v234, v235\n" \
  "ds_write_b128 v250, v[236:239] offset:8192\n" \
  "v_max3_u32 v241, v241, v236, v237\n" \
  "v_max3_u32 v241, v241, v238, v239\n" \
  "v_cmp_eq_u32 vcc, -1, v241\n" \
  "s_cbranch_vccz .Lp1b_%=\n" \
  "s_sub_u32 %[cnt], %[cnt], 1\n" \
  "s_cbranch_scc0 .Lp1r_%=\n" \
  "s_branch .Lp1b_%=\n" \
  ".Lgo_%=:\n" \
  "ds_read_b128 v[200:203], v249 offset:0\n" \
  "ds_read_b128 v[204:207], v249 offset:1024\n" \
  "ds_read_b128 v[208:211], v249 offset:16384\n" \
  "ds_read_b128 v[212:215], v249 offset:17408\n" \
  "v_mov_b64 v[192:193], 0\n" \
  "v_mov_b64 v[194:195], 0\n" \
  "v_mov_b64 v[196:197], 0\n" \
  "v_mov_b64 v[198:199], 0\n" \
  "s_waitcnt lgkmcnt(0)\n" \
  "s_cmp_eq_u32 %[tw], 0\n" \
  "s_cbranch_scc1 .Ltq_%=\n" \
  ".Ltl_%=:\n" \
  "s_memtime vcc\n" \
  "s_waitcnt lgkmcnt(0)\n" \
  "s_sub_i32 %[cnt], %[tg], vcc_lo\n" \
  "s_cmp_lt_i32 %[cnt], 260\n" \
  "s_cbranch_scc1 .Ltf_%=\n" \
  "s_sleep 1\n" \
  "s_branch .Ltl_%=\n" \
  ".Ltf_%=:\n" \
  "s_max_i32 %[cnt], %[cnt], 0\n" \
  ".Ltg_%=:\n" \
  "s_sub_u32 %[cnt], %[cnt], 28\n" \
  "s_cbranch_scc0 .Ltg_%=\n" \
  ".Ltq_%=:\n" \
  "s_barrier\n" \
  "buffer_load_dwordx4 v[228:231], v251, %[rs], %[o0] offen sc1\n" \
  "s_nop 11\n" \
  "buffer_load_dwordx4 v[232:235], v251, %[rs], %[o1] offen sc1\n" \
  "s_nop 11\n" \
  "buffer_load_dwordx4 v[236:239], v251, %[rs], %[o2] offen sc1\n" \
  "s_waitcnt vmcnt(2)\n" \
  "ds_write_b128 v250, v[228:231] offset:0\n" \
  "v_max3_u32 v241, v228, v229, v230\n" \
  "v_max_u32 v241, v241, v231\n" \
  "v_cmp_eq_u32 vcc, -1, v241\n" \
  "s_cbranch_vccnz .Lp0_%=\n" \
  ".Lp0b_%=:\n" \
  "s_waitcnt lgkmcnt(0)\n" \
  "s_barrier\n" \
  "s_setprio 3\n" \
  "s_waitcnt vmcnt(1)\n" \
  "ds_write_b128 v250, v[232:235] offset:4096\n" \
  "s_waitcnt vmcnt(0)\n" \
  "ds_write_b128 v250, v[236:239] offset:8192\n" \
  "v_max3_u32 v241, v232, v233, v234\n" \
  "v_max3_u32 v241, v241, v235, v236\n" \
  "v_max3_u32 v241, v241, v237, v238\n" \
  "v_max_u32 v241, v241, v239\n" \
  "v_cmp_eq_u32 vcc, -1, v241\n" \
  "s_cbranch_vccnz .Lp1_%=\n" \
  ".Lp1b_%=:\n" \
  "s_waitcnt lgkmcnt(0)\n" \
  "s_barrier\n" \
  "s_memtime vcc\n" \
  "s_waitcnt lgkmcnt(0)\n" \
  "s_sub_u32 %[cnt], vcc_lo, %[ckm]\n" \
  "s_mul_hi_u32 %[cnt], %[cnt], %[inv]\n" \
  "s_add_u32 %[cnt], %[cnt], %[r16]\n" \
  "s_and_b32 %[cnt], %[cnt], -2\n" \
  "v_mov_b32 v241, %[cnt]\n" \
  "buffer_store_dword v241, v245, %[rs], %[sst] offen sc1\n" \
  "s_setprio 0\n" \
  "ds_read_b128 v[216:219], v251 offset:0\n" \
  "ds_read_b128 v[220:223], v251 offset:1024\n" \
  "ds_read_b128 v[224:227], v251 offset:2048\n" \
  "ds_read_b128 v[228:231], v251 offset:3072\n" \
  "s_waitcnt lgkmcnt(3)\n" \
  "v_smfmac_f32_16x16x64_bf16 v[192:195], v[216:219], v[200:207], v248\n" \
  "ds_read_b128 v[200:203], v249 offset:32768\n" \
  "ds_read_b128 v[204:207], v249 offset:33792\n" \
  "s_waitcnt lgkmcnt(4)\n" \
  "v_smfmac_f32_16x16x64_bf16 v[192:195], v[220:223], v[0:7], v248\n" \
  "ds_read_b128 v[232:235], v251 offset:4096\n" \
  "v_smfmac_f32_16x16x64_bf16 v[196:199], v[216:219], v[128:135], v248\n" \
  "ds_read_b128 v[216:219], v251 offset:5120\n" \
  "s_waitcnt lgkmcnt(5)\n" \
  "v_smfmac_f32_16x16x64_bf16 v[192:195], v[224:227], v[208:215], v248\n" \
  "ds_read_b128 v[208:211], v249 offset:49152\n" \
  "v_smfmac_f32_16x16x64_bf16 v[196:199], v[220:223], v[64:71], v248\n" \
  "ds_read_b128 v[212:215], v249 offset:50176\n" \
  "s_waitcnt lgkmcnt(6)\n" \
  "v_smfmac_f32_16x16x64_bf16 v[192:195], v[228:231], v[8:15], v248\n" \
  "ds_read_b128 v[220:223], v251 offset:6144\n" \
  "v_smfmac_f32_16x16x64_bf16 v[196:199], v[224:227], v[136:143], v248\n" \
  "ds_read_b128 v[224:227], v251 offset:7168\n" \
  "s_waitcnt lgkmcnt(5)\n" \
  "v_smfmac_f32_16x16x64_bf16 v[192:195], v[232:235], v[200:207], v248\n" \
  "ds_read_b128 v[200:203], v243 offset:0\n" \
  "v_smfmac_f32_16x16x64_bf16 v[196:199], v[228:231], v[72:79], v248\n" \
  "buffer_load_dwordx2 v[240:241], v240, %[rs], %[ss] offen sc1\n" \
  "ds_read_b128 v[204:207], v243 offset:1024\n" \
  "s_waitcnt lgkmcnt(6)\n" \
  "v_smfmac_f32_16x16x64_bf16 v[192:195], v[216:219], v[16:23], v248\n" \
  "ds_read_b128 v[228:231], v251 offset:8192\n" \
  "s_barrier\n" \
  "v_smfmac_f32_16x16x64_bf16 v[196:199], v[232:235], v[144:151], v248\n" \
  "ds_read_b128 v[232:235], v251 offset:9216\n" \
  "s_waitcnt lgkmcnt(5)\n" \
  "v_smfmac_f32_16x16x64_bf16 v[192:195], v[220:223], v[208:215], v248\n" \
  "s_cmp_eq_u32 %[tc], 0\n" \
  "s_cbranch_scc1 .Lnt_%=\n" \
  "buffer_load_dword v242, v242, %[rs], %[tc] offen sc1\n" \
  ".Lnt_%=:\n" \
  "ds_read_b128 v[208:211], v243 offset:16384\n" \
  "v_smfmac_f32_16x16x64_bf16 v[196:199], v[216:219], v[80:87], v248\n" \
  "ds_read_b128 v[212:215], v243 offset:17408\n" \
  "s_waitcnt lgkmcnt(6)\n" \
  "v_smfmac_f32_16x16x64_bf16 v[192:195], v[224:227], v[24:31], v248\n" \
  "ds_read_b128 v[216:219], v251 offset:10240\n" \
  "v_smfmac_f32_16x16x64_bf16 v[196:199], v[220:223], v[152:159], v248\n" \
  "ds_read_b128 v[220:223], v251 offset:11264\n" \
  "s_waitcnt lgkmcnt(5)\n" \
  "v_smfmac_f32_16x16x64_bf16 v[192:195], v[228:231], v[200:207], v248\n" \
  "ds_read_b128 v[200:203], v243 offset:32768\n" \
  "v_smfmac_f32_16x16x64_bf16 v[196:199], v[224:227], v[88:95], v248\n" \
  "ds_read_b128 v[204:207], v243 offset:33792\n" \
  "s_waitcnt lgkmcnt(6)\n" \
  "v_smfmac_f32_16x16x64_bf16 v[192:195], v[232:235], v[32:39], v248\n" \
  "ds_read_b128 v[224:227], v251 offset:12288\n" \
  "v_smfmac_f32_16x16x64_bf16 v[196:199], v[228:231], v[160:167], v248\n" \
  "ds_read_b128 v[228:231], v251 offset:13312\n" \
  "s_waitcnt lgkmcnt(5)\n" \
  "v_smfmac_f32_16x16x64_bf16 v[192:195], v[216:219], v[208:215], v248\n" \
  "ds_read_b128 v[208:211], v243 offset:49152\n" \
  "v_smfmac_f32_16x16x64_bf16 v[196:199], v[232:235], v[96:103], v248\n" \
  "ds_read_b128 v[212:215], v243 offset:50176\n" \
  "s_waitcnt lgkmcnt(6)\n" \
  "v_smfmac_f32_16x16x64_bf16 v[192:195], v[220:223], v[40:47], v248\n" \
  "ds_read_b128 v[232:235], v251 offset:14336\n" \
  "v_smfmac_f32_16x16x64_bf16 v[196:199], v[216:219], v[168:175], v248\n" \
  "ds_read_b128 v[216:219], v251 offset:15360\n" \
  "s_waitcnt lgkmcnt(5)\n" \
  "v_smfmac_f32_16x16x64_bf16 v[192:195], v[224:227], v[200:207], v248\n" \
  "v_smfmac_f32_16x16x64_bf16 v[196:199], v[220:223], v[104:111], v248\n" \
  "s_waitcnt lgkmcnt(4)\n" \
  "v_smfmac_f32_16x16x64_bf16 v[192:195], v[228:231], v[48:55], v248\n" \
  "v_smfmac_f32_16x16x64_bf16 v[196:199], v[224:227], v[176:183], v248\n" \
  "s_waitcnt lgkmcnt(1)\n" \
  "v_smfmac_f32_16x16x64_bf16 v[192:195], v[232:235], v[208:215], v248\n" \
  "v_smfmac_f32_16x16x64_bf16 v[196:199], v[228:231], v[112:119], v248\n" \
  "s_waitcnt lgkmcnt(0)\n" \
  "v_smfmac_f32_16x16x64_bf16 v[192:195], v[216:219], v[56:63], v248\n" \
  "v_smfmac_f32_16x16x64_bf16 v[196:199], v[232:235], v[184:191], v248\n" \
  "v_smfmac_f32_16x16x64_bf16 v[196:199], v[216:219], v[120:127], v248\n" \
  "s_setprio 3\n" \
  "v_add_f32 v192, v192, v194\n" \
  "v_add_f32 v193, v193, v195\n" \
  "v_cvt_pk_bf16_f32 v192, v192, v193\n" \
  "s_nop 4\n" \
  "v_add_f32 v196, v196, v198\n" \
  "v_add_f32 v197, v197, v199\n" \
  "v_cvt_pk_bf16_f32 v196, v196, v197\n" \
  "ds_write2_b32 v247, v192, v196 offset1:96\n" \
  "s_waitcnt lgkmcnt(0)\n" \
  "s_barrier\n" \
  "s_setprio 0\n" \
  "s_waitcnt vmcnt(0)\n"
#define OW_F \
  "s_branch .Lgo_%=\n" \
  ".Lbad_%=:\n" \
  "s_mov_b32 %[st], 1\n" \
  "s_branch .Lend_%=\n" \
  ".Lgo_%=:\n" \
  "v_mov_b64 v[84:85], 0\n" \
  "v_mov_b64 v[86:87], 0\n" \
  "v_mov_b64 v[88:89], 0\n" \
  "v_mov_b64 v[90:91], 0\n" \
  "v_mov_b64 v[92:93], 0\n" \
  "v_mov_b64 v[94:95], 0\n" \
  "v_mov_b64 v[96:97], 0\n" \
  "v_mov_b64 v[98:99], 0\n" \
  "v_mov_b64 v[100:101], 0\n" \
  "v_mov_b64 v[102:103], 0\n" \
  "s_mov_b32 %[st], 0\n" \
  "s_barrier\n" \
  "ds_read_b32 v104, v119\n" \
  "s_nop 15\n" \
  "s_nop 15\n" \
  "s_nop 15\n" \
  "s_nop 11\n" \
  "buffer_load_dwordx4 v[0:3], v112, %[rs], %[sv1] offen offset:0 sc1\n" \
  "buffer_load_dwordx4 v[4:7], v112, %[rs], %[sv1] offen offset:1024 sc1\n" \
  "buffer_load_dwordx4 v[8:11], v112, %[rs], %[sv1] offen offset:2048 sc1\n" \
  "buffer_load_dwordx4 v[12:15], v112, %[rs], %[sv1] offen offset:3072 sc1\n" \
  "s_waitcnt lgkmcnt(0)\n" \
  "v_readfirstlane_b32 %[st], v104\n" \
  ".Ltl_%=:\n" \
  "s_memtime vcc\n" \
  "s_waitcnt lgkmcnt(0)\n" \
  "s_sub_i32 %[cnt], %[st], vcc_lo\n" \
  "s_sub_i32 vcc_hi, %[cnt], 120\n" \
  "s_cmp_lt_u32 vcc_hi, 2000\n" \
  "s_cbranch_scc1 .Ltl_%=\n" \
  "s_mov_b32 %[st], 0\n" \
  "s_max_i32 %[cnt], %[cnt], 0\n" \
  "s_min_i32 %[cnt], %[cnt], 2000\n" \
  ".Ltg_%=:\n" \
  "s_sub_u32 %[cnt], %[cnt], 28\n" \
  "s_cbranch_scc0 .Ltg_%=\n" \
  "buffer_load_dwordx4 v[16:19], v112, %[rs], %[sv2] offen offset:0 sc1\n" \
  "buffer_load_dwordx4 v[20:23], v112, %[rs], %[sv2] offen offset:1024 sc1\n" \
  "s_nop 3\n" \
  "buffer_load_dwordx4 v[80:83], v113, %[rs], %[sp] offen sc1\n" \
  "s_nop 5\n" \
  "buffer_load_dwordx4 v[64:67], v114, %[rs], %[sp1] offen offset:0 sc1\n" \
  "buffer_load_dwordx4 v[68:71], v114, %[rs], %[sp1] offen offset:1024 sc1\n" \
  "buffer_load_dwordx4 v[72:75], v114, %[rs], %[sp2] offen offset:0 sc1\n" \
  "buffer_load_dwordx4 v[24:27], v112, %[rs], %[sv2] offen offset:2048 sc1\n" \
  "buffer_load_dwordx4 v[28:31], v112, %[rs], %[sv2] offen offset:3072 sc1\n" \
  "buffer_load_dwordx4 v[76:79], v114, %[rs], %[sp2] offen offset:1024 sc1\n" \
  "s_barrier\n" \
  "ds_read_b128 v[32:35], v115 offset:0\n" \
  "ds_read_b128 v[36:39], v115 offset:1024\n" \
  "ds_read_b128 v[40:43], v115 offset:2048\n" \
  "ds_read_b128 v[44:47], v115 offset:3072\n" \
  "ds_read_b128 v[48:51], v115 offset:4096\n" \
  "ds_read_b128 v[52:55], v115 offset:5120\n" \
  "ds_read_b128 v[56:59], v115 offset:6144\n" \
  "ds_read_b128 v[60:63], v115 offset:7168\n" \
  "s_waitcnt vmcnt(6)\n" \
  "s_mov_b64 exec, %[m0]\n" \
  "v_mov_b64 v[84:85], v[80:81]\n" \
  "v_mov_b64 v[86:87], v[82:83]\n" \
  "s_mov_b64 exec, %[m1]\n" \
  "v_mov_b64 v[88:89], v[80:81]\n" \
  "v_mov_b64 v[90:91], v[82:83]\n" \
  "s_mov_b64 exec, %[m2]\n" \
  "v_mov_b64 v[92:93], v[80:81]\n" \
  "v_mov_b64 v[94:95], v[82:83]\n" \
  "s_mov_b64 exec, %[m3]\n" \
  "v_mov_b64 v[96:97], v[80:81]\n" \
  "v_mov_b64 v[98:99], v[82:83]\n" \
  "s_mov_b64 exec, -1\n" \
  "s_waitcnt vmcnt(5)\n" \
  "v_smfmac_f32_16x16x64_bf16 v[100:103], v[64:67], v[0:7], v118\n" \
  "s_waitcnt vmcnt(4)\n" \
  "v_smfmac_f32_16x16x64_bf16 v[100:103], v[68:71], v[8:15], v118\n" \
  "s_waitcnt lgkmcnt(6)\n" \
  "v_smfmac_f32_16x16x64_bf16 v[100:103], v[84:87], v[32:39], v118\n" \
  "s_waitcnt lgkmcnt(4)\n" \
  "v_smfmac_f32_16x16x64_bf16 v[100:103], v[88:91], v[40:47], v118 abid:1\n" \
  "s_waitcnt vmcnt(3)\n" \
  "v_smfmac_f32_16x16x64_bf16 v[100:103], v[72:75], v[16:23], v118\n" \
  "s_waitcnt lgkmcnt(2)\n" \
  "v_smfmac_f32_16x16x64_bf16 v[100:103], v[92:95], v[48:55], v118\n" \
  "s_waitcnt lgkmcnt(0)\n" \
  "v_smfmac_f32_16x16x64_bf16 v[100:103], v[96:99], v[56:63], v118 abid:1\n" \
  "s_waitcnt vmcnt(0)\n" \
  "v_smfmac_f32_16x16x64_bf16 v[100:103], v[76:79], v[24:31], v118\n" \
  "s_nop 8\n" \
  "v_add_f32 v100, v100, v102\n" \
  "v_add_f32 v101, v101, v103\n" \
  "v_cvt_pk_bf16_f32 v104, v100, v101\n" \
  "v_cmp_u_f32 vcc, v100, v101\n" \
  "s_nop 0\n" \
  "v_mov_b32_dpp v105, v104 quad_perm:[1,0,3,2] row_mask:0xf bank_mask:0xf\n" \
  "v_perm_b32 v104, v105, v104, v117\n" \
  "s_cbranch_vccnz .Lbad_%=\n" \
  "buffer_store_dword v104, v116, %[rs], %[sm0] offen sc1\n" \
  "buffer_store_dword v104, v116, %[rs], %[sm1] offen sc1\n" \
  "buffer_store_dword v104, v116, %[rs], %[sm2] offen sc1\n" \
  "buffer_store_dword v104, v116, %[rs], %[sm3] offen sc1\n" \
  ".Lend_%=:\n"
#define OW_E \
  "s_branch .Lgo_%=\n" \
  ".Lbad_%=:\n" \
  "s_mov_b32 %[st], 1\n" \
  "s_branch .Lend_%=\n" \
  ".Lgo_%=:\n" \
  "v_mov_b64 v[208:209], 0\n" \
  "v_mov_b64 v[210:211], 0\n" \
  "v_mov_b64 v[212:213], 0\n" \
  "v_mov_b64 v[214:215], 0\n" \
  "v_mov_b64 v[216:217], 0\n" \
  "v_mov_b64 v[218:219], 0\n" \
  "v_mov_b64 v[220:221], 0\n" \
  "v_mov_b64 v[222:223], 0\n" \
  "v_mov_b64 v[224:225], 0\n" \
  "v_mov_b64 v[226:227], 0\n" \
  "v_mov_b64 v[228:229], 0\n" \
  "v_mov_b64 v[230:231], 0\n" \
  "v_mov_b64 v[232:233], 0\n" \
  "v_mov_b64 v[234:235], 0\n" \
  "v_mov_b64 v[236:237], 0\n" \
  "v_mov_b64 v[238:239], 0\n" \
  "s_mov_b32 %[st], 0\n" \
  "s_barrier\n" \
  "buffer_load_dwordx4 v[192:195], v241, %[rs], %[se] offen offset:0 sc1\n" \
  "s_nop 11\n" \
  "buffer_load_dwordx4 v[196:199], v241, %[rs], %[se] offen offset:1024 sc1\n" \
  "s_nop 11\n" \
  "buffer_load_dwordx4 v[200:203], v241, %[rs], %[se] offen offset:2048 sc1\n" \
  "buffer_load_dwordx4 v[204:207], v241, %[rs], %[se] offen offset:3072 sc1\n" \
  "s_waitcnt vmcnt(3)\n" \
  "v_smfmac_f32_16x16x64_bf16 v[208:211], v[192:195], %[b00], v243\n" \
  "v_smfmac_f32_16x16x64_bf16 v[224:227], v[192:195], %[b10], v243\n" \
  "v_max3_u32 v240, v192, v192, v193\n" \
  "v_max3_u32 v240, v240, v194, v195\n" \
  "s_waitcnt vmcnt(2)\n" \
  "v_smfmac_f32_16x16x64_bf16 v[212:215], v[196:199], %[b01], v243\n" \
  "v_smfmac_f32_16x16x64_bf16 v[228:231], v[196:199], %[b11], v243\n" \
  "v_max3_u32 v240, v240, v196, v197\n" \
  "v_max3_u32 v240, v240, v198, v199\n" \
  "v_cvt_pk_bf16_f32 v208, v208, v209\n" \
  "v_cvt_pk_bf16_f32 v209, v210, v211\n" \
  "v_cvt_pk_bf16_f32 v224, v224, v225\n" \
  "v_cvt_pk_bf16_f32 v225, v226, v227\n" \
  "v_cvt_pk_bf16_f32 v210, v212, v213\n" \
  "v_cvt_pk_bf16_f32 v211, v214, v215\n" \
  "ds_write_b128 v242, v[208:211] offset:0\n" \
  "v_cvt_pk_bf16_f32 v226, v228, v229\n" \
  "v_cvt_pk_bf16_f32 v227, v230, v231\n" \
  "ds_write_b128 v242, v[224:227] offset:8192\n" \
  "s_waitcnt vmcnt(1)\n" \
  "v_smfmac_f32_16x16x64_bf16 v[216:219], v[200:203], %[b02], v243\n" \
  "v_smfmac_f32_16x16x64_bf16 v[232:235], v[200:203], %[b12], v243\n" \
  "s_waitcnt vmcnt(0)\n" \
  "v_smfmac_f32_16x16x64_bf16 v[220:223], v[204:207], %[b03], v243\n" \
  "v_smfmac_f32_16x16x64_bf16 v[236:239], v[204:207], %[b13], v243\n" \
  "v_cvt_pk_bf16_f32 v216, v216, v217\n" \
  "v_cvt_pk_bf16_f32 v217, v218, v219\n" \
  "v_cvt_pk_bf16_f32 v232, v232, v233\n" \
  "v_cvt_pk_bf16_f32 v233, v234, v235\n" \
  "s_nop 0\n" \
  "v_cvt_pk_bf16_f32 v218, v220, v221\n" \
  "v_cvt_pk_bf16_f32 v219, v222, v223\n" \
  "ds_write_b128 v242, v[216:219] offset:1024\n" \
  "s_nop 0\n" \
  "v_cvt_pk_bf16_f32 v234, v236, v237\n" \
  "v_cvt_pk_bf16_f32 v235, v238, v239\n" \
  "ds_write_b128 v242, v[232:235] offset:9216\n" \
  "v_max3_u32 v240, v240, v200, v201\n" \
  "v_max3_u32 v240, v240, v202, v203\n" \
  "v_max3_u32 v240, v240, v204, v205\n" \
  "v_max3_u32 v240, v240, v206, v207\n" \
  "v_cmp_eq_u32 vcc, -1, v240\n" \
  "s_cbranch_vccnz .Lbad_%=\n" \
  "s_waitcnt lgkmcnt(0)\n" \
  "s_barrier\n" \
  ".Lend_%=:\n"
#define ST_Y \
  "s_mov_b32 %[keep], m0\n" \
  "ds_read_b128 v[240:243], v250 offset:0\n" \
  "ds_read_b128 v[244:247], v250 offset:1024\n" \
  "v_mfma_f32_16x16x32_bf16 v[192:195], v[0:3], v[208:211], 0\n" \
  "v_mfma_f32_16x16x32_bf16 v[196:199], v[64:67], v[208:211], 0\n" \
  "ds_read_b128 v[208:211], v252 offset:8192\n" \
  "v_mfma_f32_16x16x32_bf16 v[200:203], v[0:3], v[212:215], 0\n" \
  "v_mfma_f32_16x16x32_bf16 v[204:207], v[64:67], v[212:215], 0\n" \
  "ds_read_b128 v[212:215], v252 offset:9216\n" \
  "v_mfma_f32_16x16x32_bf16 v[192:195], v[128:131], v[216:219], v[192:195]\n" \
  "v_mfma_f32_16x16x32_bf16 v[196:199], v[132:135], v[216:219], v[196:199]\n" \
  "ds_read_b128 v[216:219], v252 offset:10240\n" \
  "v_mfma_f32_16x16x32_bf16 v[200:203], v[128:131], v[220:223], v[200:203]\n" \
  "v_mfma_f32_16x16x32_bf16 v[204:207], v[132:135], v[220:223], v[204:207]\n" \
  "ds_read_b128 v[220:223], v252 offset:11264\n" \
  "v_mfma_f32_16x16x32_bf16 v[192:195], v[4:7], v[224:227], v[192:195]\n" \
  "v_mfma_f32_16x16x32_bf16 v[196:199], v[68:71], v[224:227], v[196:199]\n" \
  "ds_read_b128 v[224:227], v252 offset:12288\n" \
  "v_mfma_f32_16x16x32_bf16 v[200:203], v[4:7], v[228:231], v[200:203]\n" \
  "v_mfma_f32_16x16x32_bf16 v[204:207], v[68:71], v[228:231], v[204:207]\n" \
  "ds_read_b128 v[228:231], v252 offset:13312\n" \
  "s_waitcnt lgkmcnt(7)\n" \
  "v_mfma_f32_16x16x32_bf16 v[192:195], v[240:243], v[232:235], v[192:195]\n" \
  "s_waitcnt lgkmcnt(6)\n" \
  "v_mfma_f32_16x16x32_bf16 v[196:199], v[244:247], v[232:235], v[196:199]\n" \
  "ds_read_b128 v[232:235], v252 offset:14336\n" \
  "v_mfma_f32_16x16x32_bf16 v[200:203], v[240:243], v[236:239], v[200:203]\n" \
  "v_mfma_f32_16x16x32_bf16 v[204:207], v[244:247], v[236:239], v[204:207]\n" \
  "ds_read_b128 v[236:239], v252 offset:15360\n" \
  "ds_read_b128 v[240:243], v250 offset:16384\n" \
  "ds_read_b128 v[244:247], v250 offset:17408\n" \
  "s_waitcnt lgkmcnt(9)\n" \
  "v_mfma_f32_16x16x32_bf16 v[192:195], v[8:11], v[208:211], v[192:195]\n" \
  "v_mfma_f32_16x16x32_bf16 v[196:199], v[72:75], v[208:211], v[196:199]\n" \
  "s_waitcnt lgkmcnt(8)\n" \
  "v_mfma_f32_16x16x32_bf16 v[200:203], v[8:11], v[212:215], v[200:203]\n" \
  "v_mfma_f32_16x16x32_bf16 v[204:207], v[72:75], v[212:215], v[204:207]\n" \
  "s_waitcnt vmcnt(2)\n" \
  "s_waitcnt lgkmcnt(0)\n" \
  "s_barrier\n" \
  "ds_read_b128 v[208:211], v252 offset:16384\n" \
  "ds_read_b128 v[212:215], v252 offset:17408\n" \
  "s_add_u32 m0, %[mw], 0\n" \
  "s_xor_b32 %[cnt], %[sc], 512\n" \
  "buffer_load_dwordx4 v248, %[rx], %[cnt] offen lds\n" \
  "s_add_u32 m0, %[mw], 8192\n" \
  "s_xor_b32 %[cnt], %[sc], 640\n" \
  "buffer_load_dwordx4 v248, %[rx], %[cnt] offen lds\n" \
  "v_mfma_f32_16x16x32_bf16 v[192:195], v[136:139], v[216:219], v[192:195]\n" \
  "v_mfma_f32_16x16x32_bf16 v[196:199], v[140:143], v[216:219], v[196:199]\n" \
  "ds_read_b128 v[216:219], v252 offset:18432\n" \
  "v_mfma_f32_16x16x32_bf16 v[200:203], v[136:139], v[220:223], v[200:203]\n" \
  "v_mfma_f32_16x16x32_bf16 v[204:207], v[140:143], v[220:223], v[204:207]\n" \
  "ds_read_b128 v[220:223], v252 offset:19456\n" \
  "v_mfma_f32_16x16x32_bf16 v[192:195], v[12:15], v[224:227], v[192:195]\n" \
  "v_mfma_f32_16x16x32_bf16 v[196:199], v[76:79], v[224:227], v[196:199]\n" \
  "ds_read_b128 v[224:227], v252 offset:20480\n" \
  "v_mfma_f32_16x16x32_bf16 v[200:203], v[12:15], v[228:231], v[200:203]\n" \
  "v_mfma_f32_16x16x32_bf16 v[204:207], v[76:79], v[228:231], v[204:207]\n" \
  "ds_read_b128 v[228:231], v252 offset:21504\n" \
  "v_mfma_f32_16x16x32_bf16 v[192:195], v[240:243], v[232:235], v[192:195]\n" \
  "v_mfma_f32_16x16x32_bf16 v[196:199], v[244:247], v[232:235], v[196:199]\n" \
  "ds_read_b128 v[232:235], v252 offset:22528\n" \
  "v_mfma_f32_16x16x32_bf16 v[200:203], v[240:243], v[236:239], v[200:203]\n" \
  "v_mfma_f32_16x16x32_bf16 v[204:207], v[244:247], v[236:239], v[204:207]\n" \
  "ds_read_b128 v[236:239], v252 offset:23552\n" \
  "ds_read_b128 v[240:243], v250 offset:32768\n" \
  "ds_read_b128 v[244:247], v250 offset:33792\n" \
  "s_waitcnt lgkmcnt(9)\n" \
  "v_mfma_f32_16x16x32_bf16 v[192:195], v[16:19], v[208:211], v[192:195]\n" \
  "v_mfma_f32_16x16x32_bf16 v[196:199], v[80:83], v[208:211], v[196:199]\n" \
  "ds_read_b128 v[208:211], v252 offset:24576\n" \
  "s_waitcnt lgkmcnt(9)\n" \
  "v_mfma_f32_16x16x32_bf16 v[200:203], v[16:19], v[212:215], v[200:203]\n" \
  "v_mfma_f32_16x16x32_bf16 v[204:207], v[80:83], v[212:215], v[204:207]\n" \
  "ds_read_b128 v[212:215], v252 offset:25600\n" \
  "s_waitcnt lgkmcnt(9)\n" \
  "v_mfma_f32_16x16x32_bf16 v[192:195], v[144:147], v[216:219], v[192:195]\n" \
  "v_mfma_f32_16x16x32_bf16 v[196:199], v[148:151], v[216:219], v[196:199]\n" \
  "ds_read_b128 v[216:219], v252 offset:26624\n" \
  "s_waitcnt lgkmcnt(9)\n" \
  "v_mfma_f32_16x16x32_bf16 v[200:203], v[144:147], v[220:223], v[200:203]\n" \
  "v_mfma_f32_16x16x32_bf16 v[204:207], v[148:151], v[220:223], v[204:207]\n" \
  "ds_read_b128 v[220:223], v252 offset:27648\n" \
  "s_waitcnt lgkmcnt(9)\n" \
  "v_mfma_f32_16x16x32_bf16 v[192:195], v[20:23], v[224:227], v[192:195]\n" \
  "v_mfma_f32_16x16x32_bf16 v[196:199], v[84:87], v[224:227], v[196:199]\n" \
  "ds_read_b128 v[224:227], v252 offset:28672\n" \
  "s_waitcnt lgkmcnt(9)\n" \
  "v_mfma_f32_16x16x32_bf16 v[200:203], v[20:23], v[228:231], v[200:203]\n" \
  "v_mfma_f32_16x16x32_bf16 v[204:207], v[84:87], v[228:231], v[204:207]\n" \
  "ds_read_b128 v[228:231], v252 offset:29696\n" \
  "s_waitcnt lgkmcnt(7)\n" \
  "v_mfma_f32_16x16x32_bf16 v[192:195], v[240:243], v[232:235], v[192:195]\n" \
  "s_waitcnt lgkmcnt(6)\n" \
  "v_mfma_f32_16x16x32_bf16 v[196:199], v[244:247], v[232:235], v[196:199]\n" \
  "ds_read_b128 v[232:235], v252 offset:30720\n" \
  "v_mfma_f32_16x16x32_bf16 v[200:203], v[240:243], v[236:239], v[200:203]\n" \
  "v_mfma_f32_16x16x32_bf16 v[204:207], v[244:247], v[236:239], v[204:207]\n" \
  "ds_read_b128 v[236:239], v252 offset:31744\n" \
  "ds_read_b128 v[240:243], v250 offset:49152\n" \
  "ds_read_b128 v[244:247], v250 offset:50176\n" \
  "s_waitcnt lgkmcnt(9)\n" \
  "v_mfma_f32_16x16x32_bf16 v[192:195], v[24:27], v[208:211], v[192:195]\n" \
  "v_mfma_f32_16x16x32_bf16 v[196:199], v[88:91], v[208:211], v[196:199]\n" \
  "s_waitcnt lgkmcnt(8)\n" \
  "v_mfma_f32_16x16x32_bf16 v[200:203], v[24:27], v[212:215], v[200:203]\n" \
  "v_mfma_f32_16x16x32_bf16 v[204:207], v[88:91], v[212:215], v[204:207]\n" \
  "s_waitcnt vmcnt(0)\n" \
  "s_waitcnt lgkmcnt(0)\n" \
  "s_barrier\n" \
  "ds_read_b128 v[208:211], v252 offset:0\n" \
  "ds_read_b128 v[212:215], v252 offset:1024\n" \
  "s_add_u32 m0, %[mw], 16384\n" \
  "s_xor_b32 %[cnt], %[sc], 768\n" \
  "buffer_load_dwordx4 v248, %[rx], %[cnt] offen lds\n" \
  "s_add_u32 m0, %[mw], 24576\n" \
  "s_xor_b32 %[cnt], %[sc], 896\n" \
  "buffer_load_dwordx4 v248, %[rx], %[cnt] offen lds\n" \
  "v_lshl_or_b32 v253, v251, 24, %[fm]\n" \
  "buffer_store_dword v253, v253, %[rs], %[fo] offen sc1\n" \
  "v_mfma_f32_16x16x32_bf16 v[192:195], v[152:155], v[216:219], v[192:195]\n" \
  "v_mfma_f32_16x16x32_bf16 v[196:199], v[156:159], v[216:219], v[196:199]\n" \
  "ds_read_b128 v[216:219], v252 offset:2048\n" \
  "v_mfma_f32_16x16x32_bf16 v[200:203], v[152:155], v[220:223], v[200:203]\n" \
  "v_mfma_f32_16x16x32_bf16 v[204:207], v[156:159], v[220:223], v[204:207]\n" \
  "ds_read_b128 v[220:223], v252 offset:3072\n" \
  "v_mfma_f32_16x16x32_bf16 v[192:195], v[28:31], v[224:227], v[192:195]\n" \
  "v_mfma_f32_16x16x32_bf16 v[196:199], v[92:95], v[224:227], v[196:199]\n" \
  "ds_read_b128 v[224:227], v252 offset:4096\n" \
  "v_mfma_f32_16x16x32_bf16 v[200:203], v[28:31], v[228:231], v[200:203]\n" \
  "v_mfma_f32_16x16x32_bf16 v[204:207], v[92:95], v[228:231], v[204:207]\n" \
  "ds_read_b128 v[228:231], v252 offset:5120\n" \
  "v_mfma_f32_16x16x32_bf16 v[192:195], v[240:243], v[232:235], v[192:195]\n" \
  "v_mfma_f32_16x16x32_bf16 v[196:199], v[244:247], v[232:235], v[196:199]\n" \
  "ds_read_b128 v[232:235], v252 offset:6144\n" \
  "v_mfma_f32_16x16x32_bf16 v[200:203], v[240:243], v[236:239], v[200:203]\n" \
  "v_mfma_f32_16x16x32_bf16 v[204:207], v[244:247], v[236:239], v[204:207]\n" \
  "ds_read_b128 v[236:239], v252 offset:7168\n" \
  "ds_read_b128 v[240:243], v249 offset:0\n" \
  "ds_read_b128 v[244:247], v249 offset:1024\n" \
  "s_waitcnt lgkmcnt(9)\n" \
  "v_mfma_f32_16x16x32_bf16 v[192:195], v[32:35], v[208:211], v[192:195]\n" \
  "v_mfma_f32_16x16x32_bf16 v[196:199], v[96:99], v[208:211], v[196:199]\n" \
  "ds_read_b128 v[208:211], v252 offset:8192\n" \
  "s_waitcnt lgkmcnt(9)\n" \
  "v_mfma_f32_16x16x32_bf16 v[200:203], v[32:35], v[212:215], v[200:203]\n" \
  "v_mfma_f32_16x16x32_bf16 v[204:207], v[96:99], v[212:215], v[204:207]\n" \
  "ds_read_b128 v[212:215], v252 offset:9216\n" \
  "s_waitcnt lgkmcnt(9)\n" \
  "v_mfma_f32_16x16x32_bf16 v[192:195], v[160:163], v[216:219], v[192:195]\n" \
  "v_mfma_f32_16x16x32_bf16 v[196:199], v[164:167], v[216:219], v[196:199]\n" \
  "ds_read_b128 v[216:219], v252 offset:10240\n" \
  "s_waitcnt lgkmcnt(9)\n" \
  "v_mfma_f32_16x16x32_bf16 v[200:203], v[160:163], v[220:223], v[200:203]\n" \
  "v_mfma_f32_16x16x32_bf16 v[204:207], v[164:167], v[220:223], v[204:207]\n" \
  "ds_read_b128 v[220:223], v252 offset:11264\n" \
  "s_waitcnt lgkmcnt(9)\n" \
  "v_mfma_f32_16x16x32_bf16 v[192:195], v[36:39], v[224:227], v[192:195]\n" \
  "v_mfma_f32_16x16x32_bf16 v[196:199], v[100:103], v[224:227], v[196:199]\n" \
  "ds_read_b128 v[224:227], v252 offset:12288\n" \
  "s_waitcnt lgkmcnt(9)\n" \
  "v_mfma_f32_16x16x32_bf16 v[200:203], v[36:39], v[228:231], v[200:203]\n" \
  "v_mfma_f32_16x16x32_bf16 v[204:207], v[100:103], v[228:231], v[204:207]\n" \
  "ds_read_b128 v[228:231], v252 offset:13312\n" \
  "s_waitcnt lgkmcnt(7)\n" \
  "v_mfma_f32_16x16x32_bf16 v[192:195], v[240:243], v[232:235], v[192:195]\n" \
  "s_waitcnt lgkmcnt(6)\n" \
  "v_mfma_f32_16x16x32_bf16 v[196:199], v[244:247], v[232:235], v[196:199]\n" \
  "ds_read_b128 v[232:235], v252 offset:14336\n" \
  "v_mfma_f32_16x16x32_bf16 v[200:203], v[240:243], v[236:239], v[200:203]\n" \
  "v_mfma_f32_16x16x32_bf16 v[204:207], v[244:247], v[236:239], v[204:207]\n" \
  "ds_read_b128 v[236:239], v252 offset:15360\n" \
  "ds_read_b128 v[240:243], v249 offset:16384\n" \
  "ds_read_b128 v[244:247], v249 offset:17408\n" \
  "s_waitcnt lgkmcnt(9)\n" \
  "v_mfma_f32_16x16x32_bf16 v[192:195], v[40:43], v[208:211], v[192:195]\n" \
  "v_mfma_f32_16x16x32_bf16 v[196:199], v[104:107], v[208:211], v[196:199]\n" \
  "s_waitcnt lgkmcnt(8)\n" \
  "v_mfma_f32_16x16x32_bf16 v[200:203], v[40:43], v[212:215], v[200:203]\n" \
  "v_mfma_f32_16x16x32_bf16 v[204:207], v[104:107], v[212:215], v[204:207]\n" \
  "s_waitcnt vmcnt(1)\n" \
  "s_waitcnt lgkmcnt(0)\n" \
  "s_barrier\n" \
  "ds_read_b128 v[208:211], v252 offset:16384\n" \
  "ds_read_b128 v[212:215], v252 offset:17408\n" \
  "s_add_u32 m0, %[mw], 0\n" \
  "s_xor_b32 %[cnt], %[sn], 0\n" \
  "buffer_load_dwordx4 v248, %[rx], %[cnt] offen lds\n" \
  "s_add_u32 m0, %[mw], 8192\n" \
  "s_xor_b32 %[cnt], %[sn], 128\n" \
  "buffer_load_dwordx4 v248, %[rx], %[cnt] offen lds\n" \
  "v_lshl_or_b32 v253, v251, 3, %[tm]\n" \
  "buffer_load_dword v253, v253, %[rx], %[tc] offen\n" \
  "v_mfma_f32_16x16x32_bf16 v[192:195], v[168:171], v[216:219], v[192:195]\n" \
  "v_mfma_f32_16x16x32_bf16 v[196:199], v[172:175], v[216:219], v[196:199]\n" \
  "ds_read_b128 v[216:219], v252 offset:18432\n" \
  "v_mfma_f32_16x16x32_bf16 v[200:203], v[168:171], v[220:223], v[200:203]\n" \
  "v_mfma_f32_16x16x32_bf16 v[204:207], v[172:175], v[220:223], v[204:207]\n" \
  "ds_read_b128 v[220:223], v252 offset:19456\n" \
  "v_mfma_f32_16x16x32_bf16 v[192:195], v[44:47], v[224:227], v[192:195]\n" \
  "v_mfma_f32_16x16x32_bf16 v[196:199], v[108:111], v[224:227], v[196:199]\n" \
  "ds_read_b128 v[224:227], v252 offset:20480\n" \
  "v_mfma_f32_16x16x32_bf16 v[200:203], v[44:47], v[228:231], v[200:203]\n" \
  "v_mfma_f32_16x16x32_bf16 v[204:207], v[108:111], v[228:231], v[204:207]\n" \
  "ds_read_b128 v[228:231], v252 offset:21504\n" \
  "v_mfma_f32_16x16x32_bf16 v[192:195], v[240:243], v[232:235], v[192:195]\n" \
  "v_mfma_f32_16x16x32_bf16 v[196:199], v[244:247], v[232:235], v[196:199]\n" \
  "ds_read_b128 v[232:235], v252 offset:22528\n" \
  "v_mfma_f32_16x16x32_bf16 v[200:203], v[240:243], v[236:239], v[200:203]\n" \
  "v_mfma_f32_16x16x32_bf16 v[204:207], v[244:247], v[236:239], v[204:207]\n" \
  "ds_read_b128 v[236:239], v252 offset:23552\n" \
  "ds_read_b128 v[240:243], v249 offset:32768\n" \
  "ds_read_b128 v[244:247], v249 offset:33792\n" \
  "s_waitcnt lgkmcnt(9)\n" \
  "v_mfma_f32_16x16x32_bf16 v[192:195], v[48:51], v[208:211], v[192:195]\n" \
  "v_mfma_f32_16x16x32_bf16 v[196:199], v[112:115], v[208:211], v[196:199]\n" \
  "ds_read_b128 v[208:211], v252 offset:24576\n" \
  "s_waitcnt lgkmcnt(9)\n" \
  "v_mfma_f32_16x16x32_bf16 v[200:203], v[48:51], v[212:215], v[200:203]\n" \
  "v_mfma_f32_16x16x32_bf16 v[204:207], v[112:115], v[212:215], v[204:207]\n" \
  "ds_read_b128 v[212:215], v252 offset:25600\n" \
  "s_waitcnt lgkmcnt(9)\n" \
  "v_mfma_f32_16x16x32_bf16 v[192:195], v[176:179], v[216:219], v[192:195]\n" \
  "v_mfma_f32_16x16x32_bf16 v[196:199], v[180:183], v[216:219], v[196:199]\n" \
  "ds_read_b128 v[216:219], v252 offset:26624\n" \
  "s_waitcnt lgkmcnt(9)\n" \
  "v_mfma_f32_16x16x32_bf16 v[200:203], v[176:179], v[220:223], v[200:203]\n" \
  "v_mfma_f32_16x16x32_bf16 v[204:207], v[180:183], v[220:223], v[204:207]\n" \
  "ds_read_b128 v[220:223], v252 offset:27648\n" \
  "s_waitcnt lgkmcnt(9)\n" \
  "v_mfma_f32_16x16x32_bf16 v[192:195], v[52:55], v[224:227], v[192:195]\n" \
  "v_mfma_f32_16x16x32_bf16 v[196:199], v[116:119], v[224:227], v[196:199]\n" \
  "ds_read_b128 v[224:227], v252 offset:28672\n" \
  "s_waitcnt lgkmcnt(9)\n" \
  "v_mfma_f32_16x16x32_bf16 v[200:203], v[52:55], v[228:231], v[200:203]\n" \
  "v_mfma_f32_16x16x32_bf16 v[204:207], v[116:119], v[228:231], v[204:207]\n" \
  "ds_read_b128 v[228:231], v252 offset:29696\n" \
  "s_waitcnt lgkmcnt(7)\n" \
  "v_mfma_f32_16x16x32_bf16 v[192:195], v[240:243], v[232:235], v[192:195]\n" \
  "s_waitcnt lgkmcnt(6)\n" \
  "v_mfma_f32_16x16x32_bf16 v[196:199], v[244:247], v[232:235], v[196:199]\n" \
  "ds_read_b128 v[232:235], v252 offset:30720\n" \
  "v_mfma_f32_16x16x32_bf16 v[200:203], v[240:243], v[236:239], v[200:203]\n" \
  "v_mfma_f32_16x16x32_bf16 v[204:207], v[244:247], v[236:239], v[204:207]\n" \
  "ds_read_b128 v[236:239], v252 offset:31744\n" \
  "ds_read_b128 v[240:243], v249 offset:49152\n" \
  "ds_read_b128 v[244:247], v249 offset:50176\n" \
  "s_waitcnt lgkmcnt(9)\n" \
  "v_mfma_f32_16x16x32_bf16 v[192:195], v[56:59], v[208:211], v[192:195]\n" \
  "v_mfma_f32_16x16x32_bf16 v[196:199], v[120:123], v[208:211], v[196:199]\n" \
  "s_waitcnt lgkmcnt(8)\n" \
  "v_mfma_f32_16x16x32_bf16 v[200:203], v[56:59], v[212:215], v[200:203]\n" \
  "v_mfma_f32_16x16x32_bf16 v[204:207], v[120:123], v[212:215], v[204:207]\n" \
  "s_waitcnt vmcnt(1)\n" \
  "s_waitcnt lgkmcnt(0)\n" \
  "s_barrier\n" \
  "ds_read_b128 v[208:211], v252 offset:0\n" \
  "ds_read_b128 v[212:215], v252 offset:1024\n" \
  "s_add_u32 m0, %[mw], 16384\n" \
  "s_xor_b32 %[cnt], %[sn], 256\n" \
  "buffer_load_dwordx4 v248, %[rx], %[cnt] offen lds\n" \
  "s_add_u32 m0, %[mw], 24576\n" \
  "s_xor_b32 %[cnt], %[sn], 384\n" \
  "buffer_load_dwordx4 v248, %[rx], %[cnt] offen lds\n" \
  "v_mfma_f32_16x16x32_bf16 v[192:195], v[184:187], v[216:219], v[192:195]\n" \
  "v_mfma_f32_16x16x32_bf16 v[196:199], v[188:191], v[216:219], v[196:199]\n" \
  "ds_read_b128 v[216:219], v252 offset:2048\n" \
  "v_mfma_f32_16x16x32_bf16 v[200:203], v[184:187], v[220:223], v[200:203]\n" \
  "v_mfma_f32_16x16x32_bf16 v[204:207], v[188:191], v[220:223], v[204:207]\n" \
  "ds_read_b128 v[220:223], v252 offset:3072\n" \
  "v_mfma_f32_16x16x32_bf16 v[192:195], v[60:63], v[224:227], v[192:195]\n" \
  "v_mfma_f32_16x16x32_bf16 v[196:199], v[124:127], v[224:227], v[196:199]\n" \
  "ds_read_b128 v[224:227], v252 offset:4096\n" \
  "v_mfma_f32_16x16x32_bf16 v[200:203], v[60:63], v[228:231], v[200:203]\n" \
  "v_mfma_f32_16x16x32_bf16 v[204:207], v[124:127], v[228:231], v[204:207]\n" \
  "ds_read_b128 v[228:231], v252 offset:5120\n" \
  "v_mfma_f32_16x16x32_bf16 v[192:195], v[240:243], v[232:235], v[192:195]\n" \
  "v_mfma_f32_16x16x32_bf16 v[196:199], v[244:247], v[232:235], v[196:199]\n" \
  "ds_read_b128 v[232:235], v252 offset:6144\n" \
  "v_mfma_f32_16x16x32_bf16 v[200:203], v[240:243], v[236:239], v[200:203]\n" \
  "v_mfma_f32_16x16x32_bf16 v[204:207], v[244:247], v[236:239], v[204:207]\n" \
  "ds_read_b128 v[236:239], v252 offset:7168\n" \
  "v_cvt_pk_bf16_f32 v192, v192, v193\n" \
  "v_cvt_pk_bf16_f32 v193, v194, v195\n" \
  "v_cvt_pk_bf16_f32 v194, v196, v197\n" \
  "v_cvt_pk_bf16_f32 v195, v198, v199\n" \
  "buffer_store_dwordx4 v[192:195], v251, %[rs], %[so] offen sc1\n" \
  "v_cvt_pk_bf16_f32 v200, v200, v201\n" \
  "v_cvt_pk_bf16_f32 v201, v202, v203\n" \
  "s_nop 0\n" \
  "v_cvt_pk_bf16_f32 v202, v204, v205\n" \
  "v_cvt_pk_bf16_f32 v203, v206, v207\n" \
  "s_add_u32 %[cnt], %[so], %c[rt]\n" \
  "buffer_store_dwordx4 v[200:203], v251, %[rs], %[cnt] offen sc1\n" \
  "s_mov_b32 m0, %[keep]\n" \
  "s_waitcnt lgkmcnt(0)\n"
#define ST_XA \
  "s_mov_b32 %[keep], m0\n" \
  "ds_read_b128 v[240:243], v250 offset:0\n" \
  "ds_read_b128 v[244:247], v250 offset:1024\n" \
  "v_mfma_f32_16x16x32_bf16 v[192:195], v[0:3], v[208:211], 0\n" \
  "v_mfma_f32_16x16x32_bf16 v[196:199], v[64:67], v[208:211], 0\n" \
  "ds_read_b128 v[208:211], v252 offset:8192\n" \
  "v_mfma_f32_16x16x32_bf16 v[200:203], v[0:3], v[212:215], 0\n" \
  "v_mfma_f32_16x16x32_bf16 v[204:207], v[64:67], v[212:215], 0\n" \
  "ds_read_b128 v[212:215], v252 offset:9216\n" \
  "v_mfma_f32_16x16x32_bf16 v[192:195], v[128:131], v[216:219], v[192:195]\n" \
  "v_mfma_f32_16x16x32_bf16 v[196:199], v[132:135], v[216:219], v[196:199]\n" \
  "ds_read_b128 v[216:219], v252 offset:10240\n" \
  "v_mfma_f32_16x16x32_bf16 v[200:203], v[128:131], v[220:223], v[200:203]\n" \
  "v_mfma_f32_16x16x32_bf16 v[204:207], v[132:135], v[220:223], v[204:207]\n" \
  "ds_read_b128 v[220:223], v252 offset:11264\n" \
  "v_mfma_f32_16x16x32_bf16 v[192:195], v[4:7], v[224:227], v[192:195]\n" \
  "v_mfma_f32_16x16x32_bf16 v[196:199], v[68:71], v[224:227], v[196:199]\n" \
  "v_mfma_f32_16x16x32_bf16 v[200:203], v[4:7], v[228:231], v[200:203]\n" \
  "v_mfma_f32_16x16x32_bf16 v[204:207], v[68:71], v[228:231], v[204:207]\n" \
  "s_waitcnt vmcnt(0)\n" \
  "s_waitcnt lgkmcnt(0)\n" \
  "s_barrier\n" \
  "ds_read_b128 v[224:227], v252 offset:12288\n" \
  "ds_read_b128 v[228:231], v252 offset:13312\n" \
  "s_add_u32 m0, %[mw], 0\n" \
  "s_xor_b32 %[cnt], %[sc], 384\n" \
  "buffer_load_dwordx4 v248, %[rx], %[cnt] offen lds\n" \
  "s_add_u32 m0, %[mw], 4096\n" \
  "s_xor_b32 %[cnt], %[sc], 448\n" \
  "buffer_load_dwordx4 v248, %[rx], %[cnt] offen lds\n" \
  "v_mfma_f32_16x16x32_bf16 v[192:195], v[240:243], v[232:235], v[192:195]\n" \
  "v_mfma_f32_16x16x32_bf16 v[196:199], v[244:247], v[232:235], v[196:199]\n" \
  "ds_read_b128 v[232:235], v252 offset:14336\n" \
  "v_mfma_f32_16x16x32_bf16 v[200:203], v[240:243], v[236:239], v[200:203]\n" \
  "v_mfma_f32_16x16x32_bf16 v[204:207], v[244:247], v[236:239], v[204:207]\n" \
  "ds_read_b128 v[236:239], v252 offset:15360\n" \
  "ds_read_b128 v[240:243], v250 offset:16384\n" \
  "ds_read_b128 v[244:247], v250 offset:17408\n" \
  "v_mfma_f32_16x16x32_bf16 v[192:195], v[8:11], v[208:211], v[192:195]\n" \
  "v_mfma_f32_16x16x32_bf16 v[196:199], v[72:75], v[208:211], v[196:199]\n" \
  "ds_read_b128 v[208:211], v252 offset:16384\n" \
  "v_mfma_f32_16x16x32_bf16 v[200:203], v[8:11], v[212:215], v[200:203]\n" \
  "v_mfma_f32_16x16x32_bf16 v[204:207], v[72:75], v[212:215], v[204:207]\n" \
  "ds_read_b128 v[212:215], v252 offset:17408\n" \
  "v_mfma_f32_16x16x32_bf16 v[192:195], v[136:139], v[216:219], v[192:195]\n" \
  "v_mfma_f32_16x16x32_bf16 v[196:199], v[140:143], v[216:219], v[196:199]\n" \
  "ds_read_b128 v[216:219], v252 offset:18432\n" \
  "v_mfma_f32_16x16x32_bf16 v[200:203], v[136:139], v[220:223], v[200:203]\n" \
  "v_mfma_f32_16x16x32_bf16 v[204:207], v[140:143], v[220:223], v[204:207]\n" \
  "ds_read_b128 v[220:223], v252 offset:19456\n" \
  "s_waitcnt lgkmcnt(9)\n" \
  "v_mfma_f32_16x16x32_bf16 v[192:195], v[12:15], v[224:227], v[192:195]\n" \
  "v_mfma_f32_16x16x32_bf16 v[196:199], v[76:79], v[224:227], v[196:199]\n" \
  "ds_read_b128 v[224:227], v252 offset:20480\n" \
  "s_waitcnt lgkmcnt(9)\n" \
  "v_mfma_f32_16x16x32_bf16 v[200:203], v[12:15], v[228:231], v[200:203]\n" \
  "v_mfma_f32_16x16x32_bf16 v[204:207], v[76:79], v[228:231], v[204:207]\n" \
  "ds_read_b128 v[228:231], v252 offset:21504\n" \
  "s_waitcnt lgkmcnt(7)\n" \
  "v_mfma_f32_16x16x32_bf16 v[192:195], v[240:243], v[232:235], v[192:195]\n" \
  "s_waitcnt lgkmcnt(6)\n" \
  "v_mfma_f32_16x16x32_bf16 v[196:199], v[244:247], v[232:235], v[196:199]\n" \
  "ds_read_b128 v[232:235], v252 offset:22528\n" \
  "v_mfma_f32_16x16x32_bf16 v[200:203], v[240:243], v[236:239], v[200:203]\n" \
  "v_mfma_f32_16x16x32_bf16 v[204:207], v[244:247], v[236:239], v[204:207]\n" \
  "ds_read_b128 v[236:239], v252 offset:23552\n" \
  "ds_read_b128 v[240:243], v250 offset:32768\n" \
  "ds_read_b128 v[244:247], v250 offset:33792\n" \
  "s_waitcnt lgkmcnt(9)\n" \
  "v_mfma_f32_16x16x32_bf16 v[192:195], v[16:19], v[208:211], v[192:195]\n" \
  "v_mfma_f32_16x16x32_bf16 v[196:199], v[80:83], v[208:211], v[196:199]\n" \
  "s_waitcnt lgkmcnt(8)\n" \
  "v_mfma_f32_16x16x32_bf16 v[200:203], v[16:19], v[212:215], v[200:203]\n" \
  "v_mfma_f32_16x16x32_bf16 v[204:207], v[80:83], v[212:215], v[204:207]\n" \
  "s_waitcnt vmcnt(0)\n" \
  "s_waitcnt lgkmcnt(0)\n" \
  "s_barrier\n" \
  "ds_read_b128 v[208:211], v252 offset:0\n" \
  "ds_read_b128 v[212:215], v252 offset:1024\n" \
  "s_add_u32 m0, %[mw], 12288\n" \
  "s_xor_b32 %[cnt], %[sc], 512\n" \
  "buffer_load_dwordx4 v248, %[rx], %[cnt] offen lds\n" \
  "s_add_u32 m0, %[mw], 16384\n" \
  "s_xor_b32 %[cnt], %[sc], 576\n" \
  "buffer_load_dwordx4 v248, %[rx], %[cnt] offen lds\n" \
  "s_add_u32 m0, %[mw], 20480\n" \
  "s_xor_b32 %[cnt], %[sc], 640\n" \
  "buffer_load_dwordx4 v248, %[rx], %[cnt] offen lds\n" \
  "v_mfma_f32_16x16x32_bf16 v[192:195], v[144:147], v[216:219], v[192:195]\n" \
  "v_mfma_f32_16x16x32_bf16 v[196:199], v[148:151], v[216:219], v[196:199]\n" \
  "ds_read_b128 v[216:219], v252 offset:2048\n" \
  "v_mfma_f32_16x16x32_bf16 v[200:203], v[144:147], v[220:223], v[200:203]\n" \
  "v_mfma_f32_16x16x32_bf16 v[204:207], v[148:151], v[220:223], v[204:207]\n" \
  "ds_read_b128 v[220:223], v252 offset:3072\n" \
  "v_mfma_f32_16x16x32_bf16 v[192:195], v[20:23], v[224:227], v[192:195]\n" \
  "v_mfma_f32_16x16x32_bf16 v[196:199], v[84:87], v[224:227], v[196:199]\n" \
  "ds_read_b128 v[224:227], v252 offset:4096\n" \
  "v_mfma_f32_16x16x32_bf16 v[200:203], v[20:23], v[228:231], v[200:203]\n" \
  "v_mfma_f32_16x16x32_bf16 v[204:207], v[84:87], v[228:231], v[204:207]\n" \
  "ds_read_b128 v[228:231], v252 offset:5120\n" \
  "v_mfma_f32_16x16x32_bf16 v[192:195], v[240:243], v[232:235], v[192:195]\n" \
  "v_mfma_f32_16x16x32_bf16 v[196:199], v[244:247], v[232:235], v[196:199]\n" \
  "ds_read_b128 v[232:235], v252 offset:6144\n" \
  "v_mfma_f32_16x16x32_bf16 v[200:203], v[240:243], v[236:239], v[200:203]\n" \
  "v_mfma_f32_16x16x32_bf16 v[204:207], v[244:247], v[236:239], v[204:207]\n" \
  "ds_read_b128 v[236:239], v252 offset:7168\n" \
  "ds_read_b128 v[240:243], v250 offset:49152\n" \
  "ds_read_b128 v[244:247], v250 offset:50176\n" \
  "s_waitcnt lgkmcnt(9)\n" \
  "v_mfma_f32_16x16x32_bf16 v[192:195], v[24:27], v[208:211], v[192:195]\n" \
  "v_mfma_f32_16x16x32_bf16 v[196:199], v[88:91], v[208:211], v[196:199]\n" \
  "s_waitcnt lgkmcnt(8)\n" \
  "v_mfma_f32_16x16x32_bf16 v[200:203], v[24:27], v[212:215], v[200:203]\n" \
  "v_mfma_f32_16x16x32_bf16 v[204:207], v[88:91], v[212:215], v[204:207]\n" \
  "s_waitcnt vmcnt(0)\n" \
  "s_waitcnt lgkmcnt(0)\n" \
  "s_barrier\n" \
  "ds_read_b128 v[208:211], v252 offset:12288\n" \
  "ds_read_b128 v[212:215], v252 offset:13312\n" \
  "s_add_u32 m0, %[mw], 0\n" \
  "s_xor_b32 %[cnt], %[sc], 704\n" \
  "buffer_load_dwordx4 v248, %[rx], %[cnt] offen lds\n" \
  "s_add_u32 m0, %[mw], 4096\n" \
  "s_xor_b32 %[cnt], %[sc], 768\n" \
  "buffer_load_dwordx4 v248, %[rx], %[cnt] offen lds\n" \
  "s_add_u32 m0, %[mw], 8192\n" \
  "s_xor_b32 %[cnt], %[sc], 832\n" \
  "buffer_load_dwordx4 v248, %[rx], %[cnt] offen lds\n" \
  "v_lshl_or_b32 v253, v251, 3, %[tm]\n" \
  "buffer_load_dword v253, v253, %[rx], %[tc] offen\n" \
  "v_mfma_f32_16x16x32_bf16 v[192:195], v[152:155], v[216:219], v[192:195]\n" \
  "v_mfma_f32_16x16x32_bf16 v[196:199], v[156:159], v[216:219], v[196:199]\n" \
  "ds_read_b128 v[216:219], v252 offset:14336\n" \
  "v_mfma_f32_16x16x32_bf16 v[200:203], v[152:155], v[220:223], v[200:203]\n" \
  "v_mfma_f32_16x16x32_bf16 v[204:207], v[156:159], v[220:223], v[204:207]\n" \
  "ds_read_b128 v[220:223], v252 offset:15360\n" \
  "v_mfma_f32_16x16x32_bf16 v[192:195], v[28:31], v[224:227], v[192:195]\n" \
  "v_mfma_f32_16x16x32_bf16 v[196:199], v[92:95], v[224:227], v[196:199]\n" \
  "ds_read_b128 v[224:227], v252 offset:16384\n" \
  "v_mfma_f32_16x16x32_bf16 v[200:203], v[28:31], v[228:231], v[200:203]\n" \
  "v_mfma_f32_16x16x32_bf16 v[204:207], v[92:95], v[228:231], v[204:207]\n" \
  "ds_read_b128 v[228:231], v252 offset:17408\n" \
  "v_mfma_f32_16x16x32_bf16 v[192:195], v[240:243], v[232:235], v[192:195]\n" \
  "v_mfma_f32_16x16x32_bf16 v[196:199], v[244:247], v[232:235], v[196:199]\n" \
  "ds_read_b128 v[232:235], v252 offset:18432\n" \
  "v_mfma_f32_16x16x32_bf16 v[200:203], v[240:243], v[236:239], v[200:203]\n" \
  "v_mfma_f32_16x16x32_bf16 v[204:207], v[244:247], v[236:239], v[204:207]\n" \
  "ds_read_b128 v[236:239], v252 offset:19456\n" \
  "ds_read_b128 v[240:243], v249 offset:0\n" \
  "ds_read_b128 v[244:247], v249 offset:1024\n" \
  "s_waitcnt lgkmcnt(9)\n" \
  "v_mfma_f32_16x16x32_bf16 v[192:195], v[32:35], v[208:211], v[192:195]\n" \
  "v_mfma_f32_16x16x32_bf16 v[196:199], v[96:99], v[208:211], v[196:199]\n" \
  "ds_read_b128 v[208:211], v252 offset:20480\n" \
  "s_waitcnt lgkmcnt(9)\n" \
  "v_mfma_f32_16x16x32_bf16 v[200:203], v[32:35], v[212:215], v[200:203]\n" \
  "v_mfma_f32_16x16x32_bf16 v[204:207], v[96:99], v[212:215], v[204:207]\n" \
  "ds_read_b128 v[212:215], v252 offset:21504\n" \
  "s_waitcnt lgkmcnt(9)\n" \
  "v_mfma_f32_16x16x32_bf16 v[192:195], v[160:163], v[216:219], v[192:195]\n" \
  "v_mfma_f32_16x16x32_bf16 v[196:199], v[164:167], v[216:219], v[196:199]\n" \
  "ds_read_b128 v[216:219], v252 offset:22528\n" \
  "s_waitcnt lgkmcnt(9)\n" \
  "v_mfma_f32_16x16x32_bf16 v[200:203], v[160:163], v[220:223], v[200:203]\n" \
  "v_mfma_f32_16x16x32_bf16 v[204:207], v[164:167], v[220:223], v[204:207]\n" \
  "ds_read_b128 v[220:223], v252 offset:23552\n" \
  "s_waitcnt lgkmcnt(9)\n" \
  "v_mfma_f32_16x16x32_bf16 v[192:195], v[36:39], v[224:227], v[192:195]\n" \
  "v_mfma_f32_16x16x32_bf16 v[196:199], v[100:103], v[224:227], v[196:199]\n" \
  "s_waitcnt lgkmcnt(8)\n" \
  "v_mfma_f32_16x16x32_bf16 v[200:203], v[36:39], v[228:231], v[200:203]\n" \
  "v_mfma_f32_16x16x32_bf16 v[204:207], v[100:103], v[228:231], v[204:207]\n" \
  "s_waitcnt vmcnt(1)\n" \
  "s_waitcnt lgkmcnt(0)\n" \
  "s_barrier\n" \
  "ds_read_b128 v[224:227], v252 offset:0\n" \
  "ds_read_b128 v[228:231], v252 offset:1024\n" \
  "s_add_u32 m0, %[mw], 12288\n" \
  "s_xor_b32 %[cnt], %[sc], 896\n" \
  "buffer_load_dwordx4 v248, %[rx], %[cnt] offen lds\n" \
  "s_add_u32 m0, %[mw], 16384\n" \
  "s_xor_b32 %[cnt], %[sc], 960\n" \
  "buffer_load_dwordx4 v248, %[rx], %[cnt] offen lds\n" \
  "v_mfma_f32_16x16x32_bf16 v[192:195], v[240:243], v[232:235], v[192:195]\n" \
  "v_mfma_f32_16x16x32_bf16 v[196:199], v[244:247], v[232:235], v[196:199]\n" \
  "ds_read_b128 v[232:235], v252 offset:2048\n" \
  "v_mfma_f32_16x16x32_bf16 v[200:203], v[240:243], v[236:239], v[200:203]\n" \
  "v_mfma_f32_16x16x32_bf16 v[204:207], v[244:247], v[236:239], v[204:207]\n" \
  "ds_read_b128 v[236:239], v252 offset:3072\n" \
  "ds_read_b128 v[240:243], v249 offset:16384\n" \
  "ds_read_b128 v[244:247], v249 offset:17408\n" \
  "v_mfma_f32_16x16x32_bf16 v[192:195], v[40:43], v[208:211], v[192:195]\n" \
  "v_mfma_f32_16x16x32_bf16 v[196:199], v[104:107], v[208:211], v[196:199]\n" \
  "ds_read_b128 v[208:211], v252 offset:4096\n" \
  "v_mfma_f32_16x16x32_bf16 v[200:203], v[40:43], v[212:215], v[200:203]\n" \
  "v_mfma_f32_16x16x32_bf16 v[204:207], v[104:107], v[212:215], v[204:207]\n" \
  "ds_read_b128 v[212:215], v252 offset:5120\n" \
  "v_mfma_f32_16x16x32_bf16 v[192:195], v[168:171], v[216:219], v[192:195]\n" \
  "v_mfma_f32_16x16x32_bf16 v[196:199], v[172:175], v[216:219], v[196:199]\n" \
  "ds_read_b128 v[216:219], v252 offset:6144\n" \
  "v_mfma_f32_16x16x32_bf16 v[200:203], v[168:171], v[220:223], v[200:203]\n" \
  "v_mfma_f32_16x16x32_bf16 v[204:207], v[172:175], v[220:223], v[204:207]\n" \
  "ds_read_b128 v[220:223], v252 offset:7168\n" \
  "s_waitcnt lgkmcnt(9)\n" \
  "v_mfma_f32_16x16x32_bf16 v[192:195], v[44:47], v[224:227], v[192:195]\n" \
  "v_mfma_f32_16x16x32_bf16 v[196:199], v[108:111], v[224:227], v[196:199]\n" \
  "ds_read_b128 v[224:227], v252 offset:8192\n" \
  "s_waitcnt lgkmcnt(9)\n" \
  "v_mfma_f32_16x16x32_bf16 v[200:203], v[44:47], v[228:231], v[200:203]\n" \
  "v_mfma_f32_16x16x32_bf16 v[204:207], v[108:111], v[228:231], v[204:207]\n" \
  "ds_read_b128 v[228:231], v252 offset:9216\n" \
  "s_waitcnt lgkmcnt(7)\n" \
  "v_mfma_f32_16x16x32_bf16 v[192:195], v[240:243], v[232:235], v[192:195]\n" \
  "s_waitcnt lgkmcnt(6)\n" \
  "v_mfma_f32_16x16x32_bf16 v[196:199], v[244:247], v[232:235], v[196:199]\n" \
  "ds_read_b128 v[232:235], v252 offset:10240\n" \
  "v_mfma_f32_16x16x32_bf16 v[200:203], v[240:243], v[236:239], v[200:203]\n" \
  "v_mfma_f32_16x16x32_bf16 v[204:207], v[244:247], v[236:239], v[204:207]\n" \
  "ds_read_b128 v[236:239], v252 offset:11264\n" \
  "ds_read_b128 v[240:243], v249 offset:32768\n" \
  "ds_read_b128 v[244:247], v249 offset:33792\n" \
  "s_waitcnt lgkmcnt(9)\n" \
  "v_mfma_f32_16x16x32_bf16 v[192:195], v[48:51], v[208:211], v[192:195]\n" \
  "v_mfma_f32_16x16x32_bf16 v[196:199], v[112:115], v[208:211], v[196:199]\n" \
  "s_waitcnt lgkmcnt(8)\n" \
  "v_mfma_f32_16x16x32_bf16 v[200:203], v[48:51], v[212:215], v[200:203]\n" \
  "v_mfma_f32_16x16x32_bf16 v[204:207], v[112:115], v[212:215], v[204:207]\n" \
  "s_waitcnt vmcnt(0)\n" \
  "s_waitcnt lgkmcnt(0)\n" \
  "s_barrier\n" \
  "ds_read_b128 v[208:211], v252 offset:12288\n" \
  "ds_read_b128 v[212:215], v252 offset:13312\n" \
  "s_add_u32 m0, %[mw], 0\n" \
  "s_xor_b32 %[cnt], %[sn], 0\n" \
  "buffer_load_dwordx4 v248, %[rx], %[cnt] offen lds\n" \
  "s_add_u32 m0, %[mw], 4096\n" \
  "s_xor_b32 %[cnt], %[sn], 64\n" \
  "buffer_load_dwordx4 v248, %[rx], %[cnt] offen lds\n" \
  "s_add_u32 m0, %[mw], 8192\n" \
  "s_xor_b32 %[cnt], %[sn], 128\n" \
  "buffer_load_dwordx4 v248, %[rx], %[cnt] offen lds\n" \
  "v_mfma_f32_16x16x32_bf16 v[192:195], v[176:179], v[216:219], v[192:195]\n" \
  "v_mfma_f32_16x16x32_bf16 v[196:199], v[180:183], v[216:219], v[196:199]\n" \
  "ds_read_b128 v[216:219], v252 offset:14336\n" \
  "v_mfma_f32_16x16x32_bf16 v[200:203], v[176:179], v[220:223], v[200:203]\n" \
  "v_mfma_f32_16x16x32_bf16 v[204:207], v[180:183], v[220:223], v[204:207]\n" \
  "ds_read_b128 v[220:223], v252 offset:15360\n" \
  "v_mfma_f32_16x16x32_bf16 v[192:195], v[52:55], v[224:227], v[192:195]\n" \
  "v_mfma_f32_16x16x32_bf16 v[196:199], v[116:119], v[224:227], v[196:199]\n" \
  "ds_read_b128 v[224:227], v252 offset:16384\n" \
  "v_mfma_f32_16x16x32_bf16 v[200:203], v[52:55], v[228:231], v[200:203]\n" \
  "v_mfma_f32_16x16x32_bf16 v[204:207], v[116:119], v[228:231], v[204:207]\n" \
  "ds_read_b128 v[228:231], v252 offset:17408\n" \
  "v_mfma_f32_16x16x32_bf16 v[192:195], v[240:243], v[232:235], v[192:195]\n" \
  "v_mfma_f32_16x16x32_bf16 v[196:199], v[244:247], v[232:235], v[196:199]\n" \
  "ds_read_b128 v[232:235], v252 offset:18432\n" \
  "v_mfma_f32_16x16x32_bf16 v[200:203], v[240:243], v[236:239], v[200:203]\n" \
  "v_mfma_f32_16x16x32_bf16 v[204:207], v[244:247], v[236:239], v[204:207]\n" \
  "ds_read_b128 v[236:239], v252 offset:19456\n" \
  "ds_read_b128 v[240:243], v249 offset:49152\n" \
  "ds_read_b128 v[244:247], v249 offset:50176\n" \
  "s_waitcnt lgkmcnt(9)\n" \
  "v_mfma_f32_16x16x32_bf16 v[192:195], v[56:59], v[208:211], v[192:195]\n" \
  "v_mfma_f32_16x16x32_bf16 v[196:199], v[120:123], v[208:211], v[196:199]\n" \
  "s_waitcnt lgkmcnt(8)\n" \
  "v_mfma_f32_16x16x32_bf16 v[200:203], v[56:59], v[212:215], v[200:203]\n" \
  "v_mfma_f32_16x16x32_bf16 v[204:207], v[120:123], v[212:215], v[204:207]\n" \
  "s_waitcnt vmcnt(0)\n" \
  "s_waitcnt lgkmcnt(0)\n" \
  "s_barrier\n" \
  "ds_read_b128 v[208:211], v252 offset:0\n" \
  "ds_read_b128 v[212:215], v252 offset:1024\n" \
  "s_add_u32 m0, %[mw], 12288\n" \
  "s_xor_b32 %[cnt], %[sn], 192\n" \
  "buffer_load_dwordx4 v248, %[rx], %[cnt] offen lds\n" \
  "s_add_u32 m0, %[mw], 16384\n" \
  "s_xor_b32 %[cnt], %[sn], 256\n" \
  "buffer_load_dwordx4 v248, %[rx], %[cnt] offen lds\n" \
  "s_add_u32 m0, %[mw], 20480\n" \
  "s_xor_b32 %[cnt], %[sn], 320\n" \
  "buffer_load_dwordx4 v248, %[rx], %[cnt] offen lds\n" \
  "v_mfma_f32_16x16x32_bf16 v[192:195], v[184:187], v[216:219], v[192:195]\n" \
  "v_mfma_f32_16x16x32_bf16 v[196:199], v[188:191], v[216:219], v[196:199]\n" \
  "ds_read_b128 v[216:219], v252 offset:2048\n" \
  "v_mfma_f32_16x16x32_bf16 v[200:203], v[184:187], v[220:223], v[200:203]\n" \
  "v_mfma_f32_16x16x32_bf16 v[204:207], v[188:191], v[220:223], v[204:207]\n" \
  "ds_read_b128 v[220:223], v252 offset:3072\n" \
  "v_mfma_f32_16x16x32_bf16 v[192:195], v[60:63], v[224:227], v[192:195]\n" \
  "v_mfma_f32_16x16x32_bf16 v[196:199], v[124:127], v[224:227], v[196:199]\n" \
  "ds_read_b128 v[224:227], v252 offset:4096\n" \
  "v_mfma_f32_16x16x32_bf16 v[200:203], v[60:63], v[228:231], v[200:203]\n" \
  "v_mfma_f32_16x16x32_bf16 v[204:207], v[124:127], v[228:231], v[204:207]\n" \
  "ds_read_b128 v[228:231], v252 offset:5120\n" \
  "v_mfma_f32_16x16x32_bf16 v[192:195], v[240:243], v[232:235], v[192:195]\n" \
  "v_mfma_f32_16x16x32_bf16 v[196:199], v[244:247], v[232:235], v[196:199]\n" \
  "ds_read_b128 v[232:235], v252 offset:6144\n" \
  "v_mfma_f32_16x16x32_bf16 v[200:203], v[240:243], v[236:239], v[200:203]\n" \
  "v_mfma_f32_16x16x32_bf16 v[204:207], v[244:247], v[236:239], v[204:207]\n" \
  "ds_read_b128 v[236:239], v252 offset:7168\n" \
  "v_cvt_pk_bf16_f32 v192, v192, v193\n" \
  "v_cvt_pk_bf16_f32 v193, v194, v195\n" \
  "v_cvt_pk_bf16_f32 v194, v196, v197\n" \
  "v_cvt_pk_bf16_f32 v195, v198, v199\n" \
  "v_add_u32 v196, %[so], v251\n" \
  "ds_write_b128 v196, v[192:195] offset:24576\n" \
  "v_cvt_pk_bf16_f32 v200, v200, v201\n" \
  "v_cvt_pk_bf16_f32 v201, v202, v203\n" \
  "v_cvt_pk_bf16_f32 v202, v204, v205\n" \
  "v_cvt_pk_bf16_f32 v203, v206, v207\n" \
  "ds_write_b128 v196, v[200:203] offset:28672\n" \
  "s_mov_b32 m0, %[keep]\n" \
  "s_waitcnt lgkmcnt(0)\n"
#define ST_XB \
  "s_barrier\n" \
  "ds_read_b128 v[192:195], v251 offset:24576\n" \
  "ds_read_b128 v[208:211], v251 offset:28672\n" \
  "ds_read_b128 v[196:199], v251 offset:25600\n" \
  "ds_read_b128 v[212:215], v251 offset:29696\n" \
  "ds_read_b128 v[200:203], v251 offset:26624\n" \
  "ds_read_b128 v[216:219], v251 offset:30720\n" \
  "ds_read_b128 v[204:207], v251 offset:27648\n" \
  "ds_read_b128 v[220:223], v251 offset:31744\n" \
  "ds_read_b128 v[240:243], v250 offset:0\n" \
  "ds_read_b128 v[244:247], v250 offset:16384\n" \
  "s_waitcnt lgkmcnt(9)\n" \
  "v_mfma_f32_16x16x32_bf16 v[224:227], v[192:195], v[0:3], 0\n" \
  "s_waitcnt lgkmcnt(8)\n" \
  "v_mfma_f32_16x16x32_bf16 v[228:231], v[208:211], v[0:3], 0\n" \
  "s_waitcnt lgkmcnt(7)\n" \
  "v_mfma_f32_16x16x32_bf16 v[224:227], v[196:199], v[4:7], v[224:227]\n" \
  "s_waitcnt lgkmcnt(6)\n" \
  "v_mfma_f32_16x16x32_bf16 v[228:231], v[212:215], v[4:7], v[228:231]\n" \
  "s_waitcnt lgkmcnt(5)\n" \
  "v_mfma_f32_16x16x32_bf16 v[224:227], v[200:203], v[8:11], v[224:227]\n" \
  "s_waitcnt lgkmcnt(4)\n" \
  "v_mfma_f32_16x16x32_bf16 v[228:231], v[216:219], v[8:11], v[228:231]\n" \
  "s_waitcnt lgkmcnt(3)\n" \
  "v_mfma_f32_16x16x32_bf16 v[224:227], v[204:207], v[12:15], v[224:227]\n" \
  "s_waitcnt lgkmcnt(2)\n" \
  "v_mfma_f32_16x16x32_bf16 v[228:231], v[220:223], v[12:15], v[228:231]\n" \
  "v_mfma_f32_16x16x32_bf16 v[232:235], v[192:195], v[128:131], 0\n" \
  "v_mfma_f32_16x16x32_bf16 v[236:239], v[208:211], v[128:131], 0\n" \
  "v_mfma_f32_16x16x32_bf16 v[232:235], v[200:203], v[136:139], v[232:235]\n" \
  "v_cvt_pk_bf16_f32 v224, v224, v225\n" \
  "v_cvt_pk_bf16_f32 v226, v226, v227\n" \
  "v_mfma_f32_16x16x32_bf16 v[236:239], v[216:219], v[136:139], v[236:239]\n" \
  "v_cvt_pk_bf16_f32 v225, v228, v229\n" \
  "v_cvt_pk_bf16_f32 v227, v230, v231\n" \
  "s_waitcnt lgkmcnt(1)\n" \
  "v_mfma_f32_16x16x32_bf16 v[232:235], v[196:199], v[240:243], v[232:235]\n" \
  "s_add_u32 %[cnt], %[so], 0*%c[js]+0*%c[ss]\n" \
  "buffer_store_dwordx4 v[224:227], v251, %[rs], %[cnt] offen sc1\n" \
  "v_mfma_f32_16x16x32_bf16 v[236:239], v[212:215], v[240:243], v[236:239]\n" \
  "s_waitcnt lgkmcnt(0)\n" \
  "v_mfma_f32_16x16x32_bf16 v[232:235], v[204:207], v[244:247], v[232:235]\n" \
  "v_mfma_f32_16x16x32_bf16 v[236:239], v[220:223], v[244:247], v[236:239]\n" \
  "ds_read_b128 v[240:243], v250 offset:1024\n" \
  "ds_read_b128 v[244:247], v250 offset:17408\n" \
  "v_mfma_f32_16x16x32_bf16 v[224:227], v[192:195], v[64:67], 0\n" \
  "v_mfma_f32_16x16x32_bf16 v[228:231], v[208:211], v[64:67], 0\n" \
  "v_mfma_f32_16x16x32_bf16 v[224:227], v[196:199], v[68:71], v[224:227]\n" \
  "v_cvt_pk_bf16_f32 v232, v232, v233\n" \
  "v_cvt_pk_bf16_f32 v234, v234, v235\n" \
  "v_mfma_f32_16x16x32_bf16 v[228:231], v[212:215], v[68:71], v[228:231]\n" \
  "v_cvt_pk_bf16_f32 v233, v236, v237\n" \
  "v_cvt_pk_bf16_f32 v235, v238, v239\n" \
  "v_mfma_f32_16x16x32_bf16 v[224:227], v[200:203], v[72:75], v[224:227]\n" \
  "s_add_u32 %[cnt], %[so], 4*%c[js]+0*%c[ss]\n" \
  "buffer_store_dwordx4 v[232:235], v251, %[rs], %[cnt] offen sc1\n" \
  "v_mfma_f32_16x16x32_bf16 v[228:231], v[216:219], v[72:75], v[228:231]\n" \
  "v_mfma_f32_16x16x32_bf16 v[224:227], v[204:207], v[76:79], v[224:227]\n" \
  "v_mfma_f32_16x16x32_bf16 v[228:231], v[220:223], v[76:79], v[228:231]\n" \
  "v_lshlrev_b32 v253, 24, v251\n" \
  "s_waitcnt vmcnt(2)\n" \
  "buffer_store_byte v253, v253, %[rs], %[fo] offen sc1\n" \
  "s_barrier\n" \
  "v_mfma_f32_16x16x32_bf16 v[232:235], v[192:195], v[132:135], 0\n" \
  "v_mfma_f32_16x16x32_bf16 v[236:239], v[208:211], v[132:135], 0\n" \
  "v_mfma_f32_16x16x32_bf16 v[232:235], v[200:203], v[140:143], v[232:235]\n" \
  "v_cvt_pk_bf16_f32 v224, v224, v225\n" \
  "v_cvt_pk_bf16_f32 v226, v226, v227\n" \
  "v_mfma_f32_16x16x32_bf16 v[236:239], v[216:219], v[140:143], v[236:239]\n" \
  "v_cvt_pk_bf16_f32 v225, v228, v229\n" \
  "v_cvt_pk_bf16_f32 v227, v230, v231\n" \
  "s_waitcnt lgkmcnt(1)\n" \
  "v_mfma_f32_16x16x32_bf16 v[232:235], v[196:199], v[240:243], v[232:235]\n" \
  "s_add_u32 %[cnt], %[so], 0*%c[js]+1*%c[ss]\n" \
  "buffer_store_dwordx4 v[224:227], v251, %[rs], %[cnt] offen sc1\n" \
  "v_mfma_f32_16x16x32_bf16 v[236:239], v[212:215], v[240:243], v[236:239]\n" \
  "s_waitcnt lgkmcnt(0)\n" \
  "v_mfma_f32_16x16x32_bf16 v[232:235], v[204:207], v[244:247], v[232:235]\n" \
  "v_mfma_f32_16x16x32_bf16 v[236:239], v[220:223], v[244:247], v[236:239]\n" \
  "ds_read_b128 v[240:243], v250 offset:32768\n" \
  "ds_read_b128 v[244:247], v250 offset:49152\n" \
  "v_mfma_f32_16x16x32_bf16 v[224:227], v[192:195], v[16:19], 0\n" \
  "v_mfma_f32_16x16x32_bf16 v[228:231], v[208:211], v[16:19], 0\n" \
  "v_mfma_f32_16x16x32_bf16 v[224:227], v[196:199], v[20:23], v[224:227]\n" \
  "v_cvt_pk_bf16_f32 v232, v232, v233\n" \
  "v_cvt_pk_bf16_f32 v234, v234, v235\n" \
  "v_mfma_f32_16x16x32_bf16 v[228:231], v[212:215], v[20:23], v[228:231]\n" \
  "v_cvt_pk_bf16_f32 v233, v236, v237\n" \
  "v_cvt_pk_bf16_f32 v235, v238, v239\n" \
  "v_mfma_f32_16x16x32_bf16 v[224:227], v[200:203], v[24:27], v[224:227]\n" \
  "s_add_u32 %[cnt], %[so], 4*%c[js]+1*%c[ss]\n" \
  "buffer_store_dwordx4 v[232:235], v251, %[rs], %[cnt] offen sc1\n" \
  "v_mfma_f32_16x16x32_bf16 v[228:231], v[216:219], v[24:27], v[228:231]\n" \
  "v_mfma_f32_16x16x32_bf16 v[224:227], v[204:207], v[28:31], v[224:227]\n" \
  "v_mfma_f32_16x16x32_bf16 v[228:231], v[220:223], v[28:31], v[228:231]\n" \
  "s_barrier\n" \
  "v_mfma_f32_16x16x32_bf16 v[232:235], v[192:195], v[144:147], 0\n" \
  "v_mfma_f32_16x16x32_bf16 v[236:239], v[208:211], v[144:147], 0\n" \
  "v_mfma_f32_16x16x32_bf16 v[232:235], v[200:203], v[152:155], v[232:235]\n" \
  "v_cvt_pk_bf16_f32 v224, v224, v225\n" \
  "v_cvt_pk_bf16_f32 v226, v226, v227\n" \
  "v_mfma_f32_16x16x32_bf16 v[236:239], v[216:219], v[152:155], v[236:239]\n" \
  "v_cvt_pk_bf16_f32 v225, v228, v229\n" \
  "v_cvt_pk_bf16_f32 v227, v230, v231\n" \
  "s_waitcnt lgkmcnt(1)\n" \
  "v_mfma_f32_16x16x32_bf16 v[232:235], v[196:199], v[240:243], v[232:235]\n" \
  "s_add_u32 %[cnt], %[so], 1*%c[js]+0*%c[ss]\n" \
  "buffer_store_dwordx4 v[224:227], v251, %[rs], %[cnt] offen sc1\n" \
  "v_mfma_f32_16x16x32_bf16 v[236:239], v[212:215], v[240:243], v[236:239]\n" \
  "s_waitcnt lgkmcnt(0)\n" \
  "v_mfma_f32_16x16x32_bf16 v[232:235], v[204:207], v[244:247], v[232:235]\n" \
  "v_mfma_f32_16x16x32_bf16 v[236:239], v[220:223], v[244:247], v[236:239]\n" \
  "ds_read_b128 v[240:243], v250 offset:33792\n" \
  "ds_read_b128 v[244:247], v250 offset:50176\n" \
  "v_mfma_f32_16x16x32_bf16 v[224:227], v[192:195], v[80:83], 0\n" \
  "v_mfma_f32_16x16x32_bf16 v[228:231], v[208:211], v[80:83], 0\n" \
  "v_mfma_f32_16x16x32_bf16 v[224:227], v[196:199], v[84:87], v[224:227]\n" \
  "v_cvt_pk_bf16_f32 v232, v232, v233\n" \
  "v_cvt_pk_bf16_f32 v234, v234, v235\n" \
  "v_mfma_f32_16x16x32_bf16 v[228:231], v[212:215], v[84:87], v[228:231]\n" \
  "v_cvt_pk_bf16_f32 v233, v236, v237\n" \
  "v_cvt_pk_bf16_f32 v235, v238, v239\n" \
  "v_mfma_f32_16x16x32_bf16 v[224:227], v[200:203], v[88:91], v[224:227]\n" \
  "s_add_u32 %[cnt], %[so], 5*%c[js]+0*%c[ss]\n" \
  "buffer_store_dwordx4 v[232:235], v251, %[rs], %[cnt] offen sc1\n" \
  "v_mfma_f32_16x16x32_bf16 v[228:231], v[216:219], v[88:91], v[228:231]\n" \
  "v_mfma_f32_16x16x32_bf16 v[224:227], v[204:207], v[92:95], v[224:227]\n" \
  "v_mfma_f32_16x16x32_bf16 v[228:231], v[220:223], v[92:95], v[228:231]\n" \
  "v_mfma_f32_16x16x32_bf16 v[232:235], v[192:195], v[148:151], 0\n" \
  "v_mfma_f32_16x16x32_bf16 v[236:239], v[208:211], v[148:151], 0\n" \
  "v_mfma_f32_16x16x32_bf16 v[232:235], v[200:203], v[156:159], v[232:235]\n" \
  "v_cvt_pk_bf16_f32 v224, v224, v225\n" \
  "v_cvt_pk_bf16_f32 v226, v226, v227\n" \
  "v_mfma_f32_16x16x32_bf16 v[236:239], v[216:219], v[156:159], v[236:239]\n" \
  "v_cvt_pk_bf16_f32 v225, v228, v229\n" \
  "v_cvt_pk_bf16_f32 v227, v230, v231\n" \
  "s_waitcnt lgkmcnt(1)\n" \
  "v_mfma_f32_16x16x32_bf16 v[232:235], v[196:199], v[240:243], v[232:235]\n" \
  "s_add_u32 %[cnt], %[so], 1*%c[js]+1*%c[ss]\n" \
  "buffer_store_dwordx4 v[224:227], v251, %[rs], %[cnt] offen sc1\n" \
  "v_mfma_f32_16x16x32_bf16 v[236:239], v[212:215], v[240:243], v[236:239]\n" \
  "s_waitcnt lgkmcnt(0)\n" \
  "v_mfma_f32_16x16x32_bf16 v[232:235], v[204:207], v[244:247], v[232:235]\n" \
  "v_mfma_f32_16x16x32_bf16 v[236:239], v[220:223], v[244:247], v[236:239]\n" \
  "ds_read_b128 v[240:243], v249 offset:0\n" \
  "ds_read_b128 v[244:247], v249 offset:16384\n" \
  "s_barrier\n" \
  "v_mfma_f32_16x16x32_bf16 v[224:227], v[192:195], v[32:35], 0\n" \
  "v_mfma_f32_16x16x32_bf16 v[228:231], v[208:211], v[32:35], 0\n" \
  "v_mfma_f32_16x16x32_bf16 v[224:227], v[196:199], v[36:39], v[224:227]\n" \
  "v_cvt_pk_bf16_f32 v232, v232, v233\n" \
  "v_cvt_pk_bf16_f32 v234, v234, v235\n" \
  "v_mfma_f32_16x16x32_bf16 v[228:231], v[212:215], v[36:39], v[228:231]\n" \
  "v_cvt_pk_bf16_f32 v233, v236, v237\n" \
  "v_cvt_pk_bf16_f32 v235, v238, v239\n" \
  "v_mfma_f32_16x16x32_bf16 v[224:227], v[200:203], v[40:43], v[224:227]\n" \
  "s_add_u32 %[cnt], %[so], 5*%c[js]+1*%c[ss]\n" \
  "buffer_store_dwordx4 v[232:235], v251, %[rs], %[cnt] offen sc1\n" \
  "v_mfma_f32_16x16x32_bf16 v[228:231], v[216:219], v[40:43], v[228:231]\n" \
  "v_mfma_f32_16x16x32_bf16 v[224:227], v[204:207], v[44:47], v[224:227]\n" \
  "v_mfma_f32_16x16x32_bf16 v[228:231], v[220:223], v[44:47], v[228:231]\n" \
  "v_mfma_f32_16x16x32_bf16 v[232:235], v[192:195], v[160:163], 0\n" \
  "v_mfma_f32_16x16x32_bf16 v[236:239], v[208:211], v[160:163], 0\n" \
  "v_mfma_f32_16x16x32_bf16 v[232:235], v[200:203], v[168:171], v[232:235]\n" \
  "v_cvt_pk_bf16_f32 v224, v224, v225\n" \
  "v_cvt_pk_bf16_f32 v226, v226, v227\n" \
  "v_mfma_f32_16x16x32_bf16 v[236:239], v[216:219], v[168:171], v[236:239]\n" \
  "v_cvt_pk_bf16_f32 v225, v228, v229\n" \
  "v_cvt_pk_bf16_f32 v227, v230, v231\n" \
  "s_waitcnt lgkmcnt(1)\n" \
  "v_mfma_f32_16x16x32_bf16 v[232:235], v[196:199], v[240:243], v[232:235]\n" \
  "s_add_u32 %[cnt], %[so], 2*%c[js]+0*%c[ss]\n" \
  "buffer_store_dwordx4 v[224:227], v251, %[rs], %[cnt] offen sc1\n" \
  "v_mfma_f32_16x16x32_bf16 v[236:239], v[212:215], v[240:243], v[236:239]\n" \
  "s_waitcnt lgkmcnt(0)\n" \
  "v_mfma_f32_16x16x32_bf16 v[232:235], v[204:207], v[244:247], v[232:235]\n" \
  "v_mfma_f32_16x16x32_bf16 v[236:239], v[220:223], v[244:247], v[236:239]\n" \
  "ds_read_b128 v[240:243], v249 offset:1024\n" \
  "ds_read_b128 v[244:247], v249 offset:17408\n" \
  "v_mfma_f32_16x16x32_bf16 v[224:227], v[192:195], v[96:99], 0\n" \
  "v_mfma_f32_16x16x32_bf16 v[228:231], v[208:211], v[96:99], 0\n" \
  "v_mfma_f32_16x16x32_bf16 v[224:227], v[196:199], v[100:103], v[224:227]\n" \
  "v_cvt_pk_bf16_f32 v232, v232, v233\n" \
  "v_cvt_pk_bf16_f32 v234, v234, v235\n" \
  "v_mfma_f32_16x16x32_bf16 v[228:231], v[212:215], v[100:103], v[228:231]\n" \
  "v_cvt_pk_bf16_f32 v233, v236, v237\n" \
  "v_cvt_pk_bf16_f32 v235, v238, v239\n" \
  "v_mfma_f32_16x16x32_bf16 v[224:227], v[200:203], v[104:107], v[224:227]\n" \
  "s_add_u32 %[cnt], %[so], 6*%c[js]+0*%c[ss]\n" \
  "buffer_store_dwordx4 v[232:235], v251, %[rs], %[cnt] offen sc1\n" \
  "v_mfma_f32_16x16x32_bf16 v[228:231], v[216:219], v[104:107], v[228:231]\n" \
  "v_mfma_f32_16x16x32_bf16 v[224:227], v[204:207], v[108:111], v[224:227]\n" \
  "v_mfma_f32_16x16x32_bf16 v[228:231], v[220:223], v[108:111], v[228:231]\n" \
  "s_barrier\n" \
  "v_mfma_f32_16x16x32_bf16 v[232:235], v[192:195], v[164:167], 0\n" \
  "v_mfma_f32_16x16x32_bf16 v[236:239], v[208:211], v[164:167], 0\n" \
  "v_mfma_f32_16x16x32_bf16 v[232:235], v[200:203], v[172:175], v[232:235]\n" \
  "v_cvt_pk_bf16_f32 v224, v224, v225\n" \
  "v_cvt_pk_bf16_f32 v226, v226, v227\n" \
  "v_mfma_f32_16x16x32_bf16 v[236:239], v[216:219], v[172:175], v[236:239]\n" \
  "v_cvt_pk_bf16_f32 v225, v228, v229\n" \
  "v_cvt_pk_bf16_f32 v227, v230, v231\n" \
  "s_waitcnt lgkmcnt(1)\n" \
  "v_mfma_f32_16x16x32_bf16 v[232:235], v[196:199], v[240:243], v[232:235]\n" \
  "s_add_u32 %[cnt], %[so], 2*%c[js]+1*%c[ss]\n" \
  "buffer_store_dwordx4 v[224:227], v251, %[rs], %[cnt] offen sc1\n" \
  "v_mfma_f32_16x16x32_bf16 v[236:239], v[212:215], v[240:243], v[236:239]\n" \
  "s_waitcnt lgkmcnt(0)\n" \
  "v_mfma_f32_16x16x32_bf16 v[232:235], v[204:207], v[244:247], v[232:235]\n" \
  "v_mfma_f32_16x16x32_bf16 v[236:239], v[220:223], v[244:247], v[236:239]\n" \
  "ds_read_b128 v[240:243], v249 offset:32768\n" \
  "ds_read_b128 v[244:247], v249 offset:49152\n" \
  "v_mfma_f32_16x16x32_bf16 v[224:227], v[192:195], v[48:51], 0\n" \
  "v_mfma_f32_16x16x32_bf16 v[228:231], v[208:211], v[48:51], 0\n" \
  "v_mfma_f32_16x16x32_bf16 v[224:227], v[196:199], v[52:55], v[224:227]\n" \
  "v_cvt_pk_bf16_f32 v232, v232, v233\n" \
  "v_cvt_pk_bf16_f32 v234, v234, v235\n" \
  "v_mfma_f32_16x16x32_bf16 v[228:231], v[212:215], v[52:55], v[228:231]\n" \
  "v_cvt_pk_bf16_f32 v233, v236, v237\n" \
  "v_cvt_pk_bf16_f32 v235, v238, v239\n" \
  "v_mfma_f32_16x16x32_bf16 v[224:227], v[200:203], v[56:59], v[224:227]\n" \
  "s_add_u32 %[cnt], %[so], 6*%c[js]+1*%c[ss]\n" \
  "buffer_store_dwordx4 v[232:235], v251, %[rs], %[cnt] offen sc1\n" \
  "v_mfma_f32_16x16x32_bf16 v[228:231], v[216:219], v[56:59], v[228:231]\n" \
  "v_mfma_f32_16x16x32_bf16 v[224:227], v[204:207], v[60:63], v[224:227]\n" \
  "v_mfma_f32_16x16x32_bf16 v[228:231], v[220:223], v[60:63], v[228:231]\n" \
  "s_barrier\n" \
  "v_mfma_f32_16x16x32_bf16 v[232:235], v[192:195], v[176:179], 0\n" \
  "v_mfma_f32_16x16x32_bf16 v[236:239], v[208:211], v[176:179], 0\n" \
  "v_mfma_f32_16x16x32_bf16 v[232:235], v[200:203], v[184:187], v[232:235]\n" \
  "v_cvt_pk_bf16_f32 v224, v224, v225\n" \
  "v_cvt_pk_bf16_f32 v226, v226, v227\n" \
  "v_mfma_f32_16x16x32_bf16 v[236:239], v[216:219], v[184:187], v[236:239]\n" \
  "v_cvt_pk_bf16_f32 v225, v228, v229\n" \
  "v_cvt_pk_bf16_f32 v227, v230, v231\n" \
  "s_waitcnt lgkmcnt(1)\n" \
  "v_mfma_f32_16x16x32_bf16 v[232:235], v[196:199], v[240:243], v[232:235]\n" \
  "s_add_u32 %[cnt], %[so], 3*%c[js]+0*%c[ss]\n" \
  "buffer_store_dwordx4 v[224:227], v251, %[rs], %[cnt] offen sc1\n" \
  "v_mfma_f32_16x16x32_bf16 v[236:239], v[212:215], v[240:243], v[236:239]\n" \
  "s_waitcnt lgkmcnt(0)\n" \
  "v_mfma_f32_16x16x32_bf16 v[232:235], v[204:207], v[244:247], v[232:235]\n" \
  "v_mfma_f32_16x16x32_bf16 v[236:239], v[220:223], v[244:247], v[236:239]\n" \
  "ds_read_b128 v[240:243], v249 offset:33792\n" \
  "ds_read_b128 v[244:247], v249 offset:50176\n" \
  "v_mfma_f32_16x16x32_bf16 v[224:227], v[192:195], v[112:115], 0\n" \
  "v_mfma_f32_16x16x32_bf16 v[228:231], v[208:211], v[112:115], 0\n" \
  "v_mfma_f32_16x16x32_bf16 v[224:227], v[196:199], v[116:119], v[224:227]\n" \
  "v_cvt_pk_bf16_f32 v232, v232, v233\n" \
  "v_cvt_pk_bf16_f32 v234, v234, v235\n" \
  "v_mfma_f32_16x16x32_bf16 v[228:231], v[212:215], v[116:119], v[228:231]\n" \
  "v_cvt_pk_bf16_f32 v233, v236, v237\n" \
  "v_cvt_pk_bf16_f32 v235, v238, v239\n" \
  "v_mfma_f32_16x16x32_bf16 v[224:227], v[200:203], v[120:123], v[224:227]\n" \
  "s_add_u32 %[cnt], %[so], 7*%c[js]+0*%c[ss]\n" \
  "buffer_store_dwordx4 v[232:235], v251, %[rs], %[cnt] offen sc1\n" \
  "v_mfma_f32_16x16x32_bf16 v[228:231], v[216:219], v[120:123], v[228:231]\n" \
  "v_mfma_f32_16x16x32_bf16 v[224:227], v[204:207], v[124:127], v[224:227]\n" \
  "v_mfma_f32_16x16x32_bf16 v[228:231], v[220:223], v[124:127], v[228:231]\n" \
  "v_mfma_f32_16x16x32_bf16 v[232:235], v[192:195], v[180:183], 0\n" \
  "v_mfma_f32_16x16x32_bf16 v[236:239], v[208:211], v[180:183], 0\n" \
  "v_mfma_f32_16x16x32_bf16 v[232:235], v[200:203], v[188:191], v[232:235]\n" \
  "v_cvt_pk_bf16_f32 v224, v224, v225\n" \
  "v_cvt_pk_bf16_f32 v226, v226, v227\n" \
  "v_mfma_f32_16x16x32_bf16 v[236:239], v[216:219], v[188:191], v[236:239]\n" \
  "v_cvt_pk_bf16_f32 v225, v228, v229\n" \
  "v_cvt_pk_bf16_f32 v227, v230, v231\n" \
  "s_waitcnt lgkmcnt(1)\n" \
  "v_mfma_f32_16x16x32_bf16 v[232:235], v[196:199], v[240:243], v[232:235]\n" \
  "s_add_u32 %[cnt], %[so], 3*%c[js]+1*%c[ss]\n" \
  "buffer_store_dwordx4 v[224:227], v251, %[rs], %[cnt] offen sc1\n" \
  "v_mfma_f32_16x16x32_bf16 v[236:239], v[212:215], v[240:243], v[236:239]\n" \
  "s_waitcnt lgkmcnt(0)\n" \
  "v_mfma_f32_16x16x32_bf16 v[232:235], v[204:207], v[244:247], v[232:235]\n" \
  "v_mfma_f32_16x16x32_bf16 v[236:239], v[220:223], v[244:247], v[236:239]\n" \
  "s_nop 4\n" \
  "v_cvt_pk_bf16_f32 v232, v232, v233\n" \
  "v_cvt_pk_bf16_f32 v234, v234, v235\n" \
  "s_nop 1\n" \
  "v_cvt_pk_bf16_f32 v233, v236, v237\n" \
  "v_cvt_pk_bf16_f32 v235, v238, v239\n" \
  "s_add_u32 %[cnt], %[so], 7*%c[js]+1*%c[ss]\n" \
  "buffer_store_dwordx4 v[232:235], v251, %[rs], %[cnt] offen sc1\n" \
  "s_nop 1\n"
#define ST_XBF \
  "s_barrier\n" \
  "ds_read_b128 v[192:195], v251 offset:24576\n" \
  "ds_read_b128 v[208:211], v251 offset:28672\n" \
  "ds_read_b128 v[196:199], v251 offset:25600\n" \
  "ds_read_b128 v[212:215], v251 offset:29696\n" \
  "ds_read_b128 v[200:203], v251 offset:26624\n" \
  "ds_read_b128 v[216:219], v251 offset:30720\n" \
  "ds_read_b128 v[204:207], v251 offset:27648\n" \
  "ds_read_b128 v[220:223], v251 offset:31744\n" \
  "ds_read_b128 v[240:243], v250 offset:0\n" \
  "ds_read_b128 v[244:247], v250 offset:16384\n" \
  "s_waitcnt lgkmcnt(9)\n" \
  "v_mfma_f32_16x16x32_bf16 v[224:227], v[192:195], v[0:3], 0\n" \
  "s_waitcnt lgkmcnt(8)\n" \
  "v_mfma_f32_16x16x32_bf16 v[228:231], v[208:211], v[0:3], 0\n" \
  "s_waitcnt lgkmcnt(7)\n" \
  "v_mfma_f32_16x16x32_bf16 v[224:227], v[196:199], v[4:7], v[224:227]\n" \
  "s_waitcnt lgkmcnt(6)\n" \
  "v_mfma_f32_16x16x32_bf16 v[228:231], v[212:215], v[4:7], v[228:231]\n" \
  "s_waitcnt lgkmcnt(5)\n" \
  "v_mfma_f32_16x16x32_bf16 v[224:227], v[200:203], v[8:11], v[224:227]\n" \
  "s_waitcnt lgkmcnt(4)\n" \
  "v_mfma_f32_16x16x32_bf16 v[228:231], v[216:219], v[8:11], v[228:231]\n" \
  "s_waitcnt lgkmcnt(3)\n" \
  "v_mfma_f32_16x16x32_bf16 v[224:227], v[204:207], v[12:15], v[224:227]\n" \
  "s_waitcnt lgkmcnt(2)\n" \
  "v_mfma_f32_16x16x32_bf16 v[228:231], v[220:223], v[12:15], v[228:231]\n" \
  "v_mfma_f32_16x16x32_bf16 v[232:235], v[192:195], v[128:131], 0\n" \
  "v_mfma_f32_16x16x32_bf16 v[236:239], v[208:211], v[128:131], 0\n" \
  "v_mfma_f32_16x16x32_bf16 v[232:235], v[200:203], v[136:139], v[232:235]\n" \
  "v_cvt_pk_bf16_f32 v224, v224, v225\n" \
  "v_cvt_pk_bf16_f32 v226, v226, v227\n" \
  "v_mfma_f32_16x16x32_bf16 v[236:239], v[216:219], v[136:139], v[236:239]\n" \
  "v_cvt_pk_bf16_f32 v225, v228, v229\n" \
  "v_cvt_pk_bf16_f32 v227, v230, v231\n" \
  "s_waitcnt lgkmcnt(1)\n" \
  "v_mfma_f32_16x16x32_bf16 v[232:235], v[196:199], v[240:243], v[232:235]\n" \
  "s_add_u32 %[cnt], %[so], 0*%c[js]+0*%c[ss]\n" \
  "buffer_store_dwordx4 v[224:227], v251, %[rs], %[cnt] offen sc1\n" \
  "v_mfma_f32_16x16x32_bf16 v[236:239], v[212:215], v[240:243], v[236:239]\n" \
  "s_waitcnt lgkmcnt(0)\n" \
  "v_mfma_f32_16x16x32_bf16 v[232:235], v[204:207], v[244:247], v[232:235]\n" \
  "v_mfma_f32_16x16x32_bf16 v[236:239], v[220:223], v[244:247], v[236:239]\n" \
  "ds_read_b128 v[240:243], v250 offset:1024\n" \
  "ds_read_b128 v[244:247], v250 offset:17408\n" \
  "v_mfma_f32_16x16x32_bf16 v[224:227], v[192:195], v[64:67], 0\n" \
  "v_mfma_f32_16x16x32_bf16 v[228:231], v[208:211], v[64:67], 0\n" \
  "v_mfma_f32_16x16x32_bf16 v[224:227], v[196:199], v[68:71], v[224:227]\n" \
  "v_cvt_pk_bf16_f32 v232, v232, v233\n" \
  "v_cvt_pk_bf16_f32 v234, v234, v235\n" \
  "v_mfma_f32_16x16x32_bf16 v[228:231], v[212:215], v[68:71], v[228:231]\n" \
  "v_cvt_pk_bf16_f32 v233, v236, v237\n" \
  "v_cvt_pk_bf16_f32 v235, v238, v239\n" \
  "v_mfma_f32_16x16x32_bf16 v[224:227], v[200:203], v[72:75], v[224:227]\n" \
  "s_add_u32 %[cnt], %[so], 4*%c[js]+0*%c[ss]\n" \
  "buffer_store_dwordx4 v[232:235], v251, %[rs], %[cnt] offen sc1\n" \
  "v_mfma_f32_16x16x32_bf16 v[228:231], v[216:219], v[72:75], v[228:231]\n" \
  "v_mfma_f32_16x16x32_bf16 v[224:227], v[204:207], v[76:79], v[224:227]\n" \
  "v_mfma_f32_16x16x32_bf16 v[228:231], v[220:223], v[76:79], v[228:231]\n" \
  "v_mfma_f32_16x16x32_bf16 v[232:235], v[192:195], v[132:135], 0\n" \
  "v_mfma_f32_16x16x32_bf16 v[236:239], v[208:211], v[132:135], 0\n" \
  "v_mfma_f32_16x16x32_bf16 v[232:235], v[200:203], v[140:143], v[232:235]\n" \
  "v_cvt_pk_bf16_f32 v224, v224, v225\n" \
  "v_cvt_pk_bf16_f32 v226, v226, v227\n" \
  "v_mfma_f32_16x16x32_bf16 v[236:239], v[216:219], v[140:143], v[236:239]\n" \
  "v_cvt_pk_bf16_f32 v225, v228, v229\n" \
  "v_cvt_pk_bf16_f32 v227, v230, v231\n" \
  "s_waitcnt lgkmcnt(1)\n" \
  "v_mfma_f32_16x16x32_bf16 v[232:235], v[196:199], v[240:243], v[232:235]\n" \
  "s_add_u32 %[cnt], %[so], 0*%c[js]+1*%c[ss]\n" \
  "buffer_store_dwordx4 v[224:227], v251, %[rs], %[cnt] offen sc1\n" \
  "v_mfma_f32_16x16x32_bf16 v[236:239], v[212:215], v[240:243], v[236:239]\n" \
  "s_waitcnt lgkmcnt(0)\n" \
  "v_mfma_f32_16x16x32_bf16 v[232:235], v[204:207], v[244:247], v[232:235]\n" \
  "v_mfma_f32_16x16x32_bf16 v[236:239], v[220:223], v[244:247], v[236:239]\n" \
  "ds_read_b128 v[240:243], v250 offset:32768\n" \
  "ds_read_b128 v[244:247], v250 offset:49152\n" \
  "s_barrier\n" \
  "v_mfma_f32_16x16x32_bf16 v[224:227], v[192:195], v[16:19], 0\n" \
  "v_mfma_f32_16x16x32_bf16 v[228:231], v[208:211], v[16:19], 0\n" \
  "v_mfma_f32_16x16x32_bf16 v[224:227], v[196:199], v[20:23], v[224:227]\n" \
  "v_cvt_pk_bf16_f32 v232, v232, v233\n" \
  "v_cvt_pk_bf16_f32 v234, v234, v235\n" \
  "v_mfma_f32_16x16x32_bf16 v[228:231], v[212:215], v[20:23], v[228:231]\n" \
  "v_cvt_pk_bf16_f32 v233, v236, v237\n" \
  "v_cvt_pk_bf16_f32 v235, v238, v239\n" \
  "v_mfma_f32_16x16x32_bf16 v[224:227], v[200:203], v[24:27], v[224:227]\n" \
  "s_add_u32 %[cnt], %[so], 4*%c[js]+1*%c[ss]\n" \
  "buffer_store_dwordx4 v[232:235], v251, %[rs], %[cnt] offen sc1\n" \
  "v_mfma_f32_16x16x32_bf16 v[228:231], v[216:219], v[24:27], v[228:231]\n" \
  "v_mfma_f32_16x16x32_bf16 v[224:227], v[204:207], v[28:31], v[224:227]\n" \
  "v_mfma_f32_16x16x32_bf16 v[228:231], v[220:223], v[28:31], v[228:231]\n" \
  "v_mfma_f32_16x16x32_bf16 v[232:235], v[192:195], v[144:147], 0\n" \
  "v_mfma_f32_16x16x32_bf16 v[236:239], v[208:211], v[144:147], 0\n" \
  "v_mfma_f32_16x16x32_bf16 v[232:235], v[200:203], v[152:155], v[232:235]\n" \
  "v_cvt_pk_bf16_f32 v224, v224, v225\n" \
  "v_cvt_pk_bf16_f32 v226, v226, v227\n" \
  "v_mfma_f32_16x16x32_bf16 v[236:239], v[216:219], v[152:155], v[236:239]\n" \
  "v_cvt_pk_bf16_f32 v225, v228, v229\n" \
  "v_cvt_pk_bf16_f32 v227, v230, v231\n" \
  "s_waitcnt lgkmcnt(1)\n" \
  "v_mfma_f32_16x16x32_bf16 v[232:235], v[196:199], v[240:243], v[232:235]\n" \
  "s_add_u32 %[cnt], %[so], 1*%c[js]+0*%c[ss]\n" \
  "buffer_store_dwordx4 v[224:227], v251, %[rs], %[cnt] offen sc1\n" \
  "v_mfma_f32_16x16x32_bf16 v[236:239], v[212:215], v[240:243], v[236:239]\n" \
  "s_waitcnt lgkmcnt(0)\n" \
  "v_mfma_f32_16x16x32_bf16 v[232:235], v[204:207], v[244:247], v[232:235]\n" \
  "v_mfma_f32_16x16x32_bf16 v[236:239], v[220:223], v[244:247], v[236:239]\n" \
  "ds_read_b128 v[240:243], v250 offset:33792\n" \
  "ds_read_b128 v[244:247], v250 offset:50176\n" \
  "v_mfma_f32_16x16x32_bf16 v[224:227], v[192:195], v[80:83], 0\n" \
  "v_mfma_f32_16x16x32_bf16 v[228:231], v[208:211], v[80:83], 0\n" \
  "v_mfma_f32_16x16x32_bf16 v[224:227], v[196:199], v[84:87], v[224:227]\n" \
  "v_cvt_pk_bf16_f32 v232, v232, v233\n" \
  "v_cvt_pk_bf16_f32 v234, v234, v235\n" \
  "v_mfma_f32_16x16x32_bf16 v[228:231], v[212:215], v[84:87], v[228:231]\n" \
  "v_cvt_pk_bf16_f32 v233, v236, v237\n" \
  "v_cvt_pk_bf16_f32 v235, v238, v239\n" \
  "v_mfma_f32_16x16x32_bf16 v[224:227], v[200:203], v[88:91], v[224:227]\n" \
  "s_add_u32 %[cnt], %[so], 5*%c[js]+0*%c[ss]\n" \
  "buffer_store_dwordx4 v[232:235], v251, %[rs], %[cnt] offen sc1\n" \
  "v_mfma_f32_16x16x32_bf16 v[228:231], v[216:219], v[88:91], v[228:231]\n" \
  "v_mfma_f32_16x16x32_bf16 v[224:227], v[204:207], v[92:95], v[224:227]\n" \
  "v_mfma_f32_16x16x32_bf16 v[228:231], v[220:223], v[92:95], v[228:231]\n" \
  "v_mfma_f32_16x16x32_bf16 v[232:235], v[192:195], v[148:151], 0\n" \
  "v_mfma_f32_16x16x32_bf16 v[236:239], v[208:211], v[148:151], 0\n" \
  "v_mfma_f32_16x16x32_bf16 v[232:235], v[200:203], v[156:159], v[232:235]\n" \
  "v_cvt_pk_bf16_f32 v224, v224, v225\n" \
  "v_cvt_pk_bf16_f32 v226, v226, v227\n" \
  "v_mfma_f32_16x16x32_bf16 v[236:239], v[216:219], v[156:159], v[236:239]\n" \
  "v_cvt_pk_bf16_f32 v225, v228, v229\n" \
  "v_cvt_pk_bf16_f32 v227, v230, v231\n" \
  "s_waitcnt lgkmcnt(1)\n" \
  "v_mfma_f32_16x16x32_bf16 v[232:235], v[196:199], v[240:243], v[232:235]\n" \
  "s_add_u32 %[cnt], %[so], 1*%c[js]+1*%c[ss]\n" \
  "buffer_store_dwordx4 v[224:227], v251, %[rs], %[cnt] offen sc1\n" \
  "v_mfma_f32_16x16x32_bf16 v[236:239], v[212:215], v[240:243], v[236:239]\n" \
  "s_waitcnt lgkmcnt(0)\n" \
  "v_mfma_f32_16x16x32_bf16 v[232:235], v[204:207], v[244:247], v[232:235]\n" \
  "v_mfma_f32_16x16x32_bf16 v[236:239], v[220:223], v[244:247], v[236:239]\n" \
  "ds_read_b128 v[240:243], v249 offset:0\n" \
  "ds_read_b128 v[244:247], v249 offset:16384\n" \
  "s_barrier\n" \
  "v_mfma_f32_16x16x32_bf16 v[224:227], v[192:195], v[32:35], 0\n" \
  "v_mfma_f32_16x16x32_bf16 v[228:231], v[208:211], v[32:35], 0\n" \
  "v_mfma_f32_16x16x32_bf16 v[224:227], v[196:199], v[36:39], v[224:227]\n" \
  "v_cvt_pk_bf16_f32 v232, v232, v233\n" \
  "v_cvt_pk_bf16_f32 v234, v234, v235\n" \
  "v_mfma_f32_16x16x32_bf16 v[228:231], v[212:215], v[36:39], v[228:231]\n" \
  "v_cvt_pk_bf16_f32 v233, v236, v237\n" \
  "v_cvt_pk_bf16_f32 v235, v238, v239\n" \
  "v_mfma_f32_16x16x32_bf16 v[224:227], v[200:203], v[40:43], v[224:227]\n" \
  "s_add_u32 %[cnt], %[so], 5*%c[js]+1*%c[ss]\n" \
  "buffer_store_dwordx4 v[232:235], v251, %[rs], %[cnt] offen sc1\n" \
  "v_mfma_f32_16x16x32_bf16 v[228:231], v[216:219], v[40:43], v[228:231]\n" \
  "v_mfma_f32_16x16x32_bf16 v[224:227], v[204:207], v[44:47], v[224:227]\n" \
  "v_mfma_f32_16x16x32_bf16 v[228:231], v[220:223], v[44:47], v[228:231]\n" \
  "v_mfma_f32_16x16x32_bf16 v[232:235], v[192:195], v[160:163], 0\n" \
  "v_mfma_f32_16x16x32_bf16 v[236:239], v[208:211], v[160:163], 0\n" \
  "v_mfma_f32_16x16x32_bf16 v[232:235], v[200:203], v[168:171], v[232:235]\n" \
  "v_cvt_pk_bf16_f32 v224, v224, v225\n" \
  "v_cvt_pk_bf16_f32 v226, v226, v227\n" \
  "v_mfma_f32_16x16x32_bf16 v[236:239], v[216:219], v[168:171], v[236:239]\n" \
  "v_cvt_pk_bf16_f32 v225, v228, v229\n" \
  "v_cvt_pk_bf16_f32 v227, v230, v231\n" \
  "s_waitcnt lgkmcnt(1)\n" \
  "v_mfma_f32_16x16x32_bf16 v[232:235], v[196:199], v[240:243], v[232:235]\n" \
  "s_add_u32 %[cnt], %[so], 2*%c[js]+0*%c[ss]\n" \
  "buffer_store_dwordx4 v[224:227], v251, %[rs], %[cnt] offen sc1\n" \
  "v_mfma_f32_16x16x32_bf16 v[236:239], v[212:215], v[240:243], v[236:239]\n" \
  "s_waitcnt lgkmcnt(0)\n" \
  "v_mfma_f32_16x16x32_bf16 v[232:235], v[204:207], v[244:247], v[232:235]\n" \
  "v_mfma_f32_16x16x32_bf16 v[236:239], v[220:223], v[244:247], v[236:239]\n" \
  "ds_read_b128 v[240:243], v249 offset:1024\n" \
  "ds_read_b128 v[244:247], v249 offset:17408\n" \
  "v_mfma_f32_16x16x32_bf16 v[224:227], v[192:195], v[96:99], 0\n" \
  "v_mfma_f32_16x16x32_bf16 v[228:231], v[208:211], v[96:99], 0\n" \
  "v_mfma_f32_16x16x32_bf16 v[224:227], v[196:199], v[100:103], v[224:227]\n" \
  "v_cvt_pk_bf16_f32 v232, v232, v233\n" \
  "v_cvt_pk_bf16_f32 v234, v234, v235\n" \
  "v_mfma_f32_16x16x32_bf16 v[228:231], v[212:215], v[100:103], v[228:231]\n" \
  "v_cvt_pk_bf16_f32 v233, v236, v237\n" \
  "v_cvt_pk_bf16_f32 v235, v238, v239\n" \
  "v_mfma_f32_16x16x32_bf16 v[224:227], v[200:203], v[104:107], v[224:227]\n" \
  "s_add_u32 %[cnt], %[so], 6*%c[js]+0*%c[ss]\n" \
  "buffer_store_dwordx4 v[232:235], v251, %[rs], %[cnt] offen sc1\n" \
  "v_mfma_f32_16x16x32_bf16 v[228:231], v[216:219], v[104:107], v[228:231]\n" \
  "v_mfma_f32_16x16x32_bf16 v[224:227], v[204:207], v[108:111], v[224:227]\n" \
  "v_mfma_f32_16x16x32_bf16 v[228:231], v[220:223], v[108:111], v[228:231]\n" \
  "v_mfma_f32_16x16x32_bf16 v[232:235], v[192:195], v[164:167], 0\n" \
  "v_mfma_f32_16x16x32_bf16 v[236:239], v[208:211], v[164:167], 0\n" \
  "v_mfma_f32_16x16x32_bf16 v[232:235], v[200:203], v[172:175], v[232:235]\n" \
  "v_cvt_pk_bf16_f32 v224, v224, v225\n" \
  "v_cvt_pk_bf16_f32 v226, v226, v227\n" \
  "v_mfma_f32_16x16x32_bf16 v[236:239], v[216:219], v[172:175], v[236:239]\n" \
  "v_cvt_pk_bf16_f32 v225, v228, v229\n" \
  "v_cvt_pk_bf16_f32 v227, v230, v231\n" \
  "s_waitcnt lgkmcnt(1)\n" \
  "v_mfma_f32_16x16x32_bf16 v[232:235], v[196:199], v[240:243], v[232:235]\n" \
  "s_add_u32 %[cnt], %[so], 2*%c[js]+1*%c[ss]\n" \
  "buffer_store_dwordx4 v[224:227], v251, %[rs], %[cnt] offen sc1\n" \
  "v_mfma_f32_16x16x32_bf16 v[236:239], v[212:215], v[240:243], v[236:239]\n" \
  "s_waitcnt lgkmcnt(0)\n" \
  "v_mfma_f32_16x16x32_bf16 v[232:235], v[204:207], v[244:247], v[232:235]\n" \
  "v_mfma_f32_16x16x32_bf16 v[236:239], v[220:223], v[244:247], v[236:239]\n" \
  "ds_read_b128 v[240:243], v249 offset:32768\n" \
  "ds_read_b128 v[244:247], v249 offset:49152\n" \
  "s_barrier\n" \
  "v_mfma_f32_16x16x32_bf16 v[224:227], v[192:195], v[48:51], 0\n" \
  "v_mfma_f32_16x16x32_bf16 v[228:231], v[208:211], v[48:51], 0\n" \
  "v_mfma_f32_16x16x32_bf16 v[224:227], v[196:199], v[52:55], v[224:227]\n" \
  "v_cvt_pk_bf16_f32 v232, v232, v233\n" \
  "v_cvt_pk_bf16_f32 v234, v234, v235\n" \
  "v_mfma_f32_16x16x32_bf16 v[228:231], v[212:215], v[52:55], v[228:231]\n" \
  "v_cvt_pk_bf16_f32 v233, v236, v237\n" \
  "v_cvt_pk_bf16_f32 v235, v238, v239\n" \
  "v_mfma_f32_16x16x32_bf16 v[224:227], v[200:203], v[56:59], v[224:227]\n" \
  "s_add_u32 %[cnt], %[so], 6*%c[js]+1*%c[ss]\n" \
  "buffer_store_dwordx4 v[232:235], v251, %[rs], %[cnt] offen sc1\n" \
  "v_mfma_f32_16x16x32_bf16 v[228:231], v[216:219], v[56:59], v[228:231]\n" \
  "v_mfma_f32_16x16x32_bf16 v[224:227], v[204:207], v[60:63], v[224:227]\n" \
  "v_mfma_f32_16x16x32_bf16 v[228:231], v[220:223], v[60:63], v[228:231]\n" \
  "v_mfma_f32_16x16x32_bf16 v[232:235], v[192:195], v[176:179], 0\n" \
  "v_mfma_f32_16x16x32_bf16 v[236:239], v[208:211], v[176:179], 0\n" \
  "v_mfma_f32_16x16x32_bf16 v[232:235], v[200:203], v[184:187], v[232:235]\n" \
  "v_cvt_pk_bf16_f32 v224, v224, v225\n" \
  "v_cvt_pk_bf16_f32 v226, v226, v227\n" \
  "v_mfma_f32_16x16x32_bf16 v[236:239], v[216:219], v[184:187], v[236:239]\n" \
  "v_cvt_pk_bf16_f32 v225, v228, v229\n" \
  "v_cvt_pk_bf16_f32 v227, v230, v231\n" \
  "s_waitcnt lgkmcnt(1)\n" \
  "v_mfma_f32_16x16x32_bf16 v[232:235], v[196:199], v[240:243], v[232:235]\n" \
  "s_add_u32 %[cnt], %[so], 3*%c[js]+0*%c[ss]\n" \
  "buffer_store_dwordx4 v[224:227], v251, %[rs], %[cnt] offen sc1\n" \
  "v_mfma_f32_16x16x32_bf16 v[236:239], v[212:215], v[240:243], v[236:239]\n" \
  "s_waitcnt lgkmcnt(0)\n" \
  "v_mfma_f32_16x16x32_bf16 v[232:235], v[204:207], v[244:247], v[232:235]\n" \
  "v_mfma_f32_16x16x32_bf16 v[236:239], v[220:223], v[244:247], v[236:239]\n" \
  "ds_read_b128 v[240:243], v249 offset:33792\n" \
  "ds_read_b128 v[244:247], v249 offset:50176\n" \
  "v_mfma_f32_16x16x32_bf16 v[224:227], v[192:195], v[112:115], 0\n" \
  "v_mfma_f32_16x16x32_bf16 v[228:231], v[208:211], v[112:115], 0\n" \
  "v_mfma_f32_16x16x32_bf16 v[224:227], v[196:199], v[116:119], v[224:227]\n" \
  "v_cvt_pk_bf16_f32 v232, v232, v233\n" \
  "v_cvt_pk_bf16_f32 v234, v234, v235\n" \
  "v_mfma_f32_16x16x32_bf16 v[228:231], v[212:215], v[116:119], v[228:231]\n" \
  "v_cvt_pk_bf16_f32 v233, v236, v237\n" \
  "v_cvt_pk_bf16_f32 v235, v238, v239\n" \
  "v_mfma_f32_16x16x32_bf16 v[224:227], v[200:203], v[120:123], v[224:227]\n" \
  "s_add_u32 %[cnt], %[so], 7*%c[js]+0*%c[ss]\n" \
  "buffer_store_dwordx4 v[232:235], v251, %[rs], %[cnt] offen sc1\n" \
  "v_mfma_f32_16x16x32_bf16 v[228:231], v[216:219], v[120:123], v[228:231]\n" \
  "v_mfma_f32_16x16x32_bf16 v[224:227], v[204:207], v[124:127], v[224:227]\n" \
  "v_mfma_f32_16x16x32_bf16 v[228:231], v[220:223], v[124:127], v[228:231]\n" \
  "v_mfma_f32_16x16x32_bf16 v[232:235], v[192:195], v[180:183], 0\n" \
  "v_mfma_f32_16x16x32_bf16 v[236:239], v[208:211], v[180:183], 0\n" \
  "v_mfma_f32_16x16x32_bf16 v[232:235], v[200:203], v[188:191], v[232:235]\n" \
  "v_cvt_pk_bf16_f32 v224, v224, v225\n" \
  "v_cvt_pk_bf16_f32 v226, v226, v227\n" \
  "v_mfma_f32_16x16x32_bf16 v[236:239], v[216:219], v[188:191], v[236:239]\n" \
  "v_cvt_pk_bf16_f32 v225, v228, v229\n" \
  "v_cvt_pk_bf16_f32 v227, v230, v231\n" \
  "s_waitcnt lgkmcnt(1)\n" \
  "v_mfma_f32_16x16x32_bf16 v[232:235], v[196:199], v[240:243], v[232:235]\n" \
  "s_add_u32 %[cnt], %[so], 3*%c[js]+1*%c[ss]\n" \
  "buffer_store_dwordx4 v[224:227], v251, %[rs], %[cnt] offen sc1\n" \
  "v_mfma_f32_16x16x32_bf16 v[236:239], v[212:215], v[240:243], v[236:239]\n" \
  "s_waitcnt lgkmcnt(0)\n" \
  "v_mfma_f32_16x16x32_bf16 v[232:235], v[204:207], v[244:247], v[232:235]\n" \
  "v_mfma_f32_16x16x32_bf16 v[236:239], v[220:223], v[244:247], v[236:239]\n" \
  "s_nop 4\n" \
  "v_cvt_pk_bf16_f32 v232, v232, v233\n" \
  "v_cvt_pk_bf16_f32 v234, v234, v235\n" \
  "s_nop 1\n" \
  "v_cvt_pk_bf16_f32 v233, v236, v237\n" \
  "v_cvt_pk_bf16_f32 v235, v238, v239\n" \
  "s_add_u32 %[cnt], %[so], 7*%c[js]+1*%c[ss]\n" \
  "buffer_store_dwordx4 v[232:235], v251, %[rs], %[cnt] offen sc1\n" \
  "s_barrier\n" \
  "s_nop 1\n"
#define ST_XBS \
  "ds_read_b128 v[192:195], v251 offset:24576\n" \
  "ds_read_b128 v[208:211], v251 offset:28672\n" \
  "ds_read_b128 v[196:199], v251 offset:25600\n" \
  "ds_read_b128 v[212:215], v251 offset:29696\n" \
  "ds_read_b128 v[200:203], v251 offset:26624\n" \
  "ds_read_b128 v[216:219], v251 offset:30720\n" \
  "ds_read_b128 v[204:207], v251 offset:27648\n" \
  "ds_read_b128 v[220:223], v251 offset:31744\n" \
  "ds_read_b128 v[240:243], v250 offset:0\n" \
  "ds_read_b128 v[244:247], v250 offset:16384\n" \
  "s_waitcnt lgkmcnt(9)\n" \
  "v_mfma_f32_16x16x32_bf16 v[224:227], v[192:195], v[0:3], 0\n" \
  "s_waitcnt lgkmcnt(8)\n" \
  "v_mfma_f32_16x16x32_bf16 v[228:231], v[208:211], v[0:3], 0\n" \
  "s_waitcnt lgkmcnt(7)\n" \
  "v_mfma_f32_16x16x32_bf16 v[224:227], v[196:199], v[4:7], v[224:227]\n" \
  "s_waitcnt lgkmcnt(6)\n" \
  "v_mfma_f32_16x16x32_bf16 v[228:231], v[212:215], v[4:7], v[228:231]\n" \
  "s_waitcnt lgkmcnt(5)\n" \
  "v_mfma_f32_16x16x32_bf16 v[224:227], v[200:203], v[8:11], v[224:227]\n" \
  "s_waitcnt lgkmcnt(4)\n" \
  "v_mfma_f32_16x16x32_bf16 v[228:231], v[216:219], v[8:11], v[228:231]\n" \
  "s_waitcnt lgkmcnt(3)\n" \
  "v_mfma_f32_16x16x32_bf16 v[224:227], v[204:207], v[12:15], v[224:227]\n" \
  "s_waitcnt lgkmcnt(2)\n" \
  "v_mfma_f32_16x16x32_bf16 v[228:231], v[220:223], v[12:15], v[228:231]\n" \
  "v_mfma_f32_16x16x32_bf16 v[232:235], v[192:195], v[128:131], 0\n" \
  "v_mfma_f32_16x16x32_bf16 v[236:239], v[208:211], v[128:131], 0\n" \
  "v_mfma_f32_16x16x32_bf16 v[232:235], v[200:203], v[136:139], v[232:235]\n" \
  "v_cvt_pk_bf16_f32 v224, v224, v225\n" \
  "v_cvt_pk_bf16_f32 v226, v226, v227\n" \
  "v_mfma_f32_16x16x32_bf16 v[236:239], v[216:219], v[136:139], v[236:239]\n" \
  "v_cvt_pk_bf16_f32 v225, v228, v229\n" \
  "v_cvt_pk_bf16_f32 v227, v230, v231\n" \
  "s_waitcnt lgkmcnt(1)\n" \
  "v_mfma_f32_16x16x32_bf16 v[232:235], v[196:199], v[240:243], v[232:235]\n" \
  "s_add_u32 %[cnt], %[so], 0*%c[js]+0*%c[ss]\n" \
  "buffer_store_dwordx4 v[224:227], v251, %[rs], %[cnt] offen sc1\n" \
  "v_mfma_f32_16x16x32_bf16 v[236:239], v[212:215], v[240:243], v[236:239]\n" \
  "s_waitcnt lgkmcnt(0)\n" \
  "v_mfma_f32_16x16x32_bf16 v[232:235], v[204:207], v[244:247], v[232:235]\n" \
  "v_mfma_f32_16x16x32_bf16 v[236:239], v[220:223], v[244:247], v[236:239]\n" \
  "ds_read_b128 v[240:243], v250 offset:1024\n" \
  "ds_read_b128 v[244:247], v250 offset:17408\n" \
  "v_mfma_f32_16x16x32_bf16 v[224:227], v[192:195], v[64:67], 0\n" \
  "v_mfma_f32_16x16x32_bf16 v[228:231], v[208:211], v[64:67], 0\n" \
  "v_mfma_f32_16x16x32_bf16 v[224:227], v[196:199], v[68:71], v[224:227]\n" \
  "v_cvt_pk_bf16_f32 v232, v232, v233\n" \
  "v_cvt_pk_bf16_f32 v234, v234, v235\n" \
  "v_mfma_f32_16x16x32_bf16 v[228:231], v[212:215], v[68:71], v[228:231]\n" \
  "v_cvt_pk_bf16_f32 v233, v236, v237\n" \
  "v_cvt_pk_bf16_f32 v235, v238, v239\n" \
  "v_mfma_f32_16x16x32_bf16 v[224:227], v[200:203], v[72:75], v[224:227]\n" \
  "s_add_u32 %[cnt], %[so], 4*%c[js]+0*%c[ss]\n" \
  "buffer_store_dwordx4 v[232:235], v251, %[rs], %[cnt] offen sc1\n" \
  "v_mfma_f32_16x16x32_bf16 v[228:231], v[216:219], v[72:75], v[228:231]\n" \
  "v_mfma_f32_16x16x32_bf16 v[224:227], v[204:207], v[76:79], v[224:227]\n" \
  "v_mfma_f32_16x16x32_bf16 v[228:231], v[220:223], v[76:79], v[228:231]\n" \
  "v_mfma_f32_16x16x32_bf16 v[232:235], v[192:195], v[132:135], 0\n" \
  "v_mfma_f32_16x16x32_bf16 v[236:239], v[208:211], v[132:135], 0\n" \
  "v_mfma_f32_16x16x32_bf16 v[232:235], v[200:203], v[140:143], v[232:235]\n" \
  "v_cvt_pk_bf16_f32 v224, v224, v225\n" \
  "v_cvt_pk_bf16_f32 v226, v226, v227\n" \
  "v_mfma_f32_16x16x32_bf16 v[236:239], v[216:219], v[140:143], v[236:239]\n" \
  "v_cvt_pk_bf16_f32 v225, v228, v229\n" \
  "v_cvt_pk_bf16_f32 v227, v230, v231\n" \
  "s_waitcnt lgkmcnt(1)\n" \
  "v_mfma_f32_16x16x32_bf16 v[232:235], v[196:199], v[240:243], v[232:235]\n" \
  "s_add_u32 %[cnt], %[so], 0*%c[js]+1*%c[ss]\n" \
  "buffer_store_dwordx4 v[224:227], v251, %[rs], %[cnt] offen sc1\n" \
  "v_mfma_f32_16x16x32_bf16 v[236:239], v[212:215], v[240:243], v[236:239]\n" \
  "s_waitcnt lgkmcnt(0)\n" \
  "v_mfma_f32_16x16x32_bf16 v[232:235], v[204:207], v[244:247], v[232:235]\n" \
  "v_mfma_f32_16x16x32_bf16 v[236:239], v[220:223], v[244:247], v[236:239]\n" \
  "ds_read_b128 v[240:243], v250 offset:32768\n" \
  "ds_read_b128 v[244:247], v250 offset:49152\n" \
  "v_mfma_f32_16x16x32_bf16 v[224:227], v[192:195], v[16:19], 0\n" \
  "v_mfma_f32_16x16x32_bf16 v[228:231], v[208:211], v[16:19], 0\n" \
  "v_mfma_f32_16x16x32_bf16 v[224:227], v[196:199], v[20:23], v[224:227]\n" \
  "v_cvt_pk_bf16_f32 v232, v232, v233\n" \
  "v_cvt_pk_bf16_f32 v234, v234, v235\n" \
  "v_mfma_f32_16x16x32_bf16 v[228:231], v[212:215], v[20:23], v[228:231]\n" \
  "v_cvt_pk_bf16_f32 v233, v236, v237\n" \
  "v_cvt_pk_bf16_f32 v235, v238, v239\n" \
  "v_mfma_f32_16x16x32_bf16 v[224:227], v[200:203], v[24:27], v[224:227]\n" \
  "s_add_u32 %[cnt], %[so], 4*%c[js]+1*%c[ss]\n" \
  "buffer_store_dwordx4 v[232:235], v251, %[rs], %[cnt] offen sc1\n" \
  "v_mfma_f32_16x16x32_bf16 v[228:231], v[216:219], v[24:27], v[228:231]\n" \
  "v_mfma_f32_16x16x32_bf16 v[224:227], v[204:207], v[28:31], v[224:227]\n" \
  "v_mfma_f32_16x16x32_bf16 v[228:231], v[220:223], v[28:31], v[228:231]\n" \
  "v_mfma_f32_16x16x32_bf16 v[232:235], v[192:195], v[144:147], 0\n" \
  "v_mfma_f32_16x16x32_bf16 v[236:239], v[208:211], v[144:147], 0\n" \
  "v_mfma_f32_16x16x32_bf16 v[232:235], v[200:203], v[152:155], v[232:235]\n" \
  "v_cvt_pk_bf16_f32 v224, v224, v225\n" \
  "v_cvt_pk_bf16_f32 v226, v226, v227\n" \
  "v_mfma_f32_16x16x32_bf16 v[236:239], v[216:219], v[152:155], v[236:239]\n" \
  "v_cvt_pk_bf16_f32 v225, v228, v229\n" \
  "v_cvt_pk_bf16_f32 v227, v230, v231\n" \
  "s_waitcnt lgkmcnt(1)\n" \
  "v_mfma_f32_16x16x32_bf16 v[232:235], v[196:199], v[240:243], v[232:235]\n" \
  "s_add_u32 %[cnt], %[so], 1*%c[js]+0*%c[ss]\n" \
  "buffer_store_dwordx4 v[224:227], v251, %[rs], %[cnt] offen sc1\n" \
  "v_mfma_f32_16x16x32_bf16 v[236:239], v[212:215], v[240:243], v[236:239]\n" \
  "s_waitcnt lgkmcnt(0)\n" \
  "v_mfma_f32_16x16x32_bf16 v[232:235], v[204:207], v[244:247], v[232:235]\n" \
  "v_mfma_f32_16x16x32_bf16 v[236:239], v[220:223], v[244:247], v[236:239]\n" \
  "ds_read_b128 v[240:243], v250 offset:33792\n" \
  "ds_read_b128 v[244:247], v250 offset:50176\n" \
  "v_mfma_f32_16x16x32_bf16 v[224:227], v[192:195], v[80:83], 0\n" \
  "v_mfma_f32_16x16x32_bf16 v[228:231], v[208:211], v[80:83], 0\n" \
  "v_mfma_f32_16x16x32_bf16 v[224:227], v[196:199], v[84:87], v[224:227]\n" \
  "v_cvt_pk_bf16_f32 v232, v232, v233\n" \
  "v_cvt_pk_bf16_f32 v234, v234, v235\n" \
  "v_mfma_f32_16x16x32_bf16 v[228:231], v[212:215], v[84:87], v[228:231]\n" \
  "v_cvt_pk_bf16_f32 v233, v236, v237\n" \
  "v_cvt_pk_bf16_f32 v235, v238, v239\n" \
  "v_mfma_f32_16x16x32_bf16 v[224:227], v[200:203], v[88:91], v[224:227]\n" \
  "s_add_u32 %[cnt], %[so], 5*%c[js]+0*%c[ss]\n" \
  "buffer_store_dwordx4 v[232:235], v251, %[rs], %[cnt] offen sc1\n" \
  "v_mfma_f32_16x16x32_bf16 v[228:231], v[216:219], v[88:91], v[228:231]\n" \
  "v_mfma_f32_16x16x32_bf16 v[224:227], v[204:207], v[92:95], v[224:227]\n" \
  "v_mfma_f32_16x16x32_bf16 v[228:231], v[220:223], v[92:95], v[228:231]\n" \
  "v_mfma_f32_16x16x32_bf16 v[232:235], v[192:195], v[148:151], 0\n" \
  "v_mfma_f32_16x16x32_bf16 v[236:239], v[208:211], v[148:151], 0\n" \
  "v_mfma_f32_16x16x32_bf16 v[232:235], v[200:203], v[156:159], v[232:235]\n" \
  "v_cvt_pk_bf16_f32 v224, v224, v225\n" \
  "v_cvt_pk_bf16_f32 v226, v226, v227\n" \
  "v_mfma_f32_16x16x32_bf16 v[236:239], v[216:219], v[156:159], v[236:239]\n" \
  "v_cvt_pk_bf16_f32 v225, v228, v229\n" \
  "v_cvt_pk_bf16_f32 v227, v230, v231\n" \
  "s_waitcnt lgkmcnt(1)\n" \
  "v_mfma_f32_16x16x32_bf16 v[232:235], v[196:199], v[240:243], v[232:235]\n" \
  "s_add_u32 %[cnt], %[so], 1*%c[js]+1*%c[ss]\n" \
  "buffer_store_dwordx4 v[224:227], v251, %[rs], %[cnt] offen sc1\n" \
  "v_mfma_f32_16x16x32_bf16 v[236:239], v[212:215], v[240:243], v[236:239]\n" \
  "s_waitcnt lgkmcnt(0)\n" \
  "v_mfma_f32_16x16x32_bf16 v[232:235], v[204:207], v[244:247], v[232:235]\n" \
  "v_mfma_f32_16x16x32_bf16 v[236:239], v[220:223], v[244:247], v[236:239]\n" \
  "ds_read_b128 v[240:243], v249 offset:0\n" \
  "ds_read_b128 v[244:247], v249 offset:16384\n" \
  "s_barrier\n" \
  "v_mfma_f32_16x16x32_bf16 v[224:227], v[192:195], v[32:35], 0\n" \
  "v_mfma_f32_16x16x32_bf16 v[228:231], v[208:211], v[32:35], 0\n" \
  "v_mfma_f32_16x16x32_bf16 v[224:227], v[196:199], v[36:39], v[224:227]\n" \
  "v_cvt_pk_bf16_f32 v232, v232, v233\n" \
  "v_cvt_pk_bf16_f32 v234, v234, v235\n" \
  "v_mfma_f32_16x16x32_bf16 v[228:231], v[212:215], v[36:39], v[228:231]\n" \
  "v_cvt_pk_bf16_f32 v233, v236, v237\n" \
  "v_cvt_pk_bf16_f32 v235, v238, v239\n" \
  "v_mfma_f32_16x16x32_bf16 v[224:227], v[200:203], v[40:43], v[224:227]\n" \
  "s_add_u32 %[cnt], %[so], 5*%c[js]+1*%c[ss]\n" \
  "buffer_store_dwordx4 v[232:235], v251, %[rs], %[cnt] offen sc1\n" \
  "v_mfma_f32_16x16x32_bf16 v[228:231], v[216:219], v[40:43], v[228:231]\n" \
  "v_mfma_f32_16x16x32_bf16 v[224:227], v[204:207], v[44:47], v[224:227]\n" \
  "v_mfma_f32_16x16x32_bf16 v[228:231], v[220:223], v[44:47], v[228:231]\n" \
  "v_mfma_f32_16x16x32_bf16 v[232:235], v[192:195], v[160:163], 0\n" \
  "v_mfma_f32_16x16x32_bf16 v[236:239], v[208:211], v[160:163], 0\n" \
  "v_mfma_f32_16x16x32_bf16 v[232:235], v[200:203], v[168:171], v[232:235]\n" \
  "v_cvt_pk_bf16_f32 v224, v224, v225\n" \
  "v_cvt_pk_bf16_f32 v226, v226, v227\n" \
  "v_mfma_f32_16x16x32_bf16 v[236:239], v[216:219], v[168:171], v[236:239]\n" \
  "v_cvt_pk_bf16_f32 v225, v228, v229\n" \
  "v_cvt_pk_bf16_f32 v227, v230, v231\n" \
  "s_waitcnt lgkmcnt(1)\n" \
  "v_mfma_f32_16x16x32_bf16 v[232:235], v[196:199], v[240:243], v[232:235]\n" \
  "s_add_u32 %[cnt], %[so], 2*%c[js]+0*%c[ss]\n" \
  "buffer_store_dwordx4 v[224:227], v251, %[rs], %[cnt] offen sc1\n" \
  "v_mfma_f32_16x16x32_bf16 v[236:239], v[212:215], v[240:243], v[236:239]\n" \
  "s_waitcnt lgkmcnt(0)\n" \
  "v_mfma_f32_16x16x32_bf16 v[232:235], v[204:207], v[244:247], v[232:235]\n" \
  "v_mfma_f32_16x16x32_bf16 v[236:239], v[220:223], v[244:247], v[236:239]\n" \
  "ds_read_b128 v[240:243], v249 offset:1024\n" \
  "ds_read_b128 v[244:247], v249 offset:17408\n" \
  "v_mfma_f32_16x16x32_bf16 v[224:227], v[192:195], v[96:99], 0\n" \
  "v_mfma_f32_16x16x32_bf16 v[228:231], v[208:211], v[96:99], 0\n" \
  "v_mfma_f32_16x16x32_bf16 v[224:227], v[196:199], v[100:103], v[224:227]\n" \
  "v_cvt_pk_bf16_f32 v232, v232, v233\n" \
  "v_cvt_pk_bf16_f32 v234, v234, v235\n" \
  "v_mfma_f32_16x16x32_bf16 v[228:231], v[212:215], v[100:103], v[228:231]\n" \
  "v_cvt_pk_bf16_f32 v233, v236, v237\n" \
  "v_cvt_pk_bf16_f32 v235, v238, v239\n" \
  "v_mfma_f32_16x16x32_bf16 v[224:227], v[200:203], v[104:107], v[224:227]\n" \
  "s_add_u32 %[cnt], %[so], 6*%c[js]+0*%c[ss]\n" \
  "buffer_store_dwordx4 v[232:235], v251, %[rs], %[cnt] offen sc1\n" \
  "v_mfma_f32_16x16x32_bf16 v[228:231], v[216:219], v[104:107], v[228:231]\n" \
  "v_mfma_f32_16x16x32_bf16 v[224:227], v[204:207], v[108:111], v[224:227]\n" \
  "v_mfma_f32_16x16x32_bf16 v[228:231], v[220:223], v[108:111], v[228:231]\n" \
  "v_mfma_f32_16x16x32_bf16 v[232:235], v[192:195], v[164:167], 0\n" \
  "v_mfma_f32_16x16x32_bf16 v[236:239], v[208:211], v[164:167], 0\n" \
  "v_mfma_f32_16x16x32_bf16 v[232:235], v[200:203], v[172:175], v[232:235]\n" \
  "v_cvt_pk_bf16_f32 v224, v224, v225\n" \
  "v_cvt_pk_bf16_f32 v226, v226, v227\n" \
  "v_mfma_f32_16x16x32_bf16 v[236:239], v[216:219], v[172:175], v[236:239]\n" \
  "v_cvt_pk_bf16_f32 v225, v228, v229\n" \
  "v_cvt_pk_bf16_f32 v227, v230, v231\n" \
  "s_waitcnt lgkmcnt(1)\n" \
  "v_mfma_f32_16x16x32_bf16 v[232:235], v[196:199], v[240:243], v[232:235]\n" \
  "s_add_u32 %[cnt], %[so], 2*%c[js]+1*%c[ss]\n" \
  "buffer_store_dwordx4 v[224:227], v251, %[rs], %[cnt] offen sc1\n" \
  "v_mfma_f32_16x16x32_bf16 v[236:239], v[212:215], v[240:243], v[236:239]\n" \
  "s_waitcnt lgkmcnt(0)\n" \
  "v_mfma_f32_16x16x32_bf16 v[232:235], v[204:207], v[244:247], v[232:235]\n" \
  "v_mfma_f32_16x16x32_bf16 v[236:239], v[220:223], v[244:247], v[236:239]\n" \
  "ds_read_b128 v[240:243], v249 offset:32768\n" \
  "ds_read_b128 v[244:247], v249 offset:49152\n" \
  "s_barrier\n" \
  "v_mfma_f32_16x16x32_bf16 v[224:227], v[192:195], v[48:51], 0\n" \
  "v_mfma_f32_16x16x32_bf16 v[228:231], v[208:211], v[48:51], 0\n" \
  "v_mfma_f32_16x16x32_bf16 v[224:227], v[196:199], v[52:55], v[224:227]\n" \
  "v_cvt_pk_bf16_f32 v232, v232, v233\n" \
  "v_cvt_pk_bf16_f32 v234, v234, v235\n" \
  "v_mfma_f32_16x16x32_bf16 v[228:231], v[212:215], v[52:55], v[228:231]\n" \
  "v_cvt_pk_bf16_f32 v233, v236, v237\n" \
  "v_cvt_pk_bf16_f32 v235, v238, v239\n" \
  "v_mfma_f32_16x16x32_bf16 v[224:227], v[200:203], v[56:59], v[224:227]\n" \
  "s_add_u32 %[cnt], %[so], 6*%c[js]+1*%c[ss]\n" \
  "buffer_store_dwordx4 v[232:235], v251, %[rs], %[cnt] offen sc1\n" \
  "v_mfma_f32_16x16x32_bf16 v[228:231], v[216:219], v[56:59], v[228:231]\n" \
  "v_mfma_f32_16x16x32_bf16 v[224:227], v[204:207], v[60:63], v[224:227]\n" \
  "v_mfma_f32_16x16x32_bf16 v[228:231], v[220:223], v[60:63], v[228:231]\n" \
  "v_mfma_f32_16x16x32_bf16 v[232:235], v[192:195], v[176:179], 0\n" \
  "v_mfma_f32_16x16x32_bf16 v[236:239], v[208:211], v[176:179], 0\n" \
  "v_mfma_f32_16x16x32_bf16 v[232:235], v[200:203], v[184:187], v[232:235]\n" \
  "v_cvt_pk_bf16_f32 v224, v224, v225\n" \
  "v_cvt_pk_bf16_f32 v226, v226, v227\n" \
  "v_mfma_f32_16x16x32_bf16 v[236:239], v[216:219], v[184:187], v[236:239]\n" \
  "v_cvt_pk_bf16_f32 v225, v228, v229\n" \
  "v_cvt_pk_bf16_f32 v227, v230, v231\n" \
  "s_waitcnt lgkmcnt(1)\n" \
  "v_mfma_f32_16x16x32_bf16 v[232:235], v[196:199], v[240:243], v[232:235]\n" \
  "s_add_u32 %[cnt], %[so], 3*%c[js]+0*%c[ss]\n" \
  "buffer_store_dwordx4 v[224:227], v251, %[rs], %[cnt] offen sc1\n" \
  "v_mfma_f32_16x16x32_bf16 v[236:239], v[212:215], v[240:243], v[236:239]\n" \
  "s_waitcnt lgkmcnt(0)\n" \
  "v_mfma_f32_16x16x32_bf16 v[232:235], v[204:207], v[244:247], v[232:235]\n" \
  "v_mfma_f32_16x16x32_bf16 v[236:239], v[220:223], v[244:247], v[236:239]\n" \
  "ds_read_b128 v[240:243], v249 offset:33792\n" \
  "ds_read_b128 v[244:247], v249 offset:50176\n" \
  "v_mfma_f32_16x16x32_bf16 v[224:227], v[192:195], v[112:115], 0\n" \
  "v_mfma_f32_16x16x32_bf16 v[228:231], v[208:211], v[112:115], 0\n" \
  "v_mfma_f32_16x16x32_bf16 v[224:227], v[196:199], v[116:119], v[224:227]\n" \
  "v_cvt_pk_bf16_f32 v232, v232, v233\n" \
  "v_cvt_pk_bf16_f32 v234, v234, v235\n" \
  "v_mfma_f32_16x16x32_bf16 v[228:231], v[212:215], v[116:119], v[228:231]\n" \
  "v_cvt_pk_bf16_f32 v233, v236, v237\n" \
  "v_cvt_pk_bf16_f32 v235, v238, v239\n" \
  "v_mfma_f32_16x16x32_bf16 v[224:227], v[200:203], v[120:123], v[224:227]\n" \
  "s_add_u32 %[cnt], %[so], 7*%c[js]+0*%c[ss]\n" \
  "buffer_store_dwordx4 v[232:235], v251, %[rs], %[cnt] offen sc1\n" \
  "v_mfma_f32_16x16x32_bf16 v[228:231], v[216:219], v[120:123], v[228:231]\n" \
  "v_mfma_f32_16x16x32_bf16 v[224:227], v[204:207], v[124:127], v[224:227]\n" \
  "v_mfma_f32_16x16x32_bf16 v[228:231], v[220:223], v[124:127], v[228:231]\n" \
  "v_mfma_f32_16x16x32_bf16 v[232:235], v[192:195], v[180:183], 0\n" \
  "v_mfma_f32_16x16x32_bf16 v[236:239], v[208:211], v[180:183], 0\n" \
  "v_mfma_f32_16x16x32_bf16 v[232:235], v[200:203], v[188:191], v[232:235]\n" \
  "v_cvt_pk_bf16_f32 v224, v224, v225\n" \
  "v_cvt_pk_bf16_f32 v226, v226, v227\n" \
  "v_mfma_f32_16x16x32_bf16 v[236:239], v[216:219], v[188:191], v[236:239]\n" \
  "v_cvt_pk_bf16_f32 v225, v228, v229\n" \
  "v_cvt_pk_bf16_f32 v227, v230, v231\n" \
  "s_waitcnt lgkmcnt(1)\n" \
  "v_mfma_f32_16x16x32_bf16 v[232:235], v[196:199], v[240:243], v[232:235]\n" \
  "s_add_u32 %[cnt], %[so], 3*%c[js]+1*%c[ss]\n" \
  "buffer_store_dwordx4 v[224:227], v251, %[rs], %[cnt] offen sc1\n" \
  "v_mfma_f32_16x16x32_bf16 v[236:239], v[212:215], v[240:243], v[236:239]\n" \
  "s_waitcnt lgkmcnt(0)\n" \
  "v_mfma_f32_16x16x32_bf16 v[232:235], v[204:207], v[244:247], v[232:235]\n" \
  "v_mfma_f32_16x16x32_bf16 v[236:239], v[220:223], v[244:247], v[236:239]\n" \
  "s_nop 4\n" \
  "v_cvt_pk_bf16_f32 v232, v232, v233\n" \
  "v_cvt_pk_bf16_f32 v234, v234, v235\n" \
  "s_nop 1\n" \
  "v_cvt_pk_bf16_f32 v233, v236, v237\n" \
  "v_cvt_pk_bf16_f32 v235, v238, v239\n" \
  "s_add_u32 %[cnt], %[so], 7*%c[js]+1*%c[ss]\n" \
  "buffer_store_dwordx4 v[232:235], v251, %[rs], %[cnt] offen sc1\n" \
  "s_nop 1\n"
#define ST_XB0 \
  "ds_read_b128 v[192:195], v251 offset:24576\n" \
  "ds_read_b128 v[208:211], v251 offset:28672\n" \
  "ds_read_b128 v[196:199], v251 offset:25600\n" \
  "ds_read_b128 v[212:215], v251 offset:29696\n" \
  "ds_read_b128 v[200:203], v251 offset:26624\n" \
  "ds_read_b128 v[216:219], v251 offset:30720\n" \
  "ds_read_b128 v[204:207], v251 offset:27648\n" \
  "ds_read_b128 v[220:223], v251 offset:31744\n" \
  "ds_read_b128 v[240:243], v250 offset:0\n" \
  "ds_read_b128 v[244:247], v250 offset:16384\n" \
  "s_waitcnt lgkmcnt(9)\n" \
  "v_mfma_f32_16x16x32_bf16 v[224:227], v[192:195], v[0:3], 0\n" \
  "s_waitcnt lgkmcnt(8)\n" \
  "v_mfma_f32_16x16x32_bf16 v[228:231], v[208:211], v[0:3], 0\n" \
  "s_waitcnt lgkmcnt(7)\n" \
  "v_mfma_f32_16x16x32_bf16 v[224:227], v[196:199], v[4:7], v[224:227]\n" \
  "s_waitcnt lgkmcnt(6)\n" \
  "v_mfma_f32_16x16x32_bf16 v[228:231], v[212:215], v[4:7], v[228:231]\n" \
  "s_waitcnt lgkmcnt(5)\n" \
  "v_mfma_f32_16x16x32_bf16 v[224:227], v[200:203], v[8:11], v[224:227]\n" \
  "s_waitcnt lgkmcnt(4)\n" \
  "v_mfma_f32_16x16x32_bf16 v[228:231], v[216:219], v[8:11], v[228:231]\n" \
  "s_waitcnt lgkmcnt(3)\n" \
  "v_mfma_f32_16x16x32_bf16 v[224:227], v[204:207], v[12:15], v[224:227]\n" \
  "s_waitcnt lgkmcnt(2)\n" \
  "v_mfma_f32_16x16x32_bf16 v[228:231], v[220:223], v[12:15], v[228:231]\n" \
  "v_mfma_f32_16x16x32_bf16 v[232:235], v[192:195], v[128:131], 0\n" \
  "v_mfma_f32_16x16x32_bf16 v[236:239], v[208:211], v[128:131], 0\n" \
  "v_mfma_f32_16x16x32_bf16 v[232:235], v[200:203], v[136:139], v[232:235]\n" \
  "v_cvt_pk_bf16_f32 v224, v224, v225\n" \
  "v_cvt_pk_bf16_f32 v226, v226, v227\n" \
  "v_mfma_f32_16x16x32_bf16 v[236:239], v[216:219], v[136:139], v[236:239]\n" \
  "v_cvt_pk_bf16_f32 v225, v228, v229\n" \
  "v_cvt_pk_bf16_f32 v227, v230, v231\n" \
  "s_waitcnt lgkmcnt(1)\n" \
  "v_mfma_f32_16x16x32_bf16 v[232:235], v[196:199], v[240:243], v[232:235]\n" \
  "s_add_u32 %[cnt], %[so], 0*%c[js]+0*%c[ss]\n" \
  "buffer_store_dwordx4 v[224:227], v251, %[rs], %[cnt] offen sc1\n" \
  "v_mfma_f32_16x16x32_bf16 v[236:239], v[212:215], v[240:243], v[236:239]\n" \
  "s_waitcnt lgkmcnt(0)\n" \
  "v_mfma_f32_16x16x32_bf16 v[232:235], v[204:207], v[244:247], v[232:235]\n" \
  "v_mfma_f32_16x16x32_bf16 v[236:239], v[220:223], v[244:247], v[236:239]\n" \
  "ds_read_b128 v[240:243], v250 offset:1024\n" \
  "ds_read_b128 v[244:247], v250 offset:17408\n" \
  "v_mfma_f32_16x16x32_bf16 v[224:227], v[192:195], v[64:67], 0\n" \
  "v_mfma_f32_16x16x32_bf16 v[228:231], v[208:211], v[64:67], 0\n" \
  "v_mfma_f32_16x16x32_bf16 v[224:227], v[196:199], v[68:71], v[224:227]\n" \
  "v_cvt_pk_bf16_f32 v232, v232, v233\n" \
  "v_cvt_pk_bf16_f32 v234, v234, v235\n" \
  "v_mfma_f32_16x16x32_bf16 v[228:231], v[212:215], v[68:71], v[228:231]\n" \
  "v_cvt_pk_bf16_f32 v233, v236, v237\n" \
  "v_cvt_pk_bf16_f32 v235, v238, v239\n" \
  "v_mfma_f32_16x16x32_bf16 v[224:227], v[200:203], v[72:75], v[224:227]\n" \
  "s_add_u32 %[cnt], %[so], 4*%c[js]+0*%c[ss]\n" \
  "buffer_store_dwordx4 v[232:235], v251, %[rs], %[cnt] offen sc1\n" \
  "v_mfma_f32_16x16x32_bf16 v[228:231], v[216:219], v[72:75], v[228:231]\n" \
  "v_mfma_f32_16x16x32_bf16 v[224:227], v[204:207], v[76:79], v[224:227]\n" \
  "v_mfma_f32_16x16x32_bf16 v[228:231], v[220:223], v[76:79], v[228:231]\n" \
  "v_mfma_f32_16x16x32_bf16 v[232:235], v[192:195], v[132:135], 0\n" \
  "v_mfma_f32_16x16x32_bf16 v[236:239], v[208:211], v[132:135], 0\n" \
  "v_mfma_f32_16x16x32_bf16 v[232:235], v[200:203], v[140:143], v[232:235]\n" \
  "v_cvt_pk_bf16_f32 v224, v224, v225\n" \
  "v_cvt_pk_bf16_f32 v226, v226, v227\n" \
  "v_mfma_f32_16x16x32_bf16 v[236:239], v[216:219], v[140:143], v[236:239]\n" \
  "v_cvt_pk_bf16_f32 v225, v228, v229\n" \
  "v_cvt_pk_bf16_f32 v227, v230, v231\n" \
  "s_waitcnt lgkmcnt(1)\n" \
  "v_mfma_f32_16x16x32_bf16 v[232:235], v[196:199], v[240:243], v[232:235]\n" \
  "s_add_u32 %[cnt], %[so], 0*%c[js]+1*%c[ss]\n" \
  "buffer_store_dwordx4 v[224:227], v251, %[rs], %[cnt] offen sc1\n" \
  "v_mfma_f32_16x16x32_bf16 v[236:239], v[212:215], v[240:243], v[236:239]\n" \
  "s_waitcnt lgkmcnt(0)\n" \
  "v_mfma_f32_16x16x32_bf16 v[232:235], v[204:207], v[244:247], v[232:235]\n" \
  "v_mfma_f32_16x16x32_bf16 v[236:239], v[220:223], v[244:247], v[236:239]\n" \
  "ds_read_b128 v[240:243], v250 offset:32768\n" \
  "ds_read_b128 v[244:247], v250 offset:49152\n" \
  "v_mfma_f32_16x16x32_bf16 v[224:227], v[192:195], v[16:19], 0\n" \
  "v_mfma_f32_16x16x32_bf16 v[228:231], v[208:211], v[16:19], 0\n" \
  "v_mfma_f32_16x16x32_bf16 v[224:227], v[196:199], v[20:23], v[224:227]\n" \
  "v_cvt_pk_bf16_f32 v232, v232, v233\n" \
  "v_cvt_pk_bf16_f32 v234, v234, v235\n" \
  "v_mfma_f32_16x16x32_bf16 v[228:231], v[212:215], v[20:23], v[228:231]\n" \
  "v_cvt_pk_bf16_f32 v233, v236, v237\n" \
  "v_cvt_pk_bf16_f32 v235, v238, v239\n" \
  "v_mfma_f32_16x16x32_bf16 v[224:227], v[200:203], v[24:27], v[224:227]\n" \
  "s_add_u32 %[cnt], %[so], 4*%c[js]+1*%c[ss]\n" \
  "buffer_store_dwordx4 v[232:235], v251, %[rs], %[cnt] offen sc1\n" \
  "v_mfma_f32_16x16x32_bf16 v[228:231], v[216:219], v[24:27], v[228:231]\n" \
  "v_mfma_f32_16x16x32_bf16 v[224:227], v[204:207], v[28:31], v[224:227]\n" \
  "v_mfma_f32_16x16x32_bf16 v[228:231], v[220:223], v[28:31], v[228:231]\n" \
  "v_mfma_f32_16x16x32_bf16 v[232:235], v[192:195], v[144:147], 0\n" \
  "v_mfma_f32_16x16x32_bf16 v[236:239], v[208:211], v[144:147], 0\n" \
  "v_mfma_f32_16x16x32_bf16 v[232:235], v[200:203], v[152:155], v[232:235]\n" \
  "v_cvt_pk_bf16_f32 v224, v224, v225\n" \
  "v_cvt_pk_bf16_f32 v226, v226, v227\n" \
  "v_mfma_f32_16x16x32_bf16 v[236:239], v[216:219], v[152:155], v[236:239]\n" \
  "v_cvt_pk_bf16_f32 v225, v228, v229\n" \
  "v_cvt_pk_bf16_f32 v227, v230, v231\n" \
  "s_waitcnt lgkmcnt(1)\n" \
  "v_mfma_f32_16x16x32_bf16 v[232:235], v[196:199], v[240:243], v[232:235]\n" \
  "s_add_u32 %[cnt], %[so], 1*%c[js]+0*%c[ss]\n" \
  "buffer_store_dwordx4 v[224:227], v251, %[rs], %[cnt] offen sc1\n" \
  "v_mfma_f32_16x16x32_bf16 v[236:239], v[212:215], v[240:243], v[236:239]\n" \
  "s_waitcnt lgkmcnt(0)\n" \
  "v_mfma_f32_16x16x32_bf16 v[232:235], v[204:207], v[244:247], v[232:235]\n" \
  "v_mfma_f32_16x16x32_bf16 v[236:239], v[220:223], v[244:247], v[236:239]\n" \
  "ds_read_b128 v[240:243], v250 offset:33792\n" \
  "ds_read_b128 v[244:247], v250 offset:50176\n" \
  "v_mfma_f32_16x16x32_bf16 v[224:227], v[192:195], v[80:83], 0\n" \
  "v_mfma_f32_16x16x32_bf16 v[228:231], v[208:211], v[80:83], 0\n" \
  "v_mfma_f32_16x16x32_bf16 v[224:227], v[196:199], v[84:87], v[224:227]\n" \
  "v_cvt_pk_bf16_f32 v232, v232, v233\n" \
  "v_cvt_pk_bf16_f32 v234, v234, v235\n" \
  "v_mfma_f32_16x16x32_bf16 v[228:231], v[212:215], v[84:87], v[228:231]\n" \
  "v_cvt_pk_bf16_f32 v233, v236, v237\n" \
  "v_cvt_pk_bf16_f32 v235, v238, v239\n" \
  "v_mfma_f32_16x16x32_bf16 v[224:227], v[200:203], v[88:91], v[224:227]\n" \
  "s_add_u32 %[cnt], %[so], 5*%c[js]+0*%c[ss]\n" \
  "buffer_store_dwordx4 v[232:235], v251, %[rs], %[cnt] offen sc1\n" \
  "v_mfma_f32_16x16x32_bf16 v[228:231], v[216:219], v[88:91], v[228:231]\n" \
  "v_mfma_f32_16x16x32_bf16 v[224:227], v[204:207], v[92:95], v[224:227]\n" \
  "v_mfma_f32_16x16x32_bf16 v[228:231], v[220:223], v[92:95], v[228:231]\n" \
  "v_mfma_f32_16x16x32_bf16 v[232:235], v[192:195], v[148:151], 0\n" \
  "v_mfma_f32_16x16x32_bf16 v[236:239], v[208:211], v[148:151], 0\n" \
  "v_mfma_f32_16x16x32_bf16 v[232:235], v[200:203], v[156:159], v[232:235]\n" \
  "v_cvt_pk_bf16_f32 v224, v224, v225\n" \
  "v_cvt_pk_bf16_f32 v226, v226, v227\n" \
  "v_mfma_f32_16x16x32_bf16 v[236:239], v[216:219], v[156:159], v[236:239]\n" \
  "v_cvt_pk_bf16_f32 v225, v228, v229\n" \
  "v_cvt_pk_bf16_f32 v227, v230, v231\n" \
  "s_waitcnt lgkmcnt(1)\n" \
  "v_mfma_f32_16x16x32_bf16 v[232:235], v[196:199], v[240:243], v[232:235]\n" \
  "s_add_u32 %[cnt], %[so], 1*%c[js]+1*%c[ss]\n" \
  "buffer_store_dwordx4 v[224:227], v251, %[rs], %[cnt] offen sc1\n" \
  "v_mfma_f32_16x16x32_bf16 v[236:239], v[212:215], v[240:243], v[236:239]\n" \
  "s_waitcnt lgkmcnt(0)\n" \
  "v_mfma_f32_16x16x32_bf16 v[232:235], v[204:207], v[244:247], v[232:235]\n" \
  "v_mfma_f32_16x16x32_bf16 v[236:239], v[220:223], v[244:247], v[236:239]\n" \
  "ds_read_b128 v[240:243], v249 offset:0\n" \
  "ds_read_b128 v[244:247], v249 offset:16384\n" \
  "v_mfma_f32_16x16x32_bf16 v[224:227], v[192:195], v[32:35], 0\n" \
  "v_mfma_f32_16x16x32_bf16 v[228:231], v[208:211], v[32:35], 0\n" \
  "v_mfma_f32_16x16x32_bf16 v[224:227], v[196:199], v[36:39], v[224:227]\n" \
  "v_cvt_pk_bf16_f32 v232, v232, v233\n" \
  "v_cvt_pk_bf16_f32 v234, v234, v235\n" \
  "v_mfma_f32_16x16x32_bf16 v[228:231], v[212:215], v[36:39], v[228:231]\n" \
  "v_cvt_pk_bf16_f32 v233, v236, v237\n" \
  "v_cvt_pk_bf16_f32 v235, v238, v239\n" \
  "v_mfma_f32_16x16x32_bf16 v[224:227], v[200:203], v[40:43], v[224:227]\n" \
  "s_add_u32 %[cnt], %[so], 5*%c[js]+1*%c[ss]\n" \
  "buffer_store_dwordx4 v[232:235], v251, %[rs], %[cnt] offen sc1\n" \
  "v_mfma_f32_16x16x32_bf16 v[228:231], v[216:219], v[40:43], v[228:231]\n" \
  "v_mfma_f32_16x16x32_bf16 v[224:227], v[204:207], v[44:47], v[224:227]\n" \
  "v_mfma_f32_16x16x32_bf16 v[228:231], v[220:223], v[44:47], v[228:231]\n" \
  "v_mfma_f32_16x16x32_bf16 v[232:235], v[192:195], v[160:163], 0\n" \
  "v_mfma_f32_16x16x32_bf16 v[236:239], v[208:211], v[160:163], 0\n" \
  "v_mfma_f32_16x16x32_bf16 v[232:235], v[200:203], v[168:171], v[232:235]\n" \
  "v_cvt_pk_bf16_f32 v224, v224, v225\n" \
  "v_cvt_pk_bf16_f32 v226, v226, v227\n" \
  "v_mfma_f32_16x16x32_bf16 v[236:239], v[216:219], v[168:171], v[236:239]\n" \
  "v_cvt_pk_bf16_f32 v225, v228, v229\n" \
  "v_cvt_pk_bf16_f32 v227, v230, v231\n" \
  "s_waitcnt lgkmcnt(1)\n" \
  "v_mfma_f32_16x16x32_bf16 v[232:235], v[196:199], v[240:243], v[232:235]\n" \
  "s_add_u32 %[cnt], %[so], 2*%c[js]+0*%c[ss]\n" \
  "buffer_store_dwordx4 v[224:227], v251, %[rs], %[cnt] offen sc1\n" \
  "v_mfma_f32_16x16x32_bf16 v[236:239], v[212:215], v[240:243], v[236:239]\n" \
  "s_waitcnt lgkmcnt(0)\n" \
  "v_mfma_f32_16x16x32_bf16 v[232:235], v[204:207], v[244:247], v[232:235]\n" \
  "v_mfma_f32_16x16x32_bf16 v[236:239], v[220:223], v[244:247], v[236:239]\n" \
  "ds_read_b128 v[240:243], v249 offset:1024\n" \
  "ds_read_b128 v[244:247], v249 offset:17408\n" \
  "v_mfma_f32_16x16x32_bf16 v[224:227], v[192:195], v[96:99], 0\n" \
  "v_mfma_f32_16x16x32_bf16 v[228:231], v[208:211], v[96:99], 0\n" \
  "v_mfma_f32_16x16x32_bf16 v[224:227], v[196:199], v[100:103], v[224:227]\n" \
  "v_cvt_pk_bf16_f32 v232, v232, v233\n" \
  "v_cvt_pk_bf16_f32 v234, v234, v235\n" \
  "v_mfma_f32_16x16x32_bf16 v[228:231], v[212:215], v[100:103], v[228:231]\n" \
  "v_cvt_pk_bf16_f32 v233, v236, v237\n" \
  "v_cvt_pk_bf16_f32 v235, v238, v239\n" \
  "v_mfma_f32_16x16x32_bf16 v[224:227], v[200:203], v[104:107], v[224:227]\n" \
  "s_add_u32 %[cnt], %[so], 6*%c[js]+0*%c[ss]\n" \
  "buffer_store_dwordx4 v[232:235], v251, %[rs], %[cnt] offen sc1\n" \
  "v_mfma_f32_16x16x32_bf16 v[228:231], v[216:219], v[104:107], v[228:231]\n" \
  "v_mfma_f32_16x16x32_bf16 v[224:227], v[204:207], v[108:111], v[224:227]\n" \
  "v_mfma_f32_16x16x32_bf16 v[228:231], v[220:223], v[108:111], v[228:231]\n" \
  "v_mfma_f32_16x16x32_bf16 v[232:235], v[192:195], v[164:167], 0\n" \
  "v_mfma_f32_16x16x32_bf16 v[236:239], v[208:211], v[164:167], 0\n" \
  "v_mfma_f32_16x16x32_bf16 v[232:235], v[200:203], v[172:175], v[232:235]\n" \
  "v_cvt_pk_bf16_f32 v224, v224, v225\n" \
  "v_cvt_pk_bf16_f32 v226, v226, v227\n" \
  "v_mfma_f32_16x16x32_bf16 v[236:239], v[216:219], v[172:175], v[236:239]\n" \
  "v_cvt_pk_bf16_f32 v225, v228, v229\n" \
  "v_cvt_pk_bf16_f32 v227, v230, v231\n" \
  "s_waitcnt lgkmcnt(1)\n" \
  "v_mfma_f32_16x16x32_bf16 v[232:235], v[196:199], v[240:243], v[232:235]\n" \
  "s_add_u32 %[cnt], %[so], 2*%c[js]+1*%c[ss]\n" \
  "buffer_store_dwordx4 v[224:227], v251, %[rs], %[cnt] offen sc1\n" \
  "v_mfma_f32_16x16x32_bf16 v[236:239], v[212:215], v[240:243], v[236:239]\n" \
  "s_waitcnt lgkmcnt(0)\n" \
  "v_mfma_f32_16x16x32_bf16 v[232:235], v[204:207], v[244:247], v[232:235]\n" \
  "v_mfma_f32_16x16x32_bf16 v[236:239], v[220:223], v[244:247], v[236:239]\n" \
  "ds_read_b128 v[240:243], v249 offset:32768\n" \
  "ds_read_b128 v[244:247], v249 offset:49152\n" \
  "v_mfma_f32_16x16x32_bf16 v[224:227], v[192:195], v[48:51], 0\n" \
  "v_mfma_f32_16x16x32_bf16 v[228:231], v[208:211], v[48:51], 0\n" \
  "v_mfma_f32_16x16x32_bf16 v[224:227], v[196:199], v[52:55], v[224:227]\n" \
  "v_cvt_pk_bf16_f32 v232, v232, v233\n" \
  "v_cvt_pk_bf16_f32 v234, v234, v235\n" \
  "v_mfma_f32_16x16x32_bf16 v[228:231], v[212:215], v[52:55], v[228:231]\n" \
  "v_cvt_pk_bf16_f32 v233, v236, v237\n" \
  "v_cvt_pk_bf16_f32 v235, v238, v239\n" \
  "v_mfma_f32_16x16x32_bf16 v[224:227], v[200:203], v[56:59], v[224:227]\n" \
  "s_add_u32 %[cnt], %[so], 6*%c[js]+1*%c[ss]\n" \
  "buffer_store_dwordx4 v[232:235], v251, %[rs], %[cnt] offen sc1\n" \
  "v_mfma_f32_16x16x32_bf16 v[228:231], v[216:219], v[56:59], v[228:231]\n" \
  "v_mfma_f32_16x16x32_bf16 v[224:227], v[204:207], v[60:63], v[224:227]\n" \
  "v_mfma_f32_16x16x32_bf16 v[228:231], v[220:223], v[60:63], v[228:231]\n" \
  "v_mfma_f32_16x16x32_bf16 v[232:235], v[192:195], v[176:179], 0\n" \
  "v_mfma_f32_16x16x32_bf16 v[236:239], v[208:211], v[176:179], 0\n" \
  "v_mfma_f32_16x16x32_bf16 v[232:235], v[200:203], v[184:187], v[232:235]\n" \
  "v_cvt_pk_bf16_f32 v224, v224, v225\n" \
  "v_cvt_pk_bf16_f32 v226, v226, v227\n" \
  "v_mfma_f32_16x16x32_bf16 v[236:239], v[216:219], v[184:187], v[236:239]\n" \
  "v_cvt_pk_bf16_f32 v225, v228, v229\n" \
  "v_cvt_pk_bf16_f32 v227, v230, v231\n" \
  "s_waitcnt lgkmcnt(1)\n" \
  "v_mfma_f32_16x16x32_bf16 v[232:235], v[196:199], v[240:243], v[232:235]\n" \
  "s_add_u32 %[cnt], %[so], 3*%c[js]+0*%c[ss]\n" \
  "buffer_store_dwordx4 v[224:227], v251, %[rs], %[cnt] offen sc1\n" \
  "v_mfma_f32_16x16x32_bf16 v[236:239], v[212:215], v[240:243], v[236:239]\n" \
  "s_waitcnt lgkmcnt(0)\n" \
  "v_mfma_f32_16x16x32_bf16 v[232:235], v[204:207], v[244:247], v[232:235]\n" \
  "v_mfma_f32_16x16x32_bf16 v[236:239], v[220:223], v[244:247], v[236:239]\n" \
  "ds_read_b128 v[240:243], v249 offset:33792\n" \
  "ds_read_b128 v[244:247], v249 offset:50176\n" \
  "v_mfma_f32_16x16x32_bf16 v[224:227], v[192:195], v[112:115], 0\n" \
  "v_mfma_f32_16x16x32_bf16 v[228:231], v[208:211], v[112:115], 0\n" \
  "v_mfma_f32_16x16x32_bf16 v[224:227], v[196:199], v[116:119], v[224:227]\n" \
  "v_cvt_pk_bf16_f32 v232, v232, v233\n" \
  "v_cvt_pk_bf16_f32 v234, v234, v235\n" \
  "v_mfma_f32_16x16x32_bf16 v[228:231], v[212:215], v[116:119], v[228:231]\n" \
  "v_cvt_pk_bf16_f32 v233, v236, v237\n" \
  "v_cvt_pk_bf16_f32 v235, v238, v239\n" \
  "v_mfma_f32_16x16x32_bf16 v[224:227], v[200:203], v[120:123], v[224:227]\n" \
  "s_add_u32 %[cnt], %[so], 7*%c[js]+0*%c[ss]\n" \
  "buffer_store_dwordx4 v[232:235], v251, %[rs], %[cnt] offen sc1\n" \
  "v_mfma_f32_16x16x32_bf16 v[228:231], v[216:219], v[120:123], v[228:231]\n" \
  "v_mfma_f32_16x16x32_bf16 v[224:227], v[204:207], v[124:127], v[224:227]\n" \
  "v_mfma_f32_16x16x32_bf16 v[228:231], v[220:223], v[124:127], v[228:231]\n" \
  "v_mfma_f32_16x16x32_bf16 v[232:235], v[192:195], v[180:183], 0\n" \
  "v_mfma_f32_16x16x32_bf16 v[236:239], v[208:211], v[180:183], 0\n" \
  "v_mfma_f32_16x16x32_bf16 v[232:235], v[200:203], v[188:191], v[232:235]\n" \
  "v_cvt_pk_bf16_f32 v224, v224, v225\n" \
  "v_cvt_pk_bf16_f32 v226, v226, v227\n" \
  "v_mfma_f32_16x16x32_bf16 v[236:239], v[216:219], v[188:191], v[236:239]\n" \
  "v_cvt_pk_bf16_f32 v225, v228, v229\n" \
  "v_cvt_pk_bf16_f32 v227, v230, v231\n" \
  "s_waitcnt lgkmcnt(1)\n" \
  "v_mfma_f32_16x16x32_bf16 v[232:235], v[196:199], v[240:243], v[232:235]\n" \
  "s_add_u32 %[cnt], %[so], 3*%c[js]+1*%c[ss]\n" \
  "buffer_store_dwordx4 v[224:227], v251, %[rs], %[cnt] offen sc1\n" \
  "v_mfma_f32_16x16x32_bf16 v[236:239], v[212:215], v[240:243], v[236:239]\n" \
  "s_waitcnt lgkmcnt(0)\n" \
  "v_mfma_f32_16x16x32_bf16 v[232:235], v[204:207], v[244:247], v[232:235]\n" \
  "v_mfma_f32_16x16x32_bf16 v[236:239], v[220:223], v[244:247], v[236:239]\n" \
  "s_nop 4\n" \
  "v_cvt_pk_bf16_f32 v232, v232, v233\n" \
  "v_cvt_pk_bf16_f32 v234, v234, v235\n" \
  "s_nop 1\n" \
  "v_cvt_pk_bf16_f32 v233, v236, v237\n" \
  "v_cvt_pk_bf16_f32 v235, v238, v239\n" \
  "s_add_u32 %[cnt], %[so], 7*%c[js]+1*%c[ss]\n" \
  "buffer_store_dwordx4 v[232:235], v251, %[rs], %[cnt] offen sc1\n" \
  "s_nop 1\n"
#define XBF_NB 5
#define XBS_NB 2
#define F_ORD "01ab2cd3"
#define VO_CLOB "v192", "v193", "v194", "v195", "v196", "v197", "v198", "v199", "v200", "v201", "v202", "v203", "v204", "v205", "v206", "v207", "v208", "v209", "v210", "v211", "v212", "v213", "v214", "v215", "v216", "v217", "v218", "v219", "v220", "v221", "v222", "v223", "v224", "v225", "v226", "v227", "v228", "v229", "v230", "v231", "v232", "v233", "v234", "v235", "v236", "v237", "v238", "v239"
#define QK_CLOB "v192", "v193", "v194", "v195", "v196", "v197", "v198", "v199", "v200", "v201", "v202", "v203", "v204", "v205", "v206", "v207", "v208", "v209", "v210", "v211", "v212", "v213", "v214", "v215", "v216", "v217", "v218", "v219", "v220", "v221", "v222", "v223", "v224", "v225", "v226", "v227", "v228", "v229", "v230", "v231", "v232", "v233", "v234", "v235", "v236", "v237", "v238", "v239"
#define QX_CLOB "v216", "v217", "v218", "v219", "v220", "v221", "v222", "v223", "v224", "v225", "v226", "v227", "v228", "v229", "v230", "v231", "v232", "v233", "v234", "v235", "v236", "v237", "v238", "v239", "v241"
#define ST_CLOB "v192", "v193", "v194", "v195", "v196", "v197", "v198", "v199", "v200", "v201", "v202", "v203", "v204", "v205", "v206", "v207", "v240", "v241", "v242", "v243", "v244", "v245", "v246", "v247", "v253"
#define XB_CLOB "v192", "v193", "v194", "v195", "v196", "v197", "v198", "v199", "v200", "v201", "v202", "v203", "v204", "v205", "v206", "v207", "v208", "v209", "v210", "v211", "v212", "v213", "v214", "v215", "v216", "v217", "v218", "v219", "v220", "v221", "v222", "v223", "v224", "v225", "v226", "v227", "v228", "v229", "v230", "v231", "v232", "v233", "v234", "v235", "v236", "v237", "v238", "v239", "v240", "v241", "v242", "v243", "v244", "v245", "v246", "v247", "v253"
#define OF_CLOB "v32", "v33", "v34", "v35", "v36", "v37", "v38", "v39", "v40", "v41", "v42", "v43", "v44", "v45", "v46", "v47", "v48", "v49", "v50", "v51", "v52", "v53", "v54", "v55", "v56", "v57", "v58", "v59", "v60", "v61", "v62", "v63", "v64", "v65", "v66", "v67", "v68", "v69", "v70", "v71", "v72", "v73", "v74", "v75", "v76", "v77", "v78", "v79", "v80", "v81", "v82", "v83", "v84", "v85", "v86", "v87", "v88", "v89", "v90", "v91", "v92", "v93", "v94", "v95", "v96", "v97", "v98", "v99", "v100", "v101", "v102", "v103", "v104", "v105", "v106", "v107", "v108", "v109", "v110", "v111"
#define OE_CLOB "v192", "v193", "v194", "v195", "v196", "v197", "v198", "v199", "v200", "v201", "v202", "v203", "v204", "v205", "v206", "v207", "v208", "v209", "v210", "v211", "v212", "v213", "v214", "v215", "v216", "v217", "v218", "v219", "v220", "v221", "v222", "v223", "v224", "v225", "v226", "v227", "v228", "v229", "v230", "v231", "v232", "v233", "v234", "v235", "v236", "v237", "v238", "v239", "v240"
constexpr int L_W = 32768;
constexpr int L_HI = 16384;
#define MARG 110
#define ED0 720
#define EA0 1290
#define SDK 450
#define TOL 145
#define XLATE 130
#define TFR 900
#define QT 0
#define HSU 8
#define WNT 3
#define KROT 7
#define KSL2 0
#define KSL 5
#define NCY 1600
#define NCYV (NCY + 55)
#define NRY 24
#define NRX 30
#define NF1O 2
#define NPUB 2
#define NXF 4
#define RSL 2
#define PFL 4
#define EVT2R
__device__ __forceinline__ int slow_head(const u16* wm) { return (int)(((3u - (unsigned)((uintptr_t)wm >> 7)) & 7u) >> 1); }
template <int role>
__device__ __forceinline__ void wroles(const int h, const int par, const int sl, const int xg, const int w, const u64 nonce, const u16* __restrict__ x,
                                       const u16* __restrict__ Wq, const u16* __restrict__ Wk, const u16* __restrict__ Wv,
                                       const u16* __restrict__ Wo, char* ws) {
  LAS char* const L = (LAS char*)g_lds;
  gu32* tmo = (gu32*)(ws + OFF_TMO);
  const __amdgpu_buffer_rsrc_t rs = __builtin_amdgcn_make_buffer_rsrc(ws, 0, (int)WS_USED, 0x00020000);
  constexpr bool vo = role & 1;
  v8u wa[8], wb[16];
  constexpr bool fuse = role >= 2;
  constexpr int RG = fuse ? 4 : 5, RB = fuse ? 32768 : 0;
  f32x4 fac[2][2];
  v2u nin = {0u, 0u}, hsv;
  unsigned arm = 0;
  v8u t13;
  PLDECL;
  const int krot = h & KROT;
  {
    const int lane = olane();
    const int l15 = lane & 15, g = lane >> 4, trq = l15 >> 2, trp = lane & 3;
    const char* bl = (const char*)((vo ? Wv : Wq) + h * DH);
    const char* br = (const char*)(vo ? Wo + (size_t)h * DH * D : Wk + h * DH);
    const int rot = role == 1 ? (h >> 4) * 4 : 0;
    int vl[2], vr[2];
#pragma unroll
    for (int q = 0; q < 2; ++q) {
      const int row = 4 * (2 * w + q) + g;
      const int cb = ((((l15 >> 1) ^ ((row & 3) | ((row >> 1) & 4))) << 5) | ((l15 & 1) << 4));
      const int i = row & 7;
      const int prow = (row & 32) + ((i & 4) << 2) + ((row >> 1) & 12) + (i & 3);
      vl[q] = row * 8192 + cb;
      vr[q] = vo ? (role == 1 ? row : prow) * 2048 + cb : vl[q];
    }
#define W_ISSUE(u)                                                                                   \
  {                                                                                                  \
    const char* pl = bl + (size_t)((u) ^ krot) * 64 * 8192;                                          \
    const char* pr = br + (role == 1 ? (size_t)(((u) >> 1) & 1) * 64 * 2048 + (2 * ((u) >> 2) + ((u) & 1)) * 256          \
                                     : (vo ? (size_t)((u) & 1) * 64 * 2048 + ((u) >> 1) * 256 : (size_t)((u) ^ krot) * 64 * 8192)); \
    _Pragma("unroll") for (int q = 0; q < 2; ++q) {                                                  \
      glds16<((WNT >> (role >> 1)) & 1) != 0>(pl, vl[q], RB + ((u) % RG) * 32768 + (2 * w + q) * 1024);                                \
      glds16<((WNT >> (role >> 1)) & 1) != 0>(pr, vr[q], RB + ((u) % RG) * 32768 + 16384 + (2 * w + q) * 1024);                        \
    }                                                                                                \
  }
    const int ta = role == 1 ? 8 + (w ^ rot) : 2 * w, tb = role == 1 ? 2 * (w & 3) : 2 * w + 1;
    const int sw = trq | ((g & 1) << 2);
    int pa = (ta >> 3) * 16384 + (8 * g + trq) * 256 + (((ta & 7) ^ sw) << 5) + trp * 8;
    int pb = (tb >> 3) * 16384 + (8 * g + trq) * 256 + (((tb & 7) ^ sw) << 5) + trp * 8;
    const int pw = L_W + w * 2048 + lane * 16;
#define W_FRAG(dst, p, u)                                                                            \
  {                                                                                                  \
    int p_ = p + RB + ((u) % RG) * 32768;                                                             \
    asm volatile("" : "+v"(p_));                                                                     \
    const v2u t0 = trd(L + p_), t1 = trd(L + p_ + 1024), t2 = trd(L + p_ + 8192), t3 = trd(L + p_ + 9216); \
    dst = v8u{t0[0], t0[1], t1[0], t1[1], t2[0], t2[1], t3[0], t3[1]};                               \
  }
#define W_PUT(c)                                           \
  {                                                        \
    int p_ = pw + ((c) - 8) * 16384;                       \
    asm volatile("" : "+v"(p_));                           \
    *(LAS v4u*)(L + p_) = lo4(tmp[(c) - 8]);               \
    *(LAS v4u*)(L + p_ + 1024) = hi4(tmp[(c) - 8]);        \
  }
    PL(0);
    const int fxv = (16 * (w & 1) + (lane >> 2)) * 2048 + (((lane ^ -g) & 3) << 4) + ((w >> 1) & 1) * 64;
    const int fxr = l15 * 64 + (((g ^ -trq) & 3) << 4);
#define X_ISSUE(j) glds16((const char*)x + par * 65536 + ((2 * (j) + (w >> 2)) ^ krot) * 128, fxv, (((j) + 1) & 3) * 8192 + w * 1024)
#define HWU 99
#define W_HELP(u)                                                                                                     \
  if (fuse && w >= 5) {                                                                                               \
    const int h0 = slow_head(Wq), rk = (h - h0 - 1) & 3;                                                              \
    const int q = (((role & 1) * 4 + par * 2 + ((h >> 2) & 1)) * 3 + rk) * 3 + w - 5, m = q / 24;                     \
    const u16* wm = m == 2 ? Wv : (m ? Wk : Wq);                                                                      \
    const int hs = (h & 24) + slow_head(wm) + 4 * ((q / 12) & 1);                                                     \
    if (rk < 3)                                                                                                       \
      touch4((const char*)wm + hs * 256 + (size_t)((15 - (q % 12 >> 1)) ^ (hs & KROT)) * 524288,                      \
             ((q & 1) * 32 + (lane >> 1)) * 8192 + (lane & 1) * 128, RB + ((u) % RG) * 32768 + 2 * w * 1024);          \
  }
    if (HWU == -1) W_HELP(0)
    W_ISSUE(0);
    if (fuse) {
#pragma unroll
      for (int j = 0; j < 4; ++j) X_ISSUE(j);
    }
#pragma unroll
    for (int u = 1; u < RG; ++u) W_ISSUE(u);
    PL(1);
    v8u tmp[8];
#pragma unroll
    for (int u = 0; u < 16; ++u) {
      {
        int young = 4 * (u + RG - 1 < 16 ? RG - 1 : 15 - u);
        if (fuse)
          for (int i = u - RG; i < u; ++i) young += i >= 0 && i < 8 && (i & 1);
        if (u > HSU && u < HSU + RG) young += 1;
        if (role < 2 && u > 13) young += 3;
        if (VFAR && role == 3 && u > 12 && u < 15) young += 2;
        asm volatile("s_waitcnt vmcnt(%0)" ::"n"(young) : "memory");
      }
      RAW_BARRIER();
      if ((u & 3) == 0 || u == 15) PL(2 + (u + 1) / 4);
      if constexpr (role == 1) {
#define WO_(n) ((n) < 8 ? wa[(n) & 7] : ((n) < 10 ? wb[14 + ((n) & 1)] : tmp[(n) >= 10 ? (n) - 10 : 0]))
#define WV_(n) ((n) < 14 ? wb[(n) < 14 ? (n) : 0] : tmp[6 + ((n) & 1)])
        if (u == 12) { W_PUT(8); W_PUT(9); }
        W_FRAG(WO_(u), pa, u)
        if ((w >> 2) == (u >> 3)) {
          W_FRAG(WV_(2 * (u & 7)), pb, u)
          W_FRAG(WV_(2 * (u & 7) + 1), (pb ^ 32), u)
        }
        if (u & 1) {
          v8u& f0 = WO_(u & ~1);
          v8u& f1 = WO_(u);
          const v8u n0 = {f0[0], f1[0], f0[1], f1[1], f0[2], f1[2], f0[3], f1[3]};
          const v8u n1 = {f0[4], f1[4], f0[5], f1[5], f0[6], f1[6], f0[7], f1[7]};
          f0 = n0;
          f1 = n1;
          asm volatile("" : "+v"(f0), "+v"(f1));
        }
        if (u == 13) { W_PUT(10); W_PUT(11); }
        if (u == 15) { W_PUT(12); W_PUT(13); W_PUT(14); W_PUT(15); }
      } else if constexpr (role == 0) {
        if (u == 12) { W_PUT(8); W_PUT(9); }
        if (u == 13) { W_PUT(10); W_PUT(11); }
        if (u == 14) { W_PUT(12); W_PUT(13); }
        if (u == 15) { W_PUT(14); }
        if (u < 8) W_FRAG(wa[u < 8 ? u : 0], pa, u) else W_FRAG(tmp[u >= 8 ? u - 8 : 0], pa, u)
        W_FRAG(wb[u], pb, u)
        if (u == 15) W_PUT(15);
      } else {
        if (u == 15 && role == 2 && w == 0) hs_wait(rs, nonce, hsv);
        if (u >= 13) W_PUT(u >= 13 ? 2 * u - 18 : 8);
        if (u == 13 || u == 14) W_PUT(u >= 13 ? 2 * u - 17 : 8);
        v8u fa, fb;
        W_FRAG(fa, pa, u)
        W_FRAG(fb, pb, u)
        if (u < 8) {
          wa[u & 7] = fa;
          wb[u] = fb;
        } else {
          wb[u] = cat8(lo4(fa), lo4(fb));
          tmp[u & 7] = cat8(hi4(fa), hi4(fb));
        }
        if (role == 2 || w < 4) {
#pragma unroll
          for (int hf = 0; hf < 2; ++hf)
#pragma unroll
            for (int rt = 0; rt < 2; ++rt) {
              const v4u xq = *(const LAS v4u*)(L + fxr + ((u + 2) & 7) * 4096 + hf * 2048 + rt * 1024);
              fac[0][rt] = mma(hf ? hi4(fa) : lo4(fa), xq, u + hf == 0 ? f32x4{0.f, 0.f, 0.f, 0.f} : fac[0][rt]);
              fac[1][rt] = mma(hf ? hi4(fb) : lo4(fb), xq, u + hf == 0 ? f32x4{0.f, 0.f, 0.f, 0.f} : fac[1][rt]);
            }
          asm volatile("" : "+v"(fac[0][0]), "+v"(fac[0][1]), "+v"(fac[1][0]), "+v"(fac[1][1]));
          if (u == 15) {
            PL(16);
            const int l16 = olane() * 16;
#pragma unroll
            for (int rt = 0; rt < 2; ++rt) {
              const v4u o = {pk2(fac[0][rt][0], fac[0][rt][1]), pk2(fac[0][rt][2], fac[0][rt][3]), pk2(fac[1][rt][0], fac[1][rt][1]),
                             pk2(fac[1][rt][2], fac[1][rt][3])};
              if (role == 3) *(LAS v4u*)(L + 24576 + rt * 4096 + w * 1024 + l16) = o;
              else st128(o, rs, l16, qk_addr(w >> 2, par, h) + rt * QK_RT + (w & 3) * 1024);
            }
            if (role == 2) {
              W_PUT(13);
              VMCNT(0);
            }
          }
        } else if (u == 15) {
          W_PUT(13);
          if (w == 7) {
            const bool ok = hs_wait(rs, nonce, hsv);
            if (VFAR) {
              if (__builtin_expect(!ok || !__all((nin[0] != 0xffffffffu) & (nin[1] != 0xffffffffu)), 0)) nin = numa_load(rs, tmo);
              *(volatile LAS int*)(L + L_W + 5 * 16384) = numa_far(nin);
            }
          }
        }
      }
      if (u == HSU) hsv = __builtin_amdgcn_raw_buffer_load_b64(rs, w == (role == 3 ? 7 : 0) ? 0 : OOB, OFF_ACK + (int)BIDX * 8, AUX_SC1);
      if (role < 2 && u == 13) {
        const int a = w == 0 ? olane() * 4 : OOB;
        arm = __builtin_amdgcn_raw_buffer_load_b32(rs, a & OOB, CNT_ARM, AUX_SC1);
        nin[0] = __builtin_amdgcn_raw_buffer_load_b32(rs, a, (int)OFF_CAL, AUX_SC1);
        nin[1] = __builtin_amdgcn_raw_buffer_load_b32(rs, (a & OOB) | (((a >> 5) & (NXC - 1)) * 4), (int)OFF_XID, AUX_SC1);
      }
      if (VFAR && role == 3 && u == 12) {
        const int a = w == 7 ? olane() * 4 : OOB;
        nin[0] = __builtin_amdgcn_raw_buffer_load_b32(rs, a, (int)OFF_CAL, AUX_SC1);
        nin[1] = __builtin_amdgcn_raw_buffer_load_b32(rs, (a & OOB) | (((a >> 5) & (NXC - 1)) * 4), (int)OFF_XID, AUX_SC1);
      }
      if (u + RG < 16) {
        RAW_BARRIER();
        if (HWU == u) W_HELP(u + RG)
        W_ISSUE(u + RG);
        if (fuse && u < 8 && (u & 1)) X_ISSUE(4 + (u >> 1));
      }
    }
    PL(17);
    RAW_BARRIER();
    PL(18);
    if (VFAR && role == 3 && w >= 4) arm = __builtin_amdgcn_readfirstlane(*(volatile LAS int*)(L + L_W + 5 * 16384));
    if (role == 2 && w == 0) __builtin_amdgcn_raw_buffer_store_b32(0u, rs, olane() << 28, fqk(h, par), AUX_SC1);
    if constexpr (fuse) {
      if (role == 2 || w < 4) {
        constexpr int B0 = role == 3 ? 6 : 8, PER = role == 3 ? 2 : 4, SL = role == 3 ? 12288 : 16384;
        const int ln = olane();
        const int xv = (16 * (w & 1) + (ln >> 2)) * 2048 + (((ln ^ -(ln >> 4)) & 3) << 4) + ((w >> 1) & 1) * 1024 + (role == 3 ? 0 : (w >> 2) * 64);
#pragma unroll
        for (int k = 0; k < 2; ++k)
#pragma unroll
          for (int j = 0; j < B0 / PER; ++j)
            glds16((const char*)x + (2 + par) * 65536 + (((k * B0 + j * PER) / 2 * 64) ^ (krot * 128)), xv, k * SL + j * PER * 2048 + w * 1024);
        PL(19);
        if (role == 3) t13 = tmp[5];
      }
      W_PUT(14);
      W_PUT(15);
    }
  }
  const int lane = olane();
  const int l15 = lane & 15, g = lane >> 4, trq = l15 >> 2, trp = lane & 3;
  int pw0 = L_W + w * 2048 + lane * 16, pw1 = pw0 + 65536;
  asm volatile("" : "+v"(pw0), "+v"(pw1));
#define WA_P(c) (L + ((c) < 12 ? pw0 : pw1) + ((c) & 3) * 16384)
#define WA_LDS(c) cat8(*(const LAS v4u*)WA_P(c), *(const LAS v4u*)(WA_P(c) + 1024))
  const int r_a = (int)(((uintptr_t)ws >> 19) & 7);
  const int r_s = r_a + (int)(OFF_STEP / UNIT);

  typedef int v4i __attribute__((ext_vector_type(4)));
#define WPIN "{v[0:31]}"(W0), "{v[32:63]}"(W1), "{v[64:95]}"(W2), "{v[96:127]}"(W3), "{v[128:159]}"(W4), "{v[160:191]}"(W5)
  const v4u rsv = {(unsigned)(uintptr_t)ws, (unsigned)((uintptr_t)ws >> 32) & 0xffffu, (unsigned)WS_USED, 0x00020000u};
  if constexpr (role >= 2) {
    constexpr bool isx = role == 3;
    const v32u W0 = cat32(wa[0], wa[1], wa[2], wa[3]), W1 = cat32(wa[4], wa[5], wa[6], wa[7]), W2 = cat32(wb[0], wb[1], wb[2], wb[3]);
    const v32u W3 = cat32(wb[4], wb[5], wb[6], wb[7]), W4 = cat32(wb[8], wb[9], wb[10], wb[11]), W5 = cat32(wb[12], wb[13], wb[14], wb[15]);
    const v4u rxv = {(unsigned)(uintptr_t)x, (unsigned)((uintptr_t)x >> 32) & 0xffffu, (unsigned)(S * D * 2), 0x00020000u};
    v4i adr;
    adr[3] = lane * 16;
    adr[2] = pw0;
    adr[1] = pw1;
    adr[0] = (16 * (w & 1) + (lane >> 2)) * 2048 + (((lane ^ -g) & 3) << 4) + ((w >> 1) & 1) * 1024 + (isx ? 0 : (w >> 2) * 64);
    const int xr = l15 * 64 + (((g ^ -trq) & 3) << 4);
#define FOF(sg) (isx ? fvw(w & 3, h, sg) : fqk(h, sg))
    const int nrel = isx ? TN(8, NRX) : TN(7, NRY);
    const int nit = par ? nrel : NSEG - nrel;
#define SEGOF(i) ((i) < nrel ? 2 * (i) + par : (i) + nrel)
#define ST_FLAG(sg)                                                                                                   \
  {                                                                                                                   \
    VMCNT(0);                                                                                                         \
    RAW_BARRIER();                                                                                                    \
    if (w == 0) __builtin_amdgcn_raw_buffer_store_b32(0u, rs, olane() << 28, FOF(sg), AUX_SC1);               \
  }
#define XB_OPS(sg, sp)                                                                                                  \
  : [cnt] "=&s"(cnt) EVOUT                                                                                              \
  : WPIN, "{v[248:251]}"(adr), [rs] "s"(rsv), [so] "s"(vw_addr(vfr, 2 * (w - 4), sg, h >> 2) + (h & 3) * 1024),               \
    [js] "i"((VFAR ? 64 : GST) * 262144), [ss] "i"(VFAR ? 65536 : 32768), [fo] "s"(FOF(sp))                                                 \
  : XB_CLOB, "scc", "memory"
#define XB_FLAG(sg) __builtin_amdgcn_raw_buffer_store_b8(0, rs, olane() << 28, FOF(sg), AUX_SC1)
    PL(7);
    if (isx && w >= 4) {
      const int vfr = (__builtin_amdgcn_readfirstlane(arm) ^ (VFAR == 2 ? 8192 : 0)) | (r_a << 19);
      {
        EVDECL;
        unsigned cnt;
        PL(20);
        asm volatile(ST_XBS XB_OPS(par, par));
        PL(21);
      }
      int it = 1;
#pragma unroll 1
      for (;;) {
        VMCNT(0);
        XB_FLAG(SEGOF(it - 1));
        if (it == 1) { PL(9); PLDUMP(4, 0x300200); }
        if (it == 2) PTW(14, 4);
        if (it == 3) PTW(15, 4);
        for (int k = it == 1 ? XBS_NB - 1 : XBF_NB; k < 6; ++k) BARE_BARRIER();
        if (++it > NXF) break;
        EVDECL;
        unsigned cnt;
        asm volatile(ST_XBF XB_OPS(SEGOF(it - 1), par));
        EVT2R
      }
#pragma unroll 1
      for (; it < nit; ++it) {
        EVDECL;
        unsigned cnt;
        asm volatile(ST_XB XB_OPS(SEGOF(it - 1), SEGOF(it - 2)));
        EVT2R
      }
      BARE_BARRIER();
      {
        EVDECL;
        unsigned cnt;
        asm volatile(ST_XB0 XB_OPS(SEGOF(nit - 1), par));
      }
      BARE_BARRIER();
      VMCNT(0);
      XB_FLAG(SEGOF(nit - 2));
      XB_FLAG(SEGOF(nit - 1));
      return;
    }
    if (w == 1) cnt_inc(ws, CNT_HS);
    VMCNT(0);
    RAW_BARRIER();
    if (isx) {
      *(LAS v4u*)(L + pw1 + 16384) = lo4(t13);
      *(LAS v4u*)(L + pw1 + 17408) = hi4(t13);
    }
    PL(10);
    PLDUMP(0, 0xf05ff);
    {
      v32u xf;
#pragma unroll
      for (int q = 0; q < 8; ++q) {
        const v4u t = *(const LAS v4u*)(L + xr + q * 1024);
#pragma unroll
        for (int j = 0; j < 4; ++j) xf[4 * q + j] = t[j];
      }
#pragma unroll 1
      for (int it = 1; it < nit; ++it) {
        EVDECL;
        const int seg = SEGOF(it);
        unsigned cnt, keep;
#define ST_OPS                                                                                                                 \
  : [cnt] "=&s"(cnt), [keep] "=&s"(keep), "+{v[208:239]}"(xf) EVOUT                                                            \
  : WPIN, "{v[248:251]}"(adr), "{v252}"(xr), [rs] "s"(rsv), [rx] "s"(rxv), [sc] "s"(seg * 65536 + krot * 128),                    \
    [sn] "s"(SEGOF(min(it + 1, nit - 1)) * 65536 + krot * 128),                                                                  \
    [mw] "s"(w * 1024), [so] "s"(isx ? w * 1024 : qk_addr(w >> 2, seg, h) + (w & 3) * 1024), [rt] "i"(QK_RT), \
    [fo] "s"(FOF(SEGOF(it - 1))), [fm] "s"(w == 0 ? 0 : 1 << 30), [tm] "s"(w == 3 ? 0 : 1 << 30),            \
    [tc] "s"(min(SEGOF(it + 2), NSEG - 1) * 65536 + sl * 8192)                                                                   \
  : ST_CLOB, "scc", "memory"
        if constexpr (isx) asm volatile(ST_XA ST_OPS);
        else asm volatile(ST_Y ST_OPS);
        EVT2R
        if (it < 4) PTW(10 + it, 2);
        if (!isx && it < NPUB) ST_FLAG(seg)
      }
    }
    if (isx) {
      RAW_BARRIER();
      BARE_BARRIER();
    } else ST_FLAG(SEGOF(nit - 1))
    return;
  }

#define KF0 4
  volatile LAS unsigned* sword = (volatile LAS unsigned*)(L + L_HI + 6408);
  if (w == 0) {
    const bool ok = hs_wait(rs, nonce, hsv);
    cnt_inc(ws, CNT_HS);
    if (__builtin_expect(!ok || !__all((arm + 1u >= 64u) & (nin[0] != 0xffffffffu) & (nin[1] != 0xffffffffu)), 0)) {
      arm_wait(rs, tmo);
      nin = numa_load(rs, tmo);
    }
    sword[0] = numa_perm(nin, (LAS unsigned*)(L + L_HI + 6656));
    sword[1] = 0u;
  }
  Clk ck = {0u, 0u, 92500u, 46432u};
  PL(7);
  RAW_BARRIER();
  PL(8);
  PLDUMP(0, 0xf01ff);
  const unsigned perm = __builtin_amdgcn_readfirstlane(sword[0]);
  const int cown = cls_of(perm, xg);
  const int l16 = lane * 16;
  const int sidx = ((l15 & 2) ? 0xeeee : 0x4444) | ((l15 & 8) ? 0xeeee0000 : 0x44440000);
  LAS char* const sm = L + l16;
  constexpr int TWV = 5;
  typedef int v8i __attribute__((ext_vector_type(8)));
  const int mg_ = NS2F(TN(0, MARG)), tol_ = NS2F(TN(13, TOL)), xl_ = NS2F(TN(14, XLATE));
  Pace pc = {0u, NS2F(ED0), NS2F(EA0), 0};
  unsigned tg = 0, tw = 0;
#define MEM_OFF(c) (R_MEM + ((c) >> 2) * 32768 + ((c) & 3) * 1024)
#define TLB_TOUCH                    \
  if (((r_s + s + 2) & 3) == 0)      \
    tch = __builtin_amdgcn_raw_buffer_load_b32(rs, 0, so + 2 * (int)UNIT + R_DUM + (role * 32 + h) * 128, AUX_SC1);
#define TQOUT
#define TQACC
#define ST_ADR (lst * 32 + 32768 + 2048)
#define TIMING_WAVE                                                                                  \
  {                                                                                                  \
    if (__builtin_expect(anysent(st[0]), 0)) _Pragma("clang loop unroll(disable)") for (unsigned spins = 0;;) { \
      if (spins) __builtin_amdgcn_s_sleep(4);                                                        \
      if (spin_fail(spins, tmo, 0x30009)) break;                                                     \
      const int lst = olane();                                                                       \
      OPQ(va, ST_ADR - 2048);                                                                        \
      st = __builtin_amdgcn_raw_buffer_load_b64(rs, va, so + R_ST + ((cown ^ xr) << 12), AUX_SC1);   \
      if (!anysent(st[0])) break;                                                                       \
    }                                                                                                \
    TQACC                                                                                            \
    const int dm = stamp_max(ck, st[0]);                                                             \
    const unsigned rf = (ck.r << 4) + dm;                                                            \
    pace_step(pc, s, rf, st[1], mg_, tol_, xl_);                                                     \
    EV(6);                                                                                           \
    EVDUMP(s);                                                                                       \
    EVCLK(s, ck);                                                                                    \
  }
#define TIMING_GO                                                                                    \
  if (w == TWV && s > 0) {                                                                           \
    wait_cyc(clk_cyc(ck, (int)(pc.rf - (ck.r << 4)) + pc.dq) - TFR);                                 \
    clk_update(ck, s == 1);                                                \
    tg = clk_cyc(ck, (int)(pc.rf - (ck.r << 4)) + pc.dq);                                            \
    tw = (unsigned)max(CMAX - (int)(tg - ck.m), 0);                                                  \
  }

  if constexpr (role == 0) {
    const v32u W0 = cat32(wa[0], wa[1], wa[2], wa[3]), W1 = cat32(wa[4], wa[5], wa[6], wa[7]), W2 = cat32(wb[0], wb[1], wb[2], wb[3]);
    const v32u W3 = cat32(wb[4], wb[5], wb[6], wb[7]), W4 = cat32(wb[8], wb[9], wb[10], wb[11]), W5 = cat32(wb[12], wb[13], wb[14], wb[15]);
    v8i adr;
    adr[7] = l16;
    adr[6] = l16 + w * 1024;
    adr[5] = pw0;
    adr[4] = sidx;
    adr[3] = L_HI + (w >> 2) * 3072 + (32 * (w & 3) + l15) * 24 + 4 * g;
    adr[2] = (4 * g + trq) * 24 + (trp & 1) * 8;
#define CORD(i) ((((i) & 1) ? (i) >> 1 : 8 + ((i) >> 1)) ^ krot)
    const int mo0 = MEM_OFF(CORD(w));
#define K_STAGE(sn)                                                                                                   \
  {                                                                                                                   \
    v4u kn[4];                                                                                                        \
    _Pragma("clang loop unroll(disable)") for (unsigned spins = 0; krdy <= (sn);) {    \
      OPQ(vfl, fqk(h, min((sn) + (olane() & 15), NSEG - 1)));                                           \
      const u64 miss = __builtin_amdgcn_ballot_w64(__builtin_amdgcn_raw_buffer_load_b32(rs, vfl, 0, AUX_SC1) != 0u);  \
      krdy = (sn) + (miss ? __builtin_ctzll(miss) : 16);                                                              \
      if (krdy > (sn)) break;                                                                                         \
      __builtin_amdgcn_s_sleep(8);                                                                                    \
      if (spin_fail(spins, tmo, 0x30000)) break;                                                                      \
    }                                                                                                                 \
    const int lk = olane() * 16;                                                                                      \
    _Pragma("unroll") for (int q = 0; q < 4; ++q) kn[q] = __builtin_amdgcn_raw_buffer_load_b128(                      \
        rs, lk + q * 1024, qk_addr(1, sn, h) + (w & 1) * QK_RT, AUX_SC1);                 \
    _Pragma("unroll") for (int q = 0; q < 4; ++q) *(LAS v4u*)(L + L_HI + 8192 + (w & 1) * 4096 + q * 1024 + lk) = kn[q]; \
  }
    int krdy = 1;
    const bool kst = w >= 4 && ((w ^ TWV) & 2);
    PTW(9, 7);
    if (kst) {
      wait_flag<KF0>(rs, fqk(h, 0), tmo, 0x30001);
      sword[1] = 1u;
      K_STAGE(0)
    }
    PTW(10, 7);
    if (w == TWV) {
      _Pragma("clang loop unroll(disable)") for (unsigned spins = 0; !__builtin_amdgcn_readfirstlane(sword[1]) && spins < kSpinCap; ++spins)
        __builtin_amdgcn_s_sleep(2);
      clk_edge(ck.r, ck.m, 2);
    }
    if (w < 2) {
      const int lane = olane(), l15 = lane & 15, l16 = lane * 16;
      const bool up = lane >= 32;
      adr[1] = R_EX + (h >> 3) * 32768 + (h & 7) * 512 + w * 256 + (lane & 32) * 4 + (lane & 7) * 16 + (lane & 24) / 2;
      const int rq = (lane & 1) + 2 * (lane & 6);
      adr[0] = up ? R_PM + (h >> 3) * 1024 + (rq + (l15 >> 3) * 2 + 16 * ((h & 7) >> 1)) * 16 + (h & 1) * 8 + (lane & 16) / 4
                  : (lane < 8 ? R_SC + SC_POS(h >> 2, lane) + (2 * (h & 1) + ((h >> 1) & 1)) * 4 : OOB);
      const unsigned msk = up ? 0x7fffffffu : ((lane & 1) ? 0x7fff0000u : 0xffffu);
      const int kxo = L_HI + 8192 + w * 4096 + l16;
#pragma unroll 1
      for (int s = 0; s < NSEG; ++s) {
        EVDECL;
        const int so = (int)OFF_STEP + s * (int)UNIT, xr = xreg(r_s + s);
#define CG(r) (so + ((cls_of(perm, (r) ^ (2 * w)) ^ xr) << 12))
        unsigned cnt;
        u64 wd;
        asm volatile(QK_A
                     : [cnt] "=&s"(cnt), [wd] "=&s"(wd) EVOUT
                     : WPIN, "{v[244:251]}"(adr), "{v243}"(pw1), "{v242}"(kxo), "{v240}"(msk), [rs] "s"(rsv),
                       [o0] "s"(so + mo0 + ((cown ^ xr) << 12)), [c0] "s"(CG(0)), [c1] "s"(CG(1)), [c2] "s"(CG(2)), [c3] "s"(CG(3)),
                       [hf] "s"(0xff00ff00ff00ff00ull), [up] "s"(0xffffffff00000000ull)
                     : QK_CLOB, "v241", "vcc", "scc", "memory");
        if (s < 4) PT(11 + s);
        EVDUMP(s);
      }
    } else if (w < 4) {
      const float scale = 0.08838834764831845f, L2E = 1.4426950408889634f;
      adr[0] = adr[1] = 0;
#pragma unroll 1
      for (int s = 0; s < NSEG; ++s) {
        {
          EVDECL;
          const int so = (int)OFF_STEP + s * (int)UNIT, xr = xreg(r_s + s);
          unsigned cnt;
          v16u qv;
          f32x4 sc[3];
          asm volatile(QK_X
                       : [cnt] "=&s"(cnt), "={v[200:215]}"(qv), "={v[192:195]}"(sc[0]), "={v[196:199]}"(sc[1]) EVOUT
                       : WPIN, "{v[244:251]}"(adr), "{v243}"(pw1), [rs] "s"(rsv), [o0] "s"(so + mo0 + ((cown ^ xr) << 12)),
                         [qo] "s"(qk_addr(0, s, h) + (w - 2) * QK_RT)
                       : QX_CLOB, "vcc", "scc", "memory");
          const int ln = olane(), l15 = ln & 15, g = ln >> 4, l16 = ln * 16;
          v4u qx[4];
#pragma unroll
          for (int ks = 0; ks < 4; ++ks) qx[ks] = v4u{qv[4 * ks], qv[4 * ks + 1], qv[4 * ks + 2], qv[4 * ks + 3]};
          LAS char* const q_rd = L + L_HI + adr[2];
#define QFRAG(o, ks) [&] { const v2u tx = trd(q_rd + (o) + (ks) * 768), ty = trd(q_rd + (o) + (ks) * 768 + 384); return v4u{tx[0], tx[1], ty[0], ty[1]}; }()
          sc[2] = f32x4{0.f, 0.f, 0.f, 0.f};
#pragma unroll
          for (int ks = 0; ks < 4; ++ks) sc[2] = mma(QFRAG(3072, ks), qx[ks], sc[2]);
          EV(4);
          const int tok = 16 * (w - 2) + l15 - 4 * g;
#pragma unroll
          for (int t = 0; t < 3; ++t)
#pragma unroll
            for (int j = 0; j < 4; ++j) {
              float v = rbf(mul1(rbf(sc[t][j]), scale));
              if (t < 2 && 16 * t + j > tok) v = -1000000000.0f;
              sc[t][j] = v;
            }
          float mx;
          asm("v_max3_f32 %0, %1, %2, %3\n\tv_max3_f32 %0, %0, %4, %5\n\tv_max3_f32 %0, %0, %6, %7\n\tv_max3_f32 %0, %0, %8, %9\n\t"
              "v_max3_f32 %0, %0, %10, %11\n\tv_max_f32 %0, %0, %12\n\ts_nop 1"
              : "=&v"(mx)
              : "v"(sc[0][0]), "v"(sc[0][1]), "v"(sc[0][2]), "v"(sc[0][3]), "v"(sc[1][0]), "v"(sc[1][1]), "v"(sc[1][2]), "v"(sc[1][3]),
                "v"(sc[2][0]), "v"(sc[2][1]), "v"(sc[2][2]), "v"(sc[2][3]));
          mx = xmax16_32(mx);
          const float nmx = -mx * L2E;
#pragma unroll
          for (int t = 0; t < 3; ++t)
#pragma unroll
            for (int j = 0; j < 4; ++j) sc[t][j] = __builtin_amdgcn_exp2f(__builtin_fmaf(sc[t][j], L2E, nmx));
          const f32x4 s4 = sc[0] + sc[1] + 0.5f * sc[2];
          const float inv = __builtin_amdgcn_rcpf(xsum16_32((s4[0] + s4[1]) + (s4[2] + s4[3])));
#pragma unroll
          for (int t = 0; t < 3; ++t) sc[t] *= inv;
          const v4u o = {pk2(sc[0][0], sc[0][1]), pk2(sc[1][0], sc[1][1]), pk2(sc[0][2], sc[0][3]), pk2(sc[1][2], sc[1][3])};
          const v2u o2 = {pk2(sc[2][0], sc[2][1]), pk2(sc[2][2], sc[2][3])};
          const int pb = so + R_PX + (w - 2) * 40960;
          st128(o, rs, l16, pb + h * 1024);
          __builtin_amdgcn_raw_buffer_store_b64(o2, rs, g >= 2 ? l15 * 16 + (g & 1) * 8 : OOB,
                                                pb + (32 + (h >> 2)) * 1024 + (h & 3) * 256, AUX_SC1);
          EV(6);
          EVDUMP(s);
        }
      }
    } else {
      const int mo1 = MEM_OFF(CORD(w + 4)), mo2 = MEM_OFF(CORD(w + 8));
      adr[0] = 0;
#pragma unroll 1
      for (int s = 0; s < NSEG; ++s) {
        EVDECL;
        const int so = (int)OFF_STEP + s * (int)UNIT, xr = xreg(r_s + s), sc_ = so + ((cown ^ xr) << 12);
        const int lst = olane();
        int a1 = ((cls_of(perm, lst & 3) ^ xr) << 12) | ((lst & 4) << 9) | ((lst & 8) << 12) | (((lst >> 4) | (w != TWV)) << 28);
        const bool fq = kst && krdy <= s + 2;
        v2u st = {(unsigned)(fq ? fqk(h, min(s + 1 + (lst & 15), NSEG - 1)) : ST_ADR | ((w != TWV) << 30)), 0u};
        unsigned cnt, tca = (lst & 31) * 128 + (lst >> 5) * QK_RT;
        int o0 = sc_ + mo0, o1 = sc_ + mo1, o2 = sc_ + mo2, sst = so + R_ST + role * 1024 + h * 32, ss = kst ? 0 : sc_ + R_ST;
        int tc = kst ? qk_addr(w & 1, min(s + 1, NSEG - 1), h) : 0;
        asm volatile("" : "+v"(a1), "+{v[240:241]}"(st), "+{v242}"(tca));
        adr[1] = a1;
        asm volatile("" : "+s"(o0), "+s"(o1), "+s"(o2), "+s"(sst), "+s"(ss), "+s"(tc));
        TIMING_GO
        asm volatile(QK_K
                     : [cnt] "=&s"(cnt), "+{v[240:241]}"(st), "+{v242}"(tca) EVOUT TQOUT
                     : WPIN, "{v[244:251]}"(adr), "{v243}"(pw1), [rs] "s"(rsv), [o0] "s"(o0), [o1] "s"(o1), [o2] "s"(o2),
                       [ckm] "s"(ck.m - NCY), [inv] "s"(ck.inv << 16), [r16] "s"(ck.r << 4), [sst] "s"(sst), [tg] "s"(tg), [tw] "s"(tw),
                       [ss] "s"(ss), [tc] "s"(tc)
                     : QK_CLOB, "vcc", "scc", "memory");
        if (s < NSEG - 1 && w == TWV) {
          __builtin_amdgcn_s_sleep(KSL);
          TIMING_WAVE
        } else if (s < NSEG - 1 && kst) {
          if (fq) {
            const u64 miss = __builtin_amdgcn_ballot_w64(st[0] != 0u);
            krdy = s + 1 + (miss ? __builtin_ctzll(miss) : 16);
          }
          if (KSL2) __builtin_amdgcn_s_sleep(KSL2);
          K_STAGE(s + 1)
        } else {
          __builtin_amdgcn_s_sleep(KSL);
          unsigned tch = 0;
          TLB_TOUCH
          asm volatile("" ::"v"(tch));
        }
        if (w != TWV) EV(6);
        if (w != TWV) EVDUMP(s);
      }
    }
  } else {
    const v32u W0 = cat32(wb[0], wb[1], wb[2], wb[3]), W1 = cat32(wb[4], wb[5], wb[6], wb[7]), W2 = cat32(wb[8], wb[9], wb[10], wb[11]);
    const v32u W3 = cat32(wb[12], wb[13], wa[0], wa[1]), W4 = cat32(wa[2], wa[3], wa[4], wa[5]), W5 = cat32(wa[6], wa[7], wb[14], wb[15]);
    v8i adr;
    adr[7] = l16;
    adr[6] = l16 + w * 1024;
    adr[5] = pw0;
    adr[4] = sidx;
#pragma unroll
    for (int q = 0; q < 2; ++q) {
      const int c = 8 * (2 * q + (g >> 1) + ((g & 1) ? 0 : 4)) + (w ^ ((h >> 4) * 4));
      const int blk = (c % PO) * 2 + (h >> 4);
      adr[3 - q] = (int)OFF_VM + blk * 32768 + ((cls_of(perm, c / PO) ^ xreg(r_a + (int)(OFF_VM / UNIT) + (blk >> 4))) << 12) +
                   (h & 15) * 256 + l15 * 16;
    }
    adr[1] = (16 * (g & 1) + 4 * (g >> 1) + trq) * 24 + (trp & 1) * 8;
    adr[0] = L_HI + (32 * (w & 3) + l15) * 24 + 4 * g;
#define VCH(i) (((i) & 8) | ((((i) - 1) & 7) ^ krot))
    const int mo0 = MEM_OFF(VCH(w)), mo1 = MEM_OFF(VCH(w + 4)), mo2 = MEM_OFF(VCH(w + 8));
    if (w == TWV) clk_edge(ck.r, ck.m, 2);
    if (w < 4) {
#pragma unroll 1
      for (int s = 0; s < NSEG; ++s) {
        EVDECL;
        const int so = (int)OFF_STEP + s * (int)UNIT, xr = xreg(r_s + s);
        unsigned cnt;
        asm volatile(VO_A
                     : [cnt] "=&s"(cnt) EVOUT
                     : WPIN, "{v[244:251]}"(adr), "{v243}"(pw1), [rs] "s"(rsv), [o0] "s"(so + mo0 + ((cown ^ xr) << 12)), [vs] "s"(vm_slot(s))
                     : VO_CLOB, "v241", "vcc", "scc", "memory");
        if (s < 4) PT(11 + s);
        if (w == 0) {
          unsigned tch = 0;
          TLB_TOUCH
          asm volatile("" ::"v"(tch));
        }
        EVDUMP(s);
      }
    } else {
#pragma unroll 1
      for (int s = 0; s < NSEG; ++s) {
        EVDECL;
        const int so = (int)OFF_STEP + s * (int)UNIT, xr = xreg(r_s + s);
        const int lst = olane();
        int vst = ((cls_of(perm, lst & 3) ^ xr) << 12) | ((lst & 4) << 9) | ((lst & 8) << 12) | (((lst >> 4) | (w != TWV)) << 28);
        unsigned cnt;
        v2u st = {(unsigned)(ST_ADR | ((w != TWV) << 30)), 0u};
        int o0 = so + mo0 + ((cown ^ xr) << 12), o1 = o0 - mo0 + mo1, o2 = o0 - mo0 + mo2, vs = vm_slot(s), sst = so + R_ST + role * 1024 + h * 32;
        int ss = so + R_ST + ((cown ^ xr) << 12);
        asm volatile("" : "+{v[240:241]}"(st), "+{v242}"(vst));
        asm volatile("" : "+s"(o0), "+s"(o1), "+s"(o2), "+s"(vs), "+s"(sst), "+s"(ss));
        TIMING_GO
        asm volatile(VO_B
                     : [cnt] "=&s"(cnt), "+{v[240:241]}"(st) EVOUT TQOUT
                     : WPIN, "{v[244:251]}"(adr), "{v243}"(pw1), "{v242}"(vst), [rs] "s"(rsv), [o0] "s"(o0), [o1] "s"(o1), [o2] "s"(o2),
                       [vs] "s"(vs), [ckm] "s"(ck.m - NCYV), [inv] "s"(ck.inv << 16), [r16] "s"(ck.r << 4), [sst] "s"(sst), [tg] "s"(tg),
                       [tw] "s"(tw), [ss] "s"(ss)
                     : VO_CLOB, "vcc", "scc", "memory");
        if (w == TWV && s < NSEG - 1) TIMING_WAVE
        if (w != TWV) EVDUMP(s);
      }
    }
  }
  __builtin_amdgcn_endpgm();
}
typedef f32x2 c2;
#define PKOP(name, txt)                                   \
  __device__ __forceinline__ c2 name(c2 a, c2 b) {        \
    c2 d;                                                 \
    asm("v_pk_add_f32 %0, %1, %2 " txt : "=v"(d) : "v"(a), "v"(b)); \
    return d;                                             \
  }
PKOP(c_addi, "op_sel:[0,1] op_sel_hi:[1,0] neg_lo:[0,1]")
PKOP(c_addc, "neg_hi:[0,1]")
PKOP(c_sepb, "op_sel:[1,1] op_sel_hi:[0,0] neg_hi:[1,0]")
PKOP(c_mixc, "op_sel:[0,1] op_sel_hi:[1,0] neg_hi:[1,0]")
#define T_ADD(d, a, b) "v_pk_add_f32 %" #d ", %" #a ", %" #b "\n\t"
#define T_SUB(d, a, b) "v_pk_add_f32 %" #d ", %" #a ", %" #b " neg_lo:[0,1] neg_hi:[0,1]\n\t"
#define T_ROT(d, a, b, n) "v_pk_add_f32 %" #d ", %" #a ", %" #b " op_sel:[0,1] op_sel_hi:[1,0] " n ":[0,1]\n\t"
#define T_MUL(d, a, w) "v_pk_mul_f32 %" #d ", %" #a ", %" #w " op_sel_hi:[1,0]\n\t"
#define T_FRE(d, a, w, c) "v_pk_fma_f32 %" #d ", %" #a ", %" #w ", %" #c " op_sel_hi:[1,0,1]\n\t"
#define T_FIM(d, a, w, c, n) "v_pk_fma_f32 %" #d ", %" #a ", %" #w ", %" #c " op_sel:[1,1,0] op_sel_hi:[0,1,1] " n ":[1,0,0]\n\t"
#define T_2MN(d, a, t) "v_pk_fma_f32 %" #d ", %" #a ", 2.0, %" #t " op_sel_hi:[1,0,1] neg_lo:[0,0,1] neg_hi:[0,0,1]\n\t"
#define T_TAIL(n, m) T_ADD(0, 4, 5) T_ROT(1, 2, 3, m) T_SUB(4, 4, 5) T_ROT(3, 2, 3, n)
#define T_D0(n, m) T_ADD(4, 0, 2) T_ADD(5, 1, 3) T_SUB(2, 0, 2) T_SUB(3, 1, 3) T_TAIL(n, m)
#define T_D1(n, m) T_MUL(5, 1, 7) T_ROT(4, 0, 2, m) T_FIM(1, 1, 7, 5, n) T_ROT(2, 0, 2, n) T_FRE(5, 3, 9, 1) T_FIM(5, 3, 9, 5, n) T_2MN(3, 1, 5) T_TAIL(n, m)
#define T_D2(n, m) T_MUL(5, 1, 7) T_FRE(4, 2, 8, 0) T_FIM(1, 1, 7, 5, n) T_FIM(4, 2, 8, 4, n) T_FRE(5, 3, 9, 1) T_2MN(2, 0, 4) T_FIM(5, 3, 9, 5, n) \
    T_ADD(0, 4, 5) T_2MN(3, 1, 5) T_SUB(4, 4, 5) T_ROT(1, 2, 3, m) T_ROT(3, 2, 3, n)
#define T_D3(n, m) T_MUL(4, 0, 6) T_MUL(5, 1, 7) T_FIM(0, 0, 6, 4, n) T_FIM(1, 1, 7, 5, n) T_FRE(4, 2, 8, 0) T_FRE(5, 3, 9, 1) \
    T_FIM(4, 2, 8, 4, n) T_FIM(5, 3, 9, 5, n) T_2MN(2, 0, 4) T_2MN(3, 1, 5) T_TAIL(n, m)
#define D4_ASM(T, C)                                                                                                       \
  {                                                                                                                        \
    if constexpr (INV) asm(T("neg_hi", "neg_lo") : "+v"(x0), "+v"(x1), "+v"(x2), "+v"(x3), "=&v"(t0), "=&v"(t1) : C(w0), C(w1), C(w2), C(w3)); \
    else asm(T("neg_lo", "neg_hi") : "+v"(x0), "+v"(x1), "+v"(x2), "+v"(x3), "=&v"(t0), "=&v"(t1) : C(w0), C(w1), C(w2), C(w3));               \
  }
__device__ __forceinline__ constexpr float cos32(int m) {
  constexpr float C[9] = {1.f, 0.98078528040323044f, 0.92387953251128674f, 0.83146961230254524f, 0.70710678118654752f,
                          0.55557023301960218f, 0.38268343236508977f, 0.19509032201612825f, 0.f};
  m &= 31;
  if (m > 16) m = 32 - m;
  return m > 8 ? -C[16 - m] : C[m];
}
#define W32(m) c2{cos32(m), -cos32((m) - 8)}
template <bool INV, int M0, int M1, int M2, int M3>
__device__ __forceinline__ void dft4k(c2& x0, c2& x1, c2& x2, c2& x3) {
  c2 t0, t1;
  if constexpr (M3 == 0) {
    const int w0 = 0, w1 = 0, w2 = 0, w3 = 0;
    D4_ASM(T_D0, "n")
  } else {
    const c2 w1 = W32(M1), w3 = W32(M3), w0 = M0 ? W32(M0) : w1, w2 = M0 == 0 && M2 == 8 ? w1 : W32(M2);
    if constexpr (M0 != 0) D4_ASM(T_D3, "s")
    else if constexpr (M2 == 8) D4_ASM(T_D1, "s")
    else D4_ASM(T_D2, "s")
  }
  x2 = t0;
}
template <bool INV, bool W0>
__device__ __forceinline__ void dft4w(c2& x0, c2& x1, c2& x2, c2& x3, c2 w0, c2 w1, c2 w2, c2 w3) {
  c2 t0, t1;
  if constexpr (W0) D4_ASM(T_D3, "v")
  else D4_ASM(T_D2, "v")
  x2 = t0;
}
#define T_B2(n, m) T_FRE(4, 1, 6, 0) T_FRE(5, 3, 7, 2) T_FIM(4, 1, 6, 4, n) T_FIM(5, 3, 7, 5, n) T_2MN(1, 0, 4) T_2MN(3, 2, 5)
#define T_B0(n, m) T_ADD(4, 0, 1) T_ROT(5, 2, 3, m) T_SUB(1, 0, 1) T_ROT(3, 2, 3, n)
template <bool INV, int K>
__device__ __forceinline__ void bf2(c2& x0, c2& x1, c2& x2, c2& x3, c2 w0, c2 w1) {
  c2 t0, t1;
  const c2 w2 = w0, w3 = w0;
  if constexpr (K == 0) D4_ASM(T_B2, "v")
  else if constexpr (K == 1) D4_ASM(T_B2, "s")
  else D4_ASM(T_B0, "s")
  x0 = t0;
  x2 = t1;
}
__device__ __forceinline__ void mul2(c2& d0, c2& d1, c2 a0, c2 w0, c2 a1, c2 w1) {
  asm(T_MUL(0, 2, 3) T_MUL(1, 4, 5) T_FIM(0, 2, 3, 0, "neg_lo") T_FIM(1, 4, 5, 1, "neg_lo") : "=&v"(d0), "=&v"(d1) : "v"(a0), "v"(w0), "v"(a1), "v"(w1));
}
template <int N, bool INV, int TW>
__device__ __forceinline__ void dftn(c2 (&v)[N], const c2* w) {
  constexpr int Q = N / 4;
#pragma unroll
  for (int n = 0; n < Q; ++n) {
    if (TW == 0) dft4k<INV, 0, 0, 0, 0>(v[n], v[Q + n], v[2 * Q + n], v[3 * Q + n]);
    else if (TW == 1 && n == 0) dft4w<INV, false>(v[n], v[Q + n], v[2 * Q + n], v[3 * Q + n], w[Q], w[Q + n], w[2 * Q + n], w[3 * Q + n]);
    else dft4w<INV, true>(v[n], v[Q + n], v[2 * Q + n], v[3 * Q + n], w[n], w[Q + n], w[2 * Q + n], w[3 * Q + n]);
  }
  c2 o[N];
  if constexpr (N == 32) {
#define D8A(K1)                                                                                          \
  dft4k<INV, 0, 2 * K1, 4 * K1, 6 * K1>(v[8 * K1], v[8 * K1 + 2], v[8 * K1 + 4], v[8 * K1 + 6]);          \
  dft4k<INV, K1, 3 * K1, 5 * K1, 7 * K1>(v[8 * K1 + 1], v[8 * K1 + 3], v[8 * K1 + 5], v[8 * K1 + 7]);
    D8A(0) D8A(1) D8A(2) D8A(3)
#pragma unroll
    for (int k = 0; k < 32; k += 8) {
      bf2<INV, 2>(v[k], v[k + 1], v[k + 4], v[k + 5], W32(4), W32(12));
      bf2<INV, 1>(v[k + 2], v[k + 3], v[k + 6], v[k + 7], W32(4), W32(12));
    }
#pragma unroll
    for (int i = 0; i < 32; ++i) o[(i >> 3) + 2 * (i & 6) + 16 * (i & 1)] = v[i];
  } else {
    dft4k<INV, 0, 0, 0, 0>(v[0], v[1], v[2], v[3]);
    dft4k<INV, 0, 2, 4, 6>(v[4], v[5], v[6], v[7]);
    dft4k<INV, 0, 4, 8, 12>(v[8], v[9], v[10], v[11]);
    dft4k<INV, 0, 6, 12, 18>(v[12], v[13], v[14], v[15]);
#pragma unroll
    for (int i = 0; i < 16; ++i) o[(i >> 2) + 4 * (i & 3)] = v[i];
  }
#pragma unroll
  for (int i = 0; i < N; ++i) v[i] = o[i];
}
template <bool H32>
__device__ __forceinline__ void c_swap(c2& a, c2& b) {
#pragma unroll
  for (int j = 0; j < 2; ++j) {
    const unsigned x = __float_as_uint(a[j]), y = __float_as_uint(b[j]);
    const auto r = H32 ? __builtin_amdgcn_permlane32_swap(x, y, false, false) : __builtin_amdgcn_permlane16_swap(x, y, false, false);
    a[j] = __uint_as_float(r[0]);
    b[j] = __uint_as_float(r[1]);
  }
}
#define LANE_NOP()                  \
  do {                              \
    SB();                           \
    asm volatile("s_nop 1");        \
    SB();                           \
  } while (0)
__device__ __forceinline__ c2 lds_c2(int a) { return *(volatile LAS c2*)((LAS char*)g_lds + a); }
template <int N>
__device__ __forceinline__ void lds_put(const c2 (&v)[N], int a) {
#pragma unroll
  for (int i = 0; i < N / 2; ++i) *(volatile LAS f32x4*)((LAS char*)g_lds + a + 16 * i) = f32x4{v[2 * i][0], v[2 * i][1], v[2 * i + 1][0], v[2 * i + 1][1]};
}
constexpr int XS = 32768, WS_ = 16384, WT_OFF = 18 << 20;
__device__ __forceinline__ int side_adr(int hi, int lo, int sd) { return hi + ((lo >> 13) << 14) + (lo & 8191) + (sd ^ ((hi >> 8) & 8192)); }
constexpr int LDS_TW = 0;
constexpr int LDS_EB = 4096;
constexpr int LDS_STG = 4096 + 8 * 9216;
__device__ __forceinline__ void tw2048(c2 (&w)[32], int lane) {
#pragma unroll
  for (int k = 1; k <= 8; ++k) w[k] = lds_c2(LDS_TW + lane * k * 8);
  const c2 h = lds_c2(LDS_TW + (lane & 31) * 128);
  mul2(w[0], w[16], h, h, w[8], w[8]);
#pragma unroll
  for (int k = 9; k < 15; k += 2) mul2(w[k], w[k + 1], w[8], w[k - 8], w[8], w[k - 7]);
  mul2(w[15], w[24], w[8], w[7], w[16], w[8]);
#pragma unroll
  for (int k = 1; k < 8; ++k) mul2(w[16 + k], w[24 + k], w[16], w[k], w[24], w[k]);
}
template <bool INV>
__device__ __forceinline__ void fft2048(c2 (&v)[32], const c2 (&w)[32], int eb, int lane) {
  dftn<32, INV, 0>(v, w);
  lds_put<32>(v, eb + lane * 272);
  const int ra = eb + (lane >> 5) * 4352 + (lane & 31) * 8;
#pragma unroll
  for (int k = 0; k < 16; ++k) {
    v[k] = lds_c2(ra + k * 272);
    v[k + 16] = lds_c2(ra + k * 272 + 8704);
  }
#pragma unroll
  for (int k = 0; k < 16; k += 2) bf2<INV, 0>(v[k], v[k + 16], v[k + 1], v[k + 17], w[0], w[0]);
  LANE_NOP();
#pragma unroll
  for (int k = 0; k < 16; ++k) c_swap<true>(v[k], v[k + 16]);
  dftn<32, INV, 1>(v, w);
}
template <bool SC>
__device__ __forceinline__ void tw1024(c2 (&w)[16], c2 (&u)[4], int lane, float s) {
#pragma unroll
  for (int k = 1; k <= 4; ++k) w[k] = lds_c2(LDS_TW + lane * k * 16);
  u[1] = lds_c2(LDS_TW + (lane & 15) * 256);
  u[0] = u[1];
  mul2(u[2], w[8], u[1], u[1], w[4], w[4]);
  mul2(u[3], w[12], u[2], u[1], w[8], w[4]);
  w[0] = c2{s, 0.f};
  if (SC) {
#pragma unroll
    for (int k = 1; k < 4; ++k) w[k] *= s;
  }
  c2 z;
  mul2(w[5], w[6], w[4], w[1], w[4], w[2]);
  mul2(w[7], z, w[4], w[3], w[4], w[3]);
#pragma unroll
  for (int k = 1; k < 4; ++k) mul2(w[8 + k], w[12 + k], w[8], w[k], w[12], w[k]);
  if (SC) {
#pragma unroll
    for (int k = 4; k < 16; k += 4) w[k] *= s;
  }
}
template <bool INV>
__device__ __forceinline__ void fft1024(c2 (&v)[16], const c2 (&w)[16], const c2 (&u)[4], int eb, int lane) {
  dftn<16, INV, 0>(v, w);
  lds_put<16>(v, eb + lane * 144);
  const int ra = eb + ((lane >> 1) & 8) * 144 + (lane >> 5) * 576 + (lane & 15) * 8;
  c2 t[4][4];
#pragma unroll
  for (int a = 0; a < 4; ++a)
#pragma unroll
    for (int r = 0; r < 4; ++r) t[a][r] = lds_c2(ra + (a + 16 * r) * 144);
#pragma unroll
  for (int a = 0; a < 4; ++a) {
    dft4w<INV, false>(t[a][0], t[a][1], t[a][2], t[a][3], u[0], u[1], u[2], u[3]);
#pragma unroll
    for (int q = 0; q < 4; ++q) v[a + 8 * (q & 1) + 4 * (q >> 1)] = t[a][q];
  }
  LANE_NOP();
#pragma unroll
  for (int i = 0; i < 8; ++i) c_swap<false>(v[i], v[i + 8]);
#pragma unroll
  for (int i = 0; i < 8; ++i) c_swap<true>(v[(i & 3) + 2 * (i & 4)], v[(i & 3) + 2 * (i & 4) + 4]);
  dftn<16, INV, INV ? 2 : 1>(v, w);
}

__device__ __forceinline__ void fft_table(int w) {
  const int m = w * 64 + olane();
  float sn, cs;
  sincospif(-(float)m * (1.f / 1024.f), &sn, &cs);
  *(LAS c2*)((LAS char*)g_lds + LDS_TW + m * 8) = c2{cs, sn};
  RAW_BARRIER();
}
__device__ __forceinline__ void count_done(char* ws, int cnt_off, int w) {
  VMCNT(0);
  RAW_BARRIER();
  if (w == 0) cnt_inc(ws, cnt_off);
}
__device__ __forceinline__ void stg_adr(int lane, int w, int& up, int& mid, int& dn) {
  up = LDS_STG + lane * 128 + ((w ^ (lane >> 1)) & 7) * 16;
  dn = LDS_STG + (64 - lane) * 128 + ((w ^ ((64 - lane) >> 1)) & 7) * 16 + 8;
  mid = dn + 57344 - (lane == 0 ? 8 : 0);
}

#define P1A 2
#define P1B 0
#define P1X 0
#define FT(i)
#define FTV(i, x)
__device__ __forceinline__ void fft_pass1(char* ws, char* x1, int sd, const u16* __restrict__ x, const u16* __restrict__ wr,
                                          const u16* __restrict__ wi, int task, int w) {
  LAS char* const L = (LAS char*)g_lds;
  const __amdgpu_buffer_rsrc_t rx = __builtin_amdgcn_make_buffer_rsrc(x1, 0, 513 * XS, 0x00020000);
  const __amdgpu_buffer_rsrc_t rt = __builtin_amdgcn_make_buffer_rsrc(x1 + WT_OFF, 0, 513 * WS_, 0x00020000);
  const __amdgpu_buffer_rsrc_t ri = __builtin_amdgcn_make_buffer_rsrc((void*)x, 0, S * D * 2, 0x00020000);
  const __amdgpu_buffer_rsrc_t rr = __builtin_amdgcn_make_buffer_rsrc((void*)wr, 0, S * DF * 2, 0x00020000);
  const __amdgpu_buffer_rsrc_t rm = __builtin_amdgcn_make_buffer_rsrc((void*)wi, 0, S * DF * 2, 0x00020000);
  const int lane = olane();
  const int p = (P1X & 4) ? w : task * 8 + w, r0 = (p & 63) + 128 * (p >> 6);
  unsigned xa[16], xb[16], fr[2][9], fi[2][9];
  FT(0);
#pragma unroll
  for (int t = 0; t < 16; ++t) {
    xa[t] = __builtin_amdgcn_raw_buffer_load_b16(ri, lane * 2 + t * 128, r0 * 2048, P1A);
    xb[t] = __builtin_amdgcn_raw_buffer_load_b16(ri, lane * 2 + t * 128, r0 * 2048 + 131072, P1A);
  }
  const int l8 = lane == 0 ? 1024 : OOB;
#pragma unroll
  for (int r = 0; r < 2; ++r)
#pragma unroll
    for (int t = 0; t < 9; ++t) {
      fr[r][t] = __builtin_amdgcn_raw_buffer_load_b16(rr, t < 8 ? lane * 2 + t * 128 : l8, (r0 + 64 * r) * (DF * 2), P1B);
      fi[r][t] = __builtin_amdgcn_raw_buffer_load_b16(rm, t < 8 ? lane * 2 + t * 128 : l8, (r0 + 64 * r) * (DF * 2), P1B);
    }
  FT(1);
  c2 tw[16], tu[4], v[16];
  tw1024<false>(tw, tu, lane, 1.f);
  FTV(2, xb[15]);
#pragma unroll
  for (int t = 0; t < 16; ++t) v[t] = c2{__uint_as_float(xa[t] << 16), __uint_as_float(xb[t] << 16)};
  fft1024<false>(v, tw, tu, LDS_EB + w * 9216, lane);
  int up, mid, dn;
  stg_adr(lane, w, up, mid, dn);
#pragma unroll
  for (int j = 0; j < 16; ++j) *(volatile LAS c2*)(L + (j < 8 ? up + 8192 * j : (j == 8 ? mid : dn + 8192 * (15 - j)))) = v[j];
  FT(3);
  RAW_BARRIER();
  FTV(4, fi[1][8]);
  {
    const int fa = LDS_EB + lane * 64 + ((w ^ lane) & 6) * 8 + (w & 1) * 8;
#pragma unroll
    for (int t = 0; t < 9; ++t)
      if (t < 8 || lane == 0) *(LAS v2u*)(L + fa + t * 4096) = v2u{fr[0][t] | (fi[0][t] << 16), fr[1][t] | (fi[1][t] << 16)};
  }
  const int kq = w * 8 + (lane >> 3);
  {
    const int ra = LDS_STG + kq * 128 + ((lane ^ (kq >> 1)) & 7) * 16, ga = kq * XS + (lane & 7) * 16;
    v4u d[9];
#pragma unroll
    for (int i = 0; i < 9; ++i) d[i] = *(const LAS v4u*)(L + ra + i * 8192);
#pragma unroll
    for (int i = 0; i < 9; ++i)
      if ((i < 8 || w == 0) && !(P1X & 1))
        st128(d[i], rx, ga + (i == 8 ? 512 * XS : 0), side_adr((i & 7) * (64 * XS), task * 128, sd));
  }
  FT(5);
  RAW_BARRIER();
  {
    const int k4 = w * 16 + (lane >> 2);
    const int ra = LDS_EB + k4 * 64 + (((2 * lane) ^ k4) & 6) * 8, ga = k4 * WS_ + (lane & 3) * 16;
    v4u d[5];
#pragma unroll
    for (int i = 0; i < 5; ++i) d[i] = *(const LAS v4u*)(L + ra + i * 8192);
#pragma unroll
    for (int i = 0; i < 5; ++i)
      if ((i < 4 || w == 0) && !(P1X & 2))
        st128(d[i], rt, ga + (i == 4 ? 512 * WS_ : 0), side_adr((i & 3) * (128 * WS_), task * 64, sd ^ 8192));
  }
  FT(6);
  FT(7);
}
__device__ __forceinline__ void fft_pass2(char* ws, char* x1, int sd, int task, int w) {
  const __amdgpu_buffer_rsrc_t rx = __builtin_amdgcn_make_buffer_rsrc(x1, 0, 513 * XS, 0x00020000);
  const __amdgpu_buffer_rsrc_t rt = __builtin_amdgcn_make_buffer_rsrc(x1 + WT_OFF, 0, 513 * WS_, 0x00020000);
  const int lane = olane();
  const int k = task * 8 + w;
  if (k > 512) return;
  f32x4 d[16];
  FT(0);
#pragma unroll
  for (int t = 0; t < 16; ++t)
    d[t] = __builtin_bit_cast(f32x4, __builtin_amdgcn_raw_buffer_load_b128(rx, lane * 16 + (t & 3) * 1024, side_adr(k * XS, (t >> 2) * 4096, sd), AUX_SC1));
  c2 tw[32], v[32];
  tw2048(tw, lane);
  FT(1);
  const bool edge = (k & 511) == 0;
  FTV(2, d[15]);
  if (edge) {
#pragma unroll
    for (int t = 0; t < 16; ++t) {
      d[t][2] = d[t][0];
      d[t][3] = d[t][1];
    }
  }
#pragma unroll
  for (int t = 0; t < 16; ++t) {
    v[2 * t] = c_addc(c2{d[t][0], d[t][1]}, c2{d[t][2], d[t][3]});
    v[2 * t + 1] = c_sepb(c2{d[t][0], d[t][1]}, c2{d[t][2], d[t][3]});
  }
  SB();
  v2u f[16];
#pragma unroll
  for (int t = 0; t < 16; ++t) f[t] = __builtin_amdgcn_raw_buffer_load_b64(rt, lane * 8 + (t & 7) * 512, side_adr(k * WS_, (t >> 3) * 4096, sd ^ 8192), AUX_SC1);
  SB();
  const int eb = LDS_EB + w * 17408;
  fft2048<false>(v, tw, eb, lane);
  FTV(3, v[31]);
  FTV(4, f[15]);
#pragma unroll
  for (int t = 0; t < 16; ++t)
    mul2(v[2 * t], v[2 * t + 1], v[2 * t], c2{__uint_as_float(f[t][0] << 16), __uint_as_float(f[t][0] & 0xffff0000u)}, v[2 * t + 1],
         c2{__uint_as_float(f[t][1] << 16), __uint_as_float(f[t][1] & 0xffff0000u)});
  fft2048<true>(v, tw, eb, lane);
  FTV(5, v[31]);
  if (edge) {
#pragma unroll
    for (int t = 0; t < 32; ++t) v[t][1] = 0.f;
  }
#pragma unroll
  for (int t = 0; t < 16; ++t) {
    const c2 a = c_addi(v[2 * t], v[2 * t + 1]), b = c_mixc(v[2 * t], v[2 * t + 1]);
    st128(__builtin_bit_cast(v4u, f32x4{a[0], a[1], b[0], b[1]}), rx, lane * 16 + (t & 3) * 1024, side_adr(k * XS, (t >> 2) * 4096, sd));
  }
  FT(6);
  FT(7);
}
__device__ __forceinline__ void fft_pass3(char* ws, char* x1, int sd, u16* __restrict__ out, int task, int w) {
  LAS char* const L = (LAS char*)g_lds;
  const __amdgpu_buffer_rsrc_t rx = __builtin_amdgcn_make_buffer_rsrc(x1, 0, 513 * XS, 0x00020000);
  const __amdgpu_buffer_rsrc_t ro = __builtin_amdgcn_make_buffer_rsrc(out, 0, S * 4096, 0x00020000);
  const int lane = olane();
  const int kq = w * 8 + (lane >> 3);
  const int ra = LDS_STG + kq * 128 + ((lane ^ (kq >> 1)) & 7) * 16, ga = kq * XS + (lane & 7) * 16;
  v4u d[9];
  FT(0);
#pragma unroll
  for (int i = 0; i < 9; ++i) d[i] = __builtin_amdgcn_raw_buffer_load_b128(rx, ga + (i == 8 ? 512 * XS : 0), side_adr((i & 7) * (64 * XS), task * 128, sd), AUX_SC1);
  c2 tw[16], tu[4], v[16];
  tw1024<true>(tw, tu, lane, 1.f / (2.f * 2048.f * 1024.f));
  FT(1);
  FTV(2, d[8]);
#pragma unroll
  for (int i = 0; i < 9; ++i)
    if (i < 8 || w == 0) *(LAS v4u*)(L + ra + i * 8192) = d[i];
  int up, mid, dn;
  stg_adr(lane, w, up, mid, dn);
  FT(3);
  RAW_BARRIER();
  FT(4);
#pragma unroll
  for (int j = 0; j < 16; ++j) v[j] = lds_c2(j < 8 ? up + 8192 * j : (j == 8 ? mid : dn + 8192 * (15 - j)));
  fft1024<true>(v, tw, tu, LDS_EB + w * 9216, lane);
  FTV(5, v[15]);
  const int p = task * 8 + w, r0 = (p & 63) + 128 * (p >> 6);
#pragma unroll
  for (int t = 0; t < 16; ++t) {
    const unsigned o = pk2(v[t][0], v[t][1]);
    __builtin_amdgcn_raw_buffer_store_b16((u16)o, ro, lane * 2 + t * 128, r0 * 4096, 0);
    __builtin_amdgcn_raw_buffer_store_b16((u16)(o >> 16), ro, lane * 2 + t * 128, r0 * 4096 + 262144, 0);
  }
  FT(6);
  FT(7);
}

#define W7P 3
#define VTC 0
#define NXA 0
#define ARA 0
#define STA 0
#define SLR 2
#define RSO 5
#define NCAL 6
#define DOX2 300
#define DTC 100
#define NX1 4
#define NXG 2
#define NXK 5
#define OFD 400
#define STI 500
#define TNX 0
#define NXP 0
#define PACE 150
#define PFB 4
#define CV1 -70
#define CV2 110
#define EDL 0
#define EVB(k) EV(k)
constexpr int L_D = 16384, L_R = 32768, L_F = 122880;
__device__ __forceinline__ volatile LAS unsigned* flw(int i, int m) {
  return (volatile LAS unsigned*)((LAS char*)g_lds + L_F + i * 4 + (olane() & m) * 4);
}
template <int SLP = 0>
__device__ __forceinline__ void lds_wait_ge(int i, int m, unsigned v, gu32* tmo, unsigned code) {
  volatile LAS unsigned* p = flw(i, m);
  _Pragma("clang loop unroll(disable)") for (unsigned spins = 0;;) {
    if (__all(*p >= v)) break;
    if (SLP) __builtin_amdgcn_s_sleep(SLP);
    if (++spins > (kSpinCap << 5)) {
      tmo_report(tmo, code);
      break;
    }
  }
  asm volatile("" ::: "memory");
}
#define HLP 6
#define HLT 0
__device__ __forceinline__ void help(int c, int w, const u16* Wq, const u16* Wk, const u16* Wv) {
  const int j = c & 7, m = j >> 1, xg = c / PO, ln = olane();
  if ((c & 8) && w >= 1 && w <= HLP) {
    const u16* wm = m == 2 ? Wv : ((m == 3 ? j : m) & 1 ? Wk : Wq);
    const int hs = m == 3 ? 31 : PQ * xg + slow_head(wm) + 4 * (j & 1);
    if (m < 3 || xg == NXC - 1)
      for (int q = 0; q < 2; ++q)
        touch4((const char*)wm + hs * 256 + (size_t)((16 - w) ^ (hs & KROT)) * 524288, (q * 32 + (ln >> 1)) * 8192 + (ln & 1) * 128, 65536 + w * 256);
  }
}
template <bool PRE>
__device__ __forceinline__ unsigned owner(const int c, const int w, const u64 nonce, const u16* __restrict__ mem0, char* ws,
                                          u16* __restrict__ out, unsigned perm, const u16* Wq, const u16* Wk, const u16* Wv) {
  LAS char* const L = (LAS char*)g_lds;
  gu32* tmo = (gu32*)(ws + OFF_TMO);
  const int lane = olane();
  const int l15 = lane & 15, g = lane >> 4;
  const __amdgpu_buffer_rsrc_t rs = __builtin_amdgcn_make_buffer_rsrc(ws, 0, (int)WS_USED, 0x00020000);
  const int xg = c / PO, li = c % PO;
  const int r_a = (int)(((uintptr_t)ws >> 19) & 7);
  const int r_s = r_a + (int)(OFF_STEP / UNIT);
  volatile LAS unsigned* fl = (volatile LAS unsigned*)(L + L_F);
  unsigned m0v = 0;
  if constexpr (PRE) {
  if (w == 7) m0v = *(const unsigned*)(mem0 + (lane >> 3) * D + 16 * c + 2 * (lane & 7));
  if (c == 0 && w == 6) hs_lead<4>(rs, true, CNT_HSO, 64u);
  unsigned cal = 0;
  if (w == 0)
    st128(v4u{(unsigned)lane, 0u, 0u, 0u}, rs, (lane & 31) * 16 + (lane >> 5) * 32768, (int)OFF_SCR + (((li & 7) ^ xreg(r_a)) << 12) + (c >> 3) * 512);
  if (w == 0 && lane == 0) {
    const int cl = li & 7;
    int off = (int)OFF_SCR + ((cl ^ xreg(r_a)) << 12) + (c >> 3) * 512;
    unsigned best = 0xfffeu;
#pragma unroll 1
    for (int r = 0; r < NCAL; ++r) {
      VMCNT(0);
      const u64 t1 = __builtin_amdgcn_s_memtime();
      asm volatile("s_waitcnt lgkmcnt(0)" : "+v"(off)::"memory");
      const unsigned v = __builtin_amdgcn_raw_buffer_load_b32(rs, off, (r & 3) * 128 + (r >> 2) * 32768, AUX_SC1);
      asm volatile("s_waitcnt vmcnt(0)" ::"v"(v) : "memory");
      const u64 t2 = __builtin_amdgcn_s_memtime();
      best = min(best, (unsigned)(t2 - t1));
    }
    if (HLT == 0 && li >= 8) best = 0xfffeu;
    cal = ((unsigned)cl << 16) | best;
  }
  if (w == 0) {
    PT(0);
    hs_wait(rs, nonce, v2u{~(unsigned)nonce, 0u});
    cnt_inc(ws, CNT_HSO);
    PT(1);
    unsigned id;
    asm volatile("s_getreg_b32 %0, hwreg(HW_REG_XCC_ID)" : "=s"(id));
    if (lane == 0) {
      __builtin_amdgcn_raw_buffer_store_b32(((id & 15u) << 19) | cal, rs, c * 4, (int)OFF_CAL, AUX_SC1);
      if (li == 0) __builtin_amdgcn_raw_buffer_store_b32(id & 15u, rs, xg * 4, (int)OFF_XID, AUX_SC1);
    }
  }
  static_assert(NF1O == 64 || NF1O == 2, "one task per owner but the first two, or none");
  __syncthreads();
  if (HLT == 1) help(c, w, Wq, Wk, Wv);
  if (w == 7) {
    const unsigned pm = numa_perm(numa_load(rs, tmo), (LAS unsigned*)(L + L_F + 512));
    fl[10] = pm;
  }
  __syncthreads();
  perm = __builtin_amdgcn_readfirstlane(fl[10]);
  PT(2);
  if (HLT == 2) help(c, w, Wq, Wk, Wv);
  }
  const int xr0 = xreg(r_s + (PRE ? 0 : NARM + (li & 1)));
  const v4u sent = {0xffffffffu, 0xffffffffu, 0xffffffffu, 0xffffffffu};
  const int ibs = (int)OFF_VM + li * 65536 + ((cls_of(perm, xg) ^ xreg(r_a + (int)(OFF_VM / UNIT) + (li >> 3))) << 12);

  int am = (int)OFF_STEP + R_MEM + (c >> 4) * 32768 + ((cls_of(perm, g) ^ xr0) << 12) + (c & 15) * 256 + l15 * 16;
  int ap[2];
#pragma unroll
  for (int q = 0; q < 2; ++q) {
    const int o = c * 384 + (lane & 31) * 16;
    ap[q] = (lane & 31) < 24 && c < 59 ? (int)OFF_STEP + R_EX + (o >> 12) * 32768 + ((cls_of(perm, 2 * q + (lane >> 5)) ^ xr0) << 12) + (o & 4095)
                             : OOB;
  }
  int as = lane < 8 * NXC ? (int)OFF_STEP + R_ST + ((cls_of(perm, lane >> 3) ^ xr0) << 12) + (c >> 5) * 32768 + (c & 31) * 128 + (lane & 7) * 16
                          : OOB;
  const int ax = (int)OFF_STEP + R_PX + c * 1280 + lane * 16;
  const int ax2 = lane < 16 ? ax + 1024 : (lane < 24 ? (int)OFF_STEP + R_DUM + c * 128 + (lane - 16) * 16 : OOB);
#define ARM_REC(r)                                                                                                   \
  {                                                                                                                  \
    const int so_ = (r) * (int)UNIT, dx_ = (xreg(r_s + (r)) ^ xreg(r_s + (r) + 1)) << 12;                            \
    __builtin_amdgcn_raw_buffer_store_b128(sent, rs, am, so_, AUX_SC1);                                              \
    am ^= dx_;                                                                                                       \
    _Pragma("unroll") for (int q = 0; q < 2; ++q) {                                                                  \
      __builtin_amdgcn_raw_buffer_store_b128(sent, rs, ap[q], so_, AUX_SC1);                                         \
      ap[q] ^= dx_;                                                                                                  \
    }                                                                                                                \
    __builtin_amdgcn_raw_buffer_store_b128(sent, rs, as, so_, AUX_SC1);                                              \
    as ^= dx_;                                                                                                       \
    __builtin_amdgcn_raw_buffer_store_b128(sent, rs, ax, so_, AUX_SC1);                                              \
    __builtin_amdgcn_raw_buffer_store_b128(sent, rs, ax2, so_, AUX_SC1);                                             \
  }
  if (PRE && w == 7) {
    {
      const int row = lane >> 3, np = lane & 7;
#pragma unroll
      for (int r = 0; r < NXC; ++r)
        __builtin_amdgcn_raw_buffer_store_b32(min(m0v, 0xfffffffeu), rs,
                                              (int)OFF_STEP + R_MEM + (c >> 4) * 32768 + ((cls_of(perm, r) ^ xreg(r_s)) << 12) +
                                                  (c & 15) * 256 + ((row & 1) + 2 * (np & 1) + 4 * (row >> 1)) * 16 + (np >> 1) * 4,
                                              0, AUX_SC1);
    }
    am |= 1 << 30;
#pragma unroll 1
    for (int r = 0; r < NARM + (li & 1); ++r) {
      ARM_REC(r)
      am &= ~(1 << 30);
    }
#pragma unroll
    for (int k = 0; k < 32; ++k)
      __builtin_amdgcn_raw_buffer_store_b128(sent, rs, lane * 16 + (k & 3) * 1024, ibs + vm_slot(k >> 3) + ((k >> 2) & 1) * 32768, AUX_SC1);
    VMCNT(0);
    PTW(3, 7);
    cnt_inc(ws, CNT_ARM);
  }
  if constexpr (PRE) return perm;
  PT(5);
  __syncthreads();
  if (w == 7 && lane < 32) fl[lane] = 0;
  __syncthreads();

  const int team = li & 1, c0 = c & ~1;
  const int l16 = lane * 16;
  const int cown = cls_of(perm, xg);
  if (w < 2) {
    const int r = (l15 & 1) + 2 * (l15 >> 2), kh = (l15 >> 1) & 1;
    const int nl = (kh ^ ((r >> 1) & 1)) + 2 * ((g & 1) ^ (r >> 2));
    typedef int v8i __attribute__((ext_vector_type(8)));
    v8i adr;
    adr[0] = l16;
    adr[1] = 32768 + SC_POS(2 * nl + (g >> 1), r);
    adr[2] = l16;
    adr[3] = L_D + w * 8192 + l16;
    adr[4] = ((l15 & 3) + 4 * g) * 16 + (l15 >> 2) * 4;
    adr[5] = (l15 & 1) ? 0x03020706 : 0x05040100;
    adr[6] = (l15 & 2) ? 0x4444eeee : 0xeeee4444;
    adr[7] = L_F + 48;
    u64 mk[4];
#pragma unroll
    for (int k = 0; k < 4; ++k) mk[k] = __builtin_amdgcn_ballot_w64(nl == k);
    const v4u rsv = {(unsigned)(uintptr_t)ws, (unsigned)((uintptr_t)ws >> 32) & 0xffffu, (unsigned)WS_USED, 0x00020000u};
    const int dtc_ = TN(12, DTC);
    const int eg = (c0 >> 2) & 1;
#pragma unroll 1
    for (int s = team; s < NSEG; s += 2) {
      EVDECL;
      const int so = (int)OFF_STEP + s * (int)UNIT;
      int sp = so + R_PM + ((cown ^ xreg(r_s + s)) << 12);
      int sv = ibs + (w - team) * 65536 + vm_slot(s);
      int sv1 = sv + eg * 32768, sv2 = sv + (1 - eg) * 32768, sp1 = sp + eg * 2048, sp2 = sp + (1 - eg) * 2048;
      const int xn = xreg(r_s + s + 1);
      int sm[NXC];
#pragma unroll
      for (int q = 0; q < NXC; ++q) {
        sm[q] = so + (int)UNIT + R_MEM + (c0 >> 4) * 32768 + ((cls_of(perm, q) ^ xn) << 12) + ((c0 + w) & 15) * 256;
        asm volatile("" : "+s"(sm[q]));
      }
      asm volatile("" : "+s"(sp), "+s"(sv1), "+s"(sv2), "+s"(sp1), "+s"(sp2));
      if (s >= 2) lds_wait_ge<4>(20, 3, s - 1, tmo, 0x30017);
      if (VTC && w == 0) {
        const unsigned tc = __builtin_amdgcn_raw_buffer_load_b32(rs, 0, sv ^ 8192, AUX_SC1);
        asm volatile("" ::"v"(tc));
      }
      v4u bl[8];
      if (s < NSEG - 1) {
        v32u blv;
        unsigned cnt, bad;
        asm volatile(OW_F
                     : [cnt] "=&s"(cnt), [st] "=&s"(bad), "={v[0:31]}"(blv) EVOUT
                     : "{v[112:119]}"(adr), [rs] "s"(rsv), [sv1] "s"(sv1), [sv2] "s"(sv2), [sp] "s"(sp), [sp1] "s"(sp1), [sp2] "s"(sp2),
                       [sm0] "s"(sm[0]), [sm1] "s"(sm[1]), [sm2] "s"(sm[2]), [sm3] "s"(sm[3]), [m0] "s"(mk[0]), [m1] "s"(mk[1]),
                       [m2] "s"(mk[2]), [m3] "s"(mk[3])
                     : OF_CLOB, "vcc", "scc", "memory");
#pragma unroll
        for (int k = 0; k < 8; ++k) bl[k] = v4u{blv[4 * k], blv[4 * k + 1], blv[4 * k + 2], blv[4 * k + 3]};
        if (__builtin_expect(bad != 0u, 0)) {
          v4u xs, pv[4];
          _Pragma("clang loop unroll(disable)") for (unsigned spins = 0;;) {
            OPQ(va, l16);
            OPQ(v1, adr[1]);
            OPQ(v2, adr[2]);
#pragma unroll
            for (int k = 0; k < 8; ++k) bl[k] = __builtin_amdgcn_raw_buffer_load_b128(rs, va + (k & 3) * 1024, k < 4 ? sv1 : sv2, AUX_SC1);
            xs = __builtin_amdgcn_raw_buffer_load_b128(rs, v1, sp, AUX_SC1);
#pragma unroll
            for (int k = 0; k < 4; ++k) pv[k] = __builtin_amdgcn_raw_buffer_load_b128(rs, v2 + (k & 1) * 1024, k < 2 ? sp1 : sp2, AUX_SC1);
            unsigned m = mx4(0u, xs);
#pragma unroll
            for (int k = 0; k < 4; ++k) m = mx4(m, pv[k]);
#pragma unroll
            for (int k = 0; k < 8; ++k) m = mx4(m, bl[k]);
            if (!anysent(m)) break;
            __builtin_amdgcn_s_sleep(RSO);
            if (spin_fail(spins, tmo, 0x20000 + s)) break;
          }
          f32x4 a0 = {0.f, 0.f, 0.f, 0.f};
#pragma unroll
          for (int i = 0; i < 8; ++i) {
            const int k = F_ORD[i] >= 'a' ? F_ORD[i] - 'a' : F_ORD[i] - '0';
            if (F_ORD[i] >= 'a') {
              const v4u z = v4u{nl == k ? xs[0] : 0u, nl == k ? xs[1] : 0u, nl == k ? xs[2] : 0u, nl == k ? xs[3] : 0u};
              const v8u db = cat8(*(const LAS v4u*)(L + adr[3] + k * 2048), *(const LAS v4u*)(L + adr[3] + k * 2048 + 1024));
              a0 = (k & 1) ? smf1(z, db, a0, adr[6]) : smf(z, db, a0, adr[6]);
            } else
              a0 = smf(pv[k], cat8(bl[2 * k], bl[2 * k + 1]), a0, adr[6]);
          }
          const unsigned own = pk2(a0[0] + a0[2], a0[1] + a0[3]);
          const unsigned nb = (unsigned)__builtin_amdgcn_mov_dpp((int)own, 0xb1, 0xf, 0xf, true);
          const unsigned o = __builtin_amdgcn_perm(nb, own, (unsigned)adr[5]);
#pragma unroll
          for (int q = 0; q < NXC; ++q) __builtin_amdgcn_raw_buffer_store_b32(o, rs, adr[4], sm[q], AUX_SC1);
          EV(3);
        }
      } else {
        BARE_BARRIER();
        wait_cyc(*flw(12, 0));
        _Pragma("clang loop unroll(disable)") for (unsigned spins = 0;;) {
          OPQ(va, l16);
          unsigned m = 0;
#pragma unroll
          for (int k = 0; k < 8; ++k) bl[k] = __builtin_amdgcn_raw_buffer_load_b128(rs, va + (k & 3) * 1024, k < 4 ? sv1 : sv2, AUX_SC1);
#pragma unroll
          for (int k = 0; k < 8; ++k) m = mx4(m, bl[k]);
          if (!anysent(m)) break;
          __builtin_amdgcn_s_sleep(RSO);
          if (spin_fail(spins, tmo, 0x20000 + s)) break;
        }
      }
      const unsigned tp = cyc32();
      if (s < 4) PT(9 + (s >> 1));
      *flw(2 + w, 0) = s + 1;
      if (s < NSEG - 1) {
        const v4u tv = *(volatile LAS v4u*)flw(12, 0);
        const int lr = olane();
        __builtin_amdgcn_raw_buffer_store_b32(min(((tp - tv[1]) * tv[2]) >> 16, 60000u), rs,
                                              ((cls_of(perm, lr & 3) ^ xn) << 12) | ((lr & 4) << 9) | ((lr & 8) << 12) | ((lr >> 4) << 28),
                                              so + (int)UNIT + R_ST + (c0 + w) * 32 + 4, AUX_SC1);
      }
      OPQ(vs, l16);
#pragma unroll
      for (int k = 0; k < 8; ++k) *(LAS v4u*)(L + w * 8192 + ((k ^ (4 * eg)) * 1024) + vs) = bl[k];
      asm volatile("s_waitcnt lgkmcnt(0)" ::: "memory");
      *flw(4 + w, 0) = s + 1;
      if (s < NSEG - 4) {
        lds_wait_ge(2, 1, s + 1, tmo, 0x3001a);
        v4u se;
        asm volatile("v_mov_b32 %0, -1" : "=v"(se[0]));
        se[1] = se[2] = se[3] = se[0];
#pragma unroll
        for (int k = 0; k < 8; ++k) __builtin_amdgcn_raw_buffer_store_b128(se, rs, vs + (k & 3) * 1024, sv + (k >> 2) * 32768, AUX_SC1 | ARA);
        asm volatile("s_nop 1" ::"v"(se));
      }
      if (s < NSEG - 2) {
        wait_cyc(tp + dtc_);
        const int lt = olane();
        const unsigned tc = __builtin_amdgcn_raw_buffer_load_b32(rs, lt < 40 ? lt * 128 : OOB, so + R_PX + ((li & ~1) + w) * 5120, AUX_SC1);
        asm volatile("" ::"v"(tc));
      }
      EVDUMP(s);
    }
  } else if (w < 6) {
    const int n = w - 2;
    const v4u rsv = {(unsigned)(uintptr_t)ws, (unsigned)((uintptr_t)ws >> 32) & 0xffffu, (unsigned)WS_USED, 0x00020000u};
    const int vfr = (int)((perm & 8192u) ^ (VFAR == 2 ? 8192u : 0u)) | (r_a << 19);
    const int fv0 = fvw((c0 >> 1) & 3, 8 * n, team);
    const int dox_ = TN(10, DOX2), pace_ = TN(11, PACE);
    v4u raw[2][8];
#define RAW_LD(dst, sg)                                                                                       \
  _Pragma("unroll") for (int t = 0; t < 2; ++t) _Pragma("unroll") for (int k = 0; k < 8; ++k) dst[t][k] =     \
      __builtin_amdgcn_raw_buffer_load_b128(rs, l16 + (k & 3) * 1024, vw_addr(vfr, c0 + t, sg, 2 * n + (k >> 2)), AUX_SC1);
    wait_flag<KF0>(rs, fv0 + (lane & 7), tmo, 0x30018);
    PTW(6, 2);
    RAW_LD(raw, team)
    int nrdy = team;
#pragma unroll 1
    for (int s = team; s < NSEG; s += 2) {
      EVDECL;
      const int so = (int)OFF_STEP + s * (int)UNIT;
      if (s < NSEG - 1) {
        const int ln = olane();
        OPQ(eidx, (ln & 4) ? 0xeeee : 0x4444);
        OPQ(vea, (ln & 4) * 128 + (ln >> 4) * 128 + ((ln & 3) + 4 * (((ln >> 3) & 1) ^ (n >> 1))) * 16);
        int se = so + R_EX + n * 32768 + ((cown ^ xreg(r_s + s)) << 12);
        v8u bx[2][4];
#pragma unroll
        for (int t = 0; t < 2; ++t)
#pragma unroll
          for (int k = 0; k < 4; ++k) {
            const v4u u = raw[t][2 * k], v = raw[t][2 * k + 1];
            bx[t][k] = v8u{u[0], v[0], u[2], v[2], u[1], v[1], u[3], v[3]};
            asm volatile("" : "+v"(bx[t][k]));
          }
        OPQ(vd, L_D + n * 2048 + ln * 16);
        asm volatile("" : "+s"(se));
        EV(3);
        if (s < 4) PTW(7 + (s >> 1), 2);
        unsigned bad;
        asm volatile(OW_E
                     : [st] "=&s"(bad) EVOUT
                     : "{v241}"(vea), "{v242}"(vd), "{v243}"(eidx), [rs] "s"(rsv), [se] "s"(se), [b00] "v"(bx[0][0]), [b01] "v"(bx[0][1]),
                       [b02] "v"(bx[0][2]), [b03] "v"(bx[0][3]), [b10] "v"(bx[1][0]), [b11] "v"(bx[1][1]), [b12] "v"(bx[1][2]),
                       [b13] "v"(bx[1][3])
                     : OE_CLOB, "vcc", "scc", "memory");
        if (__builtin_expect(bad != 0u, 0)) {
          v4u pv[4];
          _Pragma("clang loop unroll(disable)") for (unsigned spins = 0;;) {
            __builtin_amdgcn_s_sleep(RSO);
            asm volatile("" : "+v"(vea));
            unsigned m = 0;
#pragma unroll
            for (int k = 0; k < 4; ++k) pv[k] = __builtin_amdgcn_raw_buffer_load_b128(rs, vea + k * 1024, se, AUX_SC1);
#pragma unroll
            for (int k = 0; k < 4; ++k) m = mx4(m, pv[k]);
            if (!anysent(m) || spin_fail(spins, tmo, 0x10000 + s)) break;
          }
#pragma unroll
          for (int q = 0; q < 4; ++q) {
            const f32x4 z = {0.f, 0.f, 0.f, 0.f};
            const f32x4 d = smf(pv[2 * (q & 1)], bx[q >> 1][2 * (q & 1)], z, eidx), e = smf(pv[2 * (q & 1) + 1], bx[q >> 1][2 * (q & 1) + 1], z, eidx);
            *(LAS v4u*)(L + vd + (q >> 1) * 8192 + (q & 1) * 1024) = v4u{pk2(d[0], d[1]), pk2(d[2], d[3]), pk2(e[0], e[1]), pk2(e[2], e[3])};
          }
          EV(2);
          RAW_BARRIER();
        }
      } else BARE_BARRIER();
      v4u nx[2][8];
      const int sn = min(s + 2, NSEG - 1);
      unsigned tpb = 0;
#define VW_FLAGS                                                                                                      \
  {                                                                                                                   \
    const int lf = olane();                                                                                           \
    OPQ(vfl, fv0 + min((s >> 1) + 1 + (lf >> 3), NSEG / 2 - 1) * 8 + (lf & 7));                                        \
    const u64 miss = __builtin_amdgcn_ballot_w64(__builtin_amdgcn_raw_buffer_load_b8(rs, vfl, 0, AUX_SC1) != 0);      \
    nrdy = s + 2 * (miss ? __builtin_ctzll(miss) >> 3 : 8);                                                           \
  }
      if (s < NSEG - 2) {
        __builtin_amdgcn_s_sleep(5);
        lds_wait_ge(2, 1, s + 1, tmo, 0x30014);
        tpb = cyc32();
        const unsigned tw = tpb + dox_;
        if (nrdy < s + 2) VW_FLAGS
        wait_cyc(tw);
      }
      EVB(6);
      OPQ(va, s < NSEG - 2 && nrdy >= s + 2 ? l16 : OOB);
#define NX_LD(dst, q0, q1)                                                                              \
  _Pragma("unroll") for (int q = q0; q < q1; ++q) dst[q >> 3][q & 7] = __builtin_amdgcn_raw_buffer_load_b128( \
      rs, va + (q & 3) * 1024, vw_addr(vfr, c0 + (q >> 3), sn, 2 * n + ((q >> 2) & 1)), AUX_SC1 | NXA);
      NX_LD(nx, 0, NX1)
      SB();
      if (s < NSEG - 2) wait_cyc(cyc32() + ((n + (li >> 1)) & 3) * pace_);
      else lds_wait_ge(4, 1, s + 1, tmo, 0x30014);
      EVB(4);
      f32x4 ac[2][2];
      const int sx = so + R_PX + n * 8192, sy = so + R_PX + 32768 + n * 2048;
      v4u pv[2][10], vm[2][2];
      OPQ(vb, l16);
#define P_LD                                                                                         \
  _Pragma("unroll") for (int k = 0; k < 10; ++k) _Pragma("unroll") for (int rt = 0; rt < 2; ++rt) pv[rt][k] = \
      __builtin_amdgcn_raw_buffer_load_b128(rs, vb + (k & 3) * 1024, (k < 8 ? sx + (k >> 2) * 4096 : sy) + rt * 40960, AUX_SC1);
#define P_MM(k0, k1)                                                                                 \
  _Pragma("unroll") for (int k = k0; k < k1; ++k) _Pragma("unroll") for (int rt = 0; rt < 2; ++rt) {  \
    _Pragma("unroll") for (int t = 0; t < 2; ++t)                                                    \
      ac[rt][t] = mma(k < 8 ? raw[t][k & 7] : vm[t][k & 1], pv[rt][k], k == 0 ? f32x4{0.f, 0.f, 0.f, 0.f} : ac[rt][t]); \
    asm volatile("" : "+v"(ac[rt][0]), "+v"(ac[rt][1]));                                             \
  }
      P_LD
      SB();
      lds_wait_ge(4, 1, s + 1, tmo, 0x30014);
#pragma unroll
      for (int t = 0; t < 2; ++t)
#pragma unroll
        for (int k = 0; k < 2; ++k) vm[t][k] = *(const LAS v4u*)(L + vb + t * 8192 + n * 2048 + k * 1024);
      SB();
      P_MM(0, 10)
      if (__builtin_expect(__builtin_amdgcn_ballot_w64(__builtin_isunordered(ac[0][0][0], ac[1][0][0])) != 0ull, 0)) {
        _Pragma("clang loop unroll(disable)") for (unsigned spins = 0;;) {
          asm volatile("" : "+v"(vb));
          P_LD
          unsigned m = 0;
#pragma unroll
          for (int k = 0; k < 10; ++k) m = mx4(mx4(m, pv[0][k]), pv[1][k]);
          if (!anysent(m)) break;
          __builtin_amdgcn_s_sleep(8);
          if (spin_fail(spins, tmo, 0x30015)) break;
        }
        P_MM(0, 10)
      }
      EVB(7);
      if (__builtin_expect(s < NSEG - 2 && nrdy < s + 2, 0)) {
        _Pragma("clang loop unroll(disable)") for (unsigned spins = 0;;) {
          VW_FLAGS
          if (nrdy >= s + 2) break;
          __builtin_amdgcn_s_sleep(8);
          if (spin_fail(spins, tmo, 0x30018)) break;
        }
        va = vb;
        NX_LD(nx, 0, NX1)
      }
      NX_LD(raw, NX1, 16)
      SB();
      OPQ(vr, L_R + (s & 2) * 8192 + l16);
#pragma unroll
      for (int q = 0; q < 4; ++q) *(LAS f32x4*)(L + vr + q * 4096 + n * 1024) = ac[q >> 1][q & 1];
      asm volatile("s_waitcnt lgkmcnt(0)" ::: "memory");
      fl[20 + n] = s + 1;
      lds_wait_ge(20, 3, s + 1, tmo, 0x30016);
      {
        const LAS f32x4* pr = (const LAS f32x4*)(L + vr + n * 4096);
        const f32x4 o = (pr[0] + pr[64]) + (pr[128] + pr[192]);
        const int lo = olane();
        *(v2u*)(out + (size_t)(s * 32 + 16 * (n >> 1) + (lo & 15)) * 2048 + 1024 + 16 * (c0 + (n & 1)) + 4 * (lo >> 4)) =
            v2u{pk2(o[0], o[1]), pk2(o[2], o[3])};
      }
      EVB(5);
#pragma unroll
      for (int q = 0; q < NX1; ++q) raw[q >> 3][q & 7] = nx[q >> 3][q & 7];
      EVDUMP(s);
    }
  } else if (w == 6) {
    if (c == 1) hs_lead<64>(rs, false, CNT_HS, 192u);
  } else {
    int narm = NARM + team;
    if (W7P) __builtin_amdgcn_s_setprio(W7P);
    const int sdk_ = NS2F(TN(2, SDK)), cv1_ = NS2F(TN(3, CV1)), cv2_ = NS2F(TN(4, CV2));
    arm_wait(rs, tmo);
    PTW(4, 7);
    Clk ck;
    clk_init(ck);
    unsigned t1 = cyc32();
    const int mg_ = NS2F(TN(0, MARG)), tol_ = NS2F(TN(13, TOL)), xl_ = NS2F(TN(14, XLATE));
    Pace pc = {0u, NS2F(ED0), NS2F(EA0), 0};
    const int sti_ = TN(15, STI), ofd_ = NS2F(TN(9, OFD));
#pragma unroll 1
    for (int s = 0; s < NSEG; ++s) {
      EVDECL;
      const bool on = (s & 1) == team;
      if (!on || s < 3) clk_update(ck, s < 3);
      int k1 = (cv1_ * (int)ck.cq) >> 16, k2 = (cv2_ * (int)ck.cq) >> 16;
      int sst = (int)OFF_STEP + s * (int)UNIT + R_ST + ((cown ^ xreg(r_s + s)) << 12);
      asm volatile("" : "+s"(k1), "+s"(k2), "+s"(sst));
      OPQ(va, lane * 32);
      OPQ(vt, L_F + 48);
      wait_cyc(t1);
      EV(0);
      v2u st = __builtin_amdgcn_raw_buffer_load_b64(rs, va, sst, AUX_SC1 | STA);
      clk_fresh(ck);
      asm volatile("" : "+s"(ck.r), "+s"(ck.m));
      asm volatile("" : "+v"(st));
      if (__builtin_expect(anysent(st[0]), 0)) {
        _Pragma("clang loop unroll(disable)") for (unsigned spins = 1;; ++spins) {
          asm volatile("" : "+v"(va));
          if (spins <= 2 || !on) wait_cyc(t1 + spins * sti_);
          else if (spins > 24) __builtin_amdgcn_s_sleep(4);
          else if (SLR) __builtin_amdgcn_s_sleep(SLR);
          st = __builtin_amdgcn_raw_buffer_load_b64(rs, va, sst + (spins == 1 ? 2048 : (spins == 2 ? 32768 : 0)), AUX_SC1 | STA);
          if (!anysent(st[0])) break;
          if (spins > kSpinCap) {
            tmo_report(tmo, 0x3000b);
            break;
          }
        }
        if (s == 0) clk_fresh(ck);
      }
      if (s < 4) PTW(11 + s, 7);
      const int dm = stamp_max(ck, st[0]);
      const unsigned tr = clk_cyc(ck, dm);
      if (on) {
        *(volatile LAS v4u*)(L + vt) = v4u{tr + k2, tr, ck.inv, 0u};
        EV(1);
        wait_cyc(tr + k1);
        RAW_BARRIER();
        if (s < NSEG - 1) BARE_BARRIER();
        if (narm < NSEG) {
          __builtin_amdgcn_s_sleep(5);
          lds_wait_ge(2, 1, s + 1, tmo, 0x3001b);
#pragma unroll 1
          for (int q = 0; q < 2; ++q) {
            if (narm < NSEG) ARM_REC(narm)
            ++narm;
          }
        }
      }
      pace_step(pc, s, (ck.r << 4) + dm, st[1], mg_, tol_, xl_);
      t1 = clk_cyc(ck, dm + pc.dq + pc.ea - sdk_ + (on ? ofd_ : 0));
      EVDUMP(s);
      EVCLK(s, ck);
    }
  }
  return 0u;
}
__global__ __launch_bounds__(512, 2) void mega_kernel(const u16* __restrict__ x, const u16* __restrict__ mem0,
                                                      const u16* __restrict__ Wq, const u16* __restrict__ Wk,
                                                      const u16* __restrict__ Wv, const u16* __restrict__ Wo,
                                                      char* __restrict__ ws, u16* __restrict__ out,
                                                      const u16* __restrict__ ax, const u16* __restrict__ awr,
                                                      const u16* __restrict__ awi) {
  ws += (size_t)(-(uintptr_t)ws) & (UNIT - 1);
  const int bx = BIDX;
  const int w = __builtin_amdgcn_readfirstlane(threadIdx.x >> 6);
  const int xg = bx & 7, idx = bx >> 3;
#define TEND
#define TMID(k)
#define CODEPF 16
  if (w == 7 && idx < CODEPF) {
    u64 pc;
    unsigned len, keep;
    asm volatile("s_getpc_b64 %0\n.Lcode_here:\n\ts_mov_b32 %1, .Lcode_end-.Lcode_here\n\t.subsection 1\n.Lcode_end:\n\t.subsection 0" : "=s"(pc), "=s"(len));
    const v4u rc = {(unsigned)pc, (unsigned)(pc >> 32) & 0xffffu, len, 0x00020000u};
    asm volatile("s_mov_b32 %0, m0\n\ts_mov_b32 m0, %3\n\ts_nop 0\n\tbuffer_load_dword %1, %2, 0 offen lds\n\ts_mov_b32 m0, %0"
                 : "=&s"(keep)
                 : "v"(olane() * 128 + idx * 8192), "s"(rc), "s"(32768 + 7 * 2048)
                 : "memory");
  }
  const u64 nonce = hs_post(__builtin_amdgcn_make_buffer_rsrc(ws, 0, 65536, 0x00020000), w, xg >= NXC && (idx & PQ) ? 7 : 0);
  TMID(0);
  int role, h, par = 0;
  if (xg < NXC) {
    role = idx >= 2 * PQ ? 4 : idx >= PQ;
    h = xg * PQ + idx % PQ;
  } else {
    role = 2 + (idx / PQ & 1);
    par = idx / (2 * PQ);
    h = (xg - NXC) * PQ + idx % PQ;
  }
  if (role == 0) wroles<0>(h, par, idx & 7, xg, w, nonce, x, Wq, Wk, Wv, Wo, ws);
  if (role == 1) wroles<1>(h, par, idx & 7, xg, w, nonce, x, Wq, Wk, Wv, Wo, ws);
  if (role == 2) wroles<2>(h, par, idx & 7, xg, w, nonce, x, Wq, Wk, Wv, Wo, ws);
  if (role == 3) wroles<3>(h, par, idx & 7, xg, w, nonce, x, Wq, Wk, Wv, Wo, ws);
  const int oc = xg * PO + idx - 2 * PQ;
  unsigned perm = 0;
  if (role == 4 && HLT == 0) help(oc, w, Wq, Wk, Wv);
  if (role == 4) {
    perm = owner<true>(oc, w, nonce, mem0, ws, out, 0u, Wq, Wk, Wv);
    VMCNT(0);
#define PFT 1300
#define PFM 5
#define PFG 0
    if (PFT && w < 6 && ((PFM >> (w >> 1)) & 1)) {
      const u16* src = w < 2 ? x : (w < 4 ? ax : (w == 4 ? awr : awi));
      const __amdgpu_buffer_rsrc_t ri = __builtin_amdgcn_make_buffer_rsrc((void*)src, 0, w < 4 ? S * D * 2 : S * DF * 2, 0x00020000);
      _Pragma("clang loop unroll(disable)") for (int k = 0; k < 4096 && now32() - (unsigned)nonce < PFT + (w >> 1) * PFG; ++k) __builtin_amdgcn_s_sleep(4);
      unsigned t[5];
#pragma unroll
      for (int q = 0; q < 5; ++q)
        t[q] = __builtin_amdgcn_raw_buffer_load_b32(ri, olane() * 128 + oc * 8192 + (q + (w < 4 ? (w & 1) * 4 : 0)) * 524288 + (w < 4 && q == 4 ? OOB : 0), 0, 0);
      asm volatile("" ::"v"(t[0]), "v"(t[1]), "v"(t[2]), "v"(t[3]), "v"(t[4]));
    }
  }
  TMID(2);
  const __amdgpu_buffer_rsrc_t rs = __builtin_amdgcn_make_buffer_rsrc(ws, 0, 65536, 0x00020000);
  gu32* tmo = (gu32*)(ws + OFF_TMO);
  volatile LAS int* sact = (volatile LAS int*)(g_lds + 163584);
  unsigned idle = 0;
  bool tab = false;
#define FSIDE 0
  char* const x1 = ws + OFF_X1 + ((size_t)(-(uintptr_t)(ws + OFF_X1)) & ((2u << 20) - 1));
  int sd = 0;
#define TLOG(id)
  auto pick = [&]() __attribute__((always_inline)) {
    int off = CNT_FFT1 + (olane() & 7) * 128;
    asm volatile("" : "+v"(off));
    const unsigned v = __builtin_amdgcn_raw_buffer_load_b32(rs, off, 0, AUX_SC1) + 1u;
    const unsigned d1 = __builtin_amdgcn_readlane(v, 0), d2 = __builtin_amdgcn_readlane(v, 1);
    const unsigned q1 = __builtin_amdgcn_readlane(v, 4), q2 = __builtin_amdgcn_readlane(v, 5), q3 = __builtin_amdgcn_readlane(v, 6);
    int kind = -1, qoff = 0;
    unsigned lim = 0;
    if (q1 < 130u - NF1O) kind = 1, qoff = Q_FFT1, lim = 130u - NF1O;
    else if (d1 >= 128u && q2 < 65u) kind = 2, qoff = Q_FFT2, lim = 65u;
    else if (d2 >= 65u && q3 < 128u) kind = 3, qoff = Q_FFT3, lim = 128u;
    else if (q3 >= 128u) kind = -2;
    int act = kind;
    if (kind >= 0) {
      unsigned t = 0;
      if (olane() == 0) t = __hip_atomic_fetch_add((gu32*)(ws + qoff), 1u, RLX_AGENT) + 1u;
      t = __builtin_amdgcn_readfirstlane(t);
      act = t < lim ? (kind << 16) | (int)t : -3;
    }
    return act;
  };
  for (;;) {
    if (w == 0) *sact = role == 4 ? (oc > 1 && oc < NF1O ? 0x18000 | oc : -2) : pick();
    RAW_BARRIER();
    const int act = __builtin_amdgcn_readfirstlane(*sact);
    RAW_BARRIER();
    if (act == -2) break;
    if (act >= 0) {
      const int kind = act >> 16, t = act & 0xffff;
      TLOG(1000 + kind);
      if (!tab) {
        if (w == 7) sact[1] = numa_far(numa_load(rs, tmo));
        fft_table(w);
        sd = (__builtin_amdgcn_readfirstlane(sact[1]) ^ (int)((uintptr_t)x1 >> 8) ^ FSIDE) & 8192;
      }
      tab = true;
      if (kind == 1) {
        fft_pass1(ws, x1, sd, ax, awr, awi, (t & 0x8000) ? t & 0xff : (t < 2 ? t : NF1O - 2 + t), w);
        count_done(ws, CNT_FFT1, w);
      } else if (kind == 2) {
        fft_pass2(ws, x1, sd, t == 0 ? 64 : t - 1, w);
        count_done(ws, CNT_FFT2, w);
      } else fft_pass3(ws, x1, sd, out, t, w);
      TLOG(999);
      if (role == 4) break;
    } else if (act == -1) {
      __builtin_amdgcn_s_sleep(64);
      if (spin_fail(idle, tmo, 0x30022)) break;
    }
  }
  if (role == 4) {
    owner<false>(oc, w, nonce, mem0, ws, out, perm, Wq, Wk, Wv);
    return;
  }
  TEND;
}

extern "C" void kernel_launch(void* const* d_in, const int* in_sizes, int n_in, void* d_out, int out_size, void* d_ws,
                              size_t ws_size, hipStream_t stream) {
  const u16* A_x = (const u16*)d_in[0];
  const u16* A_wr = (const u16*)d_in[1];
  const u16* A_wi = (const u16*)d_in[2];
  const u16* B_x = (const u16*)d_in[3];
  const u16* B_mem0 = (const u16*)d_in[4];
  const u16* Wq = (const u16*)d_in[5];
  const u16* Wk = (const u16*)d_in[6];
  const u16* Wv = (const u16*)d_in[7];
  const u16* Wo = (const u16*)d_in[8];
  u16* out = (u16*)d_out;
  char* ws = (char*)d_ws;

  mega_kernel<<<256, 512, 0, stream>>>(B_x, B_mem0, Wq, Wk, Wv, Wo, ws, out, A_x, A_wr, A_wi);
}
```

```cpp
#include <hip/hip_runtime.h>
#include <hip/hip_bf16.h>
#include <stdint.h>
#include <type_traits>

typedef unsigned short u16;
typedef unsigned long long u64;
using bf16x8 = __attribute__((ext_vector_type(8))) __bf16;
using bf16x16 = __attribute__((ext_vector_type(16))) __bf16;
using f32x4 = __attribute__((ext_vector_type(4))) float;
typedef unsigned v32u __attribute__((ext_vector_type(32)));
typedef unsigned v16u __attribute__((ext_vector_type(16)));
typedef unsigned v8u __attribute__((ext_vector_type(8)));
typedef unsigned v4u __attribute__((ext_vector_type(4)));
typedef unsigned v2u __attribute__((ext_vector_type(2)));
typedef short s16x4 __attribute__((ext_vector_type(4)));
typedef float f32x2 __attribute__((ext_vector_type(2)));
#define GAS __attribute__((address_space(1)))
#define LAS __attribute__((address_space(3)))
typedef GAS unsigned gu32;
#define RLX_AGENT __ATOMIC_RELAXED, __HIP_MEMORY_SCOPE_AGENT

constexpr int S = 2048, D = 1024, H = 32, DH = 128, SEG = 32, NSEG = 64;
constexpr int DF = 513;

#define XSW 4
#define BIDX (blockIdx.x ^ XSW)
constexpr int NXC = 4;
constexpr int PQ = 32 / NXC, PO = 64 / NXC;

__shared__ __attribute__((aligned(16))) char g_lds[163840];
#define SPINCAP (1u << 18)
constexpr unsigned kSpinCap = SPINCAP;
constexpr int AUX_SC1 = 16;
constexpr int OOB = 0x7ff00000;
constexpr size_t UNIT = 524288;
constexpr int F_QK = 0, F_VW = 8192, CNT_FFT1 = 16384, CNT_FFT2 = 16512, CNT_ARM = 16640;
constexpr int Q_FFT1 = 16896, Q_FFT2 = 17024, Q_FFT3 = 17152;
constexpr size_t OFF_TMO = 20480;
constexpr size_t OFF_CAL = 24576, OFF_XID = 25088;
constexpr size_t ARM_HOST = 28672;
constexpr int OFF_REQ = 49152, OFF_ACK = 53248, CNT_HS = 17280, CNT_HSO = 17408;
constexpr size_t OFF_SCR = 65536;
constexpr size_t OFF_VM = UNIT;
constexpr int VM_SLOT = 4 << 20, VM_SLOT2 = 8 << 20;
constexpr size_t OFF_DBG = 3 * UNIT;
constexpr size_t OFF_STEP = 27 * UNIT;
constexpr int R_MEM = 0, R_EX = 131072, R_PM = 262144, R_SC = 294912, R_ST = 327680, R_PX = 393216, R_DUM = 475136;
constexpr size_t OFF_QK = (size_t)48 << 20;
constexpr size_t OFF_X1 = (size_t)217 << 20;
#define GST 64
constexpr size_t OFF_VWX = (size_t)(GST == 64 ? 89 : 80) << 20;
constexpr size_t WS_USED = (size_t)217 << 20;
constexpr int NARM = 4;
#define SCL 1
#define SC_POS(q, r) ((q) * 256 + ((r) & 4) * 32 + ((r) & 3) * 16)

__device__ __forceinline__ int olane() {
  int t;
  asm volatile("v_mbcnt_lo_u32_b32 %0, -1, 0\n\tv_mbcnt_hi_u32_b32 %0, -1, %0" : "=v"(t));
  __builtin_assume(t >= 0 && t < 64);
  return t;
}
__device__ __forceinline__ float bf2f(u16 v) { return __uint_as_float((unsigned)v << 16); }
__device__ __forceinline__ unsigned pk2(float a, float b) {
  typedef __bf16 b2 __attribute__((ext_vector_type(2)));
  f32x2 f = {a, b};
  b2 r = __builtin_convertvector(f, b2);
  return __builtin_bit_cast(unsigned, r);
}
__device__ __forceinline__ unsigned pk2s(float a, float b) { return min(pk2(a, b), 0xfffffffeu); }
__device__ __forceinline__ u16 f2bf(float f) { return (u16)(pk2(f, 0.f) & 0xffffu); }
__device__ __forceinline__ float rbf(float f) { return __uint_as_float(pk2(0.f, f)); }
__device__ __forceinline__ float opq_one() {
  float o = 1.f;
  asm("" : "+s"(o));
  return o;
}
__device__ __forceinline__ unsigned fold_pk(f32x4 a, float one) { return pk2(a[0] + a[2], __builtin_fmaf(a[1], one, a[3])); }
__device__ __forceinline__ float mul1(float a, float c) {
  float r;
  asm("v_mul_f32 %0, %2, %1" : "=v"(r) : "v"(a), "s"(c));
  return r;
}
__device__ __forceinline__ float fma1(float a, float c, float b) {
  float r;
  asm("v_fma_f32 %0, %1, %2, %3" : "=v"(r) : "v"(a), "s"(c), "v"(b));
  return r;
}
__device__ __forceinline__ int xreg(int r) { return ((r & 1) << 2) | ((r >> 1) & 1) | ((r >> 1) & 2); }

__device__ __forceinline__ void tmo_report(gu32* tmo, unsigned code) {
  unsigned z = 0, c = code;
  asm volatile("" : "+v"(z), "+v"(c));
  asm volatile("global_store_dword %0, %1, %2 sc1" ::"v"(z), "v"(c), "s"(tmo) : "memory");
}
__device__ __forceinline__ bool spin_fail(unsigned& spins, gu32* tmo, unsigned code) {
  asm volatile("" ::: "memory");
  if (__builtin_expect(++spins > kSpinCap, 0)) {
    tmo_report(tmo, code);
    return true;
  }
  return false;
}
__device__ __forceinline__ unsigned wave_min(unsigned v) {
  v = min(v, (unsigned)__builtin_amdgcn_mov_dpp((int)v, 0x121, 0xf, 0xf, true));
  v = min(v, (unsigned)__builtin_amdgcn_mov_dpp((int)v, 0x122, 0xf, 0xf, true));
  v = min(v, (unsigned)__builtin_amdgcn_mov_dpp((int)v, 0x124, 0xf, 0xf, true));
  v = min(v, (unsigned)__builtin_amdgcn_mov_dpp((int)v, 0x128, 0xf, 0xf, true));
  auto r = __builtin_amdgcn_permlane32_swap(v, v, false, false);
  v = min(r[0], r[1]);
  auto q = __builtin_amdgcn_permlane16_swap(v, v, false, false);
  return min(q[0], q[1]);
}
__device__ __forceinline__ float xmax16_32(float x) {
  auto r = __builtin_amdgcn_permlane32_swap(__float_as_uint(x), __float_as_uint(x), false, false);
  x = __builtin_amdgcn_fmed3f(__uint_as_float(r[0]), __uint_as_float(r[1]), __builtin_inff());
  auto q = __builtin_amdgcn_permlane16_swap(__float_as_uint(x), __float_as_uint(x), false, false);
  return __builtin_amdgcn_fmed3f(__uint_as_float(q[0]), __uint_as_float(q[1]), __builtin_inff());
}
__device__ __forceinline__ float xsum16(float x) {
  auto q = __builtin_amdgcn_permlane16_swap(__float_as_uint(x), __float_as_uint(x), false, false);
  return __uint_as_float(q[0]) + __uint_as_float(q[1]);
}
__device__ __forceinline__ float xsum16_32(float x) {
  auto r = __builtin_amdgcn_permlane32_swap(__float_as_uint(x), __float_as_uint(x), false, false);
  return xsum16(__uint_as_float(r[0]) + __uint_as_float(r[1]));
}
__device__ __forceinline__ unsigned mx4(unsigned m, v4u v) { return max(max(max(max(m, v[0]), v[1]), v[2]), v[3]); }
__device__ __forceinline__ bool anysent(unsigned m) { return __builtin_amdgcn_ballot_w64(m == 0xffffffffu) != 0ull; }

__device__ __forceinline__ unsigned now32() { return (unsigned)__builtin_amdgcn_s_memrealtime(); }
__device__ __forceinline__ unsigned cyc32() { return (unsigned)__builtin_amdgcn_s_memtime(); }
#define CMAX 14000
__device__ __forceinline__ void wait_cyc(unsigned t, int d = 0x7fffffff) {
  t = __builtin_amdgcn_readfirstlane(t);
  if (d == 0x7fffffff) d = (int)(t - cyc32());
  if (d > CMAX) return;
  while (d > 260) {
    __builtin_amdgcn_s_sleep(1);
    d = (int)(t - cyc32());
  }
  d = __builtin_amdgcn_readfirstlane(d - 70);
  if (d > 0) asm volatile("1: s_sub_u32 %0, %0, 28\n\ts_cbranch_scc0 1b" : "+s"(d)::"scc", "memory");
}
#define CLKSPAN 124u
struct Clk {
  unsigned r, m;
  unsigned cq, inv;
};
__device__ __forceinline__ bool clk_edge(unsigned& r, unsigned& m, int tries) {
  constexpr int NS = 14;
#pragma unroll 1
  for (int k = 0; k < tries; ++k) {
    u64 t[NS], m0, m1;
    asm volatile("s_memtime %0" : "=s"(m0));
#pragma unroll
    for (int i = 0; i < NS; ++i) {
      asm volatile("s_memrealtime %0\n\ts_nop 0" : "=s"(t[i]));
      if (i == NS - 2) asm volatile("s_memtime %0" : "=s"(m1));
    }
    asm volatile("s_waitcnt lgkmcnt(0)" : "+s"(m0), "+s"(m1), "+s"(t[0]), "+s"(t[1]), "+s"(t[2]), "+s"(t[3]), "+s"(t[4]), "+s"(t[5]),
                 "+s"(t[6]), "+s"(t[7]), "+s"(t[8]), "+s"(t[9]), "+s"(t[10]), "+s"(t[11]), "+s"(t[12]), "+s"(t[13]));
    unsigned n = 0;
    static_assert(NS == 14, "");
    asm("s_cmp_eq_u32 %2, %1\n\ts_addc_u32 %0, %0, 0\n\t"
        "s_cmp_eq_u32 %3, %1\n\ts_addc_u32 %0, %0, 0\n\t"
        "s_cmp_eq_u32 %4, %1\n\ts_addc_u32 %0, %0, 0\n\t"
        "s_cmp_eq_u32 %5, %1\n\ts_addc_u32 %0, %0, 0\n\t"
        "s_cmp_eq_u32 %6, %1\n\ts_addc_u32 %0, %0, 0\n\t"
        "s_cmp_eq_u32 %7, %1\n\ts_addc_u32 %0, %0, 0\n\t"
        "s_cmp_eq_u32 %8, %1\n\ts_addc_u32 %0, %0, 0\n\t"
        "s_cmp_eq_u32 %9, %1\n\ts_addc_u32 %0, %0, 0\n\t"
        "s_cmp_eq_u32 %10, %1\n\ts_addc_u32 %0, %0, 0\n\t"
        "s_cmp_eq_u32 %11, %1\n\ts_addc_u32 %0, %0, 0\n\t"
        "s_cmp_eq_u32 %12, %1\n\ts_addc_u32 %0, %0, 0\n\t"
        "s_cmp_eq_u32 %13, %1\n\ts_addc_u32 %0, %0, 0\n\t"
        "s_cmp_eq_u32 %14, %1\n\ts_addc_u32 %0, %0, 0\n\t"
        : "+s"(n)
        : "s"((unsigned)t[0]), "s"((unsigned)t[1]), "s"((unsigned)t[2]), "s"((unsigned)t[3]), "s"((unsigned)t[4]), "s"((unsigned)t[5]), "s"((unsigned)t[6]), "s"((unsigned)t[7]), "s"((unsigned)t[8]), "s"((unsigned)t[9]), "s"((unsigned)t[10]), "s"((unsigned)t[11]), "s"((unsigned)t[12]), "s"((unsigned)t[13])
        : "scc");
    r = (unsigned)t[0] + 4;
    m = (unsigned)m0 + n * 8;
    asm volatile("" : "+s"(m));
    if ((unsigned)m1 - (unsigned)m0 <= CLKSPAN) return true;
  }
  return false;
}
__device__ __forceinline__ unsigned sdiv(unsigned a, unsigned b) {
  unsigned r;
  float t, u;
  asm volatile("v_cvt_f32_u32 %1, %4\n\tv_cvt_f32_u32 %2, %3\n\tv_rcp_f32 %1, %1\n\ts_nop 0\n\tv_mul_f32 %1, %1, %2\n\tv_cvt_u32_f32 %1, %1\n\t"
               "s_nop 0\n\tv_readfirstlane_b32 %0, %1\n\ts_nop 3"
               : "=s"(r), "=&v"(t), "=&v"(u)
               : "s"(a), "s"(b));
  return r;
}
__device__ __forceinline__ void clk_init(Clk& c) {
  clk_edge(c.r, c.m, 4);
  c.cq = 92500u;
  c.inv = 46432u;
}
__device__ __forceinline__ void clk_update(Clk& c, bool first = false) {
  unsigned r, m;
  if (!clk_edge(r, m, first ? 2 : 1) && r - c.r < 1500u) return;
  if (r - c.r >= 100u && r - c.r < 40000u) {
    unsigned q = sdiv((m - c.m) << 12, r - c.r);
    asm("s_max_u32 %0, %0, 0x101d0\n\ts_min_u32 %0, %0, 0x1b580" : "+s"(q)::"scc");
    c.cq = first ? q : (3 * c.cq + q) >> 2;
    c.inv = sdiv(0xffffffffu, c.cq);
  }
  c.r = r;
  c.m = m;
}
__device__ __forceinline__ void clk_step(Clk& c) {
  unsigned r, m;
  if (!clk_edge(r, m, 1) && r - c.r < 1500u) return;
  const unsigned dr = r - c.r;
  if (dr - 100u < 400u) {
    unsigned q = c.cq + 5 * (m - c.m - ((dr * c.cq) >> 12));
    asm("s_max_u32 %0, %0, 0x101d0\n\ts_min_u32 %0, %0, 0x1b580" : "+s"(q)::"scc");
    c.cq = q;
    c.inv += (unsigned)__mulhi((int)c.inv, -(int)(q * c.inv));
  }
  c.r = r;
  c.m = m;
}
__device__ __forceinline__ void wave_min2(unsigned& a, unsigned& b) {
  unsigned t, u;
#define M2(c) "v_min_u32_dpp %0, %0, %0 " c " row_mask:0xf bank_mask:0xf\n\tv_min_u32_dpp %1, %1, %1 " c " row_mask:0xf bank_mask:0xf\n\ts_nop 0\n\t"
#define S2(n) "v_mov_b32 %2, %0\n\tv_mov_b32 %3, %1\n\ts_nop 0\n\tv_permlane" n "_swap_b32 %0, %2\n\tv_permlane" n "_swap_b32 %1, %3\n\tv_min_u32 %0, %0, %2\n\tv_min_u32 %1, %1, %3\n\t"
  asm("s_nop 1\n\t" M2("row_ror:1") M2("row_ror:2") M2("row_ror:4") M2("row_ror:8") S2("32") S2("16") "s_nop 0" : "+v"(a), "+v"(b), "=&v"(t), "=&v"(u));
#undef M2
#undef S2
}
__device__ __forceinline__ void clk_fresh(Clk& c) {
  unsigned r, m;
  if (clk_edge(r, m, 1)) {
    c.r = r;
    c.m = m;
  }
}
__device__ __forceinline__ unsigned clk_fine(const Clk& c, unsigned m) {
  return (c.r << 4) + (unsigned)(((u64)(m - c.m) * c.inv) >> 16);
}
__device__ __forceinline__ int stamp_max(const Clk& c, unsigned st) {
  const unsigned tn = (c.r << 4) + 65536u;
  return 65536 - __builtin_amdgcn_readfirstlane((int)wave_min(tn - st));
}
__device__ __forceinline__ unsigned clk_cyc(const Clk& c, int d) { return c.m + (unsigned)(((long long)d * (int)c.cq) >> 16); }
#define TN(k, d) (d)
#define XL0 0
#define XL1 0
#define XL2 0
#define NS2F(ns) (((ns) * 8) / 5)
struct Pace {
  unsigned rf;
  int ed, ea, dq;
};
template <bool RED = true>
__device__ __forceinline__ void pace_step(Pace& p, int s, unsigned rf, unsigned rep, int marg, int tol, int xlate) {
  const int arr = (int)(rf - p.rf) - p.dq;
  const bool late = s == 0 || arr > p.ea + tol;
  if (s > 0) {
    const int md = RED ? (int)~__builtin_amdgcn_readfirstlane(wave_min(~rep)) : (int)rep;
    p.ea = min(arr, p.ea + 5);
    p.ed = (int)min((unsigned)md, (unsigned)p.ed + 5u);
  }
  p.rf = rf;
  p.dq = p.ed + marg + (late ? xlate : 0);
}

template <int SLP>
__device__ __forceinline__ void wait_flag(__amdgpu_buffer_rsrc_t rs, int off, gu32* tmo, unsigned code) {
  _Pragma("clang loop unroll(disable)") for (unsigned spins = 0;;) {
    asm volatile("" : "+v"(off));
    unsigned v = __builtin_amdgcn_raw_buffer_load_b8(rs, off, 0, AUX_SC1);
    if (__all(v == 0u)) break;
    __builtin_amdgcn_s_sleep(SLP);
    if (spin_fail(spins, tmo, code)) break;
  }
}
#define RAW_BARRIER()                                    \
  do {                                                   \
    asm volatile("" ::: "memory");                       \
    __builtin_amdgcn_s_waitcnt(0xc07f);                  \
    __builtin_amdgcn_s_barrier();                        \
    asm volatile("" ::: "memory");                       \
  } while (0)
#define BARE_BARRIER()                 \
  do {                                 \
    asm volatile("" ::: "memory");     \
    __builtin_amdgcn_s_barrier();      \
    asm volatile("" ::: "memory");     \
  } while (0)
#define VMCNT(n) asm volatile("s_waitcnt vmcnt(" #n ")" ::: "memory")
#define SB() __builtin_amdgcn_sched_barrier(0)
#define OPQ(v, e) int v = (e); asm volatile("" : "+v"(v))
template <bool NT = false>
__device__ __forceinline__ void glds16(const void* base, int voff, unsigned dst) {
  unsigned keep;
  if constexpr (NT)
    asm volatile("s_mov_b32 %0, m0\n\ts_mov_b32 m0, %3\n\ts_nop 0\n\tglobal_load_lds_dwordx4 %1, %2 nt\n\ts_mov_b32 m0, %0"
                 : "=&s"(keep)
                 : "v"(voff), "s"(base), "s"(dst)
                 : "memory");
  else
  asm volatile("s_mov_b32 %0, m0\n\ts_mov_b32 m0, %3\n\ts_nop 0\n\tglobal_load_lds_dwordx4 %1, %2\n\ts_mov_b32 m0, %0"
               : "=&s"(keep)
               : "v"(voff), "s"(base), "s"(dst)
               : "memory");
}
__device__ __forceinline__ void touch4(const void* base, int voff, unsigned dst) {
  unsigned keep;
  asm volatile("s_mov_b32 %0, m0\n\ts_mov_b32 m0, %3\n\ts_nop 0\n\tglobal_load_lds_dword %1, %2\n\ts_mov_b32 m0, %0"
               : "=&s"(keep)
               : "v"(voff), "s"(base), "s"(dst)
               : "memory");
}
__device__ __forceinline__ f32x4 smf(v4u a, v8u b, f32x4 c, int idx) {
  return __builtin_amdgcn_smfmac_f32_16x16x64_bf16(__builtin_bit_cast(bf16x8, a), __builtin_bit_cast(bf16x16, b), c, idx, 0, 0);
}
__device__ __forceinline__ f32x4 smf1(v4u a, v8u b, f32x4 c, int idx) {
  return __builtin_amdgcn_smfmac_f32_16x16x64_bf16(__builtin_bit_cast(bf16x8, a), __builtin_bit_cast(bf16x16, b), c, idx, 0, 1);
}
__device__ __forceinline__ f32x4 mma(v4u a, v4u b, f32x4 c) {
  return __builtin_amdgcn_mfma_f32_16x16x32_bf16(__builtin_bit_cast(bf16x8, a), __builtin_bit_cast(bf16x8, b), c, 0, 0, 0);
}
__device__ __forceinline__ void st128(v4u d, __amdgpu_buffer_rsrc_t rs, int voff, int soff) {
  __builtin_amdgcn_raw_buffer_store_b128(d, rs, voff, soff, AUX_SC1);
  asm volatile("s_nop 1" ::"v"(d));
}
__device__ __forceinline__ f32x4 zero4() {
  f32x2 a, b;
  asm volatile("v_mov_b64 %0, 0" : "=v"(a));
  asm volatile("v_mov_b64 %0, 0" : "=v"(b));
  return f32x4{a[0], a[1], b[0], b[1]};
}
__device__ __forceinline__ f32x2 zero2() {
  f32x2 a;
  asm volatile("v_mov_b64 %0, 0" : "=v"(a));
  return a;
}
__device__ __forceinline__ v4u lo4(v8u v) { return v4u{v[0], v[1], v[2], v[3]}; }
__device__ __forceinline__ v4u hi4(v8u v) { return v4u{v[4], v[5], v[6], v[7]}; }
__device__ __forceinline__ v8u cat8(v4u a, v4u b) { return v8u{a[0], a[1], a[2], a[3], b[0], b[1], b[2], b[3]}; }
__device__ __forceinline__ v32u cat32(v8u a, v8u b, v8u c, v8u d) {
  const v16u x = __builtin_shufflevector(a, b, 0, 1, 2, 3, 4, 5, 6, 7, 8, 9, 10, 11, 12, 13, 14, 15);
  const v16u y = __builtin_shufflevector(c, d, 0, 1, 2, 3, 4, 5, 6, 7, 8, 9, 10, 11, 12, 13, 14, 15);
  return __builtin_shufflevector(x, y, 0, 1, 2, 3, 4, 5, 6, 7, 8, 9, 10, 11, 12, 13, 14, 15, 16, 17, 18, 19, 20, 21, 22, 23, 24, 25, 26,
                                 27, 28, 29, 30, 31);
}
__device__ __forceinline__ v2u trd(LAS char* p) {
  return __builtin_bit_cast(v2u, __builtin_amdgcn_ds_read_tr16_b64_v4i16((LAS s16x4*)p));
}
__device__ __forceinline__ int vm_slot(int s) { return (s & 1) * VM_SLOT + ((s >> 1) & 1) * VM_SLOT2; }
__device__ __forceinline__ int cls_of(unsigned perm, int x) { return (int)((perm >> (3 * x)) & 7u); }

__device__ __forceinline__ void arm_wait(__amdgpu_buffer_rsrc_t rs, gu32* tmo) {
  _Pragma("clang loop unroll(disable)") for (unsigned spins = 0;;) {
    OPQ(off, CNT_ARM);
    if (__all(__builtin_amdgcn_raw_buffer_load_b32(rs, off, 0, AUX_SC1) + 1u >= 64u)) break;
    __builtin_amdgcn_s_sleep(8);
    if (spin_fail(spins, tmo, 0x30007)) break;
  }
}
__device__ __forceinline__ v2u numa_load(__amdgpu_buffer_rsrc_t rs, gu32* tmo) {
  unsigned c, xid;
  _Pragma("clang loop unroll(disable)") for (unsigned spins = 0;;) {
    OPQ(a, olane() * 4);
    c = __builtin_amdgcn_raw_buffer_load_b32(rs, a, (int)OFF_CAL, AUX_SC1);
    xid = __builtin_amdgcn_raw_buffer_load_b32(rs, ((a >> 5) & (NXC - 1)) * 4, (int)OFF_XID, AUX_SC1);
    if (__all((c != 0xffffffffu) & (xid != 0xffffffffu))) break;
    __builtin_amdgcn_s_sleep(16);
    if (spin_fail(spins, tmo, 0x30008)) break;
  }
  return v2u{c, xid};
}
__device__ __forceinline__ unsigned numa_perm(v2u in, LAS unsigned* tab) {
  const int lane = olane();
  const unsigned c = in[0], xid = in[1];
  tab[lane] = 0xffffu;
  tab[64 + lane] = 0xffffu;
  tab[128 + lane] = 0u;
  __builtin_amdgcn_s_waitcnt(0xc07f);
  __hip_atomic_fetch_min(tab + ((c >> 16) & 127u), c & 0xffffu, __ATOMIC_RELAXED, __HIP_MEMORY_SCOPE_WORKGROUP);
  __hip_atomic_fetch_add(tab + 128 + ((c >> 21) & 3u) * 2 + ((c >> 17) & 1u), c & 0xffffu, __ATOMIC_RELAXED, __HIP_MEMORY_SCOPE_WORKGROUP);
  __builtin_amdgcn_s_waitcnt(0xc07f);
  asm volatile("" ::: "memory");
  const unsigned s0 = tab[128 + ((xid >> 2) & 3u) * 2], s1 = tab[129 + ((xid >> 2) & 3u) * 2];
  const bool far = ((lane >> 1) & 1) != (s1 < s0 ? 1 : 0);
  unsigned key = ((tab[(xid & 15u) * 8 + (lane & 7)] + (far ? 0x10000u : 0u)) << 6) | (unsigned)lane;
  if ((lane >> 3) >= NXC) key = 0xffffffffu;
  unsigned perm = 0;
#pragma unroll 1
  for (int r = 0; r < NXC; ++r) {
    const unsigned m = wave_min(key);
    const unsigned bx = (m >> 3) & 7u, bc = m & 7u;
    perm |= bc << (3 * bx);
    if ((unsigned)(lane >> 3) == bx || (unsigned)(lane & 7) == bc) key = 0xffffffffu;
  }
  return __builtin_amdgcn_readfirstlane(perm | (s1 < s0 ? 0u : 8192u));
}
__device__ __forceinline__ int numa_far(v2u in) {
  const unsigned c = in[0], x0 = __builtin_amdgcn_readfirstlane(in[1]);
  int d = (int)(c & 0xffffu);
  d = ((c >> 21) & 3u) == ((x0 >> 2) & 3u) ? ((c & 0x20000u) ? -d : d) : 0;
  d += __builtin_amdgcn_mov_dpp(d, 0x121, 0xf, 0xf, true);
  d += __builtin_amdgcn_mov_dpp(d, 0x122, 0xf, 0xf, true);
  d += __builtin_amdgcn_mov_dpp(d, 0x124, 0xf, 0xf, true);
  d += __builtin_amdgcn_mov_dpp(d, 0x128, 0xf, 0xf, true);
  d = __builtin_amdgcn_readlane(d, 0) + __builtin_amdgcn_readlane(d, 16) + __builtin_amdgcn_readlane(d, 32) + __builtin_amdgcn_readlane(d, 48);
  return d > 0 ? 0 : 8192;
}
#define VRING 64
#define QRING 64
#define VFAR 0
__device__ __forceinline__ int vw_addr(int fr, int tile, int seg, int gq) {
  return (int)OFF_VWX + ((((tile >> 3) * GST + (seg & (VRING - 1))) * 8 + (tile & 7)) * 8 + gq) * 4096;
}
__device__ __forceinline__ int fqk(int h, int seg) { return F_QK + h * 256 + seg * 4; }
__device__ __forceinline__ int fvw(int pr, int h, int seg) { return F_VW + ((pr * 4 + (h >> 3)) * 2 + (seg & 1)) * 256 + (seg >> 1) * 8 + (h & 7); }
constexpr int QK_RT = 4096;
__device__ __forceinline__ int qk_addr(int mat, int seg, int h) {
  return (int)OFF_QK + ((mat * 64 + (seg & (QRING - 1))) * 32 + h) * 8192;
}

__device__ __forceinline__ u64 hs_post(__amdgpu_buffer_rsrc_t rs, int w, int HSW) {
  const u64 n = __builtin_amdgcn_s_memrealtime();
  if (w == HSW) __builtin_amdgcn_raw_buffer_store_b64(v2u{(unsigned)n, (unsigned)(n >> 32)}, rs, olane() == 0 ? 0 : OOB,
                                                    OFF_REQ + (int)BIDX * 8, AUX_SC1);
  return n;
}
__device__ __forceinline__ bool hs_wait(__amdgpu_buffer_rsrc_t rs, u64 n, v2u v) {
  unsigned spins = 0;
  _Pragma("clang loop unroll(disable)") for (; spins < (kSpinCap << 2); ++spins) {
    if (__builtin_expect(__all(v[0] == (unsigned)n && v[1] == (unsigned)(n >> 32)), 1)) break;
    if (spins) __builtin_amdgcn_s_sleep(8);
    int z = 0;
    asm volatile("" : "+v"(z));
    v = __builtin_amdgcn_raw_buffer_load_b64(rs, z, OFF_ACK + (int)BIDX * 8, AUX_SC1);
  }
  return spins == 0;
}
__device__ __forceinline__ void cnt_inc(char* ws, int cnt) {
  if (olane() == 0) __hip_atomic_fetch_add((gu32*)(ws + cnt), 1u, RLX_AGENT);
}
template <int SLP>
__device__ __forceinline__ void hs_lead(__amdgpu_buffer_rsrc_t rs, bool fill, int cnt, unsigned want) {
  const int l16 = olane() * 16;
  if (fill) {
    for (int o = 0; o < (int)ARM_HOST; o += 1024) __builtin_amdgcn_raw_buffer_store_b128(v4u{~0u, ~0u, ~0u, ~0u}, rs, l16, o, AUX_SC1);
    VMCNT(0);
  }
  _Pragma("clang loop unroll(disable)") for (unsigned spins = 0; spins < (kSpinCap << 2); ++spins) {
    int va = l16;
    asm volatile("" : "+v"(va));
    const v4u a = __builtin_amdgcn_raw_buffer_load_b128(rs, va, OFF_REQ, AUX_SC1);
    const v4u b = __builtin_amdgcn_raw_buffer_load_b128(rs, va + 1024, OFF_REQ, AUX_SC1);
    const unsigned n = __builtin_amdgcn_raw_buffer_load_b32(rs, va & 0, cnt, AUX_SC1);
    if (__all(n + 1u >= want)) break;
    __builtin_amdgcn_raw_buffer_store_b128(a, rs, va, OFF_ACK, AUX_SC1);
    __builtin_amdgcn_raw_buffer_store_b128(b, rs, va + 1024, OFF_ACK, AUX_SC1);
    __builtin_amdgcn_s_sleep(SLP);
  }
}

#define PTW(i, wv)
#define PLDECL
#define PL(i)
#define PLDUMP(wv, mask)
#define PT(i) PTW(i, 0)
#define EVDECL
#define EV(k)
#define EVDUMP(s)
#define EVCLK(s, c)
#define EVOUT
#define VO_A \
  "s_branch .Lgo_%=\n" \
  ".Lrs_%=:\n" \
  "s_mov_b32 %[fs], -1\n" \
  "s_nop 15\n" \
  "s_nop 15\n" \
  ".Lrt_%=:\n" \
  "s_setprio 0\n" \
  "s_waitcnt vmcnt(0) lgkmcnt(0)\n" \
  "ds_read_b128 v[200:203], v243 offset:32768\n" \
  "ds_read_b128 v[204:207], v243 offset:33792\n" \
  "ds_read_b128 v[208:211], v243 offset:49152\n" \
  "ds_read_b128 v[212:215], v243 offset:50176\n" \
  "v_mov_b64 v[192:193], 0\n" \
  "v_mov_b64 v[194:195], 0\n" \
  "v_mov_b64 v[196:197], 0\n" \
  "v_mov_b64 v[198:199], 0\n" \
  "s_barrier\n" \
  "ds_read_b128 v[216:219], v251 offset:0\n" \
  "ds_read_b128 v[220:223], v251 offset:1024\n" \
  "ds_read_b128 v[224:227], v251 offset:2048\n" \
  "ds_read_b128 v[228:231], v251 offset:3072\n" \
  "s_waitcnt lgkmcnt(0)\n" \
  "s_barrier\n" \
  "s_branch .Ll1_%=\n" \
  ".Lgo_%=:\n" \
  "ds_read_b128 v[200:203], v243 offset:32768\n" \
  "ds_read_b128 v[204:207], v243 offset:33792\n" \
  "ds_read_b128 v[208:211], v243 offset:49152\n" \
  "ds_read_b128 v[212:215], v243 offset:50176\n" \
  "v_mov_b64 v[192:193], 0\n" \
  "v_mov_b64 v[194:195], 0\n" \
  "v_mov_b64 v[196:197], 0\n" \
  "v_mov_b64 v[198:199], 0\n" \
  "s_mov_b32 %[keep], m0\n" \
  "v_readfirstlane_b32 %[cnt], v250\n" \
  "s_mov_b32 m0, %[cnt]\n" \
  "s_waitcnt lgkmcnt(0)\n" \
  "s_barrier\n" \
  "buffer_load_dwordx4 v251, %[rs], %[o0] offen sc1 lds\n" \
  "s_waitcnt vmcnt(0)\n" \
  "s_barrier\n" \
  "s_mov_b32 m0, %[keep]\n" \
  "ds_read_b128 v[216:219], v251 offset:0\n" \
  "ds_read_b128 v[220:223], v251 offset:1024\n" \
  "ds_read_b128 v[224:227], v251 offset:2048\n" \
  "ds_read_b128 v[228:231], v251 offset:3072\n" \
  "s_barrier\n" \
  ".Ll1_%=:\n" \
  "s_waitcnt lgkmcnt(3)\n" \
  "v_smfmac_f32_16x16x64_bf16 v[192:195], v[216:219], v[200:207], v248\n" \
  "v_smfmac_f32_16x16x64_bf16 v[196:199], v[216:219], v[208:215], v248\n" \
  "ds_read_b128 v[232:235], v251 offset:4096\n" \
  "ds_read_b128 v[236:239], v251 offset:5120\n" \
  "ds_read_b128 v[216:219], v251 offset:6144\n" \
  "s_waitcnt lgkmcnt(5)\n" \
  "v_smfmac_f32_16x16x64_bf16 v[192:195], v[220:223], v[0:7], v248\n" \
  "v_smfmac_f32_16x16x64_bf16 v[196:199], v[220:223], v[8:15], v248\n" \
  "ds_read_b128 v[220:223], v251 offset:7168\n" \
  "s_barrier\n" \
  "s_waitcnt lgkmcnt(5)\n" \
  "v_smfmac_f32_16x16x64_bf16 v[192:195], v[224:227], v[16:23], v248\n" \
  "v_smfmac_f32_16x16x64_bf16 v[196:199], v[224:227], v[24:31], v248\n" \
  "s_waitcnt lgkmcnt(4)\n" \
  "v_smfmac_f32_16x16x64_bf16 v[192:195], v[228:231], v[32:39], v248\n" \
  "v_smfmac_f32_16x16x64_bf16 v[196:199], v[228:231], v[40:47], v248\n" \
  "s_waitcnt lgkmcnt(3)\n" \
  "v_smfmac_f32_16x16x64_bf16 v[192:195], v[232:235], v[48:55], v248\n" \
  "v_smfmac_f32_16x16x64_bf16 v[196:199], v[232:235], v[56:63], v248\n" \
  "s_waitcnt lgkmcnt(2)\n" \
  "v_smfmac_f32_16x16x64_bf16 v[192:195], v[236:239], v[64:71], v248\n" \
  "v_smfmac_f32_16x16x64_bf16 v[196:199], v[236:239], v[72:79], v248\n" \
  "s_waitcnt lgkmcnt(1)\n" \
  "v_smfmac_f32_16x16x64_bf16 v[192:195], v[216:219], v[80:87], v248\n" \
  "s_waitcnt lgkmcnt(0)\n" \
  "v_smfmac_f32_16x16x64_bf16 v[192:195], v[220:223], v[96:103], v248\n" \
  "v_smfmac_f32_16x16x64_bf16 v[196:199], v[216:219], v[88:95], v248\n" \
  "v_smfmac_f32_16x16x64_bf16 v[196:199], v[220:223], v[104:111], v248\n" \
  "s_setprio 3\n" \
  "v_add_f32 v192, v192, v194\n" \
  "v_add_f32 v193, v193, v195\n" \
  "s_nop 5\n" \
  "v_add_f32 v194, v196, v198\n" \
  "v_add_f32 v195, v197, v199\n" \
  "ds_write_b128 v250, v[192:195] offset:24576\n" \
  "v_mov_b64 v[200:201], 0\n" \
  "v_mov_b64 v[202:203], 0\n" \
  "v_mov_b64 v[204:205], 0\n" \
  "v_mov_b64 v[206:207], 0\n" \
  "v_mov_b64 v[208:209], 0\n" \
  "v_mov_b64 v[210:211], 0\n" \
  "v_mov_b64 v[212:213], 0\n" \
  "v_mov_b64 v[214:215], 0\n" \
  "s_waitcnt lgkmcnt(0)\n" \
  "s_setprio 0\n" \
  "s_barrier\n" \
  "s_nop 11\n" \
  "ds_read_b32 v241, v251 offset:23564\n" \
  "ds_read_b128 v[216:219], v249 offset:0\n" \
  "ds_read_b128 v[220:223], v249 offset:1024\n" \
  "ds_read_b128 v[224:227], v249 offset:16384\n" \
  "ds_read_b128 v[228:231], v249 offset:17408\n" \
  "s_waitcnt lgkmcnt(4)\n" \
  "v_cmp_eq_u32 vcc, %[fs], v241\n" \
  "s_cbranch_vccnz .Lrs_%=\n" \
  "s_barrier\n" \
  "ds_read_b64_tr_b16 v[232:233], v245 offset:17920\n" \
  "ds_read_b64_tr_b16 v[234:235], v245 offset:18112\n" \
  "ds_read_b64_tr_b16 v[236:237], v245 offset:18688\n" \
  "ds_read_b64_tr_b16 v[238:239], v245 offset:18880\n" \
  "ds_read_b64_tr_b16 v[192:193], v245 offset:16384\n" \
  "ds_read_b64_tr_b16 v[194:195], v245 offset:16576\n" \
  "ds_read_b64_tr_b16 v[196:197], v245 offset:17152\n" \
  "ds_read_b64_tr_b16 v[198:199], v245 offset:17344\n" \
  "ds_read_b32 v241, v251 offset:23560\n" \
  "s_waitcnt lgkmcnt(7)\n" \
  "v_smfmac_f32_16x16x64_bf16 v[200:203], v[232:235], v[216:223], v248 abid:1\n" \
  "ds_read_b128 v[216:219], v249 offset:32768\n" \
  "ds_read_b128 v[220:223], v249 offset:33792\n" \
  "v_smfmac_f32_16x16x64_bf16 v[204:207], v[232:235], v[128:135], v248 abid:1\n" \
  "s_waitcnt lgkmcnt(7)\n" \
  "v_smfmac_f32_16x16x64_bf16 v[200:203], v[236:239], v[224:231], v248 abid:1\n" \
  "ds_read_b128 v[224:227], v249 offset:49152\n" \
  "ds_read_b128 v[228:231], v249 offset:50176\n" \
  "v_smfmac_f32_16x16x64_bf16 v[204:207], v[236:239], v[136:143], v248 abid:1\n" \
  "s_waitcnt lgkmcnt(7)\n" \
  "v_smfmac_f32_16x16x64_bf16 v[200:203], v[192:195], v[176:183], v248 abid:1\n" \
  "v_smfmac_f32_16x16x64_bf16 v[204:207], v[192:195], v[112:119], v248 abid:1\n" \
  "s_waitcnt lgkmcnt(5)\n" \
  "v_smfmac_f32_16x16x64_bf16 v[200:203], v[196:199], v[184:191], v248 abid:1\n" \
  "s_waitcnt lgkmcnt(4)\n" \
  "v_cmp_eq_u32 vcc, %[fs], v241\n" \
  "v_smfmac_f32_16x16x64_bf16 v[204:207], v[196:199], v[120:127], v248 abid:1\n" \
  "s_waitcnt lgkmcnt(2)\n" \
  "v_smfmac_f32_16x16x64_bf16 v[208:211], v[192:195], v[216:223], v248 abid:1\n" \
  "ds_read_b128 v[216:219], v243 offset:0\n" \
  "ds_read_b128 v[220:223], v243 offset:1024\n" \
  "v_smfmac_f32_16x16x64_bf16 v[212:215], v[192:195], v[144:151], v248 abid:1\n" \
  "s_waitcnt lgkmcnt(2)\n" \
  "v_smfmac_f32_16x16x64_bf16 v[208:211], v[196:199], v[224:231], v248 abid:1\n" \
  "ds_read_b128 v[224:227], v243 offset:16384\n" \
  "ds_read_b128 v[228:231], v243 offset:17408\n" \
  "v_cvt_pk_bf16_f32 v200, v200, v201\n" \
  "v_smfmac_f32_16x16x64_bf16 v[212:215], v[196:199], v[152:159], v248 abid:1\n" \
  "v_cvt_pk_bf16_f32 v201, v202, v203\n" \
  "v_smfmac_f32_16x16x64_bf16 v[212:215], v[232:235], v[160:167], v248 abid:1\n" \
  "v_cvt_pk_bf16_f32 v202, v204, v205\n" \
  "v_smfmac_f32_16x16x64_bf16 v[212:215], v[236:239], v[168:175], v248 abid:1\n" \
  "v_cvt_pk_bf16_f32 v203, v206, v207\n" \
  "s_cbranch_vccnz .Lrs_%=\n" \
  "s_waitcnt lgkmcnt(2)\n" \
  "v_smfmac_f32_16x16x64_bf16 v[208:211], v[232:235], v[216:223], v248 abid:1\n" \
  "v_permlane16_swap_b32 v200, v202\n" \
  "s_waitcnt lgkmcnt(0)\n" \
  "v_smfmac_f32_16x16x64_bf16 v[208:211], v[236:239], v[224:231], v248 abid:1\n" \
  "v_permlane16_swap_b32 v201, v203\n" \
  "buffer_store_dwordx4 v[200:203], v247, %[rs], %[vs] offen sc1\n" \
  "v_cvt_pk_bf16_f32 v212, v212, v213\n" \
  "v_cvt_pk_bf16_f32 v213, v214, v215\n" \
  "s_nop 4\n" \
  "v_cvt_pk_bf16_f32 v211, v210, v211\n" \
  "v_cvt_pk_bf16_f32 v210, v208, v209\n" \
  "s_nop 0\n" \
  "v_permlane16_swap_b32 v211, v213\n" \
  "v_permlane16_swap_b32 v210, v212\n" \
  "buffer_store_dwordx4 v[210:213], v246, %[rs], %[vs] offen sc1\n" \
  "s_nop 1\n"
#define VO_B \
  "s_branch .Lgo_%=\n" \
  ".Lsf_%=:\n" \
  "s_cmp_eq_u32 %[fs], -1\n" \
  "s_cbranch_scc1 .Lsfb_%=\n" \
  "v_mov_b32 v241, %[fs]\n" \
  "ds_write_b32 v251, v241 offset:23564\n" \
  "s_branch .Lrt_%=\n" \
  ".Lrs_%=:\n" \
  "s_mov_b32 %[fs], -1\n" \
  "s_nop 15\n" \
  "s_nop 15\n" \
  ".Lrt_%=:\n" \
  "s_setprio 0\n" \
  "s_waitcnt vmcnt(0) lgkmcnt(0)\n" \
  "buffer_load_dwordx4 v[216:219], v251, %[rs], %[o3] offen sc1\n" \
  "buffer_load_dwordx4 v[220:223], v251, %[rs], %[o0] offen sc1\n" \
  "buffer_load_dwordx4 v[224:227], v251, %[rs], %[o1] offen sc1\n" \
  "buffer_load_dwordx4 v[228:231], v251, %[rs], %[o2] offen sc1\n" \
  "v_subrev_u32 v232, 0x1000, v250\n" \
  "s_mov_b32 %[cnt], -2\n" \
  "ds_read_b128 v[200:203], v232 offset:0\n" \
  "ds_read_b128 v[204:207], v232 offset:4096\n" \
  "ds_read_b128 v[208:211], v232 offset:8192\n" \
  "ds_read_b128 v[212:215], v232 offset:12288\n" \
  "s_waitcnt lgkmcnt(0)\n" \
  "v_max_u32 v241, v200, v201\n" \
  "v_max3_u32 v241, v241, v202, v203\n" \
  "v_max3_u32 v241, v241, v204, v205\n" \
  "v_max3_u32 v241, v241, v206, v207\n" \
  "v_max3_u32 v241, v241, v208, v209\n" \
  "v_max3_u32 v241, v241, v210, v211\n" \
  "v_max3_u32 v241, v241, v212, v213\n" \
  "v_max3_u32 v241, v241, v214, v215\n" \
  "v_cmp_eq_u32 vcc, -1, v241\n" \
  "s_cbranch_vccz .Lrd_%=\n" \
  "s_mov_b32 %[cnt], 0x40000\n" \
  ".Lrr_%=:\n" \
  "s_waitcnt vmcnt(0)\n" \
  "v_max_u32 v241, v216, v217\n" \
  "v_max3_u32 v241, v241, v218, v219\n" \
  "v_max3_u32 v241, v241, v220, v221\n" \
  "v_max3_u32 v241, v241, v222, v223\n" \
  "v_max3_u32 v241, v241, v224, v225\n" \
  "v_max3_u32 v241, v241, v226, v227\n" \
  "v_max3_u32 v241, v241, v228, v229\n" \
  "v_max3_u32 v241, v241, v230, v231\n" \
  "v_cmp_eq_u32 vcc, -1, v241\n" \
  "s_cbranch_vccz .Lrd_%=\n" \
  "s_sleep 2\n" \
  "buffer_load_dwordx4 v[216:219], v251, %[rs], %[o3] offen sc1\n" \
  "buffer_load_dwordx4 v[220:223], v251, %[rs], %[o0] offen sc1\n" \
  "buffer_load_dwordx4 v[224:227], v251, %[rs], %[o1] offen sc1\n" \
  "buffer_load_dwordx4 v[228:231], v251, %[rs], %[o2] offen sc1\n" \
  "s_sub_u32 %[cnt], %[cnt], 1\n" \
  "s_cbranch_scc0 .Lrr_%=\n" \
  ".Lrd_%=:\n" \
  "ds_read_b128 v[200:203], v243 offset:32768\n" \
  "ds_read_b128 v[204:207], v243 offset:33792\n" \
  "ds_read_b128 v[208:211], v243 offset:49152\n" \
  "ds_read_b128 v[212:215], v243 offset:50176\n" \
  "v_mov_b64 v[192:193], 0\n" \
  "v_mov_b64 v[194:195], 0\n" \
  "v_mov_b64 v[196:197], 0\n" \
  "v_mov_b64 v[198:199], 0\n" \
  "s_cmp_eq_u32 %[fs], -1\n" \
  "s_cbranch_scc1 .Lre_%=\n" \
  "s_mov_b32 %[fs], -1\n" \
  "s_barrier\n" \
  ".Lre_%=:\n" \
  "s_waitcnt vmcnt(0)\n" \
  "s_cmp_eq_u32 %[cnt], -2\n" \
  "s_cbranch_scc1 .Lrf_%=\n" \
  "ds_write_b128 v232, v[216:219] offset:0\n" \
  "ds_write_b128 v232, v[220:223] offset:4096\n" \
  "ds_write_b128 v232, v[224:227] offset:8192\n" \
  "ds_write_b128 v232, v[228:231] offset:12288\n" \
  ".Lrf_%=:\n" \
  "v_mov_b32 v241, 0\n" \
  "s_waitcnt lgkmcnt(0)\n" \
  "s_barrier\n" \
  "s_waitcnt lgkmcnt(0)\n" \
  "s_barrier\n" \
  "s_branch .Ll1_%=\n" \
  ".Lnf_%=:\n" \
  "s_cmp_eq_u32 %[fs], -1\n" \
  "s_cbranch_scc1 .Lnfb_%=\n" \
  "v_mov_b32 v241, 0\n" \
  "ds_read_b128 v[216:219], v251 offset:0\n" \
  "s_waitcnt lgkmcnt(0)\n" \
  "v_max3_u32 v241, v241, v216, v217\n" \
  "v_max3_u32 v241, v241, v218, v219\n" \
  "ds_read_b128 v[216:219], v251 offset:1024\n" \
  "s_waitcnt lgkmcnt(0)\n" \
  "v_max3_u32 v241, v241, v216, v217\n" \
  "v_max3_u32 v241, v241, v218, v219\n" \
  "ds_read_b128 v[216:219], v251 offset:2048\n" \
  "s_waitcnt lgkmcnt(0)\n" \
  "v_max3_u32 v241, v241, v216, v217\n" \
  "v_max3_u32 v241, v241, v218, v219\n" \
  "ds_read_b128 v[216:219], v251 offset:3072\n" \
  "s_waitcnt lgkmcnt(0)\n" \
  "v_max3_u32 v241, v241, v216, v217\n" \
  "v_max3_u32 v241, v241, v218, v219\n" \
  "ds_read_b128 v[216:219], v251 offset:4096\n" \
  "s_waitcnt lgkmcnt(0)\n" \
  "v_max3_u32 v241, v241, v216, v217\n" \
  "v_max3_u32 v241, v241, v218, v219\n" \
  "ds_read_b128 v[216:219], v251 offset:5120\n" \
  "s_waitcnt lgkmcnt(0)\n" \
  "v_max3_u32 v241, v241, v216, v217\n" \
  "v_max3_u32 v241, v241, v218, v219\n" \
  "ds_read_b128 v[216:219], v251 offset:6144\n" \
  "s_waitcnt lgkmcnt(0)\n" \
  "v_max3_u32 v241, v241, v216, v217\n" \
  "v_max3_u32 v241, v241, v218, v219\n" \
  "ds_read_b128 v[216:219], v251 offset:7168\n" \
  "s_waitcnt lgkmcnt(0)\n" \
  "v_max3_u32 v241, v241, v216, v217\n" \
  "v_max3_u32 v241, v241, v218, v219\n" \
  "ds_read_b128 v[216:219], v251 offset:8192\n" \
  "s_waitcnt lgkmcnt(0)\n" \
  "v_max3_u32 v241, v241, v216, v217\n" \
  "v_max3_u32 v241, v241, v218, v219\n" \
  "ds_read_b128 v[216:219], v251 offset:9216\n" \
  "s_waitcnt lgkmcnt(0)\n" \
  "v_max3_u32 v241, v241, v216, v217\n" \
  "v_max3_u32 v241, v241, v218, v219\n" \
  "ds_read_b128 v[216:219], v251 offset:10240\n" \
  "s_waitcnt lgkmcnt(0)\n" \
  "v_max3_u32 v241, v241, v216, v217\n" \
  "v_max3_u32 v241, v241, v218, v219\n" \
  "ds_read_b128 v[216:219], v251 offset:11264\n" \
  "s_waitcnt lgkmcnt(0)\n" \
  "v_max3_u32 v241, v241, v216, v217\n" \
  "v_max3_u32 v241, v241, v218, v219\n" \
  "ds_read_b128 v[216:219], v251 offset:12288\n" \
  "s_waitcnt lgkmcnt(0)\n" \
  "v_max3_u32 v241, v241, v216, v217\n" \
  "v_max3_u32 v241, v241, v218, v219\n" \
  "ds_read_b128 v[216:219], v251 offset:13312\n" \
  "s_waitcnt lgkmcnt(0)\n" \
  "v_max3_u32 v241, v241, v216, v217\n" \
  "v_max3_u32 v241, v241, v218, v219\n" \
  "ds_read_b128 v[216:219], v251 offset:14336\n" \
  "s_waitcnt lgkmcnt(0)\n" \
  "v_max3_u32 v241, v241, v216, v217\n" \
  "v_max3_u32 v241, v241, v218, v219\n" \
  "ds_read_b128 v[216:219], v251 offset:15360\n" \
  "s_waitcnt lgkmcnt(0)\n" \
  "v_max3_u32 v241, v241, v216, v217\n" \
  "v_max3_u32 v241, v241, v218, v219\n" \
  "v_cmp_eq_u32 vcc, -1, v241\n" \
  "s_cbranch_vccz .Lnfb_%=\n" \
  "v_mov_b32 v241, %[fs]\n" \
  "ds_write_b32 v251, v241 offset:23560\n" \
  "s_waitcnt lgkmcnt(0)\n" \
  "s_mov_b32 %[fs], -1\n" \
  "s_barrier\n" \
  "s_branch .Lrt_%=\n" \
  ".Lgo_%=:\n" \
  "ds_read_b128 v[200:203], v243 offset:32768\n" \
  "ds_read_b128 v[204:207], v243 offset:33792\n" \
  "ds_read_b128 v[208:211], v243 offset:49152\n" \
  "ds_read_b128 v[212:215], v243 offset:50176\n" \
  "v_mov_b64 v[192:193], 0\n" \
  "v_mov_b64 v[194:195], 0\n" \
  "v_mov_b64 v[196:197], 0\n" \
  "v_mov_b64 v[198:199], 0\n" \
  "s_mov_b32 %[keep], m0\n" \
  "v_readfirstlane_b32 %[cnt], v250\n" \
  "s_mov_b32 m0, %[cnt]\n" \
  "v_bfe_u32 v241, v251, 2, 7\n" \
  "v_lshl_add_u32 v241, v251, 4, v241\n" \
  "s_waitcnt lgkmcnt(0)\n" \
  "s_cmp_eq_u32 %[tw], 0\n" \
  "s_cbranch_scc1 .Ltq_%=\n" \
  ".Ltl_%=:\n" \
  "s_memtime vcc\n" \
  "s_waitcnt lgkmcnt(0)\n" \
  "s_sub_i32 %[cnt], %[tg], vcc_lo\n" \
  "s_cmp_lt_i32 %[cnt], 260\n" \
  "s_cbranch_scc1 .Ltf_%=\n" \
  "s_sleep 1\n" \
  "s_branch .Ltl_%=\n" \
  ".Ltf_%=:\n" \
  "s_max_i32 %[cnt], %[cnt], 0\n" \
  ".Ltg_%=:\n" \
  "s_sub_u32 %[cnt], %[cnt], 28\n" \
  "s_cbranch_scc0 .Ltg_%=\n" \
  ".Ltq_%=:\n" \
  "s_barrier\n" \
  "buffer_load_dwordx4 v251, %[rs], %[o0] offen sc1 lds\n" \
  "s_add_u32 m0, m0, 0x1000\n" \
  "s_nop 4\n" \
  "buffer_load_dwordx4 v251, %[rs], %[o1] offen sc1 lds\n" \
  "s_add_u32 m0, m0, 0x1000\n" \
  "s_nop 4\n" \
  "buffer_load_dwordx4 v251, %[rs], %[o2] offen sc1 lds\n" \
  "s_barrier\n" \
  "s_waitcnt vmcnt(2)\n" \
  "s_barrier\n" \
  "s_mov_b32 m0, %[keep]\n" \
  ".Ll1_%=:\n" \
  "s_memtime vcc\n" \
  "s_setprio 3\n" \
  "s_waitcnt vmcnt(0)\n" \
  "s_waitcnt lgkmcnt(0)\n" \
  "s_barrier\n" \
  "ds_read2_b32 v[236:237], v241 offset1:32\n" \
  "ds_read_b128 v[216:219], v251 offset:8192\n" \
  "ds_read_b128 v[220:223], v251 offset:9216\n" \
  "ds_read_b128 v[224:227], v251 offset:10240\n" \
  "ds_read_b128 v[228:231], v251 offset:11264\n" \
  "ds_read_b128 v[232:235], v251 offset:12288\n" \
  "s_sub_u32 %[cnt], vcc_lo, %[ckm]\n" \
  "s_mul_hi_u32 %[cnt], %[cnt], %[inv]\n" \
  "s_add_u32 %[cnt], %[cnt], %[r16]\n" \
  "s_and_b32 %[cnt], %[cnt], -2\n" \
  "s_waitcnt lgkmcnt(5)\n" \
  "v_max_u32 v241, v236, v237\n" \
  "ds_read_b128 v[236:239], v251 offset:13312\n" \
  "v_cmp_eq_u32 vcc, -1, v241\n" \
  "s_cbranch_vccnz .Lsf_%=\n" \
  ".Lsfb_%=:\n" \
  "v_mov_b32 v241, %[cnt]\n" \
  "buffer_store_dword v241, v242, %[rs], %[sst] offen sc1\n" \
  "s_setprio 0\n" \
  "s_waitcnt lgkmcnt(5)\n" \
  "v_smfmac_f32_16x16x64_bf16 v[192:195], v[216:219], v[200:207], v248\n" \
  "v_smfmac_f32_16x16x64_bf16 v[196:199], v[216:219], v[208:215], v248\n" \
  "s_waitcnt lgkmcnt(4)\n" \
  "v_smfmac_f32_16x16x64_bf16 v[192:195], v[220:223], v[0:7], v248\n" \
  "v_smfmac_f32_16x16x64_bf16 v[196:199], v[220:223], v[8:15], v248\n" \
  "ds_read_b128 v[216:219], v251 offset:14336\n" \
  "ds_read_b128 v[220:223], v251 offset:15360\n" \
  "s_waitcnt lgkmcnt(5)\n" \
  "v_smfmac_f32_16x16x64_bf16 v[192:195], v[224:227], v[16:23], v248\n" \
  "v_smfmac_f32_16x16x64_bf16 v[196:199], v[224:227], v[24:31], v248\n" \
  "s_waitcnt lgkmcnt(4)\n" \
  "v_smfmac_f32_16x16x64_bf16 v[192:195], v[228:231], v[32:39], v248\n" \
  "v_smfmac_f32_16x16x64_bf16 v[196:199], v[228:231], v[40:47], v248\n" \
  "s_waitcnt lgkmcnt(3)\n" \
  "v_smfmac_f32_16x16x64_bf16 v[192:195], v[232:235], v[48:55], v248\n" \
  "v_smfmac_f32_16x16x64_bf16 v[196:199], v[232:235], v[56:63], v248\n" \
  "s_waitcnt lgkmcnt(2)\n" \
  "v_smfmac_f32_16x16x64_bf16 v[192:195], v[236:239], v[64:71], v248\n" \
  "v_smfmac_f32_16x16x64_bf16 v[196:199], v[236:239], v[72:79], v248\n" \
  "s_barrier\n" \
  "ds_read_b128 v[224:227], v250 offset:20480\n" \
  "s_waitcnt lgkmcnt(2)\n" \
  "v_smfmac_f32_16x16x64_bf16 v[192:195], v[216:219], v[80:87], v248\n" \
  "s_waitcnt lgkmcnt(1)\n" \
  "v_smfmac_f32_16x16x64_bf16 v[192:195], v[220:223], v[96:103], v248\n" \
  "v_smfmac_f32_16x16x64_bf16 v[196:199], v[216:219], v[88:95], v248\n" \
  "v_smfmac_f32_16x16x64_bf16 v[196:199], v[220:223], v[104:111], v248\n" \
  "s_nop 0\n" \
  "v_add_f32 v192, v192, v194\n" \
  "v_add_f32 v193, v193, v195\n" \
  "s_waitcnt lgkmcnt(0)\n" \
  "v_add_f32 v192, v192, v224\n" \
  "v_add_f32 v193, v193, v225\n" \
  "v_cvt_pk_bf16_f32 v194, v192, v193\n" \
  "s_nop 1\n" \
  "v_pk_add_f32 v[196:197], v[196:197], v[198:199]\n" \
  "v_pk_add_f32 v[196:197], v[196:197], v[226:227]\n" \
  "v_cvt_pk_bf16_f32 v198, v196, v197\n" \
  "ds_write2_b32 v244, v194, v198 offset1:96\n" \
  "v_cmp_u_f32 vcc, v192, v193\n" \
  "s_cbranch_vccnz .Lnf_%=\n" \
  ".Lnfb_%=:\n" \
  "s_waitcnt lgkmcnt(0)\n" \
  "s_barrier\n" \
  "v_mov_b64 v[200:201], 0\n" \
  "v_mov_b64 v[202:203], 0\n" \
  "v_mov_b64 v[204:205], 0\n" \
  "v_mov_b64 v[206:207], 0\n" \
  "v_mov_b64 v[208:209], 0\n" \
  "v_mov_b64 v[210:211], 0\n" \
  "v_mov_b64 v[212:213], 0\n" \
  "v_mov_b64 v[214:215], 0\n" \
  "ds_read_b64_tr_b16 v[232:233], v245 offset:17920\n" \
  "ds_read_b64_tr_b16 v[234:235], v245 offset:18112\n" \
  "ds_read_b64_tr_b16 v[236:237], v245 offset:18688\n" \
  "ds_read_b64_tr_b16 v[238:239], v245 offset:18880\n" \
  "ds_read_b64_tr_b16 v[192:193], v245 offset:16384\n" \
  "ds_read_b64_tr_b16 v[194:195], v245 offset:16576\n" \
  "ds_read_b64_tr_b16 v[196:197], v245 offset:17152\n" \
  "ds_read_b64_tr_b16 v[198:199], v245 offset:17344\n" \
  "ds_read_b128 v[216:219], v249 offset:0\n" \
  "ds_read_b128 v[220:223], v249 offset:1024\n" \
  "ds_read_b128 v[224:227], v249 offset:16384\n" \
  "ds_read_b128 v[228:231], v249 offset:17408\n" \
  "buffer_load_dwordx2 v[240:241], v240, %[rs], %[ss] offen sc1\n" \
  "s_waitcnt lgkmcnt(2)\n" \
  "v_smfmac_f32_16x16x64_bf16 v[200:203], v[232:235], v[216:223], v248 abid:1\n" \
  "ds_read_b128 v[216:219], v249 offset:32768\n" \
  "ds_read_b128 v[220:223], v249 offset:33792\n" \
  "v_smfmac_f32_16x16x64_bf16 v[204:207], v[232:235], v[128:135], v248 abid:1\n" \
  "s_waitcnt lgkmcnt(2)\n" \
  "v_smfmac_f32_16x16x64_bf16 v[200:203], v[236:239], v[224:231], v248 abid:1\n" \
  "ds_read_b128 v[224:227], v249 offset:49152\n" \
  "ds_read_b128 v[228:231], v249 offset:50176\n" \
  "v_smfmac_f32_16x16x64_bf16 v[204:207], v[236:239], v[136:143], v248 abid:1\n" \
  "v_smfmac_f32_16x16x64_bf16 v[200:203], v[192:195], v[176:183], v248 abid:1\n" \
  "v_smfmac_f32_16x16x64_bf16 v[204:207], v[192:195], v[112:119], v248 abid:1\n" \
  "v_smfmac_f32_16x16x64_bf16 v[200:203], v[196:199], v[184:191], v248 abid:1\n" \
  "v_smfmac_f32_16x16x64_bf16 v[204:207], v[196:199], v[120:127], v248 abid:1\n" \
  "s_waitcnt lgkmcnt(2)\n" \
  "v_smfmac_f32_16x16x64_bf16 v[208:211], v[192:195], v[216:223], v248 abid:1\n" \
  "ds_read_b128 v[216:219], v243 offset:0\n" \
  "ds_read_b128 v[220:223], v243 offset:1024\n" \
  "v_smfmac_f32_16x16x64_bf16 v[212:215], v[192:195], v[144:151], v248 abid:1\n" \
  "s_waitcnt lgkmcnt(2)\n" \
  "v_smfmac_f32_16x16x64_bf16 v[208:211], v[196:199], v[224:231], v248 abid:1\n" \
  "ds_read_b128 v[224:227], v243 offset:16384\n" \
  "ds_read_b128 v[228:231], v243 offset:17408\n" \
  "v_cvt_pk_bf16_f32 v200, v200, v201\n" \
  "v_smfmac_f32_16x16x64_bf16 v[212:215], v[196:199], v[152:159], v248 abid:1\n" \
  "v_cvt_pk_bf16_f32 v201, v202, v203\n" \
  "v_smfmac_f32_16x16x64_bf16 v[212:215], v[232:235], v[160:167], v248 abid:1\n" \
  "v_cvt_pk_bf16_f32 v202, v204, v205\n" \
  "v_smfmac_f32_16x16x64_bf16 v[212:215], v[236:239], v[168:175], v248 abid:1\n" \
  "v_cvt_pk_bf16_f32 v203, v206, v207\n" \
  "s_waitcnt lgkmcnt(2)\n" \
  "v_smfmac_f32_16x16x64_bf16 v[208:211], v[232:235], v[216:223], v248 abid:1\n" \
  "v_permlane16_swap_b32 v200, v202\n" \
  "s_waitcnt lgkmcnt(0)\n" \
  "v_smfmac_f32_16x16x64_bf16 v[208:211], v[236:239], v[224:231], v248 abid:1\n" \
  "v_permlane16_swap_b32 v201, v203\n" \
  "buffer_store_dwordx4 v[200:203], v247, %[rs], %[vs] offen sc1\n" \
  "v_cvt_pk_bf16_f32 v212, v212, v213\n" \
  "v_cvt_pk_bf16_f32 v213, v214, v215\n" \
  "s_nop 4\n" \
  "v_cvt_pk_bf16_f32 v211, v210, v211\n" \
  "v_cvt_pk_bf16_f32 v210, v208, v209\n" \
  "s_nop 0\n" \
  "v_permlane16_swap_b32 v211, v213\n" \
  "v_permlane16_swap_b32 v210, v212\n" \
  "buffer_store_dwordx4 v[210:213], v246, %[rs], %[vs] offen sc1\n" \
  "s_nop 1\n" \
  "s_waitcnt vmcnt(2)\n"
#define QK_A \
  "s_branch .Lgo_%=\n" \
  ".Lrs_%=:\n" \
  "s_mov_b32 %[fs], -1\n" \
  "s_nop 15\n" \
  "s_nop 15\n" \
  ".Lrt_%=:\n" \
  "s_setprio 0\n" \
  "s_waitcnt vmcnt(0) lgkmcnt(0)\n" \
  "ds_read_b128 v[200:203], v249 offset:0\n" \
  "ds_read_b128 v[204:207], v249 offset:1024\n" \
  "ds_read_b128 v[208:211], v249 offset:16384\n" \
  "ds_read_b128 v[212:215], v249 offset:17408\n" \
  "v_mov_b32 v241, 0x7f800000\n" \
  "ds_write_b32 v250, v241 offset:22528\n" \
  "v_mov_b64 v[192:193], 0\n" \
  "v_mov_b64 v[194:195], 0\n" \
  "v_mov_b64 v[196:197], 0\n" \
  "v_mov_b64 v[198:199], 0\n" \
  "s_barrier\n" \
  "ds_read_b128 v[216:219], v251 offset:0\n" \
  "ds_read_b128 v[220:223], v251 offset:1024\n" \
  "ds_read_b128 v[224:227], v251 offset:2048\n" \
  "ds_read_b128 v[228:231], v251 offset:3072\n" \
  "s_waitcnt lgkmcnt(0)\n" \
  "s_barrier\n" \
  "s_branch .Ll1_%=\n" \
  ".Lnf_%=:\n" \
  "s_cmp_eq_u32 %[fs], -1\n" \
  "s_cbranch_scc1 .Lnfb_%=\n" \
  "v_mov_b32 v241, %[fs]\n" \
  "ds_write_b32 v251, v241 offset:23560\n" \
  "s_waitcnt lgkmcnt(0)\n" \
  "s_mov_b32 %[fs], -1\n" \
  "s_barrier\n" \
  "s_branch .Lrt_%=\n" \
  ".Lw1_%=:\n" \
  "ds_read_b32 v238, v251 offset:23560\n" \
  "s_waitcnt lgkmcnt(0)\n" \
  "v_cmp_eq_u32 vcc, %[fs], v238\n" \
  "s_cbranch_vccnz .Lrs_%=\n" \
  "v_xor_b32 v238, 0x1000, v242\n" \
  "ds_read_b128 v[216:219], v238 offset:0\n" \
  "ds_read_b128 v[228:231], v238 offset:1024\n" \
  "ds_read_b128 v[232:235], v238 offset:2048\n" \
  "ds_read_b128 v[200:203], v238 offset:3072\n" \
  "s_waitcnt lgkmcnt(0)\n" \
  "v_mfma_f32_16x16x32_bf16 v[216:219], v[216:219], v[192:195], 0\n" \
  "v_mfma_f32_16x16x32_bf16 v[216:219], v[228:231], v[196:199], v[216:219]\n" \
  "v_mfma_f32_16x16x32_bf16 v[216:219], v[232:235], v[208:211], v[216:219]\n" \
  "v_mfma_f32_16x16x32_bf16 v[216:219], v[200:203], v[212:215], v[216:219]\n" \
  "s_nop 9\n" \
  "v_cndmask_b32 v238, v216, v218, %[hf]\n" \
  "v_cndmask_b32 v239, v217, v219, %[hf]\n" \
  "v_max3_f32 v222, |v238|, |v239|, v221\n" \
  "v_cmp_nge_f32 vcc, 0x43aa0000, v222\n" \
  "s_cbranch_vccz .Lw1b_%=\n" \
  "s_mov_b64 %[wd], -1\n" \
  "v_max3_f32 v222, v204, v205, v238\n" \
  "v_max_f32 v222, v222, v239\n" \
  "s_nop 1\n" \
  "v_max_f32_dpp v222, v222, v222 row_ror:8 row_mask:0xf bank_mask:0xf\n" \
  "v_mov_b32 v221, v222\n" \
  "s_nop 1\n" \
  "v_permlane32_swap_b32 v222, v221\n" \
  "v_max_f32 v222, v222, v221\n" \
  "v_mov_b32 v221, v222\n" \
  "s_nop 1\n" \
  "v_permlane16_swap_b32 v222, v221\n" \
  "v_max_f32 v222, v222, v221\n" \
  "v_mul_f32 v221, 0xbe0293ee, v222\n" \
  "v_fmamk_f32 v236, v204, 0x3e0293ee, v221\n" \
  "v_fmamk_f32 v237, v205, 0x3e0293ee, v221\n" \
  "v_exp_f32 v236, v236\n" \
  "v_exp_f32 v237, v237\n" \
  "s_nop 1\n" \
  "v_cvt_pk_bf16_f32 v220, v236, v237\n" \
  "v_and_b32 v220, 0x7fffffff, v220\n" \
  "s_branch .Lw1b_%=\n" \
  ".Lw2_%=:\n" \
  "v_max3_f32 v206, v204, v205, v222\n" \
  "s_nop 1\n" \
  "v_max_f32_dpp v206, v206, v206 row_ror:8 row_mask:0xf bank_mask:0xf\n" \
  "v_mov_b32 v221, v206\n" \
  "s_nop 1\n" \
  "v_permlane32_swap_b32 v206, v221\n" \
  "v_max_f32 v206, v206, v221\n" \
  "v_mov_b32 v221, v206\n" \
  "s_nop 1\n" \
  "v_permlane16_swap_b32 v206, v221\n" \
  "v_max_f32 v206, v206, v221\n" \
  "v_mul_f32 v221, 0xbe0293ee, v206\n" \
  "v_fmamk_f32 v236, v204, 0x3e0293ee, v221\n" \
  "v_fmamk_f32 v237, v205, 0x3e0293ee, v221\n" \
  "v_fmamk_f32 v239, v222, 0x3e0293ee, v221\n" \
  "v_exp_f32 v236, v236\n" \
  "v_exp_f32 v237, v237\n" \
  "v_exp_f32 v239, v239\n" \
  "s_nop 1\n" \
  "v_fma_f32 v238, v223, v239, v236\n" \
  "v_add_f32 v238, v238, v237\n" \
  "s_nop 1\n" \
  "s_branch .Lw2b_%=\n" \
  ".Lgo_%=:\n" \
  "ds_read_b128 v[200:203], v249 offset:0\n" \
  "ds_read_b128 v[204:207], v249 offset:1024\n" \
  "ds_read_b128 v[208:211], v249 offset:16384\n" \
  "ds_read_b128 v[212:215], v249 offset:17408\n" \
  "v_mov_b32 v241, 0x7f800000\n" \
  "ds_write_b32 v250, v241 offset:22528\n" \
  "v_mov_b64 v[192:193], 0\n" \
  "v_mov_b64 v[194:195], 0\n" \
  "v_mov_b64 v[196:197], 0\n" \
  "v_mov_b64 v[198:199], 0\n" \
  "s_mov_b32 %[keep], m0\n" \
  "v_readfirstlane_b32 %[cnt], v250\n" \
  "s_mov_b32 m0, %[cnt]\n" \
  "s_waitcnt lgkmcnt(0)\n" \
  "s_barrier\n" \
  "buffer_load_dwordx4 v251, %[rs], %[o0] offen sc1 lds\n" \
  "s_waitcnt vmcnt(0)\n" \
  "s_barrier\n" \
  "s_mov_b32 m0, %[keep]\n" \
  "ds_read_b128 v[216:219], v251 offset:0\n" \
  "ds_read_b128 v[220:223], v251 offset:1024\n" \
  "ds_read_b128 v[224:227], v251 offset:2048\n" \
  "ds_read_b128 v[228:231], v251 offset:3072\n" \
  "s_barrier\n" \
  ".Ll1_%=:\n" \
  "s_waitcnt lgkmcnt(3)\n" \
  "v_smfmac_f32_16x16x64_bf16 v[192:195], v[216:219], v[200:207], v248\n" \
  "ds_read_b128 v[200:203], v249 offset:32768\n" \
  "ds_read_b128 v[204:207], v249 offset:33792\n" \
  "s_waitcnt lgkmcnt(4)\n" \
  "v_smfmac_f32_16x16x64_bf16 v[192:195], v[220:223], v[0:7], v248\n" \
  "ds_read_b128 v[232:235], v251 offset:4096\n" \
  "v_smfmac_f32_16x16x64_bf16 v[196:199], v[216:219], v[128:135], v248\n" \
  "ds_read_b128 v[216:219], v251 offset:5120\n" \
  "s_waitcnt lgkmcnt(5)\n" \
  "v_smfmac_f32_16x16x64_bf16 v[192:195], v[224:227], v[208:215], v248\n" \
  "ds_read_b128 v[208:211], v249 offset:49152\n" \
  "v_smfmac_f32_16x16x64_bf16 v[196:199], v[220:223], v[64:71], v248\n" \
  "ds_read_b128 v[212:215], v249 offset:50176\n" \
  "s_waitcnt lgkmcnt(6)\n" \
  "v_smfmac_f32_16x16x64_bf16 v[192:195], v[228:231], v[8:15], v248\n" \
  "ds_read_b128 v[220:223], v251 offset:6144\n" \
  "v_smfmac_f32_16x16x64_bf16 v[196:199], v[224:227], v[136:143], v248\n" \
  "ds_read_b128 v[224:227], v251 offset:7168\n" \
  "s_waitcnt lgkmcnt(5)\n" \
  "v_smfmac_f32_16x16x64_bf16 v[192:195], v[232:235], v[200:207], v248\n" \
  "ds_read_b128 v[200:203], v243 offset:0\n" \
  "s_barrier\n" \
  "v_smfmac_f32_16x16x64_bf16 v[196:199], v[228:231], v[72:79], v248\n" \
  "ds_read_b128 v[204:207], v243 offset:1024\n" \
  "s_waitcnt lgkmcnt(6)\n" \
  "v_smfmac_f32_16x16x64_bf16 v[192:195], v[216:219], v[16:23], v248\n" \
  "ds_read_b128 v[228:231], v251 offset:8192\n" \
  "v_smfmac_f32_16x16x64_bf16 v[196:199], v[232:235], v[144:151], v248\n" \
  "ds_read_b128 v[232:235], v251 offset:9216\n" \
  "s_waitcnt lgkmcnt(5)\n" \
  "v_smfmac_f32_16x16x64_bf16 v[192:195], v[220:223], v[208:215], v248\n" \
  "ds_read_b128 v[208:211], v243 offset:16384\n" \
  "v_smfmac_f32_16x16x64_bf16 v[196:199], v[216:219], v[80:87], v248\n" \
  "ds_read_b128 v[212:215], v243 offset:17408\n" \
  "s_waitcnt lgkmcnt(6)\n" \
  "v_smfmac_f32_16x16x64_bf16 v[192:195], v[224:227], v[24:31], v248\n" \
  "ds_read_b128 v[216:219], v251 offset:10240\n" \
  "v_smfmac_f32_16x16x64_bf16 v[196:199], v[220:223], v[152:159], v248\n" \
  "ds_read_b128 v[220:223], v251 offset:11264\n" \
  "s_waitcnt lgkmcnt(5)\n" \
  "v_smfmac_f32_16x16x64_bf16 v[192:195], v[228:231], v[200:207], v248\n" \
  "ds_read_b128 v[200:203], v243 offset:32768\n" \
  "v_smfmac_f32_16x16x64_bf16 v[196:199], v[224:227], v[88:95], v248\n" \
  "ds_read_b128 v[204:207], v243 offset:33792\n" \
  "s_waitcnt lgkmcnt(6)\n" \
  "v_smfmac_f32_16x16x64_bf16 v[192:195], v[232:235], v[32:39], v248\n" \
  "ds_read_b128 v[224:227], v251 offset:12288\n" \
  "v_smfmac_f32_16x16x64_bf16 v[196:199], v[228:231], v[160:167], v248\n" \
  "ds_read_b128 v[228:231], v251 offset:13312\n" \
  "s_waitcnt lgkmcnt(5)\n" \
  "v_smfmac_f32_16x16x64_bf16 v[192:195], v[216:219], v[208:215], v248\n" \
  "ds_read_b128 v[208:211], v243 offset:49152\n" \
  "ds_read_b128 v[212:215], v243 offset:50176\n" \
  "v_smfmac_f32_16x16x64_bf16 v[196:199], v[232:235], v[96:103], v248\n" \
  "ds_read_b128 v[236:239], v242 offset:2048\n" \
  "s_waitcnt lgkmcnt(7)\n" \
  "v_smfmac_f32_16x16x64_bf16 v[192:195], v[220:223], v[40:47], v248\n" \
  "ds_read_b128 v[232:235], v251 offset:14336\n" \
  "v_smfmac_f32_16x16x64_bf16 v[196:199], v[216:219], v[168:175], v248\n" \
  "ds_read_b128 v[216:219], v251 offset:15360\n" \
  "s_waitcnt lgkmcnt(6)\n" \
  "v_smfmac_f32_16x16x64_bf16 v[192:195], v[224:227], v[200:207], v248\n" \
  "ds_read_b128 v[200:203], v242 offset:0\n" \
  "v_smfmac_f32_16x16x64_bf16 v[196:199], v[220:223], v[104:111], v248\n" \
  "ds_read_b128 v[204:207], v242 offset:1024\n" \
  "s_waitcnt lgkmcnt(7)\n" \
  "v_smfmac_f32_16x16x64_bf16 v[192:195], v[228:231], v[48:55], v248\n" \
  "ds_read_b128 v[220:223], v242 offset:3072\n" \
  "v_smfmac_f32_16x16x64_bf16 v[196:199], v[224:227], v[176:183], v248\n" \
  "s_waitcnt lgkmcnt(4)\n" \
  "v_smfmac_f32_16x16x64_bf16 v[192:195], v[232:235], v[208:215], v248\n" \
  "s_waitcnt lgkmcnt(3)\n" \
  "v_smfmac_f32_16x16x64_bf16 v[192:195], v[216:219], v[56:63], v248\n" \
  "v_smfmac_f32_16x16x64_bf16 v[196:199], v[228:231], v[112:119], v248\n" \
  "v_smfmac_f32_16x16x64_bf16 v[196:199], v[232:235], v[184:191], v248\n" \
  "s_nop 0\n" \
  "v_add_f32 v192, v192, v194\n" \
  "v_smfmac_f32_16x16x64_bf16 v[196:199], v[216:219], v[120:127], v248\n" \
  "v_add_f32 v193, v193, v195\n" \
  "v_cvt_pk_bf16_f32 v194, v192, v193\n" \
  "s_nop 6\n" \
  "v_add_f32 v196, v196, v198\n" \
  "v_add_f32 v197, v197, v199\n" \
  "v_cvt_pk_bf16_f32 v198, v196, v197\n" \
  "ds_write2_b32 v247, v194, v198 offset1:96\n" \
  "v_cmp_u_f32 vcc, v192, v193\n" \
  "s_cbranch_vccnz .Lnf_%=\n" \
  ".Lnfb_%=:\n" \
  "v_xor_b32 v241, 0x400, v250\n" \
  "s_waitcnt lgkmcnt(0)\n" \
  "s_barrier\n" \
  "ds_read_b64_tr_b16 v[192:193], v246 offset:16384\n" \
  "ds_read_b64_tr_b16 v[194:195], v246 offset:16768\n" \
  "ds_read_b64_tr_b16 v[196:197], v246 offset:17152\n" \
  "ds_read_b64_tr_b16 v[198:199], v246 offset:17536\n" \
  "ds_read_b64_tr_b16 v[208:209], v246 offset:17920\n" \
  "ds_read_b64_tr_b16 v[210:211], v246 offset:18304\n" \
  "ds_read_b64_tr_b16 v[212:213], v246 offset:18688\n" \
  "ds_read_b64_tr_b16 v[214:215], v246 offset:19072\n" \
  "s_waitcnt lgkmcnt(6)\n" \
  "v_mfma_f32_16x16x32_bf16 v[200:203], v[200:203], v[192:195], 0\n" \
  "s_waitcnt lgkmcnt(4)\n" \
  "v_mfma_f32_16x16x32_bf16 v[200:203], v[204:207], v[196:199], v[200:203]\n" \
  "s_waitcnt lgkmcnt(2)\n" \
  "v_mfma_f32_16x16x32_bf16 v[200:203], v[236:239], v[208:211], v[200:203]\n" \
  "s_waitcnt lgkmcnt(0)\n" \
  "v_mfma_f32_16x16x32_bf16 v[200:203], v[220:223], v[212:215], v[200:203]\n" \
  "s_nop 8\n" \
  "v_cndmask_b32 v204, v200, v202, %[hf]\n" \
  "v_cndmask_b32 v205, v201, v203, %[hf]\n" \
  "v_max_f32 v221, |v204|, |v205|\n" \
  "ds_write_b32 v250, v221 offset:22528\n" \
  "v_mul_f32 v236, 0x3e0293ee, v204\n" \
  "v_mul_f32 v237, 0x3e0293ee, v205\n" \
  "v_exp_f32 v236, v236\n" \
  "v_exp_f32 v237, v237\n" \
  "ds_read_b32 v238, v241 offset:22528\n" \
  "v_cvt_pk_bf16_f32 v220, v236, v237\n" \
  "s_mov_b64 %[wd], 0\n" \
  "s_waitcnt lgkmcnt(0)\n" \
  "v_max_f32 v238, v238, v221\n" \
  "v_cmp_nge_f32 vcc, 0x43aa0000, v238\n" \
  "s_cbranch_vccnz .Lw1_%=\n" \
  ".Lw1b_%=:\n" \
  "buffer_store_dword v220, v245, %[rs], %[c0] offen sc1\n" \
  "buffer_store_dword v220, v245, %[rs], %[c1] offen sc1\n" \
  "buffer_store_dword v220, v245, %[rs], %[c2] offen sc1\n" \
  "buffer_store_dword v220, v245, %[rs], %[c3] offen sc1\n" \
  "v_add_f32 v223, v236, v237\n" \
  "ds_write_b32 v250, v223 offset:22532\n" \
  "v_cndmask_b32 v222, 0, v222, %[wd]\n" \
  "v_mov_b32 v239, 1.0\n" \
  "s_waitcnt lgkmcnt(0)\n" \
  "s_barrier\n" \
  "ds_read_b64_tr_b16 v[216:217], v246 offset:19456\n" \
  "ds_read_b64_tr_b16 v[218:219], v246 offset:19840\n" \
  "ds_read_b64_tr_b16 v[228:229], v246 offset:20224\n" \
  "ds_read_b64_tr_b16 v[230:231], v246 offset:20608\n" \
  "ds_read_b64_tr_b16 v[232:233], v246 offset:20992\n" \
  "ds_read_b64_tr_b16 v[234:235], v246 offset:21376\n" \
  "ds_read_b64_tr_b16 v[200:201], v246 offset:21760\n" \
  "ds_read_b64_tr_b16 v[202:203], v246 offset:22144\n" \
  "ds_read_b32 v238, v241 offset:22532\n" \
  "s_waitcnt lgkmcnt(7)\n" \
  "v_mfma_f32_16x16x32_bf16 v[216:219], v[216:219], v[192:195], 0\n" \
  "s_waitcnt lgkmcnt(5)\n" \
  "v_mfma_f32_16x16x32_bf16 v[216:219], v[228:231], v[196:199], v[216:219]\n" \
  "s_waitcnt lgkmcnt(3)\n" \
  "v_mfma_f32_16x16x32_bf16 v[216:219], v[232:235], v[208:211], v[216:219]\n" \
  "s_waitcnt lgkmcnt(1)\n" \
  "v_mfma_f32_16x16x32_bf16 v[216:219], v[200:203], v[212:215], v[216:219]\n" \
  "s_waitcnt lgkmcnt(0)\n" \
  "v_add_f32 v223, v223, v238\n" \
  "v_mov_b32 v238, v223\n" \
  "s_nop 1\n" \
  "v_permlane32_swap_b32 v223, v238\n" \
  "v_add_f32 v223, v223, v238\n" \
  "s_nop 1\n" \
  "v_cndmask_b32 v204, v216, v218, %[hf]\n" \
  "v_cndmask_b32 v205, v217, v219, %[hf]\n" \
  "v_mul_f32 v236, 0x3e0293ee, v204\n" \
  "v_mul_f32 v237, 0x3e0293ee, v205\n" \
  "v_exp_f32 v236, v236\n" \
  "v_exp_f32 v237, v237\n" \
  "v_max_f32 v221, |v204|, |v205|\n" \
  "v_add_f32 v238, v223, v236\n" \
  "v_cmp_nge_f32 vcc, 0x43aa0000, v221\n" \
  "v_add_f32 v238, v238, v237\n" \
  "s_or_b64 vcc, vcc, %[wd]\n" \
  "s_cbranch_scc1 .Lw2_%=\n" \
  ".Lw2b_%=:\n" \
  "v_add_f32_dpp v238, v238, v238 row_ror:8 row_mask:0xf bank_mask:0xf\n" \
  "v_mov_b32 v221, v238\n" \
  "v_cndmask_b32 v236, v239, v236, %[up]\n" \
  "v_cndmask_b32 v237, v239, v237, %[up]\n" \
  "v_permlane16_swap_b32 v238, v221\n" \
  "v_add_f32 v238, v238, v221\n" \
  "v_rcp_f32 v238, v238\n" \
  "s_nop 0\n" \
  "v_mul_f32 v236, v236, v238\n" \
  "v_mul_f32 v237, v237, v238\n" \
  "v_cvt_pk_bf16_f32 v220, v236, v237\n" \
  "v_and_b32 v220, v220, v240\n" \
  "buffer_store_dword v220, v244, %[rs], %[c0] offen sc1\n" \
  "buffer_store_dword v220, v244, %[rs], %[c1] offen sc1\n"
#define QK_X \
  "s_branch .Lgo_%=\n" \
  ".Lrs_%=:\n" \
  "s_mov_b32 %[fs], -1\n" \
  "s_nop 15\n" \
  "s_nop 15\n" \
  ".Lrt_%=:\n" \
  "s_setprio 0\n" \
  "s_waitcnt vmcnt(0) lgkmcnt(0)\n" \
  "ds_read_b128 v[200:203], v249 offset:0\n" \
  "ds_read_b128 v[204:207], v249 offset:1024\n" \
  "ds_read_b128 v[208:211], v249 offset:16384\n" \
  "ds_read_b128 v[212:215], v249 offset:17408\n" \
  "v_mov_b64 v[192:193], 0\n" \
  "v_mov_b64 v[194:195], 0\n" \
  "v_mov_b64 v[196:197], 0\n" \
  "v_mov_b64 v[198:199], 0\n" \
  "s_barrier\n" \
  "ds_read_b128 v[216:219], v251 offset:0\n" \
  "ds_read_b128 v[220:223], v251 offset:1024\n" \
  "ds_read_b128 v[224:227], v251 offset:2048\n" \
  "ds_read_b128 v[228:231], v251 offset:3072\n" \
  "s_waitcnt lgkmcnt(0)\n" \
  "s_barrier\n" \
  "s_branch .Ll1_%=\n" \
  ".Lnf_%=:\n" \
  "s_cmp_eq_u32 %[fs], -1\n" \
  "s_cbranch_scc1 .Lnfb_%=\n" \
  "v_mov_b32 v241, %[fs]\n" \
  "ds_write_b32 v251, v241 offset:23560\n" \
  "s_waitcnt lgkmcnt(0)\n" \
  "s_mov_b32 %[fs], -1\n" \
  "s_barrier\n" \
  "s_branch .Lrt_%=\n" \
  ".Lgo_%=:\n" \
  "ds_read_b128 v[200:203], v249 offset:0\n" \
  "ds_read_b128 v[204:207], v249 offset:1024\n" \
  "ds_read_b128 v[208:211], v249 offset:16384\n" \
  "ds_read_b128 v[212:215], v249 offset:17408\n" \
  "v_mov_b64 v[192:193], 0\n" \
  "v_mov_b64 v[194:195], 0\n" \
  "v_mov_b64 v[196:197], 0\n" \
  "v_mov_b64 v[198:199], 0\n" \
  "s_mov_b32 %[keep], m0\n" \
  "v_readfirstlane_b32 %[cnt], v250\n" \
  "s_mov_b32 m0, %[cnt]\n" \
  "s_waitcnt lgkmcnt(0)\n" \
  "s_barrier\n" \
  "buffer_load_dwordx4 v251, %[rs], %[o0] offen sc1 lds\n" \
  "s_waitcnt vmcnt(0)\n" \
  "s_barrier\n" \
  "s_mov_b32 m0, %[keep]\n" \
  "ds_read_b128 v[216:219], v251 offset:0\n" \
  "ds_read_b128 v[220:223], v251 offset:1024\n" \
  "ds_read_b128 v[224:227], v251 offset:2048\n" \
  "ds_read_b128 v[228:231], v251 offset:3072\n" \
  "s_barrier\n" \
  ".Ll1_%=:\n" \
  "s_waitcnt lgkmcnt(3)\n" \
  "v_smfmac_f32_16x16x64_bf16 v[192:195], v[216:219], v[200:207], v248\n" \
  "ds_read_b128 v[200:203], v249 offset:32768\n" \
  "ds_read_b128 v[204:207], v249 offset:33792\n" \
  "s_waitcnt lgkmcnt(4)\n" \
  "v_smfmac_f32_16x16x64_bf16 v[192:195], v[220:223], v[0:7], v248\n" \
  "ds_read_b128 v[232:235], v251 offset:4096\n" \
  "v_smfmac_f32_16x16x64_bf16 v[196:199], v[216:219], v[128:135], v248\n" \
  "ds_read_b128 v[216:219], v251 offset:5120\n" \
  "s_waitcnt lgkmcnt(5)\n" \
  "v_smfmac_f32_16x16x64_bf16 v[192:195], v[224:227], v[208:215], v248\n" \
  "ds_read_b128 v[208:211], v249 offset:49152\n" \
  "v_smfmac_f32_16x16x64_bf16 v[196:199], v[220:223], v[64:71], v248\n" \
  "ds_read_b128 v[212:215], v249 offset:50176\n" \
  "s_waitcnt lgkmcnt(6)\n" \
  "v_smfmac_f32_16x16x64_bf16 v[192:195], v[228:231], v[8:15], v248\n" \
  "ds_read_b128 v[220:223], v251 offset:6144\n" \
  "v_smfmac_f32_16x16x64_bf16 v[196:199], v[224:227], v[136:143], v248\n" \
  "ds_read_b128 v[224:227], v251 offset:7168\n" \
  "s_waitcnt lgkmcnt(5)\n" \
  "v_smfmac_f32_16x16x64_bf16 v[192:195], v[232:235], v[200:207], v248\n" \
  "ds_read_b128 v[200:203], v243 offset:0\n" \
  "s_barrier\n" \
  "v_smfmac_f32_16x16x64_bf16 v[196:199], v[228:231], v[72:79], v248\n" \
  "ds_read_b128 v[204:207], v243 offset:1024\n" \
  "s_waitcnt lgkmcnt(6)\n" \
  "v_smfmac_f32_16x16x64_bf16 v[192:195], v[216:219], v[16:23], v248\n" \
  "ds_read_b128 v[228:231], v251 offset:8192\n" \
  "v_smfmac_f32_16x16x64_bf16 v[196:199], v[232:235], v[144:151], v248\n" \
  "ds_read_b128 v[232:235], v251 offset:9216\n" \
  "s_waitcnt lgkmcnt(5)\n" \
  "v_smfmac_f32_16x16x64_bf16 v[192:195], v[220:223], v[208:215], v248\n" \
  "ds_read_b128 v[208:211], v243 offset:16384\n" \
  "v_smfmac_f32_16x16x64_bf16 v[196:199], v[216:219], v[80:87], v248\n" \
  "ds_read_b128 v[212:215], v243 offset:17408\n" \
  "s_waitcnt lgkmcnt(6)\n" \
  "v_smfmac_f32_16x16x64_bf16 v[192:195], v[224:227], v[24:31], v248\n" \
  "ds_read_b128 v[216:219], v251 offset:10240\n" \
  "v_smfmac_f32_16x16x64_bf16 v[196:199], v[220:223], v[152:159], v248\n" \
  "ds_read_b128 v[220:223], v251 offset:11264\n" \
  "s_waitcnt lgkmcnt(5)\n" \
  "v_smfmac_f32_16x16x64_bf16 v[192:195], v[228:231], v[200:207], v248\n" \
  "ds_read_b128 v[200:203], v243 offset:32768\n" \
  "v_smfmac_f32_16x16x64_bf16 v[196:199], v[224:227], v[88:95], v248\n" \
  "ds_read_b128 v[204:207], v243 offset:33792\n" \
  "s_waitcnt lgkmcnt(6)\n" \
  "v_smfmac_f32_16x16x64_bf16 v[192:195], v[232:235], v[32:39], v248\n" \
  "ds_read_b128 v[224:227], v251 offset:12288\n" \
  "v_smfmac_f32_16x16x64_bf16 v[196:199], v[228:231], v[160:167], v248\n" \
  "ds_read_b128 v[228:231], v251 offset:13312\n" \
  "s_waitcnt lgkmcnt(5)\n" \
  "v_smfmac_f32_16x16x64_bf16 v[192:195], v[216:219], v[208:215], v248\n" \
  "ds_read_b128 v[208:211], v243 offset:49152\n" \
  "v_smfmac_f32_16x16x64_bf16 v[196:199], v[232:235], v[96:103], v248\n" \
  "ds_read_b128 v[212:215], v243 offset:50176\n" \
  "s_waitcnt lgkmcnt(6)\n" \
  "v_smfmac_f32_16x16x64_bf16 v[192:195], v[220:223], v[40:47], v248\n" \
  "ds_read_b128 v[232:235], v251 offset:14336\n" \
  "v_smfmac_f32_16x16x64_bf16 v[196:199], v[216:219], v[168:175], v248\n" \
  "ds_read_b128 v[216:219], v251 offset:15360\n" \
  "s_waitcnt lgkmcnt(5)\n" \
  "v_smfmac_f32_16x16x64_bf16 v[192:195], v[224:227], v[200:207], v248\n" \
  "v_smfmac_f32_16x16x64_bf16 v[196:199], v[220:223], v[104:111], v248\n" \
  "s_waitcnt lgkmcnt(4)\n" \
  "v_smfmac_f32_16x16x64_bf16 v[192:195], v[228:231], v[48:55], v248\n" \
  "v_smfmac_f32_16x16x64_bf16 v[196:199], v[224:227], v[176:183], v248\n" \
  "s_waitcnt lgkmcnt(1)\n" \
  "v_smfmac_f32_16x16x64_bf16 v[192:195], v[232:235], v[208:215], v248\n" \
  "s_waitcnt lgkmcnt(0)\n" \
  "v_smfmac_f32_16x16x64_bf16 v[192:195], v[216:219], v[56:63], v248\n" \
  "v_smfmac_f32_16x16x64_bf16 v[196:199], v[228:231], v[112:119], v248\n" \
  "v_smfmac_f32_16x16x64_bf16 v[196:199], v[232:235], v[184:191], v248\n" \
  "s_nop 0\n" \
  "v_add_f32 v192, v192, v194\n" \
  "v_smfmac_f32_16x16x64_bf16 v[196:199], v[216:219], v[120:127], v248\n" \
  "v_add_f32 v193, v193, v195\n" \
  "v_cvt_pk_bf16_f32 v194, v192, v193\n" \
  "s_nop 6\n" \
  "v_add_f32 v196, v196, v198\n" \
  "v_add_f32 v197, v197, v199\n" \
  "v_cvt_pk_bf16_f32 v198, v196, v197\n" \
  "ds_write2_b32 v247, v194, v198 offset1:96\n" \
  "v_cmp_u_f32 vcc, v192, v193\n" \
  "s_cbranch_vccnz .Lnf_%=\n" \
  ".Lnfb_%=:\n" \
  "s_waitcnt lgkmcnt(0)\n" \
  "s_barrier\n" \
  "s_nop 5\n" \
  "buffer_load_dwordx4 v[200:203], v251, %[rs], %[qo] offen offset:0 sc1\n" \
  "buffer_load_dwordx4 v[204:207], v251, %[rs], %[qo] offen offset:1024 sc1\n" \
  "buffer_load_dwordx4 v[208:211], v251, %[rs], %[qo] offen offset:2048 sc1\n" \
  "buffer_load_dwordx4 v[212:215], v251, %[rs], %[qo] offen offset:3072 sc1\n" \
  "s_nop 15\n" \
  "s_nop 15\n" \
  "s_nop 15\n" \
  "s_nop 9\n" \
  "ds_read_b32 v241, v251 offset:23560\n" \
  "s_waitcnt lgkmcnt(0)\n" \
  "v_cmp_eq_u32 vcc, %[fs], v241\n" \
  "s_cbranch_vccnz .Lrs_%=\n" \
  "ds_read_b128 v[192:195], v251 offset:24576\n" \
  "ds_read_b128 v[196:199], v251 offset:28672\n" \
  "ds_read_b128 v[216:219], v251 offset:25600\n" \
  "ds_read_b128 v[220:223], v251 offset:29696\n" \
  "ds_read_b128 v[224:227], v251 offset:26624\n" \
  "ds_read_b128 v[228:231], v251 offset:30720\n" \
  "ds_read_b128 v[232:235], v251 offset:27648\n" \
  "ds_read_b128 v[236:239], v251 offset:31744\n" \
  "s_waitcnt lgkmcnt(0)\n" \
  "s_barrier\n" \
  "s_waitcnt vmcnt(3)\n" \
  "v_mfma_f32_16x16x32_bf16 v[192:195], v[192:195], v[200:203], 0\n" \
  "v_mfma_f32_16x16x32_bf16 v[196:199], v[196:199], v[200:203], 0\n" \
  "s_waitcnt vmcnt(2)\n" \
  "v_mfma_f32_16x16x32_bf16 v[192:195], v[216:219], v[204:207], v[192:195]\n" \
  "v_mfma_f32_16x16x32_bf16 v[196:199], v[220:223], v[204:207], v[196:199]\n" \
  "s_waitcnt vmcnt(1)\n" \
  "v_mfma_f32_16x16x32_bf16 v[192:195], v[224:227], v[208:211], v[192:195]\n" \
  "v_mfma_f32_16x16x32_bf16 v[196:199], v[228:231], v[208:211], v[196:199]\n" \
  "s_waitcnt vmcnt(0)\n" \
  "v_mfma_f32_16x16x32_bf16 v[192:195], v[232:235], v[212:215], v[192:195]\n" \
  "v_mfma_f32_16x16x32_bf16 v[196:199], v[236:239], v[212:215], v[196:199]\n" \
  "s_nop 8\n"
#define QK_K \
  "s_branch .Lgo_%=\n" \
  ".Lsf_%=:\n" \
  "s_cmp_eq_u32 %[fs], -1\n" \
  "s_cbranch_scc1 .Lsfb_%=\n" \
  "v_mov_b32 v241, %[fs]\n" \
  "ds_write_b32 v251, v241 offset:23560\n" \
  "s_branch .Lrt_%=\n" \
  ".Lrs_%=:\n" \
  "s_mov_b32 %[fs], -1\n" \
  "s_nop 15\n" \
  "s_nop 15\n" \
  ".Lrt_%=:\n" \
  "s_setprio 0\n" \
  "s_waitcnt vmcnt(0) lgkmcnt(0)\n" \
  "buffer_load_dwordx4 v[216:219], v251, %[rs], %[o3] offen sc1\n" \
  "buffer_load_dwordx4 v[220:223], v251, %[rs], %[o0] offen sc1\n" \
  "buffer_load_dwordx4 v[224:227], v251, %[rs], %[o1] offen sc1\n" \
  "buffer_load_dwordx4 v[228:231], v251, %[rs], %[o2] offen sc1\n" \
  "v_subrev_u32 v232, 0x1000, v250\n" \
  "s_mov_b32 %[cnt], -2\n" \
  "ds_read_b128 v[200:203], v232 offset:0\n" \
  "ds_read_b128 v[204:207], v232 offset:4096\n" \
  "ds_read_b128 v[208:211], v232 offset:8192\n" \
  "ds_read_b128 v[212:215], v232 offset:12288\n" \
  "s_waitcnt lgkmcnt(0)\n" \
  "v_max_u32 v241, v200, v201\n" \
  "v_max3_u32 v241, v241, v202, v203\n" \
  "v_max3_u32 v241, v241, v204, v205\n" \
  "v_max3_u32 v241, v241, v206, v207\n" \
  "v_max3_u32 v241, v241, v208, v209\n" \
  "v_max3_u32 v241, v241, v210, v211\n" \
  "v_max3_u32 v241, v241, v212, v213\n" \
  "v_max3_u32 v241, v241, v214, v215\n" \
  "v_cmp_eq_u32 vcc, -1, v241\n" \
  "s_cbranch_vccz .Lrd_%=\n" \
  "s_mov_b32 %[cnt], 0x40000\n" \
  ".Lrr_%=:\n" \
  "s_waitcnt vmcnt(0)\n" \
  "v_max_u32 v241, v216, v217\n" \
  "v_max3_u32 v241, v241, v218, v219\n" \
  "v_max3_u32 v241, v241, v220, v221\n" \
  "v_max3_u32 v241, v241, v222, v223\n" \
  "v_max3_u32 v241, v241, v224, v225\n" \
  "v_max3_u32 v241, v241, v226, v227\n" \
  "v_max3_u32 v241, v241, v228, v229\n" \
  "v_max3_u32 v241, v241, v230, v231\n" \
  "v_cmp_eq_u32 vcc, -1, v241\n" \
  "s_cbranch_vccz .Lrd_%=\n" \
  "s_sleep 2\n" \
  "buffer_load_dwordx4 v[216:219], v251, %[rs], %[o3] offen sc1\n" \
  "buffer_load_dwordx4 v[220:223], v251, %[rs], %[o0] offen sc1\n" \
  "buffer_load_dwordx4 v[224:227], v251, %[rs], %[o1] offen sc1\n" \
  "buffer_load_dwordx4 v[228:231], v251, %[rs], %[o2] offen sc1\n" \
  "s_sub_u32 %[cnt], %[cnt], 1\n" \
  "s_cbranch_scc0 .Lrr_%=\n" \
  ".Lrd_%=:\n" \
  "ds_read_b128 v[200:203], v249 offset:0\n" \
  "ds_read_b128 v[204:207], v249 offset:1024\n" \
  "ds_read_b128 v[208:211], v249 offset:16384\n" \
  "ds_read_b128 v[212:215], v249 offset:17408\n" \
  "v_mov_b64 v[192:193], 0\n" \
  "v_mov_b64 v[194:195], 0\n" \
  "v_mov_b64 v[196:197], 0\n" \
  "v_mov_b64 v[198:199], 0\n" \
  "s_cmp_eq_u32 %[fs], -1\n" \
  "s_cbranch_scc1 .Lre_%=\n" \
  "s_mov_b32 %[fs], -1\n" \
  "s_barrier\n" \
  ".Lre_%=:\n" \
  "s_waitcnt vmcnt(0)\n" \
  "s_cmp_eq_u32 %[cnt], -2\n" \
  "s_cbranch_scc1 .Lrf_%=\n" \
  "ds_write_b128 v232, v[216:219] offset:0\n" \
  "ds_write_b128 v232, v[220:223] offset:4096\n" \
  "ds_write_b128 v232, v[224:227] offset:8192\n" \
  "ds_write_b128 v232, v[228:231] offset:12288\n" \
  ".Lrf_%=:\n" \
  "v_mov_b32 v241, 0\n" \
  "s_waitcnt lgkmcnt(0)\n" \
  "s_barrier\n" \
  "s_waitcnt lgkmcnt(0)\n" \
  "s_barrier\n" \
  "s_branch .Ll1_%=\n" \
  ".Lgo_%=:\n" \
  "ds_read_b128 v[200:203], v249 offset:0\n" \
  "ds_read_b128 v[204:207], v249 offset:1024\n" \
  "ds_read_b128 v[208:211], v249 offset:16384\n" \
  "ds_read_b128 v[212:215], v249 offset:17408\n" \
  "v_mov_b64 v[192:193], 0\n" \
  "v_mov_b64 v[194:195], 0\n" \
  "v_mov_b64 v[196:197], 0\n" \
  "v_mov_b64 v[198:199], 0\n" \
  "s_mov_b32 %[keep], m0\n" \
  "v_readfirstlane_b32 %[cnt], v250\n" \
  "s_mov_b32 m0, %[cnt]\n" \
  "v_bfe_u32 v241, v251, 2, 7\n" \
  "v_lshl_add_u32 v241, v251, 4, v241\n" \
  "s_waitcnt lgkmcnt(0)\n" \
  "s_cmp_eq_u32 %[tw], 0\n" \
  "s_cbranch_scc1 .Ltq_%=\n" \
  ".Ltl_%=:\n" \
  "s_memtime vcc\n" \
  "s_waitcnt lgkmcnt(0)\n" \
  "s_sub_i32 %[cnt], %[tg], vcc_lo\n" \
  "s_cmp_lt_i32 %[cnt], 260\n" \
  "s_cbranch_scc1 .Ltf_%=\n" \
  "s_sleep 1\n" \
  "s_branch .Ltl_%=\n" \
  ".Ltf_%=:\n" \
  "s_max_i32 %[cnt], %[cnt], 0\n" \
  ".Ltg_%=:\n" \
  "s_sub_u32 %[cnt], %[cnt], 28\n" \
  "s_cbranch_scc0 .Ltg_%=\n" \
  ".Ltq_%=:\n" \
  "s_barrier\n" \
  "buffer_load_dwordx4 v251, %[rs], %[o0] offen sc1 lds\n" \
  "s_add_u32 m0, m0, 0x1000\n" \
  "s_nop 4\n" \
  "buffer_load_dwordx4 v251, %[rs], %[o1] offen sc1 lds\n" \
  "s_add_u32 m0, m0, 0x1000\n" \
  "s_nop 4\n" \
  "buffer_load_dwordx4 v251, %[rs], %[o2] offen sc1 lds\n" \
  "s_barrier\n" \
  "s_waitcnt vmcnt(2)\n" \
  "s_barrier\n" \
  "s_mov_b32 m0, %[keep]\n" \
  ".Ll1_%=:\n" \
  "s_memtime vcc\n" \
  "s_setprio 3\n" \
  "s_waitcnt vmcnt(0)\n" \
  "s_waitcnt lgkmcnt(0)\n" \
  "s_barrier\n" \
  "ds_read2_b32 v[236:237], v241 offset1:32\n" \
  "s_sub_u32 %[cnt], vcc_lo, %[ckm]\n" \
  "s_mul_hi_u32 %[cnt], %[cnt], %[inv]\n" \
  "s_add_u32 %[cnt], %[cnt], %[r16]\n" \
  "s_and_b32 %[cnt], %[cnt], -2\n" \
  "s_waitcnt lgkmcnt(0)\n" \
  "v_max_u32 v241, v236, v237\n" \
  "v_cmp_eq_u32 vcc, -1, v241\n" \
  "s_cbranch_vccnz .Lsf_%=\n" \
  ".Lsfb_%=:\n" \
  "v_mov_b32 v241, %[cnt]\n" \
  "buffer_store_dword v241, v245, %[rs], %[sst] offen sc1\n" \
  "s_setprio 0\n" \
  "ds_read_b128 v[216:219], v251 offset:0\n" \
  "ds_read_b128 v[220:223], v251 offset:1024\n" \
  "ds_read_b128 v[224:227], v251 offset:2048\n" \
  "ds_read_b128 v[228:231], v251 offset:3072\n" \
  "s_waitcnt lgkmcnt(3)\n" \
  "v_smfmac_f32_16x16x64_bf16 v[192:195], v[216:219], v[200:207], v248\n" \
  "ds_read_b128 v[200:203], v249 offset:32768\n" \
  "ds_read_b128 v[204:207], v249 offset:33792\n" \
  "s_waitcnt lgkmcnt(4)\n" \
  "v_smfmac_f32_16x16x64_bf16 v[192:195], v[220:223], v[0:7], v248\n" \
  "ds_read_b128 v[232:235], v251 offset:4096\n" \
  "v_smfmac_f32_16x16x64_bf16 v[196:199], v[216:219], v[128:135], v248\n" \
  "ds_read_b128 v[216:219], v251 offset:5120\n" \
  "s_waitcnt lgkmcnt(5)\n" \
  "v_smfmac_f32_16x16x64_bf16 v[192:195], v[224:227], v[208:215], v248\n" \
  "ds_read_b128 v[208:211], v249 offset:49152\n" \
  "v_smfmac_f32_16x16x64_bf16 v[196:199], v[220:223], v[64:71], v248\n" \
  "ds_read_b128 v[212:215], v249 offset:50176\n" \
  "s_waitcnt lgkmcnt(6)\n" \
  "v_smfmac_f32_16x16x64_bf16 v[192:195], v[228:231], v[8:15], v248\n" \
  "ds_read_b128 v[220:223], v251 offset:6144\n" \
  "v_smfmac_f32_16x16x64_bf16 v[196:199], v[224:227], v[136:143], v248\n" \
  "ds_read_b128 v[224:227], v251 offset:7168\n" \
  "s_waitcnt lgkmcnt(5)\n" \
  "v_smfmac_f32_16x16x64_bf16 v[192:195], v[232:235], v[200:207], v248\n" \
  "ds_read_b128 v[200:203], v243 offset:0\n" \
  "s_barrier\n" \
  "ds_read_b32 v236, v251 offset:23560\n" \
  "v_smfmac_f32_16x16x64_bf16 v[196:199], v[228:231], v[72:79], v248\n" \
  "buffer_load_dwordx2 v[240:241], v244, %[rs], %[ss] offen sc1\n" \
  "ds_read_b128 v[204:207], v243 offset:1024\n" \
  "s_waitcnt lgkmcnt(7)\n" \
  "v_smfmac_f32_16x16x64_bf16 v[192:195], v[216:219], v[16:23], v248\n" \
  "s_cmp_eq_u32 %[tc], 0\n" \
  "s_cbranch_scc1 .Lnt_%=\n" \
  "buffer_load_dword v237, v242, %[rs], %[tc] offen sc1\n" \
  ".Lnt_%=:\n" \
  "ds_read_b128 v[228:231], v251 offset:8192\n" \
  "v_smfmac_f32_16x16x64_bf16 v[196:199], v[232:235], v[144:151], v248\n" \
  "ds_read_b128 v[232:235], v251 offset:9216\n" \
  "s_waitcnt lgkmcnt(3)\n" \
  "v_cmp_eq_u32 vcc, %[fs], v236\n" \
  "v_smfmac_f32_16x16x64_bf16 v[192:195], v[220:223], v[208:215], v248\n" \
  "ds_read_b128 v[208:211], v243 offset:16384\n" \
  "v_smfmac_f32_16x16x64_bf16 v[196:199], v[216:219], v[80:87], v248\n" \
  "ds_read_b128 v[212:215], v243 offset:17408\n" \
  "v_smfmac_f32_16x16x64_bf16 v[192:195], v[224:227], v[24:31], v248\n" \
  "ds_read_b128 v[216:219], v251 offset:10240\n" \
  "v_smfmac_f32_16x16x64_bf16 v[196:199], v[220:223], v[152:159], v248\n" \
  "ds_read_b128 v[220:223], v251 offset:11264\n" \
  "s_waitcnt lgkmcnt(5)\n" \
  "v_smfmac_f32_16x16x64_bf16 v[192:195], v[228:231], v[200:207], v248\n" \
  "ds_read_b128 v[200:203], v243 offset:32768\n" \
  "v_smfmac_f32_16x16x64_bf16 v[196:199], v[224:227], v[88:95], v248\n" \
  "ds_read_b128 v[204:207], v243 offset:33792\n" \
  "s_waitcnt lgkmcnt(6)\n" \
  "v_smfmac_f32_16x16x64_bf16 v[192:195], v[232:235], v[32:39], v248\n" \
  "ds_read_b128 v[224:227], v251 offset:12288\n" \
  "v_smfmac_f32_16x16x64_bf16 v[196:199], v[228:231], v[160:167], v248\n" \
  "ds_read_b128 v[228:231], v251 offset:13312\n" \
  "s_waitcnt lgkmcnt(5)\n" \
  "v_smfmac_f32_16x16x64_bf16 v[192:195], v[216:219], v[208:215], v248\n" \
  "ds_read_b128 v[208:211], v243 offset:49152\n" \
  "v_smfmac_f32_16x16x64_bf16 v[196:199], v[232:235], v[96:103], v248\n" \
  "ds_read_b128 v[212:215], v243 offset:50176\n" \
  "s_waitcnt lgkmcnt(6)\n" \
  "v_smfmac_f32_16x16x64_bf16 v[192:195], v[220:223], v[40:47], v248\n" \
  "ds_read_b128 v[232:235], v251 offset:14336\n" \
  "v_smfmac_f32_16x16x64_bf16 v[196:199], v[216:219], v[168:175], v248\n" \
  "ds_read_b128 v[216:219], v251 offset:15360\n" \
  "s_waitcnt lgkmcnt(5)\n" \
  "v_smfmac_f32_16x16x64_bf16 v[192:195], v[224:227], v[200:207], v248\n" \
  "v_smfmac_f32_16x16x64_bf16 v[196:199], v[220:223], v[104:111], v248\n" \
  "s_waitcnt lgkmcnt(4)\n" \
  "v_smfmac_f32_16x16x64_bf16 v[192:195], v[228:231], v[48:55], v248\n" \
  "v_smfmac_f32_16x16x64_bf16 v[196:199], v[224:227], v[176:183], v248\n" \
  "s_waitcnt lgkmcnt(1)\n" \
  "v_smfmac_f32_16x16x64_bf16 v[192:195], v[232:235], v[208:215], v248\n" \
  "s_waitcnt lgkmcnt(0)\n" \
  "v_smfmac_f32_16x16x64_bf16 v[192:195], v[216:219], v[56:63], v248\n" \
  "v_smfmac_f32_16x16x64_bf16 v[196:199], v[228:231], v[112:119], v248\n" \
  "v_smfmac_f32_16x16x64_bf16 v[196:199], v[232:235], v[184:191], v248\n" \
  "s_nop 0\n" \
  "v_add_f32 v192, v192, v194\n" \
  "v_smfmac_f32_16x16x64_bf16 v[196:199], v[216:219], v[120:127], v248\n" \
  "s_cbranch_vccnz .Lrs_%=\n" \
  "s_setprio 3\n" \
  "v_add_f32 v193, v193, v195\n" \
  "v_cvt_pk_bf16_f32 v194, v192, v193\n" \
  "s_nop 4\n" \
  "v_add_f32 v196, v196, v198\n" \
  "v_add_f32 v197, v197, v199\n" \
  "v_cvt_pk_bf16_f32 v198, v196, v197\n" \
  "ds_write2_b32 v247, v194, v198 offset1:96\n" \
  "s_waitcnt lgkmcnt(0)\n" \
  "s_barrier\n" \
  "s_setprio 0\n" \
  "s_waitcnt vmcnt(1)\n" \
  "s_cmp_lg_u32 %[tc], 0\n" \
  "s_cbranch_scc1 .Lnw_%=\n" \
  "s_waitcnt vmcnt(0)\n" \
  ".Lnw_%=:\n"
#define OW_F \
  "s_branch .Lgo_%=\n" \
  ".Lbad_%=:\n" \
  "s_mov_b32 %[st], 1\n" \
  "s_branch .Lend_%=\n" \
  ".Lgo_%=:\n" \
  "v_mov_b64 v[84:85], 0\n" \
  "v_mov_b64 v[86:87], 0\n" \
  "v_mov_b64 v[88:89], 0\n" \
  "v_mov_b64 v[90:91], 0\n" \
  "v_mov_b64 v[92:93], 0\n" \
  "v_mov_b64 v[94:95], 0\n" \
  "v_mov_b64 v[96:97], 0\n" \
  "v_mov_b64 v[98:99], 0\n" \
  "v_mov_b64 v[100:101], 0\n" \
  "v_mov_b64 v[102:103], 0\n" \
  "s_mov_b32 %[st], 0\n" \
  "s_barrier\n" \
  "ds_read_b32 v104, v119\n" \
  "s_nop 15\n" \
  "s_nop 15\n" \
  "s_nop 15\n" \
  "s_nop 15\n" \
  "s_nop 5\n" \
  "buffer_load_dwordx4 v[0:3], v112, %[rs], %[sv1] offen offset:0 sc1\n" \
  "buffer_load_dwordx4 v[4:7], v112, %[rs], %[sv1] offen offset:1024 sc1\n" \
  "buffer_load_dwordx4 v[8:11], v112, %[rs], %[sv1] offen offset:2048 sc1\n" \
  "buffer_load_dwordx4 v[12:15], v112, %[rs], %[sv1] offen offset:3072 sc1\n" \
  "s_waitcnt lgkmcnt(0)\n" \
  "v_readfirstlane_b32 %[st], v104\n" \
  ".Ltl_%=:\n" \
  "s_memtime vcc\n" \
  "s_waitcnt lgkmcnt(0)\n" \
  "s_sub_i32 %[cnt], %[st], vcc_lo\n" \
  "s_sub_i32 vcc_hi, %[cnt], 120\n" \
  "s_cmp_lt_u32 vcc_hi, 2000\n" \
  "s_cbranch_scc1 .Ltl_%=\n" \
  "s_mov_b32 %[st], 0\n" \
  "s_max_i32 %[cnt], %[cnt], 0\n" \
  "s_min_i32 %[cnt], %[cnt], 2000\n" \
  ".Ltg_%=:\n" \
  "s_sub_u32 %[cnt], %[cnt], 28\n" \
  "s_cbranch_scc0 .Ltg_%=\n" \
  "buffer_load_dwordx4 v[16:19], v112, %[rs], %[sv2] offen offset:0 sc1\n" \
  "buffer_load_dwordx4 v[20:23], v112, %[rs], %[sv2] offen offset:1024 sc1\n" \
  "s_nop 3\n" \
  "buffer_load_dwordx4 v[80:83], v113, %[rs], %[sp] offen sc1\n" \
  "s_nop 5\n" \
  "buffer_load_dwordx4 v[64:67], v114, %[rs], %[sp1] offen offset:0 sc1\n" \
  "buffer_load_dwordx4 v[68:71], v114, %[rs], %[sp1] offen offset:1024 sc1\n" \
  "buffer_load_dwordx4 v[72:75], v114, %[rs], %[sp2] offen offset:0 sc1\n" \
  "buffer_load_dwordx4 v[24:27], v112, %[rs], %[sv2] offen offset:2048 sc1\n" \
  "buffer_load_dwordx4 v[28:31], v112, %[rs], %[sv2] offen offset:3072 sc1\n" \
  "buffer_load_dwordx4 v[76:79], v114, %[rs], %[sp2] offen offset:1024 sc1\n" \
  "s_barrier\n" \
  "ds_read_b128 v[32:35], v115 offset:0\n" \
  "ds_read_b128 v[36:39], v115 offset:1024\n" \
  "ds_read_b128 v[40:43], v115 offset:2048\n" \
  "ds_read_b128 v[44:47], v115 offset:3072\n" \
  "ds_read_b128 v[48:51], v115 offset:4096\n" \
  "ds_read_b128 v[52:55], v115 offset:5120\n" \
  "ds_read_b128 v[56:59], v115 offset:6144\n" \
  "ds_read_b128 v[60:63], v115 offset:7168\n" \
  "s_waitcnt vmcnt(6)\n" \
  "s_mov_b64 exec, %[m0]\n" \
  "v_mov_b64 v[84:85], v[80:81]\n" \
  "v_mov_b64 v[86:87], v[82:83]\n" \
  "s_mov_b64 exec, %[m1]\n" \
  "v_mov_b64 v[88:89], v[80:81]\n" \
  "v_mov_b64 v[90:91], v[82:83]\n" \
  "s_mov_b64 exec, %[m2]\n" \
  "v_mov_b64 v[92:93], v[80:81]\n" \
  "v_mov_b64 v[94:95], v[82:83]\n" \
  "s_mov_b64 exec, %[m3]\n" \
  "v_mov_b64 v[96:97], v[80:81]\n" \
  "v_mov_b64 v[98:99], v[82:83]\n" \
  "s_mov_b64 exec, -1\n" \
  "s_waitcnt vmcnt(5)\n" \
  "v_smfmac_f32_16x16x64_bf16 v[100:103], v[64:67], v[0:7], v118\n" \
  "s_waitcnt vmcnt(4)\n" \
  "v_smfmac_f32_16x16x64_bf16 v[100:103], v[68:71], v[8:15], v118\n" \
  "s_waitcnt lgkmcnt(6)\n" \
  "v_smfmac_f32_16x16x64_bf16 v[100:103], v[84:87], v[32:39], v118\n" \
  "s_waitcnt lgkmcnt(4)\n" \
  "v_smfmac_f32_16x16x64_bf16 v[100:103], v[88:91], v[40:47], v118 abid:1\n" \
  "s_waitcnt vmcnt(3)\n" \
  "v_smfmac_f32_16x16x64_bf16 v[100:103], v[72:75], v[16:23], v118\n" \
  "s_waitcnt lgkmcnt(2)\n" \
  "v_smfmac_f32_16x16x64_bf16 v[100:103], v[92:95], v[48:55], v118\n" \
  "s_waitcnt lgkmcnt(0)\n" \
  "v_smfmac_f32_16x16x64_bf16 v[100:103], v[96:99], v[56:63], v118 abid:1\n" \
  "s_waitcnt vmcnt(0)\n" \
  "v_smfmac_f32_16x16x64_bf16 v[100:103], v[76:79], v[24:31], v118\n" \
  "s_nop 8\n" \
  "v_add_f32 v100, v100, v102\n" \
  "v_add_f32 v101, v101, v103\n" \
  "v_cvt_pk_bf16_f32 v104, v100, v101\n" \
  "v_cmp_u_f32 vcc, v100, v101\n" \
  "s_nop 0\n" \
  "v_mov_b32_dpp v105, v104 quad_perm:[1,0,3,2] row_mask:0xf bank_mask:0xf\n" \
  "v_perm_b32 v104, v105, v104, v117\n" \
  "s_cbranch_vccnz .Lbad_%=\n" \
  "buffer_store_dword v104, v116, %[rs], %[sm0] offen sc1\n" \
  "buffer_store_dword v104, v116, %[rs], %[sm1] offen sc1\n" \
  "buffer_store_dword v104, v116, %[rs], %[sm2] offen sc1\n" \
  "buffer_store_dword v104, v116, %[rs], %[sm3] offen sc1\n" \
  ".Lend_%=:\n"
#define OW_E \
  "s_branch .Lgo_%=\n" \
  ".Lbad_%=:\n" \
  "s_mov_b32 %[st], 1\n" \
  "s_branch .Lend_%=\n" \
  ".Lgo_%=:\n" \
  "v_mov_b64 v[208:209], 0\n" \
  "v_mov_b64 v[210:211], 0\n" \
  "v_mov_b64 v[212:213], 0\n" \
  "v_mov_b64 v[214:215], 0\n" \
  "v_mov_b64 v[216:217], 0\n" \
  "v_mov_b64 v[218:219], 0\n" \
  "v_mov_b64 v[220:221], 0\n" \
  "v_mov_b64 v[222:223], 0\n" \
  "v_mov_b64 v[224:225], 0\n" \
  "v_mov_b64 v[226:227], 0\n" \
  "v_mov_b64 v[228:229], 0\n" \
  "v_mov_b64 v[230:231], 0\n" \
  "v_mov_b64 v[232:233], 0\n" \
  "v_mov_b64 v[234:235], 0\n" \
  "v_mov_b64 v[236:237], 0\n" \
  "v_mov_b64 v[238:239], 0\n" \
  "s_mov_b32 %[st], 0\n" \
  "s_barrier\n" \
  "buffer_load_dwordx4 v[192:195], v241, %[rs], %[se] offen offset:0 sc1\n" \
  "s_nop 11\n" \
  "buffer_load_dwordx4 v[196:199], v241, %[rs], %[se] offen offset:1024 sc1\n" \
  "s_nop 11\n" \
  "buffer_load_dwordx4 v[200:203], v241, %[rs], %[se] offen offset:2048 sc1\n" \
  "buffer_load_dwordx4 v[204:207], v241, %[rs], %[se] offen offset:3072 sc1\n" \
  "s_waitcnt vmcnt(3)\n" \
  "v_smfmac_f32_16x16x64_bf16 v[208:211], v[192:195], %[b00], v243\n" \
  "v_smfmac_f32_16x16x64_bf16 v[224:227], v[192:195], %[b10], v243\n" \
  "v_max3_u32 v240, v192, v192, v193\n" \
  "v_max3_u32 v240, v240, v194, v195\n" \
  "s_waitcnt vmcnt(2)\n" \
  "v_smfmac_f32_16x16x64_bf16 v[212:215], v[196:199], %[b01], v243\n" \
  "v_smfmac_f32_16x16x64_bf16 v[228:231], v[196:199], %[b11], v243\n" \
  "v_max3_u32 v240, v240, v196, v197\n" \
  "v_max3_u32 v240, v240, v198, v199\n" \
  "v_cvt_pk_bf16_f32 v208, v208, v209\n" \
  "v_cvt_pk_bf16_f32 v209, v210, v211\n" \
  "v_cvt_pk_bf16_f32 v224, v224, v225\n" \
  "v_cvt_pk_bf16_f32 v225, v226, v227\n" \
  "v_cvt_pk_bf16_f32 v210, v212, v213\n" \
  "v_cvt_pk_bf16_f32 v211, v214, v215\n" \
  "ds_write_b128 v242, v[208:211] offset:0\n" \
  "v_cvt_pk_bf16_f32 v226, v228, v229\n" \
  "v_cvt_pk_bf16_f32 v227, v230, v231\n" \
  "ds_write_b128 v242, v[224:227] offset:8192\n" \
  "s_waitcnt vmcnt(1)\n" \
  "v_smfmac_f32_16x16x64_bf16 v[216:219], v[200:203], %[b02], v243\n" \
  "v_smfmac_f32_16x16x64_bf16 v[232:235], v[200:203], %[b12], v243\n" \
  "s_waitcnt vmcnt(0)\n" \
  "v_smfmac_f32_16x16x64_bf16 v[220:223], v[204:207], %[b03], v243\n" \
  "v_smfmac_f32_16x16x64_bf16 v[236:239], v[204:207], %[b13], v243\n" \
  "v_cvt_pk_bf16_f32 v216, v216, v217\n" \
  "v_cvt_pk_bf16_f32 v217, v218, v219\n" \
  "v_cvt_pk_bf16_f32 v232, v232, v233\n" \
  "v_cvt_pk_bf16_f32 v233, v234, v235\n" \
  "s_nop 0\n" \
  "v_cvt_pk_bf16_f32 v218, v220, v221\n" \
  "v_cvt_pk_bf16_f32 v219, v222, v223\n" \
  "ds_write_b128 v242, v[216:219] offset:1024\n" \
  "s_nop 0\n" \
  "v_cvt_pk_bf16_f32 v234, v236, v237\n" \
  "v_cvt_pk_bf16_f32 v235, v238, v239\n" \
  "ds_write_b128 v242, v[232:235] offset:9216\n" \
  "v_max3_u32 v240, v240, v200, v201\n" \
  "v_max3_u32 v240, v240, v202, v203\n" \
  "v_max3_u32 v240, v240, v204, v205\n" \
  "v_max3_u32 v240, v240, v206, v207\n" \
  "v_cmp_eq_u32 vcc, -1, v240\n" \
  "s_cbranch_vccnz .Lbad_%=\n" \
  "s_waitcnt lgkmcnt(0)\n" \
  "s_barrier\n" \
  ".Lend_%=:\n"
#define ST_Y \
  "s_mov_b32 %[keep], m0\n" \
  "ds_read_b128 v[240:243], v250 offset:0\n" \
  "ds_read_b128 v[244:247], v250 offset:1024\n" \
  "v_mfma_f32_16x16x32_bf16 v[192:195], v[0:3], v[208:211], 0\n" \
  "v_mfma_f32_16x16x32_bf16 v[196:199], v[64:67], v[208:211], 0\n" \
  "ds_read_b128 v[208:211], v252 offset:8192\n" \
  "v_mfma_f32_16x16x32_bf16 v[200:203], v[0:3], v[212:215], 0\n" \
  "v_mfma_f32_16x16x32_bf16 v[204:207], v[64:67], v[212:215], 0\n" \
  "ds_read_b128 v[212:215], v252 offset:9216\n" \
  "v_mfma_f32_16x16x32_bf16 v[192:195], v[128:131], v[216:219], v[192:195]\n" \
  "v_mfma_f32_16x16x32_bf16 v[196:199], v[132:135], v[216:219], v[196:199]\n" \
  "ds_read_b128 v[216:219], v252 offset:10240\n" \
  "v_mfma_f32_16x16x32_bf16 v[200:203], v[128:131], v[220:223], v[200:203]\n" \
  "v_mfma_f32_16x16x32_bf16 v[204:207], v[132:135], v[220:223], v[204:207]\n" \
  "ds_read_b128 v[220:223], v252 offset:11264\n" \
  "v_mfma_f32_16x16x32_bf16 v[192:195], v[4:7], v[224:227], v[192:195]\n" \
  "v_mfma_f32_16x16x32_bf16 v[196:199], v[68:71], v[224:227], v[196:199]\n" \
  "ds_read_b128 v[224:227], v252 offset:12288\n" \
  "v_mfma_f32_16x16x32_bf16 v[200:203], v[4:7], v[228:231], v[200:203]\n" \
  "v_mfma_f32_16x16x32_bf16 v[204:207], v[68:71], v[228:231], v[204:207]\n" \
  "ds_read_b128 v[228:231], v252 offset:13312\n" \
  "s_waitcnt lgkmcnt(7)\n" \
  "v_mfma_f32_16x16x32_bf16 v[192:195], v[240:243], v[232:235], v[192:195]\n" \
  "s_waitcnt lgkmcnt(6)\n" \
  "v_mfma_f32_16x16x32_bf16 v[196:199], v[244:247], v[232:235], v[196:199]\n" \
  "ds_read_b128 v[232:235], v252 offset:14336\n" \
  "v_mfma_f32_16x16x32_bf16 v[200:203], v[240:243], v[236:239], v[200:203]\n" \
  "v_mfma_f32_16x16x32_bf16 v[204:207], v[244:247], v[236:239], v[204:207]\n" \
  "ds_read_b128 v[236:239], v252 offset:15360\n" \
  "ds_read_b128 v[240:243], v250 offset:16384\n" \
  "ds_read_b128 v[244:247], v250 offset:17408\n" \
  "s_waitcnt lgkmcnt(9)\n" \
  "v_mfma_f32_16x16x32_bf16 v[192:195], v[8:11], v[208:211], v[192:195]\n" \
  "v_mfma_f32_16x16x32_bf16 v[196:199], v[72:75], v[208:211], v[196:199]\n" \
  "s_waitcnt lgkmcnt(8)\n" \
  "v_mfma_f32_16x16x32_bf16 v[200:203], v[8:11], v[212:215], v[200:203]\n" \
  "v_mfma_f32_16x16x32_bf16 v[204:207], v[72:75], v[212:215], v[204:207]\n" \
  "s_waitcnt vmcnt(2)\n" \
  "s_waitcnt lgkmcnt(0)\n" \
  "s_barrier\n" \
  "ds_read_b128 v[208:211], v252 offset:16384\n" \
  "ds_read_b128 v[212:215], v252 offset:17408\n" \
  "s_add_u32 m0, %[mw], 0\n" \
  "s_xor_b32 %[cnt], %[sc], 512\n" \
  "buffer_load_dwordx4 v248, %[rx], %[cnt] offen lds\n" \
  "s_add_u32 m0, %[mw], 8192\n" \
  "s_xor_b32 %[cnt], %[sc], 640\n" \
  "buffer_load_dwordx4 v248, %[rx], %[cnt] offen lds\n" \
  "v_mfma_f32_16x16x32_bf16 v[192:195], v[136:139], v[216:219], v[192:195]\n" \
  "v_mfma_f32_16x16x32_bf16 v[196:199], v[140:143], v[216:219], v[196:199]\n" \
  "ds_read_b128 v[216:219], v252 offset:18432\n" \
  "v_mfma_f32_16x16x32_bf16 v[200:203], v[136:139], v[220:223], v[200:203]\n" \
  "v_mfma_f32_16x16x32_bf16 v[204:207], v[140:143], v[220:223], v[204:207]\n" \
  "ds_read_b128 v[220:223], v252 offset:19456\n" \
  "v_mfma_f32_16x16x32_bf16 v[192:195], v[12:15], v[224:227], v[192:195]\n" \
  "v_mfma_f32_16x16x32_bf16 v[196:199], v[76:79], v[224:227], v[196:199]\n" \
  "ds_read_b128 v[224:227], v252 offset:20480\n" \
  "v_mfma_f32_16x16x32_bf16 v[200:203], v[12:15], v[228:231], v[200:203]\n" \
  "v_mfma_f32_16x16x32_bf16 v[204:207], v[76:79], v[228:231], v[204:207]\n" \
  "ds_read_b128 v[228:231], v252 offset:21504\n" \
  "v_mfma_f32_16x16x32_bf16 v[192:195], v[240:243], v[232:235], v[192:195]\n" \
  "v_mfma_f32_16x16x32_bf16 v[196:199], v[244:247], v[232:235], v[196:199]\n" \
  "ds_read_b128 v[232:235], v252 offset:22528\n" \
  "v_mfma_f32_16x16x32_bf16 v[200:203], v[240:243], v[236:239], v[200:203]\n" \
  "v_mfma_f32_16x16x32_bf16 v[204:207], v[244:247], v[236:239], v[204:207]\n" \
  "ds_read_b128 v[236:239], v252 offset:23552\n" \
  "ds_read_b128 v[240:243], v250 offset:32768\n" \
  "ds_read_b128 v[244:247], v250 offset:33792\n" \
  "s_waitcnt lgkmcnt(9)\n" \
  "v_mfma_f32_16x16x32_bf16 v[192:195], v[16:19], v[208:211], v[192:195]\n" \
  "v_mfma_f32_16x16x32_bf16 v[196:199], v[80:83], v[208:211], v[196:199]\n" \
  "ds_read_b128 v[208:211], v252 offset:24576\n" \
  "s_waitcnt lgkmcnt(9)\n" \
  "v_mfma_f32_16x16x32_bf16 v[200:203], v[16:19], v[212:215], v[200:203]\n" \
  "v_mfma_f32_16x16x32_bf16 v[204:207], v[80:83], v[212:215], v[204:207]\n" \
  "ds_read_b128 v[212:215], v252 offset:25600\n" \
  "s_waitcnt lgkmcnt(9)\n" \
  "v_mfma_f32_16x16x32_bf16 v[192:195], v[144:147], v[216:219], v[192:195]\n" \
  "v_mfma_f32_16x16x32_bf16 v[196:199], v[148:151], v[216:219], v[196:199]\n" \
  "ds_read_b128 v[216:219], v252 offset:26624\n" \
  "s_waitcnt lgkmcnt(9)\n" \
  "v_mfma_f32_16x16x32_bf16 v[200:203], v[144:147], v[220:223], v[200:203]\n" \
  "v_mfma_f32_16x16x32_bf16 v[204:207], v[148:151], v[220:223], v[204:207]\n" \
  "ds_read_b128 v[220:223], v252 offset:27648\n" \
  "s_waitcnt lgkmcnt(9)\n" \
  "v_mfma_f32_16x16x32_bf16 v[192:195], v[20:23], v[224:227], v[192:195]\n" \
  "v_mfma_f32_16x16x32_bf16 v[196:199], v[84:87], v[224:227], v[196:199]\n" \
  "ds_read_b128 v[224:227], v252 offset:28672\n" \
  "s_waitcnt lgkmcnt(9)\n" \
  "v_mfma_f32_16x16x32_bf16 v[200:203], v[20:23], v[228:231], v[200:203]\n" \
  "v_mfma_f32_16x16x32_bf16 v[204:207], v[84:87], v[228:231], v[204:207]\n" \
  "ds_read_b128 v[228:231], v252 offset:29696\n" \
  "s_waitcnt lgkmcnt(7)\n" \
  "v_mfma_f32_16x16x32_bf16 v[192:195], v[240:243], v[232:235], v[192:195]\n" \
  "s_waitcnt lgkmcnt(6)\n" \
  "v_mfma_f32_16x16x32_bf16 v[196:199], v[244:247], v[232:235], v[196:199]\n" \
  "ds_read_b128 v[232:235], v252 offset:30720\n" \
  "v_mfma_f32_16x16x32_bf16 v[200:203], v[240:243], v[236:239], v[200:203]\n" \
  "v_mfma_f32_16x16x32_bf16 v[204:207], v[244:247], v[236:239], v[204:207]\n" \
  "ds_read_b128 v[236:239], v252 offset:31744\n" \
  "ds_read_b128 v[240:243], v250 offset:49152\n" \
  "ds_read_b128 v[244:247], v250 offset:50176\n" \
  "s_waitcnt lgkmcnt(9)\n" \
  "v_mfma_f32_16x16x32_bf16 v[192:195], v[24:27], v[208:211], v[192:195]\n" \
  "v_mfma_f32_16x16x32_bf16 v[196:199], v[88:91], v[208:211], v[196:199]\n" \
  "s_waitcnt lgkmcnt(8)\n" \
  "v_mfma_f32_16x16x32_bf16 v[200:203], v[24:27], v[212:215], v[200:203]\n" \
  "v_mfma_f32_16x16x32_bf16 v[204:207], v[88:91], v[212:215], v[204:207]\n" \
  "s_waitcnt vmcnt(0)\n" \
  "s_waitcnt lgkmcnt(0)\n" \
  "s_barrier\n" \
  "ds_read_b128 v[208:211], v252 offset:0\n" \
  "ds_read_b128 v[212:215], v252 offset:1024\n" \
  "s_add_u32 m0, %[mw], 16384\n" \
  "s_xor_b32 %[cnt], %[sc], 768\n" \
  "buffer_load_dwordx4 v248, %[rx], %[cnt] offen lds\n" \
  "s_add_u32 m0, %[mw], 24576\n" \
  "s_xor_b32 %[cnt], %[sc], 896\n" \
  "buffer_load_dwordx4 v248, %[rx], %[cnt] offen lds\n" \
  "v_lshl_or_b32 v253, v251, 24, %[fm]\n" \
  "buffer_store_dword v253, v253, %[rs], %[fo] offen sc1\n" \
  "v_mfma_f32_16x16x32_bf16 v[192:195], v[152:155], v[216:219], v[192:195]\n" \
  "v_mfma_f32_16x16x32_bf16 v[196:199], v[156:159], v[216:219], v[196:199]\n" \
  "ds_read_b128 v[216:219], v252 offset:2048\n" \
  "v_mfma_f32_16x16x32_bf16 v[200:203], v[152:155], v[220:223], v[200:203]\n" \
  "v_mfma_f32_16x16x32_bf16 v[204:207], v[156:159], v[220:223], v[204:207]\n" \
  "ds_read_b128 v[220:223], v252 offset:3072\n" \
  "v_mfma_f32_16x16x32_bf16 v[192:195], v[28:31], v[224:227], v[192:195]\n" \
  "v_mfma_f32_16x16x32_bf16 v[196:199], v[92:95], v[224:227], v[196:199]\n" \
  "ds_read_b128 v[224:227], v252 offset:4096\n" \
  "v_mfma_f32_16x16x32_bf16 v[200:203], v[28:31], v[228:231], v[200:203]\n" \
  "v_mfma_f32_16x16x32_bf16 v[204:207], v[92:95], v[228:231], v[204:207]\n" \
  "ds_read_b128 v[228:231], v252 offset:5120\n" \
  "v_mfma_f32_16x16x32_bf16 v[192:195], v[240:243], v[232:235], v[192:195]\n" \
  "v_mfma_f32_16x16x32_bf16 v[196:199], v[244:247], v[232:235], v[196:199]\n" \
  "ds_read_b128 v[232:235], v252 offset:6144\n" \
  "v_mfma_f32_16x16x32_bf16 v[200:203], v[240:243], v[236:239], v[200:203]\n" \
  "v_mfma_f32_16x16x32_bf16 v[204:207], v[244:247], v[236:239], v[204:207]\n" \
  "ds_read_b128 v[236:239], v252 offset:7168\n" \
  "ds_read_b128 v[240:243], v249 offset:0\n" \
  "ds_read_b128 v[244:247], v249 offset:1024\n" \
  "s_waitcnt lgkmcnt(9)\n" \
  "v_mfma_f32_16x16x32_bf16 v[192:195], v[32:35], v[208:211], v[192:195]\n" \
  "v_mfma_f32_16x16x32_bf16 v[196:199], v[96:99], v[208:211], v[196:199]\n" \
  "ds_read_b128 v[208:211], v252 offset:8192\n" \
  "s_waitcnt lgkmcnt(9)\n" \
  "v_mfma_f32_16x16x32_bf16 v[200:203], v[32:35], v[212:215], v[200:203]\n" \
  "v_mfma_f32_16x16x32_bf16 v[204:207], v[96:99], v[212:215], v[204:207]\n" \
  "ds_read_b128 v[212:215], v252 offset:9216\n" \
  "s_waitcnt lgkmcnt(9)\n" \
  "v_mfma_f32_16x16x32_bf16 v[192:195], v[160:163], v[216:219], v[192:195]\n" \
  "v_mfma_f32_16x16x32_bf16 v[196:199], v[164:167], v[216:219], v[196:199]\n" \
  "ds_read_b128 v[216:219], v252 offset:10240\n" \
  "s_waitcnt lgkmcnt(9)\n" \
  "v_mfma_f32_16x16x32_bf16 v[200:203], v[160:163], v[220:223], v[200:203]\n" \
  "v_mfma_f32_16x16x32_bf16 v[204:207], v[164:167], v[220:223], v[204:207]\n" \
  "ds_read_b128 v[220:223], v252 offset:11264\n" \
  "s_waitcnt lgkmcnt(9)\n" \
  "v_mfma_f32_16x16x32_bf16 v[192:195], v[36:39], v[224:227], v[192:195]\n" \
  "v_mfma_f32_16x16x32_bf16 v[196:199], v[100:103], v[224:227], v[196:199]\n" \
  "ds_read_b128 v[224:227], v252 offset:12288\n" \
  "s_waitcnt lgkmcnt(9)\n" \
  "v_mfma_f32_16x16x32_bf16 v[200:203], v[36:39], v[228:231], v[200:203]\n" \
  "v_mfma_f32_16x16x32_bf16 v[204:207], v[100:103], v[228:231], v[204:207]\n" \
  "ds_read_b128 v[228:231], v252 offset:13312\n" \
  "s_waitcnt lgkmcnt(7)\n" \
  "v_mfma_f32_16x16x32_bf16 v[192:195], v[240:243], v[232:235], v[192:195]\n" \
  "s_waitcnt lgkmcnt(6)\n" \
  "v_mfma_f32_16x16x32_bf16 v[196:199], v[244:247], v[232:235], v[196:199]\n" \
  "ds_read_b128 v[232:235], v252 offset:14336\n" \
  "v_mfma_f32_16x16x32_bf16 v[200:203], v[240:243], v[236:239], v[200:203]\n" \
  "v_mfma_f32_16x16x32_bf16 v[204:207], v[244:247], v[236:239], v[204:207]\n" \
  "ds_read_b128 v[236:239], v252 offset:15360\n" \
  "ds_read_b128 v[240:243], v249 offset:16384\n" \
  "ds_read_b128 v[244:247], v249 offset:17408\n" \
  "s_waitcnt lgkmcnt(9)\n" \
  "v_mfma_f32_16x16x32_bf16 v[192:195], v[40:43], v[208:211], v[192:195]\n" \
  "v_mfma_f32_16x16x32_bf16 v[196:199], v[104:107], v[208:211], v[196:199]\n" \
  "s_waitcnt lgkmcnt(8)\n" \
  "v_mfma_f32_16x16x32_bf16 v[200:203], v[40:43], v[212:215], v[200:203]\n" \
  "v_mfma_f32_16x16x32_bf16 v[204:207], v[104:107], v[212:215], v[204:207]\n" \
  "s_waitcnt vmcnt(1)\n" \
  "s_waitcnt lgkmcnt(0)\n" \
  "s_barrier\n" \
  "ds_read_b128 v[208:211], v252 offset:16384\n" \
  "ds_read_b128 v[212:215], v252 offset:17408\n" \
  "s_add_u32 m0, %[mw], 0\n" \
  "s_xor_b32 %[cnt], %[sn], 0\n" \
  "buffer_load_dwordx4 v248, %[rx], %[cnt] offen lds\n" \
  "s_add_u32 m0, %[mw], 8192\n" \
  "s_xor_b32 %[cnt], %[sn], 128\n" \
  "buffer_load_dwordx4 v248, %[rx], %[cnt] offen lds\n" \
  "v_lshl_or_b32 v253, v251, 3, %[tm]\n" \
  "buffer_load_dword v253, v253, %[rx], %[tc] offen\n" \
  "v_mfma_f32_16x16x32_bf16 v[192:195], v[168:171], v[216:219], v[192:195]\n" \
  "v_mfma_f32_16x16x32_bf16 v[196:199], v[172:175], v[216:219], v[196:199]\n" \
  "ds_read_b128 v[216:219], v252 offset:18432\n" \
  "v_mfma_f32_16x16x32_bf16 v[200:203], v[168:171], v[220:223], v[200:203]\n" \
  "v_mfma_f32_16x16x32_bf16 v[204:207], v[172:175], v[220:223], v[204:207]\n" \
  "ds_read_b128 v[220:223], v252 offset:19456\n" \
  "v_mfma_f32_16x16x32_bf16 v[192:195], v[44:47], v[224:227], v[192:195]\n" \
  "v_mfma_f32_16x16x32_bf16 v[196:199], v[108:111], v[224:227], v[196:199]\n" \
  "ds_read_b128 v[224:227], v252 offset:20480\n" \
  "v_mfma_f32_16x16x32_bf16 v[200:203], v[44:47], v[228:231], v[200:203]\n" \
  "v_mfma_f32_16x16x32_bf16 v[204:207], v[108:111], v[228:231], v[204:207]\n" \
  "ds_read_b128 v[228:231], v252 offset:21504\n" \
  "v_mfma_f32_16x16x32_bf16 v[192:195], v[240:243], v[232:235], v[192:195]\n" \
  "v_mfma_f32_16x16x32_bf16 v[196:199], v[244:247], v[232:235], v[196:199]\n" \
  "ds_read_b128 v[232:235], v252 offset:22528\n" \
  "v_mfma_f32_16x16x32_bf16 v[200:203], v[240:243], v[236:239], v[200:203]\n" \
  "v_mfma_f32_16x16x32_bf16 v[204:207], v[244:247], v[236:239], v[204:207]\n" \
  "ds_read_b128 v[236:239], v252 offset:23552\n" \
  "ds_read_b128 v[240:243], v249 offset:32768\n" \
  "ds_read_b128 v[244:247], v249 offset:33792\n" \
  "s_waitcnt lgkmcnt(9)\n" \
  "v_mfma_f32_16x16x32_bf16 v[192:195], v[48:51], v[208:211], v[192:195]\n" \
  "v_mfma_f32_16x16x32_bf16 v[196:199], v[112:115], v[208:211], v[196:199]\n" \
  "ds_read_b128 v[208:211], v252 offset:24576\n" \
  "s_waitcnt lgkmcnt(9)\n" \
  "v_mfma_f32_16x16x32_bf16 v[200:203], v[48:51], v[212:215], v[200:203]\n" \
  "v_mfma_f32_16x16x32_bf16 v[204:207], v[112:115], v[212:215], v[204:207]\n" \
  "ds_read_b128 v[212:215], v252 offset:25600\n" \
  "s_waitcnt lgkmcnt(9)\n" \
  "v_mfma_f32_16x16x32_bf16 v[192:195], v[176:179], v[216:219], v[192:195]\n" \
  "v_mfma_f32_16x16x32_bf16 v[196:199], v[180:183], v[216:219], v[196:199]\n" \
  "ds_read_b128 v[216:219], v252 offset:26624\n" \
  "s_waitcnt lgkmcnt(9)\n" \
  "v_mfma_f32_16x16x32_bf16 v[200:203], v[176:179], v[220:223], v[200:203]\n" \
  "v_mfma_f32_16x16x32_bf16 v[204:207], v[180:183], v[220:223], v[204:207]\n" \
  "ds_read_b128 v[220:223], v252 offset:27648\n" \
  "s_waitcnt lgkmcnt(9)\n" \
  "v_mfma_f32_16x16x32_bf16 v[192:195], v[52:55], v[224:227], v[192:195]\n" \
  "v_mfma_f32_16x16x32_bf16 v[196:199], v[116:119], v[224:227], v[196:199]\n" \
  "ds_read_b128 v[224:227], v252 offset:28672\n" \
  "s_waitcnt lgkmcnt(9)\n" \
  "v_mfma_f32_16x16x32_bf16 v[200:203], v[52:55], v[228:231], v[200:203]\n" \
  "v_mfma_f32_16x16x32_bf16 v[204:207], v[116:119], v[228:231], v[204:207]\n" \
  "ds_read_b128 v[228:231], v252 offset:29696\n" \
  "s_waitcnt lgkmcnt(7)\n" \
  "v_mfma_f32_16x16x32_bf16 v[192:195], v[240:243], v[232:235], v[192:195]\n" \
  "s_waitcnt lgkmcnt(6)\n" \
  "v_mfma_f32_16x16x32_bf16 v[196:199], v[244:247], v[232:235], v[196:199]\n" \
  "ds_read_b128 v[232:235], v252 offset:30720\n" \
  "v_mfma_f32_16x16x32_bf16 v[200:203], v[240:243], v[236:239], v[200:203]\n" \
  "v_mfma_f32_16x16x32_bf16 v[204:207], v[244:247], v[236:239], v[204:207]\n" \
  "ds_read_b128 v[236:239], v252 offset:31744\n" \
  "ds_read_b128 v[240:243], v249 offset:49152\n" \
  "ds_read_b128 v[244:247], v249 offset:50176\n" \
  "s_waitcnt lgkmcnt(9)\n" \
  "v_mfma_f32_16x16x32_bf16 v[192:195], v[56:59], v[208:211], v[192:195]\n" \
  "v_mfma_f32_16x16x32_bf16 v[196:199], v[120:123], v[208:211], v[196:199]\n" \
  "s_waitcnt lgkmcnt(8)\n" \
  "v_mfma_f32_16x16x32_bf16 v[200:203], v[56:59], v[212:215], v[200:203]\n" \
  "v_mfma_f32_16x16x32_bf16 v[204:207], v[120:123], v[212:215], v[204:207]\n" \
  "s_waitcnt vmcnt(1)\n" \
  "s_waitcnt lgkmcnt(0)\n" \
  "s_barrier\n" \
  "ds_read_b128 v[208:211], v252 offset:0\n" \
  "ds_read_b128 v[212:215], v252 offset:1024\n" \
  "s_add_u32 m0, %[mw], 16384\n" \
  "s_xor_b32 %[cnt], %[sn], 256\n" \
  "buffer_load_dwordx4 v248, %[rx], %[cnt] offen lds\n" \
  "s_add_u32 m0, %[mw], 24576\n" \
  "s_xor_b32 %[cnt], %[sn], 384\n" \
  "buffer_load_dwordx4 v248, %[rx], %[cnt] offen lds\n" \
  "v_mfma_f32_16x16x32_bf16 v[192:195], v[184:187], v[216:219], v[192:195]\n" \
  "v_mfma_f32_16x16x32_bf16 v[196:199], v[188:191], v[216:219], v[196:199]\n" \
  "ds_read_b128 v[216:219], v252 offset:2048\n" \
  "v_mfma_f32_16x16x32_bf16 v[200:203], v[184:187], v[220:223], v[200:203]\n" \
  "v_mfma_f32_16x16x32_bf16 v[204:207], v[188:191], v[220:223], v[204:207]\n" \
  "ds_read_b128 v[220:223], v252 offset:3072\n" \
  "v_mfma_f32_16x16x32_bf16 v[192:195], v[60:63], v[224:227], v[192:195]\n" \
  "v_mfma_f32_16x16x32_bf16 v[196:199], v[124:127], v[224:227], v[196:199]\n" \
  "ds_read_b128 v[224:227], v252 offset:4096\n" \
  "v_mfma_f32_16x16x32_bf16 v[200:203], v[60:63], v[228:231], v[200:203]\n" \
  "v_mfma_f32_16x16x32_bf16 v[204:207], v[124:127], v[228:231], v[204:207]\n" \
  "ds_read_b128 v[228:231], v252 offset:5120\n" \
  "v_mfma_f32_16x16x32_bf16 v[192:195], v[240:243], v[232:235], v[192:195]\n" \
  "v_mfma_f32_16x16x32_bf16 v[196:199], v[244:247], v[232:235], v[196:199]\n" \
  "ds_read_b128 v[232:235], v252 offset:6144\n" \
  "v_mfma_f32_16x16x32_bf16 v[200:203], v[240:243], v[236:239], v[200:203]\n" \
  "v_mfma_f32_16x16x32_bf16 v[204:207], v[244:247], v[236:239], v[204:207]\n" \
  "ds_read_b128 v[236:239], v252 offset:7168\n" \
  "v_cvt_pk_bf16_f32 v192, v192, v193\n" \
  "v_cvt_pk_bf16_f32 v193, v194, v195\n" \
  "v_cvt_pk_bf16_f32 v194, v196, v197\n" \
  "v_cvt_pk_bf16_f32 v195, v198, v199\n" \
  "buffer_store_dwordx4 v[192:195], v251, %[rs], %[so] offen sc1\n" \
  "v_cvt_pk_bf16_f32 v200, v200, v201\n" \
  "v_cvt_pk_bf16_f32 v201, v202, v203\n" \
  "s_nop 0\n" \
  "v_cvt_pk_bf16_f32 v202, v204, v205\n" \
  "v_cvt_pk_bf16_f32 v203, v206, v207\n" \
  "s_add_u32 %[cnt], %[so], %c[rt]\n" \
  "buffer_store_dwordx4 v[200:203], v251, %[rs], %[cnt] offen sc1\n" \
  "s_mov_b32 m0, %[keep]\n" \
  "s_waitcnt lgkmcnt(0)\n"
#define ST_XA \
  "s_mov_b32 %[keep], m0\n" \
  "ds_read_b128 v[240:243], v250 offset:0\n" \
  "ds_read_b128 v[244:247], v250 offset:1024\n" \
  "v_mfma_f32_16x16x32_bf16 v[192:195], v[0:3], v[208:211], 0\n" \
  "v_mfma_f32_16x16x32_bf16 v[196:199], v[64:67], v[208:211], 0\n" \
  "ds_read_b128 v[208:211], v252 offset:8192\n" \
  "v_mfma_f32_16x16x32_bf16 v[200:203], v[0:3], v[212:215], 0\n" \
  "v_mfma_f32_16x16x32_bf16 v[204:207], v[64:67], v[212:215], 0\n" \
  "ds_read_b128 v[212:215], v252 offset:9216\n" \
  "v_mfma_f32_16x16x32_bf16 v[192:195], v[128:131], v[216:219], v[192:195]\n" \
  "v_mfma_f32_16x16x32_bf16 v[196:199], v[132:135], v[216:219], v[196:199]\n" \
  "ds_read_b128 v[216:219], v252 offset:10240\n" \
  "v_mfma_f32_16x16x32_bf16 v[200:203], v[128:131], v[220:223], v[200:203]\n" \
  "v_mfma_f32_16x16x32_bf16 v[204:207], v[132:135], v[220:223], v[204:207]\n" \
  "ds_read_b128 v[220:223], v252 offset:11264\n" \
  "v_mfma_f32_16x16x32_bf16 v[192:195], v[4:7], v[224:227], v[192:195]\n" \
  "v_mfma_f32_16x16x32_bf16 v[196:199], v[68:71], v[224:227], v[196:199]\n" \
  "v_mfma_f32_16x16x32_bf16 v[200:203], v[4:7], v[228:231], v[200:203]\n" \
  "v_mfma_f32_16x16x32_bf16 v[204:207], v[68:71], v[228:231], v[204:207]\n" \
  "s_waitcnt vmcnt(0)\n" \
  "s_waitcnt lgkmcnt(0)\n" \
  "s_barrier\n" \
  "ds_read_b128 v[224:227], v252 offset:12288\n" \
  "ds_read_b128 v[228:231], v252 offset:13312\n" \
  "s_add_u32 m0, %[mw], 0\n" \
  "s_xor_b32 %[cnt], %[sc], 384\n" \
  "buffer_load_dwordx4 v248, %[rx], %[cnt] offen lds\n" \
  "s_add_u32 m0, %[mw], 4096\n" \
  "s_xor_b32 %[cnt], %[sc], 448\n" \
  "buffer_load_dwordx4 v248, %[rx], %[cnt] offen lds\n" \
  "v_mfma_f32_16x16x32_bf16 v[192:195], v[240:243], v[232:235], v[192:195]\n" \
  "v_mfma_f32_16x16x32_bf16 v[196:199], v[244:247], v[232:235], v[196:199]\n" \
  "ds_read_b128 v[232:235], v252 offset:14336\n" \
  "v_mfma_f32_16x16x32_bf16 v[200:203], v[240:243], v[236:239], v[200:203]\n" \
  "v_mfma_f32_16x16x32_bf16 v[204:207], v[244:247], v[236:239], v[204:207]\n" \
  "ds_read_b128 v[236:239], v252 offset:15360\n" \
  "ds_read_b128 v[240:243], v250 offset:16384\n" \
  "ds_read_b128 v[244:247], v250 offset:17408\n" \
  "v_mfma_f32_16x16x32_bf16 v[192:195], v[8:11], v[208:211], v[192:195]\n" \
  "v_mfma_f32_16x16x32_bf16 v[196:199], v[72:75], v[208:211], v[196:199]\n" \
  "ds_read_b128 v[208:211], v252 offset:16384\n" \
  "v_mfma_f32_16x16x32_bf16 v[200:203], v[8:11], v[212:215], v[200:203]\n" \
  "v_mfma_f32_16x16x32_bf16 v[204:207], v[72:75], v[212:215], v[204:207]\n" \
  "ds_read_b128 v[212:215], v252 offset:17408\n" \
  "v_mfma_f32_16x16x32_bf16 v[192:195], v[136:139], v[216:219], v[192:195]\n" \
  "v_mfma_f32_16x16x32_bf16 v[196:199], v[140:143], v[216:219], v[196:199]\n" \
  "ds_read_b128 v[216:219], v252 offset:18432\n" \
  "v_mfma_f32_16x16x32_bf16 v[200:203], v[136:139], v[220:223], v[200:203]\n" \
  "v_mfma_f32_16x16x32_bf16 v[204:207], v[140:143], v[220:223], v[204:207]\n" \
  "ds_read_b128 v[220:223], v252 offset:19456\n" \
  "s_waitcnt lgkmcnt(9)\n" \
  "v_mfma_f32_16x16x32_bf16 v[192:195], v[12:15], v[224:227], v[192:195]\n" \
  "v_mfma_f32_16x16x32_bf16 v[196:199], v[76:79], v[224:227], v[196:199]\n" \
  "ds_read_b128 v[224:227], v252 offset:20480\n" \
  "s_waitcnt lgkmcnt(9)\n" \
  "v_mfma_f32_16x16x32_bf16 v[200:203], v[12:15], v[228:231], v[200:203]\n" \
  "v_mfma_f32_16x16x32_bf16 v[204:207], v[76:79], v[228:231], v[204:207]\n" \
  "ds_read_b128 v[228:231], v252 offset:21504\n" \
  "s_waitcnt lgkmcnt(7)\n" \
  "v_mfma_f32_16x16x32_bf16 v[192:195], v[240:243], v[232:235], v[192:195]\n" \
  "s_waitcnt lgkmcnt(6)\n" \
  "v_mfma_f32_16x16x32_bf16 v[196:199], v[244:247], v[232:235], v[196:199]\n" \
  "ds_read_b128 v[232:235], v252 offset:22528\n" \
  "v_mfma_f32_16x16x32_bf16 v[200:203], v[240:243], v[236:239], v[200:203]\n" \
  "v_mfma_f32_16x16x32_bf16 v[204:207], v[244:247], v[236:239], v[204:207]\n" \
  "ds_read_b128 v[236:239], v252 offset:23552\n" \
  "ds_read_b128 v[240:243], v250 offset:32768\n" \
  "ds_read_b128 v[244:247], v250 offset:33792\n" \
  "s_waitcnt lgkmcnt(9)\n" \
  "v_mfma_f32_16x16x32_bf16 v[192:195], v[16:19], v[208:211], v[192:195]\n" \
  "v_mfma_f32_16x16x32_bf16 v[196:199], v[80:83], v[208:211], v[196:199]\n" \
  "s_waitcnt lgkmcnt(8)\n" \
  "v_mfma_f32_16x16x32_bf16 v[200:203], v[16:19], v[212:215], v[200:203]\n" \
  "v_mfma_f32_16x16x32_bf16 v[204:207], v[80:83], v[212:215], v[204:207]\n" \
  "s_waitcnt vmcnt(0)\n" \
  "s_waitcnt lgkmcnt(0)\n" \
  "s_barrier\n" \
  "ds_read_b128 v[208:211], v252 offset:0\n" \
  "ds_read_b128 v[212:215], v252 offset:1024\n" \
  "s_add_u32 m0, %[mw], 12288\n" \
  "s_xor_b32 %[cnt], %[sc], 512\n" \
  "buffer_load_dwordx4 v248, %[rx], %[cnt] offen lds\n" \
  "s_add_u32 m0, %[mw], 16384\n" \
  "s_xor_b32 %[cnt], %[sc], 576\n" \
  "buffer_load_dwordx4 v248, %[rx], %[cnt] offen lds\n" \
  "s_add_u32 m0, %[mw], 20480\n" \
  "s_xor_b32 %[cnt], %[sc], 640\n" \
  "buffer_load_dwordx4 v248, %[rx], %[cnt] offen lds\n" \
  "v_mfma_f32_16x16x32_bf16 v[192:195], v[144:147], v[216:219], v[192:195]\n" \
  "v_mfma_f32_16x16x32_bf16 v[196:199], v[148:151], v[216:219], v[196:199]\n" \
  "ds_read_b128 v[216:219], v252 offset:2048\n" \
  "v_mfma_f32_16x16x32_bf16 v[200:203], v[144:147], v[220:223], v[200:203]\n" \
  "v_mfma_f32_16x16x32_bf16 v[204:207], v[148:151], v[220:223], v[204:207]\n" \
  "ds_read_b128 v[220:223], v252 offset:3072\n" \
  "v_mfma_f32_16x16x32_bf16 v[192:195], v[20:23], v[224:227], v[192:195]\n" \
  "v_mfma_f32_16x16x32_bf16 v[196:199], v[84:87], v[224:227], v[196:199]\n" \
  "ds_read_b128 v[224:227], v252 offset:4096\n" \
  "v_mfma_f32_16x16x32_bf16 v[200:203], v[20:23], v[228:231], v[200:203]\n" \
  "v_mfma_f32_16x16x32_bf16 v[204:207], v[84:87], v[228:231], v[204:207]\n" \
  "ds_read_b128 v[228:231], v252 offset:5120\n" \
  "v_mfma_f32_16x16x32_bf16 v[192:195], v[240:243], v[232:235], v[192:195]\n" \
  "v_mfma_f32_16x16x32_bf16 v[196:199], v[244:247], v[232:235], v[196:199]\n" \
  "ds_read_b128 v[232:235], v252 offset:6144\n" \
  "v_mfma_f32_16x16x32_bf16 v[200:203], v[240:243], v[236:239], v[200:203]\n" \
  "v_mfma_f32_16x16x32_bf16 v[204:207], v[244:247], v[236:239], v[204:207]\n" \
  "ds_read_b128 v[236:239], v252 offset:7168\n" \
  "ds_read_b128 v[240:243], v250 offset:49152\n" \
  "ds_read_b128 v[244:247], v250 offset:50176\n" \
  "s_waitcnt lgkmcnt(9)\n" \
  "v_mfma_f32_16x16x32_bf16 v[192:195], v[24:27], v[208:211], v[192:195]\n" \
  "v_mfma_f32_16x16x32_bf16 v[196:199], v[88:91], v[208:211], v[196:199]\n" \
  "s_waitcnt lgkmcnt(8)\n" \
  "v_mfma_f32_16x16x32_bf16 v[200:203], v[24:27], v[212:215], v[200:203]\n" \
  "v_mfma_f32_16x16x32_bf16 v[204:207], v[88:91], v[212:215], v[204:207]\n" \
  "s_waitcnt vmcnt(0)\n" \
  "s_waitcnt lgkmcnt(0)\n" \
  "s_barrier\n" \
  "ds_read_b128 v[208:211], v252 offset:12288\n" \
  "ds_read_b128 v[212:215], v252 offset:13312\n" \
  "s_add_u32 m0, %[mw], 0\n" \
  "s_xor_b32 %[cnt], %[sc], 704\n" \
  "buffer_load_dwordx4 v248, %[rx], %[cnt] offen lds\n" \
  "s_add_u32 m0, %[mw], 4096\n" \
  "s_xor_b32 %[cnt], %[sc], 768\n" \
  "buffer_load_dwordx4 v248, %[rx], %[cnt] offen lds\n" \
  "s_add_u32 m0, %[mw], 8192\n" \
  "s_xor_b32 %[cnt], %[sc], 832\n" \
  "buffer_load_dwordx4 v248, %[rx], %[cnt] offen lds\n" \
  "v_lshl_or_b32 v253, v251, 3, %[tm]\n" \
  "buffer_load_dword v253, v253, %[rx], %[tc] offen\n" \
  "v_mfma_f32_16x16x32_bf16 v[192:195], v[152:155], v[216:219], v[192:195]\n" \
  "v_mfma_f32_16x16x32_bf16 v[196:199], v[156:159], v[216:219], v[196:199]\n" \
  "ds_read_b128 v[216:219], v252 offset:14336\n" \
  "v_mfma_f32_16x16x32_bf16 v[200:203], v[152:155], v[220:223], v[200:203]\n" \
  "v_mfma_f32_16x16x32_bf16 v[204:207], v[156:159], v[220:223], v[204:207]\n" \
  "ds_read_b128 v[220:223], v252 offset:15360\n" \
  "v_mfma_f32_16x16x32_bf16 v[192:195], v[28:31], v[224:227], v[192:195]\n" \
  "v_mfma_f32_16x16x32_bf16 v[196:199], v[92:95], v[224:227], v[196:199]\n" \
  "ds_read_b128 v[224:227], v252 offset:16384\n" \
  "v_mfma_f32_16x16x32_bf16 v[200:203], v[28:31], v[228:231], v[200:203]\n" \
  "v_mfma_f32_16x16x32_bf16 v[204:207], v[92:95], v[228:231], v[204:207]\n" \
  "ds_read_b128 v[228:231], v252 offset:17408\n" \
  "v_mfma_f32_16x16x32_bf16 v[192:195], v[240:243], v[232:235], v[192:195]\n" \
  "v_mfma_f32_16x16x32_bf16 v[196:199], v[244:247], v[232:235], v[196:199]\n" \
  "ds_read_b128 v[232:235], v252 offset:18432\n" \
  "v_mfma_f32_16x16x32_bf16 v[200:203], v[240:243], v[236:239], v[200:203]\n" \
  "v_mfma_f32_16x16x32_bf16 v[204:207], v[244:247], v[236:239], v[204:207]\n" \
  "ds_read_b128 v[236:239], v252 offset:19456\n" \
  "ds_read_b128 v[240:243], v249 offset:0\n" \
  "ds_read_b128 v[244:247], v249 offset:1024\n" \
  "s_waitcnt lgkmcnt(9)\n" \
  "v_mfma_f32_16x16x32_bf16 v[192:195], v[32:35], v[208:211], v[192:195]\n" \
  "v_mfma_f32_16x16x32_bf16 v[196:199], v[96:99], v[208:211], v[196:199]\n" \
  "ds_read_b128 v[208:211], v252 offset:20480\n" \
  "s_waitcnt lgkmcnt(9)\n" \
  "v_mfma_f32_16x16x32_bf16 v[200:203], v[32:35], v[212:215], v[200:203]\n" \
  "v_mfma_f32_16x16x32_bf16 v[204:207], v[96:99], v[212:215], v[204:207]\n" \
  "ds_read_b128 v[212:215], v252 offset:21504\n" \
  "s_waitcnt lgkmcnt(9)\n" \
  "v_mfma_f32_16x16x32_bf16 v[192:195], v[160:163], v[216:219], v[192:195]\n" \
  "v_mfma_f32_16x16x32_bf16 v[196:199], v[164:167], v[216:219], v[196:199]\n" \
  "ds_read_b128 v[216:219], v252 offset:22528\n" \
  "s_waitcnt lgkmcnt(9)\n" \
  "v_mfma_f32_16x16x32_bf16 v[200:203], v[160:163], v[220:223], v[200:203]\n" \
  "v_mfma_f32_16x16x32_bf16 v[204:207], v[164:167], v[220:223], v[204:207]\n" \
  "ds_read_b128 v[220:223], v252 offset:23552\n" \
  "s_waitcnt lgkmcnt(9)\n" \
  "v_mfma_f32_16x16x32_bf16 v[192:195], v[36:39], v[224:227], v[192:195]\n" \
  "v_mfma_f32_16x16x32_bf16 v[196:199], v[100:103], v[224:227], v[196:199]\n" \
  "s_waitcnt lgkmcnt(8)\n" \
  "v_mfma_f32_16x16x32_bf16 v[200:203], v[36:39], v[228:231], v[200:203]\n" \
  "v_mfma_f32_16x16x32_bf16 v[204:207], v[100:103], v[228:231], v[204:207]\n" \
  "s_waitcnt vmcnt(1)\n" \
  "s_waitcnt lgkmcnt(0)\n" \
  "s_barrier\n" \
  "ds_read_b128 v[224:227], v252 offset:0\n" \
  "ds_read_b128 v[228:231], v252 offset:1024\n" \
  "s_add_u32 m0, %[mw], 12288\n" \
  "s_xor_b32 %[cnt], %[sc], 896\n" \
  "buffer_load_dwordx4 v248, %[rx], %[cnt] offen lds\n" \
  "s_add_u32 m0, %[mw], 16384\n" \
  "s_xor_b32 %[cnt], %[sc], 960\n" \
  "buffer_load_dwordx4 v248, %[rx], %[cnt] offen lds\n" \
  "v_mfma_f32_16x16x32_bf16 v[192:195], v[240:243], v[232:235], v[192:195]\n" \
  "v_mfma_f32_16x16x32_bf16 v[196:199], v[244:247], v[232:235], v[196:199]\n" \
  "ds_read_b128 v[232:235], v252 offset:2048\n" \
  "v_mfma_f32_16x16x32_bf16 v[200:203], v[240:243], v[236:239], v[200:203]\n" \
  "v_mfma_f32_16x16x32_bf16 v[204:207], v[244:247], v[236:239], v[204:207]\n" \
  "ds_read_b128 v[236:239], v252 offset:3072\n" \
  "ds_read_b128 v[240:243], v249 offset:16384\n" \
  "ds_read_b128 v[244:247], v249 offset:17408\n" \
  "v_mfma_f32_16x16x32_bf16 v[192:195], v[40:43], v[208:211], v[192:195]\n" \
  "v_mfma_f32_16x16x32_bf16 v[196:199], v[104:107], v[208:211], v[196:199]\n" \
  "ds_read_b128 v[208:211], v252 offset:4096\n" \
  "v_mfma_f32_16x16x32_bf16 v[200:203], v[40:43], v[212:215], v[200:203]\n" \
  "v_mfma_f32_16x16x32_bf16 v[204:207], v[104:107], v[212:215], v[204:207]\n" \
  "ds_read_b128 v[212:215], v252 offset:5120\n" \
  "v_mfma_f32_16x16x32_bf16 v[192:195], v[168:171], v[216:219], v[192:195]\n" \
  "v_mfma_f32_16x16x32_bf16 v[196:199], v[172:175], v[216:219], v[196:199]\n" \
  "ds_read_b128 v[216:219], v252 offset:6144\n" \
  "v_mfma_f32_16x16x32_bf16 v[200:203], v[168:171], v[220:223], v[200:203]\n" \
  "v_mfma_f32_16x16x32_bf16 v[204:207], v[172:175], v[220:223], v[204:207]\n" \
  "ds_read_b128 v[220:223], v252 offset:7168\n" \
  "s_waitcnt lgkmcnt(9)\n" \
  "v_mfma_f32_16x16x32_bf16 v[192:195], v[44:47], v[224:227], v[192:195]\n" \
  "v_mfma_f32_16x16x32_bf16 v[196:199], v[108:111], v[224:227], v[196:199]\n" \
  "ds_read_b128 v[224:227], v252 offset:8192\n" \
  "s_waitcnt lgkmcnt(9)\n" \
  "v_mfma_f32_16x16x32_bf16 v[200:203], v[44:47], v[228:231], v[200:203]\n" \
  "v_mfma_f32_16x16x32_bf16 v[204:207], v[108:111], v[228:231], v[204:207]\n" \
  "ds_read_b128 v[228:231], v252 offset:9216\n" \
  "s_waitcnt lgkmcnt(7)\n" \
  "v_mfma_f32_16x16x32_bf16 v[192:195], v[240:243], v[232:235], v[192:195]\n" \
  "s_waitcnt lgkmcnt(6)\n" \
  "v_mfma_f32_16x16x32_bf16 v[196:199], v[244:247], v[232:235], v[196:199]\n" \
  "ds_read_b128 v[232:235], v252 offset:10240\n" \
  "v_mfma_f32_16x16x32_bf16 v[200:203], v[240:243], v[236:239], v[200:203]\n" \
  "v_mfma_f32_16x16x32_bf16 v[204:207], v[244:247], v[236:239], v[204:207]\n" \
  "ds_read_b128 v[236:239], v252 offset:11264\n" \
  "ds_read_b128 v[240:243], v249 offset:32768\n" \
  "ds_read_b128 v[244:247], v249 offset:33792\n" \
  "s_waitcnt lgkmcnt(9)\n" \
  "v_mfma_f32_16x16x32_bf16 v[192:195], v[48:51], v[208:211], v[192:195]\n" \
  "v_mfma_f32_16x16x32_bf16 v[196:199], v[112:115], v[208:211], v[196:199]\n" \
  "s_waitcnt lgkmcnt(8)\n" \
  "v_mfma_f32_16x16x32_bf16 v[200:203], v[48:51], v[212:215], v[200:203]\n" \
  "v_mfma_f32_16x16x32_bf16 v[204:207], v[112:115], v[212:215], v[204:207]\n" \
  "s_waitcnt vmcnt(0)\n" \
  "s_waitcnt lgkmcnt(0)\n" \
  "s_barrier\n" \
  "ds_read_b128 v[208:211], v252 offset:12288\n" \
  "ds_read_b128 v[212:215], v252 offset:13312\n" \
  "s_add_u32 m0, %[mw], 0\n" \
  "s_xor_b32 %[cnt], %[sn], 0\n" \
  "buffer_load_dwordx4 v248, %[rx], %[cnt] offen lds\n" \
  "s_add_u32 m0, %[mw], 4096\n" \
  "s_xor_b32 %[cnt], %[sn], 64\n" \
  "buffer_load_dwordx4 v248, %[rx], %[cnt] offen lds\n" \
  "s_add_u32 m0, %[mw], 8192\n" \
  "s_xor_b32 %[cnt], %[sn], 128\n" \
  "buffer_load_dwordx4 v248, %[rx], %[cnt] offen lds\n" \
  "v_mfma_f32_16x16x32_bf16 v[192:195], v[176:179], v[216:219], v[192:195]\n" \
  "v_mfma_f32_16x16x32_bf16 v[196:199], v[180:183], v[216:219], v[196:199]\n" \
  "ds_read_b128 v[216:219], v252 offset:14336\n" \
  "v_mfma_f32_16x16x32_bf16 v[200:203], v[176:179], v[220:223], v[200:203]\n" \
  "v_mfma_f32_16x16x32_bf16 v[204:207], v[180:183], v[220:223], v[204:207]\n" \
  "ds_read_b128 v[220:223], v252 offset:15360\n" \
  "v_mfma_f32_16x16x32_bf16 v[192:195], v[52:55], v[224:227], v[192:195]\n" \
  "v_mfma_f32_16x16x32_bf16 v[196:199], v[116:119], v[224:227], v[196:199]\n" \
  "ds_read_b128 v[224:227], v252 offset:16384\n" \
  "v_mfma_f32_16x16x32_bf16 v[200:203], v[52:55], v[228:231], v[200:203]\n" \
  "v_mfma_f32_16x16x32_bf16 v[204:207], v[116:119], v[228:231], v[204:207]\n" \
  "ds_read_b128 v[228:231], v252 offset:17408\n" \
  "v_mfma_f32_16x16x32_bf16 v[192:195], v[240:243], v[232:235], v[192:195]\n" \
  "v_mfma_f32_16x16x32_bf16 v[196:199], v[244:247], v[232:235], v[196:199]\n" \
  "ds_read_b128 v[232:235], v252 offset:18432\n" \
  "v_mfma_f32_16x16x32_bf16 v[200:203], v[240:243], v[236:239], v[200:203]\n" \
  "v_mfma_f32_16x16x32_bf16 v[204:207], v[244:247], v[236:239], v[204:207]\n" \
  "ds_read_b128 v[236:239], v252 offset:19456\n" \
  "ds_read_b128 v[240:243], v249 offset:49152\n" \
  "ds_read_b128 v[244:247], v249 offset:50176\n" \
  "s_waitcnt lgkmcnt(9)\n" \
  "v_mfma_f32_16x16x32_bf16 v[192:195], v[56:59], v[208:211], v[192:195]\n" \
  "v_mfma_f32_16x16x32_bf16 v[196:199], v[120:123], v[208:211], v[196:199]\n" \
  "s_waitcnt lgkmcnt(8)\n" \
  "v_mfma_f32_16x16x32_bf16 v[200:203], v[56:59], v[212:215], v[200:203]\n" \
  "v_mfma_f32_16x16x32_bf16 v[204:207], v[120:123], v[212:215], v[204:207]\n" \
  "s_waitcnt vmcnt(0)\n" \
  "s_waitcnt lgkmcnt(0)\n" \
  "s_barrier\n" \
  "ds_read_b128 v[208:211], v252 offset:0\n" \
  "ds_read_b128 v[212:215], v252 offset:1024\n" \
  "s_add_u32 m0, %[mw], 12288\n" \
  "s_xor_b32 %[cnt], %[sn], 192\n" \
  "buffer_load_dwordx4 v248, %[rx], %[cnt] offen lds\n" \
  "s_add_u32 m0, %[mw], 16384\n" \
  "s_xor_b32 %[cnt], %[sn], 256\n" \
  "buffer_load_dwordx4 v248, %[rx], %[cnt] offen lds\n" \
  "s_add_u32 m0, %[mw], 20480\n" \
  "s_xor_b32 %[cnt], %[sn], 320\n" \
  "buffer_load_dwordx4 v248, %[rx], %[cnt] offen lds\n" \
  "v_mfma_f32_16x16x32_bf16 v[192:195], v[184:187], v[216:219], v[192:195]\n" \
  "v_mfma_f32_16x16x32_bf16 v[196:199], v[188:191], v[216:219], v[196:199]\n" \
  "ds_read_b128 v[216:219], v252 offset:2048\n" \
  "v_mfma_f32_16x16x32_bf16 v[200:203], v[184:187], v[220:223], v[200:203]\n" \
  "v_mfma_f32_16x16x32_bf16 v[204:207], v[188:191], v[220:223], v[204:207]\n" \
  "ds_read_b128 v[220:223], v252 offset:3072\n" \
  "v_mfma_f32_16x16x32_bf16 v[192:195], v[60:63], v[224:227], v[192:195]\n" \
  "v_mfma_f32_16x16x32_bf16 v[196:199], v[124:127], v[224:227], v[196:199]\n" \
  "ds_read_b128 v[224:227], v252 offset:4096\n" \
  "v_mfma_f32_16x16x32_bf16 v[200:203], v[60:63], v[228:231], v[200:203]\n" \
  "v_mfma_f32_16x16x32_bf16 v[204:207], v[124:127], v[228:231], v[204:207]\n" \
  "ds_read_b128 v[228:231], v252 offset:5120\n" \
  "v_mfma_f32_16x16x32_bf16 v[192:195], v[240:243], v[232:235], v[192:195]\n" \
  "v_mfma_f32_16x16x32_bf16 v[196:199], v[244:247], v[232:235], v[196:199]\n" \
  "ds_read_b128 v[232:235], v252 offset:6144\n" \
  "v_mfma_f32_16x16x32_bf16 v[200:203], v[240:243], v[236:239], v[200:203]\n" \
  "v_mfma_f32_16x16x32_bf16 v[204:207], v[244:247], v[236:239], v[204:207]\n" \
  "ds_read_b128 v[236:239], v252 offset:7168\n" \
  "v_cvt_pk_bf16_f32 v192, v192, v193\n" \
  "v_cvt_pk_bf16_f32 v193, v194, v195\n" \
  "v_cvt_pk_bf16_f32 v194, v196, v197\n" \
  "v_cvt_pk_bf16_f32 v195, v198, v199\n" \
  "v_add_u32 v196, %[so], v251\n" \
  "ds_write_b128 v196, v[192:195] offset:24576\n" \
  "v_cvt_pk_bf16_f32 v200, v200, v201\n" \
  "v_cvt_pk_bf16_f32 v201, v202, v203\n" \
  "v_cvt_pk_bf16_f32 v202, v204, v205\n" \
  "v_cvt_pk_bf16_f32 v203, v206, v207\n" \
  "ds_write_b128 v196, v[200:203] offset:28672\n" \
  "s_mov_b32 m0, %[keep]\n" \
  "s_waitcnt lgkmcnt(0)\n"
#define ST_XB \
  "s_barrier\n" \
  "ds_read_b128 v[192:195], v251 offset:24576\n" \
  "ds_read_b128 v[208:211], v251 offset:28672\n" \
  "ds_read_b128 v[196:199], v251 offset:25600\n" \
  "ds_read_b128 v[212:215], v251 offset:29696\n" \
  "ds_read_b128 v[200:203], v251 offset:26624\n" \
  "ds_read_b128 v[216:219], v251 offset:30720\n" \
  "ds_read_b128 v[204:207], v251 offset:27648\n" \
  "ds_read_b128 v[220:223], v251 offset:31744\n" \
  "ds_read_b128 v[240:243], v250 offset:0\n" \
  "ds_read_b128 v[244:247], v250 offset:16384\n" \
  "s_waitcnt lgkmcnt(9)\n" \
  "v_mfma_f32_16x16x32_bf16 v[224:227], v[192:195], v[0:3], 0\n" \
  "s_waitcnt lgkmcnt(8)\n" \
  "v_mfma_f32_16x16x32_bf16 v[228:231], v[208:211], v[0:3], 0\n" \
  "s_waitcnt lgkmcnt(7)\n" \
  "v_mfma_f32_16x16x32_bf16 v[224:227], v[196:199], v[4:7], v[224:227]\n" \
  "s_waitcnt lgkmcnt(6)\n" \
  "v_mfma_f32_16x16x32_bf16 v[228:231], v[212:215], v[4:7], v[228:231]\n" \
  "s_waitcnt lgkmcnt(5)\n" \
  "v_mfma_f32_16x16x32_bf16 v[224:227], v[200:203], v[8:11], v[224:227]\n" \
  "s_waitcnt lgkmcnt(4)\n" \
  "v_mfma_f32_16x16x32_bf16 v[228:231], v[216:219], v[8:11], v[228:231]\n" \
  "s_waitcnt lgkmcnt(3)\n" \
  "v_mfma_f32_16x16x32_bf16 v[224:227], v[204:207], v[12:15], v[224:227]\n" \
  "s_waitcnt lgkmcnt(2)\n" \
  "v_mfma_f32_16x16x32_bf16 v[228:231], v[220:223], v[12:15], v[228:231]\n" \
  "v_mfma_f32_16x16x32_bf16 v[232:235], v[192:195], v[128:131], 0\n" \
  "v_mfma_f32_16x16x32_bf16 v[236:239], v[208:211], v[128:131], 0\n" \
  "v_mfma_f32_16x16x32_bf16 v[232:235], v[200:203], v[136:139], v[232:235]\n" \
  "v_cvt_pk_bf16_f32 v224, v224, v225\n" \
  "v_cvt_pk_bf16_f32 v226, v226, v227\n" \
  "v_mfma_f32_16x16x32_bf16 v[236:239], v[216:219], v[136:139], v[236:239]\n" \
  "v_cvt_pk_bf16_f32 v225, v228, v229\n" \
  "v_cvt_pk_bf16_f32 v227, v230, v231\n" \
  "s_waitcnt lgkmcnt(1)\n" \
  "v_mfma_f32_16x16x32_bf16 v[232:235], v[196:199], v[240:243], v[232:235]\n" \
  "s_add_u32 %[cnt], %[so], 0*%c[js]+0*%c[ss]\n" \
  "buffer_store_dwordx4 v[224:227], v251, %[rs], %[cnt] offen sc1\n" \
  "v_mfma_f32_16x16x32_bf16 v[236:239], v[212:215], v[240:243], v[236:239]\n" \
  "s_waitcnt lgkmcnt(0)\n" \
  "v_mfma_f32_16x16x32_bf16 v[232:235], v[204:207], v[244:247], v[232:235]\n" \
  "v_mfma_f32_16x16x32_bf16 v[236:239], v[220:223], v[244:247], v[236:239]\n" \
  "ds_read_b128 v[240:243], v250 offset:1024\n" \
  "ds_read_b128 v[244:247], v250 offset:17408\n" \
  "v_mfma_f32_16x16x32_bf16 v[224:227], v[192:195], v[64:67], 0\n" \
  "v_mfma_f32_16x16x32_bf16 v[228:231], v[208:211], v[64:67], 0\n" \
  "v_mfma_f32_16x16x32_bf16 v[224:227], v[196:199], v[68:71], v[224:227]\n" \
  "v_cvt_pk_bf16_f32 v232, v232, v233\n" \
  "v_cvt_pk_bf16_f32 v234, v234, v235\n" \
  "v_mfma_f32_16x16x32_bf16 v[228:231], v[212:215], v[68:71], v[228:231]\n" \
  "v_cvt_pk_bf16_f32 v233, v236, v237\n" \
  "v_cvt_pk_bf16_f32 v235, v238, v239\n" \
  "v_mfma_f32_16x16x32_bf16 v[224:227], v[200:203], v[72:75], v[224:227]\n" \
  "s_add_u32 %[cnt], %[so], 4*%c[js]+0*%c[ss]\n" \
  "buffer_store_dwordx4 v[232:235], v251, %[rs], %[cnt] offen sc1\n" \
  "v_mfma_f32_16x16x32_bf16 v[228:231], v[216:219], v[72:75], v[228:231]\n" \
  "v_mfma_f32_16x16x32_bf16 v[224:227], v[204:207], v[76:79], v[224:227]\n" \
  "v_mfma_f32_16x16x32_bf16 v[228:231], v[220:223], v[76:79], v[228:231]\n" \
  "v_lshlrev_b32 v253, 24, v251\n" \
  "s_waitcnt vmcnt(2)\n" \
  "buffer_store_byte v253, v253, %[rs], %[fo] offen sc1\n" \
  "s_barrier\n" \
  "v_mfma_f32_16x16x32_bf16 v[232:235], v[192:195], v[132:135], 0\n" \
  "v_mfma_f32_16x16x32_bf16 v[236:239], v[208:211], v[132:135], 0\n" \
  "v_mfma_f32_16x16x32_bf16 v[232:235], v[200:203], v[140:143], v[232:235]\n" \
  "v_cvt_pk_bf16_f32 v224, v224, v225\n" \
  "v_cvt_pk_bf16_f32 v226, v226, v227\n" \
  "v_mfma_f32_16x16x32_bf16 v[236:239], v[216:219], v[140:143], v[236:239]\n" \
  "v_cvt_pk_bf16_f32 v225, v228, v229\n" \
  "v_cvt_pk_bf16_f32 v227, v230, v231\n" \
  "s_waitcnt lgkmcnt(1)\n" \
  "v_mfma_f32_16x16x32_bf16 v[232:235], v[196:199], v[240:243], v[232:235]\n" \
  "s_add_u32 %[cnt], %[so], 0*%c[js]+1*%c[ss]\n" \
  "buffer_store_dwordx4 v[224:227], v251, %[rs], %[cnt] offen sc1\n" \
  "v_mfma_f32_16x16x32_bf16 v[236:239], v[212:215], v[240:243], v[236:239]\n" \
  "s_waitcnt lgkmcnt(0)\n" \
  "v_mfma_f32_16x16x32_bf16 v[232:235], v[204:207], v[244:247], v[232:235]\n" \
  "v_mfma_f32_16x16x32_bf16 v[236:239], v[220:223], v[244:247], v[236:239]\n" \
  "ds_read_b128 v[240:243], v250 offset:32768\n" \
  "ds_read_b128 v[244:247], v250 offset:49152\n" \
  "v_mfma_f32_16x16x32_bf16 v[224:227], v[192:195], v[16:19], 0\n" \
  "v_mfma_f32_16x16x32_bf16 v[228:231], v[208:211], v[16:19], 0\n" \
  "v_mfma_f32_16x16x32_bf16 v[224:227], v[196:199], v[20:23], v[224:227]\n" \
  "v_cvt_pk_bf16_f32 v232, v232, v233\n" \
  "v_cvt_pk_bf16_f32 v234, v234, v235\n" \
  "v_mfma_f32_16x16x32_bf16 v[228:231], v[212:215], v[20:23], v[228:231]\n" \
  "v_cvt_pk_bf16_f32 v233, v236, v237\n" \
  "v_cvt_pk_bf16_f32 v235, v238, v239\n" \
  "v_mfma_f32_16x16x32_bf16 v[224:227], v[200:203], v[24:27], v[224:227]\n" \
  "s_add_u32 %[cnt], %[so], 4*%c[js]+1*%c[ss]\n" \
  "buffer_store_dwordx4 v[232:235], v251, %[rs], %[cnt] offen sc1\n" \
  "v_mfma_f32_16x16x32_bf16 v[228:231], v[216:219], v[24:27], v[228:231]\n" \
  "v_mfma_f32_16x16x32_bf16 v[224:227], v[204:207], v[28:31], v[224:227]\n" \
  "v_mfma_f32_16x16x32_bf16 v[228:231], v[220:223], v[28:31], v[228:231]\n" \
  "s_barrier\n" \
  "v_mfma_f32_16x16x32_bf16 v[232:235], v[192:195], v[144:147], 0\n" \
  "v_mfma_f32_16x16x32_bf16 v[236:239], v[208:211], v[144:147], 0\n" \
  "v_mfma_f32_16x16x32_bf16 v[232:235], v[200:203], v[152:155], v[232:235]\n" \
  "v_cvt_pk_bf16_f32 v224, v224, v225\n" \
  "v_cvt_pk_bf16_f32 v226, v226, v227\n" \
  "v_mfma_f32_16x16x32_bf16 v[236:239], v[216:219], v[152:155], v[236:239]\n" \
  "v_cvt_pk_bf16_f32 v225, v228, v229\n" \
  "v_cvt_pk_bf16_f32 v227, v230, v231\n" \
  "s_waitcnt lgkmcnt(1)\n" \
  "v_mfma_f32_16x16x32_bf16 v[232:235], v[196:199], v[240:243], v[232:235]\n" \
  "s_add_u32 %[cnt], %[so], 1*%c[js]+0*%c[ss]\n" \
  "buffer_store_dwordx4 v[224:227], v251, %[rs], %[cnt] offen sc1\n" \
  "v_mfma_f32_16x16x32_bf16 v[236:239], v[212:215], v[240:243], v[236:239]\n" \
  "s_waitcnt lgkmcnt(0)\n" \
  "v_mfma_f32_16x16x32_bf16 v[232:235], v[204:207], v[244:247], v[232:235]\n" \
  "v_mfma_f32_16x16x32_bf16 v[236:239], v[220:223], v[244:247], v[236:239]\n" \
  "ds_read_b128 v[240:243], v250 offset:33792\n" \
  "ds_read_b128 v[244:247], v250 offset:50176\n" \
  "v_mfma_f32_16x16x32_bf16 v[224:227], v[192:195], v[80:83], 0\n" \
  "v_mfma_f32_16x16x32_bf16 v[228:231], v[208:211], v[80:83], 0\n" \
  "v_mfma_f32_16x16x32_bf16 v[224:227], v[196:199], v[84:87], v[224:227]\n" \
  "v_cvt_pk_bf16_f32 v232, v232, v233\n" \
  "v_cvt_pk_bf16_f32 v234, v234, v235\n" \
  "v_mfma_f32_16x16x32_bf16 v[228:231], v[212:215], v[84:87], v[228:231]\n" \
  "v_cvt_pk_bf16_f32 v233, v236, v237\n" \
  "v_cvt_pk_bf16_f32 v235, v238, v239\n" \
  "v_mfma_f32_16x16x32_bf16 v[224:227], v[200:203], v[88:91], v[224:227]\n" \
  "s_add_u32 %[cnt], %[so], 5*%c[js]+0*%c[ss]\n" \
  "buffer_store_dwordx4 v[232:235], v251, %[rs], %[cnt] offen sc1\n" \
  "v_mfma_f32_16x16x32_bf16 v[228:231], v[216:219], v[88:91], v[228:231]\n" \
  "v_mfma_f32_16x16x32_bf16 v[224:227], v[204:207], v[92:95], v[224:227]\n" \
  "v_mfma_f32_16x16x32_bf16 v[228:231], v[220:223], v[92:95], v[228:231]\n" \
  "v_mfma_f32_16x16x32_bf16 v[232:235], v[192:195], v[148:151], 0\n" \
  "v_mfma_f32_16x16x32_bf16 v[236:239], v[208:211], v[148:151], 0\n" \
  "v_mfma_f32_16x16x32_bf16 v[232:235], v[200:203], v[156:159], v[232:235]\n" \
  "v_cvt_pk_bf16_f32 v224, v224, v225\n" \
  "v_cvt_pk_bf16_f32 v226, v226, v227\n" \
  "v_mfma_f32_16x16x32_bf16 v[236:239], v[216:219], v[156:159], v[236:239]\n" \
  "v_cvt_pk_bf16_f32 v225, v228, v229\n" \
  "v_cvt_pk_bf16_f32 v227, v230, v231\n" \
  "s_waitcnt lgkmcnt(1)\n" \
  "v_mfma_f32_16x16x32_bf16 v[232:235], v[196:199], v[240:243], v[232:235]\n" \
  "s_add_u32 %[cnt], %[so], 1*%c[js]+1*%c[ss]\n" \
  "buffer_store_dwordx4 v[224:227], v251, %[rs], %[cnt] offen sc1\n" \
  "v_mfma_f32_16x16x32_bf16 v[236:239], v[212:215], v[240:243], v[236:239]\n" \
  "s_waitcnt lgkmcnt(0)\n" \
  "v_mfma_f32_16x16x32_bf16 v[232:235], v[204:207], v[244:247], v[232:235]\n" \
  "v_mfma_f32_16x16x32_bf16 v[236:239], v[220:223], v[244:247], v[236:239]\n" \
  "ds_read_b128 v[240:243], v249 offset:0\n" \
  "ds_read_b128 v[244:247], v249 offset:16384\n" \
  "s_barrier\n" \
  "v_mfma_f32_16x16x32_bf16 v[224:227], v[192:195], v[32:35], 0\n" \
  "v_mfma_f32_16x16x32_bf16 v[228:231], v[208:211], v[32:35], 0\n" \
  "v_mfma_f32_16x16x32_bf16 v[224:227], v[196:199], v[36:39], v[224:227]\n" \
  "v_cvt_pk_bf16_f32 v232, v232, v233\n" \
  "v_cvt_pk_bf16_f32 v234, v234, v235\n" \
  "v_mfma_f32_16x16x32_bf16 v[228:231], v[212:215], v[36:39], v[228:231]\n" \
  "v_cvt_pk_bf16_f32 v233, v236, v237\n" \
  "v_cvt_pk_bf16_f32 v235, v238, v239\n" \
  "v_mfma_f32_16x16x32_bf16 v[224:227], v[200:203], v[40:43], v[224:227]\n" \
  "s_add_u32 %[cnt], %[so], 5*%c[js]+1*%c[ss]\n" \
  "buffer_store_dwordx4 v[232:235], v251, %[rs], %[cnt] offen sc1\n" \
  "v_mfma_f32_16x16x32_bf16 v[228:231], v[216:219], v[40:43], v[228:231]\n" \
  "v_mfma_f32_16x16x32_bf16 v[224:227], v[204:207], v[44:47], v[224:227]\n" \
  "v_mfma_f32_16x16x32_bf16 v[228:231], v[220:223], v[44:47], v[228:231]\n" \
  "v_mfma_f32_16x16x32_bf16 v[232:235], v[192:195], v[160:163], 0\n" \
  "v_mfma_f32_16x16x32_bf16 v[236:239], v[208:211], v[160:163], 0\n" \
  "v_mfma_f32_16x16x32_bf16 v[232:235], v[200:203], v[168:171], v[232:235]\n" \
  "v_cvt_pk_bf16_f32 v224, v224, v225\n" \
  "v_cvt_pk_bf16_f32 v226, v226, v227\n" \
  "v_mfma_f32_16x16x32_bf16 v[236:239], v[216:219], v[168:171], v[236:239]\n" \
  "v_cvt_pk_bf16_f32 v225, v228, v229\n" \
  "v_cvt_pk_bf16_f32 v227, v230, v231\n" \
  "s_waitcnt lgkmcnt(1)\n" \
  "v_mfma_f32_16x16x32_bf16 v[232:235], v[196:199], v[240:243], v[232:235]\n" \
  "s_add_u32 %[cnt], %[so], 2*%c[js]+0*%c[ss]\n" \
  "buffer_store_dwordx4 v[224:227], v251, %[rs], %[cnt] offen sc1\n" \
  "v_mfma_f32_16x16x32_bf16 v[236:239], v[212:215], v[240:243], v[236:239]\n" \
  "s_waitcnt lgkmcnt(0)\n" \
  "v_mfma_f32_16x16x32_bf16 v[232:235], v[204:207], v[244:247], v[232:235]\n" \
  "v_mfma_f32_16x16x32_bf16 v[236:239], v[220:223], v[244:247], v[236:239]\n" \
  "ds_read_b128 v[240:243], v249 offset:1024\n" \
  "ds_read_b128 v[244:247], v249 offset:17408\n" \
  "v_mfma_f32_16x16x32_bf16 v[224:227], v[192:195], v[96:99], 0\n" \
  "v_mfma_f32_16x16x32_bf16 v[228:231], v[208:211], v[96:99], 0\n" \
  "v_mfma_f32_16x16x32_bf16 v[224:227], v[196:199], v[100:103], v[224:227]\n" \
  "v_cvt_pk_bf16_f32 v232, v232, v233\n" \
  "v_cvt_pk_bf16_f32 v234, v234, v235\n" \
  "v_mfma_f32_16x16x32_bf16 v[228:231], v[212:215], v[100:103], v[228:231]\n" \
  "v_cvt_pk_bf16_f32 v233, v236, v237\n" \
  "v_cvt_pk_bf16_f32 v235, v238, v239\n" \
  "v_mfma_f32_16x16x32_bf16 v[224:227], v[200:203], v[104:107], v[224:227]\n" \
  "s_add_u32 %[cnt], %[so], 6*%c[js]+0*%c[ss]\n" \
  "buffer_store_dwordx4 v[232:235], v251, %[rs], %[cnt] offen sc1\n" \
  "v_mfma_f32_16x16x32_bf16 v[228:231], v[216:219], v[104:107], v[228:231]\n" \
  "v_mfma_f32_16x16x32_bf16 v[224:227], v[204:207], v[108:111], v[224:227]\n" \
  "v_mfma_f32_16x16x32_bf16 v[228:231], v[220:223], v[108:111], v[228:231]\n" \
  "s_barrier\n" \
  "v_mfma_f32_16x16x32_bf16 v[232:235], v[192:195], v[164:167], 0\n" \
  "v_mfma_f32_16x16x32_bf16 v[236:239], v[208:211], v[164:167], 0\n" \
  "v_mfma_f32_16x16x32_bf16 v[232:235], v[200:203], v[172:175], v[232:235]\n" \
  "v_cvt_pk_bf16_f32 v224, v224, v225\n" \
  "v_cvt_pk_bf16_f32 v226, v226, v227\n" \
  "v_mfma_f32_16x16x32_bf16 v[236:239], v[216:219], v[172:175], v[236:239]\n" \
  "v_cvt_pk_bf16_f32 v225, v228, v229\n" \
  "v_cvt_pk_bf16_f32 v227, v230, v231\n" \
  "s_waitcnt lgkmcnt(1)\n" \
  "v_mfma_f32_16x16x32_bf16 v[232:235], v[196:199], v[240:243], v[232:235]\n" \
  "s_add_u32 %[cnt], %[so], 2*%c[js]+1*%c[ss]\n" \
  "buffer_store_dwordx4 v[224:227], v251, %[rs], %[cnt] offen sc1\n" \
  "v_mfma_f32_16x16x32_bf16 v[236:239], v[212:215], v[240:243], v[236:239]\n" \
  "s_waitcnt lgkmcnt(0)\n" \
  "v_mfma_f32_16x16x32_bf16 v[232:235], v[204:207], v[244:247], v[232:235]\n" \
  "v_mfma_f32_16x16x32_bf16 v[236:239], v[220:223], v[244:247], v[236:239]\n" \
  "ds_read_b128 v[240:243], v249 offset:32768\n" \
  "ds_read_b128 v[244:247], v249 offset:49152\n" \
  "v_mfma_f32_16x16x32_bf16 v[224:227], v[192:195], v[48:51], 0\n" \
  "v_mfma_f32_16x16x32_bf16 v[228:231], v[208:211], v[48:51], 0\n" \
  "v_mfma_f32_16x16x32_bf16 v[224:227], v[196:199], v[52:55], v[224:227]\n" \
  "v_cvt_pk_bf16_f32 v232, v232, v233\n" \
  "v_cvt_pk_bf16_f32 v234, v234, v235\n" \
  "v_mfma_f32_16x16x32_bf16 v[228:231], v[212:215], v[52:55], v[228:231]\n" \
  "v_cvt_pk_bf16_f32 v233, v236, v237\n" \
  "v_cvt_pk_bf16_f32 v235, v238, v239\n" \
  "v_mfma_f32_16x16x32_bf16 v[224:227], v[200:203], v[56:59], v[224:227]\n" \
  "s_add_u32 %[cnt], %[so], 6*%c[js]+1*%c[ss]\n" \
  "buffer_store_dwordx4 v[232:235], v251, %[rs], %[cnt] offen sc1\n" \
  "v_mfma_f32_16x16x32_bf16 v[228:231], v[216:219], v[56:59], v[228:231]\n" \
  "v_mfma_f32_16x16x32_bf16 v[224:227], v[204:207], v[60:63], v[224:227]\n" \
  "v_mfma_f32_16x16x32_bf16 v[228:231], v[220:223], v[60:63], v[228:231]\n" \
  "s_barrier\n" \
  "v_mfma_f32_16x16x32_bf16 v[232:235], v[192:195], v[176:179], 0\n" \
  "v_mfma_f32_16x16x32_bf16 v[236:239], v[208:211], v[176:179], 0\n" \
  "v_mfma_f32_16x16x32_bf16 v[232:235], v[200:203], v[184:187], v[232:235]\n" \
  "v_cvt_pk_bf16_f32 v224, v224, v225\n" \
  "v_cvt_pk_bf16_f32 v226, v226, v227\n" \
  "v_mfma_f32_16x16x32_bf16 v[236:239], v[216:219], v[184:187], v[236:239]\n" \
  "v_cvt_pk_bf16_f32 v225, v228, v229\n" \
  "v_cvt_pk_bf16_f32 v227, v230, v231\n" \
  "s_waitcnt lgkmcnt(1)\n" \
  "v_mfma_f32_16x16x32_bf16 v[232:235], v[196:199], v[240:243], v[232:235]\n" \
  "s_add_u32 %[cnt], %[so], 3*%c[js]+0*%c[ss]\n" \
  "buffer_store_dwordx4 v[224:227], v251, %[rs], %[cnt] offen sc1\n" \
  "v_mfma_f32_16x16x32_bf16 v[236:239], v[212:215], v[240:243], v[236:239]\n" \
  "s_waitcnt lgkmcnt(0)\n" \
  "v_mfma_f32_16x16x32_bf16 v[232:235], v[204:207], v[244:247], v[232:235]\n" \
  "v_mfma_f32_16x16x32_bf16 v[236:239], v[220:223], v[244:247], v[236:239]\n" \
  "ds_read_b128 v[240:243], v249 offset:33792\n" \
  "ds_read_b128 v[244:247], v249 offset:50176\n" \
  "v_mfma_f32_16x16x32_bf16 v[224:227], v[192:195], v[112:115], 0\n" \
  "v_mfma_f32_16x16x32_bf16 v[228:231], v[208:211], v[112:115], 0\n" \
  "v_mfma_f32_16x16x32_bf16 v[224:227], v[196:199], v[116:119], v[224:227]\n" \
  "v_cvt_pk_bf16_f32 v232, v232, v233\n" \
  "v_cvt_pk_bf16_f32 v234, v234, v235\n" \
  "v_mfma_f32_16x16x32_bf16 v[228:231], v[212:215], v[116:119], v[228:231]\n" \
  "v_cvt_pk_bf16_f32 v233, v236, v237\n" \
  "v_cvt_pk_bf16_f32 v235, v238, v239\n" \
  "v_mfma_f32_16x16x32_bf16 v[224:227], v[200:203], v[120:123], v[224:227]\n" \
  "s_add_u32 %[cnt], %[so], 7*%c[js]+0*%c[ss]\n" \
  "buffer_store_dwordx4 v[232:235], v251, %[rs], %[cnt] offen sc1\n" \
  "v_mfma_f32_16x16x32_bf16 v[228:231], v[216:219], v[120:123], v[228:231]\n" \
  "v_mfma_f32_16x16x32_bf16 v[224:227], v[204:207], v[124:127], v[224:227]\n" \
  "v_mfma_f32_16x16x32_bf16 v[228:231], v[220:223], v[124:127], v[228:231]\n" \
  "v_mfma_f32_16x16x32_bf16 v[232:235], v[192:195], v[180:183], 0\n" \
  "v_mfma_f32_16x16x32_bf16 v[236:239], v[208:211], v[180:183], 0\n" \
  "v_mfma_f32_16x16x32_bf16 v[232:235], v[200:203], v[188:191], v[232:235]\n" \
  "v_cvt_pk_bf16_f32 v224, v224, v225\n" \
  "v_cvt_pk_bf16_f32 v226, v226, v227\n" \
  "v_mfma_f32_16x16x32_bf16 v[236:239], v[216:219], v[188:191], v[236:239]\n" \
  "v_cvt_pk_bf16_f32 v225, v228, v229\n" \
  "v_cvt_pk_bf16_f32 v227, v230, v231\n" \
  "s_waitcnt lgkmcnt(1)\n" \
  "v_mfma_f32_16x16x32_bf16 v[232:235], v[196:199], v[240:243], v[232:235]\n" \
  "s_add_u32 %[cnt], %[so], 3*%c[js]+1*%c[ss]\n" \
  "buffer_store_dwordx4 v[224:227], v251, %[rs], %[cnt] offen sc1\n" \
  "v_mfma_f32_16x16x32_bf16 v[236:239], v[212:215], v[240:243], v[236:239]\n" \
  "s_waitcnt lgkmcnt(0)\n" \
  "v_mfma_f32_16x16x32_bf16 v[232:235], v[204:207], v[244:247], v[232:235]\n" \
  "v_mfma_f32_16x16x32_bf16 v[236:239], v[220:223], v[244:247], v[236:239]\n" \
  "s_nop 4\n" \
  "v_cvt_pk_bf16_f32 v232, v232, v233\n" \
  "v_cvt_pk_bf16_f32 v234, v234, v235\n" \
  "s_nop 1\n" \
  "v_cvt_pk_bf16_f32 v233, v236, v237\n" \
  "v_cvt_pk_bf16_f32 v235, v238, v239\n" \
  "s_add_u32 %[cnt], %[so], 7*%c[js]+1*%c[ss]\n" \
  "buffer_store_dwordx4 v[232:235], v251, %[rs], %[cnt] offen sc1\n" \
  "s_nop 1\n"
#define ST_XBF \
  "s_barrier\n" \
  "ds_read_b128 v[192:195], v251 offset:24576\n" \
  "ds_read_b128 v[208:211], v251 offset:28672\n" \
  "ds_read_b128 v[196:199], v251 offset:25600\n" \
  "ds_read_b128 v[212:215], v251 offset:29696\n" \
  "ds_read_b128 v[200:203], v251 offset:26624\n" \
  "ds_read_b128 v[216:219], v251 offset:30720\n" \
  "ds_read_b128 v[204:207], v251 offset:27648\n" \
  "ds_read_b128 v[220:223], v251 offset:31744\n" \
  "ds_read_b128 v[240:243], v250 offset:0\n" \
  "ds_read_b128 v[244:247], v250 offset:16384\n" \
  "s_waitcnt lgkmcnt(9)\n" \
  "v_mfma_f32_16x16x32_bf16 v[224:227], v[192:195], v[0:3], 0\n" \
  "s_waitcnt lgkmcnt(8)\n" \
  "v_mfma_f32_16x16x32_bf16 v[228:231], v[208:211], v[0:3], 0\n" \
  "s_waitcnt lgkmcnt(7)\n" \
  "v_mfma_f32_16x16x32_bf16 v[224:227], v[196:199], v[4:7], v[224:227]\n" \
  "s_waitcnt lgkmcnt(6)\n" \
  "v_mfma_f32_16x16x32_bf16 v[228:231], v[212:215], v[4:7], v[228:231]\n" \
  "s_waitcnt lgkmcnt(5)\n" \
  "v_mfma_f32_16x16x32_bf16 v[224:227], v[200:203], v[8:11], v[224:227]\n" \
  "s_waitcnt lgkmcnt(4)\n" \
  "v_mfma_f32_16x16x32_bf16 v[228:231], v[216:219], v[8:11], v[228:231]\n" \
  "s_waitcnt lgkmcnt(3)\n" \
  "v_mfma_f32_16x16x32_bf16 v[224:227], v[204:207], v[12:15], v[224:227]\n" \
  "s_waitcnt lgkmcnt(2)\n" \
  "v_mfma_f32_16x16x32_bf16 v[228:231], v[220:223], v[12:15], v[228:231]\n" \
  "v_mfma_f32_16x16x32_bf16 v[232:235], v[192:195], v[128:131], 0\n" \
  "v_mfma_f32_16x16x32_bf16 v[236:239], v[208:211], v[128:131], 0\n" \
  "v_mfma_f32_16x16x32_bf16 v[232:235], v[200:203], v[136:139], v[232:235]\n" \
  "v_cvt_pk_bf16_f32 v224, v224, v225\n" \
  "v_cvt_pk_bf16_f32 v226, v226, v227\n" \
  "v_mfma_f32_16x16x32_bf16 v[236:239], v[216:219], v[136:139], v[236:239]\n" \
  "v_cvt_pk_bf16_f32 v225, v228, v229\n" \
  "v_cvt_pk_bf16_f32 v227, v230, v231\n" \
  "s_waitcnt lgkmcnt(1)\n" \
  "v_mfma_f32_16x16x32_bf16 v[232:235], v[196:199], v[240:243], v[232:235]\n" \
  "s_add_u32 %[cnt], %[so], 0*%c[js]+0*%c[ss]\n" \
  "buffer_store_dwordx4 v[224:227], v251, %[rs], %[cnt] offen sc1\n" \
  "v_mfma_f32_16x16x32_bf16 v[236:239], v[212:215], v[240:243], v[236:239]\n" \
  "s_waitcnt lgkmcnt(0)\n" \
  "v_mfma_f32_16x16x32_bf16 v[232:235], v[204:207], v[244:247], v[232:235]\n" \
  "v_mfma_f32_16x16x32_bf16 v[236:239], v[220:223], v[244:247], v[236:239]\n" \
  "ds_read_b128 v[240:243], v250 offset:1024\n" \
  "ds_read_b128 v[244:247], v250 offset:17408\n" \
  "v_mfma_f32_16x16x32_bf16 v[224:227], v[192:195], v[64:67], 0\n" \
  "v_mfma_f32_16x16x32_bf16 v[228:231], v[208:211], v[64:67], 0\n" \
  "v_mfma_f32_16x16x32_bf16 v[224:227], v[196:199], v[68:71], v[224:227]\n" \
  "v_cvt_pk_bf16_f32 v232, v232, v233\n" \
  "v_cvt_pk_bf16_f32 v234, v234, v235\n" \
  "v_mfma_f32_16x16x32_bf16 v[228:231], v[212:215], v[68:71], v[228:231]\n" \
  "v_cvt_pk_bf16_f32 v233, v236, v237\n" \
  "v_cvt_pk_bf16_f32 v235, v238, v239\n" \
  "v_mfma_f32_16x16x32_bf16 v[224:227], v[200:203], v[72:75], v[224:227]\n" \
  "s_add_u32 %[cnt], %[so], 4*%c[js]+0*%c[ss]\n" \
  "buffer_store_dwordx4 v[232:235], v251, %[rs], %[cnt] offen sc1\n" \
  "v_mfma_f32_16x16x32_bf16 v[228:231], v[216:219], v[72:75], v[228:231]\n" \
  "v_mfma_f32_16x16x32_bf16 v[224:227], v[204:207], v[76:79], v[224:227]\n" \
  "v_mfma_f32_16x16x32_bf16 v[228:231], v[220:223], v[76:79], v[228:231]\n" \
  "v_mfma_f32_16x16x32_bf16 v[232:235], v[192:195], v[132:135], 0\n" \
  "v_mfma_f32_16x16x32_bf16 v[236:239], v[208:211], v[132:135], 0\n" \
  "v_mfma_f32_16x16x32_bf16 v[232:235], v[200:203], v[140:143], v[232:235]\n" \
  "v_cvt_pk_bf16_f32 v224, v224, v225\n" \
  "v_cvt_pk_bf16_f32 v226, v226, v227\n" \
  "v_mfma_f32_16x16x32_bf16 v[236:239], v[216:219], v[140:143], v[236:239]\n" \
  "v_cvt_pk_bf16_f32 v225, v228, v229\n" \
  "v_cvt_pk_bf16_f32 v227, v230, v231\n" \
  "s_waitcnt lgkmcnt(1)\n" \
  "v_mfma_f32_16x16x32_bf16 v[232:235], v[196:199], v[240:243], v[232:235]\n" \
  "s_add_u32 %[cnt], %[so], 0*%c[js]+1*%c[ss]\n" \
  "buffer_store_dwordx4 v[224:227], v251, %[rs], %[cnt] offen sc1\n" \
  "v_mfma_f32_16x16x32_bf16 v[236:239], v[212:215], v[240:243], v[236:239]\n" \
  "s_waitcnt lgkmcnt(0)\n" \
  "v_mfma_f32_16x16x32_bf16 v[232:235], v[204:207], v[244:247], v[232:235]\n" \
  "v_mfma_f32_16x16x32_bf16 v[236:239], v[220:223], v[244:247], v[236:239]\n" \
  "ds_read_b128 v[240:243], v250 offset:32768\n" \
  "ds_read_b128 v[244:247], v250 offset:49152\n" \
  "s_barrier\n" \
  "v_mfma_f32_16x16x32_bf16 v[224:227], v[192:195], v[16:19], 0\n" \
  "v_mfma_f32_16x16x32_bf16 v[228:231], v[208:211], v[16:19], 0\n" \
  "v_mfma_f32_16x16x32_bf16 v[224:227], v[196:199], v[20:23], v[224:227]\n" \
  "v_cvt_pk_bf16_f32 v232, v232, v233\n" \
  "v_cvt_pk_bf16_f32 v234, v234, v235\n" \
  "v_mfma_f32_16x16x32_bf16 v[228:231], v[212:215], v[20:23], v[228:231]\n" \
  "v_cvt_pk_bf16_f32 v233, v236, v237\n" \
  "v_cvt_pk_bf16_f32 v235, v238, v239\n" \
  "v_mfma_f32_16x16x32_bf16 v[224:227], v[200:203], v[24:27], v[224:227]\n" \
  "s_add_u32 %[cnt], %[so], 4*%c[js]+1*%c[ss]\n" \
  "buffer_store_dwordx4 v[232:235], v251, %[rs], %[cnt] offen sc1\n" \
  "v_mfma_f32_16x16x32_bf16 v[228:231], v[216:219], v[24:27], v[228:231]\n" \
  "v_mfma_f32_16x16x32_bf16 v[224:227], v[204:207], v[28:31], v[224:227]\n" \
  "v_mfma_f32_16x16x32_bf16 v[228:231], v[220:223], v[28:31], v[228:231]\n" \
  "v_mfma_f32_16x16x32_bf16 v[232:235], v[192:195], v[144:147], 0\n" \
  "v_mfma_f32_16x16x32_bf16 v[236:239], v[208:211], v[144:147], 0\n" \
  "v_mfma_f32_16x16x32_bf16 v[232:235], v[200:203], v[152:155], v[232:235]\n" \
  "v_cvt_pk_bf16_f32 v224, v224, v225\n" \
  "v_cvt_pk_bf16_f32 v226, v226, v227\n" \
  "v_mfma_f32_16x16x32_bf16 v[236:239], v[216:219], v[152:155], v[236:239]\n" \
  "v_cvt_pk_bf16_f32 v225, v228, v229\n" \
  "v_cvt_pk_bf16_f32 v227, v230, v231\n" \
  "s_waitcnt lgkmcnt(1)\n" \
  "v_mfma_f32_16x16x32_bf16 v[232:235], v[196:199], v[240:243], v[232:235]\n" \
  "s_add_u32 %[cnt], %[so], 1*%c[js]+0*%c[ss]\n" \
  "buffer_store_dwordx4 v[224:227], v251, %[rs], %[cnt] offen sc1\n" \
  "v_mfma_f32_16x16x32_bf16 v[236:239], v[212:215], v[240:243], v[236:239]\n" \
  "s_waitcnt lgkmcnt(0)\n" \
  "v_mfma_f32_16x16x32_bf16 v[232:235], v[204:207], v[244:247], v[232:235]\n" \
  "v_mfma_f32_16x16x32_bf16 v[236:239], v[220:223], v[244:247], v[236:239]\n" \
  "ds_read_b128 v[240:243], v250 offset:33792\n" \
  "ds_read_b128 v[244:247], v250 offset:50176\n" \
  "v_mfma_f32_16x16x32_bf16 v[224:227], v[192:195], v[80:83], 0\n" \
  "v_mfma_f32_16x16x32_bf16 v[228:231], v[208:211], v[80:83], 0\n" \
  "v_mfma_f32_16x16x32_bf16 v[224:227], v[196:199], v[84:87], v[224:227]\n" \
  "v_cvt_pk_bf16_f32 v232, v232, v233\n" \
  "v_cvt_pk_bf16_f32 v234, v234, v235\n" \
  "v_mfma_f32_16x16x32_bf16 v[228:231], v[212:215], v[84:87], v[228:231]\n" \
  "v_cvt_pk_bf16_f32 v233, v236, v237\n" \
  "v_cvt_pk_bf16_f32 v235, v238, v239\n" \
  "v_mfma_f32_16x16x32_bf16 v[224:227], v[200:203], v[88:91], v[224:227]\n" \
  "s_add_u32 %[cnt], %[so], 5*%c[js]+0*%c[ss]\n" \
  "buffer_store_dwordx4 v[232:235], v251, %[rs], %[cnt] offen sc1\n" \
  "v_mfma_f32_16x16x32_bf16 v[228:231], v[216:219], v[88:91], v[228:231]\n" \
  "v_mfma_f32_16x16x32_bf16 v[224:227], v[204:207], v[92:95], v[224:227]\n" \
  "v_mfma_f32_16x16x32_bf16 v[228:231], v[220:223], v[92:95], v[228:231]\n" \
  "v_mfma_f32_16x16x32_bf16 v[232:235], v[192:195], v[148:151], 0\n" \
  "v_mfma_f32_16x16x32_bf16 v[236:239], v[208:211], v[148:151], 0\n" \
  "v_mfma_f32_16x16x32_bf16 v[232:235], v[200:203], v[156:159], v[232:235]\n" \
  "v_cvt_pk_bf16_f32 v224, v224, v225\n" \
  "v_cvt_pk_bf16_f32 v226, v226, v227\n" \
  "v_mfma_f32_16x16x32_bf16 v[236:239], v[216:219], v[156:159], v[236:239]\n" \
  "v_cvt_pk_bf16_f32 v225, v228, v229\n" \
  "v_cvt_pk_bf16_f32 v227, v230, v231\n" \
  "s_waitcnt lgkmcnt(1)\n" \
  "v_mfma_f32_16x16x32_bf16 v[232:235], v[196:199], v[240:243], v[232:235]\n" \
  "s_add_u32 %[cnt], %[so], 1*%c[js]+1*%c[ss]\n" \
  "buffer_store_dwordx4 v[224:227], v251, %[rs], %[cnt] offen sc1\n" \
  "v_mfma_f32_16x16x32_bf16 v[236:239], v[212:215], v[240:243], v[236:239]\n" \
  "s_waitcnt lgkmcnt(0)\n" \
  "v_mfma_f32_16x16x32_bf16 v[232:235], v[204:207], v[244:247], v[232:235]\n" \
  "v_mfma_f32_16x16x32_bf16 v[236:239], v[220:223], v[244:247], v[236:239]\n" \
  "ds_read_b128 v[240:243], v249 offset:0\n" \
  "ds_read_b128 v[244:247], v249 offset:16384\n" \
  "s_barrier\n" \
  "v_mfma_f32_16x16x32_bf16 v[224:227], v[192:195], v[32:35], 0\n" \
  "v_mfma_f32_16x16x32_bf16 v[228:231], v[208:211], v[32:35], 0\n" \
  "v_mfma_f32_16x16x32_bf16 v[224:227], v[196:199], v[36:39], v[224:227]\n" \
  "v_cvt_pk_bf16_f32 v232, v232, v233\n" \
  "v_cvt_pk_bf16_f32 v234, v234, v235\n" \
  "v_mfma_f32_16x16x32_bf16 v[228:231], v[212:215], v[36:39], v[228:231]\n" \
  "v_cvt_pk_bf16_f32 v233, v236, v237\n" \
  "v_cvt_pk_bf16_f32 v235, v238, v239\n" \
  "v_mfma_f32_16x16x32_bf16 v[224:227], v[200:203], v[40:43], v[224:227]\n" \
  "s_add_u32 %[cnt], %[so], 5*%c[js]+1*%c[ss]\n" \
  "buffer_store_dwordx4 v[232:235], v251, %[rs], %[cnt] offen sc1\n" \
  "v_mfma_f32_16x16x32_bf16 v[228:231], v[216:219], v[40:43], v[228:231]\n" \
  "v_mfma_f32_16x16x32_bf16 v[224:227], v[204:207], v[44:47], v[224:227]\n" \
  "v_mfma_f32_16x16x32_bf16 v[228:231], v[220:223], v[44:47], v[228:231]\n" \
  "v_mfma_f32_16x16x32_bf16 v[232:235], v[192:195], v[160:163], 0\n" \
  "v_mfma_f32_16x16x32_bf16 v[236:239], v[208:211], v[160:163], 0\n" \
  "v_mfma_f32_16x16x32_bf16 v[232:235], v[200:203], v[168:171], v[232:235]\n" \
  "v_cvt_pk_bf16_f32 v224, v224, v225\n" \
  "v_cvt_pk_bf16_f32 v226, v226, v227\n" \
  "v_mfma_f32_16x16x32_bf16 v[236:239], v[216:219], v[168:171], v[236:239]\n" \
  "v_cvt_pk_bf16_f32 v225, v228, v229\n" \
  "v_cvt_pk_bf16_f32 v227, v230, v231\n" \
  "s_waitcnt lgkmcnt(1)\n" \
  "v_mfma_f32_16x16x32_bf16 v[232:235], v[196:199], v[240:243], v[232:235]\n" \
  "s_add_u32 %[cnt], %[so], 2*%c[js]+0*%c[ss]\n" \
  "buffer_store_dwordx4 v[224:227], v251, %[rs], %[cnt] offen sc1\n" \
  "v_mfma_f32_16x16x32_bf16 v[236:239], v[212:215], v[240:243], v[236:239]\n" \
  "s_waitcnt lgkmcnt(0)\n" \
  "v_mfma_f32_16x16x32_bf16 v[232:235], v[204:207], v[244:247], v[232:235]\n" \
  "v_mfma_f32_16x16x32_bf16 v[236:239], v[220:223], v[244:247], v[236:239]\n" \
  "ds_read_b128 v[240:243], v249 offset:1024\n" \
  "ds_read_b128 v[244:247], v249 offset:17408\n" \
  "v_mfma_f32_16x16x32_bf16 v[224:227], v[192:195], v[96:99], 0\n" \
  "v_mfma_f32_16x16x32_bf16 v[228:231], v[208:211], v[96:99], 0\n" \
  "v_mfma_f32_16x16x32_bf16 v[224:227], v[196:199], v[100:103], v[224:227]\n" \
  "v_cvt_pk_bf16_f32 v232, v232, v233\n" \
  "v_cvt_pk_bf16_f32 v234, v234, v235\n" \
  "v_mfma_f32_16x16x32_bf16 v[228:231], v[212:215], v[100:103], v[228:231]\n" \
  "v_cvt_pk_bf16_f32 v233, v236, v237\n" \
  "v_cvt_pk_bf16_f32 v235, v238, v239\n" \
  "v_mfma_f32_16x16x32_bf16 v[224:227], v[200:203], v[104:107], v[224:227]\n" \
  "s_add_u32 %[cnt], %[so], 6*%c[js]+0*%c[ss]\n" \
  "buffer_store_dwordx4 v[232:235], v251, %[rs], %[cnt] offen sc1\n" \
  "v_mfma_f32_16x16x32_bf16 v[228:231], v[216:219], v[104:107], v[228:231]\n" \
  "v_mfma_f32_16x16x32_bf16 v[224:227], v[204:207], v[108:111], v[224:227]\n" \
  "v_mfma_f32_16x16x32_bf16 v[228:231], v[220:223], v[108:111], v[228:231]\n" \
  "v_mfma_f32_16x16x32_bf16 v[232:235], v[192:195], v[164:167], 0\n" \
  "v_mfma_f32_16x16x32_bf16 v[236:239], v[208:211], v[164:167], 0\n" \
  "v_mfma_f32_16x16x32_bf16 v[232:235], v[200:203], v[172:175], v[232:235]\n" \
  "v_cvt_pk_bf16_f32 v224, v224, v225\n" \
  "v_cvt_pk_bf16_f32 v226, v226, v227\n" \
  "v_mfma_f32_16x16x32_bf16 v[236:239], v[216:219], v[172:175], v[236:239]\n" \
  "v_cvt_pk_bf16_f32 v225, v228, v229\n" \
  "v_cvt_pk_bf16_f32 v227, v230, v231\n" \
  "s_waitcnt lgkmcnt(1)\n" \
  "v_mfma_f32_16x16x32_bf16 v[232:235], v[196:199], v[240:243], v[232:235]\n" \
  "s_add_u32 %[cnt], %[so], 2*%c[js]+1*%c[ss]\n" \
  "buffer_store_dwordx4 v[224:227], v251, %[rs], %[cnt] offen sc1\n" \
  "v_mfma_f32_16x16x32_bf16 v[236:239], v[212:215], v[240:243], v[236:239]\n" \
  "s_waitcnt lgkmcnt(0)\n" \
  "v_mfma_f32_16x16x32_bf16 v[232:235], v[204:207], v[244:247], v[232:235]\n" \
  "v_mfma_f32_16x16x32_bf16 v[236:239], v[220:223], v[244:247], v[236:239]\n" \
  "ds_read_b128 v[240:243], v249 offset:32768\n" \
  "ds_read_b128 v[244:247], v249 offset:49152\n" \
  "s_barrier\n" \
  "v_mfma_f32_16x16x32_bf16 v[224:227], v[192:195], v[48:51], 0\n" \
  "v_mfma_f32_16x16x32_bf16 v[228:231], v[208:211], v[48:51], 0\n" \
  "v_mfma_f32_16x16x32_bf16 v[224:227], v[196:199], v[52:55], v[224:227]\n" \
  "v_cvt_pk_bf16_f32 v232, v232, v233\n" \
  "v_cvt_pk_bf16_f32 v234, v234, v235\n" \
  "v_mfma_f32_16x16x32_bf16 v[228:231], v[212:215], v[52:55], v[228:231]\n" \
  "v_cvt_pk_bf16_f32 v233, v236, v237\n" \
  "v_cvt_pk_bf16_f32 v235, v238, v239\n" \
  "v_mfma_f32_16x16x32_bf16 v[224:227], v[200:203], v[56:59], v[224:227]\n" \
  "s_add_u32 %[cnt], %[so], 6*%c[js]+1*%c[ss]\n" \
  "buffer_store_dwordx4 v[232:235], v251, %[rs], %[cnt] offen sc1\n" \
  "v_mfma_f32_16x16x32_bf16 v[228:231], v[216:219], v[56:59], v[228:231]\n" \
  "v_mfma_f32_16x16x32_bf16 v[224:227], v[204:207], v[60:63], v[224:227]\n" \
  "v_mfma_f32_16x16x32_bf16 v[228:231], v[220:223], v[60:63], v[228:231]\n" \
  "v_mfma_f32_16x16x32_bf16 v[232:235], v[192:195], v[176:179], 0\n" \
  "v_mfma_f32_16x16x32_bf16 v[236:239], v[208:211], v[176:179], 0\n" \
  "v_mfma_f32_16x16x32_bf16 v[232:235], v[200:203], v[184:187], v[232:235]\n" \
  "v_cvt_pk_bf16_f32 v224, v224, v225\n" \
  "v_cvt_pk_bf16_f32 v226, v226, v227\n" \
  "v_mfma_f32_16x16x32_bf16 v[236:239], v[216:219], v[184:187], v[236:239]\n" \
  "v_cvt_pk_bf16_f32 v225, v228, v229\n" \
  "v_cvt_pk_bf16_f32 v227, v230, v231\n" \
  "s_waitcnt lgkmcnt(1)\n" \
  "v_mfma_f32_16x16x32_bf16 v[232:235], v[196:199], v[240:243], v[232:235]\n" \
  "s_add_u32 %[cnt], %[so], 3*%c[js]+0*%c[ss]\n" \
  "buffer_store_dwordx4 v[224:227], v251, %[rs], %[cnt] offen sc1\n" \
  "v_mfma_f32_16x16x32_bf16 v[236:239], v[212:215], v[240:243], v[236:239]\n" \
  "s_waitcnt lgkmcnt(0)\n" \
  "v_mfma_f32_16x16x32_bf16 v[232:235], v[204:207], v[244:247], v[232:235]\n" \
  "v_mfma_f32_16x16x32_bf16 v[236:239], v[220:223], v[244:247], v[236:239]\n" \
  "ds_read_b128 v[240:243], v249 offset:33792\n" \
  "ds_read_b128 v[244:247], v249 offset:50176\n" \
  "v_mfma_f32_16x16x32_bf16 v[224:227], v[192:195], v[112:115], 0\n" \
  "v_mfma_f32_16x16x32_bf16 v[228:231], v[208:211], v[112:115], 0\n" \
  "v_mfma_f32_16x16x32_bf16 v[224:227], v[196:199], v[116:119], v[224:227]\n" \
  "v_cvt_pk_bf16_f32 v232, v232, v233\n" \
  "v_cvt_pk_bf16_f32 v234, v234, v235\n" \
  "v_mfma_f32_16x16x32_bf16 v[228:231], v[212:215], v[116:119], v[228:231]\n" \
  "v_cvt_pk_bf16_f32 v233, v236, v237\n" \
  "v_cvt_pk_bf16_f32 v235, v238, v239\n" \
  "v_mfma_f32_16x16x32_bf16 v[224:227], v[200:203], v[120:123], v[224:227]\n" \
  "s_add_u32 %[cnt], %[so], 7*%c[js]+0*%c[ss]\n" \
  "buffer_store_dwordx4 v[232:235], v251, %[rs], %[cnt] offen sc1\n" \
  "v_mfma_f32_16x16x32_bf16 v[228:231], v[216:219], v[120:123], v[228:231]\n" \
  "v_mfma_f32_16x16x32_bf16 v[224:227], v[204:207], v[124:127], v[224:227]\n" \
  "v_mfma_f32_16x16x32_bf16 v[228:231], v[220:223], v[124:127], v[228:231]\n" \
  "v_mfma_f32_16x16x32_bf16 v[232:235], v[192:195], v[180:183], 0\n" \
  "v_mfma_f32_16x16x32_bf16 v[236:239], v[208:211], v[180:183], 0\n" \
  "v_mfma_f32_16x16x32_bf16 v[232:235], v[200:203], v[188:191], v[232:235]\n" \
  "v_cvt_pk_bf16_f32 v224, v224, v225\n" \
  "v_cvt_pk_bf16_f32 v226, v226, v227\n" \
  "v_mfma_f32_16x16x32_bf16 v[236:239], v[216:219], v[188:191], v[236:239]\n" \
  "v_cvt_pk_bf16_f32 v225, v228, v229\n" \
  "v_cvt_pk_bf16_f32 v227, v230, v231\n" \
  "s_waitcnt lgkmcnt(1)\n" \
  "v_mfma_f32_16x16x32_bf16 v[232:235], v[196:199], v[240:243], v[232:235]\n" \
  "s_add_u32 %[cnt], %[so], 3*%c[js]+1*%c[ss]\n" \
  "buffer_store_dwordx4 v[224:227], v251, %[rs], %[cnt] offen sc1\n" \
  "v_mfma_f32_16x16x32_bf16 v[236:239], v[212:215], v[240:243], v[236:239]\n" \
  "s_waitcnt lgkmcnt(0)\n" \
  "v_mfma_f32_16x16x32_bf16 v[232:235], v[204:207], v[244:247], v[232:235]\n" \
  "v_mfma_f32_16x16x32_bf16 v[236:239], v[220:223], v[244:247], v[236:239]\n" \
  "s_nop 4\n" \
  "v_cvt_pk_bf16_f32 v232, v232, v233\n" \
  "v_cvt_pk_bf16_f32 v234, v234, v235\n" \
  "s_nop 1\n" \
  "v_cvt_pk_bf16_f32 v233, v236, v237\n" \
  "v_cvt_pk_bf16_f32 v235, v238, v239\n" \
  "s_add_u32 %[cnt], %[so], 7*%c[js]+1*%c[ss]\n" \
  "buffer_store_dwordx4 v[232:235], v251, %[rs], %[cnt] offen sc1\n" \
  "s_barrier\n" \
  "s_nop 1\n"
#define ST_XBS \
  "ds_read_b128 v[192:195], v251 offset:24576\n" \
  "ds_read_b128 v[208:211], v251 offset:28672\n" \
  "ds_read_b128 v[196:199], v251 offset:25600\n" \
  "ds_read_b128 v[212:215], v251 offset:29696\n" \
  "ds_read_b128 v[200:203], v251 offset:26624\n" \
  "ds_read_b128 v[216:219], v251 offset:30720\n" \
  "ds_read_b128 v[204:207], v251 offset:27648\n" \
  "ds_read_b128 v[220:223], v251 offset:31744\n" \
  "ds_read_b128 v[240:243], v250 offset:0\n" \
  "ds_read_b128 v[244:247], v250 offset:16384\n" \
  "s_waitcnt lgkmcnt(9)\n" \
  "v_mfma_f32_16x16x32_bf16 v[224:227], v[192:195], v[0:3], 0\n" \
  "s_waitcnt lgkmcnt(8)\n" \
  "v_mfma_f32_16x16x32_bf16 v[228:231], v[208:211], v[0:3], 0\n" \
  "s_waitcnt lgkmcnt(7)\n" \
  "v_mfma_f32_16x16x32_bf16 v[224:227], v[196:199], v[4:7], v[224:227]\n" \
  "s_waitcnt lgkmcnt(6)\n" \
  "v_mfma_f32_16x16x32_bf16 v[228:231], v[212:215], v[4:7], v[228:231]\n" \
  "s_waitcnt lgkmcnt(5)\n" \
  "v_mfma_f32_16x16x32_bf16 v[224:227], v[200:203], v[8:11], v[224:227]\n" \
  "s_waitcnt lgkmcnt(4)\n" \
  "v_mfma_f32_16x16x32_bf16 v[228:231], v[216:219], v[8:11], v[228:231]\n" \
  "s_waitcnt lgkmcnt(3)\n" \
  "v_mfma_f32_16x16x32_bf16 v[224:227], v[204:207], v[12:15], v[224:227]\n" \
  "s_waitcnt lgkmcnt(2)\n" \
  "v_mfma_f32_16x16x32_bf16 v[228:231], v[220:223], v[12:15], v[228:231]\n" \
  "v_mfma_f32_16x16x32_bf16 v[232:235], v[192:195], v[128:131], 0\n" \
  "v_mfma_f32_16x16x32_bf16 v[236:239], v[208:211], v[128:131], 0\n" \
  "v_mfma_f32_16x16x32_bf16 v[232:235], v[200:203], v[136:139], v[232:235]\n" \
  "v_cvt_pk_bf16_f32 v224, v224, v225\n" \
  "v_cvt_pk_bf16_f32 v226, v226, v227\n" \
  "v_mfma_f32_16x16x32_bf16 v[236:239], v[216:219], v[136:139], v[236:239]\n" \
  "v_cvt_pk_bf16_f32 v225, v228, v229\n" \
  "v_cvt_pk_bf16_f32 v227, v230, v231\n" \
  "s_waitcnt lgkmcnt(1)\n" \
  "v_mfma_f32_16x16x32_bf16 v[232:235], v[196:199], v[240:243], v[232:235]\n" \
  "s_add_u32 %[cnt], %[so], 0*%c[js]+0*%c[ss]\n" \
  "buffer_store_dwordx4 v[224:227], v251, %[rs], %[cnt] offen sc1\n" \
  "v_mfma_f32_16x16x32_bf16 v[236:239], v[212:215], v[240:243], v[236:239]\n" \
  "s_waitcnt lgkmcnt(0)\n" \
  "v_mfma_f32_16x16x32_bf16 v[232:235], v[204:207], v[244:247], v[232:235]\n" \
  "v_mfma_f32_16x16x32_bf16 v[236:239], v[220:223], v[244:247], v[236:239]\n" \
  "ds_read_b128 v[240:243], v250 offset:1024\n" \
  "ds_read_b128 v[244:247], v250 offset:17408\n" \
  "v_mfma_f32_16x16x32_bf16 v[224:227], v[192:195], v[64:67], 0\n" \
  "v_mfma_f32_16x16x32_bf16 v[228:231], v[208:211], v[64:67], 0\n" \
  "v_mfma_f32_16x16x32_bf16 v[224:227], v[196:199], v[68:71], v[224:227]\n" \
  "v_cvt_pk_bf16_f32 v232, v232, v233\n" \
  "v_cvt_pk_bf16_f32 v234, v234, v235\n" \
  "v_mfma_f32_16x16x32_bf16 v[228:231], v[212:215], v[68:71], v[228:231]\n" \
  "v_cvt_pk_bf16_f32 v233, v236, v237\n" \
  "v_cvt_pk_bf16_f32 v235, v238, v239\n" \
  "v_mfma_f32_16x16x32_bf16 v[224:227], v[200:203], v[72:75], v[224:227]\n" \
  "s_add_u32 %[cnt], %[so], 4*%c[js]+0*%c[ss]\n" \
  "buffer_store_dwordx4 v[232:235], v251, %[rs], %[cnt] offen sc1\n" \
  "v_mfma_f32_16x16x32_bf16 v[228:231], v[216:219], v[72:75], v[228:231]\n" \
  "v_mfma_f32_16x16x32_bf16 v[224:227], v[204:207], v[76:79], v[224:227]\n" \
  "v_mfma_f32_16x16x32_bf16 v[228:231], v[220:223], v[76:79], v[228:231]\n" \
  "v_mfma_f32_16x16x32_bf16 v[232:235], v[192:195], v[132:135], 0\n" \
  "v_mfma_f32_16x16x32_bf16 v[236:239], v[208:211], v[132:135], 0\n" \
  "v_mfma_f32_16x16x32_bf16 v[232:235], v[200:203], v[140:143], v[232:235]\n" \
  "v_cvt_pk_bf16_f32 v224, v224, v225\n" \
  "v_cvt_pk_bf16_f32 v226, v226, v227\n" \
  "v_mfma_f32_16x16x32_bf16 v[236:239], v[216:219], v[140:143], v[236:239]\n" \
  "v_cvt_pk_bf16_f32 v225, v228, v229\n" \
  "v_cvt_pk_bf16_f32 v227, v230, v231\n" \
  "s_waitcnt lgkmcnt(1)\n" \
  "v_mfma_f32_16x16x32_bf16 v[232:235], v[196:199], v[240:243], v[232:235]\n" \
  "s_add_u32 %[cnt], %[so], 0*%c[js]+1*%c[ss]\n" \
  "buffer_store_dwordx4 v[224:227], v251, %[rs], %[cnt] offen sc1\n" \
  "v_mfma_f32_16x16x32_bf16 v[236:239], v[212:215], v[240:243], v[236:239]\n" \
  "s_waitcnt lgkmcnt(0)\n" \
  "v_mfma_f32_16x16x32_bf16 v[232:235], v[204:207], v[244:247], v[232:235]\n" \
  "v_mfma_f32_16x16x32_bf16 v[236:239], v[220:223], v[244:247], v[236:239]\n" \
  "ds_read_b128 v[240:243], v250 offset:32768\n" \
  "ds_read_b128 v[244:247], v250 offset:49152\n" \
  "v_mfma_f32_16x16x32_bf16 v[224:227], v[192:195], v[16:19], 0\n" \
  "v_mfma_f32_16x16x32_bf16 v[228:231], v[208:211], v[16:19], 0\n" \
  "v_mfma_f32_16x16x32_bf16 v[224:227], v[196:199], v[20:23], v[224:227]\n" \
  "v_cvt_pk_bf16_f32 v232, v232, v233\n" \
  "v_cvt_pk_bf16_f32 v234, v234, v235\n" \
  "v_mfma_f32_16x16x32_bf16 v[228:231], v[212:215], v[20:23], v[228:231]\n" \
  "v_cvt_pk_bf16_f32 v233, v236, v237\n" \
  "v_cvt_pk_bf16_f32 v235, v238, v239\n" \
  "v_mfma_f32_16x16x32_bf16 v[224:227], v[200:203], v[24:27], v[224:227]\n" \
  "s_add_u32 %[cnt], %[so], 4*%c[js]+1*%c[ss]\n" \
  "buffer_store_dwordx4 v[232:235], v251, %[rs], %[cnt] offen sc1\n" \
  "v_mfma_f32_16x16x32_bf16 v[228:231], v[216:219], v[24:27], v[228:231]\n" \
  "v_mfma_f32_16x16x32_bf16 v[224:227], v[204:207], v[28:31], v[224:227]\n" \
  "v_mfma_f32_16x16x32_bf16 v[228:231], v[220:223], v[28:31], v[228:231]\n" \
  "v_mfma_f32_16x16x32_bf16 v[232:235], v[192:195], v[144:147], 0\n" \
  "v_mfma_f32_16x16x32_bf16 v[236:239], v[208:211], v[144:147], 0\n" \
  "v_mfma_f32_16x16x32_bf16 v[232:235], v[200:203], v[152:155], v[232:235]\n" \
  "v_cvt_pk_bf16_f32 v224, v224, v225\n" \
  "v_cvt_pk_bf16_f32 v226, v226, v227\n" \
  "v_mfma_f32_16x16x32_bf16 v[236:239], v[216:219], v[152:155], v[236:239]\n" \
  "v_cvt_pk_bf16_f32 v225, v228, v229\n" \
  "v_cvt_pk_bf16_f32 v227, v230, v231\n" \
  "s_waitcnt lgkmcnt(1)\n" \
  "v_mfma_f32_16x16x32_bf16 v[232:235], v[196:199], v[240:243], v[232:235]\n" \
  "s_add_u32 %[cnt], %[so], 1*%c[js]+0*%c[ss]\n" \
  "buffer_store_dwordx4 v[224:227], v251, %[rs], %[cnt] offen sc1\n" \
  "v_mfma_f32_16x16x32_bf16 v[236:239], v[212:215], v[240:243], v[236:239]\n" \
  "s_waitcnt lgkmcnt(0)\n" \
  "v_mfma_f32_16x16x32_bf16 v[232:235], v[204:207], v[244:247], v[232:235]\n" \
  "v_mfma_f32_16x16x32_bf16 v[236:239], v[220:223], v[244:247], v[236:239]\n" \
  "ds_read_b128 v[240:243], v250 offset:33792\n" \
  "ds_read_b128 v[244:247], v250 offset:50176\n" \
  "v_mfma_f32_16x16x32_bf16 v[224:227], v[192:195], v[80:83], 0\n" \
  "v_mfma_f32_16x16x32_bf16 v[228:231], v[208:211], v[80:83], 0\n" \
  "v_mfma_f32_16x16x32_bf16 v[224:227], v[196:199], v[84:87], v[224:227]\n" \
  "v_cvt_pk_bf16_f32 v232, v232, v233\n" \
  "v_cvt_pk_bf16_f32 v234, v234, v235\n" \
  "v_mfma_f32_16x16x32_bf16 v[228:231], v[212:215], v[84:87], v[228:231]\n" \
  "v_cvt_pk_bf16_f32 v233, v236, v237\n" \
  "v_cvt_pk_bf16_f32 v235, v238, v239\n" \
  "v_mfma_f32_16x16x32_bf16 v[224:227], v[200:203], v[88:91], v[224:227]\n" \
  "s_add_u32 %[cnt], %[so], 5*%c[js]+0*%c[ss]\n" \
  "buffer_store_dwordx4 v[232:235], v251, %[rs], %[cnt] offen sc1\n" \
  "v_mfma_f32_16x16x32_bf16 v[228:231], v[216:219], v[88:91], v[228:231]\n" \
  "v_mfma_f32_16x16x32_bf16 v[224:227], v[204:207], v[92:95], v[224:227]\n" \
  "v_mfma_f32_16x16x32_bf16 v[228:231], v[220:223], v[92:95], v[228:231]\n" \
  "v_mfma_f32_16x16x32_bf16 v[232:235], v[192:195], v[148:151], 0\n" \
  "v_mfma_f32_16x16x32_bf16 v[236:239], v[208:211], v[148:151], 0\n" \
  "v_mfma_f32_16x16x32_bf16 v[232:235], v[200:203], v[156:159], v[232:235]\n" \
  "v_cvt_pk_bf16_f32 v224, v224, v225\n" \
  "v_cvt_pk_bf16_f32 v226, v226, v227\n" \
  "v_mfma_f32_16x16x32_bf16 v[236:239], v[216:219], v[156:159], v[236:239]\n" \
  "v_cvt_pk_bf16_f32 v225, v228, v229\n" \
  "v_cvt_pk_bf16_f32 v227, v230, v231\n" \
  "s_waitcnt lgkmcnt(1)\n" \
  "v_mfma_f32_16x16x32_bf16 v[232:235], v[196:199], v[240:243], v[232:235]\n" \
  "s_add_u32 %[cnt], %[so], 1*%c[js]+1*%c[ss]\n" \
  "buffer_store_dwordx4 v[224:227], v251, %[rs], %[cnt] offen sc1\n" \
  "v_mfma_f32_16x16x32_bf16 v[236:239], v[212:215], v[240:243], v[236:239]\n" \
  "s_waitcnt lgkmcnt(0)\n" \
  "v_mfma_f32_16x16x32_bf16 v[232:235], v[204:207], v[244:247], v[232:235]\n" \
  "v_mfma_f32_16x16x32_bf16 v[236:239], v[220:223], v[244:247], v[236:239]\n" \
  "ds_read_b128 v[240:243], v249 offset:0\n" \
  "ds_read_b128 v[244:247], v249 offset:16384\n" \
  "s_barrier\n" \
  "v_mfma_f32_16x16x32_bf16 v[224:227], v[192:195], v[32:35], 0\n" \
  "v_mfma_f32_16x16x32_bf16 v[228:231], v[208:211], v[32:35], 0\n" \
  "v_mfma_f32_16x16x32_bf16 v[224:227], v[196:199], v[36:39], v[224:227]\n" \
  "v_cvt_pk_bf16_f32 v232, v232, v233\n" \
  "v_cvt_pk_bf16_f32 v234, v234, v235\n" \
  "v_mfma_f32_16x16x32_bf16 v[228:231], v[212:215], v[36:39], v[228:231]\n" \
  "v_cvt_pk_bf16_f32 v233, v236, v237\n" \
  "v_cvt_pk_bf16_f32 v235, v238, v239\n" \
  "v_mfma_f32_16x16x32_bf16 v[224:227], v[200:203], v[40:43], v[224:227]\n" \
  "s_add_u32 %[cnt], %[so], 5*%c[js]+1*%c[ss]\n" \
  "buffer_store_dwordx4 v[232:235], v251, %[rs], %[cnt] offen sc1\n" \
  "v_mfma_f32_16x16x32_bf16 v[228:231], v[216:219], v[40:43], v[228:231]\n" \
  "v_mfma_f32_16x16x32_bf16 v[224:227], v[204:207], v[44:47], v[224:227]\n" \
  "v_mfma_f32_16x16x32_bf16 v[228:231], v[220:223], v[44:47], v[228:231]\n" \
  "v_mfma_f32_16x16x32_bf16 v[232:235], v[192:195], v[160:163], 0\n" \
  "v_mfma_f32_16x16x32_bf16 v[236:239], v[208:211], v[160:163], 0\n" \
  "v_mfma_f32_16x16x32_bf16 v[232:235], v[200:203], v[168:171], v[232:235]\n" \
  "v_cvt_pk_bf16_f32 v224, v224, v225\n" \
  "v_cvt_pk_bf16_f32 v226, v226, v227\n" \
  "v_mfma_f32_16x16x32_bf16 v[236:239], v[216:219], v[168:171], v[236:239]\n" \
  "v_cvt_pk_bf16_f32 v225, v228, v229\n" \
  "v_cvt_pk_bf16_f32 v227, v230, v231\n" \
  "s_waitcnt lgkmcnt(1)\n" \
  "v_mfma_f32_16x16x32_bf16 v[232:235], v[196:199], v[240:243], v[232:235]\n" \
  "s_add_u32 %[cnt], %[so], 2*%c[js]+0*%c[ss]\n" \
  "buffer_store_dwordx4 v[224:227], v251, %[rs], %[cnt] offen sc1\n" \
  "v_mfma_f32_16x16x32_bf16 v[236:239], v[212:215], v[240:243], v[236:239]\n" \
  "s_waitcnt lgkmcnt(0)\n" \
  "v_mfma_f32_16x16x32_bf16 v[232:235], v[204:207], v[244:247], v[232:235]\n" \
  "v_mfma_f32_16x16x32_bf16 v[236:239], v[220:223], v[244:247], v[236:239]\n" \
  "ds_read_b128 v[240:243], v249 offset:1024\n" \
  "ds_read_b128 v[244:247], v249 offset:17408\n" \
  "v_mfma_f32_16x16x32_bf16 v[224:227], v[192:195], v[96:99], 0\n" \
  "v_mfma_f32_16x16x32_bf16 v[228:231], v[208:211], v[96:99], 0\n" \
  "v_mfma_f32_16x16x32_bf16 v[224:227], v[196:199], v[100:103], v[224:227]\n" \
  "v_cvt_pk_bf16_f32 v232, v232, v233\n" \
  "v_cvt_pk_bf16_f32 v234, v234, v235\n" \
  "v_mfma_f32_16x16x32_bf16 v[228:231], v[212:215], v[100:103], v[228:231]\n" \
  "v_cvt_pk_bf16_f32 v233, v236, v237\n" \
  "v_cvt_pk_bf16_f32 v235, v238, v239\n" \
  "v_mfma_f32_16x16x32_bf16 v[224:227], v[200:203], v[104:107], v[224:227]\n" \
  "s_add_u32 %[cnt], %[so], 6*%c[js]+0*%c[ss]\n" \
  "buffer_store_dwordx4 v[232:235], v251, %[rs], %[cnt] offen sc1\n" \
  "v_mfma_f32_16x16x32_bf16 v[228:231], v[216:219], v[104:107], v[228:231]\n" \
  "v_mfma_f32_16x16x32_bf16 v[224:227], v[204:207], v[108:111], v[224:227]\n" \
  "v_mfma_f32_16x16x32_bf16 v[228:231], v[220:223], v[108:111], v[228:231]\n" \
  "v_mfma_f32_16x16x32_bf16 v[232:235], v[192:195], v[164:167], 0\n" \
  "v_mfma_f32_16x16x32_bf16 v[236:239], v[208:211], v[164:167], 0\n" \
  "v_mfma_f32_16x16x32_bf16 v[232:235], v[200:203], v[172:175], v[232:235]\n" \
  "v_cvt_pk_bf16_f32 v224, v224, v225\n" \
  "v_cvt_pk_bf16_f32 v226, v226, v227\n" \
  "v_mfma_f32_16x16x32_bf16 v[236:239], v[216:219], v[172:175], v[236:239]\n" \
  "v_cvt_pk_bf16_f32 v225, v228, v229\n" \
  "v_cvt_pk_bf16_f32 v227, v230, v231\n" \
  "s_waitcnt lgkmcnt(1)\n" \
  "v_mfma_f32_16x16x32_bf16 v[232:235], v[196:199], v[240:243], v[232:235]\n" \
  "s_add_u32 %[cnt], %[so], 2*%c[js]+1*%c[ss]\n" \
  "buffer_store_dwordx4 v[224:227], v251, %[rs], %[cnt] offen sc1\n" \
  "v_mfma_f32_16x16x32_bf16 v[236:239], v[212:215], v[240:243], v[236:239]\n" \
  "s_waitcnt lgkmcnt(0)\n" \
  "v_mfma_f32_16x16x32_bf16 v[232:235], v[204:207], v[244:247], v[232:235]\n" \
  "v_mfma_f32_16x16x32_bf16 v[236:239], v[220:223], v[244:247], v[236:239]\n" \
  "ds_read_b128 v[240:243], v249 offset:32768\n" \
  "ds_read_b128 v[244:247], v249 offset:49152\n" \
  "s_barrier\n" \
  "v_mfma_f32_16x16x32_bf16 v[224:227], v[192:195], v[48:51], 0\n" \
  "v_mfma_f32_16x16x32_bf16 v[228:231], v[208:211], v[48:51], 0\n" \
  "v_mfma_f32_16x16x32_bf16 v[224:227], v[196:199], v[52:55], v[224:227]\n" \
  "v_cvt_pk_bf16_f32 v232, v232, v233\n" \
  "v_cvt_pk_bf16_f32 v234, v234, v235\n" \
  "v_mfma_f32_16x16x32_bf16 v[228:231], v[212:215], v[52:55], v[228:231]\n" \
  "v_cvt_pk_bf16_f32 v233, v236, v237\n" \
  "v_cvt_pk_bf16_f32 v235, v238, v239\n" \
  "v_mfma_f32_16x16x32_bf16 v[224:227], v[200:203], v[56:59], v[224:227]\n" \
  "s_add_u32 %[cnt], %[so], 6*%c[js]+1*%c[ss]\n" \
  "buffer_store_dwordx4 v[232:235], v251, %[rs], %[cnt] offen sc1\n" \
  "v_mfma_f32_16x16x32_bf16 v[228:231], v[216:219], v[56:59], v[228:231]\n" \
  "v_mfma_f32_16x16x32_bf16 v[224:227], v[204:207], v[60:63], v[224:227]\n" \
  "v_mfma_f32_16x16x32_bf16 v[228:231], v[220:223], v[60:63], v[228:231]\n" \
  "v_mfma_f32_16x16x32_bf16 v[232:235], v[192:195], v[176:179], 0\n" \
  "v_mfma_f32_16x16x32_bf16 v[236:239], v[208:211], v[176:179], 0\n" \
  "v_mfma_f32_16x16x32_bf16 v[232:235], v[200:203], v[184:187], v[232:235]\n" \
  "v_cvt_pk_bf16_f32 v224, v224, v225\n" \
  "v_cvt_pk_bf16_f32 v226, v226, v227\n" \
  "v_mfma_f32_16x16x32_bf16 v[236:239], v[216:219], v[184:187], v[236:239]\n" \
  "v_cvt_pk_bf16_f32 v225, v228, v229\n" \
  "v_cvt_pk_bf16_f32 v227, v230, v231\n" \
  "s_waitcnt lgkmcnt(1)\n" \
  "v_mfma_f32_16x16x32_bf16 v[232:235], v[196:199], v[240:243], v[232:235]\n" \
  "s_add_u32 %[cnt], %[so], 3*%c[js]+0*%c[ss]\n" \
  "buffer_store_dwordx4 v[224:227], v251, %[rs], %[cnt] offen sc1\n" \
  "v_mfma_f32_16x16x32_bf16 v[236:239], v[212:215], v[240:243], v[236:239]\n" \
  "s_waitcnt lgkmcnt(0)\n" \
  "v_mfma_f32_16x16x32_bf16 v[232:235], v[204:207], v[244:247], v[232:235]\n" \
  "v_mfma_f32_16x16x32_bf16 v[236:239], v[220:223], v[244:247], v[236:239]\n" \
  "ds_read_b128 v[240:243], v249 offset:33792\n" \
  "ds_read_b128 v[244:247], v249 offset:50176\n" \
  "v_mfma_f32_16x16x32_bf16 v[224:227], v[192:195], v[112:115], 0\n" \
  "v_mfma_f32_16x16x32_bf16 v[228:231], v[208:211], v[112:115], 0\n" \
  "v_mfma_f32_16x16x32_bf16 v[224:227], v[196:199], v[116:119], v[224:227]\n" \
  "v_cvt_pk_bf16_f32 v232, v232, v233\n" \
  "v_cvt_pk_bf16_f32 v234, v234, v235\n" \
  "v_mfma_f32_16x16x32_bf16 v[228:231], v[212:215], v[116:119], v[228:231]\n" \
  "v_cvt_pk_bf16_f32 v233, v236, v237\n" \
  "v_cvt_pk_bf16_f32 v235, v238, v239\n" \
  "v_mfma_f32_16x16x32_bf16 v[224:227], v[200:203], v[120:123], v[224:227]\n" \
  "s_add_u32 %[cnt], %[so], 7*%c[js]+0*%c[ss]\n" \
  "buffer_store_dwordx4 v[232:235], v251, %[rs], %[cnt] offen sc1\n" \
  "v_mfma_f32_16x16x32_bf16 v[228:231], v[216:219], v[120:123], v[228:231]\n" \
  "v_mfma_f32_16x16x32_bf16 v[224:227], v[204:207], v[124:127], v[224:227]\n" \
  "v_mfma_f32_16x16x32_bf16 v[228:231], v[220:223], v[124:127], v[228:231]\n" \
  "v_mfma_f32_16x16x32_bf16 v[232:235], v[192:195], v[180:183], 0\n" \
  "v_mfma_f32_16x16x32_bf16 v[236:239], v[208:211], v[180:183], 0\n" \
  "v_mfma_f32_16x16x32_bf16 v[232:235], v[200:203], v[188:191], v[232:235]\n" \
  "v_cvt_pk_bf16_f32 v224, v224, v225\n" \
  "v_cvt_pk_bf16_f32 v226, v226, v227\n" \
  "v_mfma_f32_16x16x32_bf16 v[236:239], v[216:219], v[188:191], v[236:239]\n" \
  "v_cvt_pk_bf16_f32 v225, v228, v229\n" \
  "v_cvt_pk_bf16_f32 v227, v230, v231\n" \
  "s_waitcnt lgkmcnt(1)\n" \
  "v_mfma_f32_16x16x32_bf16 v[232:235], v[196:199], v[240:243], v[232:235]\n" \
  "s_add_u32 %[cnt], %[so], 3*%c[js]+1*%c[ss]\n" \
  "buffer_store_dwordx4 v[224:227], v251, %[rs], %[cnt] offen sc1\n" \
  "v_mfma_f32_16x16x32_bf16 v[236:239], v[212:215], v[240:243], v[236:239]\n" \
  "s_waitcnt lgkmcnt(0)\n" \
  "v_mfma_f32_16x16x32_bf16 v[232:235], v[204:207], v[244:247], v[232:235]\n" \
  "v_mfma_f32_16x16x32_bf16 v[236:239], v[220:223], v[244:247], v[236:239]\n" \
  "s_nop 4\n" \
  "v_cvt_pk_bf16_f32 v232, v232, v233\n" \
  "v_cvt_pk_bf16_f32 v234, v234, v235\n" \
  "s_nop 1\n" \
  "v_cvt_pk_bf16_f32 v233, v236, v237\n" \
  "v_cvt_pk_bf16_f32 v235, v238, v239\n" \
  "s_add_u32 %[cnt], %[so], 7*%c[js]+1*%c[ss]\n" \
  "buffer_store_dwordx4 v[232:235], v251, %[rs], %[cnt] offen sc1\n" \
  "s_nop 1\n"
#define ST_XB0 \
  "ds_read_b128 v[192:195], v251 offset:24576\n" \
  "ds_read_b128 v[208:211], v251 offset:28672\n" \
  "ds_read_b128 v[196:199], v251 offset:25600\n" \
  "ds_read_b128 v[212:215], v251 offset:29696\n" \
  "ds_read_b128 v[200:203], v251 offset:26624\n" \
  "ds_read_b128 v[216:219], v251 offset:30720\n" \
  "ds_read_b128 v[204:207], v251 offset:27648\n" \
  "ds_read_b128 v[220:223], v251 offset:31744\n" \
  "ds_read_b128 v[240:243], v250 offset:0\n" \
  "ds_read_b128 v[244:247], v250 offset:16384\n" \
  "s_waitcnt lgkmcnt(9)\n" \
  "v_mfma_f32_16x16x32_bf16 v[224:227], v[192:195], v[0:3], 0\n" \
  "s_waitcnt lgkmcnt(8)\n" \
  "v_mfma_f32_16x16x32_bf16 v[228:231], v[208:211], v[0:3], 0\n" \
  "s_waitcnt lgkmcnt(7)\n" \
  "v_mfma_f32_16x16x32_bf16 v[224:227], v[196:199], v[4:7], v[224:227]\n" \
  "s_waitcnt lgkmcnt(6)\n" \
  "v_mfma_f32_16x16x32_bf16 v[228:231], v[212:215], v[4:7], v[228:231]\n" \
  "s_waitcnt lgkmcnt(5)\n" \
  "v_mfma_f32_16x16x32_bf16 v[224:227], v[200:203], v[8:11], v[224:227]\n" \
  "s_waitcnt lgkmcnt(4)\n" \
  "v_mfma_f32_16x16x32_bf16 v[228:231], v[216:219], v[8:11], v[228:231]\n" \
  "s_waitcnt lgkmcnt(3)\n" \
  "v_mfma_f32_16x16x32_bf16 v[224:227], v[204:207], v[12:15], v[224:227]\n" \
  "s_waitcnt lgkmcnt(2)\n" \
  "v_mfma_f32_16x16x32_bf16 v[228:231], v[220:223], v[12:15], v[228:231]\n" \
  "v_mfma_f32_16x16x32_bf16 v[232:235], v[192:195], v[128:131], 0\n" \
  "v_mfma_f32_16x16x32_bf16 v[236:239], v[208:211], v[128:131], 0\n" \
  "v_mfma_f32_16x16x32_bf16 v[232:235], v[200:203], v[136:139], v[232:235]\n" \
  "v_cvt_pk_bf16_f32 v224, v224, v225\n" \
  "v_cvt_pk_bf16_f32 v226, v226, v227\n" \
  "v_mfma_f32_16x16x32_bf16 v[236:239], v[216:219], v[136:139], v[236:239]\n" \
  "v_cvt_pk_bf16_f32 v225, v228, v229\n" \
  "v_cvt_pk_bf16_f32 v227, v230, v231\n" \
  "s_waitcnt lgkmcnt(1)\n" \
  "v_mfma_f32_16x16x32_bf16 v[232:235], v[196:199], v[240:243], v[232:235]\n" \
  "s_add_u32 %[cnt], %[so], 0*%c[js]+0*%c[ss]\n" \
  "buffer_store_dwordx4 v[224:227], v251, %[rs], %[cnt] offen sc1\n" \
  "v_mfma_f32_16x16x32_bf16 v[236:239], v[212:215], v[240:243], v[236:239]\n" \
  "s_waitcnt lgkmcnt(0)\n" \
  "v_mfma_f32_16x16x32_bf16 v[232:235], v[204:207], v[244:247], v[232:235]\n" \
  "v_mfma_f32_16x16x32_bf16 v[236:239], v[220:223], v[244:247], v[236:239]\n" \
  "ds_read_b128 v[240:243], v250 offset:1024\n" \
  "ds_read_b128 v[244:247], v250 offset:17408\n" \
  "v_mfma_f32_16x16x32_bf16 v[224:227], v[192:195], v[64:67], 0\n" \
  "v_mfma_f32_16x16x32_bf16 v[228:231], v[208:211], v[64:67], 0\n" \
  "v_mfma_f32_16x16x32_bf16 v[224:227], v[196:199], v[68:71], v[224:227]\n" \
  "v_cvt_pk_bf16_f32 v232, v232, v233\n" \
  "v_cvt_pk_bf16_f32 v234, v234, v235\n" \
  "v_mfma_f32_16x16x32_bf16 v[228:231], v[212:215], v[68:71], v[228:231]\n" \
  "v_cvt_pk_bf16_f32 v233, v236, v237\n" \
  "v_cvt_pk_bf16_f32 v235, v238, v239\n" \
  "v_mfma_f32_16x16x32_bf16 v[224:227], v[200:203], v[72:75], v[224:227]\n" \
  "s_add_u32 %[cnt], %[so], 4*%c[js]+0*%c[ss]\n" \
  "buffer_store_dwordx4 v[232:235], v251, %[rs], %[cnt] offen sc1\n" \
  "v_mfma_f32_16x16x32_bf16 v[228:231], v[216:219], v[72:75], v[228:231]\n" \
  "v_mfma_f32_16x16x32_bf16 v[224:227], v[204:207], v[76:79], v[224:227]\n" \
  "v_mfma_f32_16x16x32_bf16 v[228:231], v[220:223], v[76:79], v[228:231]\n" \
  "v_mfma_f32_16x16x32_bf16 v[232:235], v[192:195], v[132:135], 0\n" \
  "v_mfma_f32_16x16x32_bf16 v[236:239], v[208:211], v[132:135], 0\n" \
  "v_mfma_f32_16x16x32_bf16 v[232:235], v[200:203], v[140:143], v[232:235]\n" \
  "v_cvt_pk_bf16_f32 v224, v224, v225\n" \
  "v_cvt_pk_bf16_f32 v226, v226, v227\n" \
  "v_mfma_f32_16x16x32_bf16 v[236:239], v[216:219], v[140:143], v[236:239]\n" \
  "v_cvt_pk_bf16_f32 v225, v228, v229\n" \
  "v_cvt_pk_bf16_f32 v227, v230, v231\n" \
  "s_waitcnt lgkmcnt(1)\n" \
  "v_mfma_f32_16x16x32_bf16 v[232:235], v[196:199], v[240:243], v[232:235]\n" \
  "s_add_u32 %[cnt], %[so], 0*%c[js]+1*%c[ss]\n" \
  "buffer_store_dwordx4 v[224:227], v251, %[rs], %[cnt] offen sc1\n" \
  "v_mfma_f32_16x16x32_bf16 v[236:239], v[212:215], v[240:243], v[236:239]\n" \
  "s_waitcnt lgkmcnt(0)\n" \
  "v_mfma_f32_16x16x32_bf16 v[232:235], v[204:207], v[244:247], v[232:235]\n" \
  "v_mfma_f32_16x16x32_bf16 v[236:239], v[220:223], v[244:247], v[236:239]\n" \
  "ds_read_b128 v[240:243], v250 offset:32768\n" \
  "ds_read_b128 v[244:247], v250 offset:49152\n" \
  "v_mfma_f32_16x16x32_bf16 v[224:227], v[192:195], v[16:19], 0\n" \
  "v_mfma_f32_16x16x32_bf16 v[228:231], v[208:211], v[16:19], 0\n" \
  "v_mfma_f32_16x16x32_bf16 v[224:227], v[196:199], v[20:23], v[224:227]\n" \
  "v_cvt_pk_bf16_f32 v232, v232, v233\n" \
  "v_cvt_pk_bf16_f32 v234, v234, v235\n" \
  "v_mfma_f32_16x16x32_bf16 v[228:231], v[212:215], v[20:23], v[228:231]\n" \
  "v_cvt_pk_bf16_f32 v233, v236, v237\n" \
  "v_cvt_pk_bf16_f32 v235, v238, v239\n" \
  "v_mfma_f32_16x16x32_bf16 v[224:227], v[200:203], v[24:27], v[224:227]\n" \
  "s_add_u32 %[cnt], %[so], 4*%c[js]+1*%c[ss]\n" \
  "buffer_store_dwordx4 v[232:235], v251, %[rs], %[cnt] offen sc1\n" \
  "v_mfma_f32_16x16x32_bf16 v[228:231], v[216:219], v[24:27], v[228:231]\n" \
  "v_mfma_f32_16x16x32_bf16 v[224:227], v[204:207], v[28:31], v[224:227]\n" \
  "v_mfma_f32_16x16x32_bf16 v[228:231], v[220:223], v[28:31], v[228:231]\n" \
  "v_mfma_f32_16x16x32_bf16 v[232:235], v[192:195], v[144:147], 0\n" \
  "v_mfma_f32_16x16x32_bf16 v[236:239], v[208:211], v[144:147], 0\n" \
  "v_mfma_f32_16x16x32_bf16 v[232:235], v[200:203], v[152:155], v[232:235]\n" \
  "v_cvt_pk_bf16_f32 v224, v224, v225\n" \
  "v_cvt_pk_bf16_f32 v226, v226, v227\n" \
  "v_mfma_f32_16x16x32_bf16 v[236:239], v[216:219], v[152:155], v[236:239]\n" \
  "v_cvt_pk_bf16_f32 v225, v228, v229\n" \
  "v_cvt_pk_bf16_f32 v227, v230, v231\n" \
  "s_waitcnt lgkmcnt(1)\n" \
  "v_mfma_f32_16x16x32_bf16 v[232:235], v[196:199], v[240:243], v[232:235]\n" \
  "s_add_u32 %[cnt], %[so], 1*%c[js]+0*%c[ss]\n" \
  "buffer_store_dwordx4 v[224:227], v251, %[rs], %[cnt] offen sc1\n" \
  "v_mfma_f32_16x16x32_bf16 v[236:239], v[212:215], v[240:243], v[236:239]\n" \
  "s_waitcnt lgkmcnt(0)\n" \
  "v_mfma_f32_16x16x32_bf16 v[232:235], v[204:207], v[244:247], v[232:235]\n" \
  "v_mfma_f32_16x16x32_bf16 v[236:239], v[220:223], v[244:247], v[236:239]\n" \
  "ds_read_b128 v[240:243], v250 offset:33792\n" \
  "ds_read_b128 v[244:247], v250 offset:50176\n" \
  "v_mfma_f32_16x16x32_bf16 v[224:227], v[192:195], v[80:83], 0\n" \
  "v_mfma_f32_16x16x32_bf16 v[228:231], v[208:211], v[80:83], 0\n" \
  "v_mfma_f32_16x16x32_bf16 v[224:227], v[196:199], v[84:87], v[224:227]\n" \
  "v_cvt_pk_bf16_f32 v232, v232, v233\n" \
  "v_cvt_pk_bf16_f32 v234, v234, v235\n" \
  "v_mfma_f32_16x16x32_bf16 v[228:231], v[212:215], v[84:87], v[228:231]\n" \
  "v_cvt_pk_bf16_f32 v233, v236, v237\n" \
  "v_cvt_pk_bf16_f32 v235, v238, v239\n" \
  "v_mfma_f32_16x16x32_bf16 v[224:227], v[200:203], v[88:91], v[224:227]\n" \
  "s_add_u32 %[cnt], %[so], 5*%c[js]+0*%c[ss]\n" \
  "buffer_store_dwordx4 v[232:235], v251, %[rs], %[cnt] offen sc1\n" \
  "v_mfma_f32_16x16x32_bf16 v[228:231], v[216:219], v[88:91], v[228:231]\n" \
  "v_mfma_f32_16x16x32_bf16 v[224:227], v[204:207], v[92:95], v[224:227]\n" \
  "v_mfma_f32_16x16x32_bf16 v[228:231], v[220:223], v[92:95], v[228:231]\n" \
  "v_mfma_f32_16x16x32_bf16 v[232:235], v[192:195], v[148:151], 0\n" \
  "v_mfma_f32_16x16x32_bf16 v[236:239], v[208:211], v[148:151], 0\n" \
  "v_mfma_f32_16x16x32_bf16 v[232:235], v[200:203], v[156:159], v[232:235]\n" \
  "v_cvt_pk_bf16_f32 v224, v224, v225\n" \
  "v_cvt_pk_bf16_f32 v226, v226, v227\n" \
  "v_mfma_f32_16x16x32_bf16 v[236:239], v[216:219], v[156:159], v[236:239]\n" \
  "v_cvt_pk_bf16_f32 v225, v228, v229\n" \
  "v_cvt_pk_bf16_f32 v227, v230, v231\n" \
  "s_waitcnt lgkmcnt(1)\n" \
  "v_mfma_f32_16x16x32_bf16 v[232:235], v[196:199], v[240:243], v[232:235]\n" \
  "s_add_u32 %[cnt], %[so], 1*%c[js]+1*%c[ss]\n" \
  "buffer_store_dwordx4 v[224:227], v251, %[rs], %[cnt] offen sc1\n" \
  "v_mfma_f32_16x16x32_bf16 v[236:239], v[212:215], v[240:243], v[236:239]\n" \
  "s_waitcnt lgkmcnt(0)\n" \
  "v_mfma_f32_16x16x32_bf16 v[232:235], v[204:207], v[244:247], v[232:235]\n" \
  "v_mfma_f32_16x16x32_bf16 v[236:239], v[220:223], v[244:247], v[236:239]\n" \
  "ds_read_b128 v[240:243], v249 offset:0\n" \
  "ds_read_b128 v[244:247], v249 offset:16384\n" \
  "v_mfma_f32_16x16x32_bf16 v[224:227], v[192:195], v[32:35], 0\n" \
  "v_mfma_f32_16x16x32_bf16 v[228:231], v[208:211], v[32:35], 0\n" \
  "v_mfma_f32_16x16x32_bf16 v[224:227], v[196:199], v[36:39], v[224:227]\n" \
  "v_cvt_pk_bf16_f32 v232, v232, v233\n" \
  "v_cvt_pk_bf16_f32 v234, v234, v235\n" \
  "v_mfma_f32_16x16x32_bf16 v[228:231], v[212:215], v[36:39], v[228:231]\n" \
  "v_cvt_pk_bf16_f32 v233, v236, v237\n" \
  "v_cvt_pk_bf16_f32 v235, v238, v239\n" \
  "v_mfma_f32_16x16x32_bf16 v[224:227], v[200:203], v[40:43], v[224:227]\n" \
  "s_add_u32 %[cnt], %[so], 5*%c[js]+1*%c[ss]\n" \
  "buffer_store_dwordx4 v[232:235], v251, %[rs], %[cnt] offen sc1\n" \
  "v_mfma_f32_16x16x32_bf16 v[228:231], v[216:219], v[40:43], v[228:231]\n" \
  "v_mfma_f32_16x16x32_bf16 v[224:227], v[204:207], v[44:47], v[224:227]\n" \
  "v_mfma_f32_16x16x32_bf16 v[228:231], v[220:223], v[44:47], v[228:231]\n" \
  "v_mfma_f32_16x16x32_bf16 v[232:235], v[192:195], v[160:163], 0\n" \
  "v_mfma_f32_16x16x32_bf16 v[236:239], v[208:211], v[160:163], 0\n" \
  "v_mfma_f32_16x16x32_bf16 v[232:235], v[200:203], v[168:171], v[232:235]\n" \
  "v_cvt_pk_bf16_f32 v224, v224, v225\n" \
  "v_cvt_pk_bf16_f32 v226, v226, v227\n" \
  "v_mfma_f32_16x16x32_bf16 v[236:239], v[216:219], v[168:171], v[236:239]\n" \
  "v_cvt_pk_bf16_f32 v225, v228, v229\n" \
  "v_cvt_pk_bf16_f32 v227, v230, v231\n" \
  "s_waitcnt lgkmcnt(1)\n" \
  "v_mfma_f32_16x16x32_bf16 v[232:235], v[196:199], v[240:243], v[232:235]\n" \
  "s_add_u32 %[cnt], %[so], 2*%c[js]+0*%c[ss]\n" \
  "buffer_store_dwordx4 v[224:227], v251, %[rs], %[cnt] offen sc1\n" \
  "v_mfma_f32_16x16x32_bf16 v[236:239], v[212:215], v[240:243], v[236:239]\n" \
  "s_waitcnt lgkmcnt(0)\n" \
  "v_mfma_f32_16x16x32_bf16 v[232:235], v[204:207], v[244:247], v[232:235]\n" \
  "v_mfma_f32_16x16x32_bf16 v[236:239], v[220:223], v[244:247], v[236:239]\n" \
  "ds_read_b128 v[240:243], v249 offset:1024\n" \
  "ds_read_b128 v[244:247], v249 offset:17408\n" \
  "v_mfma_f32_16x16x32_bf16 v[224:227], v[192:195], v[96:99], 0\n" \
  "v_mfma_f32_16x16x32_bf16 v[228:231], v[208:211], v[96:99], 0\n" \
  "v_mfma_f32_16x16x32_bf16 v[224:227], v[196:199], v[100:103], v[224:227]\n" \
  "v_cvt_pk_bf16_f32 v232, v232, v233\n" \
  "v_cvt_pk_bf16_f32 v234, v234, v235\n" \
  "v_mfma_f32_16x16x32_bf16 v[228:231], v[212:215], v[100:103], v[228:231]\n" \
  "v_cvt_pk_bf16_f32 v233, v236, v237\n" \
  "v_cvt_pk_bf16_f32 v235, v238, v239\n" \
  "v_mfma_f32_16x16x32_bf16 v[224:227], v[200:203], v[104:107], v[224:227]\n" \
  "s_add_u32 %[cnt], %[so], 6*%c[js]+0*%c[ss]\n" \
  "buffer_store_dwordx4 v[232:235], v251, %[rs], %[cnt] offen sc1\n" \
  "v_mfma_f32_16x16x32_bf16 v[228:231], v[216:219], v[104:107], v[228:231]\n" \
  "v_mfma_f32_16x16x32_bf16 v[224:227], v[204:207], v[108:111], v[224:227]\n" \
  "v_mfma_f32_16x16x32_bf16 v[228:231], v[220:223], v[108:111], v[228:231]\n" \
  "v_mfma_f32_16x16x32_bf16 v[232:235], v[192:195], v[164:167], 0\n" \
  "v_mfma_f32_16x16x32_bf16 v[236:239], v[208:211], v[164:167], 0\n" \
  "v_mfma_f32_16x16x32_bf16 v[232:235], v[200:203], v[172:175], v[232:235]\n" \
  "v_cvt_pk_bf16_f32 v224, v224, v225\n" \
  "v_cvt_pk_bf16_f32 v226, v226, v227\n" \
  "v_mfma_f32_16x16x32_bf16 v[236:239], v[216:219], v[172:175], v[236:239]\n" \
  "v_cvt_pk_bf16_f32 v225, v228, v229\n" \
  "v_cvt_pk_bf16_f32 v227, v230, v231\n" \
  "s_waitcnt lgkmcnt(1)\n" \
  "v_mfma_f32_16x16x32_bf16 v[232:235], v[196:199], v[240:243], v[232:235]\n" \
  "s_add_u32 %[cnt], %[so], 2*%c[js]+1*%c[ss]\n" \
  "buffer_store_dwordx4 v[224:227], v251, %[rs], %[cnt] offen sc1\n" \
  "v_mfma_f32_16x16x32_bf16 v[236:239], v[212:215], v[240:243], v[236:239]\n" \
  "s_waitcnt lgkmcnt(0)\n" \
  "v_mfma_f32_16x16x32_bf16 v[232:235], v[204:207], v[244:247], v[232:235]\n" \
  "v_mfma_f32_16x16x32_bf16 v[236:239], v[220:223], v[244:247], v[236:239]\n" \
  "ds_read_b128 v[240:243], v249 offset:32768\n" \
  "ds_read_b128 v[244:247], v249 offset:49152\n" \
  "v_mfma_f32_16x16x32_bf16 v[224:227], v[192:195], v[48:51], 0\n" \
  "v_mfma_f32_16x16x32_bf16 v[228:231], v[208:211], v[48:51], 0\n" \
  "v_mfma_f32_16x16x32_bf16 v[224:227], v[196:199], v[52:55], v[224:227]\n" \
  "v_cvt_pk_bf16_f32 v232, v232, v233\n" \
  "v_cvt_pk_bf16_f32 v234, v234, v235\n" \
  "v_mfma_f32_16x16x32_bf16 v[228:231], v[212:215], v[52:55], v[228:231]\n" \
  "v_cvt_pk_bf16_f32 v233, v236, v237\n" \
  "v_cvt_pk_bf16_f32 v235, v238, v239\n" \
  "v_mfma_f32_16x16x32_bf16 v[224:227], v[200:203], v[56:59], v[224:227]\n" \
  "s_add_u32 %[cnt], %[so], 6*%c[js]+1*%c[ss]\n" \
  "buffer_store_dwordx4 v[232:235], v251, %[rs], %[cnt] offen sc1\n" \
  "v_mfma_f32_16x16x32_bf16 v[228:231], v[216:219], v[56:59], v[228:231]\n" \
  "v_mfma_f32_16x16x32_bf16 v[224:227], v[204:207], v[60:63], v[224:227]\n" \
  "v_mfma_f32_16x16x32_bf16 v[228:231], v[220:223], v[60:63], v[228:231]\n" \
  "v_mfma_f32_16x16x32_bf16 v[232:235], v[192:195], v[176:179], 0\n" \
  "v_mfma_f32_16x16x32_bf16 v[236:239], v[208:211], v[176:179], 0\n" \
  "v_mfma_f32_16x16x32_bf16 v[232:235], v[200:203], v[184:187], v[232:235]\n" \
  "v_cvt_pk_bf16_f32 v224, v224, v225\n" \
  "v_cvt_pk_bf16_f32 v226, v226, v227\n" \
  "v_mfma_f32_16x16x32_bf16 v[236:239], v[216:219], v[184:187], v[236:239]\n" \
  "v_cvt_pk_bf16_f32 v225, v228, v229\n" \
  "v_cvt_pk_bf16_f32 v227, v230, v231\n" \
  "s_waitcnt lgkmcnt(1)\n" \
  "v_mfma_f32_16x16x32_bf16 v[232:235], v[196:199], v[240:243], v[232:235]\n" \
  "s_add_u32 %[cnt], %[so], 3*%c[js]+0*%c[ss]\n" \
  "buffer_store_dwordx4 v[224:227], v251, %[rs], %[cnt] offen sc1\n" \
  "v_mfma_f32_16x16x32_bf16 v[236:239], v[212:215], v[240:243], v[236:239]\n" \
  "s_waitcnt lgkmcnt(0)\n" \
  "v_mfma_f32_16x16x32_bf16 v[232:235], v[204:207], v[244:247], v[232:235]\n" \
  "v_mfma_f32_16x16x32_bf16 v[236:239], v[220:223], v[244:247], v[236:239]\n" \
  "ds_read_b128 v[240:243], v249 offset:33792\n" \
  "ds_read_b128 v[244:247], v249 offset:50176\n" \
  "v_mfma_f32_16x16x32_bf16 v[224:227], v[192:195], v[112:115], 0\n" \
  "v_mfma_f32_16x16x32_bf16 v[228:231], v[208:211], v[112:115], 0\n" \
  "v_mfma_f32_16x16x32_bf16 v[224:227], v[196:199], v[116:119], v[224:227]\n" \
  "v_cvt_pk_bf16_f32 v232, v232, v233\n" \
  "v_cvt_pk_bf16_f32 v234, v234, v235\n" \
  "v_mfma_f32_16x16x32_bf16 v[228:231], v[212:215], v[116:119], v[228:231]\n" \
  "v_cvt_pk_bf16_f32 v233, v236, v237\n" \
  "v_cvt_pk_bf16_f32 v235, v238, v239\n" \
  "v_mfma_f32_16x16x32_bf16 v[224:227], v[200:203], v[120:123], v[224:227]\n" \
  "s_add_u32 %[cnt], %[so], 7*%c[js]+0*%c[ss]\n" \
  "buffer_store_dwordx4 v[232:235], v251, %[rs], %[cnt] offen sc1\n" \
  "v_mfma_f32_16x16x32_bf16 v[228:231], v[216:219], v[120:123], v[228:231]\n" \
  "v_mfma_f32_16x16x32_bf16 v[224:227], v[204:207], v[124:127], v[224:227]\n" \
  "v_mfma_f32_16x16x32_bf16 v[228:231], v[220:223], v[124:127], v[228:231]\n" \
  "v_mfma_f32_16x16x32_bf16 v[232:235], v[192:195], v[180:183], 0\n" \
  "v_mfma_f32_16x16x32_bf16 v[236:239], v[208:211], v[180:183], 0\n" \
  "v_mfma_f32_16x16x32_bf16 v[232:235], v[200:203], v[188:191], v[232:235]\n" \
  "v_cvt_pk_bf16_f32 v224, v224, v225\n" \
  "v_cvt_pk_bf16_f32 v226, v226, v227\n" \
  "v_mfma_f32_16x16x32_bf16 v[236:239], v[216:219], v[188:191], v[236:239]\n" \
  "v_cvt_pk_bf16_f32 v225, v228, v229\n" \
  "v_cvt_pk_bf16_f32 v227, v230, v231\n" \
  "s_waitcnt lgkmcnt(1)\n" \
  "v_mfma_f32_16x16x32_bf16 v[232:235], v[196:199], v[240:243], v[232:235]\n" \
  "s_add_u32 %[cnt], %[so], 3*%c[js]+1*%c[ss]\n" \
  "buffer_store_dwordx4 v[224:227], v251, %[rs], %[cnt] offen sc1\n" \
  "v_mfma_f32_16x16x32_bf16 v[236:239], v[212:215], v[240:243], v[236:239]\n" \
  "s_waitcnt lgkmcnt(0)\n" \
  "v_mfma_f32_16x16x32_bf16 v[232:235], v[204:207], v[244:247], v[232:235]\n" \
  "v_mfma_f32_16x16x32_bf16 v[236:239], v[220:223], v[244:247], v[236:239]\n" \
  "s_nop 4\n" \
  "v_cvt_pk_bf16_f32 v232, v232, v233\n" \
  "v_cvt_pk_bf16_f32 v234, v234, v235\n" \
  "s_nop 1\n" \
  "v_cvt_pk_bf16_f32 v233, v236, v237\n" \
  "v_cvt_pk_bf16_f32 v235, v238, v239\n" \
  "s_add_u32 %[cnt], %[so], 7*%c[js]+1*%c[ss]\n" \
  "buffer_store_dwordx4 v[232:235], v251, %[rs], %[cnt] offen sc1\n" \
  "s_nop 1\n"
#define GDM 2
#define XBF_NB 5
#define XBS_NB 2
#define F_ORD "01ab2cd3"
#define VO_CLOB "v192", "v193", "v194", "v195", "v196", "v197", "v198", "v199", "v200", "v201", "v202", "v203", "v204", "v205", "v206", "v207", "v208", "v209", "v210", "v211", "v212", "v213", "v214", "v215", "v216", "v217", "v218", "v219", "v220", "v221", "v222", "v223", "v224", "v225", "v226", "v227", "v228", "v229", "v230", "v231", "v232", "v233", "v234", "v235", "v236", "v237", "v238", "v239"
#define QK_CLOB "v192", "v193", "v194", "v195", "v196", "v197", "v198", "v199", "v200", "v201", "v202", "v203", "v204", "v205", "v206", "v207", "v208", "v209", "v210", "v211", "v212", "v213", "v214", "v215", "v216", "v217", "v218", "v219", "v220", "v221", "v222", "v223", "v224", "v225", "v226", "v227", "v228", "v229", "v230", "v231", "v232", "v233", "v234", "v235", "v236", "v237", "v238", "v239"
#define QKK_CLOB "v192", "v193", "v194", "v195", "v196", "v197", "v198", "v199", "v200", "v201", "v202", "v203", "v204", "v205", "v206", "v207", "v208", "v209", "v210", "v211", "v212", "v213", "v214", "v215", "v216", "v217", "v218", "v219", "v220", "v221", "v222", "v223", "v224", "v225", "v226", "v227", "v228", "v229", "v230", "v231", "v232", "v233", "v234", "v235", "v236", "v238", "v239"
#define QX_CLOB "v216", "v217", "v218", "v219", "v220", "v221", "v222", "v223", "v224", "v225", "v226", "v227", "v228", "v229", "v230", "v231", "v232", "v233", "v234", "v235", "v236", "v237", "v238", "v239", "v241"
#define ST_CLOB "v192", "v193", "v194", "v195", "v196", "v197", "v198", "v199", "v200", "v201", "v202", "v203", "v204", "v205", "v206", "v207", "v240", "v241", "v242", "v243", "v244", "v245", "v246", "v247", "v253"
#define XB_CLOB "v192", "v193", "v194", "v195", "v196", "v197", "v198", "v199", "v200", "v201", "v202", "v203", "v204", "v205", "v206", "v207", "v208", "v209", "v210", "v211", "v212", "v213", "v214", "v215", "v216", "v217", "v218", "v219", "v220", "v221", "v222", "v223", "v224", "v225", "v226", "v227", "v228", "v229", "v230", "v231", "v232", "v233", "v234", "v235", "v236", "v237", "v238", "v239", "v240", "v241", "v242", "v243", "v244", "v245", "v246", "v247", "v253"
#define OF_CLOB "v32", "v33", "v34", "v35", "v36", "v37", "v38", "v39", "v40", "v41", "v42", "v43", "v44", "v45", "v46", "v47", "v48", "v49", "v50", "v51", "v52", "v53", "v54", "v55", "v56", "v57", "v58", "v59", "v60", "v61", "v62", "v63", "v64", "v65", "v66", "v67", "v68", "v69", "v70", "v71", "v72", "v73", "v74", "v75", "v76", "v77", "v78", "v79", "v80", "v81", "v82", "v83", "v84", "v85", "v86", "v87", "v88", "v89", "v90", "v91", "v92", "v93", "v94", "v95", "v96", "v97", "v98", "v99", "v100", "v101", "v102", "v103", "v104", "v105", "v106", "v107", "v108", "v109", "v110", "v111"
#define OE_CLOB "v192", "v193", "v194", "v195", "v196", "v197", "v198", "v199", "v200", "v201", "v202", "v203", "v204", "v205", "v206", "v207", "v208", "v209", "v210", "v211", "v212", "v213", "v214", "v215", "v216", "v217", "v218", "v219", "v220", "v221", "v222", "v223", "v224", "v225", "v226", "v227", "v228", "v229", "v230", "v231", "v232", "v233", "v234", "v235", "v236", "v237", "v238", "v239", "v240"
constexpr int L_W = 32768;
constexpr int L_HI = 16384;
#define MARG 122
#define ED0 700
#define EA0 1220
#define SDK 452
#define TOL 145
#define XLATE 200
#define TFR 800
#define QT 0
#define HSU 8
#define WNT 3
#define VAL 1
#define KROT 3
#define KSL2 12
#define KSL 2
#define NCY 1825
#define NCYV NCY
#define NRY 24
#define NRX 30
#define NF1O 2
#define NPUB 2
#define NXF 4
#define RSL 2
#define PFL 4
#define EVT2R
__device__ __forceinline__ int slow_head(const u16* wm) { return (int)(((3u - (unsigned)((uintptr_t)wm >> 7)) & 7u) >> 1); }
template <int role>
__device__ __forceinline__ void wroles(const int h, const int par, const int sl, const int xg, const int w, const u64 nonce, const u16* __restrict__ x,
                                       const u16* __restrict__ Wq, const u16* __restrict__ Wk, const u16* __restrict__ Wv,
                                       const u16* __restrict__ Wo, char* ws) {
  LAS char* const L = (LAS char*)g_lds;
  gu32* tmo = (gu32*)(ws + OFF_TMO);
  const __amdgpu_buffer_rsrc_t rs = __builtin_amdgcn_make_buffer_rsrc(ws, 0, (int)WS_USED, 0x00020000);
  constexpr bool vo = role & 1;
  v8u wa[8], wb[16];
  constexpr bool fuse = role >= 2;
  constexpr int RG = fuse ? 4 : 5, RB = fuse ? 32768 : 0;
  f32x4 fac[2][2];
  v2u nin = {0u, 0u}, hsv;
  unsigned arm = 0;
  v8u t13;
  PLDECL;
  const int krot = h & KROT;
  {
    const int lane = olane();
    const int l15 = lane & 15, g = lane >> 4, trq = l15 >> 2, trp = lane & 3;
    const char* bl = (const char*)((vo ? Wv : Wq) + h * DH);
    const char* br = (const char*)(vo ? Wo + (size_t)h * DH * D : Wk + h * DH);
    const int rot = role == 1 ? (h >> 4) * 4 : 0;
    int vl[2], vr[2];
#pragma unroll
    for (int q = 0; q < 2; ++q) {
      const int row = 4 * (2 * w + q) + g;
      const int cb = ((((l15 >> 1) ^ ((row & 3) | ((row >> 1) & 4))) << 5) | ((l15 & 1) << 4));
      const int i = row & 7;
      const int prow = (row & 32) + ((i & 4) << 2) + ((row >> 1) & 12) + (i & 3);
      vl[q] = row * 8192 + cb;
      vr[q] = vo ? (role == 1 ? row : prow) * 2048 + cb : vl[q];
    }
#define W_ISSUE(u)                                                                                   \
  {                                                                                                  \
    const char* pl = bl + (size_t)((u) ^ krot) * 64 * 8192;                                          \
    const char* pr = br + (role == 1 ? (size_t)(((u) >> 1) & 1) * 64 * 2048 + (2 * ((u) >> 2) + ((u) & 1)) * 256          \
                                     : (vo ? (size_t)((u) & 1) * 64 * 2048 + ((u) >> 1) * 256 : (size_t)((u) ^ krot) * 64 * 8192)); \
    _Pragma("unroll") for (int q = 0; q < 2; ++q) {                                                  \
      glds16<((WNT >> (role >> 1)) & 1) != 0>(pl, vl[q], RB + ((u) % RG) * 32768 + (2 * w + q) * 1024);                                \
      glds16<((WNT >> (role >> 1)) & 1) != 0>(pr, vr[q], RB + ((u) % RG) * 32768 + 16384 + (2 * w + q) * 1024);                        \
    }                                                                                                \
  }
    const int ta = role == 1 ? 8 + (w ^ rot) : 2 * w, tb = role == 1 ? 2 * (w & 3) : 2 * w + 1;
    const int sw = trq | ((g & 1) << 2);
    int pa = (ta >> 3) * 16384 + (8 * g + trq) * 256 + (((ta & 7) ^ sw) << 5) + trp * 8;
    int pb = (tb >> 3) * 16384 + (8 * g + trq) * 256 + (((tb & 7) ^ sw) << 5) + trp * 8;
    const int pw = L_W + w * 2048 + lane * 16;
#define W_FRAG(dst, p, u)                                                                            \
  {                                                                                                  \
    int p_ = p + RB + ((u) % RG) * 32768;                                                             \
    asm volatile("" : "+v"(p_));                                                                     \
    const v2u t0 = trd(L + p_), t1 = trd(L + p_ + 1024), t2 = trd(L + p_ + 8192), t3 = trd(L + p_ + 9216); \
    dst = v8u{t0[0], t0[1], t1[0], t1[1], t2[0], t2[1], t3[0], t3[1]};                               \
  }
#define W_PUT(c)                                           \
  {                                                        \
    int p_ = pw + ((c) - 8) * 16384;                       \
    asm volatile("" : "+v"(p_));                           \
    *(LAS v4u*)(L + p_) = lo4(tmp[(c) - 8]);               \
    *(LAS v4u*)(L + p_ + 1024) = hi4(tmp[(c) - 8]);        \
  }
    PL(0);
    const int fxv = (16 * (w & 1) + (lane >> 2)) * 2048 + (((lane ^ -g) & 3) << 4) + ((w >> 1) & 1) * 64;
    const int fxr = l15 * 64 + (((g ^ -trq) & 3) << 4);
#define X_ISSUE(j) glds16((const char*)x + par * 65536 + ((2 * (j) + (w >> 2)) ^ krot) * 128, fxv, (((j) + 1) & 3) * 8192 + w * 1024)
#define HWU 99
#define W_HELP(u)                                                                                                     \
  if (fuse && w >= 5) {                                                                                               \
    const int h0 = slow_head(Wq), rk = (h - h0 - 1) & 3;                                                              \
    const int q = (((role & 1) * 4 + par * 2 + ((h >> 2) & 1)) * 3 + rk) * 3 + w - 5, m = q / 24;                     \
    const u16* wm = m == 2 ? Wv : (m ? Wk : Wq);                                                                      \
    const int hs = (h & 24) + slow_head(wm) + 4 * ((q / 12) & 1);                                                     \
    if (rk < 3)                                                                                                       \
      touch4((const char*)wm + hs * 256 + (size_t)((15 - (q % 12 >> 1)) ^ (hs & KROT)) * 524288,                      \
             ((q & 1) * 32 + (lane >> 1)) * 8192 + (lane & 1) * 128, RB + ((u) % RG) * 32768 + 2 * w * 1024);          \
  }
    if (HWU == -1) W_HELP(0)
    W_ISSUE(0);
    if (fuse) {
#pragma unroll
      for (int j = 0; j < 4; ++j) X_ISSUE(j);
    }
#pragma unroll
    for (int u = 1; u < RG; ++u) W_ISSUE(u);
    PL(1);
    v8u tmp[8];
#pragma unroll
    for (int u = 0; u < 16; ++u) {
      {
        int young = 4 * (u + RG - 1 < 16 ? RG - 1 : 15 - u);
        if (fuse)
          for (int i = u - RG; i < u; ++i) young += i >= 0 && i < 8 && (i & 1);
        if (u > HSU && u < HSU + RG) young += 1;
        if (role < 2 && u > 13) young += 3;
        if (VFAR && role == 3 && u > 12 && u < 15) young += 2;
        asm volatile("s_waitcnt vmcnt(%0)" ::"n"(young) : "memory");
      }
      RAW_BARRIER();
      if ((u & 3) == 0 || u == 15) PL(2 + (u + 1) / 4);
      if constexpr (role == 1) {
#define WO_(n) ((n) < 8 ? wa[(n) & 7] : ((n) < 10 ? wb[14 + ((n) & 1)] : tmp[(n) >= 10 ? (n) - 10 : 0]))
#define WV_(n) ((n) < 14 ? wb[(n) < 14 ? (n) : 0] : tmp[6 + ((n) & 1)])
        if (u == 12) { W_PUT(8); W_PUT(9); }
        W_FRAG(WO_(u), pa, u)
        if (VAL ? (w >> 2) == (((u ^ krot) >> 2) & 1) : (w >> 2) == (u >> 3)) {
          const int sl = VAL ? (u & 3) + 4 * (u >> 3) : u & 7;
          W_FRAG(WV_(2 * sl), pb, u)
          W_FRAG(WV_(2 * sl + 1), (pb ^ 32), u)
        }
        if (u & 1) {
          v8u& f0 = WO_(u & ~1);
          v8u& f1 = WO_(u);
          const v8u n0 = {f0[0], f1[0], f0[1], f1[1], f0[2], f1[2], f0[3], f1[3]};
          const v8u n1 = {f0[4], f1[4], f0[5], f1[5], f0[6], f1[6], f0[7], f1[7]};
          f0 = n0;
          f1 = n1;
          asm volatile("" : "+v"(f0), "+v"(f1));
        }
        if (u == 13) { W_PUT(10); W_PUT(11); }
        if (u == 15) { W_PUT(12); W_PUT(13); W_PUT(14); W_PUT(15); }
      } else if constexpr (role == 0) {
        if (u == 12) { W_PUT(8); W_PUT(9); }
        if (u == 13) { W_PUT(10); W_PUT(11); }
        if (u == 14) { W_PUT(12); W_PUT(13); }
        if (u == 15) { W_PUT(14); }
        if (u < 8) W_FRAG(wa[u < 8 ? u : 0], pa, u) else W_FRAG(tmp[u >= 8 ? u - 8 : 0], pa, u)
        W_FRAG(wb[u], pb, u)
        if (u == 15) W_PUT(15);
      } else {
        if (u == 15 && role == 2 && w == 0) hs_wait(rs, nonce, hsv);
        if (u >= 13) W_PUT(u >= 13 ? 2 * u - 18 : 8);
        if (u == 13 || u == 14) W_PUT(u >= 13 ? 2 * u - 17 : 8);
        v8u fa, fb;
        W_FRAG(fa, pa, u)
        W_FRAG(fb, pb, u)
        if (u < 8) {
          wa[u & 7] = fa;
          wb[u] = fb;
        } else {
          wb[u] = cat8(lo4(fa), lo4(fb));
          tmp[u & 7] = cat8(hi4(fa), hi4(fb));
        }
        if (role == 2 || w < 4) {
#pragma unroll
          for (int hf = 0; hf < 2; ++hf)
#pragma unroll
            for (int rt = 0; rt < 2; ++rt) {
              const v4u xq = *(const LAS v4u*)(L + fxr + ((u + 2) & 7) * 4096 + hf * 2048 + rt * 1024);
              fac[0][rt] = mma(hf ? hi4(fa) : lo4(fa), xq, u + hf == 0 ? f32x4{0.f, 0.f, 0.f, 0.f} : fac[0][rt]);
              fac[1][rt] = mma(hf ? hi4(fb) : lo4(fb), xq, u + hf == 0 ? f32x4{0.f, 0.f, 0.f, 0.f} : fac[1][rt]);
            }
          asm volatile("" : "+v"(fac[0][0]), "+v"(fac[0][1]), "+v"(fac[1][0]), "+v"(fac[1][1]));
          if (u == 15) {
            PL(16);
            const int l16 = olane() * 16;
#pragma unroll
            for (int rt = 0; rt < 2; ++rt) {
              const v4u o = {pk2(fac[0][rt][0], fac[0][rt][1]), pk2(fac[0][rt][2], fac[0][rt][3]), pk2(fac[1][rt][0], fac[1][rt][1]),
                             pk2(fac[1][rt][2], fac[1][rt][3])};
              if (role == 3) *(LAS v4u*)(L + 24576 + rt * 4096 + w * 1024 + l16) = o;
              else st128(o, rs, l16, qk_addr(w >> 2, par, h) + rt * QK_RT + (w & 3) * 1024);
            }
            if (role == 2) {
              W_PUT(13);
              VMCNT(0);
            }
          }
        } else if (u == 15) {
          W_PUT(13);
          if (w == 7) {
            const bool ok = hs_wait(rs, nonce, hsv);
            if (VFAR) {
              if (__builtin_expect(!ok || !__all((nin[0] != 0xffffffffu) & (nin[1] != 0xffffffffu)), 0)) nin = numa_load(rs, tmo);
              *(volatile LAS int*)(L + L_W + 5 * 16384) = numa_far(nin);
            }
          }
        }
      }
      if (u == HSU) hsv = __builtin_amdgcn_raw_buffer_load_b64(rs, w == (role == 3 ? 7 : 0) ? 0 : OOB, OFF_ACK + (int)BIDX * 8, AUX_SC1);
      if (role < 2 && u == 13) {
        const int a = w == 0 ? olane() * 4 : OOB;
        arm = __builtin_amdgcn_raw_buffer_load_b32(rs, a & OOB, CNT_ARM, AUX_SC1);
        nin[0] = __builtin_amdgcn_raw_buffer_load_b32(rs, a, (int)OFF_CAL, AUX_SC1);
        nin[1] = __builtin_amdgcn_raw_buffer_load_b32(rs, (a & OOB) | (((a >> 5) & (NXC - 1)) * 4), (int)OFF_XID, AUX_SC1);
      }
      if (VFAR && role == 3 && u == 12) {
        const int a = w == 7 ? olane() * 4 : OOB;
        nin[0] = __builtin_amdgcn_raw_buffer_load_b32(rs, a, (int)OFF_CAL, AUX_SC1);
        nin[1] = __builtin_amdgcn_raw_buffer_load_b32(rs, (a & OOB) | (((a >> 5) & (NXC - 1)) * 4), (int)OFF_XID, AUX_SC1);
      }
      if (u + RG < 16) {
        RAW_BARRIER();
        if (HWU == u) W_HELP(u + RG)
        W_ISSUE(u + RG);
        if (fuse && u < 8 && (u & 1)) X_ISSUE(4 + (u >> 1));
      }
    }
    PL(17);
    RAW_BARRIER();
    PL(18);
    if (VFAR && role == 3 && w >= 4) arm = __builtin_amdgcn_readfirstlane(*(volatile LAS int*)(L + L_W + 5 * 16384));
    if (role == 2 && w == 0) __builtin_amdgcn_raw_buffer_store_b32(0u, rs, olane() << 28, fqk(h, par), AUX_SC1);
    if constexpr (fuse) {
      if (role == 2 || w < 4) {
        constexpr int B0 = role == 3 ? 6 : 8, PER = role == 3 ? 2 : 4, SL = role == 3 ? 12288 : 16384;
        const int ln = olane();
        const int xv = (16 * (w & 1) + (ln >> 2)) * 2048 + (((ln ^ -(ln >> 4)) & 3) << 4) + ((w >> 1) & 1) * 1024 + (role == 3 ? 0 : (w >> 2) * 64);
#pragma unroll
        for (int k = 0; k < 2; ++k)
#pragma unroll
          for (int j = 0; j < B0 / PER; ++j)
            glds16((const char*)x + (2 + par) * 65536 + (((k * B0 + j * PER) / 2 * 64) ^ (krot * 128)), xv, k * SL + j * PER * 2048 + w * 1024);
        PL(19);
        if (role == 3) t13 = tmp[5];
      }
      W_PUT(14);
      W_PUT(15);
    }
  }
  const int lane = olane();
  const int l15 = lane & 15, g = lane >> 4, trq = l15 >> 2, trp = lane & 3;
  int pw0 = L_W + w * 2048 + lane * 16, pw1 = pw0 + 65536;
  asm volatile("" : "+v"(pw0), "+v"(pw1));
#define WA_P(c) (L + ((c) < 12 ? pw0 : pw1) + ((c) & 3) * 16384)
#define WA_LDS(c) cat8(*(const LAS v4u*)WA_P(c), *(const LAS v4u*)(WA_P(c) + 1024))
  const int r_a = (int)(((uintptr_t)ws >> 19) & 7);
  const int r_s = r_a + (int)(OFF_STEP / UNIT);

  typedef int v4i __attribute__((ext_vector_type(4)));
#define WPIN "{v[0:31]}"(W0), "{v[32:63]}"(W1), "{v[64:95]}"(W2), "{v[96:127]}"(W3), "{v[128:159]}"(W4), "{v[160:191]}"(W5)
  const v4u rsv = {(unsigned)(uintptr_t)ws, (unsigned)((uintptr_t)ws >> 32) & 0xffffu, (unsigned)WS_USED, 0x00020000u};
  if constexpr (role >= 2) {
    constexpr bool isx = role == 3;
    const v32u W0 = cat32(wa[0], wa[1], wa[2], wa[3]), W1 = cat32(wa[4], wa[5], wa[6], wa[7]), W2 = cat32(wb[0], wb[1], wb[2], wb[3]);
    const v32u W3 = cat32(wb[4], wb[5], wb[6], wb[7]), W4 = cat32(wb[8], wb[9], wb[10], wb[11]), W5 = cat32(wb[12], wb[13], wb[14], wb[15]);
    const v4u rxv = {(unsigned)(uintptr_t)x, (unsigned)((uintptr_t)x >> 32) & 0xffffu, (unsigned)(S * D * 2), 0x00020000u};
    v4i adr;
    adr[3] = lane * 16;
    adr[2] = pw0;
    adr[1] = pw1;
    adr[0] = (16 * (w & 1) + (lane >> 2)) * 2048 + (((lane ^ -g) & 3) << 4) + ((w >> 1) & 1) * 1024 + (isx ? 0 : (w >> 2) * 64);
    const int xr = l15 * 64 + (((g ^ -trq) & 3) << 4);
#define FOF(sg) (isx ? fvw(w & 3, h, sg) : fqk(h, sg))
    const int nrel = isx ? TN(8, NRX) : TN(7, NRY);
    const int nit = par ? nrel : NSEG - nrel;
#define SEGOF(i) ((i) < nrel ? 2 * (i) + par : (i) + nrel)
#define ST_FLAG(sg)                                                                                                   \
  {                                                                                                                   \
    VMCNT(0);                                                                                                         \
    RAW_BARRIER();                                                                                                    \
    if (w == 0) __builtin_amdgcn_raw_buffer_store_b32(0u, rs, olane() << 28, FOF(sg), AUX_SC1);               \
  }
#define XB_OPS(sg, sp)                                                                                                  \
  : [cnt] "=&s"(cnt) EVOUT                                                                                              \
  : WPIN, "{v[248:251]}"(adr), [rs] "s"(rsv), [so] "s"(vw_addr(vfr, 2 * (w - 4), sg, h >> 2) + (h & 3) * 1024),               \
    [js] "i"((VFAR ? 64 : GST) * 262144), [ss] "i"(VFAR ? 65536 : 32768), [fo] "s"(FOF(sp))                                                 \
  : XB_CLOB, "scc", "memory"
#define XB_FLAG(sg) __builtin_amdgcn_raw_buffer_store_b8(0, rs, olane() << 28, FOF(sg), AUX_SC1)
    PL(7);
    if (isx && w >= 4) {
      const int vfr = (__builtin_amdgcn_readfirstlane(arm) ^ (VFAR == 2 ? 8192 : 0)) | (r_a << 19);
      {
        EVDECL;
        unsigned cnt;
        PL(20);
        asm volatile(ST_XBS XB_OPS(par, par));
        PL(21);
      }
      int it = 1;
#pragma unroll 1
      for (;;) {
        VMCNT(0);
        XB_FLAG(SEGOF(it - 1));
        if (it == 1) { PL(9); PLDUMP(4, 0x300200); }
        if (it == 2) PTW(14, 4);
        if (it == 3) PTW(15, 4);
        for (int k = it == 1 ? XBS_NB - 1 : XBF_NB; k < 6; ++k) BARE_BARRIER();
        if (++it > NXF) break;
        EVDECL;
        unsigned cnt;
        asm volatile(ST_XBF XB_OPS(SEGOF(it - 1), par));
        EVT2R
      }
#pragma unroll 1
      for (; it < nit; ++it) {
        EVDECL;
        unsigned cnt;
        asm volatile(ST_XB XB_OPS(SEGOF(it - 1), SEGOF(it - 2)));
        EVT2R
      }
      BARE_BARRIER();
      {
        EVDECL;
        unsigned cnt;
        asm volatile(ST_XB0 XB_OPS(SEGOF(nit - 1), par));
      }
      BARE_BARRIER();
      VMCNT(0);
      XB_FLAG(SEGOF(nit - 2));
      XB_FLAG(SEGOF(nit - 1));
      return;
    }
    if (w == 1) cnt_inc(ws, CNT_HS);
    VMCNT(0);
    RAW_BARRIER();
    if (isx) {
      *(LAS v4u*)(L + pw1 + 16384) = lo4(t13);
      *(LAS v4u*)(L + pw1 + 17408) = hi4(t13);
    }
    PL(10);
    PLDUMP(0, 0xf05ff);
    {
      v32u xf;
#pragma unroll
      for (int q = 0; q < 8; ++q) {
        const v4u t = *(const LAS v4u*)(L + xr + q * 1024);
#pragma unroll
        for (int j = 0; j < 4; ++j) xf[4 * q + j] = t[j];
      }
#pragma unroll 1
      for (int it = 1; it < nit; ++it) {
        EVDECL;
        const int seg = SEGOF(it);
        unsigned cnt, keep;
#define ST_OPS                                                                                                                 \
  : [cnt] "=&s"(cnt), [keep] "=&s"(keep), "+{v[208:239]}"(xf) EVOUT                                                            \
  : WPIN, "{v[248:251]}"(adr), "{v252}"(xr), [rs] "s"(rsv), [rx] "s"(rxv), [sc] "s"(seg * 65536 + krot * 128),                    \
    [sn] "s"(SEGOF(min(it + 1, nit - 1)) * 65536 + krot * 128),                                                                  \
    [mw] "s"(w * 1024), [so] "s"(isx ? w * 1024 : qk_addr(w >> 2, seg, h) + (w & 3) * 1024), [rt] "i"(QK_RT), \
    [fo] "s"(FOF(SEGOF(it - 1))), [fm] "s"(w == 0 ? 0 : 1 << 30), [tm] "s"(w == 3 ? 0 : 1 << 30),            \
    [tc] "s"(min(SEGOF(it + 2), NSEG - 1) * 65536 + sl * 8192)                                                                   \
  : ST_CLOB, "scc", "memory"
        if constexpr (isx) asm volatile(ST_XA ST_OPS);
        else asm volatile(ST_Y ST_OPS);
        EVT2R
        if (it < 4) PTW(10 + it, 2);
        if (!isx && it < NPUB) ST_FLAG(seg)
      }
    }
    if (isx) {
      RAW_BARRIER();
      BARE_BARRIER();
    } else ST_FLAG(SEGOF(nit - 1))
    return;
  }

#define TSK 700
#define KDM 1
#define TC2 0
#define KF0 4
  volatile LAS unsigned* sword = (volatile LAS unsigned*)(L + L_HI + 6408);
  if (w == 0) {
    const bool ok = hs_wait(rs, nonce, hsv);
    cnt_inc(ws, CNT_HS);
    if (__builtin_expect(!ok || !__all((arm + 1u >= 64u) & (nin[0] != 0xffffffffu) & (nin[1] != 0xffffffffu)), 0)) {
      arm_wait(rs, tmo);
      nin = numa_load(rs, tmo);
    }
    sword[0] = numa_perm(nin, (LAS unsigned*)(L + L_HI + 6656));
    sword[1] = 0u;
  }
  Clk ck = {0u, 0u, 92500u, 46432u};
  PL(7);
  RAW_BARRIER();
  PL(8);
  PLDUMP(0, 0xf01ff);
  const unsigned perm = __builtin_amdgcn_readfirstlane(sword[0]);
  const int cown = cls_of(perm, xg);
  const int l16 = lane * 16;
  const int sidx = ((l15 & 2) ? 0xeeee : 0x4444) | ((l15 & 8) ? 0xeeee0000 : 0x44440000);
  LAS char* const sm = L + l16;
  constexpr int TWV = 5;
  typedef int v8i __attribute__((ext_vector_type(8)));
  const int mg_ = NS2F(TN(0, MARG)), tol_ = NS2F(TN(13, TOL)), xl_ = NS2F(TN(14, XLATE));
  Pace pc = {0u, NS2F(ED0), NS2F(EA0), 0};
  unsigned tg = 0, tw = 0;
#define MEM_OFF(c) (R_MEM + ((c) >> 2) * 32768 + ((c) & 3) * 1024)
  if (w == 0) *(volatile LAS u64*)(L + L_HI + 7176 + l16) = 0ull;
#define DMDECL unsigned keep; int fs = s + 1
#define DMOUT , [keep] "=&s"(keep), [fs] "+s"(fs)
#define DMIN(x) , [o3] "s"(x)
#define TLB_TOUCH                    \
  if (((r_s + s + 2) & 3) == 0)      \
    tch = __builtin_amdgcn_raw_buffer_load_b32(rs, 0, so + 2 * (int)UNIT + R_DUM + (role * 32 + h) * 128, AUX_SC1);
#define TQOUT
#define TQACC
#define STCNT
#define ST_ADR (lst * 32 + 32768 + 2048)
#define TIMING_WAVE                                                                                  \
  {                                                                                                  \
    if (__builtin_expect(anysent(st[0]), 0)) _Pragma("clang loop unroll(disable)") for (unsigned spins = 0;;) { \
      if (spins) __builtin_amdgcn_s_sleep(4);                                                        \
      STCNT                                                                                          \
      if (spin_fail(spins, tmo, 0x30009)) break;                                                     \
      const int lst = olane();                                                                       \
      OPQ(va, ST_ADR - 2048);                                                                        \
      st = __builtin_amdgcn_raw_buffer_load_b64(rs, va, so + R_ST + ((cown ^ xr) << 12), AUX_SC1);   \
      if (!anysent(st[0])) break;                                                                       \
    }                                                                                                \
    TQACC                                                                                            \
    unsigned m0_ = (ck.r << 4) + 65536u - st[0], m1_ = ~st[1];                                       \
    wave_min2(m0_, m1_);                                                                             \
    const unsigned rf = (ck.r << 4) + 65536u - __builtin_amdgcn_readfirstlane(m0_);                  \
    pace_step<false>(pc, s, rf, ~__builtin_amdgcn_readfirstlane(m1_), mg_, tol_, xl_);               \
    EV(6);                                                                                           \
    EVDUMP(s);                                                                                       \
    EVCLK(s, ck);                                                                                    \
  }
#define TIMING_GO                                                                                    \
  if (w == TWV && s > 0) {                                                                           \
    tg = clk_cyc(ck, (int)(pc.rf - (ck.r << 4)) + pc.dq);                                            \
    const int lf = (int)(tg - cyc32()) & ~1;                                                         \
    if (lf > TSK || (int)(tg - ck.m) > 7000 || s < 4) {                    \
      wait_cyc(tg - TFR, lf - TFR);                                                                  \
      if (__builtin_expect(s < 3, 0)) clk_update(ck, s == 1);              \
      else clk_step(ck);                                                                             \
      tg = clk_cyc(ck, (int)(pc.rf - (ck.r << 4)) + pc.dq);                                          \
    }                                                                                                \
    tw = (unsigned)max(CMAX - (int)(tg - ck.m), 0);                                                  \
  }

  if constexpr (role == 0) {
    const v32u W0 = cat32(wa[0], wa[1], wa[2], wa[3]), W1 = cat32(wa[4], wa[5], wa[6], wa[7]), W2 = cat32(wb[0], wb[1], wb[2], wb[3]);
    const v32u W3 = cat32(wb[4], wb[5], wb[6], wb[7]), W4 = cat32(wb[8], wb[9], wb[10], wb[11]), W5 = cat32(wb[12], wb[13], wb[14], wb[15]);
    v8i adr;
    adr[7] = l16;
    adr[6] = l16 + w * 1024;
    adr[5] = pw0;
    adr[4] = sidx;
    adr[3] = L_HI + (w >> 2) * 3072 + (32 * (w & 3) + l15) * 24 + 4 * g;
    adr[2] = (4 * g + trq) * 24 + (trp & 1) * 8;
#define CORD(i) ((((i) & 1) ? (i) >> 1 : 8 + ((i) >> 1)) ^ krot)
    const int mo0 = MEM_OFF(CORD(w));
#define K_STAGE(sn)                                                                                                   \
  {                                                                                                                   \
    v4u kn[4];                                                                                                        \
    _Pragma("clang loop unroll(disable)") for (unsigned spins = 0; krdy <= (sn);) {    \
      OPQ(vfl, fqk(h, min((sn) + (olane() & 15), NSEG - 1)));                                           \
      const u64 miss = __builtin_amdgcn_ballot_w64(__builtin_amdgcn_raw_buffer_load_b32(rs, vfl, 0, AUX_SC1) != 0u);  \
      krdy = (sn) + (miss ? __builtin_ctzll(miss) : 16);                                                              \
      if (krdy > (sn)) break;                                                                                         \
      __builtin_amdgcn_s_sleep(8);                                                                                    \
      if (spin_fail(spins, tmo, 0x30000)) break;                                                                      \
    }                                                                                                                 \
    const int lk = olane() * 16;                                                                                      \
    if (KDM) {                                         \
      unsigned keep;                                                                                                  \
      asm volatile("s_mov_b32 %0, m0\n\ts_mov_b32 m0, %4\n\ts_nop 4\n\t"                                              \
                   "buffer_load_dwordx4 %1, %2, %3 offen sc1 lds\n\tbuffer_load_dwordx4 %1, %2, %3 offen offset:1024 sc1 lds\n\t" \
                   "buffer_load_dwordx4 %1, %2, %3 offen offset:2048 sc1 lds\n\tbuffer_load_dwordx4 %1, %2, %3 offen offset:3072 sc1 lds\n\t" \
                   "s_mov_b32 m0, %0"                                                                                 \
                   : "=&s"(keep)                                                                                      \
                   : "v"(lk), "s"(rsv), "s"(qk_addr(1, sn, h) + (w & 1) * QK_RT), "s"(L_HI + 8192 + (w & 1) * 4096)    \
                   : "memory");                                                                                       \
    } else {                                                                                                          \
      _Pragma("unroll") for (int q = 0; q < 4; ++q) kn[q] = __builtin_amdgcn_raw_buffer_load_b128(                    \
          rs, lk + q * 1024, qk_addr(1, sn, h) + (w & 1) * QK_RT, AUX_SC1);                                           \
      _Pragma("unroll") for (int q = 0; q < 4; ++q) *(LAS v4u*)(L + L_HI + 8192 + (w & 1) * 4096 + q * 1024 + lk) = kn[q]; \
    }                                                                                                                 \
  }
    int krdy = 1;
    const bool kst = w >= 4 && ((w ^ TWV) & 2);
    PTW(9, 7);
    if (kst) {
      wait_flag<KF0>(rs, fqk(h, 0), tmo, 0x30001);
      sword[1] = 1u;
      K_STAGE(0)
    }
    PTW(10, 7);
    if (w == TWV) {
      _Pragma("clang loop unroll(disable)") for (unsigned spins = 0; !__builtin_amdgcn_readfirstlane(sword[1]) && spins < kSpinCap; ++spins)
        __builtin_amdgcn_s_sleep(2);
      clk_edge(ck.r, ck.m, 2);
    }
    if (w < 2) {
      const int lane = olane(), l15 = lane & 15, l16 = lane * 16;
      const bool up = lane >= 32;
      adr[1] = R_EX + (h >> 3) * 32768 + (h & 7) * 512 + w * 256 + (lane & 32) * 4 + (lane & 7) * 16 + (lane & 24) / 2;
      const int rq = (lane & 1) + 2 * (lane & 6);
      adr[0] = up ? R_PM + (h >> 3) * 1024 + (rq + (l15 >> 3) * 2 + 16 * ((h & 7) >> 1)) * 16 + (h & 1) * 8 + (lane & 16) / 4
                  : (lane < 8 ? R_SC + SC_POS(h >> 2, lane) + (2 * (h & 1) + ((h >> 1) & 1)) * 4 : OOB);
      const unsigned msk = up ? 0x7fffffffu : ((lane & 1) ? 0x7fff0000u : 0xffffu);
      const int kxo = L_HI + 8192 + w * 4096 + l16;
#pragma unroll 1
      for (int s = 0; s < NSEG; ++s) {
        EVDECL;
        const int so = (int)OFF_STEP + s * (int)UNIT, xr = xreg(r_s + s);
#define CG(r) (so + ((cls_of(perm, (r) ^ (2 * w)) ^ xr) << 12))
        unsigned cnt;
        u64 wd;
        DMDECL;
        asm volatile(QK_A
                     : [cnt] "=&s"(cnt), [wd] "=&s"(wd) DMOUT EVOUT
                     : WPIN, "{v[244:251]}"(adr), "{v243}"(pw1), "{v242}"(kxo), "{v240}"(msk), [rs] "s"(rsv),
                       [o0] "s"(so + mo0 + ((cown ^ xr) << 12)), [c0] "s"(CG(0)), [c1] "s"(CG(1)), [c2] "s"(CG(2)), [c3] "s"(CG(3)),
                       [hf] "s"(0xff00ff00ff00ff00ull), [up] "s"(0xffffffff00000000ull)
                     : QK_CLOB, "v241", "vcc", "scc", "memory");
        if (s < 4) PT(11 + s);
        EVDUMP(s);
      }
    } else if (w < 4) {
      const float scale = 0.08838834764831845f, L2E = 1.4426950408889634f;
      adr[0] = adr[1] = 0;
#pragma unroll 1
      for (int s = 0; s < NSEG; ++s) {
        {
          EVDECL;
          const int so = (int)OFF_STEP + s * (int)UNIT, xr = xreg(r_s + s);
          unsigned cnt;
          v16u qv;
          f32x4 sc[3];
          DMDECL;
          asm volatile(QK_X
                       : [cnt] "=&s"(cnt) DMOUT, "={v[200:215]}"(qv), "={v[192:195]}"(sc[0]), "={v[196:199]}"(sc[1]) EVOUT
                       : WPIN, "{v[244:251]}"(adr), "{v243}"(pw1), [rs] "s"(rsv), [o0] "s"(so + mo0 + ((cown ^ xr) << 12)),
                         [qo] "s"(qk_addr(0, s, h) + (w - 2) * QK_RT)
                       : QX_CLOB, "vcc", "scc", "memory");
          const int ln = olane(), l15 = ln & 15, g = ln >> 4, l16 = ln * 16;
          v4u qx[4];
#pragma unroll
          for (int ks = 0; ks < 4; ++ks) qx[ks] = v4u{qv[4 * ks], qv[4 * ks + 1], qv[4 * ks + 2], qv[4 * ks + 3]};
          LAS char* const q_rd = L + L_HI + adr[2];
#define QFRAG(o, ks) [&] { const v2u tx = trd(q_rd + (o) + (ks) * 768), ty = trd(q_rd + (o) + (ks) * 768 + 384); return v4u{tx[0], tx[1], ty[0], ty[1]}; }()
          sc[2] = f32x4{0.f, 0.f, 0.f, 0.f};
#pragma unroll
          for (int ks = 0; ks < 4; ++ks) sc[2] = mma(QFRAG(3072, ks), qx[ks], sc[2]);
          EV(4);
          const int tok = 16 * (w - 2) + l15 - 4 * g;
#pragma unroll
          for (int t = 0; t < 3; ++t)
#pragma unroll
            for (int j = 0; j < 4; ++j) {
              float v = rbf(mul1(rbf(sc[t][j]), scale));
              if (t < 2 && 16 * t + j > tok) v = -1000000000.0f;
              sc[t][j] = v;
            }
          float mx;
          asm("v_max3_f32 %0, %1, %2, %3\n\tv_max3_f32 %0, %0, %4, %5\n\tv_max3_f32 %0, %0, %6, %7\n\tv_max3_f32 %0, %0, %8, %9\n\t"
              "v_max3_f32 %0, %0, %10, %11\n\tv_max_f32 %0, %0, %12\n\ts_nop 1"
              : "=&v"(mx)
              : "v"(sc[0][0]), "v"(sc[0][1]), "v"(sc[0][2]), "v"(sc[0][3]), "v"(sc[1][0]), "v"(sc[1][1]), "v"(sc[1][2]), "v"(sc[1][3]),
                "v"(sc[2][0]), "v"(sc[2][1]), "v"(sc[2][2]), "v"(sc[2][3]));
          mx = xmax16_32(mx);
          const float nmx = -mx * L2E;
#pragma unroll
          for (int t = 0; t < 3; ++t)
#pragma unroll
            for (int j = 0; j < 4; ++j) sc[t][j] = __builtin_amdgcn_exp2f(__builtin_fmaf(sc[t][j], L2E, nmx));
          const f32x4 s4 = sc[0] + sc[1] + 0.5f * sc[2];
          const float inv = __builtin_amdgcn_rcpf(xsum16_32((s4[0] + s4[1]) + (s4[2] + s4[3])));
#pragma unroll
          for (int t = 0; t < 3; ++t) sc[t] *= inv;
          const v4u o = {pk2(sc[0][0], sc[0][1]), pk2(sc[1][0], sc[1][1]), pk2(sc[0][2], sc[0][3]), pk2(sc[1][2], sc[1][3])};
          const v2u o2 = {pk2(sc[2][0], sc[2][1]), pk2(sc[2][2], sc[2][3])};
          const int pb = so + R_PX + (w - 2) * 40960;
          st128(o, rs, l16, pb + h * 1024);
          __builtin_amdgcn_raw_buffer_store_b64(o2, rs, g >= 2 ? l15 * 16 + (g & 1) * 8 : OOB,
                                                pb + (32 + (h >> 2)) * 1024 + (h & 3) * 256, AUX_SC1);
          EV(6);
          EVDUMP(s);
        }
      }
    } else {
      const int mo1 = MEM_OFF(CORD(w + 4)), mo2 = MEM_OFF(CORD(w + 8)), mo3 = MEM_OFF(CORD(w - 4));
      adr[0] = 0;
#pragma unroll 1
      for (int s = 0; s < NSEG; ++s) {
        EVDECL;
        const int so = (int)OFF_STEP + s * (int)UNIT, xr = xreg(r_s + s), sc_ = so + ((cown ^ xr) << 12);
        const int lst = olane();
        int a1 = ((cls_of(perm, lst & 3) ^ xr) << 12) | ((lst & 4) << 9) | ((lst & 8) << 12) | (((lst >> 4) | (w != TWV)) << 28);
        const bool fq = kst && krdy <= s + 2;
        int a0 = fq ? fqk(h, min(s + 1 + (lst & 15), NSEG - 1)) : ST_ADR | ((w != TWV) << 30);
        v2u st;
        unsigned cnt, tcd, tca = (lst & 31) * 128 + (lst >> 5) * QK_RT;
        int o0 = sc_ + mo0, o1 = sc_ + mo1, o2 = sc_ + mo2, sst = so + R_ST + role * 1024 + h * 32, ss = kst ? 0 : sc_ + R_ST;
        int tc = kst && !TC2 ? qk_addr(w & 1, min(s + 1, NSEG - 1), h) : 0;
        asm volatile("" : "+v"(a1), "+v"(a0), "+{v242}"(tca));
        adr[1] = a1;
        adr[0] = a0;
        asm volatile("" : "+s"(o0), "+s"(o1), "+s"(o2), "+s"(sst), "+s"(ss), "+s"(tc));
        DMDECL;
        TIMING_GO
        asm volatile(QK_K
                     : [cnt] "=&s"(cnt) DMOUT, "={v[240:241]}"(st), "={v237}"(tcd) EVOUT TQOUT
                     : WPIN, "{v[244:251]}"(adr), "{v243}"(pw1), "{v242}"(tca), [rs] "s"(rsv), [o0] "s"(o0), [o1] "s"(o1), [o2] "s"(o2),
                       [ckm] "s"(ck.m - NCY), [inv] "s"(ck.inv << 16), [r16] "s"(ck.r << 4), [sst] "s"(sst), [tg] "s"(tg), [tw] "s"(tw),
                       [ss] "s"(ss), [tc] "s"(tc) DMIN(sc_ + mo3)
                     : QKK_CLOB, "vcc", "scc", "memory");
        if (s < NSEG - 1 && w == TWV) {
          __builtin_amdgcn_s_sleep(KSL);
          TIMING_WAVE
        } else if (s < NSEG - 1 && kst) {
          if (fq) {
            const u64 miss = __builtin_amdgcn_ballot_w64(st[0] != 0u);
            krdy = s + 1 + (miss ? __builtin_ctzll(miss) : 16);
          }
          if (KSL2) __builtin_amdgcn_s_sleep(KSL2);
          if (KDM) asm volatile("s_waitcnt vmcnt(0)" ::: "memory");
          K_STAGE(s + 1)
          asm volatile("" ::"{v237}"(tcd));
          if (TC2 == 1) {
            const unsigned tch = __builtin_amdgcn_raw_buffer_load_b32(rs, tca, qk_addr(w & 1, min(s + 1 + (w & 1), NSEG - 1), h), AUX_SC1);
            asm volatile("" ::"v"(tch));
          }
        } else {
          __builtin_amdgcn_s_sleep(KSL);
          unsigned tch = 0;
          TLB_TOUCH
          asm volatile("" ::"v"(tch));
        }
        if (w != TWV) EV(6);
        if (w != TWV) EVDUMP(s);
      }
    }
  } else {
    const v32u W0 = cat32(wb[0], wb[1], wb[2], wb[3]), W1 = cat32(wb[4], wb[5], wb[6], wb[7]), W2 = cat32(wb[8], wb[9], wb[10], wb[11]);
    const v32u W3 = cat32(wb[12], wb[13], wa[0], wa[1]), W4 = cat32(wa[2], wa[3], wa[4], wa[5]), W5 = cat32(wa[6], wa[7], wb[14], wb[15]);
    v8i adr;
    adr[7] = l16;
    adr[6] = l16 + w * 1024;
    adr[5] = pw0;
    adr[4] = sidx;
#pragma unroll
    for (int q = 0; q < 2; ++q) {
      const int c = 8 * (2 * q + (g >> 1) + ((g & 1) ? 0 : 4)) + (w ^ ((h >> 4) * 4));
      const int blk = (c % PO) * 2 + (h >> 4);
      adr[3 - q] = (int)OFF_VM + blk * 32768 + ((cls_of(perm, c / PO) ^ xreg(r_a + (int)(OFF_VM / UNIT) + (blk >> 4))) << 12) +
                   (h & 15) * 256 + l15 * 16;
    }
    adr[1] = (16 * (g & 1) + 4 * (g >> 1) + trq) * 24 + (trp & 1) * 8;
    adr[0] = L_HI + (32 * (w & 3) + l15) * 24 + 4 * g;
#define VCH(i) (VAL ? ((((i) - 1) ^ krot) & 3) | (((i) & 8) >> 1) | ((((i) - 1) & 4) << 1) : ((i) & 8) | ((((i) - 1) & 7) ^ krot))
    const int mo0 = MEM_OFF(VCH(w)), mo1 = MEM_OFF(VCH(w + 4)), mo2 = MEM_OFF(VCH(w + 8)), mo3 = MEM_OFF(VCH(w & 3));
    if (w == TWV) clk_edge(ck.r, ck.m, 2);
    if (w < 4) {
#pragma unroll 1
      for (int s = 0; s < NSEG; ++s) {
        EVDECL;
        const int so = (int)OFF_STEP + s * (int)UNIT, xr = xreg(r_s + s);
        unsigned cnt;
        DMDECL;
        asm volatile(VO_A
                     : [cnt] "=&s"(cnt) DMOUT EVOUT
                     : WPIN, "{v[244:251]}"(adr), "{v243}"(pw1), [rs] "s"(rsv), [o0] "s"(so + mo0 + ((cown ^ xr) << 12)), [vs] "s"(vm_slot(s))
                     : VO_CLOB, "v241", "vcc", "scc", "memory");
        if (s < 4) PT(11 + s);
        if (w == 0) {
          unsigned tch = 0;
          TLB_TOUCH
          asm volatile("" ::"v"(tch));
        }
        EVDUMP(s);
      }
    } else {
#pragma unroll 1
      for (int s = 0; s < NSEG; ++s) {
        EVDECL;
        const int so = (int)OFF_STEP + s * (int)UNIT, xr = xreg(r_s + s);
        const int lst = olane();
        int vst = ((cls_of(perm, lst & 3) ^ xr) << 12) | ((lst & 4) << 9) | ((lst & 8) << 12) | (((lst >> 4) | (w != TWV)) << 28);
        unsigned cnt;
        v2u st = {(unsigned)(ST_ADR | ((w != TWV) << 30)), 0u};
        int o0 = so + mo0 + ((cown ^ xr) << 12), o1 = o0 - mo0 + mo1, o2 = o0 - mo0 + mo2, vs = vm_slot(s), sst = so + R_ST + role * 1024 + h * 32;
        int ss = so + R_ST + ((cown ^ xr) << 12);
        asm volatile("" : "+{v[240:241]}"(st), "+{v242}"(vst));
        asm volatile("" : "+s"(o0), "+s"(o1), "+s"(o2), "+s"(vs), "+s"(sst), "+s"(ss));
        DMDECL;
        TIMING_GO
        asm volatile(VO_B
                     : [cnt] "=&s"(cnt) DMOUT, "+{v[240:241]}"(st) EVOUT TQOUT
                     : WPIN, "{v[244:251]}"(adr), "{v243}"(pw1), "{v242}"(vst), [rs] "s"(rsv), [o0] "s"(o0), [o1] "s"(o1), [o2] "s"(o2),
                       [vs] "s"(vs), [ckm] "s"(ck.m - NCYV), [inv] "s"(ck.inv << 16), [r16] "s"(ck.r << 4), [sst] "s"(sst), [tg] "s"(tg),
                       [tw] "s"(tw), [ss] "s"(ss) DMIN(o0 - mo0 + mo3)
                     : VO_CLOB, "vcc", "scc", "memory");
        if (w == TWV && s < NSEG - 1) TIMING_WAVE
        if (w != TWV) EVDUMP(s);
      }
    }
  }
  __builtin_amdgcn_endpgm();
}
typedef f32x2 c2;
#define PKOP(name, txt)                                   \
  __device__ __forceinline__ c2 name(c2 a, c2 b) {        \
    c2 d;                                                 \
    asm("v_pk_add_f32 %0, %1, %2 " txt : "=v"(d) : "v"(a), "v"(b)); \
    return d;                                             \
  }
PKOP(c_addi, "op_sel:[0,1] op_sel_hi:[1,0] neg_lo:[0,1]")
PKOP(c_addc, "neg_hi:[0,1]")
PKOP(c_sepb, "op_sel:[1,1] op_sel_hi:[0,0] neg_hi:[1,0]")
PKOP(c_mixc, "op_sel:[0,1] op_sel_hi:[1,0] neg_hi:[1,0]")
#define T_ADD(d, a, b) "v_pk_add_f32 %" #d ", %" #a ", %" #b "\n\t"
#define T_SUB(d, a, b) "v_pk_add_f32 %" #d ", %" #a ", %" #b " neg_lo:[0,1] neg_hi:[0,1]\n\t"
#define T_ROT(d, a, b, n) "v_pk_add_f32 %" #d ", %" #a ", %" #b " op_sel:[0,1] op_sel_hi:[1,0] " n ":[0,1]\n\t"
#define T_MUL(d, a, w) "v_pk_mul_f32 %" #d ", %" #a ", %" #w " op_sel_hi:[1,0]\n\t"
#define T_FRE(d, a, w, c) "v_pk_fma_f32 %" #d ", %" #a ", %" #w ", %" #c " op_sel_hi:[1,0,1]\n\t"
#define T_FIM(d, a, w, c, n) "v_pk_fma_f32 %" #d ", %" #a ", %" #w ", %" #c " op_sel:[1,1,0] op_sel_hi:[0,1,1] " n ":[1,0,0]\n\t"
#define T_2MN(d, a, t) "v_pk_fma_f32 %" #d ", %" #a ", 2.0, %" #t " op_sel_hi:[1,0,1] neg_lo:[0,0,1] neg_hi:[0,0,1]\n\t"
#define T_TAIL(n, m) T_ADD(0, 4, 5) T_ROT(1, 2, 3, m) T_SUB(4, 4, 5) T_ROT(3, 2, 3, n)
#define T_D0(n, m) T_ADD(4, 0, 2) T_ADD(5, 1, 3) T_SUB(2, 0, 2) T_SUB(3, 1, 3) T_TAIL(n, m)
#define T_D1(n, m) T_MUL(5, 1, 7) T_ROT(4, 0, 2, m) T_FIM(1, 1, 7, 5, n) T_ROT(2, 0, 2, n) T_FRE(5, 3, 9, 1) T_FIM(5, 3, 9, 5, n) T_2MN(3, 1, 5) T_TAIL(n, m)
#define T_D2(n, m) T_MUL(5, 1, 7) T_FRE(4, 2, 8, 0) T_FIM(1, 1, 7, 5, n) T_FIM(4, 2, 8, 4, n) T_FRE(5, 3, 9, 1) T_2MN(2, 0, 4) T_FIM(5, 3, 9, 5, n) \
    T_ADD(0, 4, 5) T_2MN(3, 1, 5) T_SUB(4, 4, 5) T_ROT(1, 2, 3, m) T_ROT(3, 2, 3, n)
#define T_D3(n, m) T_MUL(4, 0, 6) T_MUL(5, 1, 7) T_FIM(0, 0, 6, 4, n) T_FIM(1, 1, 7, 5, n) T_FRE(4, 2, 8, 0) T_FRE(5, 3, 9, 1) \
    T_FIM(4, 2, 8, 4, n) T_FIM(5, 3, 9, 5, n) T_2MN(2, 0, 4) T_2MN(3, 1, 5) T_TAIL(n, m)
#define D4_ASM(T, C)                                                                                                       \
  {                                                                                                                        \
    if constexpr (INV) asm(T("neg_hi", "neg_lo") : "+v"(x0), "+v"(x1), "+v"(x2), "+v"(x3), "=&v"(t0), "=&v"(t1) : C(w0), C(w1), C(w2), C(w3)); \
    else asm(T("neg_lo", "neg_hi") : "+v"(x0), "+v"(x1), "+v"(x2), "+v"(x3), "=&v"(t0), "=&v"(t1) : C(w0), C(w1), C(w2), C(w3));               \
  }
__device__ __forceinline__ constexpr float cos32(int m) {
  constexpr float C[9] = {1.f, 0.98078528040323044f, 0.92387953251128674f, 0.83146961230254524f, 0.70710678118654752f,
                          0.55557023301960218f, 0.38268343236508977f, 0.19509032201612825f, 0.f};
  m &= 31;
  if (m > 16) m = 32 - m;
  return m > 8 ? -C[16 - m] : C[m];
}
#define W32(m) c2{cos32(m), -cos32((m) - 8)}
template <bool INV, int M0, int M1, int M2, int M3>
__device__ __forceinline__ void dft4k(c2& x0, c2& x1, c2& x2, c2& x3) {
  c2 t0, t1;
  if constexpr (M3 == 0) {
    const int w0 = 0, w1 = 0, w2 = 0, w3 = 0;
    D4_ASM(T_D0, "n")
  } else {
    const c2 w1 = W32(M1), w3 = W32(M3), w0 = M0 ? W32(M0) : w1, w2 = M0 == 0 && M2 == 8 ? w1 : W32(M2);
    if constexpr (M0 != 0) D4_ASM(T_D3, "s")
    else if constexpr (M2 == 8) D4_ASM(T_D1, "s")
    else D4_ASM(T_D2, "s")
  }
  x2 = t0;
}
template <bool INV, bool W0>
__device__ __forceinline__ void dft4w(c2& x0, c2& x1, c2& x2, c2& x3, c2 w0, c2 w1, c2 w2, c2 w3) {
  c2 t0, t1;
  if constexpr (W0) D4_ASM(T_D3, "v")
  else D4_ASM(T_D2, "v")
  x2 = t0;
}
#define T_B2(n, m) T_FRE(4, 1, 6, 0) T_FRE(5, 3, 7, 2) T_FIM(4, 1, 6, 4, n) T_FIM(5, 3, 7, 5, n) T_2MN(1, 0, 4) T_2MN(3, 2, 5)
#define T_B0(n, m) T_ADD(4, 0, 1) T_ROT(5, 2, 3, m) T_SUB(1, 0, 1) T_ROT(3, 2, 3, n)
template <bool INV, int K>
__device__ __forceinline__ void bf2(c2& x0, c2& x1, c2& x2, c2& x3, c2 w0, c2 w1) {
  c2 t0, t1;
  const c2 w2 = w0, w3 = w0;
  if constexpr (K == 0) D4_ASM(T_B2, "v")
  else if constexpr (K == 1) D4_ASM(T_B2, "s")
  else D4_ASM(T_B0, "s")
  x0 = t0;
  x2 = t1;
}
__device__ __forceinline__ void mul2(c2& d0, c2& d1, c2 a0, c2 w0, c2 a1, c2 w1) {
  asm(T_MUL(0, 2, 3) T_MUL(1, 4, 5) T_FIM(0, 2, 3, 0, "neg_lo") T_FIM(1, 4, 5, 1, "neg_lo") : "=&v"(d0), "=&v"(d1) : "v"(a0), "v"(w0), "v"(a1), "v"(w1));
}
template <int N, bool INV, int TW>
__device__ __forceinline__ void dftn(c2 (&v)[N], const c2* w) {
  constexpr int Q = N / 4;
#pragma unroll
  for (int n = 0; n < Q; ++n) {
    if (TW == 0) dft4k<INV, 0, 0, 0, 0>(v[n], v[Q + n], v[2 * Q + n], v[3 * Q + n]);
    else if (TW == 1 && n == 0) dft4w<INV, false>(v[n], v[Q + n], v[2 * Q + n], v[3 * Q + n], w[Q], w[Q + n], w[2 * Q + n], w[3 * Q + n]);
    else dft4w<INV, true>(v[n], v[Q + n], v[2 * Q + n], v[3 * Q + n], w[n], w[Q + n], w[2 * Q + n], w[3 * Q + n]);
  }
  c2 o[N];
  if constexpr (N == 32) {
#define D8A(K1)                                                                                          \
  dft4k<INV, 0, 2 * K1, 4 * K1, 6 * K1>(v[8 * K1], v[8 * K1 + 2], v[8 * K1 + 4], v[8 * K1 + 6]);          \
  dft4k<INV, K1, 3 * K1, 5 * K1, 7 * K1>(v[8 * K1 + 1], v[8 * K1 + 3], v[8 * K1 + 5], v[8 * K1 + 7]);
    D8A(0) D8A(1) D8A(2) D8A(3)
#pragma unroll
    for (int k = 0; k < 32; k += 8) {
      bf2<INV, 2>(v[k], v[k + 1], v[k + 4], v[k + 5], W32(4), W32(12));
      bf2<INV, 1>(v[k + 2], v[k + 3], v[k + 6], v[k + 7], W32(4), W32(12));
    }
#pragma unroll
    for (int i = 0; i < 32; ++i) o[(i >> 3) + 2 * (i & 6) + 16 * (i & 1)] = v[i];
  } else {
    dft4k<INV, 0, 0, 0, 0>(v[0], v[1], v[2], v[3]);
    dft4k<INV, 0, 2, 4, 6>(v[4], v[5], v[6], v[7]);
    dft4k<INV, 0, 4, 8, 12>(v[8], v[9], v[10], v[11]);
    dft4k<INV, 0, 6, 12, 18>(v[12], v[13], v[14], v[15]);
#pragma unroll
    for (int i = 0; i < 16; ++i) o[(i >> 2) + 4 * (i & 3)] = v[i];
  }
#pragma unroll
  for (int i = 0; i < N; ++i) v[i] = o[i];
}
template <bool H32>
__device__ __forceinline__ void c_swap(c2& a, c2& b) {
#pragma unroll
  for (int j = 0; j < 2; ++j) {
    const unsigned x = __float_as_uint(a[j]), y = __float_as_uint(b[j]);
    const auto r = H32 ? __builtin_amdgcn_permlane32_swap(x, y, false, false) : __builtin_amdgcn_permlane16_swap(x, y, false, false);
    a[j] = __uint_as_float(r[0]);
    b[j] = __uint_as_float(r[1]);
  }
}
#define LANE_NOP()                  \
  do {                              \
    SB();                           \
    asm volatile("s_nop 1");        \
    SB();                           \
  } while (0)
__device__ __forceinline__ c2 lds_c2(int a) { return *(volatile LAS c2*)((LAS char*)g_lds + a); }
template <int N>
__device__ __forceinline__ void lds_put(const c2 (&v)[N], int a) {
#pragma unroll
  for (int i = 0; i < N / 2; ++i) *(volatile LAS f32x4*)((LAS char*)g_lds + a + 16 * i) = f32x4{v[2 * i][0], v[2 * i][1], v[2 * i + 1][0], v[2 * i + 1][1]};
}
constexpr int XS = 32768, WS_ = 16384, WT_OFF = 18 << 20;
__device__ __forceinline__ int side_adr(int hi, int lo, int sd) { return hi + ((lo >> 13) << 14) + (lo & 8191) + (sd ^ ((hi >> 8) & 8192)); }
constexpr int LDS_TW = 0;
constexpr int LDS_EB = 4096;
constexpr int LDS_STG = 4096 + 8 * 9216;
__device__ __forceinline__ void tw2048(c2 (&w)[32], int lane) {
#pragma unroll
  for (int k = 1; k <= 8; ++k) w[k] = lds_c2(LDS_TW + lane * k * 8);
  const c2 h = lds_c2(LDS_TW + (lane & 31) * 128);
  mul2(w[0], w[16], h, h, w[8], w[8]);
#pragma unroll
  for (int k = 9; k < 15; k += 2) mul2(w[k], w[k + 1], w[8], w[k - 8], w[8], w[k - 7]);
  mul2(w[15], w[24], w[8], w[7], w[16], w[8]);
#pragma unroll
  for (int k = 1; k < 8; ++k) mul2(w[16 + k], w[24 + k], w[16], w[k], w[24], w[k]);
}
template <bool INV>
__device__ __forceinline__ void fft2048(c2 (&v)[32], const c2 (&w)[32], int eb, int lane) {
  dftn<32, INV, 0>(v, w);
  lds_put<32>(v, eb + lane * 272);
  const int ra = eb + (lane >> 5) * 4352 + (lane & 31) * 8;
#pragma unroll
  for (int k = 0; k < 16; ++k) {
    v[k] = lds_c2(ra + k * 272);
    v[k + 16] = lds_c2(ra + k * 272 + 8704);
  }
#pragma unroll
  for (int k = 0; k < 16; k += 2) bf2<INV, 0>(v[k], v[k + 16], v[k + 1], v[k + 17], w[0], w[0]);
  LANE_NOP();
#pragma unroll
  for (int k = 0; k < 16; ++k) c_swap<true>(v[k], v[k + 16]);
  dftn<32, INV, 1>(v, w);
}
template <bool SC>
__device__ __forceinline__ void tw1024(c2 (&w)[16], c2 (&u)[4], int lane, float s) {
#pragma unroll
  for (int k = 1; k <= 4; ++k) w[k] = lds_c2(LDS_TW + lane * k * 16);
  u[1] = lds_c2(LDS_TW + (lane & 15) * 256);
  u[0] = u[1];
  mul2(u[2], w[8], u[1], u[1], w[4], w[4]);
  mul2(u[3], w[12], u[2], u[1], w[8], w[4]);
  w[0] = c2{s, 0.f};
  if (SC) {
#pragma unroll
    for (int k = 1; k < 4; ++k) w[k] *= s;
  }
  c2 z;
  mul2(w[5], w[6], w[4], w[1], w[4], w[2]);
  mul2(w[7], z, w[4], w[3], w[4], w[3]);
#pragma unroll
  for (int k = 1; k < 4; ++k) mul2(w[8 + k], w[12 + k], w[8], w[k], w[12], w[k]);
  if (SC) {
#pragma unroll
    for (int k = 4; k < 16; k += 4) w[k] *= s;
  }
}
template <bool INV>
__device__ __forceinline__ void fft1024(c2 (&v)[16], const c2 (&w)[16], const c2 (&u)[4], int eb, int lane) {
  dftn<16, INV, 0>(v, w);
  lds_put<16>(v, eb + lane * 144);
  const int ra = eb + ((lane >> 1) & 8) * 144 + (lane >> 5) * 576 + (lane & 15) * 8;
  c2 t[4][4];
#pragma unroll
  for (int a = 0; a < 4; ++a)
#pragma unroll
    for (int r = 0; r < 4; ++r) t[a][r] = lds_c2(ra + (a + 16 * r) * 144);
#pragma unroll
  for (int a = 0; a < 4; ++a) {
    dft4w<INV, false>(t[a][0], t[a][1], t[a][2], t[a][3], u[0], u[1], u[2], u[3]);
#pragma unroll
    for (int q = 0; q < 4; ++q) v[a + 8 * (q & 1) + 4 * (q >> 1)] = t[a][q];
  }
  LANE_NOP();
#pragma unroll
  for (int i = 0; i < 8; ++i) c_swap<false>(v[i], v[i + 8]);
#pragma unroll
  for (int i = 0; i < 8; ++i) c_swap<true>(v[(i & 3) + 2 * (i & 4)], v[(i & 3) + 2 * (i & 4) + 4]);
  dftn<16, INV, INV ? 2 : 1>(v, w);
}

__device__ __forceinline__ void fft_table(int w) {
  const int m = w * 64 + olane();
  float sn, cs;
  sincospif(-(float)m * (1.f / 1024.f), &sn, &cs);
  *(LAS c2*)((LAS char*)g_lds + LDS_TW + m * 8) = c2{cs, sn};
  RAW_BARRIER();
}
__device__ __forceinline__ void count_done(char* ws, int cnt_off, int w) {
  VMCNT(0);
  RAW_BARRIER();
  if (w == 0) cnt_inc(ws, cnt_off);
}
__device__ __forceinline__ void stg_adr(int lane, int w, int& up, int& mid, int& dn) {
  up = LDS_STG + lane * 128 + ((w ^ (lane >> 1)) & 7) * 16;
  dn = LDS_STG + (64 - lane) * 128 + ((w ^ ((64 - lane) >> 1)) & 7) * 16 + 8;
  mid = dn + 57344 - (lane == 0 ? 8 : 0);
}

#define P1A 2
#define P1B 0
#define P1X 0
#define FT(i)
#define FTV(i, x)
__device__ __forceinline__ void fft_pass1(char* ws, char* x1, int sd, const u16* __restrict__ x, const u16* __restrict__ wr,
                                          const u16* __restrict__ wi, int task, int w) {
  LAS char* const L = (LAS char*)g_lds;
  const __amdgpu_buffer_rsrc_t rx = __builtin_amdgcn_make_buffer_rsrc(x1, 0, 513 * XS, 0x00020000);
  const __amdgpu_buffer_rsrc_t rt = __builtin_amdgcn_make_buffer_rsrc(x1 + WT_OFF, 0, 513 * WS_, 0x00020000);
  const __amdgpu_buffer_rsrc_t ri = __builtin_amdgcn_make_buffer_rsrc((void*)x, 0, S * D * 2, 0x00020000);
  const __amdgpu_buffer_rsrc_t rr = __builtin_amdgcn_make_buffer_rsrc((void*)wr, 0, S * DF * 2, 0x00020000);
  const __amdgpu_buffer_rsrc_t rm = __builtin_amdgcn_make_buffer_rsrc((void*)wi, 0, S * DF * 2, 0x00020000);
  const int lane = olane();
  const int p = (P1X & 4) ? w : task * 8 + w, r0 = (p & 63) + 128 * (p >> 6);
  unsigned xa[16], xb[16], fr[2][9], fi[2][9];
  FT(0);
#pragma unroll
  for (int t = 0; t < 16; ++t) {
    xa[t] = __builtin_amdgcn_raw_buffer_load_b16(ri, lane * 2 + t * 128, r0 * 2048, P1A);
    xb[t] = __builtin_amdgcn_raw_buffer_load_b16(ri, lane * 2 + t * 128, r0 * 2048 + 131072, P1A);
  }
  const int l8 = lane == 0 ? 1024 : OOB;
#pragma unroll
  for (int r = 0; r < 2; ++r)
#pragma unroll
    for (int t = 0; t < 9; ++t) {
      fr[r][t] = __builtin_amdgcn_raw_buffer_load_b16(rr, t < 8 ? lane * 2 + t * 128 : l8, (r0 + 64 * r) * (DF * 2), P1B);
      fi[r][t] = __builtin_amdgcn_raw_buffer_load_b16(rm, t < 8 ? lane * 2 + t * 128 : l8, (r0 + 64 * r) * (DF * 2), P1B);
    }
  FT(1);
  c2 tw[16], tu[4], v[16];
  tw1024<false>(tw, tu, lane, 1.f);
  FTV(2, xb[15]);
#pragma unroll
  for (int t = 0; t < 16; ++t) v[t] = c2{__uint_as_float(xa[t] << 16), __uint_as_float(xb[t] << 16)};
  fft1024<false>(v, tw, tu, LDS_EB + w * 9216, lane);
  int up, mid, dn;
  stg_adr(lane, w, up, mid, dn);
#pragma unroll
  for (int j = 0; j < 16; ++j) *(volatile LAS c2*)(L + (j < 8 ? up + 8192 * j : (j == 8 ? mid : dn + 8192 * (15 - j)))) = v[j];
  FT(3);
  RAW_BARRIER();
  FTV(4, fi[1][8]);
  {
    const int fa = LDS_EB + lane * 64 + ((w ^ lane) & 6) * 8 + (w & 1) * 8;
#pragma unroll
    for (int t = 0; t < 9; ++t)
      if (t < 8 || lane == 0) *(LAS v2u*)(L + fa + t * 4096) = v2u{fr[0][t] | (fi[0][t] << 16), fr[1][t] | (fi[1][t] << 16)};
  }
  const int kq = w * 8 + (lane >> 3);
  {
    const int ra = LDS_STG + kq * 128 + ((lane ^ (kq >> 1)) & 7) * 16, ga = kq * XS + (lane & 7) * 16;
    v4u d[9];
#pragma unroll
    for (int i = 0; i < 9; ++i) d[i] = *(const LAS v4u*)(L + ra + i * 8192);
#pragma unroll
    for (int i = 0; i < 9; ++i)
      if ((i < 8 || w == 0) && !(P1X & 1))
        st128(d[i], rx, ga + (i == 8 ? 512 * XS : 0), side_adr((i & 7) * (64 * XS), task * 128, sd));
  }
  FT(5);
  RAW_BARRIER();
  {
    const int k4 = w * 16 + (lane >> 2);
    const int ra = LDS_EB + k4 * 64 + (((2 * lane) ^ k4) & 6) * 8, ga = k4 * WS_ + (lane & 3) * 16;
    v4u d[5];
#pragma unroll
    for (int i = 0; i < 5; ++i) d[i] = *(const LAS v4u*)(L + ra + i * 8192);
#pragma unroll
    for (int i = 0; i < 5; ++i)
      if ((i < 4 || w == 0) && !(P1X & 2))
        st128(d[i], rt, ga + (i == 4 ? 512 * WS_ : 0), side_adr((i & 3) * (128 * WS_), task * 64, sd ^ 8192));
  }
  FT(6);
  FT(7);
}
__device__ __forceinline__ void fft_pass2(char* ws, char* x1, int sd, int task, int w) {
  const __amdgpu_buffer_rsrc_t rx = __builtin_amdgcn_make_buffer_rsrc(x1, 0, 513 * XS, 0x00020000);
  const __amdgpu_buffer_rsrc_t rt = __builtin_amdgcn_make_buffer_rsrc(x1 + WT_OFF, 0, 513 * WS_, 0x00020000);
  const int lane = olane();
  const int k = task * 8 + w;
  if (k > 512) return;
  f32x4 d[16];
  FT(0);
#pragma unroll
  for (int t = 0; t < 16; ++t)
    d[t] = __builtin_bit_cast(f32x4, __builtin_amdgcn_raw_buffer_load_b128(rx, lane * 16 + (t & 3) * 1024, side_adr(k * XS, (t >> 2) * 4096, sd), AUX_SC1));
  c2 tw[32], v[32];
  tw2048(tw, lane);
  FT(1);
  const bool edge = (k & 511) == 0;
  FTV(2, d[15]);
  if (edge) {
#pragma unroll
    for (int t = 0; t < 16; ++t) {
      d[t][2] = d[t][0];
      d[t][3] = d[t][1];
    }
  }
#pragma unroll
  for (int t = 0; t < 16; ++t) {
    v[2 * t] = c_addc(c2{d[t][0], d[t][1]}, c2{d[t][2], d[t][3]});
    v[2 * t + 1] = c_sepb(c2{d[t][0], d[t][1]}, c2{d[t][2], d[t][3]});
  }
  SB();
  v2u f[16];
#pragma unroll
  for (int t = 0; t < 16; ++t) f[t] = __builtin_amdgcn_raw_buffer_load_b64(rt, lane * 8 + (t & 7) * 512, side_adr(k * WS_, (t >> 3) * 4096, sd ^ 8192), AUX_SC1);
  SB();
  const int eb = LDS_EB + w * 17408;
  fft2048<false>(v, tw, eb, lane);
  FTV(3, v[31]);
  FTV(4, f[15]);
#pragma unroll
  for (int t = 0; t < 16; ++t)
    mul2(v[2 * t], v[2 * t + 1], v[2 * t], c2{__uint_as_float(f[t][0] << 16), __uint_as_float(f[t][0] & 0xffff0000u)}, v[2 * t + 1],
         c2{__uint_as_float(f[t][1] << 16), __uint_as_float(f[t][1] & 0xffff0000u)});
  fft2048<true>(v, tw, eb, lane);
  FTV(5, v[31]);
  if (edge) {
#pragma unroll
    for (int t = 0; t < 32; ++t) v[t][1] = 0.f;
  }
#pragma unroll
  for (int t = 0; t < 16; ++t) {
    const c2 a = c_addi(v[2 * t], v[2 * t + 1]), b = c_mixc(v[2 * t], v[2 * t + 1]);
    st128(__builtin_bit_cast(v4u, f32x4{a[0], a[1], b[0], b[1]}), rx, lane * 16 + (t & 3) * 1024, side_adr(k * XS, (t >> 2) * 4096, sd));
  }
  FT(6);
  FT(7);
}
__device__ __forceinline__ void fft_pass3(char* ws, char* x1, int sd, u16* __restrict__ out, int task, int w) {
  LAS char* const L = (LAS char*)g_lds;
  const __amdgpu_buffer_rsrc_t rx = __builtin_amdgcn_make_buffer_rsrc(x1, 0, 513 * XS, 0x00020000);
  const __amdgpu_buffer_rsrc_t ro = __builtin_amdgcn_make_buffer_rsrc(out, 0, S * 4096, 0x00020000);
  const int lane = olane();
  const int kq = w * 8 + (lane >> 3);
  const int ra = LDS_STG + kq * 128 + ((lane ^ (kq >> 1)) & 7) * 16, ga = kq * XS + (lane & 7) * 16;
  v4u d[9];
  FT(0);
#pragma unroll
  for (int i = 0; i < 9; ++i) d[i] = __builtin_amdgcn_raw_buffer_load_b128(rx, ga + (i == 8 ? 512 * XS : 0), side_adr((i & 7) * (64 * XS), task * 128, sd), AUX_SC1);
  c2 tw[16], tu[4], v[16];
  tw1024<true>(tw, tu, lane, 1.f / (2.f * 2048.f * 1024.f));
  FT(1);
  FTV(2, d[8]);
#pragma unroll
  for (int i = 0; i < 9; ++i)
    if (i < 8 || w == 0) *(LAS v4u*)(L + ra + i * 8192) = d[i];
  int up, mid, dn;
  stg_adr(lane, w, up, mid, dn);
  FT(3);
  RAW_BARRIER();
  FT(4);
#pragma unroll
  for (int j = 0; j < 16; ++j) v[j] = lds_c2(j < 8 ? up + 8192 * j : (j == 8 ? mid : dn + 8192 * (15 - j)));
  fft1024<true>(v, tw, tu, LDS_EB + w * 9216, lane);
  FTV(5, v[15]);
  const int p = task * 8 + w, r0 = (p & 63) + 128 * (p >> 6);
#pragma unroll
  for (int t = 0; t < 16; ++t) {
    const unsigned o = pk2(v[t][0], v[t][1]);
    __builtin_amdgcn_raw_buffer_store_b16((u16)o, ro, lane * 2 + t * 128, r0 * 4096, 0);
    __builtin_amdgcn_raw_buffer_store_b16((u16)(o >> 16), ro, lane * 2 + t * 128, r0 * 4096 + 262144, 0);
  }
  FT(6);
  FT(7);
}

#define W7P 3
#define VTC 0
#define NXA 0
#define ARA 0
#define STA 0
#define SLR 2
#define RSO 5
#define NCAL 6
#define DOX2 300
#define DTC 100
#define NX1 4
#define NXG 2
#define NXK 5
#define OFD 400
#define STI 650
#define TNX 0
#define NXP 0
#define PACE 150
#define PFB 4
#define CV1 -92
#define CV2 108
#define EDL 0
#define EVB(k) EV(k)
constexpr int L_D = 16384, L_R = 32768, L_F = 122880;
__device__ __forceinline__ volatile LAS unsigned* flw(int i, int m) {
  return (volatile LAS unsigned*)((LAS char*)g_lds + L_F + i * 4 + (olane() & m) * 4);
}
template <int SLP = 0>
__device__ __forceinline__ void lds_wait_ge(int i, int m, unsigned v, gu32* tmo, unsigned code) {
  volatile LAS unsigned* p = flw(i, m);
  _Pragma("clang loop unroll(disable)") for (unsigned spins = 0;;) {
    if (__all(*p >= v)) break;
    if (SLP) __builtin_amdgcn_s_sleep(SLP);
    if (++spins > (kSpinCap << 5)) {
      tmo_report(tmo, code);
      break;
    }
  }
  asm volatile("" ::: "memory");
}
#define HLP 6
#define HLT 0
__device__ __forceinline__ void help(int c, int w, const u16* Wq, const u16* Wk, const u16* Wv) {
  const int j = c & 7, m = j >> 1, xg = c / PO, ln = olane();
  if ((c & 8) && w >= 1 && w <= HLP) {
    const u16* wm = m == 2 ? Wv : ((m == 3 ? j : m) & 1 ? Wk : Wq);
    const int hs = m == 3 ? 31 : PQ * xg + slow_head(wm) + 4 * (j & 1);
    if (m < 3 || xg == NXC - 1)
      for (int q = 0; q < 2; ++q)
        touch4((const char*)wm + hs * 256 + (size_t)((16 - w) ^ (hs & KROT)) * 524288, (q * 32 + (ln >> 1)) * 8192 + (ln & 1) * 128, 65536 + w * 256);
  }
}
template <bool PRE>
__device__ __forceinline__ unsigned owner(const int c, const int w, const u64 nonce, const u16* __restrict__ mem0, char* ws,
                                          u16* __restrict__ out, unsigned perm, const u16* Wq, const u16* Wk, const u16* Wv) {
  LAS char* const L = (LAS char*)g_lds;
  gu32* tmo = (gu32*)(ws + OFF_TMO);
  const int lane = olane();
  const int l15 = lane & 15, g = lane >> 4;
  const __amdgpu_buffer_rsrc_t rs = __builtin_amdgcn_make_buffer_rsrc(ws, 0, (int)WS_USED, 0x00020000);
  const int xg = c / PO, li = c % PO;
  const int r_a = (int)(((uintptr_t)ws >> 19) & 7);
  const int r_s = r_a + (int)(OFF_STEP / UNIT);
  volatile LAS unsigned* fl = (volatile LAS unsigned*)(L + L_F);
  unsigned m0v = 0;
  if constexpr (PRE) {
  if (w == 7) m0v = *(const unsigned*)(mem0 + (lane >> 3) * D + 16 * c + 2 * (lane & 7));
  if (c == 0 && w == 6) hs_lead<4>(rs, true, CNT_HSO, 64u);
  unsigned cal = 0;
  if (w == 0)
    st128(v4u{(unsigned)lane, 0u, 0u, 0u}, rs, (lane & 31) * 16 + (lane >> 5) * 32768, (int)OFF_SCR + (((li & 7) ^ xreg(r_a)) << 12) + (c >> 3) * 512);
  if (w == 0 && lane == 0) {
    const int cl = li & 7;
    int off = (int)OFF_SCR + ((cl ^ xreg(r_a)) << 12) + (c >> 3) * 512;
    unsigned best = 0xfffeu;
#pragma unroll 1
    for (int r = 0; r < NCAL; ++r) {
      VMCNT(0);
      const u64 t1 = __builtin_amdgcn_s_memtime();
      asm volatile("s_waitcnt lgkmcnt(0)" : "+v"(off)::"memory");
      const unsigned v = __builtin_amdgcn_raw_buffer_load_b32(rs, off, (r & 3) * 128 + (r >> 2) * 32768, AUX_SC1);
      asm volatile("s_waitcnt vmcnt(0)" ::"v"(v) : "memory");
      const u64 t2 = __builtin_amdgcn_s_memtime();
      best = min(best, (unsigned)(t2 - t1));
    }
    if (HLT == 0 && li >= 8) best = 0xfffeu;
    cal = ((unsigned)cl << 16) | best;
  }
  if (w == 0) {
    PT(0);
    hs_wait(rs, nonce, v2u{~(unsigned)nonce, 0u});
    cnt_inc(ws, CNT_HSO);
    PT(1);
    unsigned id;
    asm volatile("s_getreg_b32 %0, hwreg(HW_REG_XCC_ID)" : "=s"(id));
    if (lane == 0) {
      __builtin_amdgcn_raw_buffer_store_b32(((id & 15u) << 19) | cal, rs, c * 4, (int)OFF_CAL, AUX_SC1);
      if (li == 0) __builtin_amdgcn_raw_buffer_store_b32(id & 15u, rs, xg * 4, (int)OFF_XID, AUX_SC1);
    }
  }
  static_assert(NF1O == 64 || NF1O == 2, "one task per owner but the first two, or none");
  __syncthreads();
  if (HLT == 1) help(c, w, Wq, Wk, Wv);
  if (w == 7) {
    const unsigned pm = numa_perm(numa_load(rs, tmo), (LAS unsigned*)(L + L_F + 512));
    fl[10] = pm;
  }
  __syncthreads();
  perm = __builtin_amdgcn_readfirstlane(fl[10]);
  PT(2);
  if (HLT == 2) help(c, w, Wq, Wk, Wv);
  }
  const int xr0 = xreg(r_s + (PRE ? 0 : NARM + (li & 1)));
  const v4u sent = {0xffffffffu, 0xffffffffu, 0xffffffffu, 0xffffffffu};
  const int ibs = (int)OFF_VM + li * 65536 + ((cls_of(perm, xg) ^ xreg(r_a + (int)(OFF_VM / UNIT) + (li >> 3))) << 12);

  int am = (int)OFF_STEP + R_MEM + (c >> 4) * 32768 + ((cls_of(perm, g) ^ xr0) << 12) + (c & 15) * 256 + l15 * 16;
  int ap[2];
#pragma unroll
  for (int q = 0; q < 2; ++q) {
    const int o = c * 384 + (lane & 31) * 16;
    ap[q] = (lane & 31) < 24 && c < 59 ? (int)OFF_STEP + R_EX + (o >> 12) * 32768 + ((cls_of(perm, 2 * q + (lane >> 5)) ^ xr0) << 12) + (o & 4095)
                             : OOB;
  }
  int as = lane < 8 * NXC ? (int)OFF_STEP + R_ST + ((cls_of(perm, lane >> 3) ^ xr0) << 12) + (c >> 5) * 32768 + (c & 31) * 128 + (lane & 7) * 16
                          : OOB;
  const int ax = (int)OFF_STEP + R_PX + c * 1280 + lane * 16;
  const int ax2 = lane < 16 ? ax + 1024 : (lane < 24 ? (int)OFF_STEP + R_DUM + c * 128 + (lane - 16) * 16 : OOB);
#define ARM_REC(r)                                                                                                   \
  {                                                                                                                  \
    const int so_ = (r) * (int)UNIT, dx_ = (xreg(r_s + (r)) ^ xreg(r_s + (r) + 1)) << 12;                            \
    __builtin_amdgcn_raw_buffer_store_b128(sent, rs, am, so_, AUX_SC1);                                              \
    am ^= dx_;                                                                                                       \
    _Pragma("unroll") for (int q = 0; q < 2; ++q) {                                                                  \
      __builtin_amdgcn_raw_buffer_store_b128(sent, rs, ap[q], so_, AUX_SC1);                                         \
      ap[q] ^= dx_;                                                                                                  \
    }                                                                                                                \
    __builtin_amdgcn_raw_buffer_store_b128(sent, rs, as, so_, AUX_SC1);                                              \
    as ^= dx_;                                                                                                       \
    __builtin_amdgcn_raw_buffer_store_b128(sent, rs, ax, so_, AUX_SC1);                                              \
    __builtin_amdgcn_raw_buffer_store_b128(sent, rs, ax2, so_, AUX_SC1);                                             \
  }
  if (PRE && w == 7) {
    {
      const int row = lane >> 3, np = lane & 7;
#pragma unroll
      for (int r = 0; r < NXC; ++r)
        __builtin_amdgcn_raw_buffer_store_b32(min(m0v, 0xfffffffeu), rs,
                                              (int)OFF_STEP + R_MEM + (c >> 4) * 32768 + ((cls_of(perm, r) ^ xreg(r_s)) << 12) +
                                                  (c & 15) * 256 + ((row & 1) + 2 * (np & 1) + 4 * (row >> 1)) * 16 + (np >> 1) * 4,
                                              0, AUX_SC1);
    }
    am |= 1 << 30;
#pragma unroll 1
    for (int r = 0; r < NARM + (li & 1); ++r) {
      ARM_REC(r)
      am &= ~(1 << 30);
    }
#pragma unroll
    for (int k = 0; k < 32; ++k)
      __builtin_amdgcn_raw_buffer_store_b128(sent, rs, lane * 16 + (k & 3) * 1024, ibs + vm_slot(k >> 3) + ((k >> 2) & 1) * 32768, AUX_SC1);
    VMCNT(0);
    PTW(3, 7);
    cnt_inc(ws, CNT_ARM);
  }
  if constexpr (PRE) return perm;
  PT(5);
  __syncthreads();
  if (w == 7 && lane < 32) fl[lane] = 0;
  __syncthreads();

  const int team = li & 1, c0 = c & ~1;
  const int l16 = lane * 16;
  const int cown = cls_of(perm, xg);
  if (w < 2) {
    const int r = (l15 & 1) + 2 * (l15 >> 2), kh = (l15 >> 1) & 1;
    const int nl = (kh ^ ((r >> 1) & 1)) + 2 * ((g & 1) ^ (r >> 2));
    typedef int v8i __attribute__((ext_vector_type(8)));
    v8i adr;
    adr[0] = l16;
    adr[1] = 32768 + SC_POS(2 * nl + (g >> 1), r);
    adr[2] = l16;
    adr[3] = L_D + w * 8192 + l16;
    adr[4] = ((l15 & 3) + 4 * g) * 16 + (l15 >> 2) * 4;
    adr[5] = (l15 & 1) ? 0x03020706 : 0x05040100;
    adr[6] = (l15 & 2) ? 0x4444eeee : 0xeeee4444;
    adr[7] = L_F + 48;
    u64 mk[4];
#pragma unroll
    for (int k = 0; k < 4; ++k) mk[k] = __builtin_amdgcn_ballot_w64(nl == k);
    const v4u rsv = {(unsigned)(uintptr_t)ws, (unsigned)((uintptr_t)ws >> 32) & 0xffffu, (unsigned)WS_USED, 0x00020000u};
    const int dtc_ = TN(12, DTC);
    const int eg = (c0 >> 2) & 1;
#pragma unroll 1
    for (int s = team; s < NSEG; s += 2) {
      EVDECL;
      const int so = (int)OFF_STEP + s * (int)UNIT;
      int sp = so + R_PM + ((cown ^ xreg(r_s + s)) << 12);
      int sv = ibs + (w - team) * 65536 + vm_slot(s);
      int sv1 = sv + eg * 32768, sv2 = sv + (1 - eg) * 32768, sp1 = sp + eg * 2048, sp2 = sp + (1 - eg) * 2048;
      const int xn = xreg(r_s + s + 1);
      int sm[NXC];
#pragma unroll
      for (int q = 0; q < NXC; ++q) {
        sm[q] = so + (int)UNIT + R_MEM + (c0 >> 4) * 32768 + ((cls_of(perm, q) ^ xn) << 12) + ((c0 + w) & 15) * 256;
        asm volatile("" : "+s"(sm[q]));
      }
      asm volatile("" : "+s"(sp), "+s"(sv1), "+s"(sv2), "+s"(sp1), "+s"(sp2));
      if (s >= 2) lds_wait_ge<4>(20, 3, s - 1, tmo, 0x30017);
      if (VTC && w == 0) {
        const unsigned tc = __builtin_amdgcn_raw_buffer_load_b32(rs, 0, sv ^ 8192, AUX_SC1);
        asm volatile("" ::"v"(tc));
      }
      v4u bl[8];
      if (s < NSEG - 1) {
        v32u blv;
        unsigned cnt, bad;
        asm volatile(OW_F
                     : [cnt] "=&s"(cnt), [st] "=&s"(bad), "={v[0:31]}"(blv) EVOUT
                     : "{v[112:119]}"(adr), [rs] "s"(rsv), [sv1] "s"(sv1), [sv2] "s"(sv2), [sp] "s"(sp), [sp1] "s"(sp1), [sp2] "s"(sp2),
                       [sm0] "s"(sm[0]), [sm1] "s"(sm[1]), [sm2] "s"(sm[2]), [sm3] "s"(sm[3]), [m0] "s"(mk[0]), [m1] "s"(mk[1]),
                       [m2] "s"(mk[2]), [m3] "s"(mk[3])
                     : OF_CLOB, "vcc", "scc", "memory");
#pragma unroll
        for (int k = 0; k < 8; ++k) bl[k] = v4u{blv[4 * k], blv[4 * k + 1], blv[4 * k + 2], blv[4 * k + 3]};
        if (__builtin_expect(bad != 0u, 0)) {
          v4u xs, pv[4];
          _Pragma("clang loop unroll(disable)") for (unsigned spins = 0;;) {
            OPQ(va, l16);
            OPQ(v1, adr[1]);
            OPQ(v2, adr[2]);
#pragma unroll
            for (int k = 0; k < 8; ++k) bl[k] = __builtin_amdgcn_raw_buffer_load_b128(rs, va + (k & 3) * 1024, k < 4 ? sv1 : sv2, AUX_SC1);
            xs = __builtin_amdgcn_raw_buffer_load_b128(rs, v1, sp, AUX_SC1);
#pragma unroll
            for (int k = 0; k < 4; ++k) pv[k] = __builtin_amdgcn_raw_buffer_load_b128(rs, v2 + (k & 1) * 1024, k < 2 ? sp1 : sp2, AUX_SC1);
            unsigned m = mx4(0u, xs);
#pragma unroll
            for (int k = 0; k < 4; ++k) m = mx4(m, pv[k]);
#pragma unroll
            for (int k = 0; k < 8; ++k) m = mx4(m, bl[k]);
            if (!anysent(m)) break;
            __builtin_amdgcn_s_sleep(RSO);
            if (spin_fail(spins, tmo, 0x20000 + s)) break;
          }
          f32x4 a0 = {0.f, 0.f, 0.f, 0.f};
#pragma unroll
          for (int i = 0; i < 8; ++i) {
            const int k = F_ORD[i] >= 'a' ? F_ORD[i] - 'a' : F_ORD[i] - '0';
            if (F_ORD[i] >= 'a') {
              const v4u z = v4u{nl == k ? xs[0] : 0u, nl == k ? xs[1] : 0u, nl == k ? xs[2] : 0u, nl == k ? xs[3] : 0u};
              const v8u db = cat8(*(const LAS v4u*)(L + adr[3] + k * 2048), *(const LAS v4u*)(L + adr[3] + k * 2048 + 1024));
              a0 = (k & 1) ? smf1(z, db, a0, adr[6]) : smf(z, db, a0, adr[6]);
            } else
              a0 = smf(pv[k], cat8(bl[2 * k], bl[2 * k + 1]), a0, adr[6]);
          }
          const unsigned own = pk2(a0[0] + a0[2], a0[1] + a0[3]);
          const unsigned nb = (unsigned)__builtin_amdgcn_mov_dpp((int)own, 0xb1, 0xf, 0xf, true);
          const unsigned o = __builtin_amdgcn_perm(nb, own, (unsigned)adr[5]);
#pragma unroll
          for (int q = 0; q < NXC; ++q) __builtin_amdgcn_raw_buffer_store_b32(o, rs, adr[4], sm[q], AUX_SC1);
          EV(3);
        }
      } else {
        BARE_BARRIER();
        wait_cyc(*flw(12, 0));
        _Pragma("clang loop unroll(disable)") for (unsigned spins = 0;;) {
          OPQ(va, l16);
          unsigned m = 0;
#pragma unroll
          for (int k = 0; k < 8; ++k) bl[k] = __builtin_amdgcn_raw_buffer_load_b128(rs, va + (k & 3) * 1024, k < 4 ? sv1 : sv2, AUX_SC1);
#pragma unroll
          for (int k = 0; k < 8; ++k) m = mx4(m, bl[k]);
          if (!anysent(m)) break;
          __builtin_amdgcn_s_sleep(RSO);
          if (spin_fail(spins, tmo, 0x20000 + s)) break;
        }
      }
      const unsigned tp = cyc32();
      if (s < 4) PT(9 + (s >> 1));
      *flw(2 + w, 0) = s + 1;
      if (s < NSEG - 1) {
        const v4u tv = *(volatile LAS v4u*)flw(12, 0);
        const int lr = olane();
        __builtin_amdgcn_raw_buffer_store_b32(min(((tp - tv[1]) * tv[2]) >> 16, 60000u), rs,
                                              ((cls_of(perm, lr & 3) ^ xn) << 12) | ((lr & 4) << 9) | ((lr & 8) << 12) | ((lr >> 4) << 28),
                                              so + (int)UNIT + R_ST + (c0 + w) * 32 + 4, AUX_SC1);
      }
      OPQ(vs, l16);
#pragma unroll
      for (int k = 0; k < 8; ++k) *(LAS v4u*)(L + w * 8192 + ((k ^ (4 * eg)) * 1024) + vs) = bl[k];
      asm volatile("s_waitcnt lgkmcnt(0)" ::: "memory");
      *flw(4 + w, 0) = s + 1;
      if (s < NSEG - 4) {
        lds_wait_ge(2, 1, s + 1, tmo, 0x3001a);
        v4u se;
        asm volatile("v_mov_b32 %0, -1" : "=v"(se[0]));
        se[1] = se[2] = se[3] = se[0];
#pragma unroll
        for (int k = 0; k < 8; ++k) __builtin_amdgcn_raw_buffer_store_b128(se, rs, vs + (k & 3) * 1024, sv + (k >> 2) * 32768, AUX_SC1 | ARA);
        asm volatile("s_nop 1" ::"v"(se));
      }
      if (s < NSEG - 2) {
        wait_cyc(tp + dtc_);
        const int lt = olane();
        const unsigned tc = __builtin_amdgcn_raw_buffer_load_b32(rs, lt < 40 ? lt * 128 : OOB, so + R_PX + ((li & ~1) + w) * 5120, AUX_SC1);
        asm volatile("" ::"v"(tc));
      }
      EVDUMP(s);
    }
  } else if (w < 6) {
    const int n = w - 2;
    const v4u rsv = {(unsigned)(uintptr_t)ws, (unsigned)((uintptr_t)ws >> 32) & 0xffffu, (unsigned)WS_USED, 0x00020000u};
    const int vfr = (int)((perm & 8192u) ^ (VFAR == 2 ? 8192u : 0u)) | (r_a << 19);
    const int fv0 = fvw((c0 >> 1) & 3, 8 * n, team);
    const int dox_ = TN(10, DOX2), pace_ = TN(11, PACE);
    v4u raw[2][8];
#define RAW_LD(dst, sg)                                                                                       \
  _Pragma("unroll") for (int t = 0; t < 2; ++t) _Pragma("unroll") for (int k = 0; k < 8; ++k) dst[t][k] =     \
      __builtin_amdgcn_raw_buffer_load_b128(rs, l16 + (k & 3) * 1024, vw_addr(vfr, c0 + t, sg, 2 * n + (k >> 2)), AUX_SC1);
    wait_flag<KF0>(rs, fv0 + (lane & 7), tmo, 0x30018);
    PTW(6, 2);
    RAW_LD(raw, team)
    int nrdy = team;
#pragma unroll 1
    for (int s = team; s < NSEG; s += 2) {
      EVDECL;
      const int so = (int)OFF_STEP + s * (int)UNIT;
      if (s < NSEG - 1) {
        const int ln = olane();
        OPQ(eidx, (ln & 4) ? 0xeeee : 0x4444);
        OPQ(vea, (ln & 4) * 128 + (ln >> 4) * 128 + ((ln & 3) + 4 * (((ln >> 3) & 1) ^ (n >> 1))) * 16);
        int se = so + R_EX + n * 32768 + ((cown ^ xreg(r_s + s)) << 12);
        v8u bx[2][4];
#pragma unroll
        for (int t = 0; t < 2; ++t)
#pragma unroll
          for (int k = 0; k < 4; ++k) {
            const v4u u = raw[t][2 * k], v = raw[t][2 * k + 1];
            bx[t][k] = v8u{u[0], v[0], u[2], v[2], u[1], v[1], u[3], v[3]};
            asm volatile("" : "+v"(bx[t][k]));
          }
        OPQ(vd, L_D + n * 2048 + ln * 16);
        asm volatile("" : "+s"(se));
        EV(3);
        if (s < 4) PTW(7 + (s >> 1), 2);
        unsigned bad;
        asm volatile(OW_E
                     : [st] "=&s"(bad) EVOUT
                     : "{v241}"(vea), "{v242}"(vd), "{v243}"(eidx), [rs] "s"(rsv), [se] "s"(se), [b00] "v"(bx[0][0]), [b01] "v"(bx[0][1]),
                       [b02] "v"(bx[0][2]), [b03] "v"(bx[0][3]), [b10] "v"(bx[1][0]), [b11] "v"(bx[1][1]), [b12] "v"(bx[1][2]),
                       [b13] "v"(bx[1][3])
                     : OE_CLOB, "vcc", "scc", "memory");
        if (__builtin_expect(bad != 0u, 0)) {
          v4u pv[4];
          _Pragma("clang loop unroll(disable)") for (unsigned spins = 0;;) {
            __builtin_amdgcn_s_sleep(RSO);
            asm volatile("" : "+v"(vea));
            unsigned m = 0;
#pragma unroll
            for (int k = 0; k < 4; ++k) pv[k] = __builtin_amdgcn_raw_buffer_load_b128(rs, vea + k * 1024, se, AUX_SC1);
#pragma unroll
            for (int k = 0; k < 4; ++k) m = mx4(m, pv[k]);
            if (!anysent(m) || spin_fail(spins, tmo, 0x10000 + s)) break;
          }
#pragma unroll
          for (int q = 0; q < 4; ++q) {
            const f32x4 z = {0.f, 0.f, 0.f, 0.f};
            const f32x4 d = smf(pv[2 * (q & 1)], bx[q >> 1][2 * (q & 1)], z, eidx), e = smf(pv[2 * (q & 1) + 1], bx[q >> 1][2 * (q & 1) + 1], z, eidx);
            *(LAS v4u*)(L + vd + (q >> 1) * 8192 + (q & 1) * 1024) = v4u{pk2(d[0], d[1]), pk2(d[2], d[3]), pk2(e[0], e[1]), pk2(e[2], e[3])};
          }
          EV(2);
          RAW_BARRIER();
        }
      } else BARE_BARRIER();
      v4u nx[2][8];
      const int sn = min(s + 2, NSEG - 1);
      unsigned tpb = 0;
#define VW_FLAGS                                                                                                      \
  {                                                                                                                   \
    const int lf = olane();                                                                                           \
    OPQ(vfl, fv0 + min((s >> 1) + 1 + (lf >> 3), NSEG / 2 - 1) * 8 + (lf & 7));                                        \
    const u64 miss = __builtin_amdgcn_ballot_w64(__builtin_amdgcn_raw_buffer_load_b8(rs, vfl, 0, AUX_SC1) != 0);      \
    nrdy = s + 2 * (miss ? __builtin_ctzll(miss) >> 3 : 8);                                                           \
  }
      if (s < NSEG - 2) {
        __builtin_amdgcn_s_sleep(5);
        lds_wait_ge(2, 1, s + 1, tmo, 0x30014);
        tpb = cyc32();
        const unsigned tw = tpb + dox_;
        if (nrdy < s + 2) VW_FLAGS
        wait_cyc(tw);
      }
      EVB(6);
      OPQ(va, s < NSEG - 2 && nrdy >= s + 2 ? l16 : OOB);
#define NX_LD(dst, q0, q1)                                                                              \
  _Pragma("unroll") for (int q = q0; q < q1; ++q) dst[q >> 3][q & 7] = __builtin_amdgcn_raw_buffer_load_b128( \
      rs, va + (q & 3) * 1024, vw_addr(vfr, c0 + (q >> 3), sn, 2 * n + ((q >> 2) & 1)), AUX_SC1 | NXA);
      NX_LD(nx, 0, NX1)
      SB();
      if (s < NSEG - 2) wait_cyc(cyc32() + ((n + (li >> 1)) & 3) * pace_);
      else lds_wait_ge(4, 1, s + 1, tmo, 0x30014);
      EVB(4);
      f32x4 ac[2][2];
      const int sx = so + R_PX + n * 8192, sy = so + R_PX + 32768 + n * 2048;
      v4u pv[2][10], vm[2][2];
      OPQ(vb, l16);
#define P_LD                                                                                         \
  _Pragma("unroll") for (int k = 0; k < 10; ++k) _Pragma("unroll") for (int rt = 0; rt < 2; ++rt) pv[rt][k] = \
      __builtin_amdgcn_raw_buffer_load_b128(rs, vb + (k & 3) * 1024, (k < 8 ? sx + (k >> 2) * 4096 : sy) + rt * 40960, AUX_SC1);
#define P_MM(k0, k1)                                                                                 \
  _Pragma("unroll") for (int k = k0; k < k1; ++k) _Pragma("unroll") for (int rt = 0; rt < 2; ++rt) {  \
    _Pragma("unroll") for (int t = 0; t < 2; ++t)                                                    \
      ac[rt][t] = mma(k < 8 ? raw[t][k & 7] : vm[t][k & 1], pv[rt][k], k == 0 ? f32x4{0.f, 0.f, 0.f, 0.f} : ac[rt][t]); \
    asm volatile("" : "+v"(ac[rt][0]), "+v"(ac[rt][1]));                                             \
  }
      P_LD
      SB();
      lds_wait_ge(4, 1, s + 1, tmo, 0x30014);
#pragma unroll
      for (int t = 0; t < 2; ++t)
#pragma unroll
        for (int k = 0; k < 2; ++k) vm[t][k] = *(const LAS v4u*)(L + vb + t * 8192 + n * 2048 + k * 1024);
      SB();
      P_MM(0, 10)
      if (__builtin_expect(__builtin_amdgcn_ballot_w64(__builtin_isunordered(ac[0][0][0], ac[1][0][0])) != 0ull, 0)) {
        _Pragma("clang loop unroll(disable)") for (unsigned spins = 0;;) {
          asm volatile("" : "+v"(vb));
          P_LD
          unsigned m = 0;
#pragma unroll
          for (int k = 0; k < 10; ++k) m = mx4(mx4(m, pv[0][k]), pv[1][k]);
          if (!anysent(m)) break;
          __builtin_amdgcn_s_sleep(8);
          if (spin_fail(spins, tmo, 0x30015)) break;
        }
        P_MM(0, 10)
      }
      EVB(7);
      if (__builtin_expect(s < NSEG - 2 && nrdy < s + 2, 0)) {
        _Pragma("clang loop unroll(disable)") for (unsigned spins = 0;;) {
          VW_FLAGS
          if (nrdy >= s + 2) break;
          __builtin_amdgcn_s_sleep(8);
          if (spin_fail(spins, tmo, 0x30018)) break;
        }
        va = vb;
        NX_LD(nx, 0, NX1)
      }
      NX_LD(raw, NX1, 16)
      SB();
      OPQ(vr, L_R + (s & 2) * 8192 + l16);
#pragma unroll
      for (int q = 0; q < 4; ++q) *(LAS f32x4*)(L + vr + q * 4096 + n * 1024) = ac[q >> 1][q & 1];
      asm volatile("s_waitcnt lgkmcnt(0)" ::: "memory");
      fl[20 + n] = s + 1;
      lds_wait_ge(20, 3, s + 1, tmo, 0x30016);
      {
        const LAS f32x4* pr = (const LAS f32x4*)(L + vr + n * 4096);
        const f32x4 o = (pr[0] + pr[64]) + (pr[128] + pr[192]);
        const int lo = olane();
        *(v2u*)(out + (size_t)(s * 32 + 16 * (n >> 1) + (lo & 15)) * 2048 + 1024 + 16 * (c0 + (n & 1)) + 4 * (lo >> 4)) =
            v2u{pk2(o[0], o[1]), pk2(o[2], o[3])};
      }
      EVB(5);
#pragma unroll
      for (int q = 0; q < NX1; ++q) raw[q >> 3][q & 7] = nx[q >> 3][q & 7];
      EVDUMP(s);
    }
  } else if (w == 6) {
    if (c == 1) hs_lead<64>(rs, false, CNT_HS, 192u);
  } else {
    int narm = NARM + team;
    if (W7P) __builtin_amdgcn_s_setprio(W7P);
    const int sdk_ = NS2F(TN(2, SDK)), cv1_ = NS2F(TN(3, CV1)), cv2_ = NS2F(TN(4, CV2));
    arm_wait(rs, tmo);
    PTW(4, 7);
    Clk ck;
    clk_init(ck);
    unsigned t1 = cyc32();
    const int mg_ = NS2F(TN(0, MARG)), tol_ = NS2F(TN(13, TOL)), xl_ = NS2F(TN(14, XLATE));
    Pace pc = {0u, NS2F(ED0), NS2F(EA0), 0};
    const int sti_ = TN(15, STI), ofd_ = NS2F(TN(9, OFD));
#pragma unroll 1
    for (int s = 0; s < NSEG; ++s) {
      EVDECL;
      const bool on = (s & 1) == team;
      if (!on || s < 3) clk_update(ck, s < 3);
      int k1 = (cv1_ * (int)ck.cq) >> 16, k2 = (cv2_ * (int)ck.cq) >> 16;
      int sst = (int)OFF_STEP + s * (int)UNIT + R_ST + ((cown ^ xreg(r_s + s)) << 12);
      asm volatile("" : "+s"(k1), "+s"(k2), "+s"(sst));
      OPQ(va, lane * 32);
      OPQ(vt, L_F + 48);
      wait_cyc(t1);
      EV(0);
      v2u st = __builtin_amdgcn_raw_buffer_load_b64(rs, va, sst, AUX_SC1 | STA);
      clk_fresh(ck);
      asm volatile("" : "+s"(ck.r), "+s"(ck.m));
      asm volatile("" : "+v"(st));
      if (__builtin_expect(anysent(st[0]), 0)) {
        _Pragma("clang loop unroll(disable)") for (unsigned spins = 1;; ++spins) {
          asm volatile("" : "+v"(va));
          if (spins <= 2 || !on) wait_cyc(t1 + spins * sti_);
          else if (spins > 64) __builtin_amdgcn_s_sleep(4);
          else if (SLR) __builtin_amdgcn_s_sleep(SLR);
          st = __builtin_amdgcn_raw_buffer_load_b64(rs, va, sst + (spins == 1 ? 2048 : (spins == 2 ? 32768 : 0)), AUX_SC1 | STA);
          if (!anysent(st[0])) break;
          if (spins > kSpinCap) {
            tmo_report(tmo, 0x3000b);
            break;
          }
        }
        if (s == 0) clk_fresh(ck);
      }
      if (s < 4) PTW(11 + s, 7);
      const int dm = stamp_max(ck, st[0]);
      const unsigned tr = clk_cyc(ck, dm);
      if (on) {
        *(volatile LAS v4u*)(L + vt) = v4u{tr + k2, tr, ck.inv, 0u};
        EV(1);
        wait_cyc(tr + k1);
        RAW_BARRIER();
        if (s < NSEG - 1) BARE_BARRIER();
        if (narm < NSEG) {
          __builtin_amdgcn_s_sleep(5);
          lds_wait_ge(2, 1, s + 1, tmo, 0x3001b);
#pragma unroll 1
          for (int q = 0; q < 2; ++q) {
            if (narm < NSEG) ARM_REC(narm)
            ++narm;
          }
        }
      }
      pace_step(pc, s, (ck.r << 4) + dm, st[1], mg_, tol_, xl_);
      t1 = clk_cyc(ck, dm + pc.dq + pc.ea - sdk_ + (on ? ofd_ : 0));
      EVDUMP(s);
      EVCLK(s, ck);
    }
  }
  return 0u;
}
__global__ __launch_bounds__(512, 2) void mega_kernel(const u16* __restrict__ x, const u16* __restrict__ mem0,
                                                      const u16* __restrict__ Wq, const u16* __restrict__ Wk,
                                                      const u16* __restrict__ Wv, const u16* __restrict__ Wo,
                                                      char* __restrict__ ws, u16* __restrict__ out,
                                                      const u16* __restrict__ ax, const u16* __restrict__ awr,
                                                      const u16* __restrict__ awi) {
  ws += (size_t)(-(uintptr_t)ws) & (UNIT - 1);
  const int bx = BIDX;
  const int w = __builtin_amdgcn_readfirstlane(threadIdx.x >> 6);
  const int xg = bx & 7, idx = bx >> 3;
#define TEND
#define TMID(k)
#define CODEPF 16
  if (w == 7 && idx < CODEPF) {
    u64 pc;
    unsigned len, keep;
    asm volatile("s_getpc_b64 %0\n.Lcode_here:\n\ts_mov_b32 %1, .Lcode_end-.Lcode_here\n\t.subsection 1\n.Lcode_end:\n\t.subsection 0" : "=s"(pc), "=s"(len));
    const v4u rc = {(unsigned)pc, (unsigned)(pc >> 32) & 0xffffu, len, 0x00020000u};
    asm volatile("s_mov_b32 %0, m0\n\ts_mov_b32 m0, %3\n\ts_nop 0\n\tbuffer_load_dword %1, %2, 0 offen lds\n\ts_mov_b32 m0, %0"
                 : "=&s"(keep)
                 : "v"(olane() * 128 + idx * 8192), "s"(rc), "s"(32768 + 7 * 2048)
                 : "memory");
  }
  const u64 nonce = hs_post(__builtin_amdgcn_make_buffer_rsrc(ws, 0, 65536, 0x00020000), w, xg >= NXC && (idx & PQ) ? 7 : 0);
  TMID(0);
  int role, h, par = 0;
  if (xg < NXC) {
    role = idx >= 2 * PQ ? 4 : idx >= PQ;
    h = xg * PQ + idx % PQ;
  } else {
    role = 2 + (idx / PQ & 1);
    par = idx / (2 * PQ);
    h = (xg - NXC) * PQ + idx % PQ;
  }
  if (role == 0) wroles<0>(h, par, idx & 7, xg, w, nonce, x, Wq, Wk, Wv, Wo, ws);
  if (role == 1) wroles<1>(h, par, idx & 7, xg, w, nonce, x, Wq, Wk, Wv, Wo, ws);
  if (role == 2) wroles<2>(h, par, idx & 7, xg, w, nonce, x, Wq, Wk, Wv, Wo, ws);
  if (role == 3) wroles<3>(h, par, idx & 7, xg, w, nonce, x, Wq, Wk, Wv, Wo, ws);
  const int oc = xg * PO + idx - 2 * PQ;
  unsigned perm = 0;
  if (role == 4 && HLT == 0) help(oc, w, Wq, Wk, Wv);
  if (role == 4) {
    perm = owner<true>(oc, w, nonce, mem0, ws, out, 0u, Wq, Wk, Wv);
    VMCNT(0);
#define PFT 1300
#define PFM 5
#define PFG 0
    if (PFT && w < 6 && ((PFM >> (w >> 1)) & 1)) {
      const u16* src = w < 2 ? x : (w < 4 ? ax : (w == 4 ? awr : awi));
      const __amdgpu_buffer_rsrc_t ri = __builtin_amdgcn_make_buffer_rsrc((void*)src, 0, w < 4 ? S * D * 2 : S * DF * 2, 0x00020000);
      _Pragma("clang loop unroll(disable)") for (int k = 0; k < 4096 && now32() - (unsigned)nonce < PFT + (w >> 1) * PFG; ++k) __builtin_amdgcn_s_sleep(4);
      unsigned t[5];
#pragma unroll
      for (int q = 0; q < 5; ++q)
        t[q] = __builtin_amdgcn_raw_buffer_load_b32(ri, olane() * 128 + oc * 8192 + (q + (w < 4 ? (w & 1) * 4 : 0)) * 524288 + (w < 4 && q == 4 ? OOB : 0), 0, 0);
      asm volatile("" ::"v"(t[0]), "v"(t[1]), "v"(t[2]), "v"(t[3]), "v"(t[4]));
    }
  }
  TMID(2);
  const __amdgpu_buffer_rsrc_t rs = __builtin_amdgcn_make_buffer_rsrc(ws, 0, 65536, 0x00020000);
  gu32* tmo = (gu32*)(ws + OFF_TMO);
  volatile LAS int* sact = (volatile LAS int*)(g_lds + 163584);
  unsigned idle = 0;
  bool tab = false;
#define FSIDE 0
  char* const x1 = ws + OFF_X1 + ((size_t)(-(uintptr_t)(ws + OFF_X1)) & ((2u << 20) - 1));
  int sd = 0;
#define TLOG(id)
  auto pick = [&]() __attribute__((always_inline)) {
    int off = CNT_FFT1 + (olane() & 7) * 128;
    asm volatile("" : "+v"(off));
    const unsigned v = __builtin_amdgcn_raw_buffer_load_b32(rs, off, 0, AUX_SC1) + 1u;
    const unsigned d1 = __builtin_amdgcn_readlane(v, 0), d2 = __builtin_amdgcn_readlane(v, 1);
    const unsigned q1 = __builtin_amdgcn_readlane(v, 4), q2 = __builtin_amdgcn_readlane(v, 5), q3 = __builtin_amdgcn_readlane(v, 6);
    int kind = -1, qoff = 0;
    unsigned lim = 0;
    if (q1 < 130u - NF1O) kind = 1, qoff = Q_FFT1, lim = 130u - NF1O;
    else if (d1 >= 128u && q2 < 65u) kind = 2, qoff = Q_FFT2, lim = 65u;
    else if (d2 >= 65u && q3 < 128u) kind = 3, qoff = Q_FFT3, lim = 128u;
    else if (q3 >= 128u) kind = -2;
    int act = kind;
    if (kind >= 0) {
      unsigned t = 0;
      if (olane() == 0) t = __hip_atomic_fetch_add((gu32*)(ws + qoff), 1u, RLX_AGENT) + 1u;
      t = __builtin_amdgcn_readfirstlane(t);
      act = t < lim ? (kind << 16) | (int)t : -3;
    }
    return act;
  };
  for (;;) {
    if (w == 0) *sact = role == 4 ? (oc > 1 && oc < NF1O ? 0x18000 | oc : -2) : pick();
    RAW_BARRIER();
    const int act = __builtin_amdgcn_readfirstlane(*sact);
    RAW_BARRIER();
    if (act == -2) break;
    if (act >= 0) {
      const int kind = act >> 16, t = act & 0xffff;
      TLOG(1000 + kind);
      if (!tab) {
        if (w == 7) sact[1] = numa_far(numa_load(rs, tmo));
        fft_table(w);
        sd = (__builtin_amdgcn_readfirstlane(sact[1]) ^ (int)((uintptr_t)x1 >> 8) ^ FSIDE) & 8192;
      }
      tab = true;
      if (kind == 1) {
        fft_pass1(ws, x1, sd, ax, awr, awi, (t & 0x8000) ? t & 0xff : (t < 2 ? t : NF1O - 2 + t), w);
        count_done(ws, CNT_FFT1, w);
      } else if (kind == 2) {
        fft_pass2(ws, x1, sd, t == 0 ? 64 : t - 1, w);
        count_done(ws, CNT_FFT2, w);
      } else fft_pass3(ws, x1, sd, out, t, w);
      TLOG(999);
      if (role == 4) break;
    } else if (act == -1) {
      __builtin_amdgcn_s_sleep(64);
      if (spin_fail(idle, tmo, 0x30022)) break;
    }
  }
  if (role == 4) {
    owner<false>(oc, w, nonce, mem0, ws, out, perm, Wq, Wk, Wv);
    return;
  }
  TEND;
}

extern "C" void kernel_launch(void* const* d_in, const int* in_sizes, int n_in, void* d_out, int out_size, void* d_ws,
                              size_t ws_size, hipStream_t stream) {
  const u16* A_x = (const u16*)d_in[0];
  const u16* A_wr = (const u16*)d_in[1];
  const u16* A_wi = (const u16*)d_in[2];
  const u16* B_x = (const u16*)d_in[3];
  const u16* B_mem0 = (const u16*)d_in[4];
  const u16* Wq = (const u16*)d_in[5];
  const u16* Wk = (const u16*)d_in[6];
  const u16* Wv = (const u16*)d_in[7];
  const u16* Wo = (const u16*)d_in[8];
  u16* out = (u16*)d_out;
  char* ws = (char*)d_ws;

  mega_kernel<<<256, 512, 0, stream>>>(B_x, B_mem0, Wq, Wk, Wv, Wo, ws, out, A_x, A_wr, A_wi);
}
```

```cpp
#include <hip/hip_runtime.h>
#include <hip/hip_bf16.h>
#include <stdint.h>
#include <type_traits>

typedef unsigned short u16;
typedef unsigned long long u64;
using bf16x8 = __attribute__((ext_vector_type(8))) __bf16;
using bf16x16 = __attribute__((ext_vector_type(16))) __bf16;
using f32x4 = __attribute__((ext_vector_type(4))) float;
typedef unsigned v32u __attribute__((ext_vector_type(32)));
typedef unsigned v16u __attribute__((ext_vector_type(16)));
typedef unsigned v8u __attribute__((ext_vector_type(8)));
typedef unsigned v4u __attribute__((ext_vector_type(4)));
typedef unsigned v2u __attribute__((ext_vector_type(2)));
typedef short s16x4 __attribute__((ext_vector_type(4)));
typedef float f32x2 __attribute__((ext_vector_type(2)));
#define GAS __attribute__((address_space(1)))
#define LAS __attribute__((address_space(3)))
typedef GAS unsigned gu32;
#define RLX_AGENT __ATOMIC_RELAXED, __HIP_MEMORY_SCOPE_AGENT

constexpr int S = 2048, D = 1024, H = 32, DH = 128, SEG = 32, NSEG = 64;
constexpr int DF = 513;

#define XSW 4
#define BIDX (blockIdx.x ^ XSW)
constexpr int NXC = 4;
constexpr int PQ = 32 / NXC, PO = 64 / NXC;

__shared__ __attribute__((aligned(16))) char g_lds[163840];
#define SPINCAP (1u << 18)
constexpr unsigned kSpinCap = SPINCAP;
constexpr int AUX_SC1 = 16;
constexpr int OOB = 0x7ff00000;
constexpr size_t UNIT = 524288;
constexpr int F_QK = 0, F_VW = 8192, CNT_FFT1 = 16384, CNT_FFT2 = 16512, CNT_ARM = 16640;
constexpr int Q_FFT1 = 16896, Q_FFT2 = 17024, Q_FFT3 = 17152;
constexpr size_t OFF_TMO = 20480;
constexpr size_t OFF_CAL = 24576, OFF_XID = 25088;
constexpr size_t ARM_HOST = 28672;
constexpr int OFF_REQ = 49152, OFF_ACK = 53248, CNT_HS = 17280, CNT_HSO = 17408;
constexpr size_t OFF_SCR = 65536;
constexpr size_t OFF_VM = UNIT;
constexpr int VM_SLOT = 4 << 20, VM_SLOT2 = 8 << 20;
constexpr size_t OFF_DBG = 3 * UNIT;
constexpr size_t OFF_STEP = 27 * UNIT;
constexpr int R_MEM = 0, R_EX = 131072, R_PM = 262144, R_SC = 294912, R_ST = 327680, R_PX = 393216, R_DUM = 475136;
constexpr size_t OFF_QK = (size_t)48 << 20;
constexpr size_t OFF_X1 = (size_t)217 << 20;
#define GST 64
constexpr size_t OFF_VWX = (size_t)(GST == 64 ? 89 : 80) << 20;
constexpr size_t WS_USED = (size_t)217 << 20;
constexpr int NARM = 4;
#define SCL 1
#define SC_POS(q, r) ((q) * 256 + ((r) & 4) * 32 + ((r) & 3) * 16)

__device__ __forceinline__ int olane() {
  int t;
  asm volatile("v_mbcnt_lo_u32_b32 %0, -1, 0\n\tv_mbcnt_hi_u32_b32 %0, -1, %0" : "=v"(t));
  __builtin_assume(t >= 0 && t < 64);
  return t;
}
__device__ __forceinline__ float bf2f(u16 v) { return __uint_as_float((unsigned)v << 16); }
__device__ __forceinline__ unsigned pk2(float a, float b) {
  typedef __bf16 b2 __attribute__((ext_vector_type(2)));
  f32x2 f = {a, b};
  b2 r = __builtin_convertvector(f, b2);
  return __builtin_bit_cast(unsigned, r);
}
__device__ __forceinline__ unsigned pk2s(float a, float b) { return min(pk2(a, b), 0xfffffffeu); }
__device__ __forceinline__ u16 f2bf(float f) { return (u16)(pk2(f, 0.f) & 0xffffu); }
__device__ __forceinline__ float rbf(float f) { return __uint_as_float(pk2(0.f, f)); }
__device__ __forceinline__ float opq_one() {
  float o = 1.f;
  asm("" : "+s"(o));
  return o;
}
__device__ __forceinline__ unsigned fold_pk(f32x4 a, float one) { return pk2(a[0] + a[2], __builtin_fmaf(a[1], one, a[3])); }
__device__ __forceinline__ float mul1(float a, float c) {
  float r;
  asm("v_mul_f32 %0, %2, %1" : "=v"(r) : "v"(a), "s"(c));
  return r;
}
__device__ __forceinline__ float fma1(float a, float c, float b) {
  float r;
  asm("v_fma_f32 %0, %1, %2, %3" : "=v"(r) : "v"(a), "s"(c), "v"(b));
  return r;
}
__device__ __forceinline__ int xreg(int r) { return ((r & 1) << 2) | ((r >> 1) & 1) | ((r >> 1) & 2); }

__device__ __forceinline__ void tmo_report(gu32* tmo, unsigned code) {
  unsigned z = 0, c = code;
  asm volatile("" : "+v"(z), "+v"(c));
  asm volatile("global_store_dword %0, %1, %2 sc1" ::"v"(z), "v"(c), "s"(tmo) : "memory");
}
__device__ __forceinline__ bool spin_fail(unsigned& spins, gu32* tmo, unsigned code) {
  asm volatile("" ::: "memory");
  if (__builtin_expect(++spins > kSpinCap, 0)) {
    tmo_report(tmo, code);
    return true;
  }
  return false;
}
__device__ __forceinline__ unsigned wave_min(unsigned v) {
  v = min(v, (unsigned)__builtin_amdgcn_mov_dpp((int)v, 0x121, 0xf, 0xf, true));
  v = min(v, (unsigned)__builtin_amdgcn_mov_dpp((int)v, 0x122, 0xf, 0xf, true));
  v = min(v, (unsigned)__builtin_amdgcn_mov_dpp((int)v, 0x124, 0xf, 0xf, true));
  v = min(v, (unsigned)__builtin_amdgcn_mov_dpp((int)v, 0x128, 0xf, 0xf, true));
  auto r = __builtin_amdgcn_permlane32_swap(v, v, false, false);
  v = min(r[0], r[1]);
  auto q = __builtin_amdgcn_permlane16_swap(v, v, false, false);
  return min(q[0], q[1]);
}
__device__ __forceinline__ float xmax16_32(float x) {
  auto r = __builtin_amdgcn_permlane32_swap(__float_as_uint(x), __float_as_uint(x), false, false);
  x = __builtin_amdgcn_fmed3f(__uint_as_float(r[0]), __uint_as_float(r[1]), __builtin_inff());
  auto q = __builtin_amdgcn_permlane16_swap(__float_as_uint(x), __float_as_uint(x), false, false);
  return __builtin_amdgcn_fmed3f(__uint_as_float(q[0]), __uint_as_float(q[1]), __builtin_inff());
}
__device__ __forceinline__ float xsum16(float x) {
  auto q = __builtin_amdgcn_permlane16_swap(__float_as_uint(x), __float_as_uint(x), false, false);
  return __uint_as_float(q[0]) + __uint_as_float(q[1]);
}
__device__ __forceinline__ float xsum16_32(float x) {
  auto r = __builtin_amdgcn_permlane32_swap(__float_as_uint(x), __float_as_uint(x), false, false);
  return xsum16(__uint_as_float(r[0]) + __uint_as_float(r[1]));
}
__device__ __forceinline__ unsigned mx4(unsigned m, v4u v) { return max(max(max(max(m, v[0]), v[1]), v[2]), v[3]); }
__device__ __forceinline__ bool anysent(unsigned m) { return __builtin_amdgcn_ballot_w64(m == 0xffffffffu) != 0ull; }

__device__ __forceinline__ unsigned now32() { return (unsigned)__builtin_amdgcn_s_memrealtime(); }
__device__ __forceinline__ unsigned cyc32() { return (unsigned)__builtin_amdgcn_s_memtime(); }
#define CMAX 14000
#define LAL ".p2align 3\n"
__device__ __forceinline__ void wait_cyc(unsigned t, int d = 0x7fffffff) {
  t = __builtin_amdgcn_readfirstlane(t);
  if (d == 0x7fffffff) d = (int)(t - cyc32());
  if (d > CMAX) return;
  while (d > 260) {
    __builtin_amdgcn_s_sleep(1);
    d = (int)(t - cyc32());
  }
  d = __builtin_amdgcn_readfirstlane(d - 70);
  if (d > 0) asm volatile(LAL "1: s_sub_u32 %0, %0, 28\n\ts_cbranch_scc0 1b" : "+s"(d)::"scc", "memory");
}
#define CLKSPAN 124u
struct Clk {
  unsigned r, m;
  unsigned cq, inv;
};
__device__ __forceinline__ bool clk_edge(unsigned& r, unsigned& m, int tries) {
  constexpr int NS = 14;
#pragma unroll 1
  for (int k = 0; k < tries; ++k) {
    u64 t[NS], m0, m1;
    asm volatile("s_memtime %0" : "=s"(m0));
#pragma unroll
    for (int i = 0; i < NS; ++i) {
      asm volatile("s_memrealtime %0\n\ts_nop 0" : "=s"(t[i]));
      if (i == NS - 2) asm volatile("s_memtime %0" : "=s"(m1));
    }
    asm volatile("s_waitcnt lgkmcnt(0)" : "+s"(m0), "+s"(m1), "+s"(t[0]), "+s"(t[1]), "+s"(t[2]), "+s"(t[3]), "+s"(t[4]), "+s"(t[5]),
                 "+s"(t[6]), "+s"(t[7]), "+s"(t[8]), "+s"(t[9]), "+s"(t[10]), "+s"(t[11]), "+s"(t[12]), "+s"(t[13]));
    unsigned n = 0;
    static_assert(NS == 14, "");
    asm("s_cmp_eq_u32 %2, %1\n\ts_addc_u32 %0, %0, 0\n\t"
        "s_cmp_eq_u32 %3, %1\n\ts_addc_u32 %0, %0, 0\n\t"
        "s_cmp_eq_u32 %4, %1\n\ts_addc_u32 %0, %0, 0\n\t"
        "s_cmp_eq_u32 %5, %1\n\ts_addc_u32 %0, %0, 0\n\t"
        "s_cmp_eq_u32 %6, %1\n\ts_addc_u32 %0, %0, 0\n\t"
        "s_cmp_eq_u32 %7, %1\n\ts_addc_u32 %0, %0, 0\n\t"
        "s_cmp_eq_u32 %8, %1\n\ts_addc_u32 %0, %0, 0\n\t"
        "s_cmp_eq_u32 %9, %1\n\ts_addc_u32 %0, %0, 0\n\t"
        "s_cmp_eq_u32 %10, %1\n\ts_addc_u32 %0, %0, 0\n\t"
        "s_cmp_eq_u32 %11, %1\n\ts_addc_u32 %0, %0, 0\n\t"
        "s_cmp_eq_u32 %12, %1\n\ts_addc_u32 %0, %0, 0\n\t"
        "s_cmp_eq_u32 %13, %1\n\ts_addc_u32 %0, %0, 0\n\t"
        "s_cmp_eq_u32 %14, %1\n\ts_addc_u32 %0, %0, 0\n\t"
        : "+s"(n)
        : "s"((unsigned)t[0]), "s"((unsigned)t[1]), "s"((unsigned)t[2]), "s"((unsigned)t[3]), "s"((unsigned)t[4]), "s"((unsigned)t[5]), "s"((unsigned)t[6]), "s"((unsigned)t[7]), "s"((unsigned)t[8]), "s"((unsigned)t[9]), "s"((unsigned)t[10]), "s"((unsigned)t[11]), "s"((unsigned)t[12]), "s"((unsigned)t[13])
        : "scc");
    r = (unsigned)t[0] + 4;
    m = (unsigned)m0 + n * 8;
    asm volatile("" : "+s"(m));
    if ((unsigned)m1 - (unsigned)m0 <= CLKSPAN) return true;
  }
  return false;
}
__device__ __forceinline__ unsigned sdiv(unsigned a, unsigned b) {
  unsigned r;
  float t, u;
  asm volatile("v_cvt_f32_u32 %1, %4\n\tv_cvt_f32_u32 %2, %3\n\tv_rcp_f32 %1, %1\n\ts_nop 0\n\tv_mul_f32 %1, %1, %2\n\tv_cvt_u32_f32 %1, %1\n\t"
               "s_nop 0\n\tv_readfirstlane_b32 %0, %1\n\ts_nop 3"
               : "=s"(r), "=&v"(t), "=&v"(u)
               : "s"(a), "s"(b));
  return r;
}
__device__ __forceinline__ void clk_init(Clk& c) {
  clk_edge(c.r, c.m, 4);
  c.cq = 92500u;
  c.inv = 46432u;
}
__device__ __forceinline__ void clk_update(Clk& c, bool first = false) {
  unsigned r, m;
  if (!clk_edge(r, m, first ? 2 : 1) && r - c.r < 1500u) return;
  if (r - c.r >= 100u && r - c.r < 40000u) {
    unsigned q = sdiv((m - c.m) << 12, r - c.r);
    asm("s_max_u32 %0, %0, 0x101d0\n\ts_min_u32 %0, %0, 0x1b580" : "+s"(q)::"scc");
    c.cq = first ? q : (3 * c.cq + q) >> 2;
    c.inv = sdiv(0xffffffffu, c.cq);
  }
  c.r = r;
  c.m = m;
}
__device__ __forceinline__ void clk_step(Clk& c) {
  unsigned r, m;
  if (!clk_edge(r, m, 1) && r - c.r < 1500u) return;
  const unsigned dr = r - c.r;
  if (dr - 100u < 400u) {
    unsigned q = c.cq + 5 * (m - c.m - ((dr * c.cq) >> 12));
    asm("s_max_u32 %0, %0, 0x101d0\n\ts_min_u32 %0, %0, 0x1b580" : "+s"(q)::"scc");
    c.cq = q;
    c.inv += (unsigned)__mulhi((int)c.inv, -(int)(q * c.inv));
  }
  c.r = r;
  c.m = m;
}
__device__ __forceinline__ void wave_min2(unsigned& a, unsigned& b) {
  unsigned t, u;
#define M2(c) "v_min_u32_dpp %0, %0, %0 " c " row_mask:0xf bank_mask:0xf\n\tv_min_u32_dpp %1, %1, %1 " c " row_mask:0xf bank_mask:0xf\n\ts_nop 0\n\t"
#define S2(n) "v_mov_b32 %2, %0\n\tv_mov_b32 %3, %1\n\ts_nop 0\n\tv_permlane" n "_swap_b32 %0, %2\n\tv_permlane" n "_swap_b32 %1, %3\n\tv_min_u32 %0, %0, %2\n\tv_min_u32 %1, %1, %3\n\t"
  asm("s_nop 1\n\t" M2("row_ror:1") M2("row_ror:2") M2("row_ror:4") M2("row_ror:8") S2("32") S2("16") "s_nop 0" : "+v"(a), "+v"(b), "=&v"(t), "=&v"(u));
#undef M2
#undef S2
}
__device__ __forceinline__ void clk_fresh(Clk& c) {
  unsigned r, m;
  if (clk_edge(r, m, 1)) {
    c.r = r;
    c.m = m;
  }
}
__device__ __forceinline__ unsigned clk_fine(const Clk& c, unsigned m) {
  return (c.r << 4) + (unsigned)(((u64)(m - c.m) * c.inv) >> 16);
}
__device__ __forceinline__ int stamp_max(const Clk& c, unsigned st) {
  const unsigned tn = (c.r << 4) + 65536u;
  return 65536 - __builtin_amdgcn_readfirstlane((int)wave_min(tn - st));
}
__device__ __forceinline__ unsigned clk_cyc(const Clk& c, int d) { return c.m + (unsigned)(((long long)d * (int)c.cq) >> 16); }
#define TN(k, d) (d)
#define XL0 0
#define XL1 0
#define XL2 0
#define NS2F(ns) (((ns) * 8) / 5)
struct Pace {
  unsigned rf;
  int ed, ea, dq;
};
template <bool RED = true>
__device__ __forceinline__ void pace_step(Pace& p, int s, unsigned rf, unsigned rep, int marg, int tol, int xlate) {
  const int arr = (int)(rf - p.rf) - p.dq;
  const bool late = s == 0 || arr > p.ea + tol;
  if (s > 0) {
    const int md = RED ? (int)~__builtin_amdgcn_readfirstlane(wave_min(~rep)) : (int)rep;
    p.ea = min(arr, p.ea + 5);
    p.ed = (int)min((unsigned)md, (unsigned)p.ed + 5u);
  }
  p.rf = rf;
  p.dq = p.ed + marg + (late ? xlate : 0);
}

template <int SLP>
__device__ __forceinline__ void wait_flag(__amdgpu_buffer_rsrc_t rs, int off, gu32* tmo, unsigned code) {
  _Pragma("clang loop unroll(disable)") for (unsigned spins = 0;;) {
    asm volatile("" : "+v"(off));
    unsigned v = __builtin_amdgcn_raw_buffer_load_b8(rs, off, 0, AUX_SC1);
    if (__all(v == 0u)) break;
    __builtin_amdgcn_s_sleep(SLP);
    if (spin_fail(spins, tmo, code)) break;
  }
}
#define RAW_BARRIER()                                    \
  do {                                                   \
    asm volatile("" ::: "memory");                       \
    __builtin_amdgcn_s_waitcnt(0xc07f);                  \
    __builtin_amdgcn_s_barrier();                        \
    asm volatile("" ::: "memory");                       \
  } while (0)
#define BARE_BARRIER()                 \
  do {                                 \
    asm volatile("" ::: "memory");     \
    __builtin_amdgcn_s_barrier();      \
    asm volatile("" ::: "memory");     \
  } while (0)
#define VMCNT(n) asm volatile("s_waitcnt vmcnt(" #n ")" ::: "memory")
#define SB() __builtin_amdgcn_sched_barrier(0)
#define OPQ(v, e) int v = (e); asm volatile("" : "+v"(v))
template <bool NT = false>
__device__ __forceinline__ void glds16(const void* base, int voff, unsigned dst) {
  unsigned keep;
  if constexpr (NT)
    asm volatile("s_mov_b32 %0, m0\n\ts_mov_b32 m0, %3\n\ts_nop 0\n\tglobal_load_lds_dwordx4 %1, %2 nt\n\ts_mov_b32 m0, %0"
                 : "=&s"(keep)
                 : "v"(voff), "s"(base), "s"(dst)
                 : "memory");
  else
  asm volatile("s_mov_b32 %0, m0\n\ts_mov_b32 m0, %3\n\ts_nop 0\n\tglobal_load_lds_dwordx4 %1, %2\n\ts_mov_b32 m0, %0"
               : "=&s"(keep)
               : "v"(voff), "s"(base), "s"(dst)
               : "memory");
}
__device__ __forceinline__ void touch4(const void* base, int voff, unsigned dst) {
  unsigned keep;
  asm volatile("s_mov_b32 %0, m0\n\ts_mov_b32 m0, %3\n\ts_nop 0\n\tglobal_load_lds_dword %1, %2\n\ts_mov_b32 m0, %0"
               : "=&s"(keep)
               : "v"(voff), "s"(base), "s"(dst)
               : "memory");
}
__device__ __forceinline__ f32x4 smf(v4u a, v8u b, f32x4 c, int idx) {
  return __builtin_amdgcn_smfmac_f32_16x16x64_bf16(__builtin_bit_cast(bf16x8, a), __builtin_bit_cast(bf16x16, b), c, idx, 0, 0);
}
__device__ __forceinline__ f32x4 smf1(v4u a, v8u b, f32x4 c, int idx) {
  return __builtin_amdgcn_smfmac_f32_16x16x64_bf16(__builtin_bit_cast(bf16x8, a), __builtin_bit_cast(bf16x16, b), c, idx, 0, 1);
}
__device__ __forceinline__ f32x4 mma(v4u a, v4u b, f32x4 c) {
  return __builtin_amdgcn_mfma_f32_16x16x32_bf16(__builtin_bit_cast(bf16x8, a), __builtin_bit_cast(bf16x8, b), c, 0, 0, 0);
}
__device__ __forceinline__ void st128(v4u d, __amdgpu_buffer_rsrc_t rs, int voff, int soff) {
  __builtin_amdgcn_raw_buffer_store_b128(d, rs, voff, soff, AUX_SC1);
  asm volatile("s_nop 1" ::"v"(d));
}
__device__ __forceinline__ f32x4 zero4() {
  f32x2 a, b;
  asm volatile("v_mov_b64 %0, 0" : "=v"(a));
  asm volatile("v_mov_b64 %0, 0" : "=v"(b));
  return f32x4{a[0], a[1], b[0], b[1]};
}
__device__ __forceinline__ f32x2 zero2() {
  f32x2 a;
  asm volatile("v_mov_b64 %0, 0" : "=v"(a));
  return a;
}
__device__ __forceinline__ v4u lo4(v8u v) { return v4u{v[0], v[1], v[2], v[3]}; }
__device__ __forceinline__ v4u hi4(v8u v) { return v4u{v[4], v[5], v[6], v[7]}; }
__device__ __forceinline__ v8u cat8(v4u a, v4u b) { return v8u{a[0], a[1], a[2], a[3], b[0], b[1], b[2], b[3]}; }
__device__ __forceinline__ v32u cat32(v8u a, v8u b, v8u c, v8u d) {
  const v16u x = __builtin_shufflevector(a, b, 0, 1, 2, 3, 4, 5, 6, 7, 8, 9, 10, 11, 12, 13, 14, 15);
  const v16u y = __builtin_shufflevector(c, d, 0, 1, 2, 3, 4, 5, 6, 7, 8, 9, 10, 11, 12, 13, 14, 15);
  return __builtin_shufflevector(x, y, 0, 1, 2, 3, 4, 5, 6, 7, 8, 9, 10, 11, 12, 13, 14, 15, 16, 17, 18, 19, 20, 21, 22, 23, 24, 25, 26,
                                 27, 28, 29, 30, 31);
}
__device__ __forceinline__ v2u trd(LAS char* p) {
  return __builtin_bit_cast(v2u, __builtin_amdgcn_ds_read_tr16_b64_v4i16((LAS s16x4*)p));
}
__device__ __forceinline__ int vm_slot(int s) { return (s & 1) * VM_SLOT + ((s >> 1) & 1) * VM_SLOT2; }
__device__ __forceinline__ int cls_of(unsigned perm, int x) { return (int)((perm >> (3 * x)) & 7u); }

__device__ __forceinline__ void arm_wait(__amdgpu_buffer_rsrc_t rs, gu32* tmo) {
  _Pragma("clang loop unroll(disable)") for (unsigned spins = 0;;) {
    OPQ(off, CNT_ARM);
    if (__all(__builtin_amdgcn_raw_buffer_load_b32(rs, off, 0, AUX_SC1) + 1u >= 64u)) break;
    __builtin_amdgcn_s_sleep(8);
    if (spin_fail(spins, tmo, 0x30007)) break;
  }
}
__device__ __forceinline__ v2u numa_load(__amdgpu_buffer_rsrc_t rs, gu32* tmo) {
  unsigned c, xid;
  _Pragma("clang loop unroll(disable)") for (unsigned spins = 0;;) {
    OPQ(a, olane() * 4);
    c = __builtin_amdgcn_raw_buffer_load_b32(rs, a, (int)OFF_CAL, AUX_SC1);
    xid = __builtin_amdgcn_raw_buffer_load_b32(rs, ((a >> 5) & (NXC - 1)) * 4, (int)OFF_XID, AUX_SC1);
    if (__all((c != 0xffffffffu) & (xid != 0xffffffffu))) break;
    __builtin_amdgcn_s_sleep(16);
    if (spin_fail(spins, tmo, 0x30008)) break;
  }
  return v2u{c, xid};
}
__device__ __forceinline__ unsigned numa_perm(v2u in, LAS unsigned* tab) {
  const int lane = olane();
  const unsigned c = in[0], xid = in[1];
  tab[lane] = 0xffffu;
  tab[64 + lane] = 0xffffu;
  tab[128 + lane] = 0u;
  __builtin_amdgcn_s_waitcnt(0xc07f);
  __hip_atomic_fetch_min(tab + ((c >> 16) & 127u), c & 0xffffu, __ATOMIC_RELAXED, __HIP_MEMORY_SCOPE_WORKGROUP);
  __hip_atomic_fetch_add(tab + 128 + ((c >> 21) & 3u) * 2 + ((c >> 17) & 1u), c & 0xffffu, __ATOMIC_RELAXED, __HIP_MEMORY_SCOPE_WORKGROUP);
  __builtin_amdgcn_s_waitcnt(0xc07f);
  asm volatile("" ::: "memory");
  const unsigned s0 = tab[128 + ((xid >> 2) & 3u) * 2], s1 = tab[129 + ((xid >> 2) & 3u) * 2];
  const bool far = ((lane >> 1) & 1) != (s1 < s0 ? 1 : 0);
  unsigned key = ((tab[(xid & 15u) * 8 + (lane & 7)] + (far ? 0x10000u : 0u)) << 6) | (unsigned)lane;
  if ((lane >> 3) >= NXC) key = 0xffffffffu;
  unsigned perm = 0;
#pragma unroll 1
  for (int r = 0; r < NXC; ++r) {
    const unsigned m = wave_min(key);
    const unsigned bx = (m >> 3) & 7u, bc = m & 7u;
    perm |= bc << (3 * bx);
    if ((unsigned)(lane >> 3) == bx || (unsigned)(lane & 7) == bc) key = 0xffffffffu;
  }
  return __builtin_amdgcn_readfirstlane(perm | (s1 < s0 ? 0u : 8192u));
}
__device__ __forceinline__ int numa_far(v2u in) {
  const unsigned c = in[0], x0 = __builtin_amdgcn_readfirstlane(in[1]);
  int d = (int)(c & 0xffffu);
  d = ((c >> 21) & 3u) == ((x0 >> 2) & 3u) ? ((c & 0x20000u) ? -d : d) : 0;
  d += __builtin_amdgcn_mov_dpp(d, 0x121, 0xf, 0xf, true);
  d += __builtin_amdgcn_mov_dpp(d, 0x122, 0xf, 0xf, true);
  d += __builtin_amdgcn_mov_dpp(d, 0x124, 0xf, 0xf, true);
  d += __builtin_amdgcn_mov_dpp(d, 0x128, 0xf, 0xf, true);
  d = __builtin_amdgcn_readlane(d, 0) + __builtin_amdgcn_readlane(d, 16) + __builtin_amdgcn_readlane(d, 32) + __builtin_amdgcn_readlane(d, 48);
  return d > 0 ? 0 : 8192;
}
#define VRING 64
#define QRING 64
#define VFAR 0
__device__ __forceinline__ int vw_addr(int fr, int tile, int seg, int gq) {
  return (int)OFF_VWX + ((((tile >> 3) * GST + (seg & (VRING - 1))) * 8 + (tile & 7)) * 8 + gq) * 4096;
}
__device__ __forceinline__ int fqk(int h, int seg) { return F_QK + h * 256 + seg * 4; }
__device__ __forceinline__ int fvw(int pr, int h, int seg) { return F_VW + ((pr * 4 + (h >> 3)) * 2 + (seg & 1)) * 256 + (seg >> 1) * 8 + (h & 7); }
constexpr int QK_RT = 4096;
__device__ __forceinline__ int qk_addr(int mat, int seg, int h) {
  return (int)OFF_QK + ((mat * 64 + (seg & (QRING - 1))) * 32 + h) * 8192;
}

__device__ __forceinline__ u64 hs_post(__amdgpu_buffer_rsrc_t rs, int w, int HSW) {
  const u64 n = __builtin_amdgcn_s_memrealtime();
  if (w == HSW) __builtin_amdgcn_raw_buffer_store_b64(v2u{(unsigned)n, (unsigned)(n >> 32)}, rs, olane() == 0 ? 0 : OOB,
                                                    OFF_REQ + (int)BIDX * 8, AUX_SC1);
  return n;
}
__device__ __forceinline__ bool hs_wait(__amdgpu_buffer_rsrc_t rs, u64 n, v2u v) {
  unsigned spins = 0;
  _Pragma("clang loop unroll(disable)") for (; spins < (kSpinCap << 2); ++spins) {
    if (__builtin_expect(__all(v[0] == (unsigned)n && v[1] == (unsigned)(n >> 32)), 1)) break;
    if (spins) __builtin_amdgcn_s_sleep(8);
    int z = 0;
    asm volatile("" : "+v"(z));
    v = __builtin_amdgcn_raw_buffer_load_b64(rs, z, OFF_ACK + (int)BIDX * 8, AUX_SC1);
  }
  return spins == 0;
}
__device__ __forceinline__ void cnt_inc(char* ws, int cnt) {
  if (olane() == 0) __hip_atomic_fetch_add((gu32*)(ws + cnt), 1u, RLX_AGENT);
}
template <int SLP>
__device__ __forceinline__ void hs_lead(__amdgpu_buffer_rsrc_t rs, bool fill, int cnt, unsigned want) {
  const int l16 = olane() * 16;
  if (fill) {
    for (int o = 0; o < (int)ARM_HOST; o += 1024) __builtin_amdgcn_raw_buffer_store_b128(v4u{~0u, ~0u, ~0u, ~0u}, rs, l16, o, AUX_SC1);
    VMCNT(0);
  }
  _Pragma("clang loop unroll(disable)") for (unsigned spins = 0; spins < (kSpinCap << 2); ++spins) {
    int va = l16;
    asm volatile("" : "+v"(va));
    const v4u a = __builtin_amdgcn_raw_buffer_load_b128(rs, va, OFF_REQ, AUX_SC1);
    const v4u b = __builtin_amdgcn_raw_buffer_load_b128(rs, va + 1024, OFF_REQ, AUX_SC1);
    const unsigned n = __builtin_amdgcn_raw_buffer_load_b32(rs, va & 0, cnt, AUX_SC1);
    if (__all(n + 1u >= want)) break;
    __builtin_amdgcn_raw_buffer_store_b128(a, rs, va, OFF_ACK, AUX_SC1);
    __builtin_amdgcn_raw_buffer_store_b128(b, rs, va + 1024, OFF_ACK, AUX_SC1);
    __builtin_amdgcn_s_sleep(SLP);
  }
}

#define PTW(i, wv)
#define PLDECL
#define PL(i)
#define PLDUMP(wv, mask)
#define PT(i) PTW(i, 0)
#define EVDECL
#define EV(k)
#define EVDUMP(s)
#define EVCLK(s, c)
#define EVOUT
#define VO_A \
  "s_branch .Lgo_%=\n" \
  ".Lrs_%=:\n" \
  "s_mov_b32 %[fs], -1\n" \
  "s_nop 15\n" \
  "s_nop 15\n" \
  ".Lrt_%=:\n" \
  "s_setprio 0\n" \
  "s_waitcnt vmcnt(0) lgkmcnt(0)\n" \
  "ds_read_b128 v[200:203], v243 offset:32768\n" \
  "ds_read_b128 v[204:207], v243 offset:33792\n" \
  "ds_read_b128 v[208:211], v243 offset:49152\n" \
  "ds_read_b128 v[212:215], v243 offset:50176\n" \
  "v_mov_b64 v[192:193], 0\n" \
  "v_mov_b64 v[194:195], 0\n" \
  "v_mov_b64 v[196:197], 0\n" \
  "v_mov_b64 v[198:199], 0\n" \
  "s_barrier\n" \
  "ds_read_b128 v[216:219], v251 offset:0\n" \
  "ds_read_b128 v[220:223], v251 offset:1024\n" \
  "ds_read_b128 v[224:227], v251 offset:2048\n" \
  "ds_read_b128 v[228:231], v251 offset:3072\n" \
  "s_waitcnt lgkmcnt(0)\n" \
  "s_barrier\n" \
  "s_branch .Ll1_%=\n" \
  ".p2align 6\n" \
  ".Lgo_%=:\n" \
  "ds_read_b128 v[200:203], v243 offset:32768\n" \
  "ds_read_b128 v[204:207], v243 offset:33792\n" \
  "ds_read_b128 v[208:211], v243 offset:49152\n" \
  "ds_read_b128 v[212:215], v243 offset:50176\n" \
  "v_mov_b64 v[192:193], 0\n" \
  "v_mov_b64 v[194:195], 0\n" \
  "v_mov_b64 v[196:197], 0\n" \
  "v_mov_b64 v[198:199], 0\n" \
  "s_mov_b32 %[keep], m0\n" \
  "v_readfirstlane_b32 %[cnt], v250\n" \
  "s_mov_b32 m0, %[cnt]\n" \
  "s_waitcnt lgkmcnt(0)\n" \
  "s_barrier\n" \
  "buffer_load_dwordx4 v251, %[rs], %[o0] offen sc1 lds\n" \
  "s_waitcnt vmcnt(0)\n" \
  "s_barrier\n" \
  "s_mov_b32 m0, %[keep]\n" \
  "ds_read_b128 v[216:219], v251 offset:0\n" \
  "ds_read_b128 v[220:223], v251 offset:1024\n" \
  "ds_read_b128 v[224:227], v251 offset:2048\n" \
  "ds_read_b128 v[228:231], v251 offset:3072\n" \
  "s_barrier\n" \
  ".Ll1_%=:\n" \
  "s_waitcnt lgkmcnt(3)\n" \
  "v_smfmac_f32_16x16x64_bf16 v[192:195], v[216:219], v[200:207], v248\n" \
  "v_smfmac_f32_16x16x64_bf16 v[196:199], v[216:219], v[208:215], v248\n" \
  "ds_read_b128 v[232:235], v251 offset:4096\n" \
  "ds_read_b128 v[236:239], v251 offset:5120\n" \
  "ds_read_b128 v[216:219], v251 offset:6144\n" \
  "s_waitcnt lgkmcnt(5)\n" \
  "v_smfmac_f32_16x16x64_bf16 v[192:195], v[220:223], v[0:7], v248\n" \
  "v_smfmac_f32_16x16x64_bf16 v[196:199], v[220:223], v[8:15], v248\n" \
  "ds_read_b128 v[220:223], v251 offset:7168\n" \
  "s_barrier\n" \
  "s_waitcnt lgkmcnt(5)\n" \
  "v_smfmac_f32_16x16x64_bf16 v[192:195], v[224:227], v[16:23], v248\n" \
  "v_smfmac_f32_16x16x64_bf16 v[196:199], v[224:227], v[24:31], v248\n" \
  "s_waitcnt lgkmcnt(4)\n" \
  "v_smfmac_f32_16x16x64_bf16 v[192:195], v[228:231], v[32:39], v248\n" \
  "v_smfmac_f32_16x16x64_bf16 v[196:199], v[228:231], v[40:47], v248\n" \
  "s_waitcnt lgkmcnt(3)\n" \
  "v_smfmac_f32_16x16x64_bf16 v[192:195], v[232:235], v[48:55], v248\n" \
  "v_smfmac_f32_16x16x64_bf16 v[196:199], v[232:235], v[56:63], v248\n" \
  "s_waitcnt lgkmcnt(2)\n" \
  "v_smfmac_f32_16x16x64_bf16 v[192:195], v[236:239], v[64:71], v248\n" \
  "v_smfmac_f32_16x16x64_bf16 v[196:199], v[236:239], v[72:79], v248\n" \
  "s_waitcnt lgkmcnt(1)\n" \
  "v_smfmac_f32_16x16x64_bf16 v[192:195], v[216:219], v[80:87], v248\n" \
  "s_waitcnt lgkmcnt(0)\n" \
  "v_smfmac_f32_16x16x64_bf16 v[192:195], v[220:223], v[96:103], v248\n" \
  "v_smfmac_f32_16x16x64_bf16 v[196:199], v[216:219], v[88:95], v248\n" \
  "v_smfmac_f32_16x16x64_bf16 v[196:199], v[220:223], v[104:111], v248\n" \
  "s_setprio 3\n" \
  "v_add_f32 v192, v192, v194\n" \
  "v_add_f32 v193, v193, v195\n" \
  "s_nop 5\n" \
  "v_add_f32 v194, v196, v198\n" \
  "v_add_f32 v195, v197, v199\n" \
  "ds_write_b128 v250, v[192:195] offset:24576\n" \
  "v_mov_b64 v[200:201], 0\n" \
  "v_mov_b64 v[202:203], 0\n" \
  "v_mov_b64 v[204:205], 0\n" \
  "v_mov_b64 v[206:207], 0\n" \
  "v_mov_b64 v[208:209], 0\n" \
  "v_mov_b64 v[210:211], 0\n" \
  "v_mov_b64 v[212:213], 0\n" \
  "v_mov_b64 v[214:215], 0\n" \
  "s_waitcnt lgkmcnt(0)\n" \
  "s_setprio 0\n" \
  "s_barrier\n" \
  "s_nop 11\n" \
  "ds_read_b32 v241, v251 offset:23564\n" \
  "ds_read_b128 v[216:219], v249 offset:0\n" \
  "ds_read_b128 v[220:223], v249 offset:1024\n" \
  "ds_read_b128 v[224:227], v249 offset:16384\n" \
  "ds_read_b128 v[228:231], v249 offset:17408\n" \
  "s_waitcnt lgkmcnt(4)\n" \
  "v_cmp_eq_u32 vcc, %[fs], v241\n" \
  "s_cbranch_vccnz .Lrs_%=\n" \
  "s_barrier\n" \
  "ds_read_b64_tr_b16 v[232:233], v245 offset:17920\n" \
  "ds_read_b64_tr_b16 v[234:235], v245 offset:18112\n" \
  "ds_read_b64_tr_b16 v[236:237], v245 offset:18688\n" \
  "ds_read_b64_tr_b16 v[238:239], v245 offset:18880\n" \
  "ds_read_b64_tr_b16 v[192:193], v245 offset:16384\n" \
  "ds_read_b64_tr_b16 v[194:195], v245 offset:16576\n" \
  "ds_read_b64_tr_b16 v[196:197], v245 offset:17152\n" \
  "ds_read_b64_tr_b16 v[198:199], v245 offset:17344\n" \
  "ds_read_b32 v241, v251 offset:23560\n" \
  "s_waitcnt lgkmcnt(7)\n" \
  "v_smfmac_f32_16x16x64_bf16 v[200:203], v[232:235], v[216:223], v248 abid:1\n" \
  "ds_read_b128 v[216:219], v249 offset:32768\n" \
  "ds_read_b128 v[220:223], v249 offset:33792\n" \
  "v_smfmac_f32_16x16x64_bf16 v[204:207], v[232:235], v[128:135], v248 abid:1\n" \
  "s_waitcnt lgkmcnt(7)\n" \
  "v_smfmac_f32_16x16x64_bf16 v[200:203], v[236:239], v[224:231], v248 abid:1\n" \
  "ds_read_b128 v[224:227], v249 offset:49152\n" \
  "ds_read_b128 v[228:231], v249 offset:50176\n" \
  "v_smfmac_f32_16x16x64_bf16 v[204:207], v[236:239], v[136:143], v248 abid:1\n" \
  "s_waitcnt lgkmcnt(7)\n" \
  "v_smfmac_f32_16x16x64_bf16 v[200:203], v[192:195], v[176:183], v248 abid:1\n" \
  "v_smfmac_f32_16x16x64_bf16 v[204:207], v[192:195], v[112:119], v248 abid:1\n" \
  "s_waitcnt lgkmcnt(5)\n" \
  "v_smfmac_f32_16x16x64_bf16 v[200:203], v[196:199], v[184:191], v248 abid:1\n" \
  "s_waitcnt lgkmcnt(4)\n" \
  "v_cmp_eq_u32 vcc, %[fs], v241\n" \
  "v_smfmac_f32_16x16x64_bf16 v[204:207], v[196:199], v[120:127], v248 abid:1\n" \
  "s_waitcnt lgkmcnt(2)\n" \
  "v_smfmac_f32_16x16x64_bf16 v[208:211], v[192:195], v[216:223], v248 abid:1\n" \
  "ds_read_b128 v[216:219], v243 offset:0\n" \
  "ds_read_b128 v[220:223], v243 offset:1024\n" \
  "v_smfmac_f32_16x16x64_bf16 v[212:215], v[192:195], v[144:151], v248 abid:1\n" \
  "s_waitcnt lgkmcnt(2)\n" \
  "v_smfmac_f32_16x16x64_bf16 v[208:211], v[196:199], v[224:231], v248 abid:1\n" \
  "ds_read_b128 v[224:227], v243 offset:16384\n" \
  "ds_read_b128 v[228:231], v243 offset:17408\n" \
  "v_cvt_pk_bf16_f32 v200, v200, v201\n" \
  "v_smfmac_f32_16x16x64_bf16 v[212:215], v[196:199], v[152:159], v248 abid:1\n" \
  "v_cvt_pk_bf16_f32 v201, v202, v203\n" \
  "v_smfmac_f32_16x16x64_bf16 v[212:215], v[232:235], v[160:167], v248 abid:1\n" \
  "v_cvt_pk_bf16_f32 v202, v204, v205\n" \
  "v_smfmac_f32_16x16x64_bf16 v[212:215], v[236:239], v[168:175], v248 abid:1\n" \
  "v_cvt_pk_bf16_f32 v203, v206, v207\n" \
  "s_cbranch_vccnz .Lrs_%=\n" \
  "s_waitcnt lgkmcnt(2)\n" \
  "v_smfmac_f32_16x16x64_bf16 v[208:211], v[232:235], v[216:223], v248 abid:1\n" \
  "v_permlane16_swap_b32 v200, v202\n" \
  "s_waitcnt lgkmcnt(0)\n" \
  "v_smfmac_f32_16x16x64_bf16 v[208:211], v[236:239], v[224:231], v248 abid:1\n" \
  "v_permlane16_swap_b32 v201, v203\n" \
  "buffer_store_dwordx4 v[200:203], v247, %[rs], %[vs] offen sc1\n" \
  "v_cvt_pk_bf16_f32 v212, v212, v213\n" \
  "v_cvt_pk_bf16_f32 v213, v214, v215\n" \
  "s_nop 4\n" \
  "v_cvt_pk_bf16_f32 v211, v210, v211\n" \
  "v_cvt_pk_bf16_f32 v210, v208, v209\n" \
  "s_nop 0\n" \
  "v_permlane16_swap_b32 v211, v213\n" \
  "v_permlane16_swap_b32 v210, v212\n" \
  "buffer_store_dwordx4 v[210:213], v246, %[rs], %[vs] offen sc1\n" \
  "s_nop 1\n"
#define VO_B \
  "s_branch .Lgo_%=\n" \
  ".Lsf_%=:\n" \
  "s_cmp_eq_u32 %[fs], -1\n" \
  "s_cbranch_scc1 .Lsfb_%=\n" \
  "v_mov_b32 v241, %[fs]\n" \
  "ds_write_b32 v251, v241 offset:23564\n" \
  "s_branch .Lrt_%=\n" \
  ".Lrs_%=:\n" \
  "s_mov_b32 %[fs], -1\n" \
  "s_nop 15\n" \
  "s_nop 15\n" \
  ".Lrt_%=:\n" \
  "s_setprio 0\n" \
  "s_waitcnt vmcnt(0) lgkmcnt(0)\n" \
  "buffer_load_dwordx4 v[216:219], v251, %[rs], %[o3] offen sc1\n" \
  "buffer_load_dwordx4 v[220:223], v251, %[rs], %[o0] offen sc1\n" \
  "buffer_load_dwordx4 v[224:227], v251, %[rs], %[o1] offen sc1\n" \
  "buffer_load_dwordx4 v[228:231], v251, %[rs], %[o2] offen sc1\n" \
  "v_subrev_u32 v232, 0x1000, v250\n" \
  "s_mov_b32 %[cnt], -2\n" \
  "ds_read_b128 v[200:203], v232 offset:0\n" \
  "ds_read_b128 v[204:207], v232 offset:4096\n" \
  "ds_read_b128 v[208:211], v232 offset:8192\n" \
  "ds_read_b128 v[212:215], v232 offset:12288\n" \
  "s_waitcnt lgkmcnt(0)\n" \
  "v_max_u32 v241, v200, v201\n" \
  "v_max3_u32 v241, v241, v202, v203\n" \
  "v_max3_u32 v241, v241, v204, v205\n" \
  "v_max3_u32 v241, v241, v206, v207\n" \
  "v_max3_u32 v241, v241, v208, v209\n" \
  "v_max3_u32 v241, v241, v210, v211\n" \
  "v_max3_u32 v241, v241, v212, v213\n" \
  "v_max3_u32 v241, v241, v214, v215\n" \
  "v_cmp_eq_u32 vcc, -1, v241\n" \
  "s_cbranch_vccz .Lrd_%=\n" \
  "s_mov_b32 %[cnt], 0x40000\n" \
  ".Lrr_%=:\n" \
  "s_waitcnt vmcnt(0)\n" \
  "v_max_u32 v241, v216, v217\n" \
  "v_max3_u32 v241, v241, v218, v219\n" \
  "v_max3_u32 v241, v241, v220, v221\n" \
  "v_max3_u32 v241, v241, v222, v223\n" \
  "v_max3_u32 v241, v241, v224, v225\n" \
  "v_max3_u32 v241, v241, v226, v227\n" \
  "v_max3_u32 v241, v241, v228, v229\n" \
  "v_max3_u32 v241, v241, v230, v231\n" \
  "v_cmp_eq_u32 vcc, -1, v241\n" \
  "s_cbranch_vccz .Lrd_%=\n" \
  "s_sleep 2\n" \
  "buffer_load_dwordx4 v[216:219], v251, %[rs], %[o3] offen sc1\n" \
  "buffer_load_dwordx4 v[220:223], v251, %[rs], %[o0] offen sc1\n" \
  "buffer_load_dwordx4 v[224:227], v251, %[rs], %[o1] offen sc1\n" \
  "buffer_load_dwordx4 v[228:231], v251, %[rs], %[o2] offen sc1\n" \
  "s_sub_u32 %[cnt], %[cnt], 1\n" \
  "s_cbranch_scc0 .Lrr_%=\n" \
  ".Lrd_%=:\n" \
  "ds_read_b128 v[200:203], v243 offset:32768\n" \
  "ds_read_b128 v[204:207], v243 offset:33792\n" \
  "ds_read_b128 v[208:211], v243 offset:49152\n" \
  "ds_read_b128 v[212:215], v243 offset:50176\n" \
  "v_mov_b64 v[192:193], 0\n" \
  "v_mov_b64 v[194:195], 0\n" \
  "v_mov_b64 v[196:197], 0\n" \
  "v_mov_b64 v[198:199], 0\n" \
  "s_cmp_eq_u32 %[fs], -1\n" \
  "s_cbranch_scc1 .Lre_%=\n" \
  "s_mov_b32 %[fs], -1\n" \
  "s_barrier\n" \
  ".Lre_%=:\n" \
  "s_waitcnt vmcnt(0)\n" \
  "s_cmp_eq_u32 %[cnt], -2\n" \
  "s_cbranch_scc1 .Lrf_%=\n" \
  "ds_write_b128 v232, v[216:219] offset:0\n" \
  "ds_write_b128 v232, v[220:223] offset:4096\n" \
  "ds_write_b128 v232, v[224:227] offset:8192\n" \
  "ds_write_b128 v232, v[228:231] offset:12288\n" \
  ".Lrf_%=:\n" \
  "v_mov_b32 v241, 0\n" \
  "s_waitcnt lgkmcnt(0)\n" \
  "s_barrier\n" \
  "s_waitcnt lgkmcnt(0)\n" \
  "s_barrier\n" \
  "s_branch .Ll1_%=\n" \
  ".Lnf_%=:\n" \
  "s_cmp_eq_u32 %[fs], -1\n" \
  "s_cbranch_scc1 .Lnfb_%=\n" \
  "v_mov_b32 v241, 0\n" \
  "ds_read_b128 v[216:219], v251 offset:0\n" \
  "s_waitcnt lgkmcnt(0)\n" \
  "v_max3_u32 v241, v241, v216, v217\n" \
  "v_max3_u32 v241, v241, v218, v219\n" \
  "ds_read_b128 v[216:219], v251 offset:1024\n" \
  "s_waitcnt lgkmcnt(0)\n" \
  "v_max3_u32 v241, v241, v216, v217\n" \
  "v_max3_u32 v241, v241, v218, v219\n" \
  "ds_read_b128 v[216:219], v251 offset:2048\n" \
  "s_waitcnt lgkmcnt(0)\n" \
  "v_max3_u32 v241, v241, v216, v217\n" \
  "v_max3_u32 v241, v241, v218, v219\n" \
  "ds_read_b128 v[216:219], v251 offset:3072\n" \
  "s_waitcnt lgkmcnt(0)\n" \
  "v_max3_u32 v241, v241, v216, v217\n" \
  "v_max3_u32 v241, v241, v218, v219\n" \
  "ds_read_b128 v[216:219], v251 offset:4096\n" \
  "s_waitcnt lgkmcnt(0)\n" \
  "v_max3_u32 v241, v241, v216, v217\n" \
  "v_max3_u32 v241, v241, v218, v219\n" \
  "ds_read_b128 v[216:219], v251 offset:5120\n" \
  "s_waitcnt lgkmcnt(0)\n" \
  "v_max3_u32 v241, v241, v216, v217\n" \
  "v_max3_u32 v241, v241, v218, v219\n" \
  "ds_read_b128 v[216:219], v251 offset:6144\n" \
  "s_waitcnt lgkmcnt(0)\n" \
  "v_max3_u32 v241, v241, v216, v217\n" \
  "v_max3_u32 v241, v241, v218, v219\n" \
  "ds_read_b128 v[216:219], v251 offset:7168\n" \
  "s_waitcnt lgkmcnt(0)\n" \
  "v_max3_u32 v241, v241, v216, v217\n" \
  "v_max3_u32 v241, v241, v218, v219\n" \
  "ds_read_b128 v[216:219], v251 offset:8192\n" \
  "s_waitcnt lgkmcnt(0)\n" \
  "v_max3_u32 v241, v241, v216, v217\n" \
  "v_max3_u32 v241, v241, v218, v219\n" \
  "ds_read_b128 v[216:219], v251 offset:9216\n" \
  "s_waitcnt lgkmcnt(0)\n" \
  "v_max3_u32 v241, v241, v216, v217\n" \
  "v_max3_u32 v241, v241, v218, v219\n" \
  "ds_read_b128 v[216:219], v251 offset:10240\n" \
  "s_waitcnt lgkmcnt(0)\n" \
  "v_max3_u32 v241, v241, v216, v217\n" \
  "v_max3_u32 v241, v241, v218, v219\n" \
  "ds_read_b128 v[216:219], v251 offset:11264\n" \
  "s_waitcnt lgkmcnt(0)\n" \
  "v_max3_u32 v241, v241, v216, v217\n" \
  "v_max3_u32 v241, v241, v218, v219\n" \
  "ds_read_b128 v[216:219], v251 offset:12288\n" \
  "s_waitcnt lgkmcnt(0)\n" \
  "v_max3_u32 v241, v241, v216, v217\n" \
  "v_max3_u32 v241, v241, v218, v219\n" \
  "ds_read_b128 v[216:219], v251 offset:13312\n" \
  "s_waitcnt lgkmcnt(0)\n" \
  "v_max3_u32 v241, v241, v216, v217\n" \
  "v_max3_u32 v241, v241, v218, v219\n" \
  "ds_read_b128 v[216:219], v251 offset:14336\n" \
  "s_waitcnt lgkmcnt(0)\n" \
  "v_max3_u32 v241, v241, v216, v217\n" \
  "v_max3_u32 v241, v241, v218, v219\n" \
  "ds_read_b128 v[216:219], v251 offset:15360\n" \
  "s_waitcnt lgkmcnt(0)\n" \
  "v_max3_u32 v241, v241, v216, v217\n" \
  "v_max3_u32 v241, v241, v218, v219\n" \
  "v_cmp_eq_u32 vcc, -1, v241\n" \
  "s_cbranch_vccz .Lnfb_%=\n" \
  "v_mov_b32 v241, %[fs]\n" \
  "ds_write_b32 v251, v241 offset:23560\n" \
  "s_waitcnt lgkmcnt(0)\n" \
  "s_mov_b32 %[fs], -1\n" \
  "s_barrier\n" \
  "s_branch .Lrt_%=\n" \
  ".p2align 6\n" \
  ".Lgo_%=:\n" \
  "ds_read_b128 v[200:203], v243 offset:32768\n" \
  "ds_read_b128 v[204:207], v243 offset:33792\n" \
  "ds_read_b128 v[208:211], v243 offset:49152\n" \
  "ds_read_b128 v[212:215], v243 offset:50176\n" \
  "v_mov_b64 v[192:193], 0\n" \
  "v_mov_b64 v[194:195], 0\n" \
  "v_mov_b64 v[196:197], 0\n" \
  "v_mov_b64 v[198:199], 0\n" \
  "s_mov_b32 %[keep], m0\n" \
  "v_readfirstlane_b32 %[cnt], v250\n" \
  "s_mov_b32 m0, %[cnt]\n" \
  "v_bfe_u32 v241, v251, 2, 7\n" \
  "v_lshl_add_u32 v241, v251, 4, v241\n" \
  "s_waitcnt lgkmcnt(0)\n" \
  "s_cmp_eq_u32 %[tw], 0\n" \
  "s_cbranch_scc1 .Ltq_%=\n" \
  ".Ltl_%=:\n" \
  "s_memtime vcc\n" \
  "s_waitcnt lgkmcnt(0)\n" \
  "s_sub_i32 %[cnt], %[tg], vcc_lo\n" \
  "s_cmp_lt_i32 %[cnt], 260\n" \
  "s_cbranch_scc1 .Ltf_%=\n" \
  "s_sleep 1\n" \
  "s_branch .Ltl_%=\n" \
  ".Ltf_%=:\n" \
  "s_max_i32 %[cnt], %[cnt], 0\n" \
  ".p2align 3\n" \
  ".Ltg_%=:\n" \
  "s_sub_u32 %[cnt], %[cnt], 28\n" \
  "s_cbranch_scc0 .Ltg_%=\n" \
  ".Ltq_%=:\n" \
  "s_barrier\n" \
  "buffer_load_dwordx4 v251, %[rs], %[o0] offen sc1 lds\n" \
  "s_add_u32 m0, m0, 0x1000\n" \
  "s_nop 4\n" \
  "buffer_load_dwordx4 v251, %[rs], %[o1] offen sc1 lds\n" \
  "s_add_u32 m0, m0, 0x1000\n" \
  "s_nop 4\n" \
  "buffer_load_dwordx4 v251, %[rs], %[o2] offen sc1 lds\n" \
  "s_barrier\n" \
  "s_waitcnt vmcnt(2)\n" \
  "s_barrier\n" \
  "s_mov_b32 m0, %[keep]\n" \
  ".Ll1_%=:\n" \
  "s_memtime vcc\n" \
  "s_setprio 3\n" \
  "s_waitcnt vmcnt(0)\n" \
  "s_waitcnt lgkmcnt(0)\n" \
  "s_barrier\n" \
  "ds_read2_b32 v[236:237], v241 offset1:32\n" \
  "ds_read_b128 v[216:219], v251 offset:8192\n" \
  "ds_read_b128 v[220:223], v251 offset:9216\n" \
  "ds_read_b128 v[224:227], v251 offset:10240\n" \
  "ds_read_b128 v[228:231], v251 offset:11264\n" \
  "ds_read_b128 v[232:235], v251 offset:12288\n" \
  "s_sub_u32 %[cnt], vcc_lo, %[ckm]\n" \
  "s_mul_hi_u32 %[cnt], %[cnt], %[inv]\n" \
  "s_add_u32 %[cnt], %[cnt], %[r16]\n" \
  "s_and_b32 %[cnt], %[cnt], -2\n" \
  "s_waitcnt lgkmcnt(5)\n" \
  "v_max_u32 v241, v236, v237\n" \
  "ds_read_b128 v[236:239], v251 offset:13312\n" \
  "v_cmp_eq_u32 vcc, -1, v241\n" \
  "s_cbranch_vccnz .Lsf_%=\n" \
  ".Lsfb_%=:\n" \
  "v_mov_b32 v241, %[cnt]\n" \
  "buffer_store_dword v241, v242, %[rs], %[sst] offen sc1\n" \
  "s_setprio 0\n" \
  "s_waitcnt lgkmcnt(5)\n" \
  "v_smfmac_f32_16x16x64_bf16 v[192:195], v[216:219], v[200:207], v248\n" \
  "v_smfmac_f32_16x16x64_bf16 v[196:199], v[216:219], v[208:215], v248\n" \
  "s_waitcnt lgkmcnt(4)\n" \
  "v_smfmac_f32_16x16x64_bf16 v[192:195], v[220:223], v[0:7], v248\n" \
  "v_smfmac_f32_16x16x64_bf16 v[196:199], v[220:223], v[8:15], v248\n" \
  "ds_read_b128 v[216:219], v251 offset:14336\n" \
  "ds_read_b128 v[220:223], v251 offset:15360\n" \
  "s_waitcnt lgkmcnt(5)\n" \
  "v_smfmac_f32_16x16x64_bf16 v[192:195], v[224:227], v[16:23], v248\n" \
  "v_smfmac_f32_16x16x64_bf16 v[196:199], v[224:227], v[24:31], v248\n" \
  "s_waitcnt lgkmcnt(4)\n" \
  "v_smfmac_f32_16x16x64_bf16 v[192:195], v[228:231], v[32:39], v248\n" \
  "v_smfmac_f32_16x16x64_bf16 v[196:199], v[228:231], v[40:47], v248\n" \
  "s_waitcnt lgkmcnt(3)\n" \
  "v_smfmac_f32_16x16x64_bf16 v[192:195], v[232:235], v[48:55], v248\n" \
  "v_smfmac_f32_16x16x64_bf16 v[196:199], v[232:235], v[56:63], v248\n" \
  "s_waitcnt lgkmcnt(2)\n" \
  "v_smfmac_f32_16x16x64_bf16 v[192:195], v[236:239], v[64:71], v248\n" \
  "v_smfmac_f32_16x16x64_bf16 v[196:199], v[236:239], v[72:79], v248\n" \
  "s_barrier\n" \
  "ds_read_b128 v[224:227], v250 offset:20480\n" \
  "s_waitcnt lgkmcnt(2)\n" \
  "v_smfmac_f32_16x16x64_bf16 v[192:195], v[216:219], v[80:87], v248\n" \
  "s_waitcnt lgkmcnt(1)\n" \
  "v_smfmac_f32_16x16x64_bf16 v[192:195], v[220:223], v[96:103], v248\n" \
  "v_smfmac_f32_16x16x64_bf16 v[196:199], v[216:219], v[88:95], v248\n" \
  "v_smfmac_f32_16x16x64_bf16 v[196:199], v[220:223], v[104:111], v248\n" \
  "s_nop 0\n" \
  "v_add_f32 v192, v192, v194\n" \
  "v_add_f32 v193, v193, v195\n" \
  "s_waitcnt lgkmcnt(0)\n" \
  "v_add_f32 v192, v192, v224\n" \
  "v_add_f32 v193, v193, v225\n" \
  "v_cvt_pk_bf16_f32 v194, v192, v193\n" \
  "s_nop 1\n" \
  "v_pk_add_f32 v[196:197], v[196:197], v[198:199]\n" \
  "v_pk_add_f32 v[196:197], v[196:197], v[226:227]\n" \
  "v_cvt_pk_bf16_f32 v198, v196, v197\n" \
  "ds_write2_b32 v244, v194, v198 offset1:96\n" \
  "v_cmp_u_f32 vcc, v192, v193\n" \
  "s_cbranch_vccnz .Lnf_%=\n" \
  ".Lnfb_%=:\n" \
  "s_waitcnt lgkmcnt(0)\n" \
  "s_barrier\n" \
  "v_mov_b64 v[200:201], 0\n" \
  "v_mov_b64 v[202:203], 0\n" \
  "v_mov_b64 v[204:205], 0\n" \
  "v_mov_b64 v[206:207], 0\n" \
  "v_mov_b64 v[208:209], 0\n" \
  "v_mov_b64 v[210:211], 0\n" \
  "v_mov_b64 v[212:213], 0\n" \
  "v_mov_b64 v[214:215], 0\n" \
  "ds_read_b64_tr_b16 v[232:233], v245 offset:17920\n" \
  "ds_read_b64_tr_b16 v[234:235], v245 offset:18112\n" \
  "ds_read_b64_tr_b16 v[236:237], v245 offset:18688\n" \
  "ds_read_b64_tr_b16 v[238:239], v245 offset:18880\n" \
  "ds_read_b64_tr_b16 v[192:193], v245 offset:16384\n" \
  "ds_read_b64_tr_b16 v[194:195], v245 offset:16576\n" \
  "ds_read_b64_tr_b16 v[196:197], v245 offset:17152\n" \
  "ds_read_b64_tr_b16 v[198:199], v245 offset:17344\n" \
  "ds_read_b128 v[216:219], v249 offset:0\n" \
  "ds_read_b128 v[220:223], v249 offset:1024\n" \
  "ds_read_b128 v[224:227], v249 offset:16384\n" \
  "ds_read_b128 v[228:231], v249 offset:17408\n" \
  "buffer_load_dwordx2 v[240:241], v240, %[rs], %[ss] offen sc1\n" \
  "s_waitcnt lgkmcnt(2)\n" \
  "v_smfmac_f32_16x16x64_bf16 v[200:203], v[232:235], v[216:223], v248 abid:1\n" \
  "ds_read_b128 v[216:219], v249 offset:32768\n" \
  "ds_read_b128 v[220:223], v249 offset:33792\n" \
  "v_smfmac_f32_16x16x64_bf16 v[204:207], v[232:235], v[128:135], v248 abid:1\n" \
  "s_waitcnt lgkmcnt(2)\n" \
  "v_smfmac_f32_16x16x64_bf16 v[200:203], v[236:239], v[224:231], v248 abid:1\n" \
  "ds_read_b128 v[224:227], v249 offset:49152\n" \
  "ds_read_b128 v[228:231], v249 offset:50176\n" \
  "v_smfmac_f32_16x16x64_bf16 v[204:207], v[236:239], v[136:143], v248 abid:1\n" \
  "v_smfmac_f32_16x16x64_bf16 v[200:203], v[192:195], v[176:183], v248 abid:1\n" \
  "v_smfmac_f32_16x16x64_bf16 v[204:207], v[192:195], v[112:119], v248 abid:1\n" \
  "v_smfmac_f32_16x16x64_bf16 v[200:203], v[196:199], v[184:191], v248 abid:1\n" \
  "v_smfmac_f32_16x16x64_bf16 v[204:207], v[196:199], v[120:127], v248 abid:1\n" \
  "s_waitcnt lgkmcnt(2)\n" \
  "v_smfmac_f32_16x16x64_bf16 v[208:211], v[192:195], v[216:223], v248 abid:1\n" \
  "ds_read_b128 v[216:219], v243 offset:0\n" \
  "ds_read_b128 v[220:223], v243 offset:1024\n" \
  "v_smfmac_f32_16x16x64_bf16 v[212:215], v[192:195], v[144:151], v248 abid:1\n" \
  "s_waitcnt lgkmcnt(2)\n" \
  "v_smfmac_f32_16x16x64_bf16 v[208:211], v[196:199], v[224:231], v248 abid:1\n" \
  "ds_read_b128 v[224:227], v243 offset:16384\n" \
  "ds_read_b128 v[228:231], v243 offset:17408\n" \
  "v_cvt_pk_bf16_f32 v200, v200, v201\n" \
  "v_smfmac_f32_16x16x64_bf16 v[212:215], v[196:199], v[152:159], v248 abid:1\n" \
  "v_cvt_pk_bf16_f32 v201, v202, v203\n" \
  "v_smfmac_f32_16x16x64_bf16 v[212:215], v[232:235], v[160:167], v248 abid:1\n" \
  "v_cvt_pk_bf16_f32 v202, v204, v205\n" \
  "v_smfmac_f32_16x16x64_bf16 v[212:215], v[236:239], v[168:175], v248 abid:1\n" \
  "v_cvt_pk_bf16_f32 v203, v206, v207\n" \
  "s_waitcnt lgkmcnt(2)\n" \
  "v_smfmac_f32_16x16x64_bf16 v[208:211], v[232:235], v[216:223], v248 abid:1\n" \
  "v_permlane16_swap_b32 v200, v202\n" \
  "s_waitcnt lgkmcnt(0)\n" \
  "v_smfmac_f32_16x16x64_bf16 v[208:211], v[236:239], v[224:231], v248 abid:1\n" \
  "v_permlane16_swap_b32 v201, v203\n" \
  "buffer_store_dwordx4 v[200:203], v247, %[rs], %[vs] offen sc1\n" \
  "v_cvt_pk_bf16_f32 v212, v212, v213\n" \
  "v_cvt_pk_bf16_f32 v213, v214, v215\n" \
  "s_nop 4\n" \
  "v_cvt_pk_bf16_f32 v211, v210, v211\n" \
  "v_cvt_pk_bf16_f32 v210, v208, v209\n" \
  "s_nop 0\n" \
  "v_permlane16_swap_b32 v211, v213\n" \
  "v_permlane16_swap_b32 v210, v212\n" \
  "buffer_store_dwordx4 v[210:213], v246, %[rs], %[vs] offen sc1\n" \
  "s_nop 1\n" \
  "s_waitcnt vmcnt(2)\n"
#define QK_A \
  "s_branch .Lgo_%=\n" \
  ".Lrs_%=:\n" \
  "s_mov_b32 %[fs], -1\n" \
  "s_nop 15\n" \
  "s_nop 15\n" \
  ".Lrt_%=:\n" \
  "s_setprio 0\n" \
  "s_waitcnt vmcnt(0) lgkmcnt(0)\n" \
  "ds_read_b128 v[200:203], v249 offset:0\n" \
  "ds_read_b128 v[204:207], v249 offset:1024\n" \
  "ds_read_b128 v[208:211], v249 offset:16384\n" \
  "ds_read_b128 v[212:215], v249 offset:17408\n" \
  "v_mov_b32 v241, 0x7f800000\n" \
  "ds_write_b32 v250, v241 offset:22528\n" \
  "v_mov_b64 v[192:193], 0\n" \
  "v_mov_b64 v[194:195], 0\n" \
  "v_mov_b64 v[196:197], 0\n" \
  "v_mov_b64 v[198:199], 0\n" \
  "s_barrier\n" \
  "ds_read_b128 v[216:219], v251 offset:0\n" \
  "ds_read_b128 v[220:223], v251 offset:1024\n" \
  "ds_read_b128 v[224:227], v251 offset:2048\n" \
  "ds_read_b128 v[228:231], v251 offset:3072\n" \
  "s_waitcnt lgkmcnt(0)\n" \
  "s_barrier\n" \
  "s_branch .Ll1_%=\n" \
  ".Lnf_%=:\n" \
  "s_cmp_eq_u32 %[fs], -1\n" \
  "s_cbranch_scc1 .Lnfb_%=\n" \
  "v_mov_b32 v241, %[fs]\n" \
  "ds_write_b32 v251, v241 offset:23560\n" \
  "s_waitcnt lgkmcnt(0)\n" \
  "s_mov_b32 %[fs], -1\n" \
  "s_barrier\n" \
  "s_branch .Lrt_%=\n" \
  ".Lw1_%=:\n" \
  "ds_read_b32 v238, v251 offset:23560\n" \
  "s_waitcnt lgkmcnt(0)\n" \
  "v_cmp_eq_u32 vcc, %[fs], v238\n" \
  "s_cbranch_vccnz .Lrs_%=\n" \
  "v_xor_b32 v238, 0x1000, v242\n" \
  "ds_read_b128 v[216:219], v238 offset:0\n" \
  "ds_read_b128 v[228:231], v238 offset:1024\n" \
  "ds_read_b128 v[232:235], v238 offset:2048\n" \
  "ds_read_b128 v[200:203], v238 offset:3072\n" \
  "s_waitcnt lgkmcnt(0)\n" \
  "v_mfma_f32_16x16x32_bf16 v[216:219], v[216:219], v[192:195], 0\n" \
  "v_mfma_f32_16x16x32_bf16 v[216:219], v[228:231], v[196:199], v[216:219]\n" \
  "v_mfma_f32_16x16x32_bf16 v[216:219], v[232:235], v[208:211], v[216:219]\n" \
  "v_mfma_f32_16x16x32_bf16 v[216:219], v[200:203], v[212:215], v[216:219]\n" \
  "s_nop 9\n" \
  "v_cndmask_b32 v238, v216, v218, %[hf]\n" \
  "v_cndmask_b32 v239, v217, v219, %[hf]\n" \
  "v_max3_f32 v222, |v238|, |v239|, v221\n" \
  "v_cmp_nge_f32 vcc, 0x43aa0000, v222\n" \
  "s_cbranch_vccz .Lw1b_%=\n" \
  "s_mov_b64 %[wd], -1\n" \
  "v_max3_f32 v222, v204, v205, v238\n" \
  "v_max_f32 v222, v222, v239\n" \
  "s_nop 1\n" \
  "v_max_f32_dpp v222, v222, v222 row_ror:8 row_mask:0xf bank_mask:0xf\n" \
  "v_mov_b32 v221, v222\n" \
  "s_nop 1\n" \
  "v_permlane32_swap_b32 v222, v221\n" \
  "v_max_f32 v222, v222, v221\n" \
  "v_mov_b32 v221, v222\n" \
  "s_nop 1\n" \
  "v_permlane16_swap_b32 v222, v221\n" \
  "v_max_f32 v222, v222, v221\n" \
  "v_mul_f32 v221, 0xbe0293ee, v222\n" \
  "v_fmamk_f32 v236, v204, 0x3e0293ee, v221\n" \
  "v_fmamk_f32 v237, v205, 0x3e0293ee, v221\n" \
  "v_exp_f32 v236, v236\n" \
  "v_exp_f32 v237, v237\n" \
  "s_nop 1\n" \
  "v_cvt_pk_bf16_f32 v220, v236, v237\n" \
  "v_and_b32 v220, 0x7fffffff, v220\n" \
  "s_branch .Lw1b_%=\n" \
  ".Lw2_%=:\n" \
  "v_max3_f32 v206, v204, v205, v222\n" \
  "s_nop 1\n" \
  "v_max_f32_dpp v206, v206, v206 row_ror:8 row_mask:0xf bank_mask:0xf\n" \
  "v_mov_b32 v221, v206\n" \
  "s_nop 1\n" \
  "v_permlane32_swap_b32 v206, v221\n" \
  "v_max_f32 v206, v206, v221\n" \
  "v_mov_b32 v221, v206\n" \
  "s_nop 1\n" \
  "v_permlane16_swap_b32 v206, v221\n" \
  "v_max_f32 v206, v206, v221\n" \
  "v_mul_f32 v221, 0xbe0293ee, v206\n" \
  "v_fmamk_f32 v236, v204, 0x3e0293ee, v221\n" \
  "v_fmamk_f32 v237, v205, 0x3e0293ee, v221\n" \
  "v_fmamk_f32 v239, v222, 0x3e0293ee, v221\n" \
  "v_exp_f32 v236, v236\n" \
  "v_exp_f32 v237, v237\n" \
  "v_exp_f32 v239, v239\n" \
  "s_nop 1\n" \
  "v_fma_f32 v238, v223, v239, v236\n" \
  "v_add_f32 v238, v238, v237\n" \
  "s_nop 1\n" \
  "s_branch .Lw2b_%=\n" \
  ".p2align 6\n" \
  ".Lgo_%=:\n" \
  "ds_read_b128 v[200:203], v249 offset:0\n" \
  "ds_read_b128 v[204:207], v249 offset:1024\n" \
  "ds_read_b128 v[208:211], v249 offset:16384\n" \
  "ds_read_b128 v[212:215], v249 offset:17408\n" \
  "v_mov_b32 v241, 0x7f800000\n" \
  "ds_write_b32 v250, v241 offset:22528\n" \
  "v_mov_b64 v[192:193], 0\n" \
  "v_mov_b64 v[194:195], 0\n" \
  "v_mov_b64 v[196:197], 0\n" \
  "v_mov_b64 v[198:199], 0\n" \
  "s_mov_b32 %[keep], m0\n" \
  "v_readfirstlane_b32 %[cnt], v250\n" \
  "s_mov_b32 m0, %[cnt]\n" \
  "s_waitcnt lgkmcnt(0)\n" \
  "s_barrier\n" \
  "buffer_load_dwordx4 v251, %[rs], %[o0] offen sc1 lds\n" \
  "s_waitcnt vmcnt(0)\n" \
  "s_barrier\n" \
  "s_mov_b32 m0, %[keep]\n" \
  "ds_read_b128 v[216:219], v251 offset:0\n" \
  "ds_read_b128 v[220:223], v251 offset:1024\n" \
  "ds_read_b128 v[224:227], v251 offset:2048\n" \
  "ds_read_b128 v[228:231], v251 offset:3072\n" \
  "s_barrier\n" \
  ".Ll1_%=:\n" \
  "s_waitcnt lgkmcnt(3)\n" \
  "v_smfmac_f32_16x16x64_bf16 v[192:195], v[216:219], v[200:207], v248\n" \
  "ds_read_b128 v[200:203], v249 offset:32768\n" \
  "ds_read_b128 v[204:207], v249 offset:33792\n" \
  "s_waitcnt lgkmcnt(4)\n" \
  "v_smfmac_f32_16x16x64_bf16 v[192:195], v[220:223], v[0:7], v248\n" \
  "ds_read_b128 v[232:235], v251 offset:4096\n" \
  "v_smfmac_f32_16x16x64_bf16 v[196:199], v[216:219], v[128:135], v248\n" \
  "ds_read_b128 v[216:219], v251 offset:5120\n" \
  "s_waitcnt lgkmcnt(5)\n" \
  "v_smfmac_f32_16x16x64_bf16 v[192:195], v[224:227], v[208:215], v248\n" \
  "ds_read_b128 v[208:211], v249 offset:49152\n" \
  "v_smfmac_f32_16x16x64_bf16 v[196:199], v[220:223], v[64:71], v248\n" \
  "ds_read_b128 v[212:215], v249 offset:50176\n" \
  "s_waitcnt lgkmcnt(6)\n" \
  "v_smfmac_f32_16x16x64_bf16 v[192:195], v[228:231], v[8:15], v248\n" \
  "ds_read_b128 v[220:223], v251 offset:6144\n" \
  "v_smfmac_f32_16x16x64_bf16 v[196:199], v[224:227], v[136:143], v248\n" \
  "ds_read_b128 v[224:227], v251 offset:7168\n" \
  "s_waitcnt lgkmcnt(5)\n" \
  "v_smfmac_f32_16x16x64_bf16 v[192:195], v[232:235], v[200:207], v248\n" \
  "ds_read_b128 v[200:203], v243 offset:0\n" \
  "s_barrier\n" \
  "v_smfmac_f32_16x16x64_bf16 v[196:199], v[228:231], v[72:79], v248\n" \
  "ds_read_b128 v[204:207], v243 offset:1024\n" \
  "s_waitcnt lgkmcnt(6)\n" \
  "v_smfmac_f32_16x16x64_bf16 v[192:195], v[216:219], v[16:23], v248\n" \
  "ds_read_b128 v[228:231], v251 offset:8192\n" \
  "v_smfmac_f32_16x16x64_bf16 v[196:199], v[232:235], v[144:151], v248\n" \
  "ds_read_b128 v[232:235], v251 offset:9216\n" \
  "s_waitcnt lgkmcnt(5)\n" \
  "v_smfmac_f32_16x16x64_bf16 v[192:195], v[220:223], v[208:215], v248\n" \
  "ds_read_b128 v[208:211], v243 offset:16384\n" \
  "v_smfmac_f32_16x16x64_bf16 v[196:199], v[216:219], v[80:87], v248\n" \
  "ds_read_b128 v[212:215], v243 offset:17408\n" \
  "s_waitcnt lgkmcnt(6)\n" \
  "v_smfmac_f32_16x16x64_bf16 v[192:195], v[224:227], v[24:31], v248\n" \
  "ds_read_b128 v[216:219], v251 offset:10240\n" \
  "v_smfmac_f32_16x16x64_bf16 v[196:199], v[220:223], v[152:159], v248\n" \
  "ds_read_b128 v[220:223], v251 offset:11264\n" \
  "s_waitcnt lgkmcnt(5)\n" \
  "v_smfmac_f32_16x16x64_bf16 v[192:195], v[228:231], v[200:207], v248\n" \
  "ds_read_b128 v[200:203], v243 offset:32768\n" \
  "v_smfmac_f32_16x16x64_bf16 v[196:199], v[224:227], v[88:95], v248\n" \
  "ds_read_b128 v[204:207], v243 offset:33792\n" \
  "s_waitcnt lgkmcnt(6)\n" \
  "v_smfmac_f32_16x16x64_bf16 v[192:195], v[232:235], v[32:39], v248\n" \
  "ds_read_b128 v[224:227], v251 offset:12288\n" \
  "v_smfmac_f32_16x16x64_bf16 v[196:199], v[228:231], v[160:167], v248\n" \
  "ds_read_b128 v[228:231], v251 offset:13312\n" \
  "s_waitcnt lgkmcnt(5)\n" \
  "v_smfmac_f32_16x16x64_bf16 v[192:195], v[216:219], v[208:215], v248\n" \
  "ds_read_b128 v[208:211], v243 offset:49152\n" \
  "ds_read_b128 v[212:215], v243 offset:50176\n" \
  "v_smfmac_f32_16x16x64_bf16 v[196:199], v[232:235], v[96:103], v248\n" \
  "ds_read_b128 v[236:239], v242 offset:2048\n" \
  "s_waitcnt lgkmcnt(7)\n" \
  "v_smfmac_f32_16x16x64_bf16 v[192:195], v[220:223], v[40:47], v248\n" \
  "ds_read_b128 v[232:235], v251 offset:14336\n" \
  "v_smfmac_f32_16x16x64_bf16 v[196:199], v[216:219], v[168:175], v248\n" \
  "ds_read_b128 v[216:219], v251 offset:15360\n" \
  "s_waitcnt lgkmcnt(6)\n" \
  "v_smfmac_f32_16x16x64_bf16 v[192:195], v[224:227], v[200:207], v248\n" \
  "ds_read_b128 v[200:203], v242 offset:0\n" \
  "v_smfmac_f32_16x16x64_bf16 v[196:199], v[220:223], v[104:111], v248\n" \
  "ds_read_b128 v[204:207], v242 offset:1024\n" \
  "s_waitcnt lgkmcnt(7)\n" \
  "v_smfmac_f32_16x16x64_bf16 v[192:195], v[228:231], v[48:55], v248\n" \
  "ds_read_b128 v[220:223], v242 offset:3072\n" \
  "v_smfmac_f32_16x16x64_bf16 v[196:199], v[224:227], v[176:183], v248\n" \
  "s_waitcnt lgkmcnt(4)\n" \
  "v_smfmac_f32_16x16x64_bf16 v[192:195], v[232:235], v[208:215], v248\n" \
  "s_waitcnt lgkmcnt(3)\n" \
  "v_smfmac_f32_16x16x64_bf16 v[192:195], v[216:219], v[56:63], v248\n" \
  "v_smfmac_f32_16x16x64_bf16 v[196:199], v[228:231], v[112:119], v248\n" \
  "v_smfmac_f32_16x16x64_bf16 v[196:199], v[232:235], v[184:191], v248\n" \
  "s_nop 0\n" \
  "v_add_f32 v192, v192, v194\n" \
  "v_smfmac_f32_16x16x64_bf16 v[196:199], v[216:219], v[120:127], v248\n" \
  "v_add_f32 v193, v193, v195\n" \
  "v_cvt_pk_bf16_f32 v194, v192, v193\n" \
  "s_nop 6\n" \
  "v_add_f32 v196, v196, v198\n" \
  "v_add_f32 v197, v197, v199\n" \
  "v_cvt_pk_bf16_f32 v198, v196, v197\n" \
  "ds_write2_b32 v247, v194, v198 offset1:96\n" \
  "v_cmp_u_f32 vcc, v192, v193\n" \
  "s_cbranch_vccnz .Lnf_%=\n" \
  ".Lnfb_%=:\n" \
  "v_xor_b32 v241, 0x400, v250\n" \
  "s_waitcnt lgkmcnt(0)\n" \
  "s_barrier\n" \
  "ds_read_b64_tr_b16 v[192:193], v246 offset:16384\n" \
  "ds_read_b64_tr_b16 v[194:195], v246 offset:16768\n" \
  "ds_read_b64_tr_b16 v[196:197], v246 offset:17152\n" \
  "ds_read_b64_tr_b16 v[198:199], v246 offset:17536\n" \
  "ds_read_b64_tr_b16 v[208:209], v246 offset:17920\n" \
  "ds_read_b64_tr_b16 v[210:211], v246 offset:18304\n" \
  "ds_read_b64_tr_b16 v[212:213], v246 offset:18688\n" \
  "ds_read_b64_tr_b16 v[214:215], v246 offset:19072\n" \
  "s_waitcnt lgkmcnt(6)\n" \
  "v_mfma_f32_16x16x32_bf16 v[200:203], v[200:203], v[192:195], 0\n" \
  "s_waitcnt lgkmcnt(4)\n" \
  "v_mfma_f32_16x16x32_bf16 v[200:203], v[204:207], v[196:199], v[200:203]\n" \
  "s_waitcnt lgkmcnt(2)\n" \
  "v_mfma_f32_16x16x32_bf16 v[200:203], v[236:239], v[208:211], v[200:203]\n" \
  "s_waitcnt lgkmcnt(0)\n" \
  "v_mfma_f32_16x16x32_bf16 v[200:203], v[220:223], v[212:215], v[200:203]\n" \
  "s_nop 8\n" \
  "v_cndmask_b32 v204, v200, v202, %[hf]\n" \
  "v_cndmask_b32 v205, v201, v203, %[hf]\n" \
  "v_max_f32 v221, |v204|, |v205|\n" \
  "ds_write_b32 v250, v221 offset:22528\n" \
  "v_mul_f32 v236, 0x3e0293ee, v204\n" \
  "v_mul_f32 v237, 0x3e0293ee, v205\n" \
  "v_exp_f32 v236, v236\n" \
  "v_exp_f32 v237, v237\n" \
  "ds_read_b32 v238, v241 offset:22528\n" \
  "v_cvt_pk_bf16_f32 v220, v236, v237\n" \
  "s_mov_b64 %[wd], 0\n" \
  "s_waitcnt lgkmcnt(0)\n" \
  "v_max_f32 v238, v238, v221\n" \
  "v_cmp_nge_f32 vcc, 0x43aa0000, v238\n" \
  "s_cbranch_vccnz .Lw1_%=\n" \
  ".Lw1b_%=:\n" \
  "buffer_store_dword v220, v245, %[rs], %[c0] offen sc1\n" \
  "buffer_store_dword v220, v245, %[rs], %[c1] offen sc1\n" \
  "buffer_store_dword v220, v245, %[rs], %[c2] offen sc1\n" \
  "buffer_store_dword v220, v245, %[rs], %[c3] offen sc1\n" \
  "v_add_f32 v223, v236, v237\n" \
  "ds_write_b32 v250, v223 offset:22532\n" \
  "v_cndmask_b32 v222, 0, v222, %[wd]\n" \
  "v_mov_b32 v239, 1.0\n" \
  "s_waitcnt lgkmcnt(0)\n" \
  "s_barrier\n" \
  "ds_read_b64_tr_b16 v[216:217], v246 offset:19456\n" \
  "ds_read_b64_tr_b16 v[218:219], v246 offset:19840\n" \
  "ds_read_b64_tr_b16 v[228:229], v246 offset:20224\n" \
  "ds_read_b64_tr_b16 v[230:231], v246 offset:20608\n" \
  "ds_read_b64_tr_b16 v[232:233], v246 offset:20992\n" \
  "ds_read_b64_tr_b16 v[234:235], v246 offset:21376\n" \
  "ds_read_b64_tr_b16 v[200:201], v246 offset:21760\n" \
  "ds_read_b64_tr_b16 v[202:203], v246 offset:22144\n" \
  "ds_read_b32 v238, v241 offset:22532\n" \
  "s_waitcnt lgkmcnt(7)\n" \
  "v_mfma_f32_16x16x32_bf16 v[216:219], v[216:219], v[192:195], 0\n" \
  "s_waitcnt lgkmcnt(5)\n" \
  "v_mfma_f32_16x16x32_bf16 v[216:219], v[228:231], v[196:199], v[216:219]\n" \
  "s_waitcnt lgkmcnt(3)\n" \
  "v_mfma_f32_16x16x32_bf16 v[216:219], v[232:235], v[208:211], v[216:219]\n" \
  "s_waitcnt lgkmcnt(1)\n" \
  "v_mfma_f32_16x16x32_bf16 v[216:219], v[200:203], v[212:215], v[216:219]\n" \
  "s_waitcnt lgkmcnt(0)\n" \
  "v_add_f32 v223, v223, v238\n" \
  "v_mov_b32 v238, v223\n" \
  "s_nop 1\n" \
  "v_permlane32_swap_b32 v223, v238\n" \
  "v_add_f32 v223, v223, v238\n" \
  "s_nop 1\n" \
  "v_cndmask_b32 v204, v216, v218, %[hf]\n" \
  "v_cndmask_b32 v205, v217, v219, %[hf]\n" \
  "v_mul_f32 v236, 0x3e0293ee, v204\n" \
  "v_mul_f32 v237, 0x3e0293ee, v205\n" \
  "v_exp_f32 v236, v236\n" \
  "v_exp_f32 v237, v237\n" \
  "v_max_f32 v221, |v204|, |v205|\n" \
  "v_add_f32 v238, v223, v236\n" \
  "v_cmp_nge_f32 vcc, 0x43aa0000, v221\n" \
  "v_add_f32 v238, v238, v237\n" \
  "s_or_b64 vcc, vcc, %[wd]\n" \
  "s_cbranch_scc1 .Lw2_%=\n" \
  ".Lw2b_%=:\n" \
  "v_add_f32_dpp v238, v238, v238 row_ror:8 row_mask:0xf bank_mask:0xf\n" \
  "v_mov_b32 v221, v238\n" \
  "v_cndmask_b32 v236, v239, v236, %[up]\n" \
  "v_cndmask_b32 v237, v239, v237, %[up]\n" \
  "v_permlane16_swap_b32 v238, v221\n" \
  "v_add_f32 v238, v238, v221\n" \
  "v_rcp_f32 v238, v238\n" \
  "s_nop 0\n" \
  "v_mul_f32 v236, v236, v238\n" \
  "v_mul_f32 v237, v237, v238\n" \
  "v_cvt_pk_bf16_f32 v220, v236, v237\n" \
  "v_and_b32 v220, v220, v240\n" \
  "buffer_store_dword v220, v244, %[rs], %[c0] offen sc1\n" \
  "buffer_store_dword v220, v244, %[rs], %[c1] offen sc1\n"
#define QK_X \
  "s_branch .Lgo_%=\n" \
  ".Lrs_%=:\n" \
  "s_mov_b32 %[fs], -1\n" \
  "s_nop 15\n" \
  "s_nop 15\n" \
  ".Lrt_%=:\n" \
  "s_setprio 0\n" \
  "s_waitcnt vmcnt(0) lgkmcnt(0)\n" \
  "ds_read_b128 v[200:203], v249 offset:0\n" \
  "ds_read_b128 v[204:207], v249 offset:1024\n" \
  "ds_read_b128 v[208:211], v249 offset:16384\n" \
  "ds_read_b128 v[212:215], v249 offset:17408\n" \
  "v_mov_b64 v[192:193], 0\n" \
  "v_mov_b64 v[194:195], 0\n" \
  "v_mov_b64 v[196:197], 0\n" \
  "v_mov_b64 v[198:199], 0\n" \
  "s_barrier\n" \
  "ds_read_b128 v[216:219], v251 offset:0\n" \
  "ds_read_b128 v[220:223], v251 offset:1024\n" \
  "ds_read_b128 v[224:227], v251 offset:2048\n" \
  "ds_read_b128 v[228:231], v251 offset:3072\n" \
  "s_waitcnt lgkmcnt(0)\n" \
  "s_barrier\n" \
  "s_branch .Ll1_%=\n" \
  ".Lnf_%=:\n" \
  "s_cmp_eq_u32 %[fs], -1\n" \
  "s_cbranch_scc1 .Lnfb_%=\n" \
  "v_mov_b32 v241, %[fs]\n" \
  "ds_write_b32 v251, v241 offset:23560\n" \
  "s_waitcnt lgkmcnt(0)\n" \
  "s_mov_b32 %[fs], -1\n" \
  "s_barrier\n" \
  "s_branch .Lrt_%=\n" \
  ".p2align 6\n" \
  ".Lgo_%=:\n" \
  "ds_read_b128 v[200:203], v249 offset:0\n" \
  "ds_read_b128 v[204:207], v249 offset:1024\n" \
  "ds_read_b128 v[208:211], v249 offset:16384\n" \
  "ds_read_b128 v[212:215], v249 offset:17408\n" \
  "v_mov_b64 v[192:193], 0\n" \
  "v_mov_b64 v[194:195], 0\n" \
  "v_mov_b64 v[196:197], 0\n" \
  "v_mov_b64 v[198:199], 0\n" \
  "s_mov_b32 %[keep], m0\n" \
  "v_readfirstlane_b32 %[cnt], v250\n" \
  "s_mov_b32 m0, %[cnt]\n" \
  "s_waitcnt lgkmcnt(0)\n" \
  "s_barrier\n" \
  "buffer_load_dwordx4 v251, %[rs], %[o0] offen sc1 lds\n" \
  "s_waitcnt vmcnt(0)\n" \
  "s_barrier\n" \
  "s_mov_b32 m0, %[keep]\n" \
  "ds_read_b128 v[216:219], v251 offset:0\n" \
  "ds_read_b128 v[220:223], v251 offset:1024\n" \
  "ds_read_b128 v[224:227], v251 offset:2048\n" \
  "ds_read_b128 v[228:231], v251 offset:3072\n" \
  "s_barrier\n" \
  ".Ll1_%=:\n" \
  "s_waitcnt lgkmcnt(3)\n" \
  "v_smfmac_f32_16x16x64_bf16 v[192:195], v[216:219], v[200:207], v248\n" \
  "ds_read_b128 v[200:203], v249 offset:32768\n" \
  "ds_read_b128 v[204:207], v249 offset:33792\n" \
  "s_waitcnt lgkmcnt(4)\n" \
  "v_smfmac_f32_16x16x64_bf16 v[192:195], v[220:223], v[0:7], v248\n" \
  "ds_read_b128 v[232:235], v251 offset:4096\n" \
  "v_smfmac_f32_16x16x64_bf16 v[196:199], v[216:219], v[128:135], v248\n" \
  "ds_read_b128 v[216:219], v251 offset:5120\n" \
  "s_waitcnt lgkmcnt(5)\n" \
  "v_smfmac_f32_16x16x64_bf16 v[192:195], v[224:227], v[208:215], v248\n" \
  "ds_read_b128 v[208:211], v249 offset:49152\n" \
  "v_smfmac_f32_16x16x64_bf16 v[196:199], v[220:223], v[64:71], v248\n" \
  "ds_read_b128 v[212:215], v249 offset:50176\n" \
  "s_waitcnt lgkmcnt(6)\n" \
  "v_smfmac_f32_16x16x64_bf16 v[192:195], v[228:231], v[8:15], v248\n" \
  "ds_read_b128 v[220:223], v251 offset:6144\n" \
  "v_smfmac_f32_16x16x64_bf16 v[196:199], v[224:227], v[136:143], v248\n" \
  "ds_read_b128 v[224:227], v251 offset:7168\n" \
  "s_waitcnt lgkmcnt(5)\n" \
  "v_smfmac_f32_16x16x64_bf16 v[192:195], v[232:235], v[200:207], v248\n" \
  "ds_read_b128 v[200:203], v243 offset:0\n" \
  "s_barrier\n" \
  "v_smfmac_f32_16x16x64_bf16 v[196:199], v[228:231], v[72:79], v248\n" \
  "ds_read_b128 v[204:207], v243 offset:1024\n" \
  "s_waitcnt lgkmcnt(6)\n" \
  "v_smfmac_f32_16x16x64_bf16 v[192:195], v[216:219], v[16:23], v248\n" \
  "ds_read_b128 v[228:231], v251 offset:8192\n" \
  "v_smfmac_f32_16x16x64_bf16 v[196:199], v[232:235], v[144:151], v248\n" \
  "ds_read_b128 v[232:235], v251 offset:9216\n" \
  "s_waitcnt lgkmcnt(5)\n" \
  "v_smfmac_f32_16x16x64_bf16 v[192:195], v[220:223], v[208:215], v248\n" \
  "ds_read_b128 v[208:211], v243 offset:16384\n" \
  "v_smfmac_f32_16x16x64_bf16 v[196:199], v[216:219], v[80:87], v248\n" \
  "ds_read_b128 v[212:215], v243 offset:17408\n" \
  "s_waitcnt lgkmcnt(6)\n" \
  "v_smfmac_f32_16x16x64_bf16 v[192:195], v[224:227], v[24:31], v248\n" \
  "ds_read_b128 v[216:219], v251 offset:10240\n" \
  "v_smfmac_f32_16x16x64_bf16 v[196:199], v[220:223], v[152:159], v248\n" \
  "ds_read_b128 v[220:223], v251 offset:11264\n" \
  "s_waitcnt lgkmcnt(5)\n" \
  "v_smfmac_f32_16x16x64_bf16 v[192:195], v[228:231], v[200:207], v248\n" \
  "ds_read_b128 v[200:203], v243 offset:32768\n" \
  "v_smfmac_f32_16x16x64_bf16 v[196:199], v[224:227], v[88:95], v248\n" \
  "ds_read_b128 v[204:207], v243 offset:33792\n" \
  "s_waitcnt lgkmcnt(6)\n" \
  "v_smfmac_f32_16x16x64_bf16 v[192:195], v[232:235], v[32:39], v248\n" \
  "ds_read_b128 v[224:227], v251 offset:12288\n" \
  "v_smfmac_f32_16x16x64_bf16 v[196:199], v[228:231], v[160:167], v248\n" \
  "ds_read_b128 v[228:231], v251 offset:13312\n" \
  "s_waitcnt lgkmcnt(5)\n" \
  "v_smfmac_f32_16x16x64_bf16 v[192:195], v[216:219], v[208:215], v248\n" \
  "ds_read_b128 v[208:211], v243 offset:49152\n" \
  "v_smfmac_f32_16x16x64_bf16 v[196:199], v[232:235], v[96:103], v248\n" \
  "ds_read_b128 v[212:215], v243 offset:50176\n" \
  "s_waitcnt lgkmcnt(6)\n" \
  "v_smfmac_f32_16x16x64_bf16 v[192:195], v[220:223], v[40:47], v248\n" \
  "ds_read_b128 v[232:235], v251 offset:14336\n" \
  "v_smfmac_f32_16x16x64_bf16 v[196:199], v[216:219], v[168:175], v248\n" \
  "ds_read_b128 v[216:219], v251 offset:15360\n" \
  "s_waitcnt lgkmcnt(5)\n" \
  "v_smfmac_f32_16x16x64_bf16 v[192:195], v[224:227], v[200:207], v248\n" \
  "v_smfmac_f32_16x16x64_bf16 v[196:199], v[220:223], v[104:111], v248\n" \
  "s_waitcnt lgkmcnt(4)\n" \
  "v_smfmac_f32_16x16x64_bf16 v[192:195], v[228:231], v[48:55], v248\n" \
  "v_smfmac_f32_16x16x64_bf16 v[196:199], v[224:227], v[176:183], v248\n" \
  "s_waitcnt lgkmcnt(1)\n" \
  "v_smfmac_f32_16x16x64_bf16 v[192:195], v[232:235], v[208:215], v248\n" \
  "s_waitcnt lgkmcnt(0)\n" \
  "v_smfmac_f32_16x16x64_bf16 v[192:195], v[216:219], v[56:63], v248\n" \
  "v_smfmac_f32_16x16x64_bf16 v[196:199], v[228:231], v[112:119], v248\n" \
  "v_smfmac_f32_16x16x64_bf16 v[196:199], v[232:235], v[184:191], v248\n" \
  "s_nop 0\n" \
  "v_add_f32 v192, v192, v194\n" \
  "v_smfmac_f32_16x16x64_bf16 v[196:199], v[216:219], v[120:127], v248\n" \
  "v_add_f32 v193, v193, v195\n" \
  "v_cvt_pk_bf16_f32 v194, v192, v193\n" \
  "s_nop 6\n" \
  "v_add_f32 v196, v196, v198\n" \
  "v_add_f32 v197, v197, v199\n" \
  "v_cvt_pk_bf16_f32 v198, v196, v197\n" \
  "ds_write2_b32 v247, v194, v198 offset1:96\n" \
  "v_cmp_u_f32 vcc, v192, v193\n" \
  "s_cbranch_vccnz .Lnf_%=\n" \
  ".Lnfb_%=:\n" \
  "s_waitcnt lgkmcnt(0)\n" \
  "s_barrier\n" \
  "s_nop 5\n" \
  "buffer_load_dwordx4 v[200:203], v251, %[rs], %[qo] offen offset:0 sc1\n" \
  "buffer_load_dwordx4 v[204:207], v251, %[rs], %[qo] offen offset:1024 sc1\n" \
  "buffer_load_dwordx4 v[208:211], v251, %[rs], %[qo] offen offset:2048 sc1\n" \
  "buffer_load_dwordx4 v[212:215], v251, %[rs], %[qo] offen offset:3072 sc1\n" \
  "s_nop 15\n" \
  "s_nop 15\n" \
  "s_nop 15\n" \
  "s_nop 9\n" \
  "ds_read_b32 v241, v251 offset:23560\n" \
  "s_waitcnt lgkmcnt(0)\n" \
  "v_cmp_eq_u32 vcc, %[fs], v241\n" \
  "s_cbranch_vccnz .Lrs_%=\n" \
  "ds_read_b128 v[192:195], v251 offset:24576\n" \
  "ds_read_b128 v[196:199], v251 offset:28672\n" \
  "ds_read_b128 v[216:219], v251 offset:25600\n" \
  "ds_read_b128 v[220:223], v251 offset:29696\n" \
  "ds_read_b128 v[224:227], v251 offset:26624\n" \
  "ds_read_b128 v[228:231], v251 offset:30720\n" \
  "ds_read_b128 v[232:235], v251 offset:27648\n" \
  "ds_read_b128 v[236:239], v251 offset:31744\n" \
  "s_waitcnt lgkmcnt(0)\n" \
  "s_barrier\n" \
  "s_waitcnt vmcnt(3)\n" \
  "v_mfma_f32_16x16x32_bf16 v[192:195], v[192:195], v[200:203], 0\n" \
  "v_mfma_f32_16x16x32_bf16 v[196:199], v[196:199], v[200:203], 0\n" \
  "s_waitcnt vmcnt(2)\n" \
  "v_mfma_f32_16x16x32_bf16 v[192:195], v[216:219], v[204:207], v[192:195]\n" \
  "v_mfma_f32_16x16x32_bf16 v[196:199], v[220:223], v[204:207], v[196:199]\n" \
  "s_waitcnt vmcnt(1)\n" \
  "v_mfma_f32_16x16x32_bf16 v[192:195], v[224:227], v[208:211], v[192:195]\n" \
  "v_mfma_f32_16x16x32_bf16 v[196:199], v[228:231], v[208:211], v[196:199]\n" \
  "s_waitcnt vmcnt(0)\n" \
  "v_mfma_f32_16x16x32_bf16 v[192:195], v[232:235], v[212:215], v[192:195]\n" \
  "v_mfma_f32_16x16x32_bf16 v[196:199], v[236:239], v[212:215], v[196:199]\n" \
  "s_nop 8\n"
#define QK_K \
  "s_branch .Lgo_%=\n" \
  ".Lsf_%=:\n" \
  "s_cmp_eq_u32 %[fs], -1\n" \
  "s_cbranch_scc1 .Lsfb_%=\n" \
  "v_mov_b32 v241, %[fs]\n" \
  "ds_write_b32 v251, v241 offset:23560\n" \
  "s_branch .Lrt_%=\n" \
  ".Lrs_%=:\n" \
  "s_mov_b32 %[fs], -1\n" \
  "s_nop 15\n" \
  "s_nop 15\n" \
  ".Lrt_%=:\n" \
  "s_setprio 0\n" \
  "s_waitcnt vmcnt(0) lgkmcnt(0)\n" \
  "buffer_load_dwordx4 v[216:219], v251, %[rs], %[o3] offen sc1\n" \
  "buffer_load_dwordx4 v[220:223], v251, %[rs], %[o0] offen sc1\n" \
  "buffer_load_dwordx4 v[224:227], v251, %[rs], %[o1] offen sc1\n" \
  "buffer_load_dwordx4 v[228:231], v251, %[rs], %[o2] offen sc1\n" \
  "v_subrev_u32 v232, 0x1000, v250\n" \
  "s_mov_b32 %[cnt], -2\n" \
  "ds_read_b128 v[200:203], v232 offset:0\n" \
  "ds_read_b128 v[204:207], v232 offset:4096\n" \
  "ds_read_b128 v[208:211], v232 offset:8192\n" \
  "ds_read_b128 v[212:215], v232 offset:12288\n" \
  "s_waitcnt lgkmcnt(0)\n" \
  "v_max_u32 v241, v200, v201\n" \
  "v_max3_u32 v241, v241, v202, v203\n" \
  "v_max3_u32 v241, v241, v204, v205\n" \
  "v_max3_u32 v241, v241, v206, v207\n" \
  "v_max3_u32 v241, v241, v208, v209\n" \
  "v_max3_u32 v241, v241, v210, v211\n" \
  "v_max3_u32 v241, v241, v212, v213\n" \
  "v_max3_u32 v241, v241, v214, v215\n" \
  "v_cmp_eq_u32 vcc, -1, v241\n" \
  "s_cbranch_vccz .Lrd_%=\n" \
  "s_mov_b32 %[cnt], 0x40000\n" \
  ".Lrr_%=:\n" \
  "s_waitcnt vmcnt(0)\n" \
  "v_max_u32 v241, v216, v217\n" \
  "v_max3_u32 v241, v241, v218, v219\n" \
  "v_max3_u32 v241, v241, v220, v221\n" \
  "v_max3_u32 v241, v241, v222, v223\n" \
  "v_max3_u32 v241, v241, v224, v225\n" \
  "v_max3_u32 v241, v241, v226, v227\n" \
  "v_max3_u32 v241, v241, v228, v229\n" \
  "v_max3_u32 v241, v241, v230, v231\n" \
  "v_cmp_eq_u32 vcc, -1, v241\n" \
  "s_cbranch_vccz .Lrd_%=\n" \
  "s_sleep 2\n" \
  "buffer_load_dwordx4 v[216:219], v251, %[rs], %[o3] offen sc1\n" \
  "buffer_load_dwordx4 v[220:223], v251, %[rs], %[o0] offen sc1\n" \
  "buffer_load_dwordx4 v[224:227], v251, %[rs], %[o1] offen sc1\n" \
  "buffer_load_dwordx4 v[228:231], v251, %[rs], %[o2] offen sc1\n" \
  "s_sub_u32 %[cnt], %[cnt], 1\n" \
  "s_cbranch_scc0 .Lrr_%=\n" \
  ".Lrd_%=:\n" \
  "ds_read_b128 v[200:203], v249 offset:0\n" \
  "ds_read_b128 v[204:207], v249 offset:1024\n" \
  "ds_read_b128 v[208:211], v249 offset:16384\n" \
  "ds_read_b128 v[212:215], v249 offset:17408\n" \
  "v_mov_b64 v[192:193], 0\n" \
  "v_mov_b64 v[194:195], 0\n" \
  "v_mov_b64 v[196:197], 0\n" \
  "v_mov_b64 v[198:199], 0\n" \
  "s_cmp_eq_u32 %[fs], -1\n" \
  "s_cbranch_scc1 .Lre_%=\n" \
  "s_mov_b32 %[fs], -1\n" \
  "s_barrier\n" \
  ".Lre_%=:\n" \
  "s_waitcnt vmcnt(0)\n" \
  "s_cmp_eq_u32 %[cnt], -2\n" \
  "s_cbranch_scc1 .Lrf_%=\n" \
  "ds_write_b128 v232, v[216:219] offset:0\n" \
  "ds_write_b128 v232, v[220:223] offset:4096\n" \
  "ds_write_b128 v232, v[224:227] offset:8192\n" \
  "ds_write_b128 v232, v[228:231] offset:12288\n" \
  ".Lrf_%=:\n" \
  "v_mov_b32 v241, 0\n" \
  "s_waitcnt lgkmcnt(0)\n" \
  "s_barrier\n" \
  "s_waitcnt lgkmcnt(0)\n" \
  "s_barrier\n" \
  "s_branch .Ll1_%=\n" \
  ".p2align 6\n" \
  ".Lgo_%=:\n" \
  "ds_read_b128 v[200:203], v249 offset:0\n" \
  "ds_read_b128 v[204:207], v249 offset:1024\n" \
  "ds_read_b128 v[208:211], v249 offset:16384\n" \
  "ds_read_b128 v[212:215], v249 offset:17408\n" \
  "v_mov_b64 v[192:193], 0\n" \
  "v_mov_b64 v[194:195], 0\n" \
  "v_mov_b64 v[196:197], 0\n" \
  "v_mov_b64 v[198:199], 0\n" \
  "s_mov_b32 %[keep], m0\n" \
  "v_readfirstlane_b32 %[cnt], v250\n" \
  "s_mov_b32 m0, %[cnt]\n" \
  "v_bfe_u32 v241, v251, 2, 7\n" \
  "v_lshl_add_u32 v241, v251, 4, v241\n" \
  "s_waitcnt lgkmcnt(0)\n" \
  "s_cmp_eq_u32 %[tw], 0\n" \
  "s_cbranch_scc1 .Ltq_%=\n" \
  ".Ltl_%=:\n" \
  "s_memtime vcc\n" \
  "s_waitcnt lgkmcnt(0)\n" \
  "s_sub_i32 %[cnt], %[tg], vcc_lo\n" \
  "s_cmp_lt_i32 %[cnt], 260\n" \
  "s_cbranch_scc1 .Ltf_%=\n" \
  "s_sleep 1\n" \
  "s_branch .Ltl_%=\n" \
  ".Ltf_%=:\n" \
  "s_max_i32 %[cnt], %[cnt], 0\n" \
  ".p2align 3\n" \
  ".Ltg_%=:\n" \
  "s_sub_u32 %[cnt], %[cnt], 28\n" \
  "s_cbranch_scc0 .Ltg_%=\n" \
  ".Ltq_%=:\n" \
  "s_barrier\n" \
  "buffer_load_dwordx4 v251, %[rs], %[o0] offen sc1 lds\n" \
  "s_add_u32 m0, m0, 0x1000\n" \
  "s_nop 4\n" \
  "buffer_load_dwordx4 v251, %[rs], %[o1] offen sc1 lds\n" \
  "s_add_u32 m0, m0, 0x1000\n" \
  "s_nop 4\n" \
  "buffer_load_dwordx4 v251, %[rs], %[o2] offen sc1 lds\n" \
  "s_barrier\n" \
  "s_waitcnt vmcnt(2)\n" \
  "s_barrier\n" \
  "s_mov_b32 m0, %[keep]\n" \
  ".Ll1_%=:\n" \
  "s_memtime vcc\n" \
  "s_setprio 3\n" \
  "s_waitcnt vmcnt(0)\n" \
  "s_waitcnt lgkmcnt(0)\n" \
  "s_barrier\n" \
  "ds_read2_b32 v[236:237], v241 offset1:32\n" \
  "s_sub_u32 %[cnt], vcc_lo, %[ckm]\n" \
  "s_mul_hi_u32 %[cnt], %[cnt], %[inv]\n" \
  "s_add_u32 %[cnt], %[cnt], %[r16]\n" \
  "s_and_b32 %[cnt], %[cnt], -2\n" \
  "s_waitcnt lgkmcnt(0)\n" \
  "v_max_u32 v241, v236, v237\n" \
  "v_cmp_eq_u32 vcc, -1, v241\n" \
  "s_cbranch_vccnz .Lsf_%=\n" \
  ".Lsfb_%=:\n" \
  "v_mov_b32 v241, %[cnt]\n" \
  "buffer_store_dword v241, v245, %[rs], %[sst] offen sc1\n" \
  "s_setprio 0\n" \
  "ds_read_b128 v[216:219], v251 offset:0\n" \
  "ds_read_b128 v[220:223], v251 offset:1024\n" \
  "ds_read_b128 v[224:227], v251 offset:2048\n" \
  "ds_read_b128 v[228:231], v251 offset:3072\n" \
  "s_waitcnt lgkmcnt(3)\n" \
  "v_smfmac_f32_16x16x64_bf16 v[192:195], v[216:219], v[200:207], v248\n" \
  "ds_read_b128 v[200:203], v249 offset:32768\n" \
  "ds_read_b128 v[204:207], v249 offset:33792\n" \
  "s_waitcnt lgkmcnt(4)\n" \
  "v_smfmac_f32_16x16x64_bf16 v[192:195], v[220:223], v[0:7], v248\n" \
  "ds_read_b128 v[232:235], v251 offset:4096\n" \
  "v_smfmac_f32_16x16x64_bf16 v[196:199], v[216:219], v[128:135], v248\n" \
  "ds_read_b128 v[216:219], v251 offset:5120\n" \
  "s_waitcnt lgkmcnt(5)\n" \
  "v_smfmac_f32_16x16x64_bf16 v[192:195], v[224:227], v[208:215], v248\n" \
  "ds_read_b128 v[208:211], v249 offset:49152\n" \
  "v_smfmac_f32_16x16x64_bf16 v[196:199], v[220:223], v[64:71], v248\n" \
  "ds_read_b128 v[212:215], v249 offset:50176\n" \
  "s_waitcnt lgkmcnt(6)\n" \
  "v_smfmac_f32_16x16x64_bf16 v[192:195], v[228:231], v[8:15], v248\n" \
  "ds_read_b128 v[220:223], v251 offset:6144\n" \
  "v_smfmac_f32_16x16x64_bf16 v[196:199], v[224:227], v[136:143], v248\n" \
  "ds_read_b128 v[224:227], v251 offset:7168\n" \
  "s_waitcnt lgkmcnt(5)\n" \
  "v_smfmac_f32_16x16x64_bf16 v[192:195], v[232:235], v[200:207], v248\n" \
  "ds_read_b128 v[200:203], v243 offset:0\n" \
  "s_barrier\n" \
  "ds_read_b32 v236, v251 offset:23560\n" \
  "v_smfmac_f32_16x16x64_bf16 v[196:199], v[228:231], v[72:79], v248\n" \
  "buffer_load_dwordx2 v[240:241], v244, %[rs], %[ss] offen sc1\n" \
  "ds_read_b128 v[204:207], v243 offset:1024\n" \
  "s_waitcnt lgkmcnt(7)\n" \
  "v_smfmac_f32_16x16x64_bf16 v[192:195], v[216:219], v[16:23], v248\n" \
  "s_cmp_eq_u32 %[tc], 0\n" \
  "s_cbranch_scc1 .Lnt_%=\n" \
  "buffer_load_dword v237, v242, %[rs], %[tc] offen sc1\n" \
  ".Lnt_%=:\n" \
  "ds_read_b128 v[228:231], v251 offset:8192\n" \
  "v_smfmac_f32_16x16x64_bf16 v[196:199], v[232:235], v[144:151], v248\n" \
  "ds_read_b128 v[232:235], v251 offset:9216\n" \
  "s_waitcnt lgkmcnt(3)\n" \
  "v_cmp_eq_u32 vcc, %[fs], v236\n" \
  "v_smfmac_f32_16x16x64_bf16 v[192:195], v[220:223], v[208:215], v248\n" \
  "ds_read_b128 v[208:211], v243 offset:16384\n" \
  "v_smfmac_f32_16x16x64_bf16 v[196:199], v[216:219], v[80:87], v248\n" \
  "ds_read_b128 v[212:215], v243 offset:17408\n" \
  "v_smfmac_f32_16x16x64_bf16 v[192:195], v[224:227], v[24:31], v248\n" \
  "ds_read_b128 v[216:219], v251 offset:10240\n" \
  "v_smfmac_f32_16x16x64_bf16 v[196:199], v[220:223], v[152:159], v248\n" \
  "ds_read_b128 v[220:223], v251 offset:11264\n" \
  "s_waitcnt lgkmcnt(5)\n" \
  "v_smfmac_f32_16x16x64_bf16 v[192:195], v[228:231], v[200:207], v248\n" \
  "ds_read_b128 v[200:203], v243 offset:32768\n" \
  "v_smfmac_f32_16x16x64_bf16 v[196:199], v[224:227], v[88:95], v248\n" \
  "ds_read_b128 v[204:207], v243 offset:33792\n" \
  "s_waitcnt lgkmcnt(6)\n" \
  "v_smfmac_f32_16x16x64_bf16 v[192:195], v[232:235], v[32:39], v248\n" \
  "ds_read_b128 v[224:227], v251 offset:12288\n" \
  "v_smfmac_f32_16x16x64_bf16 v[196:199], v[228:231], v[160:167], v248\n" \
  "ds_read_b128 v[228:231], v251 offset:13312\n" \
  "s_waitcnt lgkmcnt(5)\n" \
  "v_smfmac_f32_16x16x64_bf16 v[192:195], v[216:219], v[208:215], v248\n" \
  "ds_read_b128 v[208:211], v243 offset:49152\n" \
  "v_smfmac_f32_16x16x64_bf16 v[196:199], v[232:235], v[96:103], v248\n" \
  "ds_read_b128 v[212:215], v243 offset:50176\n" \
  "s_waitcnt lgkmcnt(6)\n" \
  "v_smfmac_f32_16x16x64_bf16 v[192:195], v[220:223], v[40:47], v248\n" \
  "ds_read_b128 v[232:235], v251 offset:14336\n" \
  "v_smfmac_f32_16x16x64_bf16 v[196:199], v[216:219], v[168:175], v248\n" \
  "ds_read_b128 v[216:219], v251 offset:15360\n" \
  "s_waitcnt lgkmcnt(5)\n" \
  "v_smfmac_f32_16x16x64_bf16 v[192:195], v[224:227], v[200:207], v248\n" \
  "v_smfmac_f32_16x16x64_bf16 v[196:199], v[220:223], v[104:111], v248\n" \
  "s_waitcnt lgkmcnt(4)\n" \
  "v_smfmac_f32_16x16x64_bf16 v[192:195], v[228:231], v[48:55], v248\n" \
  "v_smfmac_f32_16x16x64_bf16 v[196:199], v[224:227], v[176:183], v248\n" \
  "s_waitcnt lgkmcnt(1)\n" \
  "v_smfmac_f32_16x16x64_bf16 v[192:195], v[232:235], v[208:215], v248\n" \
  "s_waitcnt lgkmcnt(0)\n" \
  "v_smfmac_f32_16x16x64_bf16 v[192:195], v[216:219], v[56:63], v248\n" \
  "v_smfmac_f32_16x16x64_bf16 v[196:199], v[228:231], v[112:119], v248\n" \
  "v_smfmac_f32_16x16x64_bf16 v[196:199], v[232:235], v[184:191], v248\n" \
  "s_nop 0\n" \
  "v_add_f32 v192, v192, v194\n" \
  "v_smfmac_f32_16x16x64_bf16 v[196:199], v[216:219], v[120:127], v248\n" \
  "s_cbranch_vccnz .Lrs_%=\n" \
  "s_setprio 3\n" \
  "v_add_f32 v193, v193, v195\n" \
  "v_cvt_pk_bf16_f32 v194, v192, v193\n" \
  "s_nop 4\n" \
  "v_add_f32 v196, v196, v198\n" \
  "v_add_f32 v197, v197, v199\n" \
  "v_cvt_pk_bf16_f32 v198, v196, v197\n" \
  "ds_write2_b32 v247, v194, v198 offset1:96\n" \
  "s_waitcnt lgkmcnt(0)\n" \
  "s_barrier\n" \
  "s_setprio 0\n" \
  "s_waitcnt vmcnt(1)\n" \
  "s_cmp_lg_u32 %[tc], 0\n" \
  "s_cbranch_scc1 .Lnw_%=\n" \
  "s_waitcnt vmcnt(0)\n" \
  ".Lnw_%=:\n"
#define OW_F \
  "s_branch .Lgo_%=\n" \
  ".Lbad_%=:\n" \
  "s_mov_b32 %[st], 1\n" \
  "s_branch .Lend_%=\n" \
  ".p2align 6\n" \
  "s_nop 0\n" \
  "s_nop 0\n" \
  "s_nop 0\n" \
  "s_nop 0\n" \
  "s_nop 0\n" \
  "s_nop 0\n" \
  "s_nop 0\n" \
  ".Lgo_%=:\n" \
  "v_mov_b64 v[84:85], 0\n" \
  "v_mov_b64 v[86:87], 0\n" \
  "v_mov_b64 v[88:89], 0\n" \
  "v_mov_b64 v[90:91], 0\n" \
  "v_mov_b64 v[92:93], 0\n" \
  "v_mov_b64 v[94:95], 0\n" \
  "v_mov_b64 v[96:97], 0\n" \
  "v_mov_b64 v[98:99], 0\n" \
  "v_mov_b64 v[100:101], 0\n" \
  "v_mov_b64 v[102:103], 0\n" \
  "s_mov_b32 %[st], 0\n" \
  "s_barrier\n" \
  "ds_read_b32 v104, v119\n" \
  "s_nop 15\n" \
  "s_nop 15\n" \
  "s_nop 15\n" \
  "s_nop 15\n" \
  "s_nop 5\n" \
  "buffer_load_dwordx4 v[0:3], v112, %[rs], %[sv1] offen offset:0 sc1\n" \
  "buffer_load_dwordx4 v[4:7], v112, %[rs], %[sv1] offen offset:1024 sc1\n" \
  "buffer_load_dwordx4 v[8:11], v112, %[rs], %[sv1] offen offset:2048 sc1\n" \
  "buffer_load_dwordx4 v[12:15], v112, %[rs], %[sv1] offen offset:3072 sc1\n" \
  "s_waitcnt lgkmcnt(0)\n" \
  "v_readfirstlane_b32 %[st], v104\n" \
  ".Ltl_%=:\n" \
  "s_memtime vcc\n" \
  "s_waitcnt lgkmcnt(0)\n" \
  "s_sub_i32 %[cnt], %[st], vcc_lo\n" \
  "s_sub_i32 vcc_hi, %[cnt], 120\n" \
  "s_cmp_lt_u32 vcc_hi, 2000\n" \
  "s_cbranch_scc1 .Ltl_%=\n" \
  "s_mov_b32 %[st], 0\n" \
  "s_max_i32 %[cnt], %[cnt], 0\n" \
  "s_min_i32 %[cnt], %[cnt], 2000\n" \
  ".p2align 3\n" \
  ".Ltg_%=:\n" \
  "s_sub_u32 %[cnt], %[cnt], 28\n" \
  "s_cbranch_scc0 .Ltg_%=\n" \
  "buffer_load_dwordx4 v[16:19], v112, %[rs], %[sv2] offen offset:0 sc1\n" \
  "buffer_load_dwordx4 v[20:23], v112, %[rs], %[sv2] offen offset:1024 sc1\n" \
  "s_nop 3\n" \
  "buffer_load_dwordx4 v[80:83], v113, %[rs], %[sp] offen sc1\n" \
  "s_nop 5\n" \
  "buffer_load_dwordx4 v[64:67], v114, %[rs], %[sp1] offen offset:0 sc1\n" \
  "buffer_load_dwordx4 v[68:71], v114, %[rs], %[sp1] offen offset:1024 sc1\n" \
  "buffer_load_dwordx4 v[72:75], v114, %[rs], %[sp2] offen offset:0 sc1\n" \
  "buffer_load_dwordx4 v[24:27], v112, %[rs], %[sv2] offen offset:2048 sc1\n" \
  "buffer_load_dwordx4 v[28:31], v112, %[rs], %[sv2] offen offset:3072 sc1\n" \
  "buffer_load_dwordx4 v[76:79], v114, %[rs], %[sp2] offen offset:1024 sc1\n" \
  "s_barrier\n" \
  "ds_read_b128 v[32:35], v115 offset:0\n" \
  "ds_read_b128 v[36:39], v115 offset:1024\n" \
  "ds_read_b128 v[40:43], v115 offset:2048\n" \
  "ds_read_b128 v[44:47], v115 offset:3072\n" \
  "ds_read_b128 v[48:51], v115 offset:4096\n" \
  "ds_read_b128 v[52:55], v115 offset:5120\n" \
  "ds_read_b128 v[56:59], v115 offset:6144\n" \
  "ds_read_b128 v[60:63], v115 offset:7168\n" \
  "s_waitcnt vmcnt(6)\n" \
  "s_mov_b64 exec, %[m0]\n" \
  "v_mov_b64 v[84:85], v[80:81]\n" \
  "v_mov_b64 v[86:87], v[82:83]\n" \
  "s_mov_b64 exec, %[m1]\n" \
  "v_mov_b64 v[88:89], v[80:81]\n" \
  "v_mov_b64 v[90:91], v[82:83]\n" \
  "s_mov_b64 exec, %[m2]\n" \
  "v_mov_b64 v[92:93], v[80:81]\n" \
  "v_mov_b64 v[94:95], v[82:83]\n" \
  "s_mov_b64 exec, %[m3]\n" \
  "v_mov_b64 v[96:97], v[80:81]\n" \
  "v_mov_b64 v[98:99], v[82:83]\n" \
  "s_mov_b64 exec, -1\n" \
  "s_waitcnt vmcnt(5)\n" \
  "v_smfmac_f32_16x16x64_bf16 v[100:103], v[64:67], v[0:7], v118\n" \
  "s_waitcnt vmcnt(4)\n" \
  "v_smfmac_f32_16x16x64_bf16 v[100:103], v[68:71], v[8:15], v118\n" \
  "s_waitcnt lgkmcnt(6)\n" \
  "v_smfmac_f32_16x16x64_bf16 v[100:103], v[84:87], v[32:39], v118\n" \
  "s_waitcnt lgkmcnt(4)\n" \
  "v_smfmac_f32_16x16x64_bf16 v[100:103], v[88:91], v[40:47], v118 abid:1\n" \
  "s_waitcnt vmcnt(3)\n" \
  "v_smfmac_f32_16x16x64_bf16 v[100:103], v[72:75], v[16:23], v118\n" \
  "s_waitcnt lgkmcnt(2)\n" \
  "v_smfmac_f32_16x16x64_bf16 v[100:103], v[92:95], v[48:55], v118\n" \
  "s_waitcnt lgkmcnt(0)\n" \
  "v_smfmac_f32_16x16x64_bf16 v[100:103], v[96:99], v[56:63], v118 abid:1\n" \
  "s_waitcnt vmcnt(0)\n" \
  "v_smfmac_f32_16x16x64_bf16 v[100:103], v[76:79], v[24:31], v118\n" \
  "s_nop 8\n" \
  "v_add_f32 v100, v100, v102\n" \
  "v_add_f32 v101, v101, v103\n" \
  "v_cvt_pk_bf16_f32 v104, v100, v101\n" \
  "v_cmp_u_f32 vcc, v100, v101\n" \
  "s_nop 0\n" \
  "v_mov_b32_dpp v105, v104 quad_perm:[1,0,3,2] row_mask:0xf bank_mask:0xf\n" \
  "v_perm_b32 v104, v105, v104, v117\n" \
  "s_cbranch_vccnz .Lbad_%=\n" \
  "buffer_store_dword v104, v116, %[rs], %[sm0] offen sc1\n" \
  "buffer_store_dword v104, v116, %[rs], %[sm1] offen sc1\n" \
  "buffer_store_dword v104, v116, %[rs], %[sm2] offen sc1\n" \
  "buffer_store_dword v104, v116, %[rs], %[sm3] offen sc1\n" \
  ".Lend_%=:\n"
#define OW_E \
  "s_branch .Lgo_%=\n" \
  ".Lbad_%=:\n" \
  "s_mov_b32 %[st], 1\n" \
  "s_branch .Lend_%=\n" \
  ".p2align 6\n" \
  ".Lgo_%=:\n" \
  "v_mov_b64 v[208:209], 0\n" \
  "v_mov_b64 v[210:211], 0\n" \
  "v_mov_b64 v[212:213], 0\n" \
  "v_mov_b64 v[214:215], 0\n" \
  "v_mov_b64 v[216:217], 0\n" \
  "v_mov_b64 v[218:219], 0\n" \
  "v_mov_b64 v[220:221], 0\n" \
  "v_mov_b64 v[222:223], 0\n" \
  "v_mov_b64 v[224:225], 0\n" \
  "v_mov_b64 v[226:227], 0\n" \
  "v_mov_b64 v[228:229], 0\n" \
  "v_mov_b64 v[230:231], 0\n" \
  "v_mov_b64 v[232:233], 0\n" \
  "v_mov_b64 v[234:235], 0\n" \
  "v_mov_b64 v[236:237], 0\n" \
  "v_mov_b64 v[238:239], 0\n" \
  "s_mov_b32 %[st], 0\n" \
  "s_barrier\n" \
  "buffer_load_dwordx4 v[192:195], v241, %[rs], %[se] offen offset:0 sc1\n" \
  "s_nop 11\n" \
  "buffer_load_dwordx4 v[196:199], v241, %[rs], %[se] offen offset:1024 sc1\n" \
  "s_nop 11\n" \
  "buffer_load_dwordx4 v[200:203], v241, %[rs], %[se] offen offset:2048 sc1\n" \
  "buffer_load_dwordx4 v[204:207], v241, %[rs], %[se] offen offset:3072 sc1\n" \
  "s_waitcnt vmcnt(3)\n" \
  "v_smfmac_f32_16x16x64_bf16 v[208:211], v[192:195], %[b00], v243\n" \
  "v_smfmac_f32_16x16x64_bf16 v[224:227], v[192:195], %[b10], v243\n" \
  "v_max3_u32 v240, v192, v192, v193\n" \
  "v_max3_u32 v240, v240, v194, v195\n" \
  "s_waitcnt vmcnt(2)\n" \
  "v_smfmac_f32_16x16x64_bf16 v[212:215], v[196:199], %[b01], v243\n" \
  "v_smfmac_f32_16x16x64_bf16 v[228:231], v[196:199], %[b11], v243\n" \
  "v_max3_u32 v240, v240, v196, v197\n" \
  "v_max3_u32 v240, v240, v198, v199\n" \
  "v_cvt_pk_bf16_f32 v208, v208, v209\n" \
  "v_cvt_pk_bf16_f32 v209, v210, v211\n" \
  "v_cvt_pk_bf16_f32 v224, v224, v225\n" \
  "v_cvt_pk_bf16_f32 v225, v226, v227\n" \
  "v_cvt_pk_bf16_f32 v210, v212, v213\n" \
  "v_cvt_pk_bf16_f32 v211, v214, v215\n" \
  "ds_write_b128 v242, v[208:211] offset:0\n" \
  "v_cvt_pk_bf16_f32 v226, v228, v229\n" \
  "v_cvt_pk_bf16_f32 v227, v230, v231\n" \
  "ds_write_b128 v242, v[224:227] offset:8192\n" \
  "s_waitcnt vmcnt(1)\n" \
  "v_smfmac_f32_16x16x64_bf16 v[216:219], v[200:203], %[b02], v243\n" \
  "v_smfmac_f32_16x16x64_bf16 v[232:235], v[200:203], %[b12], v243\n" \
  "s_waitcnt vmcnt(0)\n" \
  "v_smfmac_f32_16x16x64_bf16 v[220:223], v[204:207], %[b03], v243\n" \
  "v_smfmac_f32_16x16x64_bf16 v[236:239], v[204:207], %[b13], v243\n" \
  "v_cvt_pk_bf16_f32 v216, v216, v217\n" \
  "v_cvt_pk_bf16_f32 v217, v218, v219\n" \
  "v_cvt_pk_bf16_f32 v232, v232, v233\n" \
  "v_cvt_pk_bf16_f32 v233, v234, v235\n" \
  "s_nop 0\n" \
  "v_cvt_pk_bf16_f32 v218, v220, v221\n" \
  "v_cvt_pk_bf16_f32 v219, v222, v223\n" \
  "ds_write_b128 v242, v[216:219] offset:1024\n" \
  "s_nop 0\n" \
  "v_cvt_pk_bf16_f32 v234, v236, v237\n" \
  "v_cvt_pk_bf16_f32 v235, v238, v239\n" \
  "ds_write_b128 v242, v[232:235] offset:9216\n" \
  "v_max3_u32 v240, v240, v200, v201\n" \
  "v_max3_u32 v240, v240, v202, v203\n" \
  "v_max3_u32 v240, v240, v204, v205\n" \
  "v_max3_u32 v240, v240, v206, v207\n" \
  "v_cmp_eq_u32 vcc, -1, v240\n" \
  "s_cbranch_vccnz .Lbad_%=\n" \
  "s_waitcnt lgkmcnt(0)\n" \
  "s_barrier\n" \
  ".Lend_%=:\n"
#define ST_Y \
  "s_mov_b32 %[keep], m0\n" \
  "ds_read_b128 v[240:243], v250 offset:0\n" \
  "ds_read_b128 v[244:247], v250 offset:1024\n" \
  "v_mfma_f32_16x16x32_bf16 v[192:195], v[0:3], v[208:211], 0\n" \
  "v_mfma_f32_16x16x32_bf16 v[196:199], v[64:67], v[208:211], 0\n" \
  "ds_read_b128 v[208:211], v252 offset:8192\n" \
  "v_mfma_f32_16x16x32_bf16 v[200:203], v[0:3], v[212:215], 0\n" \
  "v_mfma_f32_16x16x32_bf16 v[204:207], v[64:67], v[212:215], 0\n" \
  "ds_read_b128 v[212:215], v252 offset:9216\n" \
  "v_mfma_f32_16x16x32_bf16 v[192:195], v[128:131], v[216:219], v[192:195]\n" \
  "v_mfma_f32_16x16x32_bf16 v[196:199], v[132:135], v[216:219], v[196:199]\n" \
  "ds_read_b128 v[216:219], v252 offset:10240\n" \
  "v_mfma_f32_16x16x32_bf16 v[200:203], v[128:131], v[220:223], v[200:203]\n" \
  "v_mfma_f32_16x16x32_bf16 v[204:207], v[132:135], v[220:223], v[204:207]\n" \
  "ds_read_b128 v[220:223], v252 offset:11264\n" \
  "v_mfma_f32_16x16x32_bf16 v[192:195], v[4:7], v[224:227], v[192:195]\n" \
  "v_mfma_f32_16x16x32_bf16 v[196:199], v[68:71], v[224:227], v[196:199]\n" \
  "ds_read_b128 v[224:227], v252 offset:12288\n" \
  "v_mfma_f32_16x16x32_bf16 v[200:203], v[4:7], v[228:231], v[200:203]\n" \
  "v_mfma_f32_16x16x32_bf16 v[204:207], v[68:71], v[228:231], v[204:207]\n" \
  "ds_read_b128 v[228:231], v252 offset:13312\n" \
  "s_waitcnt lgkmcnt(7)\n" \
  "v_mfma_f32_16x16x32_bf16 v[192:195], v[240:243], v[232:235], v[192:195]\n" \
  "s_waitcnt lgkmcnt(6)\n" \
  "v_mfma_f32_16x16x32_bf16 v[196:199], v[244:247], v[232:235], v[196:199]\n" \
  "ds_read_b128 v[232:235], v252 offset:14336\n" \
  "v_mfma_f32_16x16x32_bf16 v[200:203], v[240:243], v[236:239], v[200:203]\n" \
  "v_mfma_f32_16x16x32_bf16 v[204:207], v[244:247], v[236:239], v[204:207]\n" \
  "ds_read_b128 v[236:239], v252 offset:15360\n" \
  "ds_read_b128 v[240:243], v250 offset:16384\n" \
  "ds_read_b128 v[244:247], v250 offset:17408\n" \
  "s_waitcnt lgkmcnt(9)\n" \
  "v_mfma_f32_16x16x32_bf16 v[192:195], v[8:11], v[208:211], v[192:195]\n" \
  "v_mfma_f32_16x16x32_bf16 v[196:199], v[72:75], v[208:211], v[196:199]\n" \
  "s_waitcnt lgkmcnt(8)\n" \
  "v_mfma_f32_16x16x32_bf16 v[200:203], v[8:11], v[212:215], v[200:203]\n" \
  "v_mfma_f32_16x16x32_bf16 v[204:207], v[72:75], v[212:215], v[204:207]\n" \
  "s_waitcnt vmcnt(2)\n" \
  "s_waitcnt lgkmcnt(0)\n" \
  "s_barrier\n" \
  "ds_read_b128 v[208:211], v252 offset:16384\n" \
  "ds_read_b128 v[212:215], v252 offset:17408\n" \
  "s_add_u32 m0, %[mw], 0\n" \
  "s_xor_b32 %[cnt], %[sc], 512\n" \
  "buffer_load_dwordx4 v248, %[rx], %[cnt] offen lds\n" \
  "s_add_u32 m0, %[mw], 8192\n" \
  "s_xor_b32 %[cnt], %[sc], 640\n" \
  "buffer_load_dwordx4 v248, %[rx], %[cnt] offen lds\n" \
  "v_mfma_f32_16x16x32_bf16 v[192:195], v[136:139], v[216:219], v[192:195]\n" \
  "v_mfma_f32_16x16x32_bf16 v[196:199], v[140:143], v[216:219], v[196:199]\n" \
  "ds_read_b128 v[216:219], v252 offset:18432\n" \
  "v_mfma_f32_16x16x32_bf16 v[200:203], v[136:139], v[220:223], v[200:203]\n" \
  "v_mfma_f32_16x16x32_bf16 v[204:207], v[140:143], v[220:223], v[204:207]\n" \
  "ds_read_b128 v[220:223], v252 offset:19456\n" \
  "v_mfma_f32_16x16x32_bf16 v[192:195], v[12:15], v[224:227], v[192:195]\n" \
  "v_mfma_f32_16x16x32_bf16 v[196:199], v[76:79], v[224:227], v[196:199]\n" \
  "ds_read_b128 v[224:227], v252 offset:20480\n" \
  "v_mfma_f32_16x16x32_bf16 v[200:203], v[12:15], v[228:231], v[200:203]\n" \
  "v_mfma_f32_16x16x32_bf16 v[204:207], v[76:79], v[228:231], v[204:207]\n" \
  "ds_read_b128 v[228:231], v252 offset:21504\n" \
  "v_mfma_f32_16x16x32_bf16 v[192:195], v[240:243], v[232:235], v[192:195]\n" \
  "v_mfma_f32_16x16x32_bf16 v[196:199], v[244:247], v[232:235], v[196:199]\n" \
  "ds_read_b128 v[232:235], v252 offset:22528\n" \
  "v_mfma_f32_16x16x32_bf16 v[200:203], v[240:243], v[236:239], v[200:203]\n" \
  "v_mfma_f32_16x16x32_bf16 v[204:207], v[244:247], v[236:239], v[204:207]\n" \
  "ds_read_b128 v[236:239], v252 offset:23552\n" \
  "ds_read_b128 v[240:243], v250 offset:32768\n" \
  "ds_read_b128 v[244:247], v250 offset:33792\n" \
  "s_waitcnt lgkmcnt(9)\n" \
  "v_mfma_f32_16x16x32_bf16 v[192:195], v[16:19], v[208:211], v[192:195]\n" \
  "v_mfma_f32_16x16x32_bf16 v[196:199], v[80:83], v[208:211], v[196:199]\n" \
  "ds_read_b128 v[208:211], v252 offset:24576\n" \
  "s_waitcnt lgkmcnt(9)\n" \
  "v_mfma_f32_16x16x32_bf16 v[200:203], v[16:19], v[212:215], v[200:203]\n" \
  "v_mfma_f32_16x16x32_bf16 v[204:207], v[80:83], v[212:215], v[204:207]\n" \
  "ds_read_b128 v[212:215], v252 offset:25600\n" \
  "s_waitcnt lgkmcnt(9)\n" \
  "v_mfma_f32_16x16x32_bf16 v[192:195], v[144:147], v[216:219], v[192:195]\n" \
  "v_mfma_f32_16x16x32_bf16 v[196:199], v[148:151], v[216:219], v[196:199]\n" \
  "ds_read_b128 v[216:219], v252 offset:26624\n" \
  "s_waitcnt lgkmcnt(9)\n" \
  "v_mfma_f32_16x16x32_bf16 v[200:203], v[144:147], v[220:223], v[200:203]\n" \
  "v_mfma_f32_16x16x32_bf16 v[204:207], v[148:151], v[220:223], v[204:207]\n" \
  "ds_read_b128 v[220:223], v252 offset:27648\n" \
  "s_waitcnt lgkmcnt(9)\n" \
  "v_mfma_f32_16x16x32_bf16 v[192:195], v[20:23], v[224:227], v[192:195]\n" \
  "v_mfma_f32_16x16x32_bf16 v[196:199], v[84:87], v[224:227], v[196:199]\n" \
  "ds_read_b128 v[224:227], v252 offset:28672\n" \
  "s_waitcnt lgkmcnt(9)\n" \
  "v_mfma_f32_16x16x32_bf16 v[200:203], v[20:23], v[228:231], v[200:203]\n" \
  "v_mfma_f32_16x16x32_bf16 v[204:207], v[84:87], v[228:231], v[204:207]\n" \
  "ds_read_b128 v[228:231], v252 offset:29696\n" \
  "s_waitcnt lgkmcnt(7)\n" \
  "v_mfma_f32_16x16x32_bf16 v[192:195], v[240:243], v[232:235], v[192:195]\n" \
  "s_waitcnt lgkmcnt(6)\n" \
  "v_mfma_f32_16x16x32_bf16 v[196:199], v[244:247], v[232:235], v[196:199]\n" \
  "ds_read_b128 v[232:235], v252 offset:30720\n" \
  "v_mfma_f32_16x16x32_bf16 v[200:203], v[240:243], v[236:239], v[200:203]\n" \
  "v_mfma_f32_16x16x32_bf16 v[204:207], v[244:247], v[236:239], v[204:207]\n" \
  "ds_read_b128 v[236:239], v252 offset:31744\n" \
  "ds_read_b128 v[240:243], v250 offset:49152\n" \
  "ds_read_b128 v[244:247], v250 offset:50176\n" \
  "s_waitcnt lgkmcnt(9)\n" \
  "v_mfma_f32_16x16x32_bf16 v[192:195], v[24:27], v[208:211], v[192:195]\n" \
  "v_mfma_f32_16x16x32_bf16 v[196:199], v[88:91], v[208:211], v[196:199]\n" \
  "s_waitcnt lgkmcnt(8)\n" \
  "v_mfma_f32_16x16x32_bf16 v[200:203], v[24:27], v[212:215], v[200:203]\n" \
  "v_mfma_f32_16x16x32_bf16 v[204:207], v[88:91], v[212:215], v[204:207]\n" \
  "s_waitcnt vmcnt(0)\n" \
  "s_waitcnt lgkmcnt(0)\n" \
  "s_barrier\n" \
  "ds_read_b128 v[208:211], v252 offset:0\n" \
  "ds_read_b128 v[212:215], v252 offset:1024\n" \
  "s_add_u32 m0, %[mw], 16384\n" \
  "s_xor_b32 %[cnt], %[sc], 768\n" \
  "buffer_load_dwordx4 v248, %[rx], %[cnt] offen lds\n" \
  "s_add_u32 m0, %[mw], 24576\n" \
  "s_xor_b32 %[cnt], %[sc], 896\n" \
  "buffer_load_dwordx4 v248, %[rx], %[cnt] offen lds\n" \
  "v_lshl_or_b32 v253, v251, 24, %[fm]\n" \
  "buffer_store_dword v253, v253, %[rs], %[fo] offen sc1\n" \
  "v_mfma_f32_16x16x32_bf16 v[192:195], v[152:155], v[216:219], v[192:195]\n" \
  "v_mfma_f32_16x16x32_bf16 v[196:199], v[156:159], v[216:219], v[196:199]\n" \
  "ds_read_b128 v[216:219], v252 offset:2048\n" \
  "v_mfma_f32_16x16x32_bf16 v[200:203], v[152:155], v[220:223], v[200:203]\n" \
  "v_mfma_f32_16x16x32_bf16 v[204:207], v[156:159], v[220:223], v[204:207]\n" \
  "ds_read_b128 v[220:223], v252 offset:3072\n" \
  "v_mfma_f32_16x16x32_bf16 v[192:195], v[28:31], v[224:227], v[192:195]\n" \
  "v_mfma_f32_16x16x32_bf16 v[196:199], v[92:95], v[224:227], v[196:199]\n" \
  "ds_read_b128 v[224:227], v252 offset:4096\n" \
  "v_mfma_f32_16x16x32_bf16 v[200:203], v[28:31], v[228:231], v[200:203]\n" \
  "v_mfma_f32_16x16x32_bf16 v[204:207], v[92:95], v[228:231], v[204:207]\n" \
  "ds_read_b128 v[228:231], v252 offset:5120\n" \
  "v_mfma_f32_16x16x32_bf16 v[192:195], v[240:243], v[232:235], v[192:195]\n" \
  "v_mfma_f32_16x16x32_bf16 v[196:199], v[244:247], v[232:235], v[196:199]\n" \
  "ds_read_b128 v[232:235], v252 offset:6144\n" \
  "v_mfma_f32_16x16x32_bf16 v[200:203], v[240:243], v[236:239], v[200:203]\n" \
  "v_mfma_f32_16x16x32_bf16 v[204:207], v[244:247], v[236:239], v[204:207]\n" \
  "ds_read_b128 v[236:239], v252 offset:7168\n" \
  "ds_read_b128 v[240:243], v249 offset:0\n" \
  "ds_read_b128 v[244:247], v249 offset:1024\n" \
  "s_waitcnt lgkmcnt(9)\n" \
  "v_mfma_f32_16x16x32_bf16 v[192:195], v[32:35], v[208:211], v[192:195]\n" \
  "v_mfma_f32_16x16x32_bf16 v[196:199], v[96:99], v[208:211], v[196:199]\n" \
  "ds_read_b128 v[208:211], v252 offset:8192\n" \
  "s_waitcnt lgkmcnt(9)\n" \
  "v_mfma_f32_16x16x32_bf16 v[200:203], v[32:35], v[212:215], v[200:203]\n" \
  "v_mfma_f32_16x16x32_bf16 v[204:207], v[96:99], v[212:215], v[204:207]\n" \
  "ds_read_b128 v[212:215], v252 offset:9216\n" \
  "s_waitcnt lgkmcnt(9)\n" \
  "v_mfma_f32_16x16x32_bf16 v[192:195], v[160:163], v[216:219], v[192:195]\n" \
  "v_mfma_f32_16x16x32_bf16 v[196:199], v[164:167], v[216:219], v[196:199]\n" \
  "ds_read_b128 v[216:219], v252 offset:10240\n" \
  "s_waitcnt lgkmcnt(9)\n" \
  "v_mfma_f32_16x16x32_bf16 v[200:203], v[160:163], v[220:223], v[200:203]\n" \
  "v_mfma_f32_16x16x32_bf16 v[204:207], v[164:167], v[220:223], v[204:207]\n" \
  "ds_read_b128 v[220:223], v252 offset:11264\n" \
  "s_waitcnt lgkmcnt(9)\n" \
  "v_mfma_f32_16x16x32_bf16 v[192:195], v[36:39], v[224:227], v[192:195]\n" \
  "v_mfma_f32_16x16x32_bf16 v[196:199], v[100:103], v[224:227], v[196:199]\n" \
  "ds_read_b128 v[224:227], v252 offset:12288\n" \
  "s_waitcnt lgkmcnt(9)\n" \
  "v_mfma_f32_16x16x32_bf16 v[200:203], v[36:39], v[228:231], v[200:203]\n" \
  "v_mfma_f32_16x16x32_bf16 v[204:207], v[100:103], v[228:231], v[204:207]\n" \
  "ds_read_b128 v[228:231], v252 offset:13312\n" \
  "s_waitcnt lgkmcnt(7)\n" \
  "v_mfma_f32_16x16x32_bf16 v[192:195], v[240:243], v[232:235], v[192:195]\n" \
  "s_waitcnt lgkmcnt(6)\n" \
  "v_mfma_f32_16x16x32_bf16 v[196:199], v[244:247], v[232:235], v[196:199]\n" \
  "ds_read_b128 v[232:235], v252 offset:14336\n" \
  "v_mfma_f32_16x16x32_bf16 v[200:203], v[240:243], v[236:239], v[200:203]\n" \
  "v_mfma_f32_16x16x32_bf16 v[204:207], v[244:247], v[236:239], v[204:207]\n" \
  "ds_read_b128 v[236:239], v252 offset:15360\n" \
  "ds_read_b128 v[240:243], v249 offset:16384\n" \
  "ds_read_b128 v[244:247], v249 offset:17408\n" \
  "s_waitcnt lgkmcnt(9)\n" \
  "v_mfma_f32_16x16x32_bf16 v[192:195], v[40:43], v[208:211], v[192:195]\n" \
  "v_mfma_f32_16x16x32_bf16 v[196:199], v[104:107], v[208:211], v[196:199]\n" \
  "s_waitcnt lgkmcnt(8)\n" \
  "v_mfma_f32_16x16x32_bf16 v[200:203], v[40:43], v[212:215], v[200:203]\n" \
  "v_mfma_f32_16x16x32_bf16 v[204:207], v[104:107], v[212:215], v[204:207]\n" \
  "s_waitcnt vmcnt(1)\n" \
  "s_waitcnt lgkmcnt(0)\n" \
  "s_barrier\n" \
  "ds_read_b128 v[208:211], v252 offset:16384\n" \
  "ds_read_b128 v[212:215], v252 offset:17408\n" \
  "s_add_u32 m0, %[mw], 0\n" \
  "s_xor_b32 %[cnt], %[sn], 0\n" \
  "buffer_load_dwordx4 v248, %[rx], %[cnt] offen lds\n" \
  "s_add_u32 m0, %[mw], 8192\n" \
  "s_xor_b32 %[cnt], %[sn], 128\n" \
  "buffer_load_dwordx4 v248, %[rx], %[cnt] offen lds\n" \
  "v_lshl_or_b32 v253, v251, 3, %[tm]\n" \
  "buffer_load_dword v253, v253, %[rx], %[tc] offen\n" \
  "v_mfma_f32_16x16x32_bf16 v[192:195], v[168:171], v[216:219], v[192:195]\n" \
  "v_mfma_f32_16x16x32_bf16 v[196:199], v[172:175], v[216:219], v[196:199]\n" \
  "ds_read_b128 v[216:219], v252 offset:18432\n" \
  "v_mfma_f32_16x16x32_bf16 v[200:203], v[168:171], v[220:223], v[200:203]\n" \
  "v_mfma_f32_16x16x32_bf16 v[204:207], v[172:175], v[220:223], v[204:207]\n" \
  "ds_read_b128 v[220:223], v252 offset:19456\n" \
  "v_mfma_f32_16x16x32_bf16 v[192:195], v[44:47], v[224:227], v[192:195]\n" \
  "v_mfma_f32_16x16x32_bf16 v[196:199], v[108:111], v[224:227], v[196:199]\n" \
  "ds_read_b128 v[224:227], v252 offset:20480\n" \
  "v_mfma_f32_16x16x32_bf16 v[200:203], v[44:47], v[228:231], v[200:203]\n" \
  "v_mfma_f32_16x16x32_bf16 v[204:207], v[108:111], v[228:231], v[204:207]\n" \
  "ds_read_b128 v[228:231], v252 offset:21504\n" \
  "v_mfma_f32_16x16x32_bf16 v[192:195], v[240:243], v[232:235], v[192:195]\n" \
  "v_mfma_f32_16x16x32_bf16 v[196:199], v[244:247], v[232:235], v[196:199]\n" \
  "ds_read_b128 v[232:235], v252 offset:22528\n" \
  "v_mfma_f32_16x16x32_bf16 v[200:203], v[240:243], v[236:239], v[200:203]\n" \
  "v_mfma_f32_16x16x32_bf16 v[204:207], v[244:247], v[236:239], v[204:207]\n" \
  "ds_read_b128 v[236:239], v252 offset:23552\n" \
  "ds_read_b128 v[240:243], v249 offset:32768\n" \
  "ds_read_b128 v[244:247], v249 offset:33792\n" \
  "s_waitcnt lgkmcnt(9)\n" \
  "v_mfma_f32_16x16x32_bf16 v[192:195], v[48:51], v[208:211], v[192:195]\n" \
  "v_mfma_f32_16x16x32_bf16 v[196:199], v[112:115], v[208:211], v[196:199]\n" \
  "ds_read_b128 v[208:211], v252 offset:24576\n" \
  "s_waitcnt lgkmcnt(9)\n" \
  "v_mfma_f32_16x16x32_bf16 v[200:203], v[48:51], v[212:215], v[200:203]\n" \
  "v_mfma_f32_16x16x32_bf16 v[204:207], v[112:115], v[212:215], v[204:207]\n" \
  "ds_read_b128 v[212:215], v252 offset:25600\n" \
  "s_waitcnt lgkmcnt(9)\n" \
  "v_mfma_f32_16x16x32_bf16 v[192:195], v[176:179], v[216:219], v[192:195]\n" \
  "v_mfma_f32_16x16x32_bf16 v[196:199], v[180:183], v[216:219], v[196:199]\n" \
  "ds_read_b128 v[216:219], v252 offset:26624\n" \
  "s_waitcnt lgkmcnt(9)\n" \
  "v_mfma_f32_16x16x32_bf16 v[200:203], v[176:179], v[220:223], v[200:203]\n" \
  "v_mfma_f32_16x16x32_bf16 v[204:207], v[180:183], v[220:223], v[204:207]\n" \
  "ds_read_b128 v[220:223], v252 offset:27648\n" \
  "s_waitcnt lgkmcnt(9)\n" \
  "v_mfma_f32_16x16x32_bf16 v[192:195], v[52:55], v[224:227], v[192:195]\n" \
  "v_mfma_f32_16x16x32_bf16 v[196:199], v[116:119], v[224:227], v[196:199]\n" \
  "ds_read_b128 v[224:227], v252 offset:28672\n" \
  "s_waitcnt lgkmcnt(9)\n" \
  "v_mfma_f32_16x16x32_bf16 v[200:203], v[52:55], v[228:231], v[200:203]\n" \
  "v_mfma_f32_16x16x32_bf16 v[204:207], v[116:119], v[228:231], v[204:207]\n" \
  "ds_read_b128 v[228:231], v252 offset:29696\n" \
  "s_waitcnt lgkmcnt(7)\n" \
  "v_mfma_f32_16x16x32_bf16 v[192:195], v[240:243], v[232:235], v[192:195]\n" \
  "s_waitcnt lgkmcnt(6)\n" \
  "v_mfma_f32_16x16x32_bf16 v[196:199], v[244:247], v[232:235], v[196:199]\n" \
  "ds_read_b128 v[232:235], v252 offset:30720\n" \
  "v_mfma_f32_16x16x32_bf16 v[200:203], v[240:243], v[236:239], v[200:203]\n" \
  "v_mfma_f32_16x16x32_bf16 v[204:207], v[244:247], v[236:239], v[204:207]\n" \
  "ds_read_b128 v[236:239], v252 offset:31744\n" \
  "ds_read_b128 v[240:243], v249 offset:49152\n" \
  "ds_read_b128 v[244:247], v249 offset:50176\n" \
  "s_waitcnt lgkmcnt(9)\n" \
  "v_mfma_f32_16x16x32_bf16 v[192:195], v[56:59], v[208:211], v[192:195]\n" \
  "v_mfma_f32_16x16x32_bf16 v[196:199], v[120:123], v[208:211], v[196:199]\n" \
  "s_waitcnt lgkmcnt(8)\n" \
  "v_mfma_f32_16x16x32_bf16 v[200:203], v[56:59], v[212:215], v[200:203]\n" \
  "v_mfma_f32_16x16x32_bf16 v[204:207], v[120:123], v[212:215], v[204:207]\n" \
  "s_waitcnt vmcnt(1)\n" \
  "s_waitcnt lgkmcnt(0)\n" \
  "s_barrier\n" \
  "ds_read_b128 v[208:211], v252 offset:0\n" \
  "ds_read_b128 v[212:215], v252 offset:1024\n" \
  "s_add_u32 m0, %[mw], 16384\n" \
  "s_xor_b32 %[cnt], %[sn], 256\n" \
  "buffer_load_dwordx4 v248, %[rx], %[cnt] offen lds\n" \
  "s_add_u32 m0, %[mw], 24576\n" \
  "s_xor_b32 %[cnt], %[sn], 384\n" \
  "buffer_load_dwordx4 v248, %[rx], %[cnt] offen lds\n" \
  "v_mfma_f32_16x16x32_bf16 v[192:195], v[184:187], v[216:219], v[192:195]\n" \
  "v_mfma_f32_16x16x32_bf16 v[196:199], v[188:191], v[216:219], v[196:199]\n" \
  "ds_read_b128 v[216:219], v252 offset:2048\n" \
  "v_mfma_f32_16x16x32_bf16 v[200:203], v[184:187], v[220:223], v[200:203]\n" \
  "v_mfma_f32_16x16x32_bf16 v[204:207], v[188:191], v[220:223], v[204:207]\n" \
  "ds_read_b128 v[220:223], v252 offset:3072\n" \
  "v_mfma_f32_16x16x32_bf16 v[192:195], v[60:63], v[224:227], v[192:195]\n" \
  "v_mfma_f32_16x16x32_bf16 v[196:199], v[124:127], v[224:227], v[196:199]\n" \
  "ds_read_b128 v[224:227], v252 offset:4096\n" \
  "v_mfma_f32_16x16x32_bf16 v[200:203], v[60:63], v[228:231], v[200:203]\n" \
  "v_mfma_f32_16x16x32_bf16 v[204:207], v[124:127], v[228:231], v[204:207]\n" \
  "ds_read_b128 v[228:231], v252 offset:5120\n" \
  "v_mfma_f32_16x16x32_bf16 v[192:195], v[240:243], v[232:235], v[192:195]\n" \
  "v_mfma_f32_16x16x32_bf16 v[196:199], v[244:247], v[232:235], v[196:199]\n" \
  "ds_read_b128 v[232:235], v252 offset:6144\n" \
  "v_mfma_f32_16x16x32_bf16 v[200:203], v[240:243], v[236:239], v[200:203]\n" \
  "v_mfma_f32_16x16x32_bf16 v[204:207], v[244:247], v[236:239], v[204:207]\n" \
  "ds_read_b128 v[236:239], v252 offset:7168\n" \
  "v_cvt_pk_bf16_f32 v192, v192, v193\n" \
  "v_cvt_pk_bf16_f32 v193, v194, v195\n" \
  "v_cvt_pk_bf16_f32 v194, v196, v197\n" \
  "v_cvt_pk_bf16_f32 v195, v198, v199\n" \
  "buffer_store_dwordx4 v[192:195], v251, %[rs], %[so] offen sc1\n" \
  "v_cvt_pk_bf16_f32 v200, v200, v201\n" \
  "v_cvt_pk_bf16_f32 v201, v202, v203\n" \
  "s_nop 0\n" \
  "v_cvt_pk_bf16_f32 v202, v204, v205\n" \
  "v_cvt_pk_bf16_f32 v203, v206, v207\n" \
  "s_add_u32 %[cnt], %[so], %c[rt]\n" \
  "buffer_store_dwordx4 v[200:203], v251, %[rs], %[cnt] offen sc1\n" \
  "s_mov_b32 m0, %[keep]\n" \
  "s_waitcnt lgkmcnt(0)\n"
#define ST_XA \
  "s_mov_b32 %[keep], m0\n" \
  "ds_read_b128 v[240:243], v250 offset:0\n" \
  "ds_read_b128 v[244:247], v250 offset:1024\n" \
  "v_mfma_f32_16x16x32_bf16 v[192:195], v[0:3], v[208:211], 0\n" \
  "v_mfma_f32_16x16x32_bf16 v[196:199], v[64:67], v[208:211], 0\n" \
  "ds_read_b128 v[208:211], v252 offset:8192\n" \
  "v_mfma_f32_16x16x32_bf16 v[200:203], v[0:3], v[212:215], 0\n" \
  "v_mfma_f32_16x16x32_bf16 v[204:207], v[64:67], v[212:215], 0\n" \
  "ds_read_b128 v[212:215], v252 offset:9216\n" \
  "v_mfma_f32_16x16x32_bf16 v[192:195], v[128:131], v[216:219], v[192:195]\n" \
  "v_mfma_f32_16x16x32_bf16 v[196:199], v[132:135], v[216:219], v[196:199]\n" \
  "ds_read_b128 v[216:219], v252 offset:10240\n" \
  "v_mfma_f32_16x16x32_bf16 v[200:203], v[128:131], v[220:223], v[200:203]\n" \
  "v_mfma_f32_16x16x32_bf16 v[204:207], v[132:135], v[220:223], v[204:207]\n" \
  "ds_read_b128 v[220:223], v252 offset:11264\n" \
  "v_mfma_f32_16x16x32_bf16 v[192:195], v[4:7], v[224:227], v[192:195]\n" \
  "v_mfma_f32_16x16x32_bf16 v[196:199], v[68:71], v[224:227], v[196:199]\n" \
  "v_mfma_f32_16x16x32_bf16 v[200:203], v[4:7], v[228:231], v[200:203]\n" \
  "v_mfma_f32_16x16x32_bf16 v[204:207], v[68:71], v[228:231], v[204:207]\n" \
  "s_waitcnt vmcnt(0)\n" \
  "s_waitcnt lgkmcnt(0)\n" \
  "s_barrier\n" \
  "ds_read_b128 v[224:227], v252 offset:12288\n" \
  "ds_read_b128 v[228:231], v252 offset:13312\n" \
  "s_add_u32 m0, %[mw], 0\n" \
  "s_xor_b32 %[cnt], %[sc], 384\n" \
  "buffer_load_dwordx4 v248, %[rx], %[cnt] offen lds\n" \
  "s_add_u32 m0, %[mw], 4096\n" \
  "s_xor_b32 %[cnt], %[sc], 448\n" \
  "buffer_load_dwordx4 v248, %[rx], %[cnt] offen lds\n" \
  "v_mfma_f32_16x16x32_bf16 v[192:195], v[240:243], v[232:235], v[192:195]\n" \
  "v_mfma_f32_16x16x32_bf16 v[196:199], v[244:247], v[232:235], v[196:199]\n" \
  "ds_read_b128 v[232:235], v252 offset:14336\n" \
  "v_mfma_f32_16x16x32_bf16 v[200:203], v[240:243], v[236:239], v[200:203]\n" \
  "v_mfma_f32_16x16x32_bf16 v[204:207], v[244:247], v[236:239], v[204:207]\n" \
  "ds_read_b128 v[236:239], v252 offset:15360\n" \
  "ds_read_b128 v[240:243], v250 offset:16384\n" \
  "ds_read_b128 v[244:247], v250 offset:17408\n" \
  "v_mfma_f32_16x16x32_bf16 v[192:195], v[8:11], v[208:211], v[192:195]\n" \
  "v_mfma_f32_16x16x32_bf16 v[196:199], v[72:75], v[208:211], v[196:199]\n" \
  "ds_read_b128 v[208:211], v252 offset:16384\n" \
  "v_mfma_f32_16x16x32_bf16 v[200:203], v[8:11], v[212:215], v[200:203]\n" \
  "v_mfma_f32_16x16x32_bf16 v[204:207], v[72:75], v[212:215], v[204:207]\n" \
  "ds_read_b128 v[212:215], v252 offset:17408\n" \
  "v_mfma_f32_16x16x32_bf16 v[192:195], v[136:139], v[216:219], v[192:195]\n" \
  "v_mfma_f32_16x16x32_bf16 v[196:199], v[140:143], v[216:219], v[196:199]\n" \
  "ds_read_b128 v[216:219], v252 offset:18432\n" \
  "v_mfma_f32_16x16x32_bf16 v[200:203], v[136:139], v[220:223], v[200:203]\n" \
  "v_mfma_f32_16x16x32_bf16 v[204:207], v[140:143], v[220:223], v[204:207]\n" \
  "ds_read_b128 v[220:223], v252 offset:19456\n" \
  "s_waitcnt lgkmcnt(9)\n" \
  "v_mfma_f32_16x16x32_bf16 v[192:195], v[12:15], v[224:227], v[192:195]\n" \
  "v_mfma_f32_16x16x32_bf16 v[196:199], v[76:79], v[224:227], v[196:199]\n" \
  "ds_read_b128 v[224:227], v252 offset:20480\n" \
  "s_waitcnt lgkmcnt(9)\n" \
  "v_mfma_f32_16x16x32_bf16 v[200:203], v[12:15], v[228:231], v[200:203]\n" \
  "v_mfma_f32_16x16x32_bf16 v[204:207], v[76:79], v[228:231], v[204:207]\n" \
  "ds_read_b128 v[228:231], v252 offset:21504\n" \
  "s_waitcnt lgkmcnt(7)\n" \
  "v_mfma_f32_16x16x32_bf16 v[192:195], v[240:243], v[232:235], v[192:195]\n" \
  "s_waitcnt lgkmcnt(6)\n" \
  "v_mfma_f32_16x16x32_bf16 v[196:199], v[244:247], v[232:235], v[196:199]\n" \
  "ds_read_b128 v[232:235], v252 offset:22528\n" \
  "v_mfma_f32_16x16x32_bf16 v[200:203], v[240:243], v[236:239], v[200:203]\n" \
  "v_mfma_f32_16x16x32_bf16 v[204:207], v[244:247], v[236:239], v[204:207]\n" \
  "ds_read_b128 v[236:239], v252 offset:23552\n" \
  "ds_read_b128 v[240:243], v250 offset:32768\n" \
  "ds_read_b128 v[244:247], v250 offset:33792\n" \
  "s_waitcnt lgkmcnt(9)\n" \
  "v_mfma_f32_16x16x32_bf16 v[192:195], v[16:19], v[208:211], v[192:195]\n" \
  "v_mfma_f32_16x16x32_bf16 v[196:199], v[80:83], v[208:211], v[196:199]\n" \
  "s_waitcnt lgkmcnt(8)\n" \
  "v_mfma_f32_16x16x32_bf16 v[200:203], v[16:19], v[212:215], v[200:203]\n" \
  "v_mfma_f32_16x16x32_bf16 v[204:207], v[80:83], v[212:215], v[204:207]\n" \
  "s_waitcnt vmcnt(0)\n" \
  "s_waitcnt lgkmcnt(0)\n" \
  "s_barrier\n" \
  "ds_read_b128 v[208:211], v252 offset:0\n" \
  "ds_read_b128 v[212:215], v252 offset:1024\n" \
  "s_add_u32 m0, %[mw], 12288\n" \
  "s_xor_b32 %[cnt], %[sc], 512\n" \
  "buffer_load_dwordx4 v248, %[rx], %[cnt] offen lds\n" \
  "s_add_u32 m0, %[mw], 16384\n" \
  "s_xor_b32 %[cnt], %[sc], 576\n" \
  "buffer_load_dwordx4 v248, %[rx], %[cnt] offen lds\n" \
  "s_add_u32 m0, %[mw], 20480\n" \
  "s_xor_b32 %[cnt], %[sc], 640\n" \
  "buffer_load_dwordx4 v248, %[rx], %[cnt] offen lds\n" \
  "v_mfma_f32_16x16x32_bf16 v[192:195], v[144:147], v[216:219], v[192:195]\n" \
  "v_mfma_f32_16x16x32_bf16 v[196:199], v[148:151], v[216:219], v[196:199]\n" \
  "ds_read_b128 v[216:219], v252 offset:2048\n" \
  "v_mfma_f32_16x16x32_bf16 v[200:203], v[144:147], v[220:223], v[200:203]\n" \
  "v_mfma_f32_16x16x32_bf16 v[204:207], v[148:151], v[220:223], v[204:207]\n" \
  "ds_read_b128 v[220:223], v252 offset:3072\n" \
  "v_mfma_f32_16x16x32_bf16 v[192:195], v[20:23], v[224:227], v[192:195]\n" \
  "v_mfma_f32_16x16x32_bf16 v[196:199], v[84:87], v[224:227], v[196:199]\n" \
  "ds_read_b128 v[224:227], v252 offset:4096\n" \
  "v_mfma_f32_16x16x32_bf16 v[200:203], v[20:23], v[228:231], v[200:203]\n" \
  "v_mfma_f32_16x16x32_bf16 v[204:207], v[84:87], v[228:231], v[204:207]\n" \
  "ds_read_b128 v[228:231], v252 offset:5120\n" \
  "v_mfma_f32_16x16x32_bf16 v[192:195], v[240:243], v[232:235], v[192:195]\n" \
  "v_mfma_f32_16x16x32_bf16 v[196:199], v[244:247], v[232:235], v[196:199]\n" \
  "ds_read_b128 v[232:235], v252 offset:6144\n" \
  "v_mfma_f32_16x16x32_bf16 v[200:203], v[240:243], v[236:239], v[200:203]\n" \
  "v_mfma_f32_16x16x32_bf16 v[204:207], v[244:247], v[236:239], v[204:207]\n" \
  "ds_read_b128 v[236:239], v252 offset:7168\n" \
  "ds_read_b128 v[240:243], v250 offset:49152\n" \
  "ds_read_b128 v[244:247], v250 offset:50176\n" \
  "s_waitcnt lgkmcnt(9)\n" \
  "v_mfma_f32_16x16x32_bf16 v[192:195], v[24:27], v[208:211], v[192:195]\n" \
  "v_mfma_f32_16x16x32_bf16 v[196:199], v[88:91], v[208:211], v[196:199]\n" \
  "s_waitcnt lgkmcnt(8)\n" \
  "v_mfma_f32_16x16x32_bf16 v[200:203], v[24:27], v[212:215], v[200:203]\n" \
  "v_mfma_f32_16x16x32_bf16 v[204:207], v[88:91], v[212:215], v[204:207]\n" \
  "s_waitcnt vmcnt(0)\n" \
  "s_waitcnt lgkmcnt(0)\n" \
  "s_barrier\n" \
  "ds_read_b128 v[208:211], v252 offset:12288\n" \
  "ds_read_b128 v[212:215], v252 offset:13312\n" \
  "s_add_u32 m0, %[mw], 0\n" \
  "s_xor_b32 %[cnt], %[sc], 704\n" \
  "buffer_load_dwordx4 v248, %[rx], %[cnt] offen lds\n" \
  "s_add_u32 m0, %[mw], 4096\n" \
  "s_xor_b32 %[cnt], %[sc], 768\n" \
  "buffer_load_dwordx4 v248, %[rx], %[cnt] offen lds\n" \
  "s_add_u32 m0, %[mw], 8192\n" \
  "s_xor_b32 %[cnt], %[sc], 832\n" \
  "buffer_load_dwordx4 v248, %[rx], %[cnt] offen lds\n" \
  "v_lshl_or_b32 v253, v251, 3, %[tm]\n" \
  "buffer_load_dword v253, v253, %[rx], %[tc] offen\n" \
  "v_mfma_f32_16x16x32_bf16 v[192:195], v[152:155], v[216:219], v[192:195]\n" \
  "v_mfma_f32_16x16x32_bf16 v[196:199], v[156:159], v[216:219], v[196:199]\n" \
  "ds_read_b128 v[216:219], v252 offset:14336\n" \
  "v_mfma_f32_16x16x32_bf16 v[200:203], v[152:155], v[220:223], v[200:203]\n" \
  "v_mfma_f32_16x16x32_bf16 v[204:207], v[156:159], v[220:223], v[204:207]\n" \
  "ds_read_b128 v[220:223], v252 offset:15360\n" \
  "v_mfma_f32_16x16x32_bf16 v[192:195], v[28:31], v[224:227], v[192:195]\n" \
  "v_mfma_f32_16x16x32_bf16 v[196:199], v[92:95], v[224:227], v[196:199]\n" \
  "ds_read_b128 v[224:227], v252 offset:16384\n" \
  "v_mfma_f32_16x16x32_bf16 v[200:203], v[28:31], v[228:231], v[200:203]\n" \
  "v_mfma_f32_16x16x32_bf16 v[204:207], v[92:95], v[228:231], v[204:207]\n" \
  "ds_read_b128 v[228:231], v252 offset:17408\n" \
  "v_mfma_f32_16x16x32_bf16 v[192:195], v[240:243], v[232:235], v[192:195]\n" \
  "v_mfma_f32_16x16x32_bf16 v[196:199], v[244:247], v[232:235], v[196:199]\n" \
  "ds_read_b128 v[232:235], v252 offset:18432\n" \
  "v_mfma_f32_16x16x32_bf16 v[200:203], v[240:243], v[236:239], v[200:203]\n" \
  "v_mfma_f32_16x16x32_bf16 v[204:207], v[244:247], v[236:239], v[204:207]\n" \
  "ds_read_b128 v[236:239], v252 offset:19456\n" \
  "ds_read_b128 v[240:243], v249 offset:0\n" \
  "ds_read_b128 v[244:247], v249 offset:1024\n" \
  "s_waitcnt lgkmcnt(9)\n" \
  "v_mfma_f32_16x16x32_bf16 v[192:195], v[32:35], v[208:211], v[192:195]\n" \
  "v_mfma_f32_16x16x32_bf16 v[196:199], v[96:99], v[208:211], v[196:199]\n" \
  "ds_read_b128 v[208:211], v252 offset:20480\n" \
  "s_waitcnt lgkmcnt(9)\n" \
  "v_mfma_f32_16x16x32_bf16 v[200:203], v[32:35], v[212:215], v[200:203]\n" \
  "v_mfma_f32_16x16x32_bf16 v[204:207], v[96:99], v[212:215], v[204:207]\n" \
  "ds_read_b128 v[212:215], v252 offset:21504\n" \
  "s_waitcnt lgkmcnt(9)\n" \
  "v_mfma_f32_16x16x32_bf16 v[192:195], v[160:163], v[216:219], v[192:195]\n" \
  "v_mfma_f32_16x16x32_bf16 v[196:199], v[164:167], v[216:219], v[196:199]\n" \
  "ds_read_b128 v[216:219], v252 offset:22528\n" \
  "s_waitcnt lgkmcnt(9)\n" \
  "v_mfma_f32_16x16x32_bf16 v[200:203], v[160:163], v[220:223], v[200:203]\n" \
  "v_mfma_f32_16x16x32_bf16 v[204:207], v[164:167], v[220:223], v[204:207]\n" \
  "ds_read_b128 v[220:223], v252 offset:23552\n" \
  "s_waitcnt lgkmcnt(9)\n" \
  "v_mfma_f32_16x16x32_bf16 v[192:195], v[36:39], v[224:227], v[192:195]\n" \
  "v_mfma_f32_16x16x32_bf16 v[196:199], v[100:103], v[224:227], v[196:199]\n" \
  "s_waitcnt lgkmcnt(8)\n" \
  "v_mfma_f32_16x16x32_bf16 v[200:203], v[36:39], v[228:231], v[200:203]\n" \
  "v_mfma_f32_16x16x32_bf16 v[204:207], v[100:103], v[228:231], v[204:207]\n" \
  "s_waitcnt vmcnt(1)\n" \
  "s_waitcnt lgkmcnt(0)\n" \
  "s_barrier\n" \
  "ds_read_b128 v[224:227], v252 offset:0\n" \
  "ds_read_b128 v[228:231], v252 offset:1024\n" \
  "s_add_u32 m0, %[mw], 12288\n" \
  "s_xor_b32 %[cnt], %[sc], 896\n" \
  "buffer_load_dwordx4 v248, %[rx], %[cnt] offen lds\n" \
  "s_add_u32 m0, %[mw], 16384\n" \
  "s_xor_b32 %[cnt], %[sc], 960\n" \
  "buffer_load_dwordx4 v248, %[rx], %[cnt] offen lds\n" \
  "v_mfma_f32_16x16x32_bf16 v[192:195], v[240:243], v[232:235], v[192:195]\n" \
  "v_mfma_f32_16x16x32_bf16 v[196:199], v[244:247], v[232:235], v[196:199]\n" \
  "ds_read_b128 v[232:235], v252 offset:2048\n" \
  "v_mfma_f32_16x16x32_bf16 v[200:203], v[240:243], v[236:239], v[200:203]\n" \
  "v_mfma_f32_16x16x32_bf16 v[204:207], v[244:247], v[236:239], v[204:207]\n" \
  "ds_read_b128 v[236:239], v252 offset:3072\n" \
  "ds_read_b128 v[240:243], v249 offset:16384\n" \
  "ds_read_b128 v[244:247], v249 offset:17408\n" \
  "v_mfma_f32_16x16x32_bf16 v[192:195], v[40:43], v[208:211], v[192:195]\n" \
  "v_mfma_f32_16x16x32_bf16 v[196:199], v[104:107], v[208:211], v[196:199]\n" \
  "ds_read_b128 v[208:211], v252 offset:4096\n" \
  "v_mfma_f32_16x16x32_bf16 v[200:203], v[40:43], v[212:215], v[200:203]\n" \
  "v_mfma_f32_16x16x32_bf16 v[204:207], v[104:107], v[212:215], v[204:207]\n" \
  "ds_read_b128 v[212:215], v252 offset:5120\n" \
  "v_mfma_f32_16x16x32_bf16 v[192:195], v[168:171], v[216:219], v[192:195]\n" \
  "v_mfma_f32_16x16x32_bf16 v[196:199], v[172:175], v[216:219], v[196:199]\n" \
  "ds_read_b128 v[216:219], v252 offset:6144\n" \
  "v_mfma_f32_16x16x32_bf16 v[200:203], v[168:171], v[220:223], v[200:203]\n" \
  "v_mfma_f32_16x16x32_bf16 v[204:207], v[172:175], v[220:223], v[204:207]\n" \
  "ds_read_b128 v[220:223], v252 offset:7168\n" \
  "s_waitcnt lgkmcnt(9)\n" \
  "v_mfma_f32_16x16x32_bf16 v[192:195], v[44:47], v[224:227], v[192:195]\n" \
  "v_mfma_f32_16x16x32_bf16 v[196:199], v[108:111], v[224:227], v[196:199]\n" \
  "ds_read_b128 v[224:227], v252 offset:8192\n" \
  "s_waitcnt lgkmcnt(9)\n" \
  "v_mfma_f32_16x16x32_bf16 v[200:203], v[44:47], v[228:231], v[200:203]\n" \
  "v_mfma_f32_16x16x32_bf16 v[204:207], v[108:111], v[228:231], v[204:207]\n" \
  "ds_read_b128 v[228:231], v252 offset:9216\n" \
  "s_waitcnt lgkmcnt(7)\n" \
  "v_mfma_f32_16x16x32_bf16 v[192:195], v[240:243], v[232:235], v[192:195]\n" \
  "s_waitcnt lgkmcnt(6)\n" \
  "v_mfma_f32_16x16x32_bf16 v[196:199], v[244:247], v[232:235], v[196:199]\n" \
  "ds_read_b128 v[232:235], v252 offset:10240\n" \
  "v_mfma_f32_16x16x32_bf16 v[200:203], v[240:243], v[236:239], v[200:203]\n" \
  "v_mfma_f32_16x16x32_bf16 v[204:207], v[244:247], v[236:239], v[204:207]\n" \
  "ds_read_b128 v[236:239], v252 offset:11264\n" \
  "ds_read_b128 v[240:243], v249 offset:32768\n" \
  "ds_read_b128 v[244:247], v249 offset:33792\n" \
  "s_waitcnt lgkmcnt(9)\n" \
  "v_mfma_f32_16x16x32_bf16 v[192:195], v[48:51], v[208:211], v[192:195]\n" \
  "v_mfma_f32_16x16x32_bf16 v[196:199], v[112:115], v[208:211], v[196:199]\n" \
  "s_waitcnt lgkmcnt(8)\n" \
  "v_mfma_f32_16x16x32_bf16 v[200:203], v[48:51], v[212:215], v[200:203]\n" \
  "v_mfma_f32_16x16x32_bf16 v[204:207], v[112:115], v[212:215], v[204:207]\n" \
  "s_waitcnt vmcnt(0)\n" \
  "s_waitcnt lgkmcnt(0)\n" \
  "s_barrier\n" \
  "ds_read_b128 v[208:211], v252 offset:12288\n" \
  "ds_read_b128 v[212:215], v252 offset:13312\n" \
  "s_add_u32 m0, %[mw], 0\n" \
  "s_xor_b32 %[cnt], %[sn], 0\n" \
  "buffer_load_dwordx4 v248, %[rx], %[cnt] offen lds\n" \
  "s_add_u32 m0, %[mw], 4096\n" \
  "s_xor_b32 %[cnt], %[sn], 64\n" \
  "buffer_load_dwordx4 v248, %[rx], %[cnt] offen lds\n" \
  "s_add_u32 m0, %[mw], 8192\n" \
  "s_xor_b32 %[cnt], %[sn], 128\n" \
  "buffer_load_dwordx4 v248, %[rx], %[cnt] offen lds\n" \
  "v_mfma_f32_16x16x32_bf16 v[192:195], v[176:179], v[216:219], v[192:195]\n" \
  "v_mfma_f32_16x16x32_bf16 v[196:199], v[180:183], v[216:219], v[196:199]\n" \
  "ds_read_b128 v[216:219], v252 offset:14336\n" \
  "v_mfma_f32_16x16x32_bf16 v[200:203], v[176:179], v[220:223], v[200:203]\n" \
  "v_mfma_f32_16x16x32_bf16 v[204:207], v[180:183], v[220:223], v[204:207]\n" \
  "ds_read_b128 v[220:223], v252 offset:15360\n" \
  "v_mfma_f32_16x16x32_bf16 v[192:195], v[52:55], v[224:227], v[192:195]\n" \
  "v_mfma_f32_16x16x32_bf16 v[196:199], v[116:119], v[224:227], v[196:199]\n" \
  "ds_read_b128 v[224:227], v252 offset:16384\n" \
  "v_mfma_f32_16x16x32_bf16 v[200:203], v[52:55], v[228:231], v[200:203]\n" \
  "v_mfma_f32_16x16x32_bf16 v[204:207], v[116:119], v[228:231], v[204:207]\n" \
  "ds_read_b128 v[228:231], v252 offset:17408\n" \
  "v_mfma_f32_16x16x32_bf16 v[192:195], v[240:243], v[232:235], v[192:195]\n" \
  "v_mfma_f32_16x16x32_bf16 v[196:199], v[244:247], v[232:235], v[196:199]\n" \
  "ds_read_b128 v[232:235], v252 offset:18432\n" \
  "v_mfma_f32_16x16x32_bf16 v[200:203], v[240:243], v[236:239], v[200:203]\n" \
  "v_mfma_f32_16x16x32_bf16 v[204:207], v[244:247], v[236:239], v[204:207]\n" \
  "ds_read_b128 v[236:239], v252 offset:19456\n" \
  "ds_read_b128 v[240:243], v249 offset:49152\n" \
  "ds_read_b128 v[244:247], v249 offset:50176\n" \
  "s_waitcnt lgkmcnt(9)\n" \
  "v_mfma_f32_16x16x32_bf16 v[192:195], v[56:59], v[208:211], v[192:195]\n" \
  "v_mfma_f32_16x16x32_bf16 v[196:199], v[120:123], v[208:211], v[196:199]\n" \
  "s_waitcnt lgkmcnt(8)\n" \
  "v_mfma_f32_16x16x32_bf16 v[200:203], v[56:59], v[212:215], v[200:203]\n" \
  "v_mfma_f32_16x16x32_bf16 v[204:207], v[120:123], v[212:215], v[204:207]\n" \
  "s_waitcnt vmcnt(0)\n" \
  "s_waitcnt lgkmcnt(0)\n" \
  "s_barrier\n" \
  "ds_read_b128 v[208:211], v252 offset:0\n" \
  "ds_read_b128 v[212:215], v252 offset:1024\n" \
  "s_add_u32 m0, %[mw], 12288\n" \
  "s_xor_b32 %[cnt], %[sn], 192\n" \
  "buffer_load_dwordx4 v248, %[rx], %[cnt] offen lds\n" \
  "s_add_u32 m0, %[mw], 16384\n" \
  "s_xor_b32 %[cnt], %[sn], 256\n" \
  "buffer_load_dwordx4 v248, %[rx], %[cnt] offen lds\n" \
  "s_add_u32 m0, %[mw], 20480\n" \
  "s_xor_b32 %[cnt], %[sn], 320\n" \
  "buffer_load_dwordx4 v248, %[rx], %[cnt] offen lds\n" \
  "v_mfma_f32_16x16x32_bf16 v[192:195], v[184:187], v[216:219], v[192:195]\n" \
  "v_mfma_f32_16x16x32_bf16 v[196:199], v[188:191], v[216:219], v[196:199]\n" \
  "ds_read_b128 v[216:219], v252 offset:2048\n" \
  "v_mfma_f32_16x16x32_bf16 v[200:203], v[184:187], v[220:223], v[200:203]\n" \
  "v_mfma_f32_16x16x32_bf16 v[204:207], v[188:191], v[220:223], v[204:207]\n" \
  "ds_read_b128 v[220:223], v252 offset:3072\n" \
  "v_mfma_f32_16x16x32_bf16 v[192:195], v[60:63], v[224:227], v[192:195]\n" \
  "v_mfma_f32_16x16x32_bf16 v[196:199], v[124:127], v[224:227], v[196:199]\n" \
  "ds_read_b128 v[224:227], v252 offset:4096\n" \
  "v_mfma_f32_16x16x32_bf16 v[200:203], v[60:63], v[228:231], v[200:203]\n" \
  "v_mfma_f32_16x16x32_bf16 v[204:207], v[124:127], v[228:231], v[204:207]\n" \
  "ds_read_b128 v[228:231], v252 offset:5120\n" \
  "v_mfma_f32_16x16x32_bf16 v[192:195], v[240:243], v[232:235], v[192:195]\n" \
  "v_mfma_f32_16x16x32_bf16 v[196:199], v[244:247], v[232:235], v[196:199]\n" \
  "ds_read_b128 v[232:235], v252 offset:6144\n" \
  "v_mfma_f32_16x16x32_bf16 v[200:203], v[240:243], v[236:239], v[200:203]\n" \
  "v_mfma_f32_16x16x32_bf16 v[204:207], v[244:247], v[236:239], v[204:207]\n" \
  "ds_read_b128 v[236:239], v252 offset:7168\n" \
  "v_cvt_pk_bf16_f32 v192, v192, v193\n" \
  "v_cvt_pk_bf16_f32 v193, v194, v195\n" \
  "v_cvt_pk_bf16_f32 v194, v196, v197\n" \
  "v_cvt_pk_bf16_f32 v195, v198, v199\n" \
  "v_add_u32 v196, %[so], v251\n" \
  "ds_write_b128 v196, v[192:195] offset:24576\n" \
  "v_cvt_pk_bf16_f32 v200, v200, v201\n" \
  "v_cvt_pk_bf16_f32 v201, v202, v203\n" \
  "v_cvt_pk_bf16_f32 v202, v204, v205\n" \
  "v_cvt_pk_bf16_f32 v203, v206, v207\n" \
  "ds_write_b128 v196, v[200:203] offset:28672\n" \
  "s_mov_b32 m0, %[keep]\n" \
  "s_waitcnt lgkmcnt(0)\n"
#define ST_XB \
  "s_barrier\n" \
  "ds_read_b128 v[192:195], v251 offset:24576\n" \
  "ds_read_b128 v[208:211], v251 offset:28672\n" \
  "ds_read_b128 v[196:199], v251 offset:25600\n" \
  "ds_read_b128 v[212:215], v251 offset:29696\n" \
  "ds_read_b128 v[200:203], v251 offset:26624\n" \
  "ds_read_b128 v[216:219], v251 offset:30720\n" \
  "ds_read_b128 v[204:207], v251 offset:27648\n" \
  "ds_read_b128 v[220:223], v251 offset:31744\n" \
  "ds_read_b128 v[240:243], v250 offset:0\n" \
  "ds_read_b128 v[244:247], v250 offset:16384\n" \
  "s_waitcnt lgkmcnt(9)\n" \
  "v_mfma_f32_16x16x32_bf16 v[224:227], v[192:195], v[0:3], 0\n" \
  "s_waitcnt lgkmcnt(8)\n" \
  "v_mfma_f32_16x16x32_bf16 v[228:231], v[208:211], v[0:3], 0\n" \
  "s_waitcnt lgkmcnt(7)\n" \
  "v_mfma_f32_16x16x32_bf16 v[224:227], v[196:199], v[4:7], v[224:227]\n" \
  "s_waitcnt lgkmcnt(6)\n" \
  "v_mfma_f32_16x16x32_bf16 v[228:231], v[212:215], v[4:7], v[228:231]\n" \
  "s_waitcnt lgkmcnt(5)\n" \
  "v_mfma_f32_16x16x32_bf16 v[224:227], v[200:203], v[8:11], v[224:227]\n" \
  "s_waitcnt lgkmcnt(4)\n" \
  "v_mfma_f32_16x16x32_bf16 v[228:231], v[216:219], v[8:11], v[228:231]\n" \
  "s_waitcnt lgkmcnt(3)\n" \
  "v_mfma_f32_16x16x32_bf16 v[224:227], v[204:207], v[12:15], v[224:227]\n" \
  "s_waitcnt lgkmcnt(2)\n" \
  "v_mfma_f32_16x16x32_bf16 v[228:231], v[220:223], v[12:15], v[228:231]\n" \
  "v_mfma_f32_16x16x32_bf16 v[232:235], v[192:195], v[128:131], 0\n" \
  "v_mfma_f32_16x16x32_bf16 v[236:239], v[208:211], v[128:131], 0\n" \
  "v_mfma_f32_16x16x32_bf16 v[232:235], v[200:203], v[136:139], v[232:235]\n" \
  "v_cvt_pk_bf16_f32 v224, v224, v225\n" \
  "v_cvt_pk_bf16_f32 v226, v226, v227\n" \
  "v_mfma_f32_16x16x32_bf16 v[236:239], v[216:219], v[136:139], v[236:239]\n" \
  "v_cvt_pk_bf16_f32 v225, v228, v229\n" \
  "v_cvt_pk_bf16_f32 v227, v230, v231\n" \
  "s_waitcnt lgkmcnt(1)\n" \
  "v_mfma_f32_16x16x32_bf16 v[232:235], v[196:199], v[240:243], v[232:235]\n" \
  "s_add_u32 %[cnt], %[so], 0*%c[js]+0*%c[ss]\n" \
  "buffer_store_dwordx4 v[224:227], v251, %[rs], %[cnt] offen sc1\n" \
  "v_mfma_f32_16x16x32_bf16 v[236:239], v[212:215], v[240:243], v[236:239]\n" \
  "s_waitcnt lgkmcnt(0)\n" \
  "v_mfma_f32_16x16x32_bf16 v[232:235], v[204:207], v[244:247], v[232:235]\n" \
  "v_mfma_f32_16x16x32_bf16 v[236:239], v[220:223], v[244:247], v[236:239]\n" \
  "ds_read_b128 v[240:243], v250 offset:1024\n" \
  "ds_read_b128 v[244:247], v250 offset:17408\n" \
  "v_mfma_f32_16x16x32_bf16 v[224:227], v[192:195], v[64:67], 0\n" \
  "v_mfma_f32_16x16x32_bf16 v[228:231], v[208:211], v[64:67], 0\n" \
  "v_mfma_f32_16x16x32_bf16 v[224:227], v[196:199], v[68:71], v[224:227]\n" \
  "v_cvt_pk_bf16_f32 v232, v232, v233\n" \
  "v_cvt_pk_bf16_f32 v234, v234, v235\n" \
  "v_mfma_f32_16x16x32_bf16 v[228:231], v[212:215], v[68:71], v[228:231]\n" \
  "v_cvt_pk_bf16_f32 v233, v236, v237\n" \
  "v_cvt_pk_bf16_f32 v235, v238, v239\n" \
  "v_mfma_f32_16x16x32_bf16 v[224:227], v[200:203], v[72:75], v[224:227]\n" \
  "s_add_u32 %[cnt], %[so], 4*%c[js]+0*%c[ss]\n" \
  "buffer_store_dwordx4 v[232:235], v251, %[rs], %[cnt] offen sc1\n" \
  "v_mfma_f32_16x16x32_bf16 v[228:231], v[216:219], v[72:75], v[228:231]\n" \
  "v_mfma_f32_16x16x32_bf16 v[224:227], v[204:207], v[76:79], v[224:227]\n" \
  "v_mfma_f32_16x16x32_bf16 v[228:231], v[220:223], v[76:79], v[228:231]\n" \
  "v_lshlrev_b32 v253, 24, v251\n" \
  "s_waitcnt vmcnt(2)\n" \
  "buffer_store_byte v253, v253, %[rs], %[fo] offen sc1\n" \
  "s_barrier\n" \
  "v_mfma_f32_16x16x32_bf16 v[232:235], v[192:195], v[132:135], 0\n" \
  "v_mfma_f32_16x16x32_bf16 v[236:239], v[208:211], v[132:135], 0\n" \
  "v_mfma_f32_16x16x32_bf16 v[232:235], v[200:203], v[140:143], v[232:235]\n" \
  "v_cvt_pk_bf16_f32 v224, v224, v225\n" \
  "v_cvt_pk_bf16_f32 v226, v226, v227\n" \
  "v_mfma_f32_16x16x32_bf16 v[236:239], v[216:219], v[140:143], v[236:239]\n" \
  "v_cvt_pk_bf16_f32 v225, v228, v229\n" \
  "v_cvt_pk_bf16_f32 v227, v230, v231\n" \
  "s_waitcnt lgkmcnt(1)\n" \
  "v_mfma_f32_16x16x32_bf16 v[232:235], v[196:199], v[240:243], v[232:235]\n" \
  "s_add_u32 %[cnt], %[so], 0*%c[js]+1*%c[ss]\n" \
  "buffer_store_dwordx4 v[224:227], v251, %[rs], %[cnt] offen sc1\n" \
  "v_mfma_f32_16x16x32_bf16 v[236:239], v[212:215], v[240:243], v[236:239]\n" \
  "s_waitcnt lgkmcnt(0)\n" \
  "v_mfma_f32_16x16x32_bf16 v[232:235], v[204:207], v[244:247], v[232:235]\n" \
  "v_mfma_f32_16x16x32_bf16 v[236:239], v[220:223], v[244:247], v[236:239]\n" \
  "ds_read_b128 v[240:243], v250 offset:32768\n" \
  "ds_read_b128 v[244:247], v250 offset:49152\n" \
  "v_mfma_f32_16x16x32_bf16 v[224:227], v[192:195], v[16:19], 0\n" \
  "v_mfma_f32_16x16x32_bf16 v[228:231], v[208:211], v[16:19], 0\n" \
  "v_mfma_f32_16x16x32_bf16 v[224:227], v[196:199], v[20:23], v[224:227]\n" \
  "v_cvt_pk_bf16_f32 v232, v232, v233\n" \
  "v_cvt_pk_bf16_f32 v234, v234, v235\n" \
  "v_mfma_f32_16x16x32_bf16 v[228:231], v[212:215], v[20:23], v[228:231]\n" \
  "v_cvt_pk_bf16_f32 v233, v236, v237\n" \
  "v_cvt_pk_bf16_f32 v235, v238, v239\n" \
  "v_mfma_f32_16x16x32_bf16 v[224:227], v[200:203], v[24:27], v[224:227]\n" \
  "s_add_u32 %[cnt], %[so], 4*%c[js]+1*%c[ss]\n" \
  "buffer_store_dwordx4 v[232:235], v251, %[rs], %[cnt] offen sc1\n" \
  "v_mfma_f32_16x16x32_bf16 v[228:231], v[216:219], v[24:27], v[228:231]\n" \
  "v_mfma_f32_16x16x32_bf16 v[224:227], v[204:207], v[28:31], v[224:227]\n" \
  "v_mfma_f32_16x16x32_bf16 v[228:231], v[220:223], v[28:31], v[228:231]\n" \
  "s_barrier\n" \
  "v_mfma_f32_16x16x32_bf16 v[232:235], v[192:195], v[144:147], 0\n" \
  "v_mfma_f32_16x16x32_bf16 v[236:239], v[208:211], v[144:147], 0\n" \
  "v_mfma_f32_16x16x32_bf16 v[232:235], v[200:203], v[152:155], v[232:235]\n" \
  "v_cvt_pk_bf16_f32 v224, v224, v225\n" \
  "v_cvt_pk_bf16_f32 v226, v226, v227\n" \
  "v_mfma_f32_16x16x32_bf16 v[236:239], v[216:219], v[152:155], v[236:239]\n" \
  "v_cvt_pk_bf16_f32 v225, v228, v229\n" \
  "v_cvt_pk_bf16_f32 v227, v230, v231\n" \
  "s_waitcnt lgkmcnt(1)\n" \
  "v_mfma_f32_16x16x32_bf16 v[232:235], v[196:199], v[240:243], v[232:235]\n" \
  "s_add_u32 %[cnt], %[so], 1*%c[js]+0*%c[ss]\n" \
  "buffer_store_dwordx4 v[224:227], v251, %[rs], %[cnt] offen sc1\n" \
  "v_mfma_f32_16x16x32_bf16 v[236:239], v[212:215], v[240:243], v[236:239]\n" \
  "s_waitcnt lgkmcnt(0)\n" \
  "v_mfma_f32_16x16x32_bf16 v[232:235], v[204:207], v[244:247], v[232:235]\n" \
  "v_mfma_f32_16x16x32_bf16 v[236:239], v[220:223], v[244:247], v[236:239]\n" \
  "ds_read_b128 v[240:243], v250 offset:33792\n" \
  "ds_read_b128 v[244:247], v250 offset:50176\n" \
  "v_mfma_f32_16x16x32_bf16 v[224:227], v[192:195], v[80:83], 0\n" \
  "v_mfma_f32_16x16x32_bf16 v[228:231], v[208:211], v[80:83], 0\n" \
  "v_mfma_f32_16x16x32_bf16 v[224:227], v[196:199], v[84:87], v[224:227]\n" \
  "v_cvt_pk_bf16_f32 v232, v232, v233\n" \
  "v_cvt_pk_bf16_f32 v234, v234, v235\n" \
  "v_mfma_f32_16x16x32_bf16 v[228:231], v[212:215], v[84:87], v[228:231]\n" \
  "v_cvt_pk_bf16_f32 v233, v236, v237\n" \
  "v_cvt_pk_bf16_f32 v235, v238, v239\n" \
  "v_mfma_f32_16x16x32_bf16 v[224:227], v[200:203], v[88:91], v[224:227]\n" \
  "s_add_u32 %[cnt], %[so], 5*%c[js]+0*%c[ss]\n" \
  "buffer_store_dwordx4 v[232:235], v251, %[rs], %[cnt] offen sc1\n" \
  "v_mfma_f32_16x16x32_bf16 v[228:231], v[216:219], v[88:91], v[228:231]\n" \
  "v_mfma_f32_16x16x32_bf16 v[224:227], v[204:207], v[92:95], v[224:227]\n" \
  "v_mfma_f32_16x16x32_bf16 v[228:231], v[220:223], v[92:95], v[228:231]\n" \
  "v_mfma_f32_16x16x32_bf16 v[232:235], v[192:195], v[148:151], 0\n" \
  "v_mfma_f32_16x16x32_bf16 v[236:239], v[208:211], v[148:151], 0\n" \
  "v_mfma_f32_16x16x32_bf16 v[232:235], v[200:203], v[156:159], v[232:235]\n" \
  "v_cvt_pk_bf16_f32 v224, v224, v225\n" \
  "v_cvt_pk_bf16_f32 v226, v226, v227\n" \
  "v_mfma_f32_16x16x32_bf16 v[236:239], v[216:219], v[156:159], v[236:239]\n" \
  "v_cvt_pk_bf16_f32 v225, v228, v229\n" \
  "v_cvt_pk_bf16_f32 v227, v230, v231\n" \
  "s_waitcnt lgkmcnt(1)\n" \
  "v_mfma_f32_16x16x32_bf16 v[232:235], v[196:199], v[240:243], v[232:235]\n" \
  "s_add_u32 %[cnt], %[so], 1*%c[js]+1*%c[ss]\n" \
  "buffer_store_dwordx4 v[224:227], v251, %[rs], %[cnt] offen sc1\n" \
  "v_mfma_f32_16x16x32_bf16 v[236:239], v[212:215], v[240:243], v[236:239]\n" \
  "s_waitcnt lgkmcnt(0)\n" \
  "v_mfma_f32_16x16x32_bf16 v[232:235], v[204:207], v[244:247], v[232:235]\n" \
  "v_mfma_f32_16x16x32_bf16 v[236:239], v[220:223], v[244:247], v[236:239]\n" \
  "ds_read_b128 v[240:243], v249 offset:0\n" \
  "ds_read_b128 v[244:247], v249 offset:16384\n" \
  "s_barrier\n" \
  "v_mfma_f32_16x16x32_bf16 v[224:227], v[192:195], v[32:35], 0\n" \
  "v_mfma_f32_16x16x32_bf16 v[228:231], v[208:211], v[32:35], 0\n" \
  "v_mfma_f32_16x16x32_bf16 v[224:227], v[196:199], v[36:39], v[224:227]\n" \
  "v_cvt_pk_bf16_f32 v232, v232, v233\n" \
  "v_cvt_pk_bf16_f32 v234, v234, v235\n" \
  "v_mfma_f32_16x16x32_bf16 v[228:231], v[212:215], v[36:39], v[228:231]\n" \
  "v_cvt_pk_bf16_f32 v233, v236, v237\n" \
  "v_cvt_pk_bf16_f32 v235, v238, v239\n" \
  "v_mfma_f32_16x16x32_bf16 v[224:227], v[200:203], v[40:43], v[224:227]\n" \
  "s_add_u32 %[cnt], %[so], 5*%c[js]+1*%c[ss]\n" \
  "buffer_store_dwordx4 v[232:235], v251, %[rs], %[cnt] offen sc1\n" \
  "v_mfma_f32_16x16x32_bf16 v[228:231], v[216:219], v[40:43], v[228:231]\n" \
  "v_mfma_f32_16x16x32_bf16 v[224:227], v[204:207], v[44:47], v[224:227]\n" \
  "v_mfma_f32_16x16x32_bf16 v[228:231], v[220:223], v[44:47], v[228:231]\n" \
  "v_mfma_f32_16x16x32_bf16 v[232:235], v[192:195], v[160:163], 0\n" \
  "v_mfma_f32_16x16x32_bf16 v[236:239], v[208:211], v[160:163], 0\n" \
  "v_mfma_f32_16x16x32_bf16 v[232:235], v[200:203], v[168:171], v[232:235]\n" \
  "v_cvt_pk_bf16_f32 v224, v224, v225\n" \
  "v_cvt_pk_bf16_f32 v226, v226, v227\n" \
  "v_mfma_f32_16x16x32_bf16 v[236:239], v[216:219], v[168:171], v[236:239]\n" \
  "v_cvt_pk_bf16_f32 v225, v228, v229\n" \
  "v_cvt_pk_bf16_f32 v227, v230, v231\n" \
  "s_waitcnt lgkmcnt(1)\n" \
  "v_mfma_f32_16x16x32_bf16 v[232:235], v[196:199], v[240:243], v[232:235]\n" \
  "s_add_u32 %[cnt], %[so], 2*%c[js]+0*%c[ss]\n" \
  "buffer_store_dwordx4 v[224:227], v251, %[rs], %[cnt] offen sc1\n" \
  "v_mfma_f32_16x16x32_bf16 v[236:239], v[212:215], v[240:243], v[236:239]\n" \
  "s_waitcnt lgkmcnt(0)\n" \
  "v_mfma_f32_16x16x32_bf16 v[232:235], v[204:207], v[244:247], v[232:235]\n" \
  "v_mfma_f32_16x16x32_bf16 v[236:239], v[220:223], v[244:247], v[236:239]\n" \
  "ds_read_b128 v[240:243], v249 offset:1024\n" \
  "ds_read_b128 v[244:247], v249 offset:17408\n" \
  "v_mfma_f32_16x16x32_bf16 v[224:227], v[192:195], v[96:99], 0\n" \
  "v_mfma_f32_16x16x32_bf16 v[228:231], v[208:211], v[96:99], 0\n" \
  "v_mfma_f32_16x16x32_bf16 v[224:227], v[196:199], v[100:103], v[224:227]\n" \
  "v_cvt_pk_bf16_f32 v232, v232, v233\n" \
  "v_cvt_pk_bf16_f32 v234, v234, v235\n" \
  "v_mfma_f32_16x16x32_bf16 v[228:231], v[212:215], v[100:103], v[228:231]\n" \
  "v_cvt_pk_bf16_f32 v233, v236, v237\n" \
  "v_cvt_pk_bf16_f32 v235, v238, v239\n" \
  "v_mfma_f32_16x16x32_bf16 v[224:227], v[200:203], v[104:107], v[224:227]\n" \
  "s_add_u32 %[cnt], %[so], 6*%c[js]+0*%c[ss]\n" \
  "buffer_store_dwordx4 v[232:235], v251, %[rs], %[cnt] offen sc1\n" \
  "v_mfma_f32_16x16x32_bf16 v[228:231], v[216:219], v[104:107], v[228:231]\n" \
  "v_mfma_f32_16x16x32_bf16 v[224:227], v[204:207], v[108:111], v[224:227]\n" \
  "v_mfma_f32_16x16x32_bf16 v[228:231], v[220:223], v[108:111], v[228:231]\n" \
  "s_barrier\n" \
  "v_mfma_f32_16x16x32_bf16 v[232:235], v[192:195], v[164:167], 0\n" \
  "v_mfma_f32_16x16x32_bf16 v[236:239], v[208:211], v[164:167], 0\n" \
  "v_mfma_f32_16x16x32_bf16 v[232:235], v[200:203], v[172:175], v[232:235]\n" \
  "v_cvt_pk_bf16_f32 v224, v224, v225\n" \
  "v_cvt_pk_bf16_f32 v226, v226, v227\n" \
  "v_mfma_f32_16x16x32_bf16 v[236:239], v[216:219], v[172:175], v[236:239]\n" \
  "v_cvt_pk_bf16_f32 v225, v228, v229\n" \
  "v_cvt_pk_bf16_f32 v227, v230, v231\n" \
  "s_waitcnt lgkmcnt(1)\n" \
  "v_mfma_f32_16x16x32_bf16 v[232:235], v[196:199], v[240:243], v[232:235]\n" \
  "s_add_u32 %[cnt], %[so], 2*%c[js]+1*%c[ss]\n" \
  "buffer_store_dwordx4 v[224:227], v251, %[rs], %[cnt] offen sc1\n" \
  "v_mfma_f32_16x16x32_bf16 v[236:239], v[212:215], v[240:243], v[236:239]\n" \
  "s_waitcnt lgkmcnt(0)\n" \
  "v_mfma_f32_16x16x32_bf16 v[232:235], v[204:207], v[244:247], v[232:235]\n" \
  "v_mfma_f32_16x16x32_bf16 v[236:239], v[220:223], v[244:247], v[236:239]\n" \
  "ds_read_b128 v[240:243], v249 offset:32768\n" \
  "ds_read_b128 v[244:247], v249 offset:49152\n" \
  "v_mfma_f32_16x16x32_bf16 v[224:227], v[192:195], v[48:51], 0\n" \
  "v_mfma_f32_16x16x32_bf16 v[228:231], v[208:211], v[48:51], 0\n" \
  "v_mfma_f32_16x16x32_bf16 v[224:227], v[196:199], v[52:55], v[224:227]\n" \
  "v_cvt_pk_bf16_f32 v232, v232, v233\n" \
  "v_cvt_pk_bf16_f32 v234, v234, v235\n" \
  "v_mfma_f32_16x16x32_bf16 v[228:231], v[212:215], v[52:55], v[228:231]\n" \
  "v_cvt_pk_bf16_f32 v233, v236, v237\n" \
  "v_cvt_pk_bf16_f32 v235, v238, v239\n" \
  "v_mfma_f32_16x16x32_bf16 v[224:227], v[200:203], v[56:59], v[224:227]\n" \
  "s_add_u32 %[cnt], %[so], 6*%c[js]+1*%c[ss]\n" \
  "buffer_store_dwordx4 v[232:235], v251, %[rs], %[cnt] offen sc1\n" \
  "v_mfma_f32_16x16x32_bf16 v[228:231], v[216:219], v[56:59], v[228:231]\n" \
  "v_mfma_f32_16x16x32_bf16 v[224:227], v[204:207], v[60:63], v[224:227]\n" \
  "v_mfma_f32_16x16x32_bf16 v[228:231], v[220:223], v[60:63], v[228:231]\n" \
  "s_barrier\n" \
  "v_mfma_f32_16x16x32_bf16 v[232:235], v[192:195], v[176:179], 0\n" \
  "v_mfma_f32_16x16x32_bf16 v[236:239], v[208:211], v[176:179], 0\n" \
  "v_mfma_f32_16x16x32_bf16 v[232:235], v[200:203], v[184:187], v[232:235]\n" \
  "v_cvt_pk_bf16_f32 v224, v224, v225\n" \
  "v_cvt_pk_bf16_f32 v226, v226, v227\n" \
  "v_mfma_f32_16x16x32_bf16 v[236:239], v[216:219], v[184:187], v[236:239]\n" \
  "v_cvt_pk_bf16_f32 v225, v228, v229\n" \
  "v_cvt_pk_bf16_f32 v227, v230, v231\n" \
  "s_waitcnt lgkmcnt(1)\n" \
  "v_mfma_f32_16x16x32_bf16 v[232:235], v[196:199], v[240:243], v[232:235]\n" \
  "s_add_u32 %[cnt], %[so], 3*%c[js]+0*%c[ss]\n" \
  "buffer_store_dwordx4 v[224:227], v251, %[rs], %[cnt] offen sc1\n" \
  "v_mfma_f32_16x16x32_bf16 v[236:239], v[212:215], v[240:243], v[236:239]\n" \
  "s_waitcnt lgkmcnt(0)\n" \
  "v_mfma_f32_16x16x32_bf16 v[232:235], v[204:207], v[244:247], v[232:235]\n" \
  "v_mfma_f32_16x16x32_bf16 v[236:239], v[220:223], v[244:247], v[236:239]\n" \
  "ds_read_b128 v[240:243], v249 offset:33792\n" \
  "ds_read_b128 v[244:247], v249 offset:50176\n" \
  "v_mfma_f32_16x16x32_bf16 v[224:227], v[192:195], v[112:115], 0\n" \
  "v_mfma_f32_16x16x32_bf16 v[228:231], v[208:211], v[112:115], 0\n" \
  "v_mfma_f32_16x16x32_bf16 v[224:227], v[196:199], v[116:119], v[224:227]\n" \
  "v_cvt_pk_bf16_f32 v232, v232, v233\n" \
  "v_cvt_pk_bf16_f32 v234, v234, v235\n" \
  "v_mfma_f32_16x16x32_bf16 v[228:231], v[212:215], v[116:119], v[228:231]\n" \
  "v_cvt_pk_bf16_f32 v233, v236, v237\n" \
  "v_cvt_pk_bf16_f32 v235, v238, v239\n" \
  "v_mfma_f32_16x16x32_bf16 v[224:227], v[200:203], v[120:123], v[224:227]\n" \
  "s_add_u32 %[cnt], %[so], 7*%c[js]+0*%c[ss]\n" \
  "buffer_store_dwordx4 v[232:235], v251, %[rs], %[cnt] offen sc1\n" \
  "v_mfma_f32_16x16x32_bf16 v[228:231], v[216:219], v[120:123], v[228:231]\n" \
  "v_mfma_f32_16x16x32_bf16 v[224:227], v[204:207], v[124:127], v[224:227]\n" \
  "v_mfma_f32_16x16x32_bf16 v[228:231], v[220:223], v[124:127], v[228:231]\n" \
  "v_mfma_f32_16x16x32_bf16 v[232:235], v[192:195], v[180:183], 0\n" \
  "v_mfma_f32_16x16x32_bf16 v[236:239], v[208:211], v[180:183], 0\n" \
  "v_mfma_f32_16x16x32_bf16 v[232:235], v[200:203], v[188:191], v[232:235]\n" \
  "v_cvt_pk_bf16_f32 v224, v224, v225\n" \
  "v_cvt_pk_bf16_f32 v226, v226, v227\n" \
  "v_mfma_f32_16x16x32_bf16 v[236:239], v[216:219], v[188:191], v[236:239]\n" \
  "v_cvt_pk_bf16_f32 v225, v228, v229\n" \
  "v_cvt_pk_bf16_f32 v227, v230, v231\n" \
  "s_waitcnt lgkmcnt(1)\n" \
  "v_mfma_f32_16x16x32_bf16 v[232:235], v[196:199], v[240:243], v[232:235]\n" \
  "s_add_u32 %[cnt], %[so], 3*%c[js]+1*%c[ss]\n" \
  "buffer_store_dwordx4 v[224:227], v251, %[rs], %[cnt] offen sc1\n" \
  "v_mfma_f32_16x16x32_bf16 v[236:239], v[212:215], v[240:243], v[236:239]\n" \
  "s_waitcnt lgkmcnt(0)\n" \
  "v_mfma_f32_16x16x32_bf16 v[232:235], v[204:207], v[244:247], v[232:235]\n" \
  "v_mfma_f32_16x16x32_bf16 v[236:239], v[220:223], v[244:247], v[236:239]\n" \
  "s_nop 4\n" \
  "v_cvt_pk_bf16_f32 v232, v232, v233\n" \
  "v_cvt_pk_bf16_f32 v234, v234, v235\n" \
  "s_nop 1\n" \
  "v_cvt_pk_bf16_f32 v233, v236, v237\n" \
  "v_cvt_pk_bf16_f32 v235, v238, v239\n" \
  "s_add_u32 %[cnt], %[so], 7*%c[js]+1*%c[ss]\n" \
  "buffer_store_dwordx4 v[232:235], v251, %[rs], %[cnt] offen sc1\n" \
  "s_nop 1\n"
#define ST_XBF \
  "s_barrier\n" \
  "ds_read_b128 v[192:195], v251 offset:24576\n" \
  "ds_read_b128 v[208:211], v251 offset:28672\n" \
  "ds_read_b128 v[196:199], v251 offset:25600\n" \
  "ds_read_b128 v[212:215], v251 offset:29696\n" \
  "ds_read_b128 v[200:203], v251 offset:26624\n" \
  "ds_read_b128 v[216:219], v251 offset:30720\n" \
  "ds_read_b128 v[204:207], v251 offset:27648\n" \
  "ds_read_b128 v[220:223], v251 offset:31744\n" \
  "ds_read_b128 v[240:243], v250 offset:0\n" \
  "ds_read_b128 v[244:247], v250 offset:16384\n" \
  "s_waitcnt lgkmcnt(9)\n" \
  "v_mfma_f32_16x16x32_bf16 v[224:227], v[192:195], v[0:3], 0\n" \
  "s_waitcnt lgkmcnt(8)\n" \
  "v_mfma_f32_16x16x32_bf16 v[228:231], v[208:211], v[0:3], 0\n" \
  "s_waitcnt lgkmcnt(7)\n" \
  "v_mfma_f32_16x16x32_bf16 v[224:227], v[196:199], v[4:7], v[224:227]\n" \
  "s_waitcnt lgkmcnt(6)\n" \
  "v_mfma_f32_16x16x32_bf16 v[228:231], v[212:215], v[4:7], v[228:231]\n" \
  "s_waitcnt lgkmcnt(5)\n" \
  "v_mfma_f32_16x16x32_bf16 v[224:227], v[200:203], v[8:11], v[224:227]\n" \
  "s_waitcnt lgkmcnt(4)\n" \
  "v_mfma_f32_16x16x32_bf16 v[228:231], v[216:219], v[8:11], v[228:231]\n" \
  "s_waitcnt lgkmcnt(3)\n" \
  "v_mfma_f32_16x16x32_bf16 v[224:227], v[204:207], v[12:15], v[224:227]\n" \
  "s_waitcnt lgkmcnt(2)\n" \
  "v_mfma_f32_16x16x32_bf16 v[228:231], v[220:223], v[12:15], v[228:231]\n" \
  "v_mfma_f32_16x16x32_bf16 v[232:235], v[192:195], v[128:131], 0\n" \
  "v_mfma_f32_16x16x32_bf16 v[236:239], v[208:211], v[128:131], 0\n" \
  "v_mfma_f32_16x16x32_bf16 v[232:235], v[200:203], v[136:139], v[232:235]\n" \
  "v_cvt_pk_bf16_f32 v224, v224, v225\n" \
  "v_cvt_pk_bf16_f32 v226, v226, v227\n" \
  "v_mfma_f32_16x16x32_bf16 v[236:239], v[216:219], v[136:139], v[236:239]\n" \
  "v_cvt_pk_bf16_f32 v225, v228, v229\n" \
  "v_cvt_pk_bf16_f32 v227, v230, v231\n" \
  "s_waitcnt lgkmcnt(1)\n" \
  "v_mfma_f32_16x16x32_bf16 v[232:235], v[196:199], v[240:243], v[232:235]\n" \
  "s_add_u32 %[cnt], %[so], 0*%c[js]+0*%c[ss]\n" \
  "buffer_store_dwordx4 v[224:227], v251, %[rs], %[cnt] offen sc1\n" \
  "v_mfma_f32_16x16x32_bf16 v[236:239], v[212:215], v[240:243], v[236:239]\n" \
  "s_waitcnt lgkmcnt(0)\n" \
  "v_mfma_f32_16x16x32_bf16 v[232:235], v[204:207], v[244:247], v[232:235]\n" \
  "v_mfma_f32_16x16x32_bf16 v[236:239], v[220:223], v[244:247], v[236:239]\n" \
  "ds_read_b128 v[240:243], v250 offset:1024\n" \
  "ds_read_b128 v[244:247], v250 offset:17408\n" \
  "v_mfma_f32_16x16x32_bf16 v[224:227], v[192:195], v[64:67], 0\n" \
  "v_mfma_f32_16x16x32_bf16 v[228:231], v[208:211], v[64:67], 0\n" \
  "v_mfma_f32_16x16x32_bf16 v[224:227], v[196:199], v[68:71], v[224:227]\n" \
  "v_cvt_pk_bf16_f32 v232, v232, v233\n" \
  "v_cvt_pk_bf16_f32 v234, v234, v235\n" \
  "v_mfma_f32_16x16x32_bf16 v[228:231], v[212:215], v[68:71], v[228:231]\n" \
  "v_cvt_pk_bf16_f32 v233, v236, v237\n" \
  "v_cvt_pk_bf16_f32 v235, v238, v239\n" \
  "v_mfma_f32_16x16x32_bf16 v[224:227], v[200:203], v[72:75], v[224:227]\n" \
  "s_add_u32 %[cnt], %[so], 4*%c[js]+0*%c[ss]\n" \
  "buffer_store_dwordx4 v[232:235], v251, %[rs], %[cnt] offen sc1\n" \
  "v_mfma_f32_16x16x32_bf16 v[228:231], v[216:219], v[72:75], v[228:231]\n" \
  "v_mfma_f32_16x16x32_bf16 v[224:227], v[204:207], v[76:79], v[224:227]\n" \
  "v_mfma_f32_16x16x32_bf16 v[228:231], v[220:223], v[76:79], v[228:231]\n" \
  "v_mfma_f32_16x16x32_bf16 v[232:235], v[192:195], v[132:135], 0\n" \
  "v_mfma_f32_16x16x32_bf16 v[236:239], v[208:211], v[132:135], 0\n" \
  "v_mfma_f32_16x16x32_bf16 v[232:235], v[200:203], v[140:143], v[232:235]\n" \
  "v_cvt_pk_bf16_f32 v224, v224, v225\n" \
  "v_cvt_pk_bf16_f32 v226, v226, v227\n" \
  "v_mfma_f32_16x16x32_bf16 v[236:239], v[216:219], v[140:143], v[236:239]\n" \
  "v_cvt_pk_bf16_f32 v225, v228, v229\n" \
  "v_cvt_pk_bf16_f32 v227, v230, v231\n" \
  "s_waitcnt lgkmcnt(1)\n" \
  "v_mfma_f32_16x16x32_bf16 v[232:235], v[196:199], v[240:243], v[232:235]\n" \
  "s_add_u32 %[cnt], %[so], 0*%c[js]+1*%c[ss]\n" \
  "buffer_store_dwordx4 v[224:227], v251, %[rs], %[cnt] offen sc1\n" \
  "v_mfma_f32_16x16x32_bf16 v[236:239], v[212:215], v[240:243], v[236:239]\n" \
  "s_waitcnt lgkmcnt(0)\n" \
  "v_mfma_f32_16x16x32_bf16 v[232:235], v[204:207], v[244:247], v[232:235]\n" \
  "v_mfma_f32_16x16x32_bf16 v[236:239], v[220:223], v[244:247], v[236:239]\n" \
  "ds_read_b128 v[240:243], v250 offset:32768\n" \
  "ds_read_b128 v[244:247], v250 offset:49152\n" \
  "s_barrier\n" \
  "v_mfma_f32_16x16x32_bf16 v[224:227], v[192:195], v[16:19], 0\n" \
  "v_mfma_f32_16x16x32_bf16 v[228:231], v[208:211], v[16:19], 0\n" \
  "v_mfma_f32_16x16x32_bf16 v[224:227], v[196:199], v[20:23], v[224:227]\n" \
  "v_cvt_pk_bf16_f32 v232, v232, v233\n" \
  "v_cvt_pk_bf16_f32 v234, v234, v235\n" \
  "v_mfma_f32_16x16x32_bf16 v[228:231], v[212:215], v[20:23], v[228:231]\n" \
  "v_cvt_pk_bf16_f32 v233, v236, v237\n" \
  "v_cvt_pk_bf16_f32 v235, v238, v239\n" \
  "v_mfma_f32_16x16x32_bf16 v[224:227], v[200:203], v[24:27], v[224:227]\n" \
  "s_add_u32 %[cnt], %[so], 4*%c[js]+1*%c[ss]\n" \
  "buffer_store_dwordx4 v[232:235], v251, %[rs], %[cnt] offen sc1\n" \
  "v_mfma_f32_16x16x32_bf16 v[228:231], v[216:219], v[24:27], v[228:231]\n" \
  "v_mfma_f32_16x16x32_bf16 v[224:227], v[204:207], v[28:31], v[224:227]\n" \
  "v_mfma_f32_16x16x32_bf16 v[228:231], v[220:223], v[28:31], v[228:231]\n" \
  "v_mfma_f32_16x16x32_bf16 v[232:235], v[192:195], v[144:147], 0\n" \
  "v_mfma_f32_16x16x32_bf16 v[236:239], v[208:211], v[144:147], 0\n" \
  "v_mfma_f32_16x16x32_bf16 v[232:235], v[200:203], v[152:155], v[232:235]\n" \
  "v_cvt_pk_bf16_f32 v224, v224, v225\n" \
  "v_cvt_pk_bf16_f32 v226, v226, v227\n" \
  "v_mfma_f32_16x16x32_bf16 v[236:239], v[216:219], v[152:155], v[236:239]\n" \
  "v_cvt_pk_bf16_f32 v225, v228, v229\n" \
  "v_cvt_pk_bf16_f32 v227, v230, v231\n" \
  "s_waitcnt lgkmcnt(1)\n" \
  "v_mfma_f32_16x16x32_bf16 v[232:235], v[196:199], v[240:243], v[232:235]\n" \
  "s_add_u32 %[cnt], %[so], 1*%c[js]+0*%c[ss]\n" \
  "buffer_store_dwordx4 v[224:227], v251, %[rs], %[cnt] offen sc1\n" \
  "v_mfma_f32_16x16x32_bf16 v[236:239], v[212:215], v[240:243], v[236:239]\n" \
  "s_waitcnt lgkmcnt(0)\n" \
  "v_mfma_f32_16x16x32_bf16 v[232:235], v[204:207], v[244:247], v[232:235]\n" \
  "v_mfma_f32_16x16x32_bf16 v[236:239], v[220:223], v[244:247], v[236:239]\n" \
  "ds_read_b128 v[240:243], v250 offset:33792\n" \
  "ds_read_b128 v[244:247], v250 offset:50176\n" \
  "v_mfma_f32_16x16x32_bf16 v[224:227], v[192:195], v[80:83], 0\n" \
  "v_mfma_f32_16x16x32_bf16 v[228:231], v[208:211], v[80:83], 0\n" \
  "v_mfma_f32_16x16x32_bf16 v[224:227], v[196:199], v[84:87], v[224:227]\n" \
  "v_cvt_pk_bf16_f32 v232, v232, v233\n" \
  "v_cvt_pk_bf16_f32 v234, v234, v235\n" \
  "v_mfma_f32_16x16x32_bf16 v[228:231], v[212:215], v[84:87], v[228:231]\n" \
  "v_cvt_pk_bf16_f32 v233, v236, v237\n" \
  "v_cvt_pk_bf16_f32 v235, v238, v239\n" \
  "v_mfma_f32_16x16x32_bf16 v[224:227], v[200:203], v[88:91], v[224:227]\n" \
  "s_add_u32 %[cnt], %[so], 5*%c[js]+0*%c[ss]\n" \
  "buffer_store_dwordx4 v[232:235], v251, %[rs], %[cnt] offen sc1\n" \
  "v_mfma_f32_16x16x32_bf16 v[228:231], v[216:219], v[88:91], v[228:231]\n" \
  "v_mfma_f32_16x16x32_bf16 v[224:227], v[204:207], v[92:95], v[224:227]\n" \
  "v_mfma_f32_16x16x32_bf16 v[228:231], v[220:223], v[92:95], v[228:231]\n" \
  "v_mfma_f32_16x16x32_bf16 v[232:235], v[192:195], v[148:151], 0\n" \
  "v_mfma_f32_16x16x32_bf16 v[236:239], v[208:211], v[148:151], 0\n" \
  "v_mfma_f32_16x16x32_bf16 v[232:235], v[200:203], v[156:159], v[232:235]\n" \
  "v_cvt_pk_bf16_f32 v224, v224, v225\n" \
  "v_cvt_pk_bf16_f32 v226, v226, v227\n" \
  "v_mfma_f32_16x16x32_bf16 v[236:239], v[216:219], v[156:159], v[236:239]\n" \
  "v_cvt_pk_bf16_f32 v225, v228, v229\n" \
  "v_cvt_pk_bf16_f32 v227, v230, v231\n" \
  "s_waitcnt lgkmcnt(1)\n" \
  "v_mfma_f32_16x16x32_bf16 v[232:235], v[196:199], v[240:243], v[232:235]\n" \
  "s_add_u32 %[cnt], %[so], 1*%c[js]+1*%c[ss]\n" \
  "buffer_store_dwordx4 v[224:227], v251, %[rs], %[cnt] offen sc1\n" \
  "v_mfma_f32_16x16x32_bf16 v[236:239], v[212:215], v[240:243], v[236:239]\n" \
  "s_waitcnt lgkmcnt(0)\n" \
  "v_mfma_f32_16x16x32_bf16 v[232:235], v[204:207], v[244:247], v[232:235]\n" \
  "v_mfma_f32_16x16x32_bf16 v[236:239], v[220:223], v[244:247], v[236:239]\n" \
  "ds_read_b128 v[240:243], v249 offset:0\n" \
  "ds_read_b128 v[244:247], v249 offset:16384\n" \
  "s_barrier\n" \
  "v_mfma_f32_16x16x32_bf16 v[224:227], v[192:195], v[32:35], 0\n" \
  "v_mfma_f32_16x16x32_bf16 v[228:231], v[208:211], v[32:35], 0\n" \
  "v_mfma_f32_16x16x32_bf16 v[224:227], v[196:199], v[36:39], v[224:227]\n" \
  "v_cvt_pk_bf16_f32 v232, v232, v233\n" \
  "v_cvt_pk_bf16_f32 v234, v234, v235\n" \
  "v_mfma_f32_16x16x32_bf16 v[228:231], v[212:215], v[36:39], v[228:231]\n" \
  "v_cvt_pk_bf16_f32 v233, v236, v237\n" \
  "v_cvt_pk_bf16_f32 v235, v238, v239\n" \
  "v_mfma_f32_16x16x32_bf16 v[224:227], v[200:203], v[40:43], v[224:227]\n" \
  "s_add_u32 %[cnt], %[so], 5*%c[js]+1*%c[ss]\n" \
  "buffer_store_dwordx4 v[232:235], v251, %[rs], %[cnt] offen sc1\n" \
  "v_mfma_f32_16x16x32_bf16 v[228:231], v[216:219], v[40:43], v[228:231]\n" \
  "v_mfma_f32_16x16x32_bf16 v[224:227], v[204:207], v[44:47], v[224:227]\n" \
  "v_mfma_f32_16x16x32_bf16 v[228:231], v[220:223], v[44:47], v[228:231]\n" \
  "v_mfma_f32_16x16x32_bf16 v[232:235], v[192:195], v[160:163], 0\n" \
  "v_mfma_f32_16x16x32_bf16 v[236:239], v[208:211], v[160:163], 0\n" \
  "v_mfma_f32_16x16x32_bf16 v[232:235], v[200:203], v[168:171], v[232:235]\n" \
  "v_cvt_pk_bf16_f32 v224, v224, v225\n" \
  "v_cvt_pk_bf16_f32 v226, v226, v227\n" \
  "v_mfma_f32_16x16x32_bf16 v[236:239], v[216:219], v[168:171], v[236:239]\n" \
  "v_cvt_pk_bf16_f32 v225, v228, v229\n" \
  "v_cvt_pk_bf16_f32 v227, v230, v231\n" \
  "s_waitcnt lgkmcnt(1)\n" \
  "v_mfma_f32_16x16x32_bf16 v[232:235], v[196:199], v[240:243], v[232:235]\n" \
  "s_add_u32 %[cnt], %[so], 2*%c[js]+0*%c[ss]\n" \
  "buffer_store_dwordx4 v[224:227], v251, %[rs], %[cnt] offen sc1\n" \
  "v_mfma_f32_16x16x32_bf16 v[236:239], v[212:215], v[240:243], v[236:239]\n" \
  "s_waitcnt lgkmcnt(0)\n" \
  "v_mfma_f32_16x16x32_bf16 v[232:235], v[204:207], v[244:247], v[232:235]\n" \
  "v_mfma_f32_16x16x32_bf16 v[236:239], v[220:223], v[244:247], v[236:239]\n" \
  "ds_read_b128 v[240:243], v249 offset:1024\n" \
  "ds_read_b128 v[244:247], v249 offset:17408\n" \
  "v_mfma_f32_16x16x32_bf16 v[224:227], v[192:195], v[96:99], 0\n" \
  "v_mfma_f32_16x16x32_bf16 v[228:231], v[208:211], v[96:99], 0\n" \
  "v_mfma_f32_16x16x32_bf16 v[224:227], v[196:199], v[100:103], v[224:227]\n" \
  "v_cvt_pk_bf16_f32 v232, v232, v233\n" \
  "v_cvt_pk_bf16_f32 v234, v234, v235\n" \
  "v_mfma_f32_16x16x32_bf16 v[228:231], v[212:215], v[100:103], v[228:231]\n" \
  "v_cvt_pk_bf16_f32 v233, v236, v237\n" \
  "v_cvt_pk_bf16_f32 v235, v238, v239\n" \
  "v_mfma_f32_16x16x32_bf16 v[224:227], v[200:203], v[104:107], v[224:227]\n" \
  "s_add_u32 %[cnt], %[so], 6*%c[js]+0*%c[ss]\n" \
  "buffer_store_dwordx4 v[232:235], v251, %[rs], %[cnt] offen sc1\n" \
  "v_mfma_f32_16x16x32_bf16 v[228:231], v[216:219], v[104:107], v[228:231]\n" \
  "v_mfma_f32_16x16x32_bf16 v[224:227], v[204:207], v[108:111], v[224:227]\n" \
  "v_mfma_f32_16x16x32_bf16 v[228:231], v[220:223], v[108:111], v[228:231]\n" \
  "v_mfma_f32_16x16x32_bf16 v[232:235], v[192:195], v[164:167], 0\n" \
  "v_mfma_f32_16x16x32_bf16 v[236:239], v[208:211], v[164:167], 0\n" \
  "v_mfma_f32_16x16x32_bf16 v[232:235], v[200:203], v[172:175], v[232:235]\n" \
  "v_cvt_pk_bf16_f32 v224, v224, v225\n" \
  "v_cvt_pk_bf16_f32 v226, v226, v227\n" \
  "v_mfma_f32_16x16x32_bf16 v[236:239], v[216:219], v[172:175], v[236:239]\n" \
  "v_cvt_pk_bf16_f32 v225, v228, v229\n" \
  "v_cvt_pk_bf16_f32 v227, v230, v231\n" \
  "s_waitcnt lgkmcnt(1)\n" \
  "v_mfma_f32_16x16x32_bf16 v[232:235], v[196:199], v[240:243], v[232:235]\n" \
  "s_add_u32 %[cnt], %[so], 2*%c[js]+1*%c[ss]\n" \
  "buffer_store_dwordx4 v[224:227], v251, %[rs], %[cnt] offen sc1\n" \
  "v_mfma_f32_16x16x32_bf16 v[236:239], v[212:215], v[240:243], v[236:239]\n" \
  "s_waitcnt lgkmcnt(0)\n" \
  "v_mfma_f32_16x16x32_bf16 v[232:235], v[204:207], v[244:247], v[232:235]\n" \
  "v_mfma_f32_16x16x32_bf16 v[236:239], v[220:223], v[244:247], v[236:239]\n" \
  "ds_read_b128 v[240:243], v249 offset:32768\n" \
  "ds_read_b128 v[244:247], v249 offset:49152\n" \
  "s_barrier\n" \
  "v_mfma_f32_16x16x32_bf16 v[224:227], v[192:195], v[48:51], 0\n" \
  "v_mfma_f32_16x16x32_bf16 v[228:231], v[208:211], v[48:51], 0\n" \
  "v_mfma_f32_16x16x32_bf16 v[224:227], v[196:199], v[52:55], v[224:227]\n" \
  "v_cvt_pk_bf16_f32 v232, v232, v233\n" \
  "v_cvt_pk_bf16_f32 v234, v234, v235\n" \
  "v_mfma_f32_16x16x32_bf16 v[228:231], v[212:215], v[52:55], v[228:231]\n" \
  "v_cvt_pk_bf16_f32 v233, v236, v237\n" \
  "v_cvt_pk_bf16_f32 v235, v238, v239\n" \
  "v_mfma_f32_16x16x32_bf16 v[224:227], v[200:203], v[56:59], v[224:227]\n" \
  "s_add_u32 %[cnt], %[so], 6*%c[js]+1*%c[ss]\n" \
  "buffer_store_dwordx4 v[232:235], v251, %[rs], %[cnt] offen sc1\n" \
  "v_mfma_f32_16x16x32_bf16 v[228:231], v[216:219], v[56:59], v[228:231]\n" \
  "v_mfma_f32_16x16x32_bf16 v[224:227], v[204:207], v[60:63], v[224:227]\n" \
  "v_mfma_f32_16x16x32_bf16 v[228:231], v[220:223], v[60:63], v[228:231]\n" \
  "v_mfma_f32_16x16x32_bf16 v[232:235], v[192:195], v[176:179], 0\n" \
  "v_mfma_f32_16x16x32_bf16 v[236:239], v[208:211], v[176:179], 0\n" \
  "v_mfma_f32_16x16x32_bf16 v[232:235], v[200:203], v[184:187], v[232:235]\n" \
  "v_cvt_pk_bf16_f32 v224, v224, v225\n" \
  "v_cvt_pk_bf16_f32 v226, v226, v227\n" \
  "v_mfma_f32_16x16x32_bf16 v[236:239], v[216:219], v[184:187], v[236:239]\n" \
  "v_cvt_pk_bf16_f32 v225, v228, v229\n" \
  "v_cvt_pk_bf16_f32 v227, v230, v231\n" \
  "s_waitcnt lgkmcnt(1)\n" \
  "v_mfma_f32_16x16x32_bf16 v[232:235], v[196:199], v[240:243], v[232:235]\n" \
  "s_add_u32 %[cnt], %[so], 3*%c[js]+0*%c[ss]\n" \
  "buffer_store_dwordx4 v[224:227], v251, %[rs], %[cnt] offen sc1\n" \
  "v_mfma_f32_16x16x32_bf16 v[236:239], v[212:215], v[240:243], v[236:239]\n" \
  "s_waitcnt lgkmcnt(0)\n" \
  "v_mfma_f32_16x16x32_bf16 v[232:235], v[204:207], v[244:247], v[232:235]\n" \
  "v_mfma_f32_16x16x32_bf16 v[236:239], v[220:223], v[244:247], v[236:239]\n" \
  "ds_read_b128 v[240:243], v249 offset:33792\n" \
  "ds_read_b128 v[244:247], v249 offset:50176\n" \
  "v_mfma_f32_16x16x32_bf16 v[224:227], v[192:195], v[112:115], 0\n" \
  "v_mfma_f32_16x16x32_bf16 v[228:231], v[208:211], v[112:115], 0\n" \
  "v_mfma_f32_16x16x32_bf16 v[224:227], v[196:199], v[116:119], v[224:227]\n" \
  "v_cvt_pk_bf16_f32 v232, v232, v233\n" \
  "v_cvt_pk_bf16_f32 v234, v234, v235\n" \
  "v_mfma_f32_16x16x32_bf16 v[228:231], v[212:215], v[116:119], v[228:231]\n" \
  "v_cvt_pk_bf16_f32 v233, v236, v237\n" \
  "v_cvt_pk_bf16_f32 v235, v238, v239\n" \
  "v_mfma_f32_16x16x32_bf16 v[224:227], v[200:203], v[120:123], v[224:227]\n" \
  "s_add_u32 %[cnt], %[so], 7*%c[js]+0*%c[ss]\n" \
  "buffer_store_dwordx4 v[232:235], v251, %[rs], %[cnt] offen sc1\n" \
  "v_mfma_f32_16x16x32_bf16 v[228:231], v[216:219], v[120:123], v[228:231]\n" \
  "v_mfma_f32_16x16x32_bf16 v[224:227], v[204:207], v[124:127], v[224:227]\n" \
  "v_mfma_f32_16x16x32_bf16 v[228:231], v[220:223], v[124:127], v[228:231]\n" \
  "v_mfma_f32_16x16x32_bf16 v[232:235], v[192:195], v[180:183], 0\n" \
  "v_mfma_f32_16x16x32_bf16 v[236:239], v[208:211], v[180:183], 0\n" \
  "v_mfma_f32_16x16x32_bf16 v[232:235], v[200:203], v[188:191], v[232:235]\n" \
  "v_cvt_pk_bf16_f32 v224, v224, v225\n" \
  "v_cvt_pk_bf16_f32 v226, v226, v227\n" \
  "v_mfma_f32_16x16x32_bf16 v[236:239], v[216:219], v[188:191], v[236:239]\n" \
  "v_cvt_pk_bf16_f32 v225, v228, v229\n" \
  "v_cvt_pk_bf16_f32 v227, v230, v231\n" \
  "s_waitcnt lgkmcnt(1)\n" \
  "v_mfma_f32_16x16x32_bf16 v[232:235], v[196:199], v[240:243], v[232:235]\n" \
  "s_add_u32 %[cnt], %[so], 3*%c[js]+1*%c[ss]\n" \
  "buffer_store_dwordx4 v[224:227], v251, %[rs], %[cnt] offen sc1\n" \
  "v_mfma_f32_16x16x32_bf16 v[236:239], v[212:215], v[240:243], v[236:239]\n" \
  "s_waitcnt lgkmcnt(0)\n" \
  "v_mfma_f32_16x16x32_bf16 v[232:235], v[204:207], v[244:247], v[232:235]\n" \
  "v_mfma_f32_16x16x32_bf16 v[236:239], v[220:223], v[244:247], v[236:239]\n" \
  "s_nop 4\n" \
  "v_cvt_pk_bf16_f32 v232, v232, v233\n" \
  "v_cvt_pk_bf16_f32 v234, v234, v235\n" \
  "s_nop 1\n" \
  "v_cvt_pk_bf16_f32 v233, v236, v237\n" \
  "v_cvt_pk_bf16_f32 v235, v238, v239\n" \
  "s_add_u32 %[cnt], %[so], 7*%c[js]+1*%c[ss]\n" \
  "buffer_store_dwordx4 v[232:235], v251, %[rs], %[cnt] offen sc1\n" \
  "s_barrier\n" \
  "s_nop 1\n"
#define ST_XBS \
  "ds_read_b128 v[192:195], v251 offset:24576\n" \
  "ds_read_b128 v[208:211], v251 offset:28672\n" \
  "ds_read_b128 v[196:199], v251 offset:25600\n" \
  "ds_read_b128 v[212:215], v251 offset:29696\n" \
  "ds_read_b128 v[200:203], v251 offset:26624\n" \
  "ds_read_b128 v[216:219], v251 offset:30720\n" \
  "ds_read_b128 v[204:207], v251 offset:27648\n" \
  "ds_read_b128 v[220:223], v251 offset:31744\n" \
  "ds_read_b128 v[240:243], v250 offset:0\n" \
  "ds_read_b128 v[244:247], v250 offset:16384\n" \
  "s_waitcnt lgkmcnt(9)\n" \
  "v_mfma_f32_16x16x32_bf16 v[224:227], v[192:195], v[0:3], 0\n" \
  "s_waitcnt lgkmcnt(8)\n" \
  "v_mfma_f32_16x16x32_bf16 v[228:231], v[208:211], v[0:3], 0\n" \
  "s_waitcnt lgkmcnt(7)\n" \
  "v_mfma_f32_16x16x32_bf16 v[224:227], v[196:199], v[4:7], v[224:227]\n" \
  "s_waitcnt lgkmcnt(6)\n" \
  "v_mfma_f32_16x16x32_bf16 v[228:231], v[212:215], v[4:7], v[228:231]\n" \
  "s_waitcnt lgkmcnt(5)\n" \
  "v_mfma_f32_16x16x32_bf16 v[224:227], v[200:203], v[8:11], v[224:227]\n" \
  "s_waitcnt lgkmcnt(4)\n" \
  "v_mfma_f32_16x16x32_bf16 v[228:231], v[216:219], v[8:11], v[228:231]\n" \
  "s_waitcnt lgkmcnt(3)\n" \
  "v_mfma_f32_16x16x32_bf16 v[224:227], v[204:207], v[12:15], v[224:227]\n" \
  "s_waitcnt lgkmcnt(2)\n" \
  "v_mfma_f32_16x16x32_bf16 v[228:231], v[220:223], v[12:15], v[228:231]\n" \
  "v_mfma_f32_16x16x32_bf16 v[232:235], v[192:195], v[128:131], 0\n" \
  "v_mfma_f32_16x16x32_bf16 v[236:239], v[208:211], v[128:131], 0\n" \
  "v_mfma_f32_16x16x32_bf16 v[232:235], v[200:203], v[136:139], v[232:235]\n" \
  "v_cvt_pk_bf16_f32 v224, v224, v225\n" \
  "v_cvt_pk_bf16_f32 v226, v226, v227\n" \
  "v_mfma_f32_16x16x32_bf16 v[236:239], v[216:219], v[136:139], v[236:239]\n" \
  "v_cvt_pk_bf16_f32 v225, v228, v229\n" \
  "v_cvt_pk_bf16_f32 v227, v230, v231\n" \
  "s_waitcnt lgkmcnt(1)\n" \
  "v_mfma_f32_16x16x32_bf16 v[232:235], v[196:199], v[240:243], v[232:235]\n" \
  "s_add_u32 %[cnt], %[so], 0*%c[js]+0*%c[ss]\n" \
  "buffer_store_dwordx4 v[224:227], v251, %[rs], %[cnt] offen sc1\n" \
  "v_mfma_f32_16x16x32_bf16 v[236:239], v[212:215], v[240:243], v[236:239]\n" \
  "s_waitcnt lgkmcnt(0)\n" \
  "v_mfma_f32_16x16x32_bf16 v[232:235], v[204:207], v[244:247], v[232:235]\n" \
  "v_mfma_f32_16x16x32_bf16 v[236:239], v[220:223], v[244:247], v[236:239]\n" \
  "ds_read_b128 v[240:243], v250 offset:1024\n" \
  "ds_read_b128 v[244:247], v250 offset:17408\n" \
  "v_mfma_f32_16x16x32_bf16 v[224:227], v[192:195], v[64:67], 0\n" \
  "v_mfma_f32_16x16x32_bf16 v[228:231], v[208:211], v[64:67], 0\n" \
  "v_mfma_f32_16x16x32_bf16 v[224:227], v[196:199], v[68:71], v[224:227]\n" \
  "v_cvt_pk_bf16_f32 v232, v232, v233\n" \
  "v_cvt_pk_bf16_f32 v234, v234, v235\n" \
  "v_mfma_f32_16x16x32_bf16 v[228:231], v[212:215], v[68:71], v[228:231]\n" \
  "v_cvt_pk_bf16_f32 v233, v236, v237\n" \
  "v_cvt_pk_bf16_f32 v235, v238, v239\n" \
  "v_mfma_f32_16x16x32_bf16 v[224:227], v[200:203], v[72:75], v[224:227]\n" \
  "s_add_u32 %[cnt], %[so], 4*%c[js]+0*%c[ss]\n" \
  "buffer_store_dwordx4 v[232:235], v251, %[rs], %[cnt] offen sc1\n" \
  "v_mfma_f32_16x16x32_bf16 v[228:231], v[216:219], v[72:75], v[228:231]\n" \
  "v_mfma_f32_16x16x32_bf16 v[224:227], v[204:207], v[76:79], v[224:227]\n" \
  "v_mfma_f32_16x16x32_bf16 v[228:231], v[220:223], v[76:79], v[228:231]\n" \
  "v_mfma_f32_16x16x32_bf16 v[232:235], v[192:195], v[132:135], 0\n" \
  "v_mfma_f32_16x16x32_bf16 v[236:239], v[208:211], v[132:135], 0\n" \
  "v_mfma_f32_16x16x32_bf16 v[232:235], v[200:203], v[140:143], v[232:235]\n" \
  "v_cvt_pk_bf16_f32 v224, v224, v225\n" \
  "v_cvt_pk_bf16_f32 v226, v226, v227\n" \
  "v_mfma_f32_16x16x32_bf16 v[236:239], v[216:219], v[140:143], v[236:239]\n" \
  "v_cvt_pk_bf16_f32 v225, v228, v229\n" \
  "v_cvt_pk_bf16_f32 v227, v230, v231\n" \
  "s_waitcnt lgkmcnt(1)\n" \
  "v_mfma_f32_16x16x32_bf16 v[232:235], v[196:199], v[240:243], v[232:235]\n" \
  "s_add_u32 %[cnt], %[so], 0*%c[js]+1*%c[ss]\n" \
  "buffer_store_dwordx4 v[224:227], v251, %[rs], %[cnt] offen sc1\n" \
  "v_mfma_f32_16x16x32_bf16 v[236:239], v[212:215], v[240:243], v[236:239]\n" \
  "s_waitcnt lgkmcnt(0)\n" \
  "v_mfma_f32_16x16x32_bf16 v[232:235], v[204:207], v[244:247], v[232:235]\n" \
  "v_mfma_f32_16x16x32_bf16 v[236:239], v[220:223], v[244:247], v[236:239]\n" \
  "ds_read_b128 v[240:243], v250 offset:32768\n" \
  "ds_read_b128 v[244:247], v250 offset:49152\n" \
  "v_mfma_f32_16x16x32_bf16 v[224:227], v[192:195], v[16:19], 0\n" \
  "v_mfma_f32_16x16x32_bf16 v[228:231], v[208:211], v[16:19], 0\n" \
  "v_mfma_f32_16x16x32_bf16 v[224:227], v[196:199], v[20:23], v[224:227]\n" \
  "v_cvt_pk_bf16_f32 v232, v232, v233\n" \
  "v_cvt_pk_bf16_f32 v234, v234, v235\n" \
  "v_mfma_f32_16x16x32_bf16 v[228:231], v[212:215], v[20:23], v[228:231]\n" \
  "v_cvt_pk_bf16_f32 v233, v236, v237\n" \
  "v_cvt_pk_bf16_f32 v235, v238, v239\n" \
  "v_mfma_f32_16x16x32_bf16 v[224:227], v[200:203], v[24:27], v[224:227]\n" \
  "s_add_u32 %[cnt], %[so], 4*%c[js]+1*%c[ss]\n" \
  "buffer_store_dwordx4 v[232:235], v251, %[rs], %[cnt] offen sc1\n" \
  "v_mfma_f32_16x16x32_bf16 v[228:231], v[216:219], v[24:27], v[228:231]\n" \
  "v_mfma_f32_16x16x32_bf16 v[224:227], v[204:207], v[28:31], v[224:227]\n" \
  "v_mfma_f32_16x16x32_bf16 v[228:231], v[220:223], v[28:31], v[228:231]\n" \
  "v_mfma_f32_16x16x32_bf16 v[232:235], v[192:195], v[144:147], 0\n" \
  "v_mfma_f32_16x16x32_bf16 v[236:239], v[208:211], v[144:147], 0\n" \
  "v_mfma_f32_16x16x32_bf16 v[232:235], v[200:203], v[152:155], v[232:235]\n" \
  "v_cvt_pk_bf16_f32 v224, v224, v225\n" \
  "v_cvt_pk_bf16_f32 v226, v226, v227\n" \
  "v_mfma_f32_16x16x32_bf16 v[236:239], v[216:219], v[152:155], v[236:239]\n" \
  "v_cvt_pk_bf16_f32 v225, v228, v229\n" \
  "v_cvt_pk_bf16_f32 v227, v230, v231\n" \
  "s_waitcnt lgkmcnt(1)\n" \
  "v_mfma_f32_16x16x32_bf16 v[232:235], v[196:199], v[240:243], v[232:235]\n" \
  "s_add_u32 %[cnt], %[so], 1*%c[js]+0*%c[ss]\n" \
  "buffer_store_dwordx4 v[224:227], v251, %[rs], %[cnt] offen sc1\n" \
  "v_mfma_f32_16x16x32_bf16 v[236:239], v[212:215], v[240:243], v[236:239]\n" \
  "s_waitcnt lgkmcnt(0)\n" \
  "v_mfma_f32_16x16x32_bf16 v[232:235], v[204:207], v[244:247], v[232:235]\n" \
  "v_mfma_f32_16x16x32_bf16 v[236:239], v[220:223], v[244:247], v[236:239]\n" \
  "ds_read_b128 v[240:243], v250 offset:33792\n" \
  "ds_read_b128 v[244:247], v250 offset:50176\n" \
  "v_mfma_f32_16x16x32_bf16 v[224:227], v[192:195], v[80:83], 0\n" \
  "v_mfma_f32_16x16x32_bf16 v[228:231], v[208:211], v[80:83], 0\n" \
  "v_mfma_f32_16x16x32_bf16 v[224:227], v[196:199], v[84:87], v[224:227]\n" \
  "v_cvt_pk_bf16_f32 v232, v232, v233\n" \
  "v_cvt_pk_bf16_f32 v234, v234, v235\n" \
  "v_mfma_f32_16x16x32_bf16 v[228:231], v[212:215], v[84:87], v[228:231]\n" \
  "v_cvt_pk_bf16_f32 v233, v236, v237\n" \
  "v_cvt_pk_bf16_f32 v235, v238, v239\n" \
  "v_mfma_f32_16x16x32_bf16 v[224:227], v[200:203], v[88:91], v[224:227]\n" \
  "s_add_u32 %[cnt], %[so], 5*%c[js]+0*%c[ss]\n" \
  "buffer_store_dwordx4 v[232:235], v251, %[rs], %[cnt] offen sc1\n" \
  "v_mfma_f32_16x16x32_bf16 v[228:231], v[216:219], v[88:91], v[228:231]\n" \
  "v_mfma_f32_16x16x32_bf16 v[224:227], v[204:207], v[92:95], v[224:227]\n" \
  "v_mfma_f32_16x16x32_bf16 v[228:231], v[220:223], v[92:95], v[228:231]\n" \
  "v_mfma_f32_16x16x32_bf16 v[232:235], v[192:195], v[148:151], 0\n" \
  "v_mfma_f32_16x16x32_bf16 v[236:239], v[208:211], v[148:151], 0\n" \
  "v_mfma_f32_16x16x32_bf16 v[232:235], v[200:203], v[156:159], v[232:235]\n" \
  "v_cvt_pk_bf16_f32 v224, v224, v225\n" \
  "v_cvt_pk_bf16_f32 v226, v226, v227\n" \
  "v_mfma_f32_16x16x32_bf16 v[236:239], v[216:219], v[156:159], v[236:239]\n" \
  "v_cvt_pk_bf16_f32 v225, v228, v229\n" \
  "v_cvt_pk_bf16_f32 v227, v230, v231\n" \
  "s_waitcnt lgkmcnt(1)\n" \
  "v_mfma_f32_16x16x32_bf16 v[232:235], v[196:199], v[240:243], v[232:235]\n" \
  "s_add_u32 %[cnt], %[so], 1*%c[js]+1*%c[ss]\n" \
  "buffer_store_dwordx4 v[224:227], v251, %[rs], %[cnt] offen sc1\n" \
  "v_mfma_f32_16x16x32_bf16 v[236:239], v[212:215], v[240:243], v[236:239]\n" \
  "s_waitcnt lgkmcnt(0)\n" \
  "v_mfma_f32_16x16x32_bf16 v[232:235], v[204:207], v[244:247], v[232:235]\n" \
  "v_mfma_f32_16x16x32_bf16 v[236:239], v[220:223], v[244:247], v[236:239]\n" \
  "ds_read_b128 v[240:243], v249 offset:0\n" \
  "ds_read_b128 v[244:247], v249 offset:16384\n" \
  "s_barrier\n" \
  "v_mfma_f32_16x16x32_bf16 v[224:227], v[192:195], v[32:35], 0\n" \
  "v_mfma_f32_16x16x32_bf16 v[228:231], v[208:211], v[32:35], 0\n" \
  "v_mfma_f32_16x16x32_bf16 v[224:227], v[196:199], v[36:39], v[224:227]\n" \
  "v_cvt_pk_bf16_f32 v232, v232, v233\n" \
  "v_cvt_pk_bf16_f32 v234, v234, v235\n" \
  "v_mfma_f32_16x16x32_bf16 v[228:231], v[212:215], v[36:39], v[228:231]\n" \
  "v_cvt_pk_bf16_f32 v233, v236, v237\n" \
  "v_cvt_pk_bf16_f32 v235, v238, v239\n" \
  "v_mfma_f32_16x16x32_bf16 v[224:227], v[200:203], v[40:43], v[224:227]\n" \
  "s_add_u32 %[cnt], %[so], 5*%c[js]+1*%c[ss]\n" \
  "buffer_store_dwordx4 v[232:235], v251, %[rs], %[cnt] offen sc1\n" \
  "v_mfma_f32_16x16x32_bf16 v[228:231], v[216:219], v[40:43], v[228:231]\n" \
  "v_mfma_f32_16x16x32_bf16 v[224:227], v[204:207], v[44:47], v[224:227]\n" \
  "v_mfma_f32_16x16x32_bf16 v[228:231], v[220:223], v[44:47], v[228:231]\n" \
  "v_mfma_f32_16x16x32_bf16 v[232:235], v[192:195], v[160:163], 0\n" \
  "v_mfma_f32_16x16x32_bf16 v[236:239], v[208:211], v[160:163], 0\n" \
  "v_mfma_f32_16x16x32_bf16 v[232:235], v[200:203], v[168:171], v[232:235]\n" \
  "v_cvt_pk_bf16_f32 v224, v224, v225\n" \
  "v_cvt_pk_bf16_f32 v226, v226, v227\n" \
  "v_mfma_f32_16x16x32_bf16 v[236:239], v[216:219], v[168:171], v[236:239]\n" \
  "v_cvt_pk_bf16_f32 v225, v228, v229\n" \
  "v_cvt_pk_bf16_f32 v227, v230, v231\n" \
  "s_waitcnt lgkmcnt(1)\n" \
  "v_mfma_f32_16x16x32_bf16 v[232:235], v[196:199], v[240:243], v[232:235]\n" \
  "s_add_u32 %[cnt], %[so], 2*%c[js]+0*%c[ss]\n" \
  "buffer_store_dwordx4 v[224:227], v251, %[rs], %[cnt] offen sc1\n" \
  "v_mfma_f32_16x16x32_bf16 v[236:239], v[212:215], v[240:243], v[236:239]\n" \
  "s_waitcnt lgkmcnt(0)\n" \
  "v_mfma_f32_16x16x32_bf16 v[232:235], v[204:207], v[244:247], v[232:235]\n" \
  "v_mfma_f32_16x16x32_bf16 v[236:239], v[220:223], v[244:247], v[236:239]\n" \
  "ds_read_b128 v[240:243], v249 offset:1024\n" \
  "ds_read_b128 v[244:247], v249 offset:17408\n" \
  "v_mfma_f32_16x16x32_bf16 v[224:227], v[192:195], v[96:99], 0\n" \
  "v_mfma_f32_16x16x32_bf16 v[228:231], v[208:211], v[96:99], 0\n" \
  "v_mfma_f32_16x16x32_bf16 v[224:227], v[196:199], v[100:103], v[224:227]\n" \
  "v_cvt_pk_bf16_f32 v232, v232, v233\n" \
  "v_cvt_pk_bf16_f32 v234, v234, v235\n" \
  "v_mfma_f32_16x16x32_bf16 v[228:231], v[212:215], v[100:103], v[228:231]\n" \
  "v_cvt_pk_bf16_f32 v233, v236, v237\n" \
  "v_cvt_pk_bf16_f32 v235, v238, v239\n" \
  "v_mfma_f32_16x16x32_bf16 v[224:227], v[200:203], v[104:107], v[224:227]\n" \
  "s_add_u32 %[cnt], %[so], 6*%c[js]+0*%c[ss]\n" \
  "buffer_store_dwordx4 v[232:235], v251, %[rs], %[cnt] offen sc1\n" \
  "v_mfma_f32_16x16x32_bf16 v[228:231], v[216:219], v[104:107], v[228:231]\n" \
  "v_mfma_f32_16x16x32_bf16 v[224:227], v[204:207], v[108:111], v[224:227]\n" \
  "v_mfma_f32_16x16x32_bf16 v[228:231], v[220:223], v[108:111], v[228:231]\n" \
  "v_mfma_f32_16x16x32_bf16 v[232:235], v[192:195], v[164:167], 0\n" \
  "v_mfma_f32_16x16x32_bf16 v[236:239], v[208:211], v[164:167], 0\n" \
  "v_mfma_f32_16x16x32_bf16 v[232:235], v[200:203], v[172:175], v[232:235]\n" \
  "v_cvt_pk_bf16_f32 v224, v224, v225\n" \
  "v_cvt_pk_bf16_f32 v226, v226, v227\n" \
  "v_mfma_f32_16x16x32_bf16 v[236:239], v[216:219], v[172:175], v[236:239]\n" \
  "v_cvt_pk_bf16_f32 v225, v228, v229\n" \
  "v_cvt_pk_bf16_f32 v227, v230, v231\n" \
  "s_waitcnt lgkmcnt(1)\n" \
  "v_mfma_f32_16x16x32_bf16 v[232:235], v[196:199], v[240:243], v[232:235]\n" \
  "s_add_u32 %[cnt], %[so], 2*%c[js]+1*%c[ss]\n" \
  "buffer_store_dwordx4 v[224:227], v251, %[rs], %[cnt] offen sc1\n" \
  "v_mfma_f32_16x16x32_bf16 v[236:239], v[212:215], v[240:243], v[236:239]\n" \
  "s_waitcnt lgkmcnt(0)\n" \
  "v_mfma_f32_16x16x32_bf16 v[232:235], v[204:207], v[244:247], v[232:235]\n" \
  "v_mfma_f32_16x16x32_bf16 v[236:239], v[220:223], v[244:247], v[236:239]\n" \
  "ds_read_b128 v[240:243], v249 offset:32768\n" \
  "ds_read_b128 v[244:247], v249 offset:49152\n" \
  "s_barrier\n" \
  "v_mfma_f32_16x16x32_bf16 v[224:227], v[192:195], v[48:51], 0\n" \
  "v_mfma_f32_16x16x32_bf16 v[228:231], v[208:211], v[48:51], 0\n" \
  "v_mfma_f32_16x16x32_bf16 v[224:227], v[196:199], v[52:55], v[224:227]\n" \
  "v_cvt_pk_bf16_f32 v232, v232, v233\n" \
  "v_cvt_pk_bf16_f32 v234, v234, v235\n" \
  "v_mfma_f32_16x16x32_bf16 v[228:231], v[212:215], v[52:55], v[228:231]\n" \
  "v_cvt_pk_bf16_f32 v233, v236, v237\n" \
  "v_cvt_pk_bf16_f32 v235, v238, v239\n" \
  "v_mfma_f32_16x16x32_bf16 v[224:227], v[200:203], v[56:59], v[224:227]\n" \
  "s_add_u32 %[cnt], %[so], 6*%c[js]+1*%c[ss]\n" \
  "buffer_store_dwordx4 v[232:235], v251, %[rs], %[cnt] offen sc1\n" \
  "v_mfma_f32_16x16x32_bf16 v[228:231], v[216:219], v[56:59], v[228:231]\n" \
  "v_mfma_f32_16x16x32_bf16 v[224:227], v[204:207], v[60:63], v[224:227]\n" \
  "v_mfma_f32_16x16x32_bf16 v[228:231], v[220:223], v[60:63], v[228:231]\n" \
  "v_mfma_f32_16x16x32_bf16 v[232:235], v[192:195], v[176:179], 0\n" \
  "v_mfma_f32_16x16x32_bf16 v[236:239], v[208:211], v[176:179], 0\n" \
  "v_mfma_f32_16x16x32_bf16 v[232:235], v[200:203], v[184:187], v[232:235]\n" \
  "v_cvt_pk_bf16_f32 v224, v224, v225\n" \
  "v_cvt_pk_bf16_f32 v226, v226, v227\n" \
  "v_mfma_f32_16x16x32_bf16 v[236:239], v[216:219], v[184:187], v[236:239]\n" \
  "v_cvt_pk_bf16_f32 v225, v228, v229\n" \
  "v_cvt_pk_bf16_f32 v227, v230, v231\n" \
  "s_waitcnt lgkmcnt(1)\n" \
  "v_mfma_f32_16x16x32_bf16 v[232:235], v[196:199], v[240:243], v[232:235]\n" \
  "s_add_u32 %[cnt], %[so], 3*%c[js]+0*%c[ss]\n" \
  "buffer_store_dwordx4 v[224:227], v251, %[rs], %[cnt] offen sc1\n" \
  "v_mfma_f32_16x16x32_bf16 v[236:239], v[212:215], v[240:243], v[236:239]\n" \
  "s_waitcnt lgkmcnt(0)\n" \
  "v_mfma_f32_16x16x32_bf16 v[232:235], v[204:207], v[244:247], v[232:235]\n" \
  "v_mfma_f32_16x16x32_bf16 v[236:239], v[220:223], v[244:247], v[236:239]\n" \
  "ds_read_b128 v[240:243], v249 offset:33792\n" \
  "ds_read_b128 v[244:247], v249 offset:50176\n" \
  "v_mfma_f32_16x16x32_bf16 v[224:227], v[192:195], v[112:115], 0\n" \
  "v_mfma_f32_16x16x32_bf16 v[228:231], v[208:211], v[112:115], 0\n" \
  "v_mfma_f32_16x16x32_bf16 v[224:227], v[196:199], v[116:119], v[224:227]\n" \
  "v_cvt_pk_bf16_f32 v232, v232, v233\n" \
  "v_cvt_pk_bf16_f32 v234, v234, v235\n" \
  "v_mfma_f32_16x16x32_bf16 v[228:231], v[212:215], v[116:119], v[228:231]\n" \
  "v_cvt_pk_bf16_f32 v233, v236, v237\n" \
  "v_cvt_pk_bf16_f32 v235, v238, v239\n" \
  "v_mfma_f32_16x16x32_bf16 v[224:227], v[200:203], v[120:123], v[224:227]\n" \
  "s_add_u32 %[cnt], %[so], 7*%c[js]+0*%c[ss]\n" \
  "buffer_store_dwordx4 v[232:235], v251, %[rs], %[cnt] offen sc1\n" \
  "v_mfma_f32_16x16x32_bf16 v[228:231], v[216:219], v[120:123], v[228:231]\n" \
  "v_mfma_f32_16x16x32_bf16 v[224:227], v[204:207], v[124:127], v[224:227]\n" \
  "v_mfma_f32_16x16x32_bf16 v[228:231], v[220:223], v[124:127], v[228:231]\n" \
  "v_mfma_f32_16x16x32_bf16 v[232:235], v[192:195], v[180:183], 0\n" \
  "v_mfma_f32_16x16x32_bf16 v[236:239], v[208:211], v[180:183], 0\n" \
  "v_mfma_f32_16x16x32_bf16 v[232:235], v[200:203], v[188:191], v[232:235]\n" \
  "v_cvt_pk_bf16_f32 v224, v224, v225\n" \
  "v_cvt_pk_bf16_f32 v226, v226, v227\n" \
  "v_mfma_f32_16x16x32_bf16 v[236:239], v[216:219], v[188:191], v[236:239]\n" \
  "v_cvt_pk_bf16_f32 v225, v228, v229\n" \
  "v_cvt_pk_bf16_f32 v227, v230, v231\n" \
  "s_waitcnt lgkmcnt(1)\n" \
  "v_mfma_f32_16x16x32_bf16 v[232:235], v[196:199], v[240:243], v[232:235]\n" \
  "s_add_u32 %[cnt], %[so], 3*%c[js]+1*%c[ss]\n" \
  "buffer_store_dwordx4 v[224:227], v251, %[rs], %[cnt] offen sc1\n" \
  "v_mfma_f32_16x16x32_bf16 v[236:239], v[212:215], v[240:243], v[236:239]\n" \
  "s_waitcnt lgkmcnt(0)\n" \
  "v_mfma_f32_16x16x32_bf16 v[232:235], v[204:207], v[244:247], v[232:235]\n" \
  "v_mfma_f32_16x16x32_bf16 v[236:239], v[220:223], v[244:247], v[236:239]\n" \
  "s_nop 4\n" \
  "v_cvt_pk_bf16_f32 v232, v232, v233\n" \
  "v_cvt_pk_bf16_f32 v234, v234, v235\n" \
  "s_nop 1\n" \
  "v_cvt_pk_bf16_f32 v233, v236, v237\n" \
  "v_cvt_pk_bf16_f32 v235, v238, v239\n" \
  "s_add_u32 %[cnt], %[so], 7*%c[js]+1*%c[ss]\n" \
  "buffer_store_dwordx4 v[232:235], v251, %[rs], %[cnt] offen sc1\n" \
  "s_nop 1\n"
#define ST_XB0 \
  "ds_read_b128 v[192:195], v251 offset:24576\n" \
  "ds_read_b128 v[208:211], v251 offset:28672\n" \
  "ds_read_b128 v[196:199], v251 offset:25600\n" \
  "ds_read_b128 v[212:215], v251 offset:29696\n" \
  "ds_read_b128 v[200:203], v251 offset:26624\n" \
  "ds_read_b128 v[216:219], v251 offset:30720\n" \
  "ds_read_b128 v[204:207], v251 offset:27648\n" \
  "ds_read_b128 v[220:223], v251 offset:31744\n" \
  "ds_read_b128 v[240:243], v250 offset:0\n" \
  "ds_read_b128 v[244:247], v250 offset:16384\n" \
  "s_waitcnt lgkmcnt(9)\n" \
  "v_mfma_f32_16x16x32_bf16 v[224:227], v[192:195], v[0:3], 0\n" \
  "s_waitcnt lgkmcnt(8)\n" \
  "v_mfma_f32_16x16x32_bf16 v[228:231], v[208:211], v[0:3], 0\n" \
  "s_waitcnt lgkmcnt(7)\n" \
  "v_mfma_f32_16x16x32_bf16 v[224:227], v[196:199], v[4:7], v[224:227]\n" \
  "s_waitcnt lgkmcnt(6)\n" \
  "v_mfma_f32_16x16x32_bf16 v[228:231], v[212:215], v[4:7], v[228:231]\n" \
  "s_waitcnt lgkmcnt(5)\n" \
  "v_mfma_f32_16x16x32_bf16 v[224:227], v[200:203], v[8:11], v[224:227]\n" \
  "s_waitcnt lgkmcnt(4)\n" \
  "v_mfma_f32_16x16x32_bf16 v[228:231], v[216:219], v[8:11], v[228:231]\n" \
  "s_waitcnt lgkmcnt(3)\n" \
  "v_mfma_f32_16x16x32_bf16 v[224:227], v[204:207], v[12:15], v[224:227]\n" \
  "s_waitcnt lgkmcnt(2)\n" \
  "v_mfma_f32_16x16x32_bf16 v[228:231], v[220:223], v[12:15], v[228:231]\n" \
  "v_mfma_f32_16x16x32_bf16 v[232:235], v[192:195], v[128:131], 0\n" \
  "v_mfma_f32_16x16x32_bf16 v[236:239], v[208:211], v[128:131], 0\n" \
  "v_mfma_f32_16x16x32_bf16 v[232:235], v[200:203], v[136:139], v[232:235]\n" \
  "v_cvt_pk_bf16_f32 v224, v224, v225\n" \
  "v_cvt_pk_bf16_f32 v226, v226, v227\n" \
  "v_mfma_f32_16x16x32_bf16 v[236:239], v[216:219], v[136:139], v[236:239]\n" \
  "v_cvt_pk_bf16_f32 v225, v228, v229\n" \
  "v_cvt_pk_bf16_f32 v227, v230, v231\n" \
  "s_waitcnt lgkmcnt(1)\n" \
  "v_mfma_f32_16x16x32_bf16 v[232:235], v[196:199], v[240:243], v[232:235]\n" \
  "s_add_u32 %[cnt], %[so], 0*%c[js]+0*%c[ss]\n" \
  "buffer_store_dwordx4 v[224:227], v251, %[rs], %[cnt] offen sc1\n" \
  "v_mfma_f32_16x16x32_bf16 v[236:239], v[212:215], v[240:243], v[236:239]\n" \
  "s_waitcnt lgkmcnt(0)\n" \
  "v_mfma_f32_16x16x32_bf16 v[232:235], v[204:207], v[244:247], v[232:235]\n" \
  "v_mfma_f32_16x16x32_bf16 v[236:239], v[220:223], v[244:247], v[236:239]\n" \
  "ds_read_b128 v[240:243], v250 offset:1024\n" \
  "ds_read_b128 v[244:247], v250 offset:17408\n" \
  "v_mfma_f32_16x16x32_bf16 v[224:227], v[192:195], v[64:67], 0\n" \
  "v_mfma_f32_16x16x32_bf16 v[228:231], v[208:211], v[64:67], 0\n" \
  "v_mfma_f32_16x16x32_bf16 v[224:227], v[196:199], v[68:71], v[224:227]\n" \
  "v_cvt_pk_bf16_f32 v232, v232, v233\n" \
  "v_cvt_pk_bf16_f32 v234, v234, v235\n" \
  "v_mfma_f32_16x16x32_bf16 v[228:231], v[212:215], v[68:71], v[228:231]\n" \
  "v_cvt_pk_bf16_f32 v233, v236, v237\n" \
  "v_cvt_pk_bf16_f32 v235, v238, v239\n" \
  "v_mfma_f32_16x16x32_bf16 v[224:227], v[200:203], v[72:75], v[224:227]\n" \
  "s_add_u32 %[cnt], %[so], 4*%c[js]+0*%c[ss]\n" \
  "buffer_store_dwordx4 v[232:235], v251, %[rs], %[cnt] offen sc1\n" \
  "v_mfma_f32_16x16x32_bf16 v[228:231], v[216:219], v[72:75], v[228:231]\n" \
  "v_mfma_f32_16x16x32_bf16 v[224:227], v[204:207], v[76:79], v[224:227]\n" \
  "v_mfma_f32_16x16x32_bf16 v[228:231], v[220:223], v[76:79], v[228:231]\n" \
  "v_mfma_f32_16x16x32_bf16 v[232:235], v[192:195], v[132:135], 0\n" \
  "v_mfma_f32_16x16x32_bf16 v[236:239], v[208:211], v[132:135], 0\n" \
  "v_mfma_f32_16x16x32_bf16 v[232:235], v[200:203], v[140:143], v[232:235]\n" \
  "v_cvt_pk_bf16_f32 v224, v224, v225\n" \
  "v_cvt_pk_bf16_f32 v226, v226, v227\n" \
  "v_mfma_f32_16x16x32_bf16 v[236:239], v[216:219], v[140:143], v[236:239]\n" \
  "v_cvt_pk_bf16_f32 v225, v228, v229\n" \
  "v_cvt_pk_bf16_f32 v227, v230, v231\n" \
  "s_waitcnt lgkmcnt(1)\n" \
  "v_mfma_f32_16x16x32_bf16 v[232:235], v[196:199], v[240:243], v[232:235]\n" \
  "s_add_u32 %[cnt], %[so], 0*%c[js]+1*%c[ss]\n" \
  "buffer_store_dwordx4 v[224:227], v251, %[rs], %[cnt] offen sc1\n" \
  "v_mfma_f32_16x16x32_bf16 v[236:239], v[212:215], v[240:243], v[236:239]\n" \
  "s_waitcnt lgkmcnt(0)\n" \
  "v_mfma_f32_16x16x32_bf16 v[232:235], v[204:207], v[244:247], v[232:235]\n" \
  "v_mfma_f32_16x16x32_bf16 v[236:239], v[220:223], v[244:247], v[236:239]\n" \
  "ds_read_b128 v[240:243], v250 offset:32768\n" \
  "ds_read_b128 v[244:247], v250 offset:49152\n" \
  "v_mfma_f32_16x16x32_bf16 v[224:227], v[192:195], v[16:19], 0\n" \
  "v_mfma_f32_16x16x32_bf16 v[228:231], v[208:211], v[16:19], 0\n" \
  "v_mfma_f32_16x16x32_bf16 v[224:227], v[196:199], v[20:23], v[224:227]\n" \
  "v_cvt_pk_bf16_f32 v232, v232, v233\n" \
  "v_cvt_pk_bf16_f32 v234, v234, v235\n" \
  "v_mfma_f32_16x16x32_bf16 v[228:231], v[212:215], v[20:23], v[228:231]\n" \
  "v_cvt_pk_bf16_f32 v233, v236, v237\n" \
  "v_cvt_pk_bf16_f32 v235, v238, v239\n" \
  "v_mfma_f32_16x16x32_bf16 v[224:227], v[200:203], v[24:27], v[224:227]\n" \
  "s_add_u32 %[cnt], %[so], 4*%c[js]+1*%c[ss]\n" \
  "buffer_store_dwordx4 v[232:235], v251, %[rs], %[cnt] offen sc1\n" \
  "v_mfma_f32_16x16x32_bf16 v[228:231], v[216:219], v[24:27], v[228:231]\n" \
  "v_mfma_f32_16x16x32_bf16 v[224:227], v[204:207], v[28:31], v[224:227]\n" \
  "v_mfma_f32_16x16x32_bf16 v[228:231], v[220:223], v[28:31], v[228:231]\n" \
  "v_mfma_f32_16x16x32_bf16 v[232:235], v[192:195], v[144:147], 0\n" \
  "v_mfma_f32_16x16x32_bf16 v[236:239], v[208:211], v[144:147], 0\n" \
  "v_mfma_f32_16x16x32_bf16 v[232:235], v[200:203], v[152:155], v[232:235]\n" \
  "v_cvt_pk_bf16_f32 v224, v224, v225\n" \
  "v_cvt_pk_bf16_f32 v226, v226, v227\n" \
  "v_mfma_f32_16x16x32_bf16 v[236:239], v[216:219], v[152:155], v[236:239]\n" \
  "v_cvt_pk_bf16_f32 v225, v228, v229\n" \
  "v_cvt_pk_bf16_f32 v227, v230, v231\n" \
  "s_waitcnt lgkmcnt(1)\n" \
  "v_mfma_f32_16x16x32_bf16 v[232:235], v[196:199], v[240:243], v[232:235]\n" \
  "s_add_u32 %[cnt], %[so], 1*%c[js]+0*%c[ss]\n" \
  "buffer_store_dwordx4 v[224:227], v251, %[rs], %[cnt] offen sc1\n" \
  "v_mfma_f32_16x16x32_bf16 v[236:239], v[212:215], v[240:243], v[236:239]\n" \
  "s_waitcnt lgkmcnt(0)\n" \
  "v_mfma_f32_16x16x32_bf16 v[232:235], v[204:207], v[244:247], v[232:235]\n" \
  "v_mfma_f32_16x16x32_bf16 v[236:239], v[220:223], v[244:247], v[236:239]\n" \
  "ds_read_b128 v[240:243], v250 offset:33792\n" \
  "ds_read_b128 v[244:247], v250 offset:50176\n" \
  "v_mfma_f32_16x16x32_bf16 v[224:227], v[192:195], v[80:83], 0\n" \
  "v_mfma_f32_16x16x32_bf16 v[228:231], v[208:211], v[80:83], 0\n" \
  "v_mfma_f32_16x16x32_bf16 v[224:227], v[196:199], v[84:87], v[224:227]\n" \
  "v_cvt_pk_bf16_f32 v232, v232, v233\n" \
  "v_cvt_pk_bf16_f32 v234, v234, v235\n" \
  "v_mfma_f32_16x16x32_bf16 v[228:231], v[212:215], v[84:87], v[228:231]\n" \
  "v_cvt_pk_bf16_f32 v233, v236, v237\n" \
  "v_cvt_pk_bf16_f32 v235, v238, v239\n" \
  "v_mfma_f32_16x16x32_bf16 v[224:227], v[200:203], v[88:91], v[224:227]\n" \
  "s_add_u32 %[cnt], %[so], 5*%c[js]+0*%c[ss]\n" \
  "buffer_store_dwordx4 v[232:235], v251, %[rs], %[cnt] offen sc1\n" \
  "v_mfma_f32_16x16x32_bf16 v[228:231], v[216:219], v[88:91], v[228:231]\n" \
  "v_mfma_f32_16x16x32_bf16 v[224:227], v[204:207], v[92:95], v[224:227]\n" \
  "v_mfma_f32_16x16x32_bf16 v[228:231], v[220:223], v[92:95], v[228:231]\n" \
  "v_mfma_f32_16x16x32_bf16 v[232:235], v[192:195], v[148:151], 0\n" \
  "v_mfma_f32_16x16x32_bf16 v[236:239], v[208:211], v[148:151], 0\n" \
  "v_mfma_f32_16x16x32_bf16 v[232:235], v[200:203], v[156:159], v[232:235]\n" \
  "v_cvt_pk_bf16_f32 v224, v224, v225\n" \
  "v_cvt_pk_bf16_f32 v226, v226, v227\n" \
  "v_mfma_f32_16x16x32_bf16 v[236:239], v[216:219], v[156:159], v[236:239]\n" \
  "v_cvt_pk_bf16_f32 v225, v228, v229\n" \
  "v_cvt_pk_bf16_f32 v227, v230, v231\n" \
  "s_waitcnt lgkmcnt(1)\n" \
  "v_mfma_f32_16x16x32_bf16 v[232:235], v[196:199], v[240:243], v[232:235]\n" \
  "s_add_u32 %[cnt], %[so], 1*%c[js]+1*%c[ss]\n" \
  "buffer_store_dwordx4 v[224:227], v251, %[rs], %[cnt] offen sc1\n" \
  "v_mfma_f32_16x16x32_bf16 v[236:239], v[212:215], v[240:243], v[236:239]\n" \
  "s_waitcnt lgkmcnt(0)\n" \
  "v_mfma_f32_16x16x32_bf16 v[232:235], v[204:207], v[244:247], v[232:235]\n" \
  "v_mfma_f32_16x16x32_bf16 v[236:239], v[220:223], v[244:247], v[236:239]\n" \
  "ds_read_b128 v[240:243], v249 offset:0\n" \
  "ds_read_b128 v[244:247], v249 offset:16384\n" \
  "v_mfma_f32_16x16x32_bf16 v[224:227], v[192:195], v[32:35], 0\n" \
  "v_mfma_f32_16x16x32_bf16 v[228:231], v[208:211], v[32:35], 0\n" \
  "v_mfma_f32_16x16x32_bf16 v[224:227], v[196:199], v[36:39], v[224:227]\n" \
  "v_cvt_pk_bf16_f32 v232, v232, v233\n" \
  "v_cvt_pk_bf16_f32 v234, v234, v235\n" \
  "v_mfma_f32_16x16x32_bf16 v[228:231], v[212:215], v[36:39], v[228:231]\n" \
  "v_cvt_pk_bf16_f32 v233, v236, v237\n" \
  "v_cvt_pk_bf16_f32 v235, v238, v239\n" \
  "v_mfma_f32_16x16x32_bf16 v[224:227], v[200:203], v[40:43], v[224:227]\n" \
  "s_add_u32 %[cnt], %[so], 5*%c[js]+1*%c[ss]\n" \
  "buffer_store_dwordx4 v[232:235], v251, %[rs], %[cnt] offen sc1\n" \
  "v_mfma_f32_16x16x32_bf16 v[228:231], v[216:219], v[40:43], v[228:231]\n" \
  "v_mfma_f32_16x16x32_bf16 v[224:227], v[204:207], v[44:47], v[224:227]\n" \
  "v_mfma_f32_16x16x32_bf16 v[228:231], v[220:223], v[44:47], v[228:231]\n" \
  "v_mfma_f32_16x16x32_bf16 v[232:235], v[192:195], v[160:163], 0\n" \
  "v_mfma_f32_16x16x32_bf16 v[236:239], v[208:211], v[160:163], 0\n" \
  "v_mfma_f32_16x16x32_bf16 v[232:235], v[200:203], v[168:171], v[232:235]\n" \
  "v_cvt_pk_bf16_f32 v224, v224, v225\n" \
  "v_cvt_pk_bf16_f32 v226, v226, v227\n" \
  "v_mfma_f32_16x16x32_bf16 v[236:239], v[216:219], v[168:171], v[236:239]\n" \
  "v_cvt_pk_bf16_f32 v225, v228, v229\n" \
  "v_cvt_pk_bf16_f32 v227, v230, v231\n" \
  "s_waitcnt lgkmcnt(1)\n" \
  "v_mfma_f32_16x16x32_bf16 v[232:235], v[196:199], v[240:243], v[232:235]\n" \
  "s_add_u32 %[cnt], %[so], 2*%c[js]+0*%c[ss]\n" \
  "buffer_store_dwordx4 v[224:227], v251, %[rs], %[cnt] offen sc1\n" \
  "v_mfma_f32_16x16x32_bf16 v[236:239], v[212:215], v[240:243], v[236:239]\n" \
  "s_waitcnt lgkmcnt(0)\n" \
  "v_mfma_f32_16x16x32_bf16 v[232:235], v[204:207], v[244:247], v[232:235]\n" \
  "v_mfma_f32_16x16x32_bf16 v[236:239], v[220:223], v[244:247], v[236:239]\n" \
  "ds_read_b128 v[240:243], v249 offset:1024\n" \
  "ds_read_b128 v[244:247], v249 offset:17408\n" \
  "v_mfma_f32_16x16x32_bf16 v[224:227], v[192:195], v[96:99], 0\n" \
  "v_mfma_f32_16x16x32_bf16 v[228:231], v[208:211], v[96:99], 0\n" \
  "v_mfma_f32_16x16x32_bf16 v[224:227], v[196:199], v[100:103], v[224:227]\n" \
  "v_cvt_pk_bf16_f32 v232, v232, v233\n" \
  "v_cvt_pk_bf16_f32 v234, v234, v235\n" \
  "v_mfma_f32_16x16x32_bf16 v[228:231], v[212:215], v[100:103], v[228:231]\n" \
  "v_cvt_pk_bf16_f32 v233, v236, v237\n" \
  "v_cvt_pk_bf16_f32 v235, v238, v239\n" \
  "v_mfma_f32_16x16x32_bf16 v[224:227], v[200:203], v[104:107], v[224:227]\n" \
  "s_add_u32 %[cnt], %[so], 6*%c[js]+0*%c[ss]\n" \
  "buffer_store_dwordx4 v[232:235], v251, %[rs], %[cnt] offen sc1\n" \
  "v_mfma_f32_16x16x32_bf16 v[228:231], v[216:219], v[104:107], v[228:231]\n" \
  "v_mfma_f32_16x16x32_bf16 v[224:227], v[204:207], v[108:111], v[224:227]\n" \
  "v_mfma_f32_16x16x32_bf16 v[228:231], v[220:223], v[108:111], v[228:231]\n" \
  "v_mfma_f32_16x16x32_bf16 v[232:235], v[192:195], v[164:167], 0\n" \
  "v_mfma_f32_16x16x32_bf16 v[236:239], v[208:211], v[164:167], 0\n" \
  "v_mfma_f32_16x16x32_bf16 v[232:235], v[200:203], v[172:175], v[232:235]\n" \
  "v_cvt_pk_bf16_f32 v224, v224, v225\n" \
  "v_cvt_pk_bf16_f32 v226, v226, v227\n" \
  "v_mfma_f32_16x16x32_bf16 v[236:239], v[216:219], v[172:175], v[236:239]\n" \
  "v_cvt_pk_bf16_f32 v225, v228, v229\n" \
  "v_cvt_pk_bf16_f32 v227, v230, v231\n" \
  "s_waitcnt lgkmcnt(1)\n" \
  "v_mfma_f32_16x16x32_bf16 v[232:235], v[196:199], v[240:243], v[232:235]\n" \
  "s_add_u32 %[cnt], %[so], 2*%c[js]+1*%c[ss]\n" \
  "buffer_store_dwordx4 v[224:227], v251, %[rs], %[cnt] offen sc1\n" \
  "v_mfma_f32_16x16x32_bf16 v[236:239], v[212:215], v[240:243], v[236:239]\n" \
  "s_waitcnt lgkmcnt(0)\n" \
  "v_mfma_f32_16x16x32_bf16 v[232:235], v[204:207], v[244:247], v[232:235]\n" \
  "v_mfma_f32_16x16x32_bf16 v[236:239], v[220:223], v[244:247], v[236:239]\n" \
  "ds_read_b128 v[240:243], v249 offset:32768\n" \
  "ds_read_b128 v[244:247], v249 offset:49152\n" \
  "v_mfma_f32_16x16x32_bf16 v[224:227], v[192:195], v[48:51], 0\n" \
  "v_mfma_f32_16x16x32_bf16 v[228:231], v[208:211], v[48:51], 0\n" \
  "v_mfma_f32_16x16x32_bf16 v[224:227], v[196:199], v[52:55], v[224:227]\n" \
  "v_cvt_pk_bf16_f32 v232, v232, v233\n" \
  "v_cvt_pk_bf16_f32 v234, v234, v235\n" \
  "v_mfma_f32_16x16x32_bf16 v[228:231], v[212:215], v[52:55], v[228:231]\n" \
  "v_cvt_pk_bf16_f32 v233, v236, v237\n" \
  "v_cvt_pk_bf16_f32 v235, v238, v239\n" \
  "v_mfma_f32_16x16x32_bf16 v[224:227], v[200:203], v[56:59], v[224:227]\n" \
  "s_add_u32 %[cnt], %[so], 6*%c[js]+1*%c[ss]\n" \
  "buffer_store_dwordx4 v[232:235], v251, %[rs], %[cnt] offen sc1\n" \
  "v_mfma_f32_16x16x32_bf16 v[228:231], v[216:219], v[56:59], v[228:231]\n" \
  "v_mfma_f32_16x16x32_bf16 v[224:227], v[204:207], v[60:63], v[224:227]\n" \
  "v_mfma_f32_16x16x32_bf16 v[228:231], v[220:223], v[60:63], v[228:231]\n" \
  "v_mfma_f32_16x16x32_bf16 v[232:235], v[192:195], v[176:179], 0\n" \
  "v_mfma_f32_16x16x32_bf16 v[236:239], v[208:211], v[176:179], 0\n" \
  "v_mfma_f32_16x16x32_bf16 v[232:235], v[200:203], v[184:187], v[232:235]\n" \
  "v_cvt_pk_bf16_f32 v224, v224, v225\n" \
  "v_cvt_pk_bf16_f32 v226, v226, v227\n" \
  "v_mfma_f32_16x16x32_bf16 v[236:239], v[216:219], v[184:187], v[236:239]\n" \
  "v_cvt_pk_bf16_f32 v225, v228, v229\n" \
  "v_cvt_pk_bf16_f32 v227, v230, v231\n" \
  "s_waitcnt lgkmcnt(1)\n" \
  "v_mfma_f32_16x16x32_bf16 v[232:235], v[196:199], v[240:243], v[232:235]\n" \
  "s_add_u32 %[cnt], %[so], 3*%c[js]+0*%c[ss]\n" \
  "buffer_store_dwordx4 v[224:227], v251, %[rs], %[cnt] offen sc1\n" \
  "v_mfma_f32_16x16x32_bf16 v[236:239], v[212:215], v[240:243], v[236:239]\n" \
  "s_waitcnt lgkmcnt(0)\n" \
  "v_mfma_f32_16x16x32_bf16 v[232:235], v[204:207], v[244:247], v[232:235]\n" \
  "v_mfma_f32_16x16x32_bf16 v[236:239], v[220:223], v[244:247], v[236:239]\n" \
  "ds_read_b128 v[240:243], v249 offset:33792\n" \
  "ds_read_b128 v[244:247], v249 offset:50176\n" \
  "v_mfma_f32_16x16x32_bf16 v[224:227], v[192:195], v[112:115], 0\n" \
  "v_mfma_f32_16x16x32_bf16 v[228:231], v[208:211], v[112:115], 0\n" \
  "v_mfma_f32_16x16x32_bf16 v[224:227], v[196:199], v[116:119], v[224:227]\n" \
  "v_cvt_pk_bf16_f32 v232, v232, v233\n" \
  "v_cvt_pk_bf16_f32 v234, v234, v235\n" \
  "v_mfma_f32_16x16x32_bf16 v[228:231], v[212:215], v[116:119], v[228:231]\n" \
  "v_cvt_pk_bf16_f32 v233, v236, v237\n" \
  "v_cvt_pk_bf16_f32 v235, v238, v239\n" \
  "v_mfma_f32_16x16x32_bf16 v[224:227], v[200:203], v[120:123], v[224:227]\n" \
  "s_add_u32 %[cnt], %[so], 7*%c[js]+0*%c[ss]\n" \
  "buffer_store_dwordx4 v[232:235], v251, %[rs], %[cnt] offen sc1\n" \
  "v_mfma_f32_16x16x32_bf16 v[228:231], v[216:219], v[120:123], v[228:231]\n" \
  "v_mfma_f32_16x16x32_bf16 v[224:227], v[204:207], v[124:127], v[224:227]\n" \
  "v_mfma_f32_16x16x32_bf16 v[228:231], v[220:223], v[124:127], v[228:231]\n" \
  "v_mfma_f32_16x16x32_bf16 v[232:235], v[192:195], v[180:183], 0\n" \
  "v_mfma_f32_16x16x32_bf16 v[236:239], v[208:211], v[180:183], 0\n" \
  "v_mfma_f32_16x16x32_bf16 v[232:235], v[200:203], v[188:191], v[232:235]\n" \
  "v_cvt_pk_bf16_f32 v224, v224, v225\n" \
  "v_cvt_pk_bf16_f32 v226, v226, v227\n" \
  "v_mfma_f32_16x16x32_bf16 v[236:239], v[216:219], v[188:191], v[236:239]\n" \
  "v_cvt_pk_bf16_f32 v225, v228, v229\n" \
  "v_cvt_pk_bf16_f32 v227, v230, v231\n" \
  "s_waitcnt lgkmcnt(1)\n" \
  "v_mfma_f32_16x16x32_bf16 v[232:235], v[196:199], v[240:243], v[232:235]\n" \
  "s_add_u32 %[cnt], %[so], 3*%c[js]+1*%c[ss]\n" \
  "buffer_store_dwordx4 v[224:227], v251, %[rs], %[cnt] offen sc1\n" \
  "v_mfma_f32_16x16x32_bf16 v[236:239], v[212:215], v[240:243], v[236:239]\n" \
  "s_waitcnt lgkmcnt(0)\n" \
  "v_mfma_f32_16x16x32_bf16 v[232:235], v[204:207], v[244:247], v[232:235]\n" \
  "v_mfma_f32_16x16x32_bf16 v[236:239], v[220:223], v[244:247], v[236:239]\n" \
  "s_nop 4\n" \
  "v_cvt_pk_bf16_f32 v232, v232, v233\n" \
  "v_cvt_pk_bf16_f32 v234, v234, v235\n" \
  "s_nop 1\n" \
  "v_cvt_pk_bf16_f32 v233, v236, v237\n" \
  "v_cvt_pk_bf16_f32 v235, v238, v239\n" \
  "s_add_u32 %[cnt], %[so], 7*%c[js]+1*%c[ss]\n" \
  "buffer_store_dwordx4 v[232:235], v251, %[rs], %[cnt] offen sc1\n" \
  "s_nop 1\n"
#define GDM 2
#define XBF_NB 5
#define XBS_NB 2
#define F_ORD "01ab2cd3"
#define VO_CLOB "v192", "v193", "v194", "v195", "v196", "v197", "v198", "v199", "v200", "v201", "v202", "v203", "v204", "v205", "v206", "v207", "v208", "v209", "v210", "v211", "v212", "v213", "v214", "v215", "v216", "v217", "v218", "v219", "v220", "v221", "v222", "v223", "v224", "v225", "v226", "v227", "v228", "v229", "v230", "v231", "v232", "v233", "v234", "v235", "v236", "v237", "v238", "v239"
#define QK_CLOB "v192", "v193", "v194", "v195", "v196", "v197", "v198", "v199", "v200", "v201", "v202", "v203", "v204", "v205", "v206", "v207", "v208", "v209", "v210", "v211", "v212", "v213", "v214", "v215", "v216", "v217", "v218", "v219", "v220", "v221", "v222", "v223", "v224", "v225", "v226", "v227", "v228", "v229", "v230", "v231", "v232", "v233", "v234", "v235", "v236", "v237", "v238", "v239"
#define QKK_CLOB "v192", "v193", "v194", "v195", "v196", "v197", "v198", "v199", "v200", "v201", "v202", "v203", "v204", "v205", "v206", "v207", "v208", "v209", "v210", "v211", "v212", "v213", "v214", "v215", "v216", "v217", "v218", "v219", "v220", "v221", "v222", "v223", "v224", "v225", "v226", "v227", "v228", "v229", "v230", "v231", "v232", "v233", "v234", "v235", "v236", "v238", "v239"
#define QX_CLOB "v216", "v217", "v218", "v219", "v220", "v221", "v222", "v223", "v224", "v225", "v226", "v227", "v228", "v229", "v230", "v231", "v232", "v233", "v234", "v235", "v236", "v237", "v238", "v239", "v241"
#define ST_CLOB "v192", "v193", "v194", "v195", "v196", "v197", "v198", "v199", "v200", "v201", "v202", "v203", "v204", "v205", "v206", "v207", "v240", "v241", "v242", "v243", "v244", "v245", "v246", "v247", "v253"
#define XB_CLOB "v192", "v193", "v194", "v195", "v196", "v197", "v198", "v199", "v200", "v201", "v202", "v203", "v204", "v205", "v206", "v207", "v208", "v209", "v210", "v211", "v212", "v213", "v214", "v215", "v216", "v217", "v218", "v219", "v220", "v221", "v222", "v223", "v224", "v225", "v226", "v227", "v228", "v229", "v230", "v231", "v232", "v233", "v234", "v235", "v236", "v237", "v238", "v239", "v240", "v241", "v242", "v243", "v244", "v245", "v246", "v247", "v253"
#define OF_CLOB "v32", "v33", "v34", "v35", "v36", "v37", "v38", "v39", "v40", "v41", "v42", "v43", "v44", "v45", "v46", "v47", "v48", "v49", "v50", "v51", "v52", "v53", "v54", "v55", "v56", "v57", "v58", "v59", "v60", "v61", "v62", "v63", "v64", "v65", "v66", "v67", "v68", "v69", "v70", "v71", "v72", "v73", "v74", "v75", "v76", "v77", "v78", "v79", "v80", "v81", "v82", "v83", "v84", "v85", "v86", "v87", "v88", "v89", "v90", "v91", "v92", "v93", "v94", "v95", "v96", "v97", "v98", "v99", "v100", "v101", "v102", "v103", "v104", "v105", "v106", "v107", "v108", "v109", "v110", "v111"
#define OE_CLOB "v192", "v193", "v194", "v195", "v196", "v197", "v198", "v199", "v200", "v201", "v202", "v203", "v204", "v205", "v206", "v207", "v208", "v209", "v210", "v211", "v212", "v213", "v214", "v215", "v216", "v217", "v218", "v219", "v220", "v221", "v222", "v223", "v224", "v225", "v226", "v227", "v228", "v229", "v230", "v231", "v232", "v233", "v234", "v235", "v236", "v237", "v238", "v239", "v240"
constexpr int L_W = 32768;
constexpr int L_HI = 16384;
#define MARG 130
#define ED0 700
#define EA0 1220
#define SDK 448
#define TOL 145
#define XLATE 200
#define TFR 800
#define QT 0
#define HSU 8
#define WNT 3
#define VAL 1
#define KROT 3
#define KSL2 12
#define KSL 2
#define NCY 1825
#define NCYV NCY
#define NRY 22
#define NRX 30
#define NF1O 2
#define NPUB 2
#define NXF 4
#define RSL 2
#define PFL 4
#define EVT2R
__device__ __forceinline__ int slow_head(const u16* wm) { return (int)(((3u - (unsigned)((uintptr_t)wm >> 7)) & 7u) >> 1); }
template <int role>
__device__ __forceinline__ void wroles(const int h, const int par, const int sl, const int xg, const int w, const u64 nonce, const u16* __restrict__ x,
                                       const u16* __restrict__ Wq, const u16* __restrict__ Wk, const u16* __restrict__ Wv,
                                       const u16* __restrict__ Wo, char* ws) {
  LAS char* const L = (LAS char*)g_lds;
  gu32* tmo = (gu32*)(ws + OFF_TMO);
  const __amdgpu_buffer_rsrc_t rs = __builtin_amdgcn_make_buffer_rsrc(ws, 0, (int)WS_USED, 0x00020000);
  constexpr bool vo = role & 1;
  v8u wa[8], wb[16];
  constexpr bool fuse = role >= 2;
  constexpr int RG = fuse ? 4 : 5, RB = fuse ? 32768 : 0;
  f32x4 fac[2][2];
  v2u nin = {0u, 0u}, hsv;
  unsigned arm = 0;
  v8u t13;
  PLDECL;
  const int krot = h & KROT;
  {
    const int lane = olane();
    const int l15 = lane & 15, g = lane >> 4, trq = l15 >> 2, trp = lane & 3;
    const char* bl = (const char*)((vo ? Wv : Wq) + h * DH);
    const char* br = (const char*)(vo ? Wo + (size_t)h * DH * D : Wk + h * DH);
    const int rot = role == 1 ? (h >> 4) * 4 : 0;
    int vl[2], vr[2];
#pragma unroll
    for (int q = 0; q < 2; ++q) {
      const int row = 4 * (2 * w + q) + g;
      const int cb = ((((l15 >> 1) ^ ((row & 3) | ((row >> 1) & 4))) << 5) | ((l15 & 1) << 4));
      const int i = row & 7;
      const int prow = (row & 32) + ((i & 4) << 2) + ((row >> 1) & 12) + (i & 3);
      vl[q] = row * 8192 + cb;
      vr[q] = vo ? (role == 1 ? row : prow) * 2048 + cb : vl[q];
    }
#define W_ISSUE(u)                                                                                   \
  {                                                                                                  \
    const char* pl = bl + (size_t)((u) ^ krot) * 64 * 8192;                                          \
    const char* pr = br + (role == 1 ? (size_t)(((u) >> 1) & 1) * 64 * 2048 + (2 * ((u) >> 2) + ((u) & 1)) * 256          \
                                     : (vo ? (size_t)((u) & 1) * 64 * 2048 + ((u) >> 1) * 256 : (size_t)((u) ^ krot) * 64 * 8192)); \
    _Pragma("unroll") for (int q = 0; q < 2; ++q) {                                                  \
      glds16<((WNT >> (role >> 1)) & 1) != 0>(pl, vl[q], RB + ((u) % RG) * 32768 + (2 * w + q) * 1024);                                \
      glds16<((WNT >> (role >> 1)) & 1) != 0>(pr, vr[q], RB + ((u) % RG) * 32768 + 16384 + (2 * w + q) * 1024);                        \
    }                                                                                                \
  }
    const int ta = role == 1 ? 8 + (w ^ rot) : 2 * w, tb = role == 1 ? 2 * (w & 3) : 2 * w + 1;
    const int sw = trq | ((g & 1) << 2);
    int pa = (ta >> 3) * 16384 + (8 * g + trq) * 256 + (((ta & 7) ^ sw) << 5) + trp * 8;
    int pb = (tb >> 3) * 16384 + (8 * g + trq) * 256 + (((tb & 7) ^ sw) << 5) + trp * 8;
    const int pw = L_W + w * 2048 + lane * 16;
#define W_FRAG(dst, p, u)                                                                            \
  {                                                                                                  \
    int p_ = p + RB + ((u) % RG) * 32768;                                                             \
    asm volatile("" : "+v"(p_));                                                                     \
    const v2u t0 = trd(L + p_), t1 = trd(L + p_ + 1024), t2 = trd(L + p_ + 8192), t3 = trd(L + p_ + 9216); \
    dst = v8u{t0[0], t0[1], t1[0], t1[1], t2[0], t2[1], t3[0], t3[1]};                               \
  }
#define W_PUT(c)                                           \
  {                                                        \
    int p_ = pw + ((c) - 8) * 16384;                       \
    asm volatile("" : "+v"(p_));                           \
    *(LAS v4u*)(L + p_) = lo4(tmp[(c) - 8]);               \
    *(LAS v4u*)(L + p_ + 1024) = hi4(tmp[(c) - 8]);        \
  }
    PL(0);
    const int fxv = (16 * (w & 1) + (lane >> 2)) * 2048 + (((lane ^ -g) & 3) << 4) + ((w >> 1) & 1) * 64;
    const int fxr = l15 * 64 + (((g ^ -trq) & 3) << 4);
#define X_ISSUE(j) glds16((const char*)x + par * 65536 + ((2 * (j) + (w >> 2)) ^ krot) * 128, fxv, (((j) + 1) & 3) * 8192 + w * 1024)
#define HWU 99
#define W_HELP(u)                                                                                                     \
  if (fuse && w >= 5) {                                                                                               \
    const int h0 = slow_head(Wq), rk = (h - h0 - 1) & 3;                                                              \
    const int q = (((role & 1) * 4 + par * 2 + ((h >> 2) & 1)) * 3 + rk) * 3 + w - 5, m = q / 24;                     \
    const u16* wm = m == 2 ? Wv : (m ? Wk : Wq);                                                                      \
    const int hs = (h & 24) + slow_head(wm) + 4 * ((q / 12) & 1);                                                     \
    if (rk < 3)                                                                                                       \
      touch4((const char*)wm + hs * 256 + (size_t)((15 - (q % 12 >> 1)) ^ (hs & KROT)) * 524288,                      \
             ((q & 1) * 32 + (lane >> 1)) * 8192 + (lane & 1) * 128, RB + ((u) % RG) * 32768 + 2 * w * 1024);          \
  }
    if (HWU == -1) W_HELP(0)
    W_ISSUE(0);
    if (fuse) {
#pragma unroll
      for (int j = 0; j < 4; ++j) X_ISSUE(j);
    }
#pragma unroll
    for (int u = 1; u < RG; ++u) W_ISSUE(u);
    PL(1);
    v8u tmp[8];
#pragma unroll
    for (int u = 0; u < 16; ++u) {
      {
        int young = 4 * (u + RG - 1 < 16 ? RG - 1 : 15 - u);
        if (fuse)
          for (int i = u - RG; i < u; ++i) young += i >= 0 && i < 8 && (i & 1);
        if (u > HSU && u < HSU + RG) young += 1;
        if (role < 2 && u > 13) young += 3;
        if (VFAR && role == 3 && u > 12 && u < 15) young += 2;
        asm volatile("s_waitcnt vmcnt(%0)" ::"n"(young) : "memory");
      }
      RAW_BARRIER();
      if ((u & 3) == 0 || u == 15) PL(2 + (u + 1) / 4);
      if constexpr (role == 1) {
#define WO_(n) ((n) < 8 ? wa[(n) & 7] : ((n) < 10 ? wb[14 + ((n) & 1)] : tmp[(n) >= 10 ? (n) - 10 : 0]))
#define WV_(n) ((n) < 14 ? wb[(n) < 14 ? (n) : 0] : tmp[6 + ((n) & 1)])
        if (u == 12) { W_PUT(8); W_PUT(9); }
        W_FRAG(WO_(u), pa, u)
        if (VAL ? (w >> 2) == (((u ^ krot) >> 2) & 1) : (w >> 2) == (u >> 3)) {
          const int sl = VAL ? (u & 3) + 4 * (u >> 3) : u & 7;
          W_FRAG(WV_(2 * sl), pb, u)
          W_FRAG(WV_(2 * sl + 1), (pb ^ 32), u)
        }
        if (u & 1) {
          v8u& f0 = WO_(u & ~1);
          v8u& f1 = WO_(u);
          const v8u n0 = {f0[0], f1[0], f0[1], f1[1], f0[2], f1[2], f0[3], f1[3]};
          const v8u n1 = {f0[4], f1[4], f0[5], f1[5], f0[6], f1[6], f0[7], f1[7]};
          f0 = n0;
          f1 = n1;
          asm volatile("" : "+v"(f0), "+v"(f1));
        }
        if (u == 13) { W_PUT(10); W_PUT(11); }
        if (u == 15) { W_PUT(12); W_PUT(13); W_PUT(14); W_PUT(15); }
      } else if constexpr (role == 0) {
        if (u == 12) { W_PUT(8); W_PUT(9); }
        if (u == 13) { W_PUT(10); W_PUT(11); }
        if (u == 14) { W_PUT(12); W_PUT(13); }
        if (u == 15) { W_PUT(14); }
        if (u < 8) W_FRAG(wa[u < 8 ? u : 0], pa, u) else W_FRAG(tmp[u >= 8 ? u - 8 : 0], pa, u)
        W_FRAG(wb[u], pb, u)
        if (u == 15) W_PUT(15);
      } else {
        if (u == 15 && role == 2 && w == 0) hs_wait(rs, nonce, hsv);
        if (u >= 13) W_PUT(u >= 13 ? 2 * u - 18 : 8);
        if (u == 13 || u == 14) W_PUT(u >= 13 ? 2 * u - 17 : 8);
        v8u fa, fb;
        W_FRAG(fa, pa, u)
        W_FRAG(fb, pb, u)
        if (u < 8) {
          wa[u & 7] = fa;
          wb[u] = fb;
        } else {
          wb[u] = cat8(lo4(fa), lo4(fb));
          tmp[u & 7] = cat8(hi4(fa), hi4(fb));
        }
        if (role == 2 || w < 4) {
#pragma unroll
          for (int hf = 0; hf < 2; ++hf)
#pragma unroll
            for (int rt = 0; rt < 2; ++rt) {
              const v4u xq = *(const LAS v4u*)(L + fxr + ((u + 2) & 7) * 4096 + hf * 2048 + rt * 1024);
              fac[0][rt] = mma(hf ? hi4(fa) : lo4(fa), xq, u + hf == 0 ? f32x4{0.f, 0.f, 0.f, 0.f} : fac[0][rt]);
              fac[1][rt] = mma(hf ? hi4(fb) : lo4(fb), xq, u + hf == 0 ? f32x4{0.f, 0.f, 0.f, 0.f} : fac[1][rt]);
            }
          asm volatile("" : "+v"(fac[0][0]), "+v"(fac[0][1]), "+v"(fac[1][0]), "+v"(fac[1][1]));
          if (u == 15) {
            PL(16);
            const int l16 = olane() * 16;
#pragma unroll
            for (int rt = 0; rt < 2; ++rt) {
              const v4u o = {pk2(fac[0][rt][0], fac[0][rt][1]), pk2(fac[0][rt][2], fac[0][rt][3]), pk2(fac[1][rt][0], fac[1][rt][1]),
                             pk2(fac[1][rt][2], fac[1][rt][3])};
              if (role == 3) *(LAS v4u*)(L + 24576 + rt * 4096 + w * 1024 + l16) = o;
              else st128(o, rs, l16, qk_addr(w >> 2, par, h) + rt * QK_RT + (w & 3) * 1024);
            }
            if (role == 2) {
              W_PUT(13);
              VMCNT(0);
            }
          }
        } else if (u == 15) {
          W_PUT(13);
          if (w == 7) {
            const bool ok = hs_wait(rs, nonce, hsv);
            if (VFAR) {
              if (__builtin_expect(!ok || !__all((nin[0] != 0xffffffffu) & (nin[1] != 0xffffffffu)), 0)) nin = numa_load(rs, tmo);
              *(volatile LAS int*)(L + L_W + 5 * 16384) = numa_far(nin);
            }
          }
        }
      }
      if (u == HSU) hsv = __builtin_amdgcn_raw_buffer_load_b64(rs, w == (role == 3 ? 7 : 0) ? 0 : OOB, OFF_ACK + (int)BIDX * 8, AUX_SC1);
      if (role < 2 && u == 13) {
        const int a = w == 0 ? olane() * 4 : OOB;
        arm = __builtin_amdgcn_raw_buffer_load_b32(rs, a & OOB, CNT_ARM, AUX_SC1);
        nin[0] = __builtin_amdgcn_raw_buffer_load_b32(rs, a, (int)OFF_CAL, AUX_SC1);
        nin[1] = __builtin_amdgcn_raw_buffer_load_b32(rs, (a & OOB) | (((a >> 5) & (NXC - 1)) * 4), (int)OFF_XID, AUX_SC1);
      }
      if (VFAR && role == 3 && u == 12) {
        const int a = w == 7 ? olane() * 4 : OOB;
        nin[0] = __builtin_amdgcn_raw_buffer_load_b32(rs, a, (int)OFF_CAL, AUX_SC1);
        nin[1] = __builtin_amdgcn_raw_buffer_load_b32(rs, (a & OOB) | (((a >> 5) & (NXC - 1)) * 4), (int)OFF_XID, AUX_SC1);
      }
      if (u + RG < 16) {
        RAW_BARRIER();
        if (HWU == u) W_HELP(u + RG)
        W_ISSUE(u + RG);
        if (fuse && u < 8 && (u & 1)) X_ISSUE(4 + (u >> 1));
      }
    }
    PL(17);
    RAW_BARRIER();
    PL(18);
    if (VFAR && role == 3 && w >= 4) arm = __builtin_amdgcn_readfirstlane(*(volatile LAS int*)(L + L_W + 5 * 16384));
    if (role == 2 && w == 0) __builtin_amdgcn_raw_buffer_store_b32(0u, rs, olane() << 28, fqk(h, par), AUX_SC1);
    if constexpr (fuse) {
      if (role == 2 || w < 4) {
        constexpr int B0 = role == 3 ? 6 : 8, PER = role == 3 ? 2 : 4, SL = role == 3 ? 12288 : 16384;
        const int ln = olane();
        const int xv = (16 * (w & 1) + (ln >> 2)) * 2048 + (((ln ^ -(ln >> 4)) & 3) << 4) + ((w >> 1) & 1) * 1024 + (role == 3 ? 0 : (w >> 2) * 64);
#pragma unroll
        for (int k = 0; k < 2; ++k)
#pragma unroll
          for (int j = 0; j < B0 / PER; ++j)
            glds16((const char*)x + (2 + par) * 65536 + (((k * B0 + j * PER) / 2 * 64) ^ (krot * 128)), xv, k * SL + j * PER * 2048 + w * 1024);
        PL(19);
        if (role == 3) t13 = tmp[5];
      }
      W_PUT(14);
      W_PUT(15);
    }
  }
  const int lane = olane();
  const int l15 = lane & 15, g = lane >> 4, trq = l15 >> 2, trp = lane & 3;
  int pw0 = L_W + w * 2048 + lane * 16, pw1 = pw0 + 65536;
  asm volatile("" : "+v"(pw0), "+v"(pw1));
#define WA_P(c) (L + ((c) < 12 ? pw0 : pw1) + ((c) & 3) * 16384)
#define WA_LDS(c) cat8(*(const LAS v4u*)WA_P(c), *(const LAS v4u*)(WA_P(c) + 1024))
  const int r_a = (int)(((uintptr_t)ws >> 19) & 7);
  const int r_s = r_a + (int)(OFF_STEP / UNIT);

  typedef int v4i __attribute__((ext_vector_type(4)));
#define WPIN "{v[0:31]}"(W0), "{v[32:63]}"(W1), "{v[64:95]}"(W2), "{v[96:127]}"(W3), "{v[128:159]}"(W4), "{v[160:191]}"(W5)
  const v4u rsv = {(unsigned)(uintptr_t)ws, (unsigned)((uintptr_t)ws >> 32) & 0xffffu, (unsigned)WS_USED, 0x00020000u};
  if constexpr (role >= 2) {
    constexpr bool isx = role == 3;
    const v32u W0 = cat32(wa[0], wa[1], wa[2], wa[3]), W1 = cat32(wa[4], wa[5], wa[6], wa[7]), W2 = cat32(wb[0], wb[1], wb[2], wb[3]);
    const v32u W3 = cat32(wb[4], wb[5], wb[6], wb[7]), W4 = cat32(wb[8], wb[9], wb[10], wb[11]), W5 = cat32(wb[12], wb[13], wb[14], wb[15]);
    const v4u rxv = {(unsigned)(uintptr_t)x, (unsigned)((uintptr_t)x >> 32) & 0xffffu, (unsigned)(S * D * 2), 0x00020000u};
    v4i adr;
    adr[3] = lane * 16;
    adr[2] = pw0;
    adr[1] = pw1;
    adr[0] = (16 * (w & 1) + (lane >> 2)) * 2048 + (((lane ^ -g) & 3) << 4) + ((w >> 1) & 1) * 1024 + (isx ? 0 : (w >> 2) * 64);
    const int xr = l15 * 64 + (((g ^ -trq) & 3) << 4);
#define FOF(sg) (isx ? fvw(w & 3, h, sg) : fqk(h, sg))
    const int nrel = isx ? TN(8, NRX) : TN(7, NRY);
    const int nit = par ? nrel : NSEG - nrel;
#define SEGOF(i) ((i) < nrel ? 2 * (i) + par : (i) + nrel)
#define ST_FLAG(sg)                                                                                                   \
  {                                                                                                                   \
    VMCNT(0);                                                                                                         \
    RAW_BARRIER();                                                                                                    \
    if (w == 0) __builtin_amdgcn_raw_buffer_store_b32(0u, rs, olane() << 28, FOF(sg), AUX_SC1);               \
  }
#define XB_OPS(sg, sp)                                                                                                  \
  : [cnt] "=&s"(cnt) EVOUT                                                                                              \
  : WPIN, "{v[248:251]}"(adr), [rs] "s"(rsv), [so] "s"(vw_addr(vfr, 2 * (w - 4), sg, h >> 2) + (h & 3) * 1024),               \
    [js] "i"((VFAR ? 64 : GST) * 262144), [ss] "i"(VFAR ? 65536 : 32768), [fo] "s"(FOF(sp))                                                 \
  : XB_CLOB, "scc", "memory"
#define XB_FLAG(sg) __builtin_amdgcn_raw_buffer_store_b8(0, rs, olane() << 28, FOF(sg), AUX_SC1)
    PL(7);
    if (isx && w >= 4) {
      const int vfr = (__builtin_amdgcn_readfirstlane(arm) ^ (VFAR == 2 ? 8192 : 0)) | (r_a << 19);
      {
        EVDECL;
        unsigned cnt;
        PL(20);
        asm volatile(ST_XBS XB_OPS(par, par));
        PL(21);
      }
      int it = 1;
#pragma unroll 1
      for (;;) {
        VMCNT(0);
        XB_FLAG(SEGOF(it - 1));
        if (it == 1) { PL(9); PLDUMP(4, 0x300200); }
        if (it == 2) PTW(14, 4);
        if (it == 3) PTW(15, 4);
        for (int k = it == 1 ? XBS_NB - 1 : XBF_NB; k < 6; ++k) BARE_BARRIER();
        if (++it > NXF) break;
        EVDECL;
        unsigned cnt;
        asm volatile(ST_XBF XB_OPS(SEGOF(it - 1), par));
        EVT2R
      }
#pragma unroll 1
      for (; it < nit; ++it) {
        EVDECL;
        unsigned cnt;
        asm volatile(ST_XB XB_OPS(SEGOF(it - 1), SEGOF(it - 2)));
        EVT2R
      }
      BARE_BARRIER();
      {
        EVDECL;
        unsigned cnt;
        asm volatile(ST_XB0 XB_OPS(SEGOF(nit - 1), par));
      }
      BARE_BARRIER();
      VMCNT(0);
      XB_FLAG(SEGOF(nit - 2));
      XB_FLAG(SEGOF(nit - 1));
      return;
    }
    if (w == 1) cnt_inc(ws, CNT_HS);
    VMCNT(0);
    RAW_BARRIER();
    if (isx) {
      *(LAS v4u*)(L + pw1 + 16384) = lo4(t13);
      *(LAS v4u*)(L + pw1 + 17408) = hi4(t13);
    }
    PL(10);
    PLDUMP(0, 0xf05ff);
    {
      v32u xf;
#pragma unroll
      for (int q = 0; q < 8; ++q) {
        const v4u t = *(const LAS v4u*)(L + xr + q * 1024);
#pragma unroll
        for (int j = 0; j < 4; ++j) xf[4 * q + j] = t[j];
      }
#pragma unroll 1
      for (int it = 1; it < nit; ++it) {
        EVDECL;
        const int seg = SEGOF(it);
        unsigned cnt, keep;
#define ST_OPS                                                                                                                 \
  : [cnt] "=&s"(cnt), [keep] "=&s"(keep), "+{v[208:239]}"(xf) EVOUT                                                            \
  : WPIN, "{v[248:251]}"(adr), "{v252}"(xr), [rs] "s"(rsv), [rx] "s"(rxv), [sc] "s"(seg * 65536 + krot * 128),                    \
    [sn] "s"(SEGOF(min(it + 1, nit - 1)) * 65536 + krot * 128),                                                                  \
    [mw] "s"(w * 1024), [so] "s"(isx ? w * 1024 : qk_addr(w >> 2, seg, h) + (w & 3) * 1024), [rt] "i"(QK_RT), \
    [fo] "s"(FOF(SEGOF(it - 1))), [fm] "s"(w == 0 ? 0 : 1 << 30), [tm] "s"(w == 3 ? 0 : 1 << 30),            \
    [tc] "s"(min(SEGOF(it + 2), NSEG - 1) * 65536 + sl * 8192)                                                                   \
  : ST_CLOB, "scc", "memory"
        if constexpr (isx) asm volatile(ST_XA ST_OPS);
        else asm volatile(ST_Y ST_OPS);
        EVT2R
        if (it < 4) PTW(10 + it, 2);
        if (!isx && it < NPUB) ST_FLAG(seg)
      }
    }
    if (isx) {
      RAW_BARRIER();
      BARE_BARRIER();
    } else ST_FLAG(SEGOF(nit - 1))
    return;
  }

#define TSK 700
#define KDM 1
#define TC2 0
#define KF0 4
  volatile LAS unsigned* sword = (volatile LAS unsigned*)(L + L_HI + 6408);
  if (w == 0) {
    const bool ok = hs_wait(rs, nonce, hsv);
    cnt_inc(ws, CNT_HS);
    if (__builtin_expect(!ok || !__all((arm + 1u >= 64u) & (nin[0] != 0xffffffffu) & (nin[1] != 0xffffffffu)), 0)) {
      arm_wait(rs, tmo);
      nin = numa_load(rs, tmo);
    }
    sword[0] = numa_perm(nin, (LAS unsigned*)(L + L_HI + 6656));
    sword[1] = 0u;
  }
  Clk ck = {0u, 0u, 92500u, 46432u};
  PL(7);
  RAW_BARRIER();
  PL(8);
  PLDUMP(0, 0xf01ff);
  const unsigned perm = __builtin_amdgcn_readfirstlane(sword[0]);
  const int cown = cls_of(perm, xg);
  const int l16 = lane * 16;
  const int sidx = ((l15 & 2) ? 0xeeee : 0x4444) | ((l15 & 8) ? 0xeeee0000 : 0x44440000);
  LAS char* const sm = L + l16;
  constexpr int TWV = 5;
  typedef int v8i __attribute__((ext_vector_type(8)));
  const int mg_ = NS2F(TN(0, MARG)), tol_ = NS2F(TN(13, TOL)), xl_ = NS2F(TN(14, XLATE));
  Pace pc = {0u, NS2F(ED0), NS2F(EA0), 0};
  unsigned tg = 0, tw = 0;
#define MEM_OFF(c) (R_MEM + ((c) >> 2) * 32768 + ((c) & 3) * 1024)
  if (w == 0) *(volatile LAS u64*)(L + L_HI + 7176 + l16) = 0ull;
#define DMDECL unsigned keep; int fs = s + 1
#define DMOUT , [keep] "=&s"(keep), [fs] "+s"(fs)
#define DMIN(x) , [o3] "s"(x)
#define TLB_TOUCH                    \
  if (((r_s + s + 2) & 3) == 0)      \
    tch = __builtin_amdgcn_raw_buffer_load_b32(rs, 0, so + 2 * (int)UNIT + R_DUM + (role * 32 + h) * 128, AUX_SC1);
#define TQOUT
#define TQACC
#define STCNT
#define ST_ADR (lst * 32 + 32768 + 2048)
#define TIMING_WAVE                                                                                  \
  {                                                                                                  \
    if (__builtin_expect(anysent(st[0]), 0)) _Pragma("clang loop unroll(disable)") for (unsigned spins = 0;;) { \
      if (spins) __builtin_amdgcn_s_sleep(4);                                                        \
      STCNT                                                                                          \
      if (spin_fail(spins, tmo, 0x30009)) break;                                                     \
      const int lst = olane();                                                                       \
      OPQ(va, ST_ADR - 2048);                                                                        \
      st = __builtin_amdgcn_raw_buffer_load_b64(rs, va, so + R_ST + ((cown ^ xr) << 12), AUX_SC1);   \
      if (!anysent(st[0])) break;                                                                       \
    }                                                                                                \
    TQACC                                                                                            \
    unsigned m0_ = (ck.r << 4) + 65536u - st[0], m1_ = ~st[1];                                       \
    wave_min2(m0_, m1_);                                                                             \
    const unsigned rf = (ck.r << 4) + 65536u - __builtin_amdgcn_readfirstlane(m0_);                  \
    pace_step<false>(pc, s, rf, ~__builtin_amdgcn_readfirstlane(m1_), mg_, tol_, xl_);               \
    EV(6);                                                                                           \
    EVDUMP(s);                                                                                       \
    EVCLK(s, ck);                                                                                    \
  }
#define TIMING_GO                                                                                    \
  if (w == TWV && s > 0) {                                                                           \
    tg = clk_cyc(ck, (int)(pc.rf - (ck.r << 4)) + pc.dq);                                            \
    const int lf = (int)(tg - cyc32()) & ~1;                                                         \
    if (lf > TSK || (int)(tg - ck.m) > 7000 || s < 4) {                    \
      wait_cyc(tg - TFR, lf - TFR);                                                                  \
      if (__builtin_expect(s < 3, 0)) clk_update(ck, s == 1);              \
      else clk_step(ck);                                                                             \
      tg = clk_cyc(ck, (int)(pc.rf - (ck.r << 4)) + pc.dq);                                          \
    }                                                                                                \
    tw = (unsigned)max(CMAX - (int)(tg - ck.m), 0);                                                  \
  }

  if constexpr (role == 0) {
    const v32u W0 = cat32(wa[0], wa[1], wa[2], wa[3]), W1 = cat32(wa[4], wa[5], wa[6], wa[7]), W2 = cat32(wb[0], wb[1], wb[2], wb[3]);
    const v32u W3 = cat32(wb[4], wb[5], wb[6], wb[7]), W4 = cat32(wb[8], wb[9], wb[10], wb[11]), W5 = cat32(wb[12], wb[13], wb[14], wb[15]);
    v8i adr;
    adr[7] = l16;
    adr[6] = l16 + w * 1024;
    adr[5] = pw0;
    adr[4] = sidx;
    adr[3] = L_HI + (w >> 2) * 3072 + (32 * (w & 3) + l15) * 24 + 4 * g;
    adr[2] = (4 * g + trq) * 24 + (trp & 1) * 8;
#define CORD(i) ((((i) & 1) ? (i) >> 1 : 8 + ((i) >> 1)) ^ krot)
    const int mo0 = MEM_OFF(CORD(w));
#define K_STAGE(sn)                                                                                                   \
  {                                                                                                                   \
    v4u kn[4];                                                                                                        \
    _Pragma("clang loop unroll(disable)") for (unsigned spins = 0; krdy <= (sn);) {    \
      OPQ(vfl, fqk(h, min((sn) + (olane() & 15), NSEG - 1)));                                           \
      const u64 miss = __builtin_amdgcn_ballot_w64(__builtin_amdgcn_raw_buffer_load_b32(rs, vfl, 0, AUX_SC1) != 0u);  \
      krdy = (sn) + (miss ? __builtin_ctzll(miss) : 16);                                                              \
      if (krdy > (sn)) break;                                                                                         \
      __builtin_amdgcn_s_sleep(8);                                                                                    \
      if (spin_fail(spins, tmo, 0x30000)) break;                                                                      \
    }                                                                                                                 \
    const int lk = olane() * 16;                                                                                      \
    if (KDM) {                                         \
      unsigned keep;                                                                                                  \
      asm volatile("s_mov_b32 %0, m0\n\ts_mov_b32 m0, %4\n\ts_nop 4\n\t"                                              \
                   "buffer_load_dwordx4 %1, %2, %3 offen sc1 lds\n\tbuffer_load_dwordx4 %1, %2, %3 offen offset:1024 sc1 lds\n\t" \
                   "buffer_load_dwordx4 %1, %2, %3 offen offset:2048 sc1 lds\n\tbuffer_load_dwordx4 %1, %2, %3 offen offset:3072 sc1 lds\n\t" \
                   "s_mov_b32 m0, %0"                                                                                 \
                   : "=&s"(keep)                                                                                      \
                   : "v"(lk), "s"(rsv), "s"(qk_addr(1, sn, h) + (w & 1) * QK_RT), "s"(L_HI + 8192 + (w & 1) * 4096)    \
                   : "memory");                                                                                       \
    } else {                                                                                                          \
      _Pragma("unroll") for (int q = 0; q < 4; ++q) kn[q] = __builtin_amdgcn_raw_buffer_load_b128(                    \
          rs, lk + q * 1024, qk_addr(1, sn, h) + (w & 1) * QK_RT, AUX_SC1);                                           \
      _Pragma("unroll") for (int q = 0; q < 4; ++q) *(LAS v4u*)(L + L_HI + 8192 + (w & 1) * 4096 + q * 1024 + lk) = kn[q]; \
    }                                                                                                                 \
  }
    int krdy = 1;
    const bool kst = w >= 4 && ((w ^ TWV) & 2);
    PTW(9, 7);
    if (kst) {
      wait_flag<KF0>(rs, fqk(h, 0), tmo, 0x30001);
      sword[1] = 1u;
      K_STAGE(0)
    }
    PTW(10, 7);
    if (w == TWV) {
      _Pragma("clang loop unroll(disable)") for (unsigned spins = 0; !__builtin_amdgcn_readfirstlane(sword[1]) && spins < kSpinCap; ++spins)
        __builtin_amdgcn_s_sleep(2);
      clk_edge(ck.r, ck.m, 2);
    }
    if (w < 2) {
      const int lane = olane(), l15 = lane & 15, l16 = lane * 16;
      const bool up = lane >= 32;
      adr[1] = R_EX + (h >> 3) * 32768 + (h & 7) * 512 + w * 256 + (lane & 32) * 4 + (lane & 7) * 16 + (lane & 24) / 2;
      const int rq = (lane & 1) + 2 * (lane & 6);
      adr[0] = up ? R_PM + (h >> 3) * 1024 + (rq + (l15 >> 3) * 2 + 16 * ((h & 7) >> 1)) * 16 + (h & 1) * 8 + (lane & 16) / 4
                  : (lane < 8 ? R_SC + SC_POS(h >> 2, lane) + (2 * (h & 1) + ((h >> 1) & 1)) * 4 : OOB);
      const unsigned msk = up ? 0x7fffffffu : ((lane & 1) ? 0x7fff0000u : 0xffffu);
      const int kxo = L_HI + 8192 + w * 4096 + l16;
#pragma unroll 1
      for (int s = 0; s < NSEG; ++s) {
        EVDECL;
        const int so = (int)OFF_STEP + s * (int)UNIT, xr = xreg(r_s + s);
#define CG(r) (so + ((cls_of(perm, (r) ^ (2 * w)) ^ xr) << 12))
        unsigned cnt;
        u64 wd;
        DMDECL;
        asm volatile(QK_A
                     : [cnt] "=&s"(cnt), [wd] "=&s"(wd) DMOUT EVOUT
                     : WPIN, "{v[244:251]}"(adr), "{v243}"(pw1), "{v242}"(kxo), "{v240}"(msk), [rs] "s"(rsv),
                       [o0] "s"(so + mo0 + ((cown ^ xr) << 12)), [c0] "s"(CG(0)), [c1] "s"(CG(1)), [c2] "s"(CG(2)), [c3] "s"(CG(3)),
                       [hf] "s"(0xff00ff00ff00ff00ull), [up] "s"(0xffffffff00000000ull)
                     : QK_CLOB, "v241", "vcc", "scc", "memory");
        if (s < 4) PT(11 + s);
        EVDUMP(s);
      }
    } else if (w < 4) {
      const float scale = 0.08838834764831845f, L2E = 1.4426950408889634f;
      adr[0] = adr[1] = 0;
#pragma unroll 1
      for (int s = 0; s < NSEG; ++s) {
        {
          EVDECL;
          const int so = (int)OFF_STEP + s * (int)UNIT, xr = xreg(r_s + s);
          unsigned cnt;
          v16u qv;
          f32x4 sc[3];
          DMDECL;
          asm volatile(QK_X
                       : [cnt] "=&s"(cnt) DMOUT, "={v[200:215]}"(qv), "={v[192:195]}"(sc[0]), "={v[196:199]}"(sc[1]) EVOUT
                       : WPIN, "{v[244:251]}"(adr), "{v243}"(pw1), [rs] "s"(rsv), [o0] "s"(so + mo0 + ((cown ^ xr) << 12)),
                         [qo] "s"(qk_addr(0, s, h) + (w - 2) * QK_RT)
                       : QX_CLOB, "vcc", "scc", "memory");
          const int ln = olane(), l15 = ln & 15, g = ln >> 4, l16 = ln * 16;
          v4u qx[4];
#pragma unroll
          for (int ks = 0; ks < 4; ++ks) qx[ks] = v4u{qv[4 * ks], qv[4 * ks + 1], qv[4 * ks + 2], qv[4 * ks + 3]};
          LAS char* const q_rd = L + L_HI + adr[2];
#define QFRAG(o, ks) [&] { const v2u tx = trd(q_rd + (o) + (ks) * 768), ty = trd(q_rd + (o) + (ks) * 768 + 384); return v4u{tx[0], tx[1], ty[0], ty[1]}; }()
          sc[2] = f32x4{0.f, 0.f, 0.f, 0.f};
#pragma unroll
          for (int ks = 0; ks < 4; ++ks) sc[2] = mma(QFRAG(3072, ks), qx[ks], sc[2]);
          EV(4);
          const int tok = 16 * (w - 2) + l15 - 4 * g;
#pragma unroll
          for (int t = 0; t < 3; ++t)
#pragma unroll
            for (int j = 0; j < 4; ++j) {
              float v = rbf(mul1(rbf(sc[t][j]), scale));
              if (t < 2 && 16 * t + j > tok) v = -1000000000.0f;
              sc[t][j] = v;
            }
          float mx;
          asm("v_max3_f32 %0, %1, %2, %3\n\tv_max3_f32 %0, %0, %4, %5\n\tv_max3_f32 %0, %0, %6, %7\n\tv_max3_f32 %0, %0, %8, %9\n\t"
              "v_max3_f32 %0, %0, %10, %11\n\tv_max_f32 %0, %0, %12\n\ts_nop 1"
              : "=&v"(mx)
              : "v"(sc[0][0]), "v"(sc[0][1]), "v"(sc[0][2]), "v"(sc[0][3]), "v"(sc[1][0]), "v"(sc[1][1]), "v"(sc[1][2]), "v"(sc[1][3]),
                "v"(sc[2][0]), "v"(sc[2][1]), "v"(sc[2][2]), "v"(sc[2][3]));
          mx = xmax16_32(mx);
          const float nmx = -mx * L2E;
#pragma unroll
          for (int t = 0; t < 3; ++t)
#pragma unroll
            for (int j = 0; j < 4; ++j) sc[t][j] = __builtin_amdgcn_exp2f(__builtin_fmaf(sc[t][j], L2E, nmx));
          const f32x4 s4 = sc[0] + sc[1] + 0.5f * sc[2];
          const float inv = __builtin_amdgcn_rcpf(xsum16_32((s4[0] + s4[1]) + (s4[2] + s4[3])));
#pragma unroll
          for (int t = 0; t < 3; ++t) sc[t] *= inv;
          const v4u o = {pk2(sc[0][0], sc[0][1]), pk2(sc[1][0], sc[1][1]), pk2(sc[0][2], sc[0][3]), pk2(sc[1][2], sc[1][3])};
          const v2u o2 = {pk2(sc[2][0], sc[2][1]), pk2(sc[2][2], sc[2][3])};
          const int pb = so + R_PX + (w - 2) * 40960;
          st128(o, rs, l16, pb + h * 1024);
          __builtin_amdgcn_raw_buffer_store_b64(o2, rs, g >= 2 ? l15 * 16 + (g & 1) * 8 : OOB,
                                                pb + (32 + (h >> 2)) * 1024 + (h & 3) * 256, AUX_SC1);
          EV(6);
          EVDUMP(s);
        }
      }
    } else {
      const int mo1 = MEM_OFF(CORD(w + 4)), mo2 = MEM_OFF(CORD(w + 8)), mo3 = MEM_OFF(CORD(w - 4));
      adr[0] = 0;
#pragma unroll 1
      for (int s = 0; s < NSEG; ++s) {
        EVDECL;
        const int so = (int)OFF_STEP + s * (int)UNIT, xr = xreg(r_s + s), sc_ = so + ((cown ^ xr) << 12);
        const int lst = olane();
        int a1 = ((cls_of(perm, lst & 3) ^ xr) << 12) | ((lst & 4) << 9) | ((lst & 8) << 12) | (((lst >> 4) | (w != TWV)) << 28);
        const bool fq = kst && krdy <= s + 2;
        int a0 = fq ? fqk(h, min(s + 1 + (lst & 15), NSEG - 1)) : ST_ADR | ((w != TWV) << 30);
        v2u st;
        unsigned cnt, tcd, tca = (lst & 31) * 128 + (lst >> 5) * QK_RT;
        int o0 = sc_ + mo0, o1 = sc_ + mo1, o2 = sc_ + mo2, sst = so + R_ST + role * 1024 + h * 32, ss = kst ? 0 : sc_ + R_ST;
        int tc = kst && !TC2 ? qk_addr(w & 1, min(s + 1, NSEG - 1), h) : 0;
        asm volatile("" : "+v"(a1), "+v"(a0), "+{v242}"(tca));
        adr[1] = a1;
        adr[0] = a0;
        asm volatile("" : "+s"(o0), "+s"(o1), "+s"(o2), "+s"(sst), "+s"(ss), "+s"(tc));
        DMDECL;
        TIMING_GO
        asm volatile(QK_K
                     : [cnt] "=&s"(cnt) DMOUT, "={v[240:241]}"(st), "={v237}"(tcd) EVOUT TQOUT
                     : WPIN, "{v[244:251]}"(adr), "{v243}"(pw1), "{v242}"(tca), [rs] "s"(rsv), [o0] "s"(o0), [o1] "s"(o1), [o2] "s"(o2),
                       [ckm] "s"(ck.m - NCY), [inv] "s"(ck.inv << 16), [r16] "s"(ck.r << 4), [sst] "s"(sst), [tg] "s"(tg), [tw] "s"(tw),
                       [ss] "s"(ss), [tc] "s"(tc) DMIN(sc_ + mo3)
                     : QKK_CLOB, "vcc", "scc", "memory");
        if (s < NSEG - 1 && w == TWV) {
          __builtin_amdgcn_s_sleep(KSL);
          TIMING_WAVE
        } else if (s < NSEG - 1 && kst) {
          if (fq) {
            const u64 miss = __builtin_amdgcn_ballot_w64(st[0] != 0u);
            krdy = s + 1 + (miss ? __builtin_ctzll(miss) : 16);
          }
          if (KSL2) __builtin_amdgcn_s_sleep(KSL2);
          if (KDM) asm volatile("s_waitcnt vmcnt(0)" ::: "memory");
          K_STAGE(s + 1)
          asm volatile("" ::"{v237}"(tcd));
          if (TC2 == 1) {
            const unsigned tch = __builtin_amdgcn_raw_buffer_load_b32(rs, tca, qk_addr(w & 1, min(s + 1 + (w & 1), NSEG - 1), h), AUX_SC1);
            asm volatile("" ::"v"(tch));
          }
        } else {
          __builtin_amdgcn_s_sleep(KSL);
          unsigned tch = 0;
          TLB_TOUCH
          asm volatile("" ::"v"(tch));
        }
        if (w != TWV) EV(6);
        if (w != TWV) EVDUMP(s);
      }
    }
  } else {
    const v32u W0 = cat32(wb[0], wb[1], wb[2], wb[3]), W1 = cat32(wb[4], wb[5], wb[6], wb[7]), W2 = cat32(wb[8], wb[9], wb[10], wb[11]);
    const v32u W3 = cat32(wb[12], wb[13], wa[0], wa[1]), W4 = cat32(wa[2], wa[3], wa[4], wa[5]), W5 = cat32(wa[6], wa[7], wb[14], wb[15]);
    v8i adr;
    adr[7] = l16;
    adr[6] = l16 + w * 1024;
    adr[5] = pw0;
    adr[4] = sidx;
#pragma unroll
    for (int q = 0; q < 2; ++q) {
      const int c = 8 * (2 * q + (g >> 1) + ((g & 1) ? 0 : 4)) + (w ^ ((h >> 4) * 4));
      const int blk = (c % PO) * 2 + (h >> 4);
      adr[3 - q] = (int)OFF_VM + blk * 32768 + ((cls_of(perm, c / PO) ^ xreg(r_a + (int)(OFF_VM / UNIT) + (blk >> 4))) << 12) +
                   (h & 15) * 256 + l15 * 16;
    }
    adr[1] = (16 * (g & 1) + 4 * (g >> 1) + trq) * 24 + (trp & 1) * 8;
    adr[0] = L_HI + (32 * (w & 3) + l15) * 24 + 4 * g;
#define VCH(i) (VAL ? ((((i) - 1) ^ krot) & 3) | (((i) & 8) >> 1) | ((((i) - 1) & 4) << 1) : ((i) & 8) | ((((i) - 1) & 7) ^ krot))
    const int mo0 = MEM_OFF(VCH(w)), mo1 = MEM_OFF(VCH(w + 4)), mo2 = MEM_OFF(VCH(w + 8)), mo3 = MEM_OFF(VCH(w & 3));
    if (w == TWV) clk_edge(ck.r, ck.m, 2);
    if (w < 4) {
#pragma unroll 1
      for (int s = 0; s < NSEG; ++s) {
        EVDECL;
        const int so = (int)OFF_STEP + s * (int)UNIT, xr = xreg(r_s + s);
        unsigned cnt;
        DMDECL;
        asm volatile(VO_A
                     : [cnt] "=&s"(cnt) DMOUT EVOUT
                     : WPIN, "{v[244:251]}"(adr), "{v243}"(pw1), [rs] "s"(rsv), [o0] "s"(so + mo0 + ((cown ^ xr) << 12)), [vs] "s"(vm_slot(s))
                     : VO_CLOB, "v241", "vcc", "scc", "memory");
        if (s < 4) PT(11 + s);
        if (w == 0) {
          unsigned tch = 0;
          TLB_TOUCH
          asm volatile("" ::"v"(tch));
        }
        EVDUMP(s);
      }
    } else {
#pragma unroll 1
      for (int s = 0; s < NSEG; ++s) {
        EVDECL;
        const int so = (int)OFF_STEP + s * (int)UNIT, xr = xreg(r_s + s);
        const int lst = olane();
        int vst = ((cls_of(perm, lst & 3) ^ xr) << 12) | ((lst & 4) << 9) | ((lst & 8) << 12) | (((lst >> 4) | (w != TWV)) << 28);
        unsigned cnt;
        v2u st = {(unsigned)(ST_ADR | ((w != TWV) << 30)), 0u};
        int o0 = so + mo0 + ((cown ^ xr) << 12), o1 = o0 - mo0 + mo1, o2 = o0 - mo0 + mo2, vs = vm_slot(s), sst = so + R_ST + role * 1024 + h * 32;
        int ss = so + R_ST + ((cown ^ xr) << 12);
        asm volatile("" : "+{v[240:241]}"(st), "+{v242}"(vst));
        asm volatile("" : "+s"(o0), "+s"(o1), "+s"(o2), "+s"(vs), "+s"(sst), "+s"(ss));
        DMDECL;
        TIMING_GO
        asm volatile(VO_B
                     : [cnt] "=&s"(cnt) DMOUT, "+{v[240:241]}"(st) EVOUT TQOUT
                     : WPIN, "{v[244:251]}"(adr), "{v243}"(pw1), "{v242}"(vst), [rs] "s"(rsv), [o0] "s"(o0), [o1] "s"(o1), [o2] "s"(o2),
                       [vs] "s"(vs), [ckm] "s"(ck.m - NCYV), [inv] "s"(ck.inv << 16), [r16] "s"(ck.r << 4), [sst] "s"(sst), [tg] "s"(tg),
                       [tw] "s"(tw), [ss] "s"(ss) DMIN(o0 - mo0 + mo3)
                     : VO_CLOB, "vcc", "scc", "memory");
        if (w == TWV && s < NSEG - 1) TIMING_WAVE
        if (w != TWV) EVDUMP(s);
      }
    }
  }
  __builtin_amdgcn_endpgm();
}
typedef f32x2 c2;
#define PKOP(name, txt)                                   \
  __device__ __forceinline__ c2 name(c2 a, c2 b) {        \
    c2 d;                                                 \
    asm("v_pk_add_f32 %0, %1, %2 " txt : "=v"(d) : "v"(a), "v"(b)); \
    return d;                                             \
  }
PKOP(c_addi, "op_sel:[0,1] op_sel_hi:[1,0] neg_lo:[0,1]")
PKOP(c_addc, "neg_hi:[0,1]")
PKOP(c_sepb, "op_sel:[1,1] op_sel_hi:[0,0] neg_hi:[1,0]")
PKOP(c_mixc, "op_sel:[0,1] op_sel_hi:[1,0] neg_hi:[1,0]")
#define T_ADD(d, a, b) "v_pk_add_f32 %" #d ", %" #a ", %" #b "\n\t"
#define T_SUB(d, a, b) "v_pk_add_f32 %" #d ", %" #a ", %" #b " neg_lo:[0,1] neg_hi:[0,1]\n\t"
#define T_ROT(d, a, b, n) "v_pk_add_f32 %" #d ", %" #a ", %" #b " op_sel:[0,1] op_sel_hi:[1,0] " n ":[0,1]\n\t"
#define T_MUL(d, a, w) "v_pk_mul_f32 %" #d ", %" #a ", %" #w " op_sel_hi:[1,0]\n\t"
#define T_FRE(d, a, w, c) "v_pk_fma_f32 %" #d ", %" #a ", %" #w ", %" #c " op_sel_hi:[1,0,1]\n\t"
#define T_FIM(d, a, w, c, n) "v_pk_fma_f32 %" #d ", %" #a ", %" #w ", %" #c " op_sel:[1,1,0] op_sel_hi:[0,1,1] " n ":[1,0,0]\n\t"
#define T_2MN(d, a, t) "v_pk_fma_f32 %" #d ", %" #a ", 2.0, %" #t " op_sel_hi:[1,0,1] neg_lo:[0,0,1] neg_hi:[0,0,1]\n\t"
#define T_TAIL(n, m) T_ADD(0, 4, 5) T_ROT(1, 2, 3, m) T_SUB(4, 4, 5) T_ROT(3, 2, 3, n)
#define T_D0(n, m) T_ADD(4, 0, 2) T_ADD(5, 1, 3) T_SUB(2, 0, 2) T_SUB(3, 1, 3) T_TAIL(n, m)
#define T_D1(n, m) T_MUL(5, 1, 7) T_ROT(4, 0, 2, m) T_FIM(1, 1, 7, 5, n) T_ROT(2, 0, 2, n) T_FRE(5, 3, 9, 1) T_FIM(5, 3, 9, 5, n) T_2MN(3, 1, 5) T_TAIL(n, m)
#define T_D2(n, m) T_MUL(5, 1, 7) T_FRE(4, 2, 8, 0) T_FIM(1, 1, 7, 5, n) T_FIM(4, 2, 8, 4, n) T_FRE(5, 3, 9, 1) T_2MN(2, 0, 4) T_FIM(5, 3, 9, 5, n) \
    T_ADD(0, 4, 5) T_2MN(3, 1, 5) T_SUB(4, 4, 5) T_ROT(1, 2, 3, m) T_ROT(3, 2, 3, n)
#define T_D3(n, m) T_MUL(4, 0, 6) T_MUL(5, 1, 7) T_FIM(0, 0, 6, 4, n) T_FIM(1, 1, 7, 5, n) T_FRE(4, 2, 8, 0) T_FRE(5, 3, 9, 1) \
    T_FIM(4, 2, 8, 4, n) T_FIM(5, 3, 9, 5, n) T_2MN(2, 0, 4) T_2MN(3, 1, 5) T_TAIL(n, m)
#define D4_ASM(T, C)                                                                                                       \
  {                                                                                                                        \
    if constexpr (INV) asm(T("neg_hi", "neg_lo") : "+v"(x0), "+v"(x1), "+v"(x2), "+v"(x3), "=&v"(t0), "=&v"(t1) : C(w0), C(w1), C(w2), C(w3)); \
    else asm(T("neg_lo", "neg_hi") : "+v"(x0), "+v"(x1), "+v"(x2), "+v"(x3), "=&v"(t0), "=&v"(t1) : C(w0), C(w1), C(w2), C(w3));               \
  }
__device__ __forceinline__ constexpr float cos32(int m) {
  constexpr float C[9] = {1.f, 0.98078528040323044f, 0.92387953251128674f, 0.83146961230254524f, 0.70710678118654752f,
                          0.55557023301960218f, 0.38268343236508977f, 0.19509032201612825f, 0.f};
  m &= 31;
  if (m > 16) m = 32 - m;
  return m > 8 ? -C[16 - m] : C[m];
}
#define W32(m) c2{cos32(m), -cos32((m) - 8)}
template <bool INV, int M0, int M1, int M2, int M3>
__device__ __forceinline__ void dft4k(c2& x0, c2& x1, c2& x2, c2& x3) {
  c2 t0, t1;
  if constexpr (M3 == 0) {
    const int w0 = 0, w1 = 0, w2 = 0, w3 = 0;
    D4_ASM(T_D0, "n")
  } else {
    const c2 w1 = W32(M1), w3 = W32(M3), w0 = M0 ? W32(M0) : w1, w2 = M0 == 0 && M2 == 8 ? w1 : W32(M2);
    if constexpr (M0 != 0) D4_ASM(T_D3, "s")
    else if constexpr (M2 == 8) D4_ASM(T_D1, "s")
    else D4_ASM(T_D2, "s")
  }
  x2 = t0;
}
template <bool INV, bool W0>
__device__ __forceinline__ void dft4w(c2& x0, c2& x1, c2& x2, c2& x3, c2 w0, c2 w1, c2 w2, c2 w3) {
  c2 t0, t1;
  if constexpr (W0) D4_ASM(T_D3, "v")
  else D4_ASM(T_D2, "v")
  x2 = t0;
}
#define T_B2(n, m) T_FRE(4, 1, 6, 0) T_FRE(5, 3, 7, 2) T_FIM(4, 1, 6, 4, n) T_FIM(5, 3, 7, 5, n) T_2MN(1, 0, 4) T_2MN(3, 2, 5)
#define T_B0(n, m) T_ADD(4, 0, 1) T_ROT(5, 2, 3, m) T_SUB(1, 0, 1) T_ROT(3, 2, 3, n)
template <bool INV, int K>
__device__ __forceinline__ void bf2(c2& x0, c2& x1, c2& x2, c2& x3, c2 w0, c2 w1) {
  c2 t0, t1;
  const c2 w2 = w0, w3 = w0;
  if constexpr (K == 0) D4_ASM(T_B2, "v")
  else if constexpr (K == 1) D4_ASM(T_B2, "s")
  else D4_ASM(T_B0, "s")
  x0 = t0;
  x2 = t1;
}
__device__ __forceinline__ void mul2(c2& d0, c2& d1, c2 a0, c2 w0, c2 a1, c2 w1) {
  asm(T_MUL(0, 2, 3) T_MUL(1, 4, 5) T_FIM(0, 2, 3, 0, "neg_lo") T_FIM(1, 4, 5, 1, "neg_lo") : "=&v"(d0), "=&v"(d1) : "v"(a0), "v"(w0), "v"(a1), "v"(w1));
}
template <int N, bool INV, int TW>
__device__ __forceinline__ void dftn(c2 (&v)[N], const c2* w) {
  constexpr int Q = N / 4;
#pragma unroll
  for (int n = 0; n < Q; ++n) {
    if (TW == 0) dft4k<INV, 0, 0, 0, 0>(v[n], v[Q + n], v[2 * Q + n], v[3 * Q + n]);
    else if (TW == 1 && n == 0) dft4w<INV, false>(v[n], v[Q + n], v[2 * Q + n], v[3 * Q + n], w[Q], w[Q + n], w[2 * Q + n], w[3 * Q + n]);
    else dft4w<INV, true>(v[n], v[Q + n], v[2 * Q + n], v[3 * Q + n], w[n], w[Q + n], w[2 * Q + n], w[3 * Q + n]);
  }
  c2 o[N];
  if constexpr (N == 32) {
#define D8A(K1)                                                                                          \
  dft4k<INV, 0, 2 * K1, 4 * K1, 6 * K1>(v[8 * K1], v[8 * K1 + 2], v[8 * K1 + 4], v[8 * K1 + 6]);          \
  dft4k<INV, K1, 3 * K1, 5 * K1, 7 * K1>(v[8 * K1 + 1], v[8 * K1 + 3], v[8 * K1 + 5], v[8 * K1 + 7]);
    D8A(0) D8A(1) D8A(2) D8A(3)
#pragma unroll
    for (int k = 0; k < 32; k += 8) {
      bf2<INV, 2>(v[k], v[k + 1], v[k + 4], v[k + 5], W32(4), W32(12));
      bf2<INV, 1>(v[k + 2], v[k + 3], v[k + 6], v[k + 7], W32(4), W32(12));
    }
#pragma unroll
    for (int i = 0; i < 32; ++i) o[(i >> 3) + 2 * (i & 6) + 16 * (i & 1)] = v[i];
  } else {
    dft4k<INV, 0, 0, 0, 0>(v[0], v[1], v[2], v[3]);
    dft4k<INV, 0, 2, 4, 6>(v[4], v[5], v[6], v[7]);
    dft4k<INV, 0, 4, 8, 12>(v[8], v[9], v[10], v[11]);
    dft4k<INV, 0, 6, 12, 18>(v[12], v[13], v[14], v[15]);
#pragma unroll
    for (int i = 0; i < 16; ++i) o[(i >> 2) + 4 * (i & 3)] = v[i];
  }
#pragma unroll
  for (int i = 0; i < N; ++i) v[i] = o[i];
}
template <bool H32>
__device__ __forceinline__ void c_swap(c2& a, c2& b) {
#pragma unroll
  for (int j = 0; j < 2; ++j) {
    const unsigned x = __float_as_uint(a[j]), y = __float_as_uint(b[j]);
    const auto r = H32 ? __builtin_amdgcn_permlane32_swap(x, y, false, false) : __builtin_amdgcn_permlane16_swap(x, y, false, false);
    a[j] = __uint_as_float(r[0]);
    b[j] = __uint_as_float(r[1]);
  }
}
#define LANE_NOP()                  \
  do {                              \
    SB();                           \
    asm volatile("s_nop 1");        \
    SB();                           \
  } while (0)
__device__ __forceinline__ c2 lds_c2(int a) { return *(volatile LAS c2*)((LAS char*)g_lds + a); }
template <int N>
__device__ __forceinline__ void lds_put(const c2 (&v)[N], int a) {
#pragma unroll
  for (int i = 0; i < N / 2; ++i) *(volatile LAS f32x4*)((LAS char*)g_lds + a + 16 * i) = f32x4{v[2 * i][0], v[2 * i][1], v[2 * i + 1][0], v[2 * i + 1][1]};
}
constexpr int XS = 32768, WS_ = 16384, WT_OFF = 18 << 20;
__device__ __forceinline__ int side_adr(int hi, int lo, int sd) { return hi + ((lo >> 13) << 14) + (lo & 8191) + (sd ^ ((hi >> 8) & 8192)); }
constexpr int LDS_TW = 0;
constexpr int LDS_EB = 4096;
constexpr int LDS_STG = 4096 + 8 * 9216;
__device__ __forceinline__ void tw2048(c2 (&w)[32], int lane) {
#pragma unroll
  for (int k = 1; k <= 8; ++k) w[k] = lds_c2(LDS_TW + lane * k * 8);
  const c2 h = lds_c2(LDS_TW + (lane & 31) * 128);
  mul2(w[0], w[16], h, h, w[8], w[8]);
#pragma unroll
  for (int k = 9; k < 15; k += 2) mul2(w[k], w[k + 1], w[8], w[k - 8], w[8], w[k - 7]);
  mul2(w[15], w[24], w[8], w[7], w[16], w[8]);
#pragma unroll
  for (int k = 1; k < 8; ++k) mul2(w[16 + k], w[24 + k], w[16], w[k], w[24], w[k]);
}
template <bool INV>
__device__ __forceinline__ void fft2048(c2 (&v)[32], const c2 (&w)[32], int eb, int lane) {
  dftn<32, INV, 0>(v, w);
  lds_put<32>(v, eb + lane * 272);
  const int ra = eb + (lane >> 5) * 4352 + (lane & 31) * 8;
#pragma unroll
  for (int k = 0; k < 16; ++k) {
    v[k] = lds_c2(ra + k * 272);
    v[k + 16] = lds_c2(ra + k * 272 + 8704);
  }
#pragma unroll
  for (int k = 0; k < 16; k += 2) bf2<INV, 0>(v[k], v[k + 16], v[k + 1], v[k + 17], w[0], w[0]);
  LANE_NOP();
#pragma unroll
  for (int k = 0; k < 16; ++k) c_swap<true>(v[k], v[k + 16]);
  dftn<32, INV, 1>(v, w);
}
template <bool SC>
__device__ __forceinline__ void tw1024(c2 (&w)[16], c2 (&u)[4], int lane, float s) {
#pragma unroll
  for (int k = 1; k <= 4; ++k) w[k] = lds_c2(LDS_TW + lane * k * 16);
  u[1] = lds_c2(LDS_TW + (lane & 15) * 256);
  u[0] = u[1];
  mul2(u[2], w[8], u[1], u[1], w[4], w[4]);
  mul2(u[3], w[12], u[2], u[1], w[8], w[4]);
  w[0] = c2{s, 0.f};
  if (SC) {
#pragma unroll
    for (int k = 1; k < 4; ++k) w[k] *= s;
  }
  c2 z;
  mul2(w[5], w[6], w[4], w[1], w[4], w[2]);
  mul2(w[7], z, w[4], w[3], w[4], w[3]);
#pragma unroll
  for (int k = 1; k < 4; ++k) mul2(w[8 + k], w[12 + k], w[8], w[k], w[12], w[k]);
  if (SC) {
#pragma unroll
    for (int k = 4; k < 16; k += 4) w[k] *= s;
  }
}
template <bool INV>
__device__ __forceinline__ void fft1024(c2 (&v)[16], const c2 (&w)[16], const c2 (&u)[4], int eb, int lane) {
  dftn<16, INV, 0>(v, w);
  lds_put<16>(v, eb + lane * 144);
  const int ra = eb + ((lane >> 1) & 8) * 144 + (lane >> 5) * 576 + (lane & 15) * 8;
  c2 t[4][4];
#pragma unroll
  for (int a = 0; a < 4; ++a)
#pragma unroll
    for (int r = 0; r < 4; ++r) t[a][r] = lds_c2(ra + (a + 16 * r) * 144);
#pragma unroll
  for (int a = 0; a < 4; ++a) {
    dft4w<INV, false>(t[a][0], t[a][1], t[a][2], t[a][3], u[0], u[1], u[2], u[3]);
#pragma unroll
    for (int q = 0; q < 4; ++q) v[a + 8 * (q & 1) + 4 * (q >> 1)] = t[a][q];
  }
  LANE_NOP();
#pragma unroll
  for (int i = 0; i < 8; ++i) c_swap<false>(v[i], v[i + 8]);
#pragma unroll
  for (int i = 0; i < 8; ++i) c_swap<true>(v[(i & 3) + 2 * (i & 4)], v[(i & 3) + 2 * (i & 4) + 4]);
  dftn<16, INV, INV ? 2 : 1>(v, w);
}

__device__ __forceinline__ void fft_table(int w) {
  const int m = w * 64 + olane();
  float sn, cs;
  sincospif(-(float)m * (1.f / 1024.f), &sn, &cs);
  *(LAS c2*)((LAS char*)g_lds + LDS_TW + m * 8) = c2{cs, sn};
  RAW_BARRIER();
}
__device__ __forceinline__ void count_done(char* ws, int cnt_off, int w) {
  VMCNT(0);
  RAW_BARRIER();
  if (w == 0) cnt_inc(ws, cnt_off);
}
__device__ __forceinline__ void stg_adr(int lane, int w, int& up, int& mid, int& dn) {
  up = LDS_STG + lane * 128 + ((w ^ (lane >> 1)) & 7) * 16;
  dn = LDS_STG + (64 - lane) * 128 + ((w ^ ((64 - lane) >> 1)) & 7) * 16 + 8;
  mid = dn + 57344 - (lane == 0 ? 8 : 0);
}

#define P1A 2
#define P1B 0
#define P1X 0
#define FT(i)
#define FTV(i, x)
__device__ __forceinline__ void fft_pass1(char* ws, char* x1, int sd, const u16* __restrict__ x, const u16* __restrict__ wr,
                                          const u16* __restrict__ wi, int task, int w) {
  LAS char* const L = (LAS char*)g_lds;
  const __amdgpu_buffer_rsrc_t rx = __builtin_amdgcn_make_buffer_rsrc(x1, 0, 513 * XS, 0x00020000);
  const __amdgpu_buffer_rsrc_t rt = __builtin_amdgcn_make_buffer_rsrc(x1 + WT_OFF, 0, 513 * WS_, 0x00020000);
  const __amdgpu_buffer_rsrc_t ri = __builtin_amdgcn_make_buffer_rsrc((void*)x, 0, S * D * 2, 0x00020000);
  const __amdgpu_buffer_rsrc_t rr = __builtin_amdgcn_make_buffer_rsrc((void*)wr, 0, S * DF * 2, 0x00020000);
  const __amdgpu_buffer_rsrc_t rm = __builtin_amdgcn_make_buffer_rsrc((void*)wi, 0, S * DF * 2, 0x00020000);
  const int lane = olane();
  const int p = (P1X & 4) ? w : task * 8 + w, r0 = (p & 63) + 128 * (p >> 6);
  unsigned xa[16], xb[16], fr[2][9], fi[2][9];
  FT(0);
#pragma unroll
  for (int t = 0; t < 16; ++t) {
    xa[t] = __builtin_amdgcn_raw_buffer_load_b16(ri, lane * 2 + t * 128, r0 * 2048, P1A);
    xb[t] = __builtin_amdgcn_raw_buffer_load_b16(ri, lane * 2 + t * 128, r0 * 2048 + 131072, P1A);
  }
  const int l8 = lane == 0 ? 1024 : OOB;
#pragma unroll
  for (int r = 0; r < 2; ++r)
#pragma unroll
    for (int t = 0; t < 9; ++t) {
      fr[r][t] = __builtin_amdgcn_raw_buffer_load_b16(rr, t < 8 ? lane * 2 + t * 128 : l8, (r0 + 64 * r) * (DF * 2), P1B);
      fi[r][t] = __builtin_amdgcn_raw_buffer_load_b16(rm, t < 8 ? lane * 2 + t * 128 : l8, (r0 + 64 * r) * (DF * 2), P1B);
    }
  FT(1);
  c2 tw[16], tu[4], v[16];
  tw1024<false>(tw, tu, lane, 1.f);
  FTV(2, xb[15]);
#pragma unroll
  for (int t = 0; t < 16; ++t) v[t] = c2{__uint_as_float(xa[t] << 16), __uint_as_float(xb[t] << 16)};
  fft1024<false>(v, tw, tu, LDS_EB + w * 9216, lane);
  int up, mid, dn;
  stg_adr(lane, w, up, mid, dn);
#pragma unroll
  for (int j = 0; j < 16; ++j) *(volatile LAS c2*)(L + (j < 8 ? up + 8192 * j : (j == 8 ? mid : dn + 8192 * (15 - j)))) = v[j];
  FT(3);
  RAW_BARRIER();
  FTV(4, fi[1][8]);
  {
    const int fa = LDS_EB + lane * 64 + ((w ^ lane) & 6) * 8 + (w & 1) * 8;
#pragma unroll
    for (int t = 0; t < 9; ++t)
      if (t < 8 || lane == 0) *(LAS v2u*)(L + fa + t * 4096) = v2u{fr[0][t] | (fi[0][t] << 16), fr[1][t] | (fi[1][t] << 16)};
  }
  const int kq = w * 8 + (lane >> 3);
  {
    const int ra = LDS_STG + kq * 128 + ((lane ^ (kq >> 1)) & 7) * 16, ga = kq * XS + (lane & 7) * 16;
    v4u d[9];
#pragma unroll
    for (int i = 0; i < 9; ++i) d[i] = *(const LAS v4u*)(L + ra + i * 8192);
#pragma unroll
    for (int i = 0; i < 9; ++i)
      if ((i < 8 || w == 0) && !(P1X & 1))
        st128(d[i], rx, ga + (i == 8 ? 512 * XS : 0), side_adr((i & 7) * (64 * XS), task * 128, sd));
  }
  FT(5);
  RAW_BARRIER();
  {
    const int k4 = w * 16 + (lane >> 2);
    const int ra = LDS_EB + k4 * 64 + (((2 * lane) ^ k4) & 6) * 8, ga = k4 * WS_ + (lane & 3) * 16;
    v4u d[5];
#pragma unroll
    for (int i = 0; i < 5; ++i) d[i] = *(const LAS v4u*)(L + ra + i * 8192);
#pragma unroll
    for (int i = 0; i < 5; ++i)
      if ((i < 4 || w == 0) && !(P1X & 2))
        st128(d[i], rt, ga + (i == 4 ? 512 * WS_ : 0), side_adr((i & 3) * (128 * WS_), task * 64, sd ^ 8192));
  }
  FT(6);
  FT(7);
}
__device__ __forceinline__ void fft_pass2(char* ws, char* x1, int sd, int task, int w) {
  const __amdgpu_buffer_rsrc_t rx = __builtin_amdgcn_make_buffer_rsrc(x1, 0, 513 * XS, 0x00020000);
  const __amdgpu_buffer_rsrc_t rt = __builtin_amdgcn_make_buffer_rsrc(x1 + WT_OFF, 0, 513 * WS_, 0x00020000);
  const int lane = olane();
  const int k = task * 8 + w;
  if (k > 512) return;
  f32x4 d[16];
  FT(0);
#pragma unroll
  for (int t = 0; t < 16; ++t)
    d[t] = __builtin_bit_cast(f32x4, __builtin_amdgcn_raw_buffer_load_b128(rx, lane * 16 + (t & 3) * 1024, side_adr(k * XS, (t >> 2) * 4096, sd), AUX_SC1));
  c2 tw[32], v[32];
  tw2048(tw, lane);
  FT(1);
  const bool edge = (k & 511) == 0;
  FTV(2, d[15]);
  if (edge) {
#pragma unroll
    for (int t = 0; t < 16; ++t) {
      d[t][2] = d[t][0];
      d[t][3] = d[t][1];
    }
  }
#pragma unroll
  for (int t = 0; t < 16; ++t) {
    v[2 * t] = c_addc(c2{d[t][0], d[t][1]}, c2{d[t][2], d[t][3]});
    v[2 * t + 1] = c_sepb(c2{d[t][0], d[t][1]}, c2{d[t][2], d[t][3]});
  }
  SB();
  v2u f[16];
#pragma unroll
  for (int t = 0; t < 16; ++t) f[t] = __builtin_amdgcn_raw_buffer_load_b64(rt, lane * 8 + (t & 7) * 512, side_adr(k * WS_, (t >> 3) * 4096, sd ^ 8192), AUX_SC1);
  SB();
  const int eb = LDS_EB + w * 17408;
  fft2048<false>(v, tw, eb, lane);
  FTV(3, v[31]);
  FTV(4, f[15]);
#pragma unroll
  for (int t = 0; t < 16; ++t)
    mul2(v[2 * t], v[2 * t + 1], v[2 * t], c2{__uint_as_float(f[t][0] << 16), __uint_as_float(f[t][0] & 0xffff0000u)}, v[2 * t + 1],
         c2{__uint_as_float(f[t][1] << 16), __uint_as_float(f[t][1] & 0xffff0000u)});
  fft2048<true>(v, tw, eb, lane);
  FTV(5, v[31]);
  if (edge) {
#pragma unroll
    for (int t = 0; t < 32; ++t) v[t][1] = 0.f;
  }
#pragma unroll
  for (int t = 0; t < 16; ++t) {
    const c2 a = c_addi(v[2 * t], v[2 * t + 1]), b = c_mixc(v[2 * t], v[2 * t + 1]);
    st128(__builtin_bit_cast(v4u, f32x4{a[0], a[1], b[0], b[1]}), rx, lane * 16 + (t & 3) * 1024, side_adr(k * XS, (t >> 2) * 4096, sd));
  }
  FT(6);
  FT(7);
}
__device__ __forceinline__ void fft_pass3(char* ws, char* x1, int sd, u16* __restrict__ out, int task, int w) {
  LAS char* const L = (LAS char*)g_lds;
  const __amdgpu_buffer_rsrc_t rx = __builtin_amdgcn_make_buffer_rsrc(x1, 0, 513 * XS, 0x00020000);
  const __amdgpu_buffer_rsrc_t ro = __builtin_amdgcn_make_buffer_rsrc(out, 0, S * 4096, 0x00020000);
  const int lane = olane();
  const int kq = w * 8 + (lane >> 3);
  const int ra = LDS_STG + kq * 128 + ((lane ^ (kq >> 1)) & 7) * 16, ga = kq * XS + (lane & 7) * 16;
  v4u d[9];
  FT(0);
#pragma unroll
  for (int i = 0; i < 9; ++i) d[i] = __builtin_amdgcn_raw_buffer_load_b128(rx, ga + (i == 8 ? 512 * XS : 0), side_adr((i & 7) * (64 * XS), task * 128, sd), AUX_SC1);
  c2 tw[16], tu[4], v[16];
  tw1024<true>(tw, tu, lane, 1.f / (2.f * 2048.f * 1024.f));
  FT(1);
  FTV(2, d[8]);
#pragma unroll
  for (int i = 0; i < 9; ++i)
    if (i < 8 || w == 0) *(LAS v4u*)(L + ra + i * 8192) = d[i];
  int up, mid, dn;
  stg_adr(lane, w, up, mid, dn);
  FT(3);
  RAW_BARRIER();
  FT(4);
#pragma unroll
  for (int j = 0; j < 16; ++j) v[j] = lds_c2(j < 8 ? up + 8192 * j : (j == 8 ? mid : dn + 8192 * (15 - j)));
  fft1024<true>(v, tw, tu, LDS_EB + w * 9216, lane);
  FTV(5, v[15]);
  const int p = task * 8 + w, r0 = (p & 63) + 128 * (p >> 6);
#pragma unroll
  for (int t = 0; t < 16; ++t) {
    const unsigned o = pk2(v[t][0], v[t][1]);
    __builtin_amdgcn_raw_buffer_store_b16((u16)o, ro, lane * 2 + t * 128, r0 * 4096, 0);
    __builtin_amdgcn_raw_buffer_store_b16((u16)(o >> 16), ro, lane * 2 + t * 128, r0 * 4096 + 262144, 0);
  }
  FT(6);
  FT(7);
}

#define W7P 3
#define VTC 0
#define NXA 0
#define ARA 0
#define STA 0
#define SLR 2
#define RSO 5
#define NCAL 6
#define DOX2 300
#define DTC 100
#define NX1 4
#define NXG 2
#define NXK 5
#define OFD 400
#define STI 650
#define TNX 0
#define NXP 0
#define PACE 150
#define PFB 4
#define SPC 0
#define CV1 -88
#define CV2 108
#define EDL 0
#define EVB(k) EV(k)
constexpr int L_D = 16384, L_R = 32768, L_F = 122880;
__device__ __forceinline__ volatile LAS unsigned* flw(int i, int m) {
  return (volatile LAS unsigned*)((LAS char*)g_lds + L_F + i * 4 + (olane() & m) * 4);
}
template <int SLP = 0>
__device__ __forceinline__ void lds_wait_ge(int i, int m, unsigned v, gu32* tmo, unsigned code) {
  volatile LAS unsigned* p = flw(i, m);
  _Pragma("clang loop unroll(disable)") for (unsigned spins = 0;;) {
    if (__all(*p >= v)) break;
    if (SLP) __builtin_amdgcn_s_sleep(SLP);
    if (++spins > (kSpinCap << 5)) {
      tmo_report(tmo, code);
      break;
    }
  }
  asm volatile("" ::: "memory");
}
#define HLP 6
#define HLT 0
__device__ __forceinline__ void help(int c, int w, const u16* Wq, const u16* Wk, const u16* Wv) {
  const int j = c & 7, m = j >> 1, xg = c / PO, ln = olane();
  if ((c & 8) && w >= 1 && w <= HLP) {
    const u16* wm = m == 2 ? Wv : ((m == 3 ? j : m) & 1 ? Wk : Wq);
    const int hs = m == 3 ? 31 : PQ * xg + slow_head(wm) + 4 * (j & 1);
    if (m < 3 || xg == NXC - 1)
      for (int q = 0; q < 2; ++q)
        touch4((const char*)wm + hs * 256 + (size_t)((16 - w) ^ (hs & KROT)) * 524288, (q * 32 + (ln >> 1)) * 8192 + (ln & 1) * 128, 65536 + w * 256);
  }
}
template <bool PRE>
__device__ __forceinline__ unsigned owner(const int c, const int w, const u64 nonce, const u16* __restrict__ mem0, char* ws,
                                          u16* __restrict__ out, unsigned perm, const u16* Wq, const u16* Wk, const u16* Wv) {
  LAS char* const L = (LAS char*)g_lds;
  gu32* tmo = (gu32*)(ws + OFF_TMO);
  const int lane = olane();
  const int l15 = lane & 15, g = lane >> 4;
  const __amdgpu_buffer_rsrc_t rs = __builtin_amdgcn_make_buffer_rsrc(ws, 0, (int)WS_USED, 0x00020000);
  const int xg = c / PO, li = c % PO;
  const int r_a = (int)(((uintptr_t)ws >> 19) & 7);
  const int r_s = r_a + (int)(OFF_STEP / UNIT);
  volatile LAS unsigned* fl = (volatile LAS unsigned*)(L + L_F);
  unsigned m0v = 0;
  if constexpr (PRE) {
  if (w == 7) m0v = *(const unsigned*)(mem0 + (lane >> 3) * D + 16 * c + 2 * (lane & 7));
  if (c == 0 && w == 6) hs_lead<4>(rs, true, CNT_HSO, 64u);
  unsigned cal = 0;
  if (w == 0)
    st128(v4u{(unsigned)lane, 0u, 0u, 0u}, rs, (lane & 31) * 16 + (lane >> 5) * 32768, (int)OFF_SCR + (((li & 7) ^ xreg(r_a)) << 12) + (c >> 3) * 512);
  if (w == 0 && lane == 0) {
    const int cl = li & 7;
    int off = (int)OFF_SCR + ((cl ^ xreg(r_a)) << 12) + (c >> 3) * 512;
    unsigned best = 0xfffeu;
#pragma unroll 1
    for (int r = 0; r < NCAL; ++r) {
      VMCNT(0);
      const u64 t1 = __builtin_amdgcn_s_memtime();
      asm volatile("s_waitcnt lgkmcnt(0)" : "+v"(off)::"memory");
      const unsigned v = __builtin_amdgcn_raw_buffer_load_b32(rs, off, (r & 3) * 128 + (r >> 2) * 32768, AUX_SC1);
      asm volatile("s_waitcnt vmcnt(0)" ::"v"(v) : "memory");
      const u64 t2 = __builtin_amdgcn_s_memtime();
      best = min(best, (unsigned)(t2 - t1));
    }
    if (HLT == 0 && li >= 8) best = 0xfffeu;
    cal = ((unsigned)cl << 16) | best;
  }
  if (w == 0) {
    PT(0);
    hs_wait(rs, nonce, v2u{~(unsigned)nonce, 0u});
    cnt_inc(ws, CNT_HSO);
    PT(1);
    unsigned id;
    asm volatile("s_getreg_b32 %0, hwreg(HW_REG_XCC_ID)" : "=s"(id));
    if (lane == 0) {
      __builtin_amdgcn_raw_buffer_store_b32(((id & 15u) << 19) | cal, rs, c * 4, (int)OFF_CAL, AUX_SC1);
      if (li == 0) __builtin_amdgcn_raw_buffer_store_b32(id & 15u, rs, xg * 4, (int)OFF_XID, AUX_SC1);
    }
  }
  static_assert(NF1O == 64 || NF1O == 2, "one task per owner but the first two, or none");
  __syncthreads();
  if (HLT == 1) help(c, w, Wq, Wk, Wv);
  if (w == 7) {
    const unsigned pm = numa_perm(numa_load(rs, tmo), (LAS unsigned*)(L + L_F + 512));
    fl[10] = pm;
  }
  __syncthreads();
  perm = __builtin_amdgcn_readfirstlane(fl[10]);
  PT(2);
  if (HLT == 2) help(c, w, Wq, Wk, Wv);
  }
  const int xr0 = xreg(r_s + (PRE ? 0 : NARM + (li & 1)));
  const v4u sent = {0xffffffffu, 0xffffffffu, 0xffffffffu, 0xffffffffu};
  const int ibs = (int)OFF_VM + li * 65536 + ((cls_of(perm, xg) ^ xreg(r_a + (int)(OFF_VM / UNIT) + (li >> 3))) << 12);

  int am = (int)OFF_STEP + R_MEM + (c >> 4) * 32768 + ((cls_of(perm, g) ^ xr0) << 12) + (c & 15) * 256 + l15 * 16;
  int ap[2];
#pragma unroll
  for (int q = 0; q < 2; ++q) {
    const int o = c * 384 + (lane & 31) * 16;
    ap[q] = (lane & 31) < 24 && c < 59 ? (int)OFF_STEP + R_EX + (o >> 12) * 32768 + ((cls_of(perm, 2 * q + (lane >> 5)) ^ xr0) << 12) + (o & 4095)
                             : OOB;
  }
  int as = lane < 8 * NXC ? (int)OFF_STEP + R_ST + ((cls_of(perm, lane >> 3) ^ xr0) << 12) + (c >> 5) * 32768 + (c & 31) * 128 + (lane & 7) * 16
                          : OOB;
  const int ax = (int)OFF_STEP + R_PX + c * 1280 + lane * 16;
  const int ax2 = lane < 16 ? ax + 1024 : (lane < 24 ? (int)OFF_STEP + R_DUM + c * 128 + (lane - 16) * 16 : OOB);
#define ARM_REC(r)                                                                                                   \
  {                                                                                                                  \
    const int so_ = (r) * (int)UNIT, dx_ = (xreg(r_s + (r)) ^ xreg(r_s + (r) + 1)) << 12;                            \
    __builtin_amdgcn_raw_buffer_store_b128(sent, rs, am, so_, AUX_SC1);                                              \
    am ^= dx_;                                                                                                       \
    _Pragma("unroll") for (int q = 0; q < 2; ++q) {                                                                  \
      __builtin_amdgcn_raw_buffer_store_b128(sent, rs, ap[q], so_, AUX_SC1);                                         \
      ap[q] ^= dx_;                                                                                                  \
    }                                                                                                                \
    __builtin_amdgcn_raw_buffer_store_b128(sent, rs, as, so_, AUX_SC1);                                              \
    as ^= dx_;                                                                                                       \
    __builtin_amdgcn_raw_buffer_store_b128(sent, rs, ax, so_, AUX_SC1);                                              \
    __builtin_amdgcn_raw_buffer_store_b128(sent, rs, ax2, so_, AUX_SC1);                                             \
  }
  if (PRE && w == 7) {
    {
      const int row = lane >> 3, np = lane & 7;
#pragma unroll
      for (int r = 0; r < NXC; ++r)
        __builtin_amdgcn_raw_buffer_store_b32(min(m0v, 0xfffffffeu), rs,
                                              (int)OFF_STEP + R_MEM + (c >> 4) * 32768 + ((cls_of(perm, r) ^ xreg(r_s)) << 12) +
                                                  (c & 15) * 256 + ((row & 1) + 2 * (np & 1) + 4 * (row >> 1)) * 16 + (np >> 1) * 4,
                                              0, AUX_SC1);
    }
    am |= 1 << 30;
#pragma unroll 1
    for (int r = 0; r < NARM + (li & 1); ++r) {
      ARM_REC(r)
      am &= ~(1 << 30);
    }
#pragma unroll
    for (int k = 0; k < 32; ++k)
      __builtin_amdgcn_raw_buffer_store_b128(sent, rs, lane * 16 + (k & 3) * 1024, ibs + vm_slot(k >> 3) + ((k >> 2) & 1) * 32768, AUX_SC1);
    VMCNT(0);
    PTW(3, 7);
    cnt_inc(ws, CNT_ARM);
  }
  if constexpr (PRE) return perm;
  PT(5);
  __syncthreads();
  if (w == 7 && lane < 32) fl[lane] = 0;
  __syncthreads();

  const int team = li & 1, c0 = c & ~1;
  const int l16 = lane * 16;
  const int cown = cls_of(perm, xg);
  if (w < 2) {
    const int r = (l15 & 1) + 2 * (l15 >> 2), kh = (l15 >> 1) & 1;
    const int nl = (kh ^ ((r >> 1) & 1)) + 2 * ((g & 1) ^ (r >> 2));
    typedef int v8i __attribute__((ext_vector_type(8)));
    v8i adr;
    adr[0] = l16;
    adr[1] = 32768 + SC_POS(2 * nl + (g >> 1), r);
    adr[2] = l16;
    adr[3] = L_D + w * 8192 + l16;
    adr[4] = ((l15 & 3) + 4 * g) * 16 + (l15 >> 2) * 4;
    adr[5] = (l15 & 1) ? 0x03020706 : 0x05040100;
    adr[6] = (l15 & 2) ? 0x4444eeee : 0xeeee4444;
    adr[7] = L_F + 48;
    u64 mk[4];
#pragma unroll
    for (int k = 0; k < 4; ++k) mk[k] = __builtin_amdgcn_ballot_w64(nl == k);
    const v4u rsv = {(unsigned)(uintptr_t)ws, (unsigned)((uintptr_t)ws >> 32) & 0xffffu, (unsigned)WS_USED, 0x00020000u};
    const int dtc_ = TN(12, DTC);
    const int eg = (c0 >> 2) & 1;
#pragma unroll 1
    for (int s = team; s < NSEG; s += 2) {
      EVDECL;
      const int so = (int)OFF_STEP + s * (int)UNIT;
      int sp = so + R_PM + ((cown ^ xreg(r_s + s)) << 12);
      int sv = ibs + (w - team) * 65536 + vm_slot(s);
      int sv1 = sv + eg * 32768, sv2 = sv + (1 - eg) * 32768, sp1 = sp + eg * 2048, sp2 = sp + (1 - eg) * 2048;
      const int xn = xreg(r_s + s + 1);
      int sm[NXC];
#pragma unroll
      for (int q = 0; q < NXC; ++q) {
        sm[q] = so + (int)UNIT + R_MEM + (c0 >> 4) * 32768 + ((cls_of(perm, q) ^ xn) << 12) + ((c0 + w) & 15) * 256;
        asm volatile("" : "+s"(sm[q]));
      }
      asm volatile("" : "+s"(sp), "+s"(sv1), "+s"(sv2), "+s"(sp1), "+s"(sp2));
      if (s >= 2) lds_wait_ge<4>(20, 3, s - 1, tmo, 0x30017);
      if (VTC && w == 0) {
        const unsigned tc = __builtin_amdgcn_raw_buffer_load_b32(rs, 0, sv ^ 8192, AUX_SC1);
        asm volatile("" ::"v"(tc));
      }
      v4u bl[8];
      if (s < NSEG - 1) {
        v32u blv;
        unsigned cnt, bad;
        asm volatile(OW_F
                     : [cnt] "=&s"(cnt), [st] "=&s"(bad), "={v[0:31]}"(blv) EVOUT
                     : "{v[112:119]}"(adr), [rs] "s"(rsv), [sv1] "s"(sv1), [sv2] "s"(sv2), [sp] "s"(sp), [sp1] "s"(sp1), [sp2] "s"(sp2),
                       [sm0] "s"(sm[0]), [sm1] "s"(sm[1]), [sm2] "s"(sm[2]), [sm3] "s"(sm[3]), [m0] "s"(mk[0]), [m1] "s"(mk[1]),
                       [m2] "s"(mk[2]), [m3] "s"(mk[3])
                     : OF_CLOB, "vcc", "scc", "memory");
#pragma unroll
        for (int k = 0; k < 8; ++k) bl[k] = v4u{blv[4 * k], blv[4 * k + 1], blv[4 * k + 2], blv[4 * k + 3]};
        if (__builtin_expect(bad != 0u, 0)) {
          v4u xs, pv[4];
          _Pragma("clang loop unroll(disable)") for (unsigned spins = 0;;) {
            OPQ(va, l16);
            OPQ(v1, adr[1]);
            OPQ(v2, adr[2]);
#pragma unroll
            for (int k = 0; k < 8; ++k) bl[k] = __builtin_amdgcn_raw_buffer_load_b128(rs, va + (k & 3) * 1024, k < 4 ? sv1 : sv2, AUX_SC1);
            xs = __builtin_amdgcn_raw_buffer_load_b128(rs, v1, sp, AUX_SC1);
#pragma unroll
            for (int k = 0; k < 4; ++k) pv[k] = __builtin_amdgcn_raw_buffer_load_b128(rs, v2 + (k & 1) * 1024, k < 2 ? sp1 : sp2, AUX_SC1);
            unsigned m = mx4(0u, xs);
#pragma unroll
            for (int k = 0; k < 4; ++k) m = mx4(m, pv[k]);
#pragma unroll
            for (int k = 0; k < 8; ++k) m = mx4(m, bl[k]);
            if (!anysent(m)) break;
            __builtin_amdgcn_s_sleep(RSO);
            if (spin_fail(spins, tmo, 0x20000 + s)) break;
          }
          f32x4 a0 = {0.f, 0.f, 0.f, 0.f};
#pragma unroll
          for (int i = 0; i < 8; ++i) {
            const int k = F_ORD[i] >= 'a' ? F_ORD[i] - 'a' : F_ORD[i] - '0';
            if (F_ORD[i] >= 'a') {
              const v4u z = v4u{nl == k ? xs[0] : 0u, nl == k ? xs[1] : 0u, nl == k ? xs[2] : 0u, nl == k ? xs[3] : 0u};
              const v8u db = cat8(*(const LAS v4u*)(L + adr[3] + k * 2048), *(const LAS v4u*)(L + adr[3] + k * 2048 + 1024));
              a0 = (k & 1) ? smf1(z, db, a0, adr[6]) : smf(z, db, a0, adr[6]);
            } else
              a0 = smf(pv[k], cat8(bl[2 * k], bl[2 * k + 1]), a0, adr[6]);
          }
          const unsigned own = pk2(a0[0] + a0[2], a0[1] + a0[3]);
          const unsigned nb = (unsigned)__builtin_amdgcn_mov_dpp((int)own, 0xb1, 0xf, 0xf, true);
          const unsigned o = __builtin_amdgcn_perm(nb, own, (unsigned)adr[5]);
#pragma unroll
          for (int q = 0; q < NXC; ++q) __builtin_amdgcn_raw_buffer_store_b32(o, rs, adr[4], sm[q], AUX_SC1);
          EV(3);
        }
      } else {
        BARE_BARRIER();
        wait_cyc(*flw(12, 0));
        _Pragma("clang loop unroll(disable)") for (unsigned spins = 0;;) {
          OPQ(va, l16);
          unsigned m = 0;
#pragma unroll
          for (int k = 0; k < 8; ++k) bl[k] = __builtin_amdgcn_raw_buffer_load_b128(rs, va + (k & 3) * 1024, k < 4 ? sv1 : sv2, AUX_SC1);
#pragma unroll
          for (int k = 0; k < 8; ++k) m = mx4(m, bl[k]);
          if (!anysent(m)) break;
          __builtin_amdgcn_s_sleep(RSO);
          if (spin_fail(spins, tmo, 0x20000 + s)) break;
        }
      }
      const unsigned tp = cyc32();
      if (s < 4) PT(9 + (s >> 1));
      *flw(2 + w, 0) = s + 1;
      if (s < NSEG - 1) {
        const v4u tv = *(volatile LAS v4u*)flw(12, 0);
        const int lr = olane();
        __builtin_amdgcn_raw_buffer_store_b32(min(((tp - tv[1]) * tv[2]) >> 16, 60000u), rs,
                                              ((cls_of(perm, lr & 3) ^ xn) << 12) | ((lr & 4) << 9) | ((lr & 8) << 12) | ((lr >> 4) << 28),
                                              so + (int)UNIT + R_ST + (c0 + w) * 32 + 4, AUX_SC1);
      }
      OPQ(vs, l16);
#pragma unroll
      for (int k = 0; k < 8; ++k) *(LAS v4u*)(L + w * 8192 + ((k ^ (4 * eg)) * 1024) + vs) = bl[k];
      asm volatile("s_waitcnt lgkmcnt(0)" ::: "memory");
      *flw(4 + w, 0) = s + 1;
      if (s < NSEG - 4) {
        lds_wait_ge(2, 1, s + 1, tmo, 0x3001a);
        v4u se;
        asm volatile("v_mov_b32 %0, -1" : "=v"(se[0]));
        se[1] = se[2] = se[3] = se[0];
#pragma unroll
        for (int k = 0; k < 8; ++k) __builtin_amdgcn_raw_buffer_store_b128(se, rs, vs + (k & 3) * 1024, sv + (k >> 2) * 32768, AUX_SC1 | ARA);
        asm volatile("s_nop 1" ::"v"(se));
      }
      if (s < NSEG - 2) {
        wait_cyc(tp + dtc_);
        const int lt = olane();
        const unsigned tc = __builtin_amdgcn_raw_buffer_load_b32(rs, lt < 40 ? lt * 128 : OOB, so + R_PX + ((li & ~1) + w) * 5120, AUX_SC1);
        asm volatile("" ::"v"(tc));
      }
      EVDUMP(s);
    }
  } else if (w < 6) {
    const int n = w - 2;
    const v4u rsv = {(unsigned)(uintptr_t)ws, (unsigned)((uintptr_t)ws >> 32) & 0xffffu, (unsigned)WS_USED, 0x00020000u};
    const int vfr = (int)((perm & 8192u) ^ (VFAR == 2 ? 8192u : 0u)) | (r_a << 19);
    const int fv0 = fvw((c0 >> 1) & 3, 8 * n, team);
    const int dox_ = TN(10, DOX2), pace_ = TN(11, PACE);
    v4u raw[2][8];
#define RAW_LD(dst, sg)                                                                                       \
  _Pragma("unroll") for (int t = 0; t < 2; ++t) _Pragma("unroll") for (int k = 0; k < 8; ++k) dst[t][k] =     \
      __builtin_amdgcn_raw_buffer_load_b128(rs, l16 + (k & 3) * 1024, vw_addr(vfr, c0 + t, sg, 2 * n + (k >> 2)), AUX_SC1);
    wait_flag<KF0>(rs, fv0 + (lane & 7), tmo, 0x30018);
    PTW(6, 2);
    RAW_LD(raw, team)
    int nrdy = team;
#pragma unroll 1
    for (int s = team; s < NSEG; s += 2) {
      EVDECL;
      const int so = (int)OFF_STEP + s * (int)UNIT;
      if (s < NSEG - 1) {
        const int ln = olane();
        OPQ(eidx, (ln & 4) ? 0xeeee : 0x4444);
        OPQ(vea, (ln & 4) * 128 + (ln >> 4) * 128 + ((ln & 3) + 4 * (((ln >> 3) & 1) ^ (n >> 1))) * 16);
        int se = so + R_EX + n * 32768 + ((cown ^ xreg(r_s + s)) << 12);
        v8u bx[2][4];
#pragma unroll
        for (int t = 0; t < 2; ++t)
#pragma unroll
          for (int k = 0; k < 4; ++k) {
            const v4u u = raw[t][2 * k], v = raw[t][2 * k + 1];
            bx[t][k] = v8u{u[0], v[0], u[2], v[2], u[1], v[1], u[3], v[3]};
            asm volatile("" : "+v"(bx[t][k]));
          }
        OPQ(vd, L_D + n * 2048 + ln * 16);
        asm volatile("" : "+s"(se));
        EV(3);
        if (s < 4) PTW(7 + (s >> 1), 2);
        unsigned bad;
        asm volatile(OW_E
                     : [st] "=&s"(bad) EVOUT
                     : "{v241}"(vea), "{v242}"(vd), "{v243}"(eidx), [rs] "s"(rsv), [se] "s"(se), [b00] "v"(bx[0][0]), [b01] "v"(bx[0][1]),
                       [b02] "v"(bx[0][2]), [b03] "v"(bx[0][3]), [b10] "v"(bx[1][0]), [b11] "v"(bx[1][1]), [b12] "v"(bx[1][2]),
                       [b13] "v"(bx[1][3])
                     : OE_CLOB, "vcc", "scc", "memory");
        if (__builtin_expect(bad != 0u, 0)) {
          v4u pv[4];
          _Pragma("clang loop unroll(disable)") for (unsigned spins = 0;;) {
            __builtin_amdgcn_s_sleep(RSO);
            asm volatile("" : "+v"(vea));
            unsigned m = 0;
#pragma unroll
            for (int k = 0; k < 4; ++k) pv[k] = __builtin_amdgcn_raw_buffer_load_b128(rs, vea + k * 1024, se, AUX_SC1);
#pragma unroll
            for (int k = 0; k < 4; ++k) m = mx4(m, pv[k]);
            if (!anysent(m) || spin_fail(spins, tmo, 0x10000 + s)) break;
          }
#pragma unroll
          for (int q = 0; q < 4; ++q) {
            const f32x4 z = {0.f, 0.f, 0.f, 0.f};
            const f32x4 d = smf(pv[2 * (q & 1)], bx[q >> 1][2 * (q & 1)], z, eidx), e = smf(pv[2 * (q & 1) + 1], bx[q >> 1][2 * (q & 1) + 1], z, eidx);
            *(LAS v4u*)(L + vd + (q >> 1) * 8192 + (q & 1) * 1024) = v4u{pk2(d[0], d[1]), pk2(d[2], d[3]), pk2(e[0], e[1]), pk2(e[2], e[3])};
          }
          EV(2);
          RAW_BARRIER();
        }
      } else BARE_BARRIER();
      v4u nx[2][8];
      const int sn = min(s + 2, NSEG - 1);
      unsigned tpb = 0;
#define VW_FLAGS                                                                                                      \
  {                                                                                                                   \
    const int lf = olane();                                                                                           \
    OPQ(vfl, fv0 + min((s >> 1) + 1 + (lf >> 3), NSEG / 2 - 1) * 8 + (lf & 7));                                        \
    const u64 miss = __builtin_amdgcn_ballot_w64(__builtin_amdgcn_raw_buffer_load_b8(rs, vfl, 0, AUX_SC1) != 0);      \
    nrdy = s + 2 * (miss ? __builtin_ctzll(miss) >> 3 : 8);                                                           \
  }
      if (s < NSEG - 2) {
        __builtin_amdgcn_s_sleep(5);
        lds_wait_ge(2, 1, s + 1, tmo, 0x30014);
        tpb = cyc32();
        const unsigned tw = tpb + dox_;
        if (nrdy < s + 2) VW_FLAGS
        wait_cyc(tw);
      }
      EVB(6);
      OPQ(va, s < NSEG - 2 && nrdy >= s + 2 ? l16 : OOB);
#define NX_LD(dst, q0, q1)                                                                              \
  _Pragma("unroll") for (int q = q0; q < q1; ++q) dst[q >> 3][q & 7] = __builtin_amdgcn_raw_buffer_load_b128( \
      rs, va + (q & 3) * 1024, vw_addr(vfr, c0 + (q >> 3), sn, 2 * n + ((q >> 2) & 1)), AUX_SC1 | NXA);
      NX_LD(nx, 0, NX1)
      SB();
      if (s < NSEG - 2) wait_cyc(cyc32() + ((n + (li >> 1)) & 3) * pace_);
      else lds_wait_ge(4, 1, s + 1, tmo, 0x30014);
      EVB(4);
      f32x4 ac[2][2];
      const int sx = so + R_PX + n * 8192, sy = so + R_PX + 32768 + n * 2048;
      v4u pv[2][10], vm[2][2];
      OPQ(vb, l16);
#define P_LD                                                                                         \
  _Pragma("unroll") for (int k = 0; k < 10; ++k) _Pragma("unroll") for (int rt = 0; rt < 2; ++rt) pv[rt][k] = \
      __builtin_amdgcn_raw_buffer_load_b128(rs, vb + (k & 3) * 1024, (k < 8 ? sx + (k >> 2) * 4096 : sy) + rt * 40960, AUX_SC1);
#define P_MM(k0, k1)                                                                                 \
  _Pragma("unroll") for (int k = k0; k < k1; ++k) _Pragma("unroll") for (int rt = 0; rt < 2; ++rt) {  \
    _Pragma("unroll") for (int t = 0; t < 2; ++t)                                                    \
      ac[rt][t] = mma(k < 8 ? raw[t][k & 7] : vm[t][k & 1], pv[rt][k], k == 0 ? f32x4{0.f, 0.f, 0.f, 0.f} : ac[rt][t]); \
    asm volatile("" : "+v"(ac[rt][0]), "+v"(ac[rt][1]));                                             \
  }
      P_LD
      SB();
      lds_wait_ge(4, 1, s + 1, tmo, 0x30014);
#pragma unroll
      for (int t = 0; t < 2; ++t)
#pragma unroll
        for (int k = 0; k < 2; ++k) vm[t][k] = *(const LAS v4u*)(L + vb + t * 8192 + n * 2048 + k * 1024);
      SB();
      P_MM(0, 10)
      if (__builtin_expect(__builtin_amdgcn_ballot_w64(__builtin_isunordered(ac[0][0][0], ac[1][0][0])) != 0ull, 0)) {
        _Pragma("clang loop unroll(disable)") for (unsigned spins = 0;;) {
          asm volatile("" : "+v"(vb));
          P_LD
          unsigned m = 0;
#pragma unroll
          for (int k = 0; k < 10; ++k) m = mx4(mx4(m, pv[0][k]), pv[1][k]);
          if (!anysent(m)) break;
          __builtin_amdgcn_s_sleep(8);
          if (spin_fail(spins, tmo, 0x30015)) break;
        }
        P_MM(0, 10)
      }
      EVB(7);
      if (__builtin_expect(s < NSEG - 2 && nrdy < s + 2, 0)) {
        _Pragma("clang loop unroll(disable)") for (unsigned spins = 0;;) {
          VW_FLAGS
          if (nrdy >= s + 2) break;
          __builtin_amdgcn_s_sleep(8);
          if (spin_fail(spins, tmo, 0x30018)) break;
        }
        va = vb;
        NX_LD(nx, 0, NX1)
      }
      NX_LD(raw, NX1, 16)
      SB();
      OPQ(vr, L_R + (s & 2) * 8192 + l16);
#pragma unroll
      for (int q = 0; q < 4; ++q) *(LAS f32x4*)(L + vr + q * 4096 + n * 1024) = ac[q >> 1][q & 1];
      asm volatile("s_waitcnt lgkmcnt(0)" ::: "memory");
      fl[20 + n] = s + 1;
      lds_wait_ge(20, 3, s + 1, tmo, 0x30016);
      {
        const LAS f32x4* pr = (const LAS f32x4*)(L + vr + n * 4096);
        const f32x4 o = (pr[0] + pr[64]) + (pr[128] + pr[192]);
        const int lo = olane();
        *(v2u*)(out + (size_t)(s * 32 + 16 * (n >> 1) + (lo & 15)) * 2048 + 1024 + 16 * (c0 + (n & 1)) + 4 * (lo >> 4)) =
            v2u{pk2(o[0], o[1]), pk2(o[2], o[3])};
      }
      EVB(5);
#pragma unroll
      for (int q = 0; q < NX1; ++q) raw[q >> 3][q & 7] = nx[q >> 3][q & 7];
      EVDUMP(s);
    }
  } else if (w == 6) {
    if (c == 1) hs_lead<64>(rs, false, CNT_HS, 192u);
  } else {
    int narm = NARM + team;
    if (W7P) __builtin_amdgcn_s_setprio(W7P);
    const int sdk_ = NS2F(TN(2, SDK)), cv1_ = NS2F(TN(3, CV1)), cv2_ = NS2F(TN(4, CV2));
    arm_wait(rs, tmo);
    PTW(4, 7);
    Clk ck;
    clk_init(ck);
    unsigned t1 = cyc32();
    const int mg_ = NS2F(TN(0, MARG)), tol_ = NS2F(TN(13, TOL)), xl_ = NS2F(TN(14, XLATE));
    Pace pc = {0u, NS2F(ED0), NS2F(EA0), 0};
    const int sti_ = TN(15, STI), ofd_ = NS2F(TN(9, OFD));
#pragma unroll 1
    for (int s = 0; s < NSEG; ++s) {
      EVDECL;
      const bool on = (s & 1) == team;
      if (!on || s < 3) clk_update(ck, s < 3);
      int k1 = (cv1_ * (int)ck.cq) >> 16, k2 = (cv2_ * (int)ck.cq) >> 16;
      int sst = (int)OFF_STEP + s * (int)UNIT + R_ST + ((cown ^ xreg(r_s + s)) << 12);
      asm volatile("" : "+s"(k1), "+s"(k2), "+s"(sst));
      OPQ(va, lane * 32);
      OPQ(vt, L_F + 48);
      wait_cyc(t1);
      EV(0);
      v2u st = __builtin_amdgcn_raw_buffer_load_b64(rs, va, sst, AUX_SC1 | STA);
      clk_fresh(ck);
      asm volatile("" : "+s"(ck.r), "+s"(ck.m));
      v2u st2;
      if (SPC) {
        asm volatile("" : "+v"(va));
        st2 = __builtin_amdgcn_raw_buffer_load_b64(rs, va, sst + 2048, AUX_SC1 | STA);
      }
      asm volatile("" : "+v"(st));
      if (__builtin_expect(anysent(st[0]), 0)) {
        if (SPC) st = st2;
        if (!SPC || anysent(st[0])) _Pragma("clang loop unroll(disable)") for (unsigned spins = 1 + SPC;; ++spins) {
          asm volatile("" : "+v"(va));
          if (spins <= 2 || !on) wait_cyc(t1 + spins * sti_);
          else if (spins > 64) __builtin_amdgcn_s_sleep(4);
          else if (SLR) __builtin_amdgcn_s_sleep(SLR);
          st = __builtin_amdgcn_raw_buffer_load_b64(rs, va, sst + (spins == 1 ? 2048 : (spins == 2 ? 32768 : 0)), AUX_SC1 | STA);
          if (!anysent(st[0])) break;
          if (spins > kSpinCap) {
            tmo_report(tmo, 0x3000b);
            break;
          }
        }
        if (s == 0) clk_fresh(ck);
      }
      if (s < 4) PTW(11 + s, 7);
      const int dm = stamp_max(ck, st[0]);
      const unsigned tr = clk_cyc(ck, dm);
      if (on) {
        *(volatile LAS v4u*)(L + vt) = v4u{tr + k2, tr, ck.inv, 0u};
        EV(1);
        wait_cyc(tr + k1);
        RAW_BARRIER();
        if (s < NSEG - 1) BARE_BARRIER();
        if (narm < NSEG) {
          __builtin_amdgcn_s_sleep(5);
          lds_wait_ge(2, 1, s + 1, tmo, 0x3001b);
#pragma unroll 1
          for (int q = 0; q < 2; ++q) {
            if (narm < NSEG) ARM_REC(narm)
            ++narm;
          }
        }
      }
      if (SPC) asm volatile("" ::"v"(st2));
      pace_step(pc, s, (ck.r << 4) + dm, st[1], mg_, tol_, xl_);
      t1 = clk_cyc(ck, dm + pc.dq + pc.ea - sdk_ + (on ? ofd_ : 0));
      EVDUMP(s);
      EVCLK(s, ck);
    }
  }
  return 0u;
}
__global__ __launch_bounds__(512, 2) void mega_kernel(const u16* __restrict__ x, const u16* __restrict__ mem0,
                                                      const u16* __restrict__ Wq, const u16* __restrict__ Wk,
                                                      const u16* __restrict__ Wv, const u16* __restrict__ Wo,
                                                      char* __restrict__ ws, u16* __restrict__ out,
                                                      const u16* __restrict__ ax, const u16* __restrict__ awr,
                                                      const u16* __restrict__ awi) {
  ws += (size_t)(-(uintptr_t)ws) & (UNIT - 1);
  const int bx = BIDX;
  const int w = __builtin_amdgcn_readfirstlane(threadIdx.x >> 6);
  const int xg = bx & 7, idx = bx >> 3;
#define TEND
#define TMID(k)
#define CODEPF 16
  if (w == 7 && idx < CODEPF) {
    u64 pc;
    unsigned len, keep;
    asm volatile("s_getpc_b64 %0\n.Lcode_here:\n\ts_mov_b32 %1, .Lcode_end-.Lcode_here\n\t.subsection 1\n.Lcode_end:\n\t.subsection 0" : "=s"(pc), "=s"(len));
    const v4u rc = {(unsigned)pc, (unsigned)(pc >> 32) & 0xffffu, len, 0x00020000u};
    asm volatile("s_mov_b32 %0, m0\n\ts_mov_b32 m0, %3\n\ts_nop 0\n\tbuffer_load_dword %1, %2, 0 offen lds\n\ts_mov_b32 m0, %0"
                 : "=&s"(keep)
                 : "v"(olane() * 128 + idx * 8192), "s"(rc), "s"(32768 + 7 * 2048)
                 : "memory");
  }
  const u64 nonce = hs_post(__builtin_amdgcn_make_buffer_rsrc(ws, 0, 65536, 0x00020000), w, xg >= NXC && (idx & PQ) ? 7 : 0);
  TMID(0);
  int role, h, par = 0;
  if (xg < NXC) {
    role = idx >= 2 * PQ ? 4 : idx >= PQ;
    h = xg * PQ + idx % PQ;
  } else {
    role = 2 + (idx / PQ & 1);
    par = idx / (2 * PQ);
    h = (xg - NXC) * PQ + idx % PQ;
  }
  if (role == 0) wroles<0>(h, par, idx & 7, xg, w, nonce, x, Wq, Wk, Wv, Wo, ws);
  if (role == 1) wroles<1>(h, par, idx & 7, xg, w, nonce, x, Wq, Wk, Wv, Wo, ws);
  if (role == 2) wroles<2>(h, par, idx & 7, xg, w, nonce, x, Wq, Wk, Wv, Wo, ws);
  if (role == 3) wroles<3>(h, par, idx & 7, xg, w, nonce, x, Wq, Wk, Wv, Wo, ws);
  const int oc = xg * PO + idx - 2 * PQ;
  unsigned perm = 0;
  if (role == 4 && HLT == 0) help(oc, w, Wq, Wk, Wv);
  if (role == 4) {
    perm = owner<true>(oc, w, nonce, mem0, ws, out, 0u, Wq, Wk, Wv);
    VMCNT(0);
#define PFT 1300
#define PFM 5
#define PFG 0
#define PFS 0
    const int w_ = w, oc_ = oc;
    if (PFT && (PFS ? oc & 1 : w < 6 && ((PFM >> (w >> 1)) & 1))) {
      const int w = PFS ? w_ & 5 : w_, oc = PFS ? oc_ ^ ((w_ >> 1) & 1) : oc_;
      const u16* src = w < 2 ? x : (w < 4 ? ax : (w == 4 ? awr : awi));
      const __amdgpu_buffer_rsrc_t ri = __builtin_amdgcn_make_buffer_rsrc((void*)src, 0, w < 4 ? S * D * 2 : S * DF * 2, 0x00020000);
      _Pragma("clang loop unroll(disable)") for (int k = 0; k < 4096 && now32() - (unsigned)nonce < PFT + (w >> 1) * PFG; ++k) __builtin_amdgcn_s_sleep(4);
      unsigned t[5];
#pragma unroll
      for (int q = 0; q < 5; ++q)
        t[q] = __builtin_amdgcn_raw_buffer_load_b32(ri, olane() * 128 + oc * 8192 + (q + (w < 4 ? (w & 1) * 4 : 0)) * 524288 + (w < 4 && q == 4 ? OOB : 0), 0, 0);
      asm volatile("" ::"v"(t[0]), "v"(t[1]), "v"(t[2]), "v"(t[3]), "v"(t[4]));
    }
#define CPT 1500
    if (CPT && CODEPF && (PFS ? !(oc & 1) && w >= 5 && w < 7 : w == 6)) {
      _Pragma("clang loop unroll(disable)") for (int k = 0; k < 4096 && now32() - (unsigned)nonce < CPT; ++k) __builtin_amdgcn_s_sleep(4);
      u64 pc;
      unsigned len, off;
      asm volatile("s_getpc_b64 %0\n.Lcode_2:\n\ts_mov_b32 %1, .Lcode_end-.Lcode_here\n\ts_mov_b32 %2, .Lcode_2-.Lcode_here" : "=s"(pc), "=s"(len), "=s"(off));
      const unsigned t = __builtin_amdgcn_raw_buffer_load_b32(__builtin_amdgcn_make_buffer_rsrc((void*)(pc - (long long)(int)off), 0, len, 0x00020000),
                                                              olane() * 128 + (PFS ? (oc & 14) + (w & 1) : idx & 15) * 8192, 0, 0);
      asm volatile("" ::"v"(t));
    }
  }
  TMID(2);
  const __amdgpu_buffer_rsrc_t rs = __builtin_amdgcn_make_buffer_rsrc(ws, 0, 65536, 0x00020000);
  gu32* tmo = (gu32*)(ws + OFF_TMO);
  volatile LAS int* sact = (volatile LAS int*)(g_lds + 163584);
  unsigned idle = 0;
  bool tab = false;
#define FSIDE 0
  char* const x1 = ws + OFF_X1 + ((size_t)(-(uintptr_t)(ws + OFF_X1)) & ((2u << 20) - 1));
  int sd = 0;
#define TLOG(id)
  auto pick = [&]() __attribute__((always_inline)) {
    int off = CNT_FFT1 + (olane() & 7) * 128;
    asm volatile("" : "+v"(off));
    const unsigned v = __builtin_amdgcn_raw_buffer_load_b32(rs, off, 0, AUX_SC1) + 1u;
    const unsigned d1 = __builtin_amdgcn_readlane(v, 0), d2 = __builtin_amdgcn_readlane(v, 1);
    const unsigned q1 = __builtin_amdgcn_readlane(v, 4), q2 = __builtin_amdgcn_readlane(v, 5), q3 = __builtin_amdgcn_readlane(v, 6);
    int kind = -1, qoff = 0;
    unsigned lim = 0;
    if (q1 < 130u - NF1O) kind = 1, qoff = Q_FFT1, lim = 130u - NF1O;
    else if (d1 >= 128u && q2 < 65u) kind = 2, qoff = Q_FFT2, lim = 65u;
    else if (d2 >= 65u && q3 < 128u) kind = 3, qoff = Q_FFT3, lim = 128u;
    else if (q3 >= 128u) kind = -2;
    int act = kind;
    if (kind >= 0) {
      unsigned t = 0;
      if (olane() == 0) t = __hip_atomic_fetch_add((gu32*)(ws + qoff), 1u, RLX_AGENT) + 1u;
      t = __builtin_amdgcn_readfirstlane(t);
      act = t < lim ? (kind << 16) | (int)t : -3;
    }
    return act;
  };
  for (;;) {
    if (w == 0) *sact = role == 4 ? (oc > 1 && oc < NF1O ? 0x18000 | oc : -2) : pick();
    RAW_BARRIER();
    const int act = __builtin_amdgcn_readfirstlane(*sact);
    RAW_BARRIER();
    if (act == -2) break;
    if (act >= 0) {
      const int kind = act >> 16, t = act & 0xffff;
      TLOG(1000 + kind);
      if (!tab) {
        if (w == 7) sact[1] = numa_far(numa_load(rs, tmo));
        fft_table(w);
        sd = (__builtin_amdgcn_readfirstlane(sact[1]) ^ (int)((uintptr_t)x1 >> 8) ^ FSIDE) & 8192;
      }
      tab = true;
      if (kind == 1) {
        fft_pass1(ws, x1, sd, ax, awr, awi, (t & 0x8000) ? t & 0xff : (t < 2 ? t : NF1O - 2 + t), w);
        count_done(ws, CNT_FFT1, w);
      } else if (kind == 2) {
        fft_pass2(ws, x1, sd, t == 0 ? 64 : t - 1, w);
        count_done(ws, CNT_FFT2, w);
      } else fft_pass3(ws, x1, sd, out, t, w);
      TLOG(999);
      if (role == 4) break;
    } else if (act == -1) {
      __builtin_amdgcn_s_sleep(64);
      if (spin_fail(idle, tmo, 0x30022)) break;
    }
  }
  if (role == 4) {
    owner<false>(oc, w, nonce, mem0, ws, out, perm, Wq, Wk, Wv);
    return;
  }
  TEND;
}

extern "C" void kernel_launch(void* const* d_in, const int* in_sizes, int n_in, void* d_out, int out_size, void* d_ws,
                              size_t ws_size, hipStream_t stream) {
  const u16* A_x = (const u16*)d_in[0];
  const u16* A_wr = (const u16*)d_in[1];
  const u16* A_wi = (const u16*)d_in[2];
  const u16* B_x = (const u16*)d_in[3];
  const u16* B_mem0 = (const u16*)d_in[4];
  const u16* Wq = (const u16*)d_in[5];
  const u16* Wk = (const u16*)d_in[6];
  const u16* Wv = (const u16*)d_in[7];
  const u16* Wo = (const u16*)d_in[8];
  u16* out = (u16*)d_out;
  char* ws = (char*)d_ws;

  mega_kernel<<<256, 512, 0, stream>>>(B_x, B_mem0, Wq, Wk, Wv, Wo, ws, out, A_x, A_wr, A_wi);
}
```

```cpp
#include <hip/hip_runtime.h>
#include <hip/hip_bf16.h>
#include <stdint.h>
#include <type_traits>

typedef unsigned short u16;
typedef unsigned long long u64;
using bf16x8 = __attribute__((ext_vector_type(8))) __bf16;
using bf16x16 = __attribute__((ext_vector_type(16))) __bf16;
using f32x4 = __attribute__((ext_vector_type(4))) float;
typedef unsigned v32u __attribute__((ext_vector_type(32)));
typedef unsigned v16u __attribute__((ext_vector_type(16)));
typedef unsigned v8u __attribute__((ext_vector_type(8)));
typedef unsigned v4u __attribute__((ext_vector_type(4)));
typedef unsigned v2u __attribute__((ext_vector_type(2)));
typedef short s16x4 __attribute__((ext_vector_type(4)));
typedef float f32x2 __attribute__((ext_vector_type(2)));
#define GAS __attribute__((address_space(1)))
#define LAS __attribute__((address_space(3)))
typedef GAS unsigned gu32;
#define RLX_AGENT __ATOMIC_RELAXED, __HIP_MEMORY_SCOPE_AGENT

constexpr int S = 2048, D = 1024, H = 32, DH = 128, SEG = 32, NSEG = 64;
constexpr int DF = 513;

#define XSW 4
#define BIDX (blockIdx.x ^ XSW)
constexpr int NXC = 4;
constexpr int PQ = 32 / NXC, PO = 64 / NXC;

__shared__ __attribute__((aligned(16))) char g_lds[163840];
#define SPINCAP (1u << 18)
constexpr unsigned kSpinCap = SPINCAP;
constexpr int AUX_SC1 = 16;
constexpr int OOB = 0x7ff00000;
constexpr size_t UNIT = 524288;
constexpr int F_QK = 0, F_VW = 8192, CNT_FFT1 = 16384, CNT_FFT2 = 16512, CNT_ARM = 16640;
constexpr int Q_FFT1 = 16896, Q_FFT2 = 17024, Q_FFT3 = 17152;
constexpr size_t OFF_TMO = 20480;
constexpr size_t OFF_CAL = 24576, OFF_XID = 25088;
constexpr size_t ARM_HOST = 28672;
constexpr int OFF_REQ = 49152, OFF_ACK = 53248, CNT_HS = 17280, CNT_HSO = 17408;
constexpr size_t OFF_SCR = 65536;
constexpr size_t OFF_VM = UNIT;
constexpr int VM_SLOT = 4 << 20, VM_SLOT2 = 8 << 20;
constexpr size_t OFF_DBG = 3 * UNIT;
constexpr size_t OFF_STEP = 27 * UNIT;
constexpr int R_MEM = 0, R_EX = 131072, R_PM = 262144, R_SC = 294912, R_ST = 327680, R_PX = 393216, R_DUM = 475136;
constexpr size_t OFF_QK = (size_t)48 << 20;
constexpr size_t OFF_X1 = (size_t)217 << 20;
#define GST 64
constexpr size_t OFF_VWX = (size_t)(GST == 64 ? 89 : 80) << 20;
constexpr size_t WS_USED = (size_t)217 << 20;
constexpr int NARM = 4;
#define SCL 1
#define SC_POS(q, r) ((q) * 256 + ((r) & 4) * 32 + ((r) & 3) * 16)

__device__ __forceinline__ int olane() {
  int t;
  asm volatile("v_mbcnt_lo_u32_b32 %0, -1, 0\n\tv_mbcnt_hi_u32_b32 %0, -1, %0" : "=v"(t));
  __builtin_assume(t >= 0 && t < 64);
  return t;
}
__device__ __forceinline__ float bf2f(u16 v) { return __uint_as_float((unsigned)v << 16); }
__device__ __forceinline__ unsigned pk2(float a, float b) {
  typedef __bf16 b2 __attribute__((ext_vector_type(2)));
  f32x2 f = {a, b};
  b2 r = __builtin_convertvector(f, b2);
  return __builtin_bit_cast(unsigned, r);
}
__device__ __forceinline__ unsigned pk2s(float a, float b) { return min(pk2(a, b), 0xfffffffeu); }
__device__ __forceinline__ u16 f2bf(float f) { return (u16)(pk2(f, 0.f) & 0xffffu); }
__device__ __forceinline__ float rbf(float f) { return __uint_as_float(pk2(0.f, f)); }
__device__ __forceinline__ float opq_one() {
  float o = 1.f;
  asm("" : "+s"(o));
  return o;
}
__device__ __forceinline__ unsigned fold_pk(f32x4 a, float one) { return pk2(a[0] + a[2], __builtin_fmaf(a[1], one, a[3])); }
__device__ __forceinline__ float mul1(float a, float c) {
  float r;
  asm("v_mul_f32 %0, %2, %1" : "=v"(r) : "v"(a), "s"(c));
  return r;
}
__device__ __forceinline__ float fma1(float a, float c, float b) {
  float r;
  asm("v_fma_f32 %0, %1, %2, %3" : "=v"(r) : "v"(a), "s"(c), "v"(b));
  return r;
}
__device__ __forceinline__ int xreg(int r) { return ((r & 1) << 2) | ((r >> 1) & 1) | ((r >> 1) & 2); }

__device__ __forceinline__ void tmo_report(gu32* tmo, unsigned code) {
  unsigned z = 0, c = code;
  asm volatile("" : "+v"(z), "+v"(c));
  asm volatile("global_store_dword %0, %1, %2 sc1" ::"v"(z), "v"(c), "s"(tmo) : "memory");
}
__device__ __forceinline__ bool spin_fail(unsigned& spins, gu32* tmo, unsigned code) {
  asm volatile("" ::: "memory");
  if (__builtin_expect(++spins > kSpinCap, 0)) {
    tmo_report(tmo, code);
    return true;
  }
  return false;
}
__device__ __forceinline__ unsigned wave_min(unsigned v) {
  v = min(v, (unsigned)__builtin_amdgcn_mov_dpp((int)v, 0x121, 0xf, 0xf, true));
  v = min(v, (unsigned)__builtin_amdgcn_mov_dpp((int)v, 0x122, 0xf, 0xf, true));
  v = min(v, (unsigned)__builtin_amdgcn_mov_dpp((int)v, 0x124, 0xf, 0xf, true));
  v = min(v, (unsigned)__builtin_amdgcn_mov_dpp((int)v, 0x128, 0xf, 0xf, true));
  auto r = __builtin_amdgcn_permlane32_swap(v, v, false, false);
  v = min(r[0], r[1]);
  auto q = __builtin_amdgcn_permlane16_swap(v, v, false, false);
  return min(q[0], q[1]);
}
__device__ __forceinline__ float xmax16_32(float x) {
  auto r = __builtin_amdgcn_permlane32_swap(__float_as_uint(x), __float_as_uint(x), false, false);
  x = __builtin_amdgcn_fmed3f(__uint_as_float(r[0]), __uint_as_float(r[1]), __builtin_inff());
  auto q = __builtin_amdgcn_permlane16_swap(__float_as_uint(x), __float_as_uint(x), false, false);
  return __builtin_amdgcn_fmed3f(__uint_as_float(q[0]), __uint_as_float(q[1]), __builtin_inff());
}
__device__ __forceinline__ float xsum16(float x) {
  auto q = __builtin_amdgcn_permlane16_swap(__float_as_uint(x), __float_as_uint(x), false, false);
  return __uint_as_float(q[0]) + __uint_as_float(q[1]);
}
__device__ __forceinline__ float xsum16_32(float x) {
  auto r = __builtin_amdgcn_permlane32_swap(__float_as_uint(x), __float_as_uint(x), false, false);
  return xsum16(__uint_as_float(r[0]) + __uint_as_float(r[1]));
}
__device__ __forceinline__ unsigned mx4(unsigned m, v4u v) { return max(max(max(max(m, v[0]), v[1]), v[2]), v[3]); }
__device__ __forceinline__ bool anysent(unsigned m) { return __builtin_amdgcn_ballot_w64(m == 0xffffffffu) != 0ull; }

__device__ __forceinline__ unsigned now32() { return (unsigned)__builtin_amdgcn_s_memrealtime(); }
__device__ __forceinline__ unsigned cyc32() { return (unsigned)__builtin_amdgcn_s_memtime(); }
#define CMAX 14000
#define LAL ".p2align 3\n"
__device__ __forceinline__ void wait_cyc(unsigned t, int d = 0x7fffffff) {
  t = __builtin_amdgcn_readfirstlane(t);
  if (d == 0x7fffffff) d = (int)(t - cyc32());
  if (d > CMAX) return;
  while (d > 260) {
    __builtin_amdgcn_s_sleep(1);
    d = (int)(t - cyc32());
  }
  d = __builtin_amdgcn_readfirstlane(d - 70);
  if (d > 0) asm volatile(LAL "1: s_sub_u32 %0, %0, 28\n\ts_cbranch_scc0 1b" : "+s"(d)::"scc", "memory");
}
#define CLKSPAN 124u
struct Clk {
  unsigned r, m;
  unsigned cq, inv;
};
__device__ __forceinline__ bool clk_edge(unsigned& r, unsigned& m, int tries) {
  constexpr int NS = 14;
#pragma unroll 1
  for (int k = 0; k < tries; ++k) {
    u64 t[NS], m0, m1;
    asm volatile("s_memtime %0" : "=s"(m0));
#pragma unroll
    for (int i = 0; i < NS; ++i) {
      asm volatile("s_memrealtime %0\n\ts_nop 0" : "=s"(t[i]));
      if (i == NS - 2) asm volatile("s_memtime %0" : "=s"(m1));
    }
    asm volatile("s_waitcnt lgkmcnt(0)" : "+s"(m0), "+s"(m1), "+s"(t[0]), "+s"(t[1]), "+s"(t[2]), "+s"(t[3]), "+s"(t[4]), "+s"(t[5]),
                 "+s"(t[6]), "+s"(t[7]), "+s"(t[8]), "+s"(t[9]), "+s"(t[10]), "+s"(t[11]), "+s"(t[12]), "+s"(t[13]));
    unsigned n = 0;
    static_assert(NS == 14, "");
    asm("s_cmp_eq_u32 %2, %1\n\ts_addc_u32 %0, %0, 0\n\t"
        "s_cmp_eq_u32 %3, %1\n\ts_addc_u32 %0, %0, 0\n\t"
        "s_cmp_eq_u32 %4, %1\n\ts_addc_u32 %0, %0, 0\n\t"
        "s_cmp_eq_u32 %5, %1\n\ts_addc_u32 %0, %0, 0\n\t"
        "s_cmp_eq_u32 %6, %1\n\ts_addc_u32 %0, %0, 0\n\t"
        "s_cmp_eq_u32 %7, %1\n\ts_addc_u32 %0, %0, 0\n\t"
        "s_cmp_eq_u32 %8, %1\n\ts_addc_u32 %0, %0, 0\n\t"
        "s_cmp_eq_u32 %9, %1\n\ts_addc_u32 %0, %0, 0\n\t"
        "s_cmp_eq_u32 %10, %1\n\ts_addc_u32 %0, %0, 0\n\t"
        "s_cmp_eq_u32 %11, %1\n\ts_addc_u32 %0, %0, 0\n\t"
        "s_cmp_eq_u32 %12, %1\n\ts_addc_u32 %0, %0, 0\n\t"
        "s_cmp_eq_u32 %13, %1\n\ts_addc_u32 %0, %0, 0\n\t"
        "s_cmp_eq_u32 %14, %1\n\ts_addc_u32 %0, %0, 0\n\t"
        : "+s"(n)
        : "s"((unsigned)t[0]), "s"((unsigned)t[1]), "s"((unsigned)t[2]), "s"((unsigned)t[3]), "s"((unsigned)t[4]), "s"((unsigned)t[5]), "s"((unsigned)t[6]), "s"((unsigned)t[7]), "s"((unsigned)t[8]), "s"((unsigned)t[9]), "s"((unsigned)t[10]), "s"((unsigned)t[11]), "s"((unsigned)t[12]), "s"((unsigned)t[13])
        : "scc");
    r = (unsigned)t[0] + 4;
    m = (unsigned)m0 + n * 8;
    asm volatile("" : "+s"(m));
    if ((unsigned)m1 - (unsigned)m0 <= CLKSPAN) return true;
  }
  return false;
}
__device__ __forceinline__ unsigned sdiv(unsigned a, unsigned b) {
  unsigned r;
  float t, u;
  asm volatile("v_cvt_f32_u32 %1, %4\n\tv_cvt_f32_u32 %2, %3\n\tv_rcp_f32 %1, %1\n\ts_nop 0\n\tv_mul_f32 %1, %1, %2\n\tv_cvt_u32_f32 %1, %1\n\t"
               "s_nop 0\n\tv_readfirstlane_b32 %0, %1\n\ts_nop 3"
               : "=s"(r), "=&v"(t), "=&v"(u)
               : "s"(a), "s"(b));
  return r;
}
__device__ __forceinline__ void clk_init(Clk& c) {
  clk_edge(c.r, c.m, 4);
  c.cq = 92500u;
  c.inv = 46432u;
}
__device__ __forceinline__ void clk_update(Clk& c, bool first = false) {
  unsigned r, m;
  if (!clk_edge(r, m, first ? 2 : 1) && r - c.r < 1500u) return;
  if (r - c.r >= 100u && r - c.r < 40000u) {
    unsigned q = sdiv((m - c.m) << 12, r - c.r);
    asm("s_max_u32 %0, %0, 0x101d0\n\ts_min_u32 %0, %0, 0x1b580" : "+s"(q)::"scc");
    c.cq = first ? q : (3 * c.cq + q) >> 2;
    c.inv = sdiv(0xffffffffu, c.cq);
  }
  c.r = r;
  c.m = m;
}
__device__ __forceinline__ void clk_step(Clk& c) {
  unsigned r, m;
  if (!clk_edge(r, m, 1) && r - c.r < 1500u) return;
  const unsigned dr = r - c.r;
  if (dr - 100u < 400u) {
    unsigned q = c.cq + 5 * (m - c.m - ((dr * c.cq) >> 12));
    asm("s_max_u32 %0, %0, 0x101d0\n\ts_min_u32 %0, %0, 0x1b580" : "+s"(q)::"scc");
    c.cq = q;
    c.inv += (unsigned)__mulhi((int)c.inv, -(int)(q * c.inv));
  }
  c.r = r;
  c.m = m;
}
__device__ __forceinline__ void wave_min2(unsigned& a, unsigned& b) {
  unsigned t, u;
#define M2(c) "v_min_u32_dpp %0, %0, %0 " c " row_mask:0xf bank_mask:0xf\n\tv_min_u32_dpp %1, %1, %1 " c " row_mask:0xf bank_mask:0xf\n\ts_nop 0\n\t"
#define S2(n) "v_mov_b32 %2, %0\n\tv_mov_b32 %3, %1\n\ts_nop 0\n\tv_permlane" n "_swap_b32 %0, %2\n\tv_permlane" n "_swap_b32 %1, %3\n\tv_min_u32 %0, %0, %2\n\tv_min_u32 %1, %1, %3\n\t"
  asm("s_nop 1\n\t" M2("row_ror:1") M2("row_ror:2") M2("row_ror:4") M2("row_ror:8") S2("32") S2("16") "s_nop 0" : "+v"(a), "+v"(b), "=&v"(t), "=&v"(u));
#undef M2
#undef S2
}
__device__ __forceinline__ void clk_fresh(Clk& c) {
  unsigned r, m;
  if (clk_edge(r, m, 1)) {
    c.r = r;
    c.m = m;
  }
}
__device__ __forceinline__ unsigned clk_fine(const Clk& c, unsigned m) {
  return (c.r << 4) + (unsigned)(((u64)(m - c.m) * c.inv) >> 16);
}
__device__ __forceinline__ int stamp_max(const Clk& c, unsigned st) {
  const unsigned tn = (c.r << 4) + 65536u;
  return 65536 - __builtin_amdgcn_readfirstlane((int)wave_min(tn - st));
}
__device__ __forceinline__ unsigned clk_cyc(const Clk& c, int d) { return c.m + (unsigned)(((long long)d * (int)c.cq) >> 16); }
#define TN(k, d) (d)
#define XL0 0
#define XL1 0
#define XL2 0
#define NS2F(ns) (((ns) * 8) / 5)
struct Pace {
  unsigned rf;
  int ed, ea, dq;
};
template <bool RED = true>
__device__ __forceinline__ void pace_step(Pace& p, int s, unsigned rf, unsigned rep, int marg, int tol, int xlate) {
  const int arr = (int)(rf - p.rf) - p.dq;
  const bool late = s == 0 || arr > p.ea + tol;
  if (s > 0) {
    const int md = RED ? (int)~__builtin_amdgcn_readfirstlane(wave_min(~rep)) : (int)rep;
    p.ea = min(arr, p.ea + 5);
    p.ed = (int)min((unsigned)md, (unsigned)p.ed + 5u);
  }
  p.rf = rf;
  p.dq = p.ed + marg + (late ? xlate : 0);
}

template <int SLP>
__device__ __forceinline__ void wait_flag(__amdgpu_buffer_rsrc_t rs, int off, gu32* tmo, unsigned code) {
  _Pragma("clang loop unroll(disable)") for (unsigned spins = 0;;) {
    asm volatile("" : "+v"(off));
    unsigned v = __builtin_amdgcn_raw_buffer_load_b8(rs, off, 0, AUX_SC1);
    if (__all(v == 0u)) break;
    __builtin_amdgcn_s_sleep(SLP);
    if (spin_fail(spins, tmo, code)) break;
  }
}
#define RAW_BARRIER()                                    \
  do {                                                   \
    asm volatile("" ::: "memory");                       \
    __builtin_amdgcn_s_waitcnt(0xc07f);                  \
    __builtin_amdgcn_s_barrier();                        \
    asm volatile("" ::: "memory");                       \
  } while (0)
#define BARE_BARRIER()                 \
  do {                                 \
    asm volatile("" ::: "memory");     \
    __builtin_amdgcn_s_barrier();      \
    asm volatile("" ::: "memory");     \
  } while (0)
#define VMCNT(n) asm volatile("s_waitcnt vmcnt(" #n ")" ::: "memory")
#define SB() __builtin_amdgcn_sched_barrier(0)
#define OPQ(v, e) int v = (e); asm volatile("" : "+v"(v))
template <bool NT = false>
__device__ __forceinline__ void glds16(const void* base, int voff, unsigned dst) {
  unsigned keep;
  if constexpr (NT)
    asm volatile("s_mov_b32 %0, m0\n\ts_mov_b32 m0, %3\n\ts_nop 0\n\tglobal_load_lds_dwordx4 %1, %2 nt\n\ts_mov_b32 m0, %0"
                 : "=&s"(keep)
                 : "v"(voff), "s"(base), "s"(dst)
                 : "memory");
  else
  asm volatile("s_mov_b32 %0, m0\n\ts_mov_b32 m0, %3\n\ts_nop 0\n\tglobal_load_lds_dwordx4 %1, %2\n\ts_mov_b32 m0, %0"
               : "=&s"(keep)
               : "v"(voff), "s"(base), "s"(dst)
               : "memory");
}
__device__ __forceinline__ void touch4(const void* base, int voff, unsigned dst) {
  unsigned keep;
  asm volatile("s_mov_b32 %0, m0\n\ts_mov_b32 m0, %3\n\ts_nop 0\n\tglobal_load_lds_dword %1, %2\n\ts_mov_b32 m0, %0"
               : "=&s"(keep)
               : "v"(voff), "s"(base), "s"(dst)
               : "memory");
}
__device__ __forceinline__ f32x4 smf(v4u a, v8u b, f32x4 c, int idx) {
  return __builtin_amdgcn_smfmac_f32_16x16x64_bf16(__builtin_bit_cast(bf16x8, a), __builtin_bit_cast(bf16x16, b), c, idx, 0, 0);
}
__device__ __forceinline__ f32x4 smf1(v4u a, v8u b, f32x4 c, int idx) {
  return __builtin_amdgcn_smfmac_f32_16x16x64_bf16(__builtin_bit_cast(bf16x8, a), __builtin_bit_cast(bf16x16, b), c, idx, 0, 1);
}
__device__ __forceinline__ f32x4 mma(v4u a, v4u b, f32x4 c) {
  return __builtin_amdgcn_mfma_f32_16x16x32_bf16(__builtin_bit_cast(bf16x8, a), __builtin_bit_cast(bf16x8, b), c, 0, 0, 0);
}
__device__ __forceinline__ void st128(v4u d, __amdgpu_buffer_rsrc_t rs, int voff, int soff) {
  __builtin_amdgcn_raw_buffer_store_b128(d, rs, voff, soff, AUX_SC1);
  asm volatile("s_nop 1" ::"v"(d));
}
__device__ __forceinline__ f32x4 zero4() {
  f32x2 a, b;
  asm volatile("v_mov_b64 %0, 0" : "=v"(a));
  asm volatile("v_mov_b64 %0, 0" : "=v"(b));
  return f32x4{a[0], a[1], b[0], b[1]};
}
__device__ __forceinline__ f32x2 zero2() {
  f32x2 a;
  asm volatile("v_mov_b64 %0, 0" : "=v"(a));
  return a;
}
__device__ __forceinline__ v4u lo4(v8u v) { return v4u{v[0], v[1], v[2], v[3]}; }
__device__ __forceinline__ v4u hi4(v8u v) { return v4u{v[4], v[5], v[6], v[7]}; }
__device__ __forceinline__ v8u cat8(v4u a, v4u b) { return v8u{a[0], a[1], a[2], a[3], b[0], b[1], b[2], b[3]}; }
__device__ __forceinline__ v32u cat32(v8u a, v8u b, v8u c, v8u d) {
  const v16u x = __builtin_shufflevector(a, b, 0, 1, 2, 3, 4, 5, 6, 7, 8, 9, 10, 11, 12, 13, 14, 15);
  const v16u y = __builtin_shufflevector(c, d, 0, 1, 2, 3, 4, 5, 6, 7, 8, 9, 10, 11, 12, 13, 14, 15);
  return __builtin_shufflevector(x, y, 0, 1, 2, 3, 4, 5, 6, 7, 8, 9, 10, 11, 12, 13, 14, 15, 16, 17, 18, 19, 20, 21, 22, 23, 24, 25, 26,
                                 27, 28, 29, 30, 31);
}
__device__ __forceinline__ v2u trd(LAS char* p) {
  return __builtin_bit_cast(v2u, __builtin_amdgcn_ds_read_tr16_b64_v4i16((LAS s16x4*)p));
}
__device__ __forceinline__ int vm_slot(int s) { return (s & 1) * VM_SLOT + ((s >> 1) & 1) * VM_SLOT2; }
__device__ __forceinline__ int cls_of(unsigned perm, int x) { return (int)((perm >> (3 * x)) & 7u); }

__device__ __forceinline__ void arm_wait(__amdgpu_buffer_rsrc_t rs, gu32* tmo) {
  _Pragma("clang loop unroll(disable)") for (unsigned spins = 0;;) {
    OPQ(off, CNT_ARM);
    if (__all(__builtin_amdgcn_raw_buffer_load_b32(rs, off, 0, AUX_SC1) + 1u >= 64u)) break;
    __builtin_amdgcn_s_sleep(8);
    if (spin_fail(spins, tmo, 0x30007)) break;
  }
}
__device__ __forceinline__ v2u numa_load(__amdgpu_buffer_rsrc_t rs, gu32* tmo) {
  unsigned c, xid;
  _Pragma("clang loop unroll(disable)") for (unsigned spins = 0;;) {
    OPQ(a, olane() * 4);
    c = __builtin_amdgcn_raw_buffer_load_b32(rs, a, (int)OFF_CAL, AUX_SC1);
    xid = __builtin_amdgcn_raw_buffer_load_b32(rs, ((a >> 5) & (NXC - 1)) * 4, (int)OFF_XID, AUX_SC1);
    if (__all((c != 0xffffffffu) & (xid != 0xffffffffu))) break;
    __builtin_amdgcn_s_sleep(16);
    if (spin_fail(spins, tmo, 0x30008)) break;
  }
  return v2u{c, xid};
}
__device__ __forceinline__ unsigned numa_perm(v2u in, LAS unsigned* tab) {
  const int lane = olane();
  const unsigned c = in[0], xid = in[1];
  tab[lane] = 0xffffu;
  tab[64 + lane] = 0xffffu;
  tab[128 + lane] = 0u;
  __builtin_amdgcn_s_waitcnt(0xc07f);
  __hip_atomic_fetch_min(tab + ((c >> 16) & 127u), c & 0xffffu, __ATOMIC_RELAXED, __HIP_MEMORY_SCOPE_WORKGROUP);
  __hip_atomic_fetch_add(tab + 128 + ((c >> 21) & 3u) * 2 + ((c >> 17) & 1u), c & 0xffffu, __ATOMIC_RELAXED, __HIP_MEMORY_SCOPE_WORKGROUP);
  __builtin_amdgcn_s_waitcnt(0xc07f);
  asm volatile("" ::: "memory");
  const unsigned s0 = tab[128 + ((xid >> 2) & 3u) * 2], s1 = tab[129 + ((xid >> 2) & 3u) * 2];
  const bool far = ((lane >> 1) & 1) != (s1 < s0 ? 1 : 0);
  unsigned key = ((tab[(xid & 15u) * 8 + (lane & 7)] + (far ? 0x10000u : 0u)) << 6) | (unsigned)lane;
  if ((lane >> 3) >= NXC) key = 0xffffffffu;
  unsigned perm = 0;
#pragma unroll 1
  for (int r = 0; r < NXC; ++r) {
    const unsigned m = wave_min(key);
    const unsigned bx = (m >> 3) & 7u, bc = m & 7u;
    perm |= bc << (3 * bx);
    if ((unsigned)(lane >> 3) == bx || (unsigned)(lane & 7) == bc) key = 0xffffffffu;
  }
  return __builtin_amdgcn_readfirstlane(perm | (s1 < s0 ? 0u : 8192u));
}
__device__ __forceinline__ int numa_far(v2u in) {
  const unsigned c = in[0], x0 = __builtin_amdgcn_readfirstlane(in[1]);
  int d = (int)(c & 0xffffu);
  d = ((c >> 21) & 3u) == ((x0 >> 2) & 3u) ? ((c & 0x20000u) ? -d : d) : 0;
  d += __builtin_amdgcn_mov_dpp(d, 0x121, 0xf, 0xf, true);
  d += __builtin_amdgcn_mov_dpp(d, 0x122, 0xf, 0xf, true);
  d += __builtin_amdgcn_mov_dpp(d, 0x124, 0xf, 0xf, true);
  d += __builtin_amdgcn_mov_dpp(d, 0x128, 0xf, 0xf, true);
  d = __builtin_amdgcn_readlane(d, 0) + __builtin_amdgcn_readlane(d, 16) + __builtin_amdgcn_readlane(d, 32) + __builtin_amdgcn_readlane(d, 48);
  return d > 0 ? 0 : 8192;
}
#define VRING 64
#define QRING 64
#define VFAR 0
__device__ __forceinline__ int vw_addr(int fr, int tile, int seg, int gq) {
  return (int)OFF_VWX + ((((tile >> 3) * GST + (seg & (VRING - 1))) * 8 + (tile & 7)) * 8 + gq) * 4096;
}
__device__ __forceinline__ int fqk(int h, int seg) { return F_QK + h * 256 + seg * 4; }
__device__ __forceinline__ int fvw(int pr, int h, int seg) { return F_VW + ((pr * 4 + (h >> 3)) * 2 + (seg & 1)) * 256 + (seg >> 1) * 8 + (h & 7); }
constexpr int QK_RT = 4096;
__device__ __forceinline__ int qk_addr(int mat, int seg, int h) {
  return (int)OFF_QK + ((mat * 64 + (seg & (QRING - 1))) * 32 + h) * 8192;
}

__device__ __forceinline__ u64 hs_post(__amdgpu_buffer_rsrc_t rs, int w, int HSW) {
  const u64 n = __builtin_amdgcn_s_memrealtime();
  if (w == HSW) __builtin_amdgcn_raw_buffer_store_b64(v2u{(unsigned)n, (unsigned)(n >> 32)}, rs, olane() == 0 ? 0 : OOB,
                                                    OFF_REQ + (int)BIDX * 8, AUX_SC1);
  return n;
}
__device__ __forceinline__ bool hs_wait(__amdgpu_buffer_rsrc_t rs, u64 n, v2u v) {
  unsigned spins = 0;
  _Pragma("clang loop unroll(disable)") for (; spins < (kSpinCap << 2); ++spins) {
    if (__builtin_expect(__all(v[0] == (unsigned)n && v[1] == (unsigned)(n >> 32)), 1)) break;
    if (spins) __builtin_amdgcn_s_sleep(8);
    int z = 0;
    asm volatile("" : "+v"(z));
    v = __builtin_amdgcn_raw_buffer_load_b64(rs, z, OFF_ACK + (int)BIDX * 8, AUX_SC1);
  }
  return spins == 0;
}
__device__ __forceinline__ void cnt_inc(char* ws, int cnt) {
  if (olane() == 0) __hip_atomic_fetch_add((gu32*)(ws + cnt), 1u, RLX_AGENT);
}
template <int SLP>
__device__ __forceinline__ void hs_lead(__amdgpu_buffer_rsrc_t rs, bool fill, int cnt, unsigned want) {
  const int l16 = olane() * 16;
  if (fill) {
    for (int o = 0; o < (int)ARM_HOST; o += 1024) __builtin_amdgcn_raw_buffer_store_b128(v4u{~0u, ~0u, ~0u, ~0u}, rs, l16, o, AUX_SC1);
    VMCNT(0);
  }
  _Pragma("clang loop unroll(disable)") for (unsigned spins = 0; spins < (kSpinCap << 2); ++spins) {
    int va = l16;
    asm volatile("" : "+v"(va));
    const v4u a = __builtin_amdgcn_raw_buffer_load_b128(rs, va, OFF_REQ, AUX_SC1);
    const v4u b = __builtin_amdgcn_raw_buffer_load_b128(rs, va + 1024, OFF_REQ, AUX_SC1);
    const unsigned n = __builtin_amdgcn_raw_buffer_load_b32(rs, va & 0, cnt, AUX_SC1);
    if (__all(n + 1u >= want)) break;
    __builtin_amdgcn_raw_buffer_store_b128(a, rs, va, OFF_ACK, AUX_SC1);
    __builtin_amdgcn_raw_buffer_store_b128(b, rs, va + 1024, OFF_ACK, AUX_SC1);
    __builtin_amdgcn_s_sleep(SLP);
  }
}

#define PTW(i, wv)
#define PLDECL
#define PL(i)
#define PLDUMP(wv, mask)
#define PT(i) PTW(i, 0)
#define EVDECL
#define EV(k)
#define EVDUMP(s)
#define EVCLK(s, c)
#define EVOUT
#define VO_A \
  "s_branch .Lgo_%=\n" \
  ".Lrs_%=:\n" \
  "s_mov_b32 %[fs], -1\n" \
  "s_nop 15\n" \
  "s_nop 15\n" \
  ".Lrt_%=:\n" \
  "s_setprio 0\n" \
  "s_waitcnt vmcnt(0) lgkmcnt(0)\n" \
  "ds_read_b128 v[200:203], v243 offset:32768\n" \
  "ds_read_b128 v[204:207], v243 offset:33792\n" \
  "ds_read_b128 v[208:211], v243 offset:49152\n" \
  "ds_read_b128 v[212:215], v243 offset:50176\n" \
  "v_mov_b64 v[192:193], 0\n" \
  "v_mov_b64 v[194:195], 0\n" \
  "v_mov_b64 v[196:197], 0\n" \
  "v_mov_b64 v[198:199], 0\n" \
  "s_barrier\n" \
  "ds_read_b128 v[216:219], v251 offset:0\n" \
  "ds_read_b128 v[220:223], v251 offset:1024\n" \
  "ds_read_b128 v[224:227], v251 offset:2048\n" \
  "ds_read_b128 v[228:231], v251 offset:3072\n" \
  "s_waitcnt lgkmcnt(0)\n" \
  "s_barrier\n" \
  "s_branch .Ll1_%=\n" \
  ".p2align 6\n" \
  ".Lgo_%=:\n" \
  "ds_read_b128 v[200:203], v243 offset:32768\n" \
  "ds_read_b128 v[204:207], v243 offset:33792\n" \
  "ds_read_b128 v[208:211], v243 offset:49152\n" \
  "ds_read_b128 v[212:215], v243 offset:50176\n" \
  "v_mov_b64 v[192:193], 0\n" \
  "v_mov_b64 v[194:195], 0\n" \
  "v_mov_b64 v[196:197], 0\n" \
  "v_mov_b64 v[198:199], 0\n" \
  "s_mov_b32 %[keep], m0\n" \
  "v_readfirstlane_b32 %[cnt], v250\n" \
  "s_mov_b32 m0, %[cnt]\n" \
  "s_waitcnt lgkmcnt(0)\n" \
  "s_barrier\n" \
  "buffer_load_dwordx4 v251, %[rs], %[o0] offen sc1 lds\n" \
  "s_waitcnt vmcnt(0)\n" \
  "s_barrier\n" \
  "s_mov_b32 m0, %[keep]\n" \
  "ds_read_b128 v[216:219], v251 offset:0\n" \
  "ds_read_b128 v[220:223], v251 offset:1024\n" \
  "ds_read_b128 v[224:227], v251 offset:2048\n" \
  "ds_read_b128 v[228:231], v251 offset:3072\n" \
  "s_barrier\n" \
  ".Ll1_%=:\n" \
  "s_waitcnt lgkmcnt(3)\n" \
  "v_smfmac_f32_16x16x64_bf16 v[192:195], v[216:219], v[200:207], v248\n" \
  "v_smfmac_f32_16x16x64_bf16 v[196:199], v[216:219], v[208:215], v248\n" \
  "ds_read_b128 v[232:235], v251 offset:4096\n" \
  "ds_read_b128 v[236:239], v251 offset:5120\n" \
  "ds_read_b128 v[216:219], v251 offset:6144\n" \
  "s_waitcnt lgkmcnt(5)\n" \
  "v_smfmac_f32_16x16x64_bf16 v[192:195], v[220:223], v[0:7], v248\n" \
  "v_smfmac_f32_16x16x64_bf16 v[196:199], v[220:223], v[8:15], v248\n" \
  "ds_read_b128 v[220:223], v251 offset:7168\n" \
  "s_barrier\n" \
  "s_waitcnt lgkmcnt(5)\n" \
  "v_smfmac_f32_16x16x64_bf16 v[192:195], v[224:227], v[16:23], v248\n" \
  "v_smfmac_f32_16x16x64_bf16 v[196:199], v[224:227], v[24:31], v248\n" \
  "s_waitcnt lgkmcnt(4)\n" \
  "v_smfmac_f32_16x16x64_bf16 v[192:195], v[228:231], v[32:39], v248\n" \
  "v_smfmac_f32_16x16x64_bf16 v[196:199], v[228:231], v[40:47], v248\n" \
  "s_waitcnt lgkmcnt(3)\n" \
  "v_smfmac_f32_16x16x64_bf16 v[192:195], v[232:235], v[48:55], v248\n" \
  "v_smfmac_f32_16x16x64_bf16 v[196:199], v[232:235], v[56:63], v248\n" \
  "s_waitcnt lgkmcnt(2)\n" \
  "v_smfmac_f32_16x16x64_bf16 v[192:195], v[236:239], v[64:71], v248\n" \
  "v_smfmac_f32_16x16x64_bf16 v[196:199], v[236:239], v[72:79], v248\n" \
  "s_waitcnt lgkmcnt(1)\n" \
  "v_smfmac_f32_16x16x64_bf16 v[192:195], v[216:219], v[80:87], v248\n" \
  "s_waitcnt lgkmcnt(0)\n" \
  "v_smfmac_f32_16x16x64_bf16 v[192:195], v[220:223], v[96:103], v248\n" \
  "v_smfmac_f32_16x16x64_bf16 v[196:199], v[216:219], v[88:95], v248\n" \
  "v_smfmac_f32_16x16x64_bf16 v[196:199], v[220:223], v[104:111], v248\n" \
  "s_setprio 3\n" \
  "v_add_f32 v192, v192, v194\n" \
  "v_add_f32 v193, v193, v195\n" \
  "s_nop 5\n" \
  "v_add_f32 v194, v196, v198\n" \
  "v_add_f32 v195, v197, v199\n" \
  "ds_write_b128 v250, v[192:195] offset:24576\n" \
  "v_mov_b64 v[200:201], 0\n" \
  "v_mov_b64 v[202:203], 0\n" \
  "v_mov_b64 v[204:205], 0\n" \
  "v_mov_b64 v[206:207], 0\n" \
  "v_mov_b64 v[208:209], 0\n" \
  "v_mov_b64 v[210:211], 0\n" \
  "v_mov_b64 v[212:213], 0\n" \
  "v_mov_b64 v[214:215], 0\n" \
  "s_waitcnt lgkmcnt(0)\n" \
  "s_setprio 0\n" \
  "s_barrier\n" \
  "s_nop 11\n" \
  "ds_read_b32 v241, v251 offset:23564\n" \
  "ds_read_b128 v[216:219], v249 offset:0\n" \
  "ds_read_b128 v[220:223], v249 offset:1024\n" \
  "ds_read_b128 v[224:227], v249 offset:16384\n" \
  "ds_read_b128 v[228:231], v249 offset:17408\n" \
  "s_waitcnt lgkmcnt(4)\n" \
  "v_cmp_eq_u32 vcc, %[fs], v241\n" \
  "s_cbranch_vccnz .Lrs_%=\n" \
  "s_barrier\n" \
  "ds_read_b64_tr_b16 v[232:233], v245 offset:17920\n" \
  "ds_read_b64_tr_b16 v[234:235], v245 offset:18112\n" \
  "ds_read_b64_tr_b16 v[236:237], v245 offset:18688\n" \
  "ds_read_b64_tr_b16 v[238:239], v245 offset:18880\n" \
  "ds_read_b64_tr_b16 v[192:193], v245 offset:16384\n" \
  "ds_read_b64_tr_b16 v[194:195], v245 offset:16576\n" \
  "ds_read_b64_tr_b16 v[196:197], v245 offset:17152\n" \
  "ds_read_b64_tr_b16 v[198:199], v245 offset:17344\n" \
  "ds_read_b32 v241, v251 offset:23560\n" \
  "s_waitcnt lgkmcnt(7)\n" \
  "v_smfmac_f32_16x16x64_bf16 v[200:203], v[232:235], v[216:223], v248 abid:1\n" \
  "ds_read_b128 v[216:219], v249 offset:32768\n" \
  "ds_read_b128 v[220:223], v249 offset:33792\n" \
  "v_smfmac_f32_16x16x64_bf16 v[204:207], v[232:235], v[128:135], v248 abid:1\n" \
  "s_waitcnt lgkmcnt(7)\n" \
  "v_smfmac_f32_16x16x64_bf16 v[200:203], v[236:239], v[224:231], v248 abid:1\n" \
  "ds_read_b128 v[224:227], v249 offset:49152\n" \
  "ds_read_b128 v[228:231], v249 offset:50176\n" \
  "v_smfmac_f32_16x16x64_bf16 v[204:207], v[236:239], v[136:143], v248 abid:1\n" \
  "s_waitcnt lgkmcnt(7)\n" \
  "v_smfmac_f32_16x16x64_bf16 v[200:203], v[192:195], v[176:183], v248 abid:1\n" \
  "v_smfmac_f32_16x16x64_bf16 v[204:207], v[192:195], v[112:119], v248 abid:1\n" \
  "s_waitcnt lgkmcnt(5)\n" \
  "v_smfmac_f32_16x16x64_bf16 v[200:203], v[196:199], v[184:191], v248 abid:1\n" \
  "s_waitcnt lgkmcnt(4)\n" \
  "v_cmp_eq_u32 vcc, %[fs], v241\n" \
  "v_smfmac_f32_16x16x64_bf16 v[204:207], v[196:199], v[120:127], v248 abid:1\n" \
  "s_waitcnt lgkmcnt(2)\n" \
  "v_smfmac_f32_16x16x64_bf16 v[208:211], v[192:195], v[216:223], v248 abid:1\n" \
  "ds_read_b128 v[216:219], v243 offset:0\n" \
  "ds_read_b128 v[220:223], v243 offset:1024\n" \
  "v_smfmac_f32_16x16x64_bf16 v[212:215], v[192:195], v[144:151], v248 abid:1\n" \
  "s_waitcnt lgkmcnt(2)\n" \
  "v_smfmac_f32_16x16x64_bf16 v[208:211], v[196:199], v[224:231], v248 abid:1\n" \
  "ds_read_b128 v[224:227], v243 offset:16384\n" \
  "ds_read_b128 v[228:231], v243 offset:17408\n" \
  "v_cvt_pk_bf16_f32 v200, v200, v201\n" \
  "v_smfmac_f32_16x16x64_bf16 v[212:215], v[196:199], v[152:159], v248 abid:1\n" \
  "v_cvt_pk_bf16_f32 v201, v202, v203\n" \
  "v_smfmac_f32_16x16x64_bf16 v[212:215], v[232:235], v[160:167], v248 abid:1\n" \
  "v_cvt_pk_bf16_f32 v202, v204, v205\n" \
  "v_smfmac_f32_16x16x64_bf16 v[212:215], v[236:239], v[168:175], v248 abid:1\n" \
  "v_cvt_pk_bf16_f32 v203, v206, v207\n" \
  "s_cbranch_vccnz .Lrs_%=\n" \
  "s_waitcnt lgkmcnt(2)\n" \
  "v_smfmac_f32_16x16x64_bf16 v[208:211], v[232:235], v[216:223], v248 abid:1\n" \
  "v_permlane16_swap_b32 v200, v202\n" \
  "s_waitcnt lgkmcnt(0)\n" \
  "v_smfmac_f32_16x16x64_bf16 v[208:211], v[236:239], v[224:231], v248 abid:1\n" \
  "v_permlane16_swap_b32 v201, v203\n" \
  "buffer_store_dwordx4 v[200:203], v247, %[rs], %[vs] offen sc1\n" \
  "v_cvt_pk_bf16_f32 v212, v212, v213\n" \
  "v_cvt_pk_bf16_f32 v213, v214, v215\n" \
  "s_nop 4\n" \
  "v_cvt_pk_bf16_f32 v211, v210, v211\n" \
  "v_cvt_pk_bf16_f32 v210, v208, v209\n" \
  "s_nop 0\n" \
  "v_permlane16_swap_b32 v211, v213\n" \
  "v_permlane16_swap_b32 v210, v212\n" \
  "buffer_store_dwordx4 v[210:213], v246, %[rs], %[vs] offen sc1\n" \
  "s_nop 1\n"
#define VO_B \
  "s_branch .Lgo_%=\n" \
  ".Lsf_%=:\n" \
  "s_cmp_eq_u32 %[fs], -1\n" \
  "s_cbranch_scc1 .Lsfb_%=\n" \
  "v_mov_b32 v241, %[fs]\n" \
  "ds_write_b32 v251, v241 offset:23564\n" \
  "s_branch .Lrt_%=\n" \
  ".Lrs_%=:\n" \
  "s_mov_b32 %[fs], -1\n" \
  "s_nop 15\n" \
  "s_nop 15\n" \
  ".Lrt_%=:\n" \
  "s_setprio 0\n" \
  "s_waitcnt vmcnt(0) lgkmcnt(0)\n" \
  "buffer_load_dwordx4 v[216:219], v251, %[rs], %[o3] offen sc1\n" \
  "buffer_load_dwordx4 v[220:223], v251, %[rs], %[o0] offen sc1\n" \
  "buffer_load_dwordx4 v[224:227], v251, %[rs], %[o1] offen sc1\n" \
  "buffer_load_dwordx4 v[228:231], v251, %[rs], %[o2] offen sc1\n" \
  "v_subrev_u32 v232, 0x1000, v250\n" \
  "s_mov_b32 %[cnt], -2\n" \
  "ds_read_b128 v[200:203], v232 offset:0\n" \
  "ds_read_b128 v[204:207], v232 offset:4096\n" \
  "ds_read_b128 v[208:211], v232 offset:8192\n" \
  "ds_read_b128 v[212:215], v232 offset:12288\n" \
  "s_waitcnt lgkmcnt(0)\n" \
  "v_max_u32 v241, v200, v201\n" \
  "v_max3_u32 v241, v241, v202, v203\n" \
  "v_max3_u32 v241, v241, v204, v205\n" \
  "v_max3_u32 v241, v241, v206, v207\n" \
  "v_max3_u32 v241, v241, v208, v209\n" \
  "v_max3_u32 v241, v241, v210, v211\n" \
  "v_max3_u32 v241, v241, v212, v213\n" \
  "v_max3_u32 v241, v241, v214, v215\n" \
  "v_cmp_eq_u32 vcc, -1, v241\n" \
  "s_cbranch_vccz .Lrd_%=\n" \
  "s_mov_b32 %[cnt], 0x40000\n" \
  ".Lrr_%=:\n" \
  "s_waitcnt vmcnt(0)\n" \
  "v_max_u32 v241, v216, v217\n" \
  "v_max3_u32 v241, v241, v218, v219\n" \
  "v_max3_u32 v241, v241, v220, v221\n" \
  "v_max3_u32 v241, v241, v222, v223\n" \
  "v_max3_u32 v241, v241, v224, v225\n" \
  "v_max3_u32 v241, v241, v226, v227\n" \
  "v_max3_u32 v241, v241, v228, v229\n" \
  "v_max3_u32 v241, v241, v230, v231\n" \
  "v_cmp_eq_u32 vcc, -1, v241\n" \
  "s_cbranch_vccz .Lrd_%=\n" \
  "s_sleep 2\n" \
  "buffer_load_dwordx4 v[216:219], v251, %[rs], %[o3] offen sc1\n" \
  "buffer_load_dwordx4 v[220:223], v251, %[rs], %[o0] offen sc1\n" \
  "buffer_load_dwordx4 v[224:227], v251, %[rs], %[o1] offen sc1\n" \
  "buffer_load_dwordx4 v[228:231], v251, %[rs], %[o2] offen sc1\n" \
  "s_sub_u32 %[cnt], %[cnt], 1\n" \
  "s_cbranch_scc0 .Lrr_%=\n" \
  ".Lrd_%=:\n" \
  "ds_read_b128 v[200:203], v243 offset:32768\n" \
  "ds_read_b128 v[204:207], v243 offset:33792\n" \
  "ds_read_b128 v[208:211], v243 offset:49152\n" \
  "ds_read_b128 v[212:215], v243 offset:50176\n" \
  "v_mov_b64 v[192:193], 0\n" \
  "v_mov_b64 v[194:195], 0\n" \
  "v_mov_b64 v[196:197], 0\n" \
  "v_mov_b64 v[198:199], 0\n" \
  "s_cmp_eq_u32 %[fs], -1\n" \
  "s_cbranch_scc1 .Lre_%=\n" \
  "s_mov_b32 %[fs], -1\n" \
  "s_barrier\n" \
  ".Lre_%=:\n" \
  "s_waitcnt vmcnt(0)\n" \
  "s_cmp_eq_u32 %[cnt], -2\n" \
  "s_cbranch_scc1 .Lrf_%=\n" \
  "ds_write_b128 v232, v[216:219] offset:0\n" \
  "ds_write_b128 v232, v[220:223] offset:4096\n" \
  "ds_write_b128 v232, v[224:227] offset:8192\n" \
  "ds_write_b128 v232, v[228:231] offset:12288\n" \
  ".Lrf_%=:\n" \
  "v_mov_b32 v241, 0\n" \
  "s_waitcnt lgkmcnt(0)\n" \
  "s_barrier\n" \
  "s_waitcnt lgkmcnt(0)\n" \
  "s_barrier\n" \
  "s_branch .Ll1_%=\n" \
  ".Lnf_%=:\n" \
  "s_cmp_eq_u32 %[fs], -1\n" \
  "s_cbranch_scc1 .Lnfb_%=\n" \
  "v_mov_b32 v241, 0\n" \
  "ds_read_b128 v[216:219], v251 offset:0\n" \
  "s_waitcnt lgkmcnt(0)\n" \
  "v_max3_u32 v241, v241, v216, v217\n" \
  "v_max3_u32 v241, v241, v218, v219\n" \
  "ds_read_b128 v[216:219], v251 offset:1024\n" \
  "s_waitcnt lgkmcnt(0)\n" \
  "v_max3_u32 v241, v241, v216, v217\n" \
  "v_max3_u32 v241, v241, v218, v219\n" \
  "ds_read_b128 v[216:219], v251 offset:2048\n" \
  "s_waitcnt lgkmcnt(0)\n" \
  "v_max3_u32 v241, v241, v216, v217\n" \
  "v_max3_u32 v241, v241, v218, v219\n" \
  "ds_read_b128 v[216:219], v251 offset:3072\n" \
  "s_waitcnt lgkmcnt(0)\n" \
  "v_max3_u32 v241, v241, v216, v217\n" \
  "v_max3_u32 v241, v241, v218, v219\n" \
  "ds_read_b128 v[216:219], v251 offset:4096\n" \
  "s_waitcnt lgkmcnt(0)\n" \
  "v_max3_u32 v241, v241, v216, v217\n" \
  "v_max3_u32 v241, v241, v218, v219\n" \
  "ds_read_b128 v[216:219], v251 offset:5120\n" \
  "s_waitcnt lgkmcnt(0)\n" \
  "v_max3_u32 v241, v241, v216, v217\n" \
  "v_max3_u32 v241, v241, v218, v219\n" \
  "ds_read_b128 v[216:219], v251 offset:6144\n" \
  "s_waitcnt lgkmcnt(0)\n" \
  "v_max3_u32 v241, v241, v216, v217\n" \
  "v_max3_u32 v241, v241, v218, v219\n" \
  "ds_read_b128 v[216:219], v251 offset:7168\n" \
  "s_waitcnt lgkmcnt(0)\n" \
  "v_max3_u32 v241, v241, v216, v217\n" \
  "v_max3_u32 v241, v241, v218, v219\n" \
  "ds_read_b128 v[216:219], v251 offset:8192\n" \
  "s_waitcnt lgkmcnt(0)\n" \
  "v_max3_u32 v241, v241, v216, v217\n" \
  "v_max3_u32 v241, v241, v218, v219\n" \
  "ds_read_b128 v[216:219], v251 offset:9216\n" \
  "s_waitcnt lgkmcnt(0)\n" \
  "v_max3_u32 v241, v241, v216, v217\n" \
  "v_max3_u32 v241, v241, v218, v219\n" \
  "ds_read_b128 v[216:219], v251 offset:10240\n" \
  "s_waitcnt lgkmcnt(0)\n" \
  "v_max3_u32 v241, v241, v216, v217\n" \
  "v_max3_u32 v241, v241, v218, v219\n" \
  "ds_read_b128 v[216:219], v251 offset:11264\n" \
  "s_waitcnt lgkmcnt(0)\n" \
  "v_max3_u32 v241, v241, v216, v217\n" \
  "v_max3_u32 v241, v241, v218, v219\n" \
  "ds_read_b128 v[216:219], v251 offset:12288\n" \
  "s_waitcnt lgkmcnt(0)\n" \
  "v_max3_u32 v241, v241, v216, v217\n" \
  "v_max3_u32 v241, v241, v218, v219\n" \
  "ds_read_b128 v[216:219], v251 offset:13312\n" \
  "s_waitcnt lgkmcnt(0)\n" \
  "v_max3_u32 v241, v241, v216, v217\n" \
  "v_max3_u32 v241, v241, v218, v219\n" \
  "ds_read_b128 v[216:219], v251 offset:14336\n" \
  "s_waitcnt lgkmcnt(0)\n" \
  "v_max3_u32 v241, v241, v216, v217\n" \
  "v_max3_u32 v241, v241, v218, v219\n" \
  "ds_read_b128 v[216:219], v251 offset:15360\n" \
  "s_waitcnt lgkmcnt(0)\n" \
  "v_max3_u32 v241, v241, v216, v217\n" \
  "v_max3_u32 v241, v241, v218, v219\n" \
  "v_cmp_eq_u32 vcc, -1, v241\n" \
  "s_cbranch_vccz .Lnfb_%=\n" \
  "v_mov_b32 v241, %[fs]\n" \
  "ds_write_b32 v251, v241 offset:23560\n" \
  "s_waitcnt lgkmcnt(0)\n" \
  "s_mov_b32 %[fs], -1\n" \
  "s_barrier\n" \
  "s_branch .Lrt_%=\n" \
  ".p2align 6\n" \
  ".Lgo_%=:\n" \
  "ds_read_b128 v[200:203], v243 offset:32768\n" \
  "ds_read_b128 v[204:207], v243 offset:33792\n" \
  "ds_read_b128 v[208:211], v243 offset:49152\n" \
  "ds_read_b128 v[212:215], v243 offset:50176\n" \
  "v_mov_b64 v[192:193], 0\n" \
  "v_mov_b64 v[194:195], 0\n" \
  "v_mov_b64 v[196:197], 0\n" \
  "v_mov_b64 v[198:199], 0\n" \
  "s_mov_b32 %[keep], m0\n" \
  "v_readfirstlane_b32 %[cnt], v250\n" \
  "s_mov_b32 m0, %[cnt]\n" \
  "v_bfe_u32 v241, v251, 2, 7\n" \
  "v_lshl_add_u32 v241, v251, 4, v241\n" \
  "s_waitcnt lgkmcnt(0)\n" \
  "s_cmp_eq_u32 %[tw], 0\n" \
  "s_cbranch_scc1 .Ltq_%=\n" \
  ".Ltl_%=:\n" \
  "s_memtime vcc\n" \
  "s_waitcnt lgkmcnt(0)\n" \
  "s_sub_i32 %[cnt], %[tg], vcc_lo\n" \
  "s_cmp_lt_i32 %[cnt], 260\n" \
  "s_cbranch_scc1 .Ltf_%=\n" \
  "s_sleep 1\n" \
  "s_branch .Ltl_%=\n" \
  ".Ltf_%=:\n" \
  "s_max_i32 %[cnt], %[cnt], 0\n" \
  ".p2align 3\n" \
  ".Ltg_%=:\n" \
  "s_sub_u32 %[cnt], %[cnt], 28\n" \
  "s_cbranch_scc0 .Ltg_%=\n" \
  ".Ltq_%=:\n" \
  "s_barrier\n" \
  "buffer_load_dwordx4 v251, %[rs], %[o0] offen sc1 lds\n" \
  "s_add_u32 m0, m0, 0x1000\n" \
  "s_nop 4\n" \
  "buffer_load_dwordx4 v251, %[rs], %[o1] offen sc1 lds\n" \
  "s_add_u32 m0, m0, 0x1000\n" \
  "s_nop 4\n" \
  "buffer_load_dwordx4 v251, %[rs], %[o2] offen sc1 lds\n" \
  "s_barrier\n" \
  "s_waitcnt vmcnt(2)\n" \
  "s_barrier\n" \
  "s_mov_b32 m0, %[keep]\n" \
  ".Ll1_%=:\n" \
  "s_memtime vcc\n" \
  "s_setprio 3\n" \
  "s_waitcnt vmcnt(0)\n" \
  "s_waitcnt lgkmcnt(0)\n" \
  "s_barrier\n" \
  "ds_read2_b32 v[236:237], v241 offset1:32\n" \
  "ds_read_b128 v[216:219], v251 offset:8192\n" \
  "ds_read_b128 v[220:223], v251 offset:9216\n" \
  "ds_read_b128 v[224:227], v251 offset:10240\n" \
  "ds_read_b128 v[228:231], v251 offset:11264\n" \
  "ds_read_b128 v[232:235], v251 offset:12288\n" \
  "s_sub_u32 %[cnt], vcc_lo, %[ckm]\n" \
  "s_mul_hi_u32 %[cnt], %[cnt], %[inv]\n" \
  "s_add_u32 %[cnt], %[cnt], %[r16]\n" \
  "s_and_b32 %[cnt], %[cnt], -2\n" \
  "s_waitcnt lgkmcnt(5)\n" \
  "v_max_u32 v241, v236, v237\n" \
  "ds_read_b128 v[236:239], v251 offset:13312\n" \
  "v_cmp_eq_u32 vcc, -1, v241\n" \
  "s_cbranch_vccnz .Lsf_%=\n" \
  ".Lsfb_%=:\n" \
  "v_mov_b32 v241, %[cnt]\n" \
  "buffer_store_dword v241, v242, %[rs], %[sst] offen sc1\n" \
  "s_setprio 0\n" \
  "s_waitcnt lgkmcnt(5)\n" \
  "v_smfmac_f32_16x16x64_bf16 v[192:195], v[216:219], v[200:207], v248\n" \
  "v_smfmac_f32_16x16x64_bf16 v[196:199], v[216:219], v[208:215], v248\n" \
  "s_waitcnt lgkmcnt(4)\n" \
  "v_smfmac_f32_16x16x64_bf16 v[192:195], v[220:223], v[0:7], v248\n" \
  "v_smfmac_f32_16x16x64_bf16 v[196:199], v[220:223], v[8:15], v248\n" \
  "ds_read_b128 v[216:219], v251 offset:14336\n" \
  "ds_read_b128 v[220:223], v251 offset:15360\n" \
  "s_waitcnt lgkmcnt(5)\n" \
  "v_smfmac_f32_16x16x64_bf16 v[192:195], v[224:227], v[16:23], v248\n" \
  "v_smfmac_f32_16x16x64_bf16 v[196:199], v[224:227], v[24:31], v248\n" \
  "s_waitcnt lgkmcnt(4)\n" \
  "v_smfmac_f32_16x16x64_bf16 v[192:195], v[228:231], v[32:39], v248\n" \
  "v_smfmac_f32_16x16x64_bf16 v[196:199], v[228:231], v[40:47], v248\n" \
  "s_waitcnt lgkmcnt(3)\n" \
  "v_smfmac_f32_16x16x64_bf16 v[192:195], v[232:235], v[48:55], v248\n" \
  "v_smfmac_f32_16x16x64_bf16 v[196:199], v[232:235], v[56:63], v248\n" \
  "s_waitcnt lgkmcnt(2)\n" \
  "v_smfmac_f32_16x16x64_bf16 v[192:195], v[236:239], v[64:71], v248\n" \
  "v_smfmac_f32_16x16x64_bf16 v[196:199], v[236:239], v[72:79], v248\n" \
  "s_barrier\n" \
  "ds_read_b128 v[224:227], v250 offset:20480\n" \
  "s_waitcnt lgkmcnt(2)\n" \
  "v_smfmac_f32_16x16x64_bf16 v[192:195], v[216:219], v[80:87], v248\n" \
  "s_waitcnt lgkmcnt(1)\n" \
  "v_smfmac_f32_16x16x64_bf16 v[192:195], v[220:223], v[96:103], v248\n" \
  "v_smfmac_f32_16x16x64_bf16 v[196:199], v[216:219], v[88:95], v248\n" \
  "v_smfmac_f32_16x16x64_bf16 v[196:199], v[220:223], v[104:111], v248\n" \
  "s_nop 0\n" \
  "v_add_f32 v192, v192, v194\n" \
  "v_add_f32 v193, v193, v195\n" \
  "s_waitcnt lgkmcnt(0)\n" \
  "v_add_f32 v192, v192, v224\n" \
  "v_add_f32 v193, v193, v225\n" \
  "v_cvt_pk_bf16_f32 v194, v192, v193\n" \
  "s_nop 1\n" \
  "v_pk_add_f32 v[196:197], v[196:197], v[198:199]\n" \
  "v_pk_add_f32 v[196:197], v[196:197], v[226:227]\n" \
  "v_cvt_pk_bf16_f32 v198, v196, v197\n" \
  "ds_write2_b32 v244, v194, v198 offset1:96\n" \
  "v_cmp_u_f32 vcc, v192, v193\n" \
  "s_cbranch_vccnz .Lnf_%=\n" \
  ".Lnfb_%=:\n" \
  "s_waitcnt lgkmcnt(0)\n" \
  "s_barrier\n" \
  "v_mov_b64 v[200:201], 0\n" \
  "v_mov_b64 v[202:203], 0\n" \
  "v_mov_b64 v[204:205], 0\n" \
  "v_mov_b64 v[206:207], 0\n" \
  "v_mov_b64 v[208:209], 0\n" \
  "v_mov_b64 v[210:211], 0\n" \
  "v_mov_b64 v[212:213], 0\n" \
  "v_mov_b64 v[214:215], 0\n" \
  "ds_read_b64_tr_b16 v[232:233], v245 offset:17920\n" \
  "ds_read_b64_tr_b16 v[234:235], v245 offset:18112\n" \
  "ds_read_b64_tr_b16 v[236:237], v245 offset:18688\n" \
  "ds_read_b64_tr_b16 v[238:239], v245 offset:18880\n" \
  "ds_read_b64_tr_b16 v[192:193], v245 offset:16384\n" \
  "ds_read_b64_tr_b16 v[194:195], v245 offset:16576\n" \
  "ds_read_b64_tr_b16 v[196:197], v245 offset:17152\n" \
  "ds_read_b64_tr_b16 v[198:199], v245 offset:17344\n" \
  "ds_read_b128 v[216:219], v249 offset:0\n" \
  "ds_read_b128 v[220:223], v249 offset:1024\n" \
  "ds_read_b128 v[224:227], v249 offset:16384\n" \
  "ds_read_b128 v[228:231], v249 offset:17408\n" \
  "buffer_load_dwordx2 v[240:241], v240, %[rs], %[ss] offen sc1\n" \
  "s_waitcnt lgkmcnt(2)\n" \
  "v_smfmac_f32_16x16x64_bf16 v[200:203], v[232:235], v[216:223], v248 abid:1\n" \
  "ds_read_b128 v[216:219], v249 offset:32768\n" \
  "ds_read_b128 v[220:223], v249 offset:33792\n" \
  "v_smfmac_f32_16x16x64_bf16 v[204:207], v[232:235], v[128:135], v248 abid:1\n" \
  "s_waitcnt lgkmcnt(2)\n" \
  "v_smfmac_f32_16x16x64_bf16 v[200:203], v[236:239], v[224:231], v248 abid:1\n" \
  "ds_read_b128 v[224:227], v249 offset:49152\n" \
  "ds_read_b128 v[228:231], v249 offset:50176\n" \
  "v_smfmac_f32_16x16x64_bf16 v[204:207], v[236:239], v[136:143], v248 abid:1\n" \
  "v_smfmac_f32_16x16x64_bf16 v[200:203], v[192:195], v[176:183], v248 abid:1\n" \
  "v_smfmac_f32_16x16x64_bf16 v[204:207], v[192:195], v[112:119], v248 abid:1\n" \
  "v_smfmac_f32_16x16x64_bf16 v[200:203], v[196:199], v[184:191], v248 abid:1\n" \
  "v_smfmac_f32_16x16x64_bf16 v[204:207], v[196:199], v[120:127], v248 abid:1\n" \
  "s_waitcnt lgkmcnt(2)\n" \
  "v_smfmac_f32_16x16x64_bf16 v[208:211], v[192:195], v[216:223], v248 abid:1\n" \
  "ds_read_b128 v[216:219], v243 offset:0\n" \
  "ds_read_b128 v[220:223], v243 offset:1024\n" \
  "v_smfmac_f32_16x16x64_bf16 v[212:215], v[192:195], v[144:151], v248 abid:1\n" \
  "s_waitcnt lgkmcnt(2)\n" \
  "v_smfmac_f32_16x16x64_bf16 v[208:211], v[196:199], v[224:231], v248 abid:1\n" \
  "ds_read_b128 v[224:227], v243 offset:16384\n" \
  "ds_read_b128 v[228:231], v243 offset:17408\n" \
  "v_cvt_pk_bf16_f32 v200, v200, v201\n" \
  "v_smfmac_f32_16x16x64_bf16 v[212:215], v[196:199], v[152:159], v248 abid:1\n" \
  "v_cvt_pk_bf16_f32 v201, v202, v203\n" \
  "v_smfmac_f32_16x16x64_bf16 v[212:215], v[232:235], v[160:167], v248 abid:1\n" \
  "v_cvt_pk_bf16_f32 v202, v204, v205\n" \
  "v_smfmac_f32_16x16x64_bf16 v[212:215], v[236:239], v[168:175], v248 abid:1\n" \
  "v_cvt_pk_bf16_f32 v203, v206, v207\n" \
  "s_waitcnt lgkmcnt(2)\n" \
  "v_smfmac_f32_16x16x64_bf16 v[208:211], v[232:235], v[216:223], v248 abid:1\n" \
  "v_permlane16_swap_b32 v200, v202\n" \
  "s_waitcnt lgkmcnt(0)\n" \
  "v_smfmac_f32_16x16x64_bf16 v[208:211], v[236:239], v[224:231], v248 abid:1\n" \
  "v_permlane16_swap_b32 v201, v203\n" \
  "buffer_store_dwordx4 v[200:203], v247, %[rs], %[vs] offen sc1\n" \
  "v_cvt_pk_bf16_f32 v212, v212, v213\n" \
  "v_cvt_pk_bf16_f32 v213, v214, v215\n" \
  "s_nop 4\n" \
  "v_cvt_pk_bf16_f32 v211, v210, v211\n" \
  "v_cvt_pk_bf16_f32 v210, v208, v209\n" \
  "s_nop 0\n" \
  "v_permlane16_swap_b32 v211, v213\n" \
  "v_permlane16_swap_b32 v210, v212\n" \
  "buffer_store_dwordx4 v[210:213], v246, %[rs], %[vs] offen sc1\n" \
  "s_nop 1\n" \
  "s_waitcnt vmcnt(2)\n"
#define QK_A \
  "s_branch .Lgo_%=\n" \
  ".Lrs_%=:\n" \
  "s_mov_b32 %[fs], -1\n" \
  "s_nop 15\n" \
  "s_nop 15\n" \
  ".Lrt_%=:\n" \
  "s_setprio 0\n" \
  "s_waitcnt vmcnt(0) lgkmcnt(0)\n" \
  "ds_read_b128 v[200:203], v249 offset:0\n" \
  "ds_read_b128 v[204:207], v249 offset:1024\n" \
  "ds_read_b128 v[208:211], v249 offset:16384\n" \
  "ds_read_b128 v[212:215], v249 offset:17408\n" \
  "v_mov_b32 v241, 0x7f800000\n" \
  "ds_write_b32 v250, v241 offset:22528\n" \
  "v_mov_b64 v[192:193], 0\n" \
  "v_mov_b64 v[194:195], 0\n" \
  "v_mov_b64 v[196:197], 0\n" \
  "v_mov_b64 v[198:199], 0\n" \
  "s_barrier\n" \
  "ds_read_b128 v[216:219], v251 offset:0\n" \
  "ds_read_b128 v[220:223], v251 offset:1024\n" \
  "ds_read_b128 v[224:227], v251 offset:2048\n" \
  "ds_read_b128 v[228:231], v251 offset:3072\n" \
  "s_waitcnt lgkmcnt(0)\n" \
  "s_barrier\n" \
  "s_branch .Ll1_%=\n" \
  ".Lnf_%=:\n" \
  "s_cmp_eq_u32 %[fs], -1\n" \
  "s_cbranch_scc1 .Lnfb_%=\n" \
  "v_mov_b32 v241, %[fs]\n" \
  "ds_write_b32 v251, v241 offset:23560\n" \
  "s_waitcnt lgkmcnt(0)\n" \
  "s_mov_b32 %[fs], -1\n" \
  "s_barrier\n" \
  "s_branch .Lrt_%=\n" \
  ".Lw1_%=:\n" \
  "ds_read_b32 v238, v251 offset:23560\n" \
  "s_waitcnt lgkmcnt(0)\n" \
  "v_cmp_eq_u32 vcc, %[fs], v238\n" \
  "s_cbranch_vccnz .Lrs_%=\n" \
  "v_xor_b32 v238, 0x1000, v242\n" \
  "ds_read_b128 v[216:219], v238 offset:0\n" \
  "ds_read_b128 v[228:231], v238 offset:1024\n" \
  "ds_read_b128 v[232:235], v238 offset:2048\n" \
  "ds_read_b128 v[200:203], v238 offset:3072\n" \
  "s_waitcnt lgkmcnt(0)\n" \
  "v_mfma_f32_16x16x32_bf16 v[216:219], v[216:219], v[192:195], 0\n" \
  "v_mfma_f32_16x16x32_bf16 v[216:219], v[228:231], v[196:199], v[216:219]\n" \
  "v_mfma_f32_16x16x32_bf16 v[216:219], v[232:235], v[208:211], v[216:219]\n" \
  "v_mfma_f32_16x16x32_bf16 v[216:219], v[200:203], v[212:215], v[216:219]\n" \
  "s_nop 9\n" \
  "v_cndmask_b32 v238, v216, v218, %[hf]\n" \
  "v_cndmask_b32 v239, v217, v219, %[hf]\n" \
  "v_max3_f32 v222, |v238|, |v239|, v221\n" \
  "v_cmp_nge_f32 vcc, 0x43aa0000, v222\n" \
  "s_cbranch_vccz .Lw1b_%=\n" \
  "s_mov_b64 %[wd], -1\n" \
  "v_max3_f32 v222, v204, v205, v238\n" \
  "v_max_f32 v222, v222, v239\n" \
  "s_nop 1\n" \
  "v_max_f32_dpp v222, v222, v222 row_ror:8 row_mask:0xf bank_mask:0xf\n" \
  "v_mov_b32 v221, v222\n" \
  "s_nop 1\n" \
  "v_permlane32_swap_b32 v222, v221\n" \
  "v_max_f32 v222, v222, v221\n" \
  "v_mov_b32 v221, v222\n" \
  "s_nop 1\n" \
  "v_permlane16_swap_b32 v222, v221\n" \
  "v_max_f32 v222, v222, v221\n" \
  "v_mul_f32 v221, 0xbe0293ee, v222\n" \
  "v_fmamk_f32 v236, v204, 0x3e0293ee, v221\n" \
  "v_fmamk_f32 v237, v205, 0x3e0293ee, v221\n" \
  "v_exp_f32 v236, v236\n" \
  "v_exp_f32 v237, v237\n" \
  "s_nop 1\n" \
  "v_cvt_pk_bf16_f32 v220, v236, v237\n" \
  "v_and_b32 v220, 0x7fffffff, v220\n" \
  "s_branch .Lw1b_%=\n" \
  ".Lw2_%=:\n" \
  "v_max3_f32 v206, v204, v205, v222\n" \
  "s_nop 1\n" \
  "v_max_f32_dpp v206, v206, v206 row_ror:8 row_mask:0xf bank_mask:0xf\n" \
  "v_mov_b32 v221, v206\n" \
  "s_nop 1\n" \
  "v_permlane32_swap_b32 v206, v221\n" \
  "v_max_f32 v206, v206, v221\n" \
  "v_mov_b32 v221, v206\n" \
  "s_nop 1\n" \
  "v_permlane16_swap_b32 v206, v221\n" \
  "v_max_f32 v206, v206, v221\n" \
  "v_mul_f32 v221, 0xbe0293ee, v206\n" \
  "v_fmamk_f32 v236, v204, 0x3e0293ee, v221\n" \
  "v_fmamk_f32 v237, v205, 0x3e0293ee, v221\n" \
  "v_fmamk_f32 v239, v222, 0x3e0293ee, v221\n" \
  "v_exp_f32 v236, v236\n" \
  "v_exp_f32 v237, v237\n" \
  "v_exp_f32 v239, v239\n" \
  "s_nop 1\n" \
  "v_fma_f32 v238, v223, v239, v236\n" \
  "v_add_f32 v238, v238, v237\n" \
  "s_nop 1\n" \
  "s_branch .Lw2b_%=\n" \
  ".p2align 6\n" \
  ".Lgo_%=:\n" \
  "ds_read_b128 v[200:203], v249 offset:0\n" \
  "ds_read_b128 v[204:207], v249 offset:1024\n" \
  "ds_read_b128 v[208:211], v249 offset:16384\n" \
  "ds_read_b128 v[212:215], v249 offset:17408\n" \
  "v_mov_b32 v241, 0x7f800000\n" \
  "ds_write_b32 v250, v241 offset:22528\n" \
  "v_mov_b64 v[192:193], 0\n" \
  "v_mov_b64 v[194:195], 0\n" \
  "v_mov_b64 v[196:197], 0\n" \
  "v_mov_b64 v[198:199], 0\n" \
  "s_mov_b32 %[keep], m0\n" \
  "v_readfirstlane_b32 %[cnt], v250\n" \
  "s_mov_b32 m0, %[cnt]\n" \
  "s_waitcnt lgkmcnt(0)\n" \
  "s_barrier\n" \
  "buffer_load_dwordx4 v251, %[rs], %[o0] offen sc1 lds\n" \
  "s_waitcnt vmcnt(0)\n" \
  "s_barrier\n" \
  "s_mov_b32 m0, %[keep]\n" \
  "ds_read_b128 v[216:219], v251 offset:0\n" \
  "ds_read_b128 v[220:223], v251 offset:1024\n" \
  "ds_read_b128 v[224:227], v251 offset:2048\n" \
  "ds_read_b128 v[228:231], v251 offset:3072\n" \
  "s_barrier\n" \
  ".Ll1_%=:\n" \
  "s_waitcnt lgkmcnt(3)\n" \
  "v_smfmac_f32_16x16x64_bf16 v[192:195], v[216:219], v[200:207], v248\n" \
  "ds_read_b128 v[200:203], v249 offset:32768\n" \
  "ds_read_b128 v[204:207], v249 offset:33792\n" \
  "s_waitcnt lgkmcnt(4)\n" \
  "v_smfmac_f32_16x16x64_bf16 v[192:195], v[220:223], v[0:7], v248\n" \
  "ds_read_b128 v[232:235], v251 offset:4096\n" \
  "v_smfmac_f32_16x16x64_bf16 v[196:199], v[216:219], v[128:135], v248\n" \
  "ds_read_b128 v[216:219], v251 offset:5120\n" \
  "s_waitcnt lgkmcnt(5)\n" \
  "v_smfmac_f32_16x16x64_bf16 v[192:195], v[224:227], v[208:215], v248\n" \
  "ds_read_b128 v[208:211], v249 offset:49152\n" \
  "v_smfmac_f32_16x16x64_bf16 v[196:199], v[220:223], v[64:71], v248\n" \
  "ds_read_b128 v[212:215], v249 offset:50176\n" \
  "s_waitcnt lgkmcnt(6)\n" \
  "v_smfmac_f32_16x16x64_bf16 v[192:195], v[228:231], v[8:15], v248\n" \
  "ds_read_b128 v[220:223], v251 offset:6144\n" \
  "v_smfmac_f32_16x16x64_bf16 v[196:199], v[224:227], v[136:143], v248\n" \
  "ds_read_b128 v[224:227], v251 offset:7168\n" \
  "s_waitcnt lgkmcnt(5)\n" \
  "v_smfmac_f32_16x16x64_bf16 v[192:195], v[232:235], v[200:207], v248\n" \
  "ds_read_b128 v[200:203], v243 offset:0\n" \
  "s_barrier\n" \
  "v_smfmac_f32_16x16x64_bf16 v[196:199], v[228:231], v[72:79], v248\n" \
  "ds_read_b128 v[204:207], v243 offset:1024\n" \
  "s_waitcnt lgkmcnt(6)\n" \
  "v_smfmac_f32_16x16x64_bf16 v[192:195], v[216:219], v[16:23], v248\n" \
  "ds_read_b128 v[228:231], v251 offset:8192\n" \
  "v_smfmac_f32_16x16x64_bf16 v[196:199], v[232:235], v[144:151], v248\n" \
  "ds_read_b128 v[232:235], v251 offset:9216\n" \
  "s_waitcnt lgkmcnt(5)\n" \
  "v_smfmac_f32_16x16x64_bf16 v[192:195], v[220:223], v[208:215], v248\n" \
  "ds_read_b128 v[208:211], v243 offset:16384\n" \
  "v_smfmac_f32_16x16x64_bf16 v[196:199], v[216:219], v[80:87], v248\n" \
  "ds_read_b128 v[212:215], v243 offset:17408\n" \
  "s_waitcnt lgkmcnt(6)\n" \
  "v_smfmac_f32_16x16x64_bf16 v[192:195], v[224:227], v[24:31], v248\n" \
  "ds_read_b128 v[216:219], v251 offset:10240\n" \
  "v_smfmac_f32_16x16x64_bf16 v[196:199], v[220:223], v[152:159], v248\n" \
  "ds_read_b128 v[220:223], v251 offset:11264\n" \
  "s_waitcnt lgkmcnt(5)\n" \
  "v_smfmac_f32_16x16x64_bf16 v[192:195], v[228:231], v[200:207], v248\n" \
  "ds_read_b128 v[200:203], v243 offset:32768\n" \
  "v_smfmac_f32_16x16x64_bf16 v[196:199], v[224:227], v[88:95], v248\n" \
  "ds_read_b128 v[204:207], v243 offset:33792\n" \
  "s_waitcnt lgkmcnt(6)\n" \
  "v_smfmac_f32_16x16x64_bf16 v[192:195], v[232:235], v[32:39], v248\n" \
  "ds_read_b128 v[224:227], v251 offset:12288\n" \
  "v_smfmac_f32_16x16x64_bf16 v[196:199], v[228:231], v[160:167], v248\n" \
  "ds_read_b128 v[228:231], v251 offset:13312\n" \
  "s_waitcnt lgkmcnt(5)\n" \
  "v_smfmac_f32_16x16x64_bf16 v[192:195], v[216:219], v[208:215], v248\n" \
  "ds_read_b128 v[208:211], v243 offset:49152\n" \
  "ds_read_b128 v[212:215], v243 offset:50176\n" \
  "v_smfmac_f32_16x16x64_bf16 v[196:199], v[232:235], v[96:103], v248\n" \
  "ds_read_b128 v[236:239], v242 offset:2048\n" \
  "s_waitcnt lgkmcnt(7)\n" \
  "v_smfmac_f32_16x16x64_bf16 v[192:195], v[220:223], v[40:47], v248\n" \
  "ds_read_b128 v[232:235], v251 offset:14336\n" \
  "v_smfmac_f32_16x16x64_bf16 v[196:199], v[216:219], v[168:175], v248\n" \
  "ds_read_b128 v[216:219], v251 offset:15360\n" \
  "s_waitcnt lgkmcnt(6)\n" \
  "v_smfmac_f32_16x16x64_bf16 v[192:195], v[224:227], v[200:207], v248\n" \
  "ds_read_b128 v[200:203], v242 offset:0\n" \
  "v_smfmac_f32_16x16x64_bf16 v[196:199], v[220:223], v[104:111], v248\n" \
  "ds_read_b128 v[204:207], v242 offset:1024\n" \
  "s_waitcnt lgkmcnt(7)\n" \
  "v_smfmac_f32_16x16x64_bf16 v[192:195], v[228:231], v[48:55], v248\n" \
  "ds_read_b128 v[220:223], v242 offset:3072\n" \
  "v_smfmac_f32_16x16x64_bf16 v[196:199], v[224:227], v[176:183], v248\n" \
  "s_waitcnt lgkmcnt(4)\n" \
  "v_smfmac_f32_16x16x64_bf16 v[192:195], v[232:235], v[208:215], v248\n" \
  "s_waitcnt lgkmcnt(3)\n" \
  "v_smfmac_f32_16x16x64_bf16 v[192:195], v[216:219], v[56:63], v248\n" \
  "v_smfmac_f32_16x16x64_bf16 v[196:199], v[228:231], v[112:119], v248\n" \
  "v_smfmac_f32_16x16x64_bf16 v[196:199], v[232:235], v[184:191], v248\n" \
  "s_nop 0\n" \
  "v_add_f32 v192, v192, v194\n" \
  "v_smfmac_f32_16x16x64_bf16 v[196:199], v[216:219], v[120:127], v248\n" \
  "v_add_f32 v193, v193, v195\n" \
  "v_cvt_pk_bf16_f32 v194, v192, v193\n" \
  "s_nop 6\n" \
  "v_add_f32 v196, v196, v198\n" \
  "v_add_f32 v197, v197, v199\n" \
  "v_cvt_pk_bf16_f32 v198, v196, v197\n" \
  "ds_write2_b32 v247, v194, v198 offset1:96\n" \
  "v_cmp_u_f32 vcc, v192, v193\n" \
  "s_cbranch_vccnz .Lnf_%=\n" \
  ".Lnfb_%=:\n" \
  "v_xor_b32 v241, 0x400, v250\n" \
  "s_waitcnt lgkmcnt(0)\n" \
  "s_barrier\n" \
  "ds_read_b64_tr_b16 v[192:193], v246 offset:16384\n" \
  "ds_read_b64_tr_b16 v[194:195], v246 offset:16768\n" \
  "ds_read_b64_tr_b16 v[196:197], v246 offset:17152\n" \
  "ds_read_b64_tr_b16 v[198:199], v246 offset:17536\n" \
  "ds_read_b64_tr_b16 v[208:209], v246 offset:17920\n" \
  "ds_read_b64_tr_b16 v[210:211], v246 offset:18304\n" \
  "ds_read_b64_tr_b16 v[212:213], v246 offset:18688\n" \
  "ds_read_b64_tr_b16 v[214:215], v246 offset:19072\n" \
  "s_waitcnt lgkmcnt(6)\n" \
  "v_mfma_f32_16x16x32_bf16 v[200:203], v[200:203], v[192:195], 0\n" \
  "s_waitcnt lgkmcnt(4)\n" \
  "v_mfma_f32_16x16x32_bf16 v[200:203], v[204:207], v[196:199], v[200:203]\n" \
  "s_waitcnt lgkmcnt(2)\n" \
  "v_mfma_f32_16x16x32_bf16 v[200:203], v[236:239], v[208:211], v[200:203]\n" \
  "s_waitcnt lgkmcnt(0)\n" \
  "v_mfma_f32_16x16x32_bf16 v[200:203], v[220:223], v[212:215], v[200:203]\n" \
  "s_nop 8\n" \
  "v_cndmask_b32 v204, v200, v202, %[hf]\n" \
  "v_cndmask_b32 v205, v201, v203, %[hf]\n" \
  "v_max_f32 v221, |v204|, |v205|\n" \
  "ds_write_b32 v250, v221 offset:22528\n" \
  "v_mul_f32 v236, 0x3e0293ee, v204\n" \
  "v_mul_f32 v237, 0x3e0293ee, v205\n" \
  "v_exp_f32 v236, v236\n" \
  "v_exp_f32 v237, v237\n" \
  "ds_read_b32 v238, v241 offset:22528\n" \
  "v_cvt_pk_bf16_f32 v220, v236, v237\n" \
  "s_mov_b64 %[wd], 0\n" \
  "s_waitcnt lgkmcnt(0)\n" \
  "v_max_f32 v238, v238, v221\n" \
  "v_cmp_nge_f32 vcc, 0x43aa0000, v238\n" \
  "s_cbranch_vccnz .Lw1_%=\n" \
  ".Lw1b_%=:\n" \
  "buffer_store_dword v220, v245, %[rs], %[c0] offen sc1\n" \
  "buffer_store_dword v220, v245, %[rs], %[c1] offen sc1\n" \
  "buffer_store_dword v220, v245, %[rs], %[c2] offen sc1\n" \
  "buffer_store_dword v220, v245, %[rs], %[c3] offen sc1\n" \
  "v_add_f32 v223, v236, v237\n" \
  "ds_write_b32 v250, v223 offset:22532\n" \
  "v_cndmask_b32 v222, 0, v222, %[wd]\n" \
  "v_mov_b32 v239, 1.0\n" \
  "s_waitcnt lgkmcnt(0)\n" \
  "s_barrier\n" \
  "ds_read_b64_tr_b16 v[216:217], v246 offset:19456\n" \
  "ds_read_b64_tr_b16 v[218:219], v246 offset:19840\n" \
  "ds_read_b64_tr_b16 v[228:229], v246 offset:20224\n" \
  "ds_read_b64_tr_b16 v[230:231], v246 offset:20608\n" \
  "ds_read_b64_tr_b16 v[232:233], v246 offset:20992\n" \
  "ds_read_b64_tr_b16 v[234:235], v246 offset:21376\n" \
  "ds_read_b64_tr_b16 v[200:201], v246 offset:21760\n" \
  "ds_read_b64_tr_b16 v[202:203], v246 offset:22144\n" \
  "ds_read_b32 v238, v241 offset:22532\n" \
  "s_waitcnt lgkmcnt(7)\n" \
  "v_mfma_f32_16x16x32_bf16 v[216:219], v[216:219], v[192:195], 0\n" \
  "s_waitcnt lgkmcnt(5)\n" \
  "v_mfma_f32_16x16x32_bf16 v[216:219], v[228:231], v[196:199], v[216:219]\n" \
  "s_waitcnt lgkmcnt(3)\n" \
  "v_mfma_f32_16x16x32_bf16 v[216:219], v[232:235], v[208:211], v[216:219]\n" \
  "s_waitcnt lgkmcnt(1)\n" \
  "v_mfma_f32_16x16x32_bf16 v[216:219], v[200:203], v[212:215], v[216:219]\n" \
  "s_waitcnt lgkmcnt(0)\n" \
  "v_add_f32 v223, v223, v238\n" \
  "v_mov_b32 v238, v223\n" \
  "s_nop 1\n" \
  "v_permlane32_swap_b32 v223, v238\n" \
  "v_add_f32 v223, v223, v238\n" \
  "s_nop 1\n" \
  "v_cndmask_b32 v204, v216, v218, %[hf]\n" \
  "v_cndmask_b32 v205, v217, v219, %[hf]\n" \
  "v_mul_f32 v236, 0x3e0293ee, v204\n" \
  "v_mul_f32 v237, 0x3e0293ee, v205\n" \
  "v_exp_f32 v236, v236\n" \
  "v_exp_f32 v237, v237\n" \
  "v_max_f32 v221, |v204|, |v205|\n" \
  "v_add_f32 v238, v223, v236\n" \
  "v_cmp_nge_f32 vcc, 0x43aa0000, v221\n" \
  "v_add_f32 v238, v238, v237\n" \
  "s_or_b64 vcc, vcc, %[wd]\n" \
  "s_cbranch_scc1 .Lw2_%=\n" \
  ".Lw2b_%=:\n" \
  "v_add_f32_dpp v238, v238, v238 row_ror:8 row_mask:0xf bank_mask:0xf\n" \
  "v_mov_b32 v221, v238\n" \
  "v_cndmask_b32 v236, v239, v236, %[up]\n" \
  "v_cndmask_b32 v237, v239, v237, %[up]\n" \
  "v_permlane16_swap_b32 v238, v221\n" \
  "v_add_f32 v238, v238, v221\n" \
  "v_rcp_f32 v238, v238\n" \
  "s_nop 0\n" \
  "v_mul_f32 v236, v236, v238\n" \
  "v_mul_f32 v237, v237, v238\n" \
  "v_cvt_pk_bf16_f32 v220, v236, v237\n" \
  "v_and_b32 v220, v220, v240\n" \
  "buffer_store_dword v220, v244, %[rs], %[c0] offen sc1\n" \
  "buffer_store_dword v220, v244, %[rs], %[c1] offen sc1\n"
#define QK_X \
  "s_branch .Lgo_%=\n" \
  ".Lrs_%=:\n" \
  "s_mov_b32 %[fs], -1\n" \
  "s_nop 15\n" \
  "s_nop 15\n" \
  ".Lrt_%=:\n" \
  "s_setprio 0\n" \
  "s_waitcnt vmcnt(0) lgkmcnt(0)\n" \
  "ds_read_b128 v[200:203], v249 offset:0\n" \
  "ds_read_b128 v[204:207], v249 offset:1024\n" \
  "ds_read_b128 v[208:211], v249 offset:16384\n" \
  "ds_read_b128 v[212:215], v249 offset:17408\n" \
  "v_mov_b64 v[192:193], 0\n" \
  "v_mov_b64 v[194:195], 0\n" \
  "v_mov_b64 v[196:197], 0\n" \
  "v_mov_b64 v[198:199], 0\n" \
  "s_barrier\n" \
  "ds_read_b128 v[216:219], v251 offset:0\n" \
  "ds_read_b128 v[220:223], v251 offset:1024\n" \
  "ds_read_b128 v[224:227], v251 offset:2048\n" \
  "ds_read_b128 v[228:231], v251 offset:3072\n" \
  "s_waitcnt lgkmcnt(0)\n" \
  "s_barrier\n" \
  "s_branch .Ll1_%=\n" \
  ".Lnf_%=:\n" \
  "s_cmp_eq_u32 %[fs], -1\n" \
  "s_cbranch_scc1 .Lnfb_%=\n" \
  "v_mov_b32 v241, %[fs]\n" \
  "ds_write_b32 v251, v241 offset:23560\n" \
  "s_waitcnt lgkmcnt(0)\n" \
  "s_mov_b32 %[fs], -1\n" \
  "s_barrier\n" \
  "s_branch .Lrt_%=\n" \
  ".p2align 6\n" \
  ".Lgo_%=:\n" \
  "ds_read_b128 v[200:203], v249 offset:0\n" \
  "ds_read_b128 v[204:207], v249 offset:1024\n" \
  "ds_read_b128 v[208:211], v249 offset:16384\n" \
  "ds_read_b128 v[212:215], v249 offset:17408\n" \
  "v_mov_b64 v[192:193], 0\n" \
  "v_mov_b64 v[194:195], 0\n" \
  "v_mov_b64 v[196:197], 0\n" \
  "v_mov_b64 v[198:199], 0\n" \
  "s_mov_b32 %[keep], m0\n" \
  "v_readfirstlane_b32 %[cnt], v250\n" \
  "s_mov_b32 m0, %[cnt]\n" \
  "s_waitcnt lgkmcnt(0)\n" \
  "s_barrier\n" \
  "buffer_load_dwordx4 v251, %[rs], %[o0] offen sc1 lds\n" \
  "s_waitcnt vmcnt(0)\n" \
  "s_barrier\n" \
  "s_mov_b32 m0, %[keep]\n" \
  "ds_read_b128 v[216:219], v251 offset:0\n" \
  "ds_read_b128 v[220:223], v251 offset:1024\n" \
  "ds_read_b128 v[224:227], v251 offset:2048\n" \
  "ds_read_b128 v[228:231], v251 offset:3072\n" \
  "s_barrier\n" \
  ".Ll1_%=:\n" \
  "s_waitcnt lgkmcnt(3)\n" \
  "v_smfmac_f32_16x16x64_bf16 v[192:195], v[216:219], v[200:207], v248\n" \
  "ds_read_b128 v[200:203], v249 offset:32768\n" \
  "ds_read_b128 v[204:207], v249 offset:33792\n" \
  "s_waitcnt lgkmcnt(4)\n" \
  "v_smfmac_f32_16x16x64_bf16 v[192:195], v[220:223], v[0:7], v248\n" \
  "ds_read_b128 v[232:235], v251 offset:4096\n" \
  "v_smfmac_f32_16x16x64_bf16 v[196:199], v[216:219], v[128:135], v248\n" \
  "ds_read_b128 v[216:219], v251 offset:5120\n" \
  "s_waitcnt lgkmcnt(5)\n" \
  "v_smfmac_f32_16x16x64_bf16 v[192:195], v[224:227], v[208:215], v248\n" \
  "ds_read_b128 v[208:211], v249 offset:49152\n" \
  "v_smfmac_f32_16x16x64_bf16 v[196:199], v[220:223], v[64:71], v248\n" \
  "ds_read_b128 v[212:215], v249 offset:50176\n" \
  "s_waitcnt lgkmcnt(6)\n" \
  "v_smfmac_f32_16x16x64_bf16 v[192:195], v[228:231], v[8:15], v248\n" \
  "ds_read_b128 v[220:223], v251 offset:6144\n" \
  "v_smfmac_f32_16x16x64_bf16 v[196:199], v[224:227], v[136:143], v248\n" \
  "ds_read_b128 v[224:227], v251 offset:7168\n" \
  "s_waitcnt lgkmcnt(5)\n" \
  "v_smfmac_f32_16x16x64_bf16 v[192:195], v[232:235], v[200:207], v248\n" \
  "ds_read_b128 v[200:203], v243 offset:0\n" \
  "s_barrier\n" \
  "v_smfmac_f32_16x16x64_bf16 v[196:199], v[228:231], v[72:79], v248\n" \
  "ds_read_b128 v[204:207], v243 offset:1024\n" \
  "s_waitcnt lgkmcnt(6)\n" \
  "v_smfmac_f32_16x16x64_bf16 v[192:195], v[216:219], v[16:23], v248\n" \
  "ds_read_b128 v[228:231], v251 offset:8192\n" \
  "v_smfmac_f32_16x16x64_bf16 v[196:199], v[232:235], v[144:151], v248\n" \
  "ds_read_b128 v[232:235], v251 offset:9216\n" \
  "s_waitcnt lgkmcnt(5)\n" \
  "v_smfmac_f32_16x16x64_bf16 v[192:195], v[220:223], v[208:215], v248\n" \
  "ds_read_b128 v[208:211], v243 offset:16384\n" \
  "v_smfmac_f32_16x16x64_bf16 v[196:199], v[216:219], v[80:87], v248\n" \
  "ds_read_b128 v[212:215], v243 offset:17408\n" \
  "s_waitcnt lgkmcnt(6)\n" \
  "v_smfmac_f32_16x16x64_bf16 v[192:195], v[224:227], v[24:31], v248\n" \
  "ds_read_b128 v[216:219], v251 offset:10240\n" \
  "v_smfmac_f32_16x16x64_bf16 v[196:199], v[220:223], v[152:159], v248\n" \
  "ds_read_b128 v[220:223], v251 offset:11264\n" \
  "s_waitcnt lgkmcnt(5)\n" \
  "v_smfmac_f32_16x16x64_bf16 v[192:195], v[228:231], v[200:207], v248\n" \
  "ds_read_b128 v[200:203], v243 offset:32768\n" \
  "v_smfmac_f32_16x16x64_bf16 v[196:199], v[224:227], v[88:95], v248\n" \
  "ds_read_b128 v[204:207], v243 offset:33792\n" \
  "s_waitcnt lgkmcnt(6)\n" \
  "v_smfmac_f32_16x16x64_bf16 v[192:195], v[232:235], v[32:39], v248\n" \
  "ds_read_b128 v[224:227], v251 offset:12288\n" \
  "v_smfmac_f32_16x16x64_bf16 v[196:199], v[228:231], v[160:167], v248\n" \
  "ds_read_b128 v[228:231], v251 offset:13312\n" \
  "s_waitcnt lgkmcnt(5)\n" \
  "v_smfmac_f32_16x16x64_bf16 v[192:195], v[216:219], v[208:215], v248\n" \
  "ds_read_b128 v[208:211], v243 offset:49152\n" \
  "v_smfmac_f32_16x16x64_bf16 v[196:199], v[232:235], v[96:103], v248\n" \
  "ds_read_b128 v[212:215], v243 offset:50176\n" \
  "s_waitcnt lgkmcnt(6)\n" \
  "v_smfmac_f32_16x16x64_bf16 v[192:195], v[220:223], v[40:47], v248\n" \
  "ds_read_b128 v[232:235], v251 offset:14336\n" \
  "v_smfmac_f32_16x16x64_bf16 v[196:199], v[216:219], v[168:175], v248\n" \
  "ds_read_b128 v[216:219], v251 offset:15360\n" \
  "s_waitcnt lgkmcnt(5)\n" \
  "v_smfmac_f32_16x16x64_bf16 v[192:195], v[224:227], v[200:207], v248\n" \
  "v_smfmac_f32_16x16x64_bf16 v[196:199], v[220:223], v[104:111], v248\n" \
  "s_waitcnt lgkmcnt(4)\n" \
  "v_smfmac_f32_16x16x64_bf16 v[192:195], v[228:231], v[48:55], v248\n" \
  "v_smfmac_f32_16x16x64_bf16 v[196:199], v[224:227], v[176:183], v248\n" \
  "s_waitcnt lgkmcnt(1)\n" \
  "v_smfmac_f32_16x16x64_bf16 v[192:195], v[232:235], v[208:215], v248\n" \
  "s_waitcnt lgkmcnt(0)\n" \
  "v_smfmac_f32_16x16x64_bf16 v[192:195], v[216:219], v[56:63], v248\n" \
  "v_smfmac_f32_16x16x64_bf16 v[196:199], v[228:231], v[112:119], v248\n" \
  "v_smfmac_f32_16x16x64_bf16 v[196:199], v[232:235], v[184:191], v248\n" \
  "s_nop 0\n" \
  "v_add_f32 v192, v192, v194\n" \
  "v_smfmac_f32_16x16x64_bf16 v[196:199], v[216:219], v[120:127], v248\n" \
  "v_add_f32 v193, v193, v195\n" \
  "v_cvt_pk_bf16_f32 v194, v192, v193\n" \
  "s_nop 6\n" \
  "v_add_f32 v196, v196, v198\n" \
  "v_add_f32 v197, v197, v199\n" \
  "v_cvt_pk_bf16_f32 v198, v196, v197\n" \
  "ds_write2_b32 v247, v194, v198 offset1:96\n" \
  "v_cmp_u_f32 vcc, v192, v193\n" \
  "s_cbranch_vccnz .Lnf_%=\n" \
  ".Lnfb_%=:\n" \
  "s_waitcnt lgkmcnt(0)\n" \
  "s_barrier\n" \
  "s_nop 5\n" \
  "buffer_load_dwordx4 v[200:203], v251, %[rs], %[qo] offen offset:0 sc1\n" \
  "buffer_load_dwordx4 v[204:207], v251, %[rs], %[qo] offen offset:1024 sc1\n" \
  "buffer_load_dwordx4 v[208:211], v251, %[rs], %[qo] offen offset:2048 sc1\n" \
  "buffer_load_dwordx4 v[212:215], v251, %[rs], %[qo] offen offset:3072 sc1\n" \
  "s_nop 15\n" \
  "s_nop 15\n" \
  "s_nop 15\n" \
  "s_nop 9\n" \
  "ds_read_b32 v241, v251 offset:23560\n" \
  "s_waitcnt lgkmcnt(0)\n" \
  "v_cmp_eq_u32 vcc, %[fs], v241\n" \
  "s_cbranch_vccnz .Lrs_%=\n" \
  "ds_read_b128 v[192:195], v251 offset:24576\n" \
  "ds_read_b128 v[196:199], v251 offset:28672\n" \
  "ds_read_b128 v[216:219], v251 offset:25600\n" \
  "ds_read_b128 v[220:223], v251 offset:29696\n" \
  "ds_read_b128 v[224:227], v251 offset:26624\n" \
  "ds_read_b128 v[228:231], v251 offset:30720\n" \
  "ds_read_b128 v[232:235], v251 offset:27648\n" \
  "ds_read_b128 v[236:239], v251 offset:31744\n" \
  "s_waitcnt lgkmcnt(0)\n" \
  "s_barrier\n" \
  "s_waitcnt vmcnt(3)\n" \
  "v_mfma_f32_16x16x32_bf16 v[192:195], v[192:195], v[200:203], 0\n" \
  "v_mfma_f32_16x16x32_bf16 v[196:199], v[196:199], v[200:203], 0\n" \
  "s_waitcnt vmcnt(2)\n" \
  "v_mfma_f32_16x16x32_bf16 v[192:195], v[216:219], v[204:207], v[192:195]\n" \
  "v_mfma_f32_16x16x32_bf16 v[196:199], v[220:223], v[204:207], v[196:199]\n" \
  "s_waitcnt vmcnt(1)\n" \
  "v_mfma_f32_16x16x32_bf16 v[192:195], v[224:227], v[208:211], v[192:195]\n" \
  "v_mfma_f32_16x16x32_bf16 v[196:199], v[228:231], v[208:211], v[196:199]\n" \
  "s_waitcnt vmcnt(0)\n" \
  "v_mfma_f32_16x16x32_bf16 v[192:195], v[232:235], v[212:215], v[192:195]\n" \
  "v_mfma_f32_16x16x32_bf16 v[196:199], v[236:239], v[212:215], v[196:199]\n" \
  "s_nop 8\n"
#define QK_K \
  "s_branch .Lgo_%=\n" \
  ".Lsf_%=:\n" \
  "s_cmp_eq_u32 %[fs], -1\n" \
  "s_cbranch_scc1 .Lsfb_%=\n" \
  "v_mov_b32 v241, %[fs]\n" \
  "ds_write_b32 v251, v241 offset:23560\n" \
  "s_branch .Lrt_%=\n" \
  ".Lrs_%=:\n" \
  "s_mov_b32 %[fs], -1\n" \
  "s_nop 15\n" \
  "s_nop 15\n" \
  ".Lrt_%=:\n" \
  "s_setprio 0\n" \
  "s_waitcnt vmcnt(0) lgkmcnt(0)\n" \
  "buffer_load_dwordx4 v[216:219], v251, %[rs], %[o3] offen sc1\n" \
  "buffer_load_dwordx4 v[220:223], v251, %[rs], %[o0] offen sc1\n" \
  "buffer_load_dwordx4 v[224:227], v251, %[rs], %[o1] offen sc1\n" \
  "buffer_load_dwordx4 v[228:231], v251, %[rs], %[o2] offen sc1\n" \
  "v_subrev_u32 v232, 0x1000, v250\n" \
  "s_mov_b32 %[cnt], -2\n" \
  "ds_read_b128 v[200:203], v232 offset:0\n" \
  "ds_read_b128 v[204:207], v232 offset:4096\n" \
  "ds_read_b128 v[208:211], v232 offset:8192\n" \
  "ds_read_b128 v[212:215], v232 offset:12288\n" \
  "s_waitcnt lgkmcnt(0)\n" \
  "v_max_u32 v241, v200, v201\n" \
  "v_max3_u32 v241, v241, v202, v203\n" \
  "v_max3_u32 v241, v241, v204, v205\n" \
  "v_max3_u32 v241, v241, v206, v207\n" \
  "v_max3_u32 v241, v241, v208, v209\n" \
  "v_max3_u32 v241, v241, v210, v211\n" \
  "v_max3_u32 v241, v241, v212, v213\n" \
  "v_max3_u32 v241, v241, v214, v215\n" \
  "v_cmp_eq_u32 vcc, -1, v241\n" \
  "s_cbranch_vccz .Lrd_%=\n" \
  "s_mov_b32 %[cnt], 0x40000\n" \
  ".Lrr_%=:\n" \
  "s_waitcnt vmcnt(0)\n" \
  "v_max_u32 v241, v216, v217\n" \
  "v_max3_u32 v241, v241, v218, v219\n" \
  "v_max3_u32 v241, v241, v220, v221\n" \
  "v_max3_u32 v241, v241, v222, v223\n" \
  "v_max3_u32 v241, v241, v224, v225\n" \
  "v_max3_u32 v241, v241, v226, v227\n" \
  "v_max3_u32 v241, v241, v228, v229\n" \
  "v_max3_u32 v241, v241, v230, v231\n" \
  "v_cmp_eq_u32 vcc, -1, v241\n" \
  "s_cbranch_vccz .Lrd_%=\n" \
  "s_sleep 2\n" \
  "buffer_load_dwordx4 v[216:219], v251, %[rs], %[o3] offen sc1\n" \
  "buffer_load_dwordx4 v[220:223], v251, %[rs], %[o0] offen sc1\n" \
  "buffer_load_dwordx4 v[224:227], v251, %[rs], %[o1] offen sc1\n" \
  "buffer_load_dwordx4 v[228:231], v251, %[rs], %[o2] offen sc1\n" \
  "s_sub_u32 %[cnt], %[cnt], 1\n" \
  "s_cbranch_scc0 .Lrr_%=\n" \
  ".Lrd_%=:\n" \
  "ds_read_b128 v[200:203], v249 offset:0\n" \
  "ds_read_b128 v[204:207], v249 offset:1024\n" \
  "ds_read_b128 v[208:211], v249 offset:16384\n" \
  "ds_read_b128 v[212:215], v249 offset:17408\n" \
  "v_mov_b64 v[192:193], 0\n" \
  "v_mov_b64 v[194:195], 0\n" \
  "v_mov_b64 v[196:197], 0\n" \
  "v_mov_b64 v[198:199], 0\n" \
  "s_cmp_eq_u32 %[fs], -1\n" \
  "s_cbranch_scc1 .Lre_%=\n" \
  "s_mov_b32 %[fs], -1\n" \
  "s_barrier\n" \
  ".Lre_%=:\n" \
  "s_waitcnt vmcnt(0)\n" \
  "s_cmp_eq_u32 %[cnt], -2\n" \
  "s_cbranch_scc1 .Lrf_%=\n" \
  "ds_write_b128 v232, v[216:219] offset:0\n" \
  "ds_write_b128 v232, v[220:223] offset:4096\n" \
  "ds_write_b128 v232, v[224:227] offset:8192\n" \
  "ds_write_b128 v232, v[228:231] offset:12288\n" \
  ".Lrf_%=:\n" \
  "v_mov_b32 v241, 0\n" \
  "s_waitcnt lgkmcnt(0)\n" \
  "s_barrier\n" \
  "s_waitcnt lgkmcnt(0)\n" \
  "s_barrier\n" \
  "s_branch .Ll1_%=\n" \
  ".p2align 6\n" \
  ".Lgo_%=:\n" \
  "ds_read_b128 v[200:203], v249 offset:0\n" \
  "ds_read_b128 v[204:207], v249 offset:1024\n" \
  "ds_read_b128 v[208:211], v249 offset:16384\n" \
  "ds_read_b128 v[212:215], v249 offset:17408\n" \
  "v_mov_b64 v[192:193], 0\n" \
  "v_mov_b64 v[194:195], 0\n" \
  "v_mov_b64 v[196:197], 0\n" \
  "v_mov_b64 v[198:199], 0\n" \
  "s_mov_b32 %[keep], m0\n" \
  "v_readfirstlane_b32 %[cnt], v250\n" \
  "s_mov_b32 m0, %[cnt]\n" \
  "v_bfe_u32 v241, v251, 2, 7\n" \
  "v_lshl_add_u32 v241, v251, 4, v241\n" \
  "s_waitcnt lgkmcnt(0)\n" \
  "s_cmp_eq_u32 %[tw], 0\n" \
  "s_cbranch_scc1 .Ltq_%=\n" \
  ".Ltl_%=:\n" \
  "s_memtime vcc\n" \
  "s_waitcnt lgkmcnt(0)\n" \
  "s_sub_i32 %[cnt], %[tg], vcc_lo\n" \
  "s_cmp_lt_i32 %[cnt], 260\n" \
  "s_cbranch_scc1 .Ltf_%=\n" \
  "s_sleep 1\n" \
  "s_branch .Ltl_%=\n" \
  ".Ltf_%=:\n" \
  "s_max_i32 %[cnt], %[cnt], 0\n" \
  ".p2align 3\n" \
  ".Ltg_%=:\n" \
  "s_sub_u32 %[cnt], %[cnt], 28\n" \
  "s_cbranch_scc0 .Ltg_%=\n" \
  ".Ltq_%=:\n" \
  "s_barrier\n" \
  "buffer_load_dwordx4 v251, %[rs], %[o0] offen sc1 lds\n" \
  "s_add_u32 m0, m0, 0x1000\n" \
  "s_nop 4\n" \
  "buffer_load_dwordx4 v251, %[rs], %[o1] offen sc1 lds\n" \
  "s_add_u32 m0, m0, 0x1000\n" \
  "s_nop 4\n" \
  "buffer_load_dwordx4 v251, %[rs], %[o2] offen sc1 lds\n" \
  "s_barrier\n" \
  "s_waitcnt vmcnt(2)\n" \
  "s_barrier\n" \
  "s_mov_b32 m0, %[keep]\n" \
  ".Ll1_%=:\n" \
  "s_memtime vcc\n" \
  "s_setprio 3\n" \
  "s_waitcnt vmcnt(0)\n" \
  "s_waitcnt lgkmcnt(0)\n" \
  "s_barrier\n" \
  "ds_read2_b32 v[236:237], v241 offset1:32\n" \
  "s_sub_u32 %[cnt], vcc_lo, %[ckm]\n" \
  "s_mul_hi_u32 %[cnt], %[cnt], %[inv]\n" \
  "s_add_u32 %[cnt], %[cnt], %[r16]\n" \
  "s_and_b32 %[cnt], %[cnt], -2\n" \
  "s_waitcnt lgkmcnt(0)\n" \
  "v_max_u32 v241, v236, v237\n" \
  "v_cmp_eq_u32 vcc, -1, v241\n" \
  "s_cbranch_vccnz .Lsf_%=\n" \
  ".Lsfb_%=:\n" \
  "v_mov_b32 v241, %[cnt]\n" \
  "buffer_store_dword v241, v245, %[rs], %[sst] offen sc1\n" \
  "s_setprio 0\n" \
  "ds_read_b128 v[216:219], v251 offset:0\n" \
  "ds_read_b128 v[220:223], v251 offset:1024\n" \
  "ds_read_b128 v[224:227], v251 offset:2048\n" \
  "ds_read_b128 v[228:231], v251 offset:3072\n" \
  "s_waitcnt lgkmcnt(3)\n" \
  "v_smfmac_f32_16x16x64_bf16 v[192:195], v[216:219], v[200:207], v248\n" \
  "ds_read_b128 v[200:203], v249 offset:32768\n" \
  "ds_read_b128 v[204:207], v249 offset:33792\n" \
  "s_waitcnt lgkmcnt(4)\n" \
  "v_smfmac_f32_16x16x64_bf16 v[192:195], v[220:223], v[0:7], v248\n" \
  "ds_read_b128 v[232:235], v251 offset:4096\n" \
  "v_smfmac_f32_16x16x64_bf16 v[196:199], v[216:219], v[128:135], v248\n" \
  "ds_read_b128 v[216:219], v251 offset:5120\n" \
  "s_waitcnt lgkmcnt(5)\n" \
  "v_smfmac_f32_16x16x64_bf16 v[192:195], v[224:227], v[208:215], v248\n" \
  "ds_read_b128 v[208:211], v249 offset:49152\n" \
  "v_smfmac_f32_16x16x64_bf16 v[196:199], v[220:223], v[64:71], v248\n" \
  "ds_read_b128 v[212:215], v249 offset:50176\n" \
  "s_waitcnt lgkmcnt(6)\n" \
  "v_smfmac_f32_16x16x64_bf16 v[192:195], v[228:231], v[8:15], v248\n" \
  "ds_read_b128 v[220:223], v251 offset:6144\n" \
  "v_smfmac_f32_16x16x64_bf16 v[196:199], v[224:227], v[136:143], v248\n" \
  "ds_read_b128 v[224:227], v251 offset:7168\n" \
  "s_waitcnt lgkmcnt(5)\n" \
  "v_smfmac_f32_16x16x64_bf16 v[192:195], v[232:235], v[200:207], v248\n" \
  "ds_read_b128 v[200:203], v243 offset:0\n" \
  "s_barrier\n" \
  "ds_read_b32 v236, v251 offset:23560\n" \
  "v_smfmac_f32_16x16x64_bf16 v[196:199], v[228:231], v[72:79], v248\n" \
  "buffer_load_dwordx2 v[240:241], v244, %[rs], %[ss] offen sc1\n" \
  "ds_read_b128 v[204:207], v243 offset:1024\n" \
  "s_waitcnt lgkmcnt(7)\n" \
  "v_smfmac_f32_16x16x64_bf16 v[192:195], v[216:219], v[16:23], v248\n" \
  "s_cmp_eq_u32 %[tc], 0\n" \
  "s_cbranch_scc1 .Lnt_%=\n" \
  "buffer_load_dword v237, v242, %[rs], %[tc] offen sc1\n" \
  ".Lnt_%=:\n" \
  "ds_read_b128 v[228:231], v251 offset:8192\n" \
  "v_smfmac_f32_16x16x64_bf16 v[196:199], v[232:235], v[144:151], v248\n" \
  "ds_read_b128 v[232:235], v251 offset:9216\n" \
  "s_waitcnt lgkmcnt(3)\n" \
  "v_cmp_eq_u32 vcc, %[fs], v236\n" \
  "v_smfmac_f32_16x16x64_bf16 v[192:195], v[220:223], v[208:215], v248\n" \
  "ds_read_b128 v[208:211], v243 offset:16384\n" \
  "v_smfmac_f32_16x16x64_bf16 v[196:199], v[216:219], v[80:87], v248\n" \
  "ds_read_b128 v[212:215], v243 offset:17408\n" \
  "v_smfmac_f32_16x16x64_bf16 v[192:195], v[224:227], v[24:31], v248\n" \
  "ds_read_b128 v[216:219], v251 offset:10240\n" \
  "v_smfmac_f32_16x16x64_bf16 v[196:199], v[220:223], v[152:159], v248\n" \
  "ds_read_b128 v[220:223], v251 offset:11264\n" \
  "s_waitcnt lgkmcnt(5)\n" \
  "v_smfmac_f32_16x16x64_bf16 v[192:195], v[228:231], v[200:207], v248\n" \
  "ds_read_b128 v[200:203], v243 offset:32768\n" \
  "v_smfmac_f32_16x16x64_bf16 v[196:199], v[224:227], v[88:95], v248\n" \
  "ds_read_b128 v[204:207], v243 offset:33792\n" \
  "s_waitcnt lgkmcnt(6)\n" \
  "v_smfmac_f32_16x16x64_bf16 v[192:195], v[232:235], v[32:39], v248\n" \
  "ds_read_b128 v[224:227], v251 offset:12288\n" \
  "v_smfmac_f32_16x16x64_bf16 v[196:199], v[228:231], v[160:167], v248\n" \
  "ds_read_b128 v[228:231], v251 offset:13312\n" \
  "s_waitcnt lgkmcnt(5)\n" \
  "v_smfmac_f32_16x16x64_bf16 v[192:195], v[216:219], v[208:215], v248\n" \
  "ds_read_b128 v[208:211], v243 offset:49152\n" \
  "v_smfmac_f32_16x16x64_bf16 v[196:199], v[232:235], v[96:103], v248\n" \
  "ds_read_b128 v[212:215], v243 offset:50176\n" \
  "s_waitcnt lgkmcnt(6)\n" \
  "v_smfmac_f32_16x16x64_bf16 v[192:195], v[220:223], v[40:47], v248\n" \
  "ds_read_b128 v[232:235], v251 offset:14336\n" \
  "v_smfmac_f32_16x16x64_bf16 v[196:199], v[216:219], v[168:175], v248\n" \
  "ds_read_b128 v[216:219], v251 offset:15360\n" \
  "s_waitcnt lgkmcnt(5)\n" \
  "v_smfmac_f32_16x16x64_bf16 v[192:195], v[224:227], v[200:207], v248\n" \
  "v_smfmac_f32_16x16x64_bf16 v[196:199], v[220:223], v[104:111], v248\n" \
  "s_waitcnt lgkmcnt(4)\n" \
  "v_smfmac_f32_16x16x64_bf16 v[192:195], v[228:231], v[48:55], v248\n" \
  "v_smfmac_f32_16x16x64_bf16 v[196:199], v[224:227], v[176:183], v248\n" \
  "s_waitcnt lgkmcnt(1)\n" \
  "v_smfmac_f32_16x16x64_bf16 v[192:195], v[232:235], v[208:215], v248\n" \
  "s_waitcnt lgkmcnt(0)\n" \
  "v_smfmac_f32_16x16x64_bf16 v[192:195], v[216:219], v[56:63], v248\n" \
  "v_smfmac_f32_16x16x64_bf16 v[196:199], v[228:231], v[112:119], v248\n" \
  "v_smfmac_f32_16x16x64_bf16 v[196:199], v[232:235], v[184:191], v248\n" \
  "s_nop 0\n" \
  "v_add_f32 v192, v192, v194\n" \
  "v_smfmac_f32_16x16x64_bf16 v[196:199], v[216:219], v[120:127], v248\n" \
  "s_cbranch_vccnz .Lrs_%=\n" \
  "s_setprio 3\n" \
  "v_add_f32 v193, v193, v195\n" \
  "v_cvt_pk_bf16_f32 v194, v192, v193\n" \
  "s_nop 4\n" \
  "v_add_f32 v196, v196, v198\n" \
  "v_add_f32 v197, v197, v199\n" \
  "v_cvt_pk_bf16_f32 v198, v196, v197\n" \
  "ds_write2_b32 v247, v194, v198 offset1:96\n" \
  "s_waitcnt lgkmcnt(0)\n" \
  "s_barrier\n" \
  "s_setprio 0\n" \
  "s_waitcnt vmcnt(1)\n" \
  "s_cmp_lg_u32 %[tc], 0\n" \
  "s_cbranch_scc1 .Lnw_%=\n" \
  "s_waitcnt vmcnt(0)\n" \
  ".Lnw_%=:\n"
#define OW_F \
  "s_branch .Lgo_%=\n" \
  ".Lbad_%=:\n" \
  "s_mov_b32 %[st], 1\n" \
  "s_branch .Lend_%=\n" \
  ".p2align 6\n" \
  "s_nop 0\n" \
  "s_nop 0\n" \
  "s_nop 0\n" \
  "s_nop 0\n" \
  "s_nop 0\n" \
  "s_nop 0\n" \
  "s_nop 0\n" \
  ".Lgo_%=:\n" \
  "v_mov_b64 v[84:85], 0\n" \
  "v_mov_b64 v[86:87], 0\n" \
  "v_mov_b64 v[88:89], 0\n" \
  "v_mov_b64 v[90:91], 0\n" \
  "v_mov_b64 v[92:93], 0\n" \
  "v_mov_b64 v[94:95], 0\n" \
  "v_mov_b64 v[96:97], 0\n" \
  "v_mov_b64 v[98:99], 0\n" \
  "v_mov_b64 v[100:101], 0\n" \
  "v_mov_b64 v[102:103], 0\n" \
  "s_mov_b32 %[st], 0\n" \
  "s_barrier\n" \
  "ds_read_b32 v104, v119\n" \
  "s_nop 15\n" \
  "s_nop 15\n" \
  "s_nop 15\n" \
  "s_nop 15\n" \
  "s_nop 5\n" \
  "buffer_load_dwordx4 v[0:3], v112, %[rs], %[sv1] offen offset:0 sc1\n" \
  "buffer_load_dwordx4 v[4:7], v112, %[rs], %[sv1] offen offset:1024 sc1\n" \
  "buffer_load_dwordx4 v[8:11], v112, %[rs], %[sv1] offen offset:2048 sc1\n" \
  "buffer_load_dwordx4 v[12:15], v112, %[rs], %[sv1] offen offset:3072 sc1\n" \
  "s_waitcnt lgkmcnt(0)\n" \
  "v_readfirstlane_b32 %[st], v104\n" \
  ".Ltl_%=:\n" \
  "s_memtime vcc\n" \
  "s_waitcnt lgkmcnt(0)\n" \
  "s_sub_i32 %[cnt], %[st], vcc_lo\n" \
  "s_sub_i32 vcc_hi, %[cnt], 120\n" \
  "s_cmp_lt_u32 vcc_hi, 2000\n" \
  "s_cbranch_scc1 .Ltl_%=\n" \
  "s_mov_b32 %[st], 0\n" \
  "s_max_i32 %[cnt], %[cnt], 0\n" \
  "s_min_i32 %[cnt], %[cnt], 2000\n" \
  ".p2align 3\n" \
  ".Ltg_%=:\n" \
  "s_sub_u32 %[cnt], %[cnt], 28\n" \
  "s_cbranch_scc0 .Ltg_%=\n" \
  "buffer_load_dwordx4 v[16:19], v112, %[rs], %[sv2] offen offset:0 sc1\n" \
  "buffer_load_dwordx4 v[20:23], v112, %[rs], %[sv2] offen offset:1024 sc1\n" \
  "s_nop 3\n" \
  "buffer_load_dwordx4 v[80:83], v113, %[rs], %[sp] offen sc1\n" \
  "s_nop 5\n" \
  "buffer_load_dwordx4 v[64:67], v114, %[rs], %[sp1] offen offset:0 sc1\n" \
  "buffer_load_dwordx4 v[68:71], v114, %[rs], %[sp1] offen offset:1024 sc1\n" \
  "buffer_load_dwordx4 v[72:75], v114, %[rs], %[sp2] offen offset:0 sc1\n" \
  "buffer_load_dwordx4 v[24:27], v112, %[rs], %[sv2] offen offset:2048 sc1\n" \
  "buffer_load_dwordx4 v[28:31], v112, %[rs], %[sv2] offen offset:3072 sc1\n" \
  "buffer_load_dwordx4 v[76:79], v114, %[rs], %[sp2] offen offset:1024 sc1\n" \
  "s_barrier\n" \
  "ds_read_b128 v[32:35], v115 offset:0\n" \
  "ds_read_b128 v[36:39], v115 offset:1024\n" \
  "ds_read_b128 v[40:43], v115 offset:2048\n" \
  "ds_read_b128 v[44:47], v115 offset:3072\n" \
  "ds_read_b128 v[48:51], v115 offset:4096\n" \
  "ds_read_b128 v[52:55], v115 offset:5120\n" \
  "ds_read_b128 v[56:59], v115 offset:6144\n" \
  "ds_read_b128 v[60:63], v115 offset:7168\n" \
  "s_waitcnt vmcnt(6)\n" \
  "s_mov_b64 exec, %[m0]\n" \
  "v_mov_b64 v[84:85], v[80:81]\n" \
  "v_mov_b64 v[86:87], v[82:83]\n" \
  "s_mov_b64 exec, %[m1]\n" \
  "v_mov_b64 v[88:89], v[80:81]\n" \
  "v_mov_b64 v[90:91], v[82:83]\n" \
  "s_mov_b64 exec, %[m2]\n" \
  "v_mov_b64 v[92:93], v[80:81]\n" \
  "v_mov_b64 v[94:95], v[82:83]\n" \
  "s_mov_b64 exec, %[m3]\n" \
  "v_mov_b64 v[96:97], v[80:81]\n" \
  "v_mov_b64 v[98:99], v[82:83]\n" \
  "s_mov_b64 exec, -1\n" \
  "s_waitcnt vmcnt(5)\n" \
  "v_smfmac_f32_16x16x64_bf16 v[100:103], v[64:67], v[0:7], v118\n" \
  "s_waitcnt vmcnt(4)\n" \
  "v_smfmac_f32_16x16x64_bf16 v[100:103], v[68:71], v[8:15], v118\n" \
  "s_waitcnt lgkmcnt(6)\n" \
  "v_smfmac_f32_16x16x64_bf16 v[100:103], v[84:87], v[32:39], v118\n" \
  "s_waitcnt lgkmcnt(4)\n" \
  "v_smfmac_f32_16x16x64_bf16 v[100:103], v[88:91], v[40:47], v118 abid:1\n" \
  "s_waitcnt vmcnt(3)\n" \
  "v_smfmac_f32_16x16x64_bf16 v[100:103], v[72:75], v[16:23], v118\n" \
  "s_waitcnt lgkmcnt(2)\n" \
  "v_smfmac_f32_16x16x64_bf16 v[100:103], v[92:95], v[48:55], v118\n" \
  "s_waitcnt lgkmcnt(0)\n" \
  "v_smfmac_f32_16x16x64_bf16 v[100:103], v[96:99], v[56:63], v118 abid:1\n" \
  "s_waitcnt vmcnt(0)\n" \
  "v_smfmac_f32_16x16x64_bf16 v[100:103], v[76:79], v[24:31], v118\n" \
  "s_nop 8\n" \
  "v_add_f32 v100, v100, v102\n" \
  "v_add_f32 v101, v101, v103\n" \
  "v_cvt_pk_bf16_f32 v104, v100, v101\n" \
  "v_cmp_u_f32 vcc, v100, v101\n" \
  "s_nop 0\n" \
  "v_mov_b32_dpp v105, v104 quad_perm:[1,0,3,2] row_mask:0xf bank_mask:0xf\n" \
  "v_perm_b32 v104, v105, v104, v117\n" \
  "s_cbranch_vccnz .Lbad_%=\n" \
  "buffer_store_dword v104, v116, %[rs], %[sm0] offen sc1\n" \
  "buffer_store_dword v104, v116, %[rs], %[sm1] offen sc1\n" \
  "buffer_store_dword v104, v116, %[rs], %[sm2] offen sc1\n" \
  "buffer_store_dword v104, v116, %[rs], %[sm3] offen sc1\n" \
  ".Lend_%=:\n"
#define OW_E \
  "s_branch .Lgo_%=\n" \
  ".Lbad_%=:\n" \
  "s_mov_b32 %[st], 1\n" \
  "s_branch .Lend_%=\n" \
  ".p2align 6\n" \
  ".Lgo_%=:\n" \
  "v_mov_b64 v[208:209], 0\n" \
  "v_mov_b64 v[210:211], 0\n" \
  "v_mov_b64 v[212:213], 0\n" \
  "v_mov_b64 v[214:215], 0\n" \
  "v_mov_b64 v[216:217], 0\n" \
  "v_mov_b64 v[218:219], 0\n" \
  "v_mov_b64 v[220:221], 0\n" \
  "v_mov_b64 v[222:223], 0\n" \
  "v_mov_b64 v[224:225], 0\n" \
  "v_mov_b64 v[226:227], 0\n" \
  "v_mov_b64 v[228:229], 0\n" \
  "v_mov_b64 v[230:231], 0\n" \
  "v_mov_b64 v[232:233], 0\n" \
  "v_mov_b64 v[234:235], 0\n" \
  "v_mov_b64 v[236:237], 0\n" \
  "v_mov_b64 v[238:239], 0\n" \
  "s_mov_b32 %[st], 0\n" \
  "s_barrier\n" \
  "buffer_load_dwordx4 v[192:195], v241, %[rs], %[se] offen offset:0 sc1\n" \
  "s_nop 11\n" \
  "buffer_load_dwordx4 v[196:199], v241, %[rs], %[se] offen offset:1024 sc1\n" \
  "s_nop 11\n" \
  "buffer_load_dwordx4 v[200:203], v241, %[rs], %[se] offen offset:2048 sc1\n" \
  "buffer_load_dwordx4 v[204:207], v241, %[rs], %[se] offen offset:3072 sc1\n" \
  "s_waitcnt vmcnt(3)\n" \
  "v_smfmac_f32_16x16x64_bf16 v[208:211], v[192:195], %[b00], v243\n" \
  "v_smfmac_f32_16x16x64_bf16 v[224:227], v[192:195], %[b10], v243\n" \
  "v_max3_u32 v240, v192, v192, v193\n" \
  "v_max3_u32 v240, v240, v194, v195\n" \
  "s_waitcnt vmcnt(2)\n" \
  "v_smfmac_f32_16x16x64_bf16 v[212:215], v[196:199], %[b01], v243\n" \
  "v_smfmac_f32_16x16x64_bf16 v[228:231], v[196:199], %[b11], v243\n" \
  "v_max3_u32 v240, v240, v196, v197\n" \
  "v_max3_u32 v240, v240, v198, v199\n" \
  "v_cvt_pk_bf16_f32 v208, v208, v209\n" \
  "v_cvt_pk_bf16_f32 v209, v210, v211\n" \
  "v_cvt_pk_bf16_f32 v224, v224, v225\n" \
  "v_cvt_pk_bf16_f32 v225, v226, v227\n" \
  "v_cvt_pk_bf16_f32 v210, v212, v213\n" \
  "v_cvt_pk_bf16_f32 v211, v214, v215\n" \
  "ds_write_b128 v242, v[208:211] offset:0\n" \
  "v_cvt_pk_bf16_f32 v226, v228, v229\n" \
  "v_cvt_pk_bf16_f32 v227, v230, v231\n" \
  "ds_write_b128 v242, v[224:227] offset:8192\n" \
  "s_waitcnt vmcnt(1)\n" \
  "v_smfmac_f32_16x16x64_bf16 v[216:219], v[200:203], %[b02], v243\n" \
  "v_smfmac_f32_16x16x64_bf16 v[232:235], v[200:203], %[b12], v243\n" \
  "s_waitcnt vmcnt(0)\n" \
  "v_smfmac_f32_16x16x64_bf16 v[220:223], v[204:207], %[b03], v243\n" \
  "v_smfmac_f32_16x16x64_bf16 v[236:239], v[204:207], %[b13], v243\n" \
  "v_cvt_pk_bf16_f32 v216, v216, v217\n" \
  "v_cvt_pk_bf16_f32 v217, v218, v219\n" \
  "v_cvt_pk_bf16_f32 v232, v232, v233\n" \
  "v_cvt_pk_bf16_f32 v233, v234, v235\n" \
  "s_nop 0\n" \
  "v_cvt_pk_bf16_f32 v218, v220, v221\n" \
  "v_cvt_pk_bf16_f32 v219, v222, v223\n" \
  "ds_write_b128 v242, v[216:219] offset:1024\n" \
  "s_nop 0\n" \
  "v_cvt_pk_bf16_f32 v234, v236, v237\n" \
  "v_cvt_pk_bf16_f32 v235, v238, v239\n" \
  "ds_write_b128 v242, v[232:235] offset:9216\n" \
  "v_max3_u32 v240, v240, v200, v201\n" \
  "v_max3_u32 v240, v240, v202, v203\n" \
  "v_max3_u32 v240, v240, v204, v205\n" \
  "v_max3_u32 v240, v240, v206, v207\n" \
  "v_cmp_eq_u32 vcc, -1, v240\n" \
  "s_cbranch_vccnz .Lbad_%=\n" \
  "s_waitcnt lgkmcnt(0)\n" \
  "s_barrier\n" \
  ".Lend_%=:\n"
#define ST_Y \
  "s_mov_b32 %[keep], m0\n" \
  "ds_read_b128 v[240:243], v250 offset:0\n" \
  "ds_read_b128 v[244:247], v250 offset:1024\n" \
  "v_mfma_f32_16x16x32_bf16 v[192:195], v[0:3], v[208:211], 0\n" \
  "v_mfma_f32_16x16x32_bf16 v[196:199], v[64:67], v[208:211], 0\n" \
  "ds_read_b128 v[208:211], v252 offset:8192\n" \
  "v_mfma_f32_16x16x32_bf16 v[200:203], v[0:3], v[212:215], 0\n" \
  "v_mfma_f32_16x16x32_bf16 v[204:207], v[64:67], v[212:215], 0\n" \
  "ds_read_b128 v[212:215], v252 offset:9216\n" \
  "v_mfma_f32_16x16x32_bf16 v[192:195], v[128:131], v[216:219], v[192:195]\n" \
  "v_mfma_f32_16x16x32_bf16 v[196:199], v[132:135], v[216:219], v[196:199]\n" \
  "ds_read_b128 v[216:219], v252 offset:10240\n" \
  "v_mfma_f32_16x16x32_bf16 v[200:203], v[128:131], v[220:223], v[200:203]\n" \
  "v_mfma_f32_16x16x32_bf16 v[204:207], v[132:135], v[220:223], v[204:207]\n" \
  "ds_read_b128 v[220:223], v252 offset:11264\n" \
  "v_mfma_f32_16x16x32_bf16 v[192:195], v[4:7], v[224:227], v[192:195]\n" \
  "v_mfma_f32_16x16x32_bf16 v[196:199], v[68:71], v[224:227], v[196:199]\n" \
  "ds_read_b128 v[224:227], v252 offset:12288\n" \
  "v_mfma_f32_16x16x32_bf16 v[200:203], v[4:7], v[228:231], v[200:203]\n" \
  "v_mfma_f32_16x16x32_bf16 v[204:207], v[68:71], v[228:231], v[204:207]\n" \
  "ds_read_b128 v[228:231], v252 offset:13312\n" \
  "s_waitcnt lgkmcnt(7)\n" \
  "v_mfma_f32_16x16x32_bf16 v[192:195], v[240:243], v[232:235], v[192:195]\n" \
  "s_waitcnt lgkmcnt(6)\n" \
  "v_mfma_f32_16x16x32_bf16 v[196:199], v[244:247], v[232:235], v[196:199]\n" \
  "ds_read_b128 v[232:235], v252 offset:14336\n" \
  "v_mfma_f32_16x16x32_bf16 v[200:203], v[240:243], v[236:239], v[200:203]\n" \
  "v_mfma_f32_16x16x32_bf16 v[204:207], v[244:247], v[236:239], v[204:207]\n" \
  "ds_read_b128 v[236:239], v252 offset:15360\n" \
  "ds_read_b128 v[240:243], v250 offset:16384\n" \
  "ds_read_b128 v[244:247], v250 offset:17408\n" \
  "s_waitcnt lgkmcnt(9)\n" \
  "v_mfma_f32_16x16x32_bf16 v[192:195], v[8:11], v[208:211], v[192:195]\n" \
  "v_mfma_f32_16x16x32_bf16 v[196:199], v[72:75], v[208:211], v[196:199]\n" \
  "s_waitcnt lgkmcnt(8)\n" \
  "v_mfma_f32_16x16x32_bf16 v[200:203], v[8:11], v[212:215], v[200:203]\n" \
  "v_mfma_f32_16x16x32_bf16 v[204:207], v[72:75], v[212:215], v[204:207]\n" \
  "s_waitcnt vmcnt(2)\n" \
  "s_waitcnt lgkmcnt(0)\n" \
  "s_barrier\n" \
  "ds_read_b128 v[208:211], v252 offset:16384\n" \
  "ds_read_b128 v[212:215], v252 offset:17408\n" \
  "s_add_u32 m0, %[mw], 0\n" \
  "s_xor_b32 %[cnt], %[sc], 512\n" \
  "buffer_load_dwordx4 v248, %[rx], %[cnt] offen lds\n" \
  "s_add_u32 m0, %[mw], 8192\n" \
  "s_xor_b32 %[cnt], %[sc], 640\n" \
  "buffer_load_dwordx4 v248, %[rx], %[cnt] offen lds\n" \
  "v_mfma_f32_16x16x32_bf16 v[192:195], v[136:139], v[216:219], v[192:195]\n" \
  "v_mfma_f32_16x16x32_bf16 v[196:199], v[140:143], v[216:219], v[196:199]\n" \
  "ds_read_b128 v[216:219], v252 offset:18432\n" \
  "v_mfma_f32_16x16x32_bf16 v[200:203], v[136:139], v[220:223], v[200:203]\n" \
  "v_mfma_f32_16x16x32_bf16 v[204:207], v[140:143], v[220:223], v[204:207]\n" \
  "ds_read_b128 v[220:223], v252 offset:19456\n" \
  "v_mfma_f32_16x16x32_bf16 v[192:195], v[12:15], v[224:227], v[192:195]\n" \
  "v_mfma_f32_16x16x32_bf16 v[196:199], v[76:79], v[224:227], v[196:199]\n" \
  "ds_read_b128 v[224:227], v252 offset:20480\n" \
  "v_mfma_f32_16x16x32_bf16 v[200:203], v[12:15], v[228:231], v[200:203]\n" \
  "v_mfma_f32_16x16x32_bf16 v[204:207], v[76:79], v[228:231], v[204:207]\n" \
  "ds_read_b128 v[228:231], v252 offset:21504\n" \
  "v_mfma_f32_16x16x32_bf16 v[192:195], v[240:243], v[232:235], v[192:195]\n" \
  "v_mfma_f32_16x16x32_bf16 v[196:199], v[244:247], v[232:235], v[196:199]\n" \
  "ds_read_b128 v[232:235], v252 offset:22528\n" \
  "v_mfma_f32_16x16x32_bf16 v[200:203], v[240:243], v[236:239], v[200:203]\n" \
  "v_mfma_f32_16x16x32_bf16 v[204:207], v[244:247], v[236:239], v[204:207]\n" \
  "ds_read_b128 v[236:239], v252 offset:23552\n" \
  "ds_read_b128 v[240:243], v250 offset:32768\n" \
  "ds_read_b128 v[244:247], v250 offset:33792\n" \
  "s_waitcnt lgkmcnt(9)\n" \
  "v_mfma_f32_16x16x32_bf16 v[192:195], v[16:19], v[208:211], v[192:195]\n" \
  "v_mfma_f32_16x16x32_bf16 v[196:199], v[80:83], v[208:211], v[196:199]\n" \
  "ds_read_b128 v[208:211], v252 offset:24576\n" \
  "s_waitcnt lgkmcnt(9)\n" \
  "v_mfma_f32_16x16x32_bf16 v[200:203], v[16:19], v[212:215], v[200:203]\n" \
  "v_mfma_f32_16x16x32_bf16 v[204:207], v[80:83], v[212:215], v[204:207]\n" \
  "ds_read_b128 v[212:215], v252 offset:25600\n" \
  "s_waitcnt lgkmcnt(9)\n" \
  "v_mfma_f32_16x16x32_bf16 v[192:195], v[144:147], v[216:219], v[192:195]\n" \
  "v_mfma_f32_16x16x32_bf16 v[196:199], v[148:151], v[216:219], v[196:199]\n" \
  "ds_read_b128 v[216:219], v252 offset:26624\n" \
  "s_waitcnt lgkmcnt(9)\n" \
  "v_mfma_f32_16x16x32_bf16 v[200:203], v[144:147], v[220:223], v[200:203]\n" \
  "v_mfma_f32_16x16x32_bf16 v[204:207], v[148:151], v[220:223], v[204:207]\n" \
  "ds_read_b128 v[220:223], v252 offset:27648\n" \
  "s_waitcnt lgkmcnt(9)\n" \
  "v_mfma_f32_16x16x32_bf16 v[192:195], v[20:23], v[224:227], v[192:195]\n" \
  "v_mfma_f32_16x16x32_bf16 v[196:199], v[84:87], v[224:227], v[196:199]\n" \
  "ds_read_b128 v[224:227], v252 offset:28672\n" \
  "s_waitcnt lgkmcnt(9)\n" \
  "v_mfma_f32_16x16x32_bf16 v[200:203], v[20:23], v[228:231], v[200:203]\n" \
  "v_mfma_f32_16x16x32_bf16 v[204:207], v[84:87], v[228:231], v[204:207]\n" \
  "ds_read_b128 v[228:231], v252 offset:29696\n" \
  "s_waitcnt lgkmcnt(7)\n" \
  "v_mfma_f32_16x16x32_bf16 v[192:195], v[240:243], v[232:235], v[192:195]\n" \
  "s_waitcnt lgkmcnt(6)\n" \
  "v_mfma_f32_16x16x32_bf16 v[196:199], v[244:247], v[232:235], v[196:199]\n" \
  "ds_read_b128 v[232:235], v252 offset:30720\n" \
  "v_mfma_f32_16x16x32_bf16 v[200:203], v[240:243], v[236:239], v[200:203]\n" \
  "v_mfma_f32_16x16x32_bf16 v[204:207], v[244:247], v[236:239], v[204:207]\n" \
  "ds_read_b128 v[236:239], v252 offset:31744\n" \
  "ds_read_b128 v[240:243], v250 offset:49152\n" \
  "ds_read_b128 v[244:247], v250 offset:50176\n" \
  "s_waitcnt lgkmcnt(9)\n" \
  "v_mfma_f32_16x16x32_bf16 v[192:195], v[24:27], v[208:211], v[192:195]\n" \
  "v_mfma_f32_16x16x32_bf16 v[196:199], v[88:91], v[208:211], v[196:199]\n" \
  "s_waitcnt lgkmcnt(8)\n" \
  "v_mfma_f32_16x16x32_bf16 v[200:203], v[24:27], v[212:215], v[200:203]\n" \
  "v_mfma_f32_16x16x32_bf16 v[204:207], v[88:91], v[212:215], v[204:207]\n" \
  "s_waitcnt vmcnt(0)\n" \
  "s_waitcnt lgkmcnt(0)\n" \
  "s_barrier\n" \
  "ds_read_b128 v[208:211], v252 offset:0\n" \
  "ds_read_b128 v[212:215], v252 offset:1024\n" \
  "s_add_u32 m0, %[mw], 16384\n" \
  "s_xor_b32 %[cnt], %[sc], 768\n" \
  "buffer_load_dwordx4 v248, %[rx], %[cnt] offen lds\n" \
  "s_add_u32 m0, %[mw], 24576\n" \
  "s_xor_b32 %[cnt], %[sc], 896\n" \
  "buffer_load_dwordx4 v248, %[rx], %[cnt] offen lds\n" \
  "v_lshl_or_b32 v253, v251, 24, %[fm]\n" \
  "buffer_store_dword v253, v253, %[rs], %[fo] offen sc1\n" \
  "v_mfma_f32_16x16x32_bf16 v[192:195], v[152:155], v[216:219], v[192:195]\n" \
  "v_mfma_f32_16x16x32_bf16 v[196:199], v[156:159], v[216:219], v[196:199]\n" \
  "ds_read_b128 v[216:219], v252 offset:2048\n" \
  "v_mfma_f32_16x16x32_bf16 v[200:203], v[152:155], v[220:223], v[200:203]\n" \
  "v_mfma_f32_16x16x32_bf16 v[204:207], v[156:159], v[220:223], v[204:207]\n" \
  "ds_read_b128 v[220:223], v252 offset:3072\n" \
  "v_mfma_f32_16x16x32_bf16 v[192:195], v[28:31], v[224:227], v[192:195]\n" \
  "v_mfma_f32_16x16x32_bf16 v[196:199], v[92:95], v[224:227], v[196:199]\n" \
  "ds_read_b128 v[224:227], v252 offset:4096\n" \
  "v_mfma_f32_16x16x32_bf16 v[200:203], v[28:31], v[228:231], v[200:203]\n" \
  "v_mfma_f32_16x16x32_bf16 v[204:207], v[92:95], v[228:231], v[204:207]\n" \
  "ds_read_b128 v[228:231], v252 offset:5120\n" \
  "v_mfma_f32_16x16x32_bf16 v[192:195], v[240:243], v[232:235], v[192:195]\n" \
  "v_mfma_f32_16x16x32_bf16 v[196:199], v[244:247], v[232:235], v[196:199]\n" \
  "ds_read_b128 v[232:235], v252 offset:6144\n" \
  "v_mfma_f32_16x16x32_bf16 v[200:203], v[240:243], v[236:239], v[200:203]\n" \
  "v_mfma_f32_16x16x32_bf16 v[204:207], v[244:247], v[236:239], v[204:207]\n" \
  "ds_read_b128 v[236:239], v252 offset:7168\n" \
  "ds_read_b128 v[240:243], v249 offset:0\n" \
  "ds_read_b128 v[244:247], v249 offset:1024\n" \
  "s_waitcnt lgkmcnt(9)\n" \
  "v_mfma_f32_16x16x32_bf16 v[192:195], v[32:35], v[208:211], v[192:195]\n" \
  "v_mfma_f32_16x16x32_bf16 v[196:199], v[96:99], v[208:211], v[196:199]\n" \
  "ds_read_b128 v[208:211], v252 offset:8192\n" \
  "s_waitcnt lgkmcnt(9)\n" \
  "v_mfma_f32_16x16x32_bf16 v[200:203], v[32:35], v[212:215], v[200:203]\n" \
  "v_mfma_f32_16x16x32_bf16 v[204:207], v[96:99], v[212:215], v[204:207]\n" \
  "ds_read_b128 v[212:215], v252 offset:9216\n" \
  "s_waitcnt lgkmcnt(9)\n" \
  "v_mfma_f32_16x16x32_bf16 v[192:195], v[160:163], v[216:219], v[192:195]\n" \
  "v_mfma_f32_16x16x32_bf16 v[196:199], v[164:167], v[216:219], v[196:199]\n" \
  "ds_read_b128 v[216:219], v252 offset:10240\n" \
  "s_waitcnt lgkmcnt(9)\n" \
  "v_mfma_f32_16x16x32_bf16 v[200:203], v[160:163], v[220:223], v[200:203]\n" \
  "v_mfma_f32_16x16x32_bf16 v[204:207], v[164:167], v[220:223], v[204:207]\n" \
  "ds_read_b128 v[220:223], v252 offset:11264\n" \
  "s_waitcnt lgkmcnt(9)\n" \
  "v_mfma_f32_16x16x32_bf16 v[192:195], v[36:39], v[224:227], v[192:195]\n" \
  "v_mfma_f32_16x16x32_bf16 v[196:199], v[100:103], v[224:227], v[196:199]\n" \
  "ds_read_b128 v[224:227], v252 offset:12288\n" \
  "s_waitcnt lgkmcnt(9)\n" \
  "v_mfma_f32_16x16x32_bf16 v[200:203], v[36:39], v[228:231], v[200:203]\n" \
  "v_mfma_f32_16x16x32_bf16 v[204:207], v[100:103], v[228:231], v[204:207]\n" \
  "ds_read_b128 v[228:231], v252 offset:13312\n" \
  "s_waitcnt lgkmcnt(7)\n" \
  "v_mfma_f32_16x16x32_bf16 v[192:195], v[240:243], v[232:235], v[192:195]\n" \
  "s_waitcnt lgkmcnt(6)\n" \
  "v_mfma_f32_16x16x32_bf16 v[196:199], v[244:247], v[232:235], v[196:199]\n" \
  "ds_read_b128 v[232:235], v252 offset:14336\n" \
  "v_mfma_f32_16x16x32_bf16 v[200:203], v[240:243], v[236:239], v[200:203]\n" \
  "v_mfma_f32_16x16x32_bf16 v[204:207], v[244:247], v[236:239], v[204:207]\n" \
  "ds_read_b128 v[236:239], v252 offset:15360\n" \
  "ds_read_b128 v[240:243], v249 offset:16384\n" \
  "ds_read_b128 v[244:247], v249 offset:17408\n" \
  "s_waitcnt lgkmcnt(9)\n" \
  "v_mfma_f32_16x16x32_bf16 v[192:195], v[40:43], v[208:211], v[192:195]\n" \
  "v_mfma_f32_16x16x32_bf16 v[196:199], v[104:107], v[208:211], v[196:199]\n" \
  "s_waitcnt lgkmcnt(8)\n" \
  "v_mfma_f32_16x16x32_bf16 v[200:203], v[40:43], v[212:215], v[200:203]\n" \
  "v_mfma_f32_16x16x32_bf16 v[204:207], v[104:107], v[212:215], v[204:207]\n" \
  "s_waitcnt vmcnt(1)\n" \
  "s_waitcnt lgkmcnt(0)\n" \
  "s_barrier\n" \
  "ds_read_b128 v[208:211], v252 offset:16384\n" \
  "ds_read_b128 v[212:215], v252 offset:17408\n" \
  "s_add_u32 m0, %[mw], 0\n" \
  "s_xor_b32 %[cnt], %[sn], 0\n" \
  "buffer_load_dwordx4 v248, %[rx], %[cnt] offen lds\n" \
  "s_add_u32 m0, %[mw], 8192\n" \
  "s_xor_b32 %[cnt], %[sn], 128\n" \
  "buffer_load_dwordx4 v248, %[rx], %[cnt] offen lds\n" \
  "v_lshl_or_b32 v253, v251, 3, %[tm]\n" \
  "buffer_load_dword v253, v253, %[rx], %[tc] offen\n" \
  "v_mfma_f32_16x16x32_bf16 v[192:195], v[168:171], v[216:219], v[192:195]\n" \
  "v_mfma_f32_16x16x32_bf16 v[196:199], v[172:175], v[216:219], v[196:199]\n" \
  "ds_read_b128 v[216:219], v252 offset:18432\n" \
  "v_mfma_f32_16x16x32_bf16 v[200:203], v[168:171], v[220:223], v[200:203]\n" \
  "v_mfma_f32_16x16x32_bf16 v[204:207], v[172:175], v[220:223], v[204:207]\n" \
  "ds_read_b128 v[220:223], v252 offset:19456\n" \
  "v_mfma_f32_16x16x32_bf16 v[192:195], v[44:47], v[224:227], v[192:195]\n" \
  "v_mfma_f32_16x16x32_bf16 v[196:199], v[108:111], v[224:227], v[196:199]\n" \
  "ds_read_b128 v[224:227], v252 offset:20480\n" \
  "v_mfma_f32_16x16x32_bf16 v[200:203], v[44:47], v[228:231], v[200:203]\n" \
  "v_mfma_f32_16x16x32_bf16 v[204:207], v[108:111], v[228:231], v[204:207]\n" \
  "ds_read_b128 v[228:231], v252 offset:21504\n" \
  "v_mfma_f32_16x16x32_bf16 v[192:195], v[240:243], v[232:235], v[192:195]\n" \
  "v_mfma_f32_16x16x32_bf16 v[196:199], v[244:247], v[232:235], v[196:199]\n" \
  "ds_read_b128 v[232:235], v252 offset:22528\n" \
  "v_mfma_f32_16x16x32_bf16 v[200:203], v[240:243], v[236:239], v[200:203]\n" \
  "v_mfma_f32_16x16x32_bf16 v[204:207], v[244:247], v[236:239], v[204:207]\n" \
  "ds_read_b128 v[236:239], v252 offset:23552\n" \
  "ds_read_b128 v[240:243], v249 offset:32768\n" \
  "ds_read_b128 v[244:247], v249 offset:33792\n" \
  "s_waitcnt lgkmcnt(9)\n" \
  "v_mfma_f32_16x16x32_bf16 v[192:195], v[48:51], v[208:211], v[192:195]\n" \
  "v_mfma_f32_16x16x32_bf16 v[196:199], v[112:115], v[208:211], v[196:199]\n" \
  "ds_read_b128 v[208:211], v252 offset:24576\n" \
  "s_waitcnt lgkmcnt(9)\n" \
  "v_mfma_f32_16x16x32_bf16 v[200:203], v[48:51], v[212:215], v[200:203]\n" \
  "v_mfma_f32_16x16x32_bf16 v[204:207], v[112:115], v[212:215], v[204:207]\n" \
  "ds_read_b128 v[212:215], v252 offset:25600\n" \
  "s_waitcnt lgkmcnt(9)\n" \
  "v_mfma_f32_16x16x32_bf16 v[192:195], v[176:179], v[216:219], v[192:195]\n" \
  "v_mfma_f32_16x16x32_bf16 v[196:199], v[180:183], v[216:219], v[196:199]\n" \
  "ds_read_b128 v[216:219], v252 offset:26624\n" \
  "s_waitcnt lgkmcnt(9)\n" \
  "v_mfma_f32_16x16x32_bf16 v[200:203], v[176:179], v[220:223], v[200:203]\n" \
  "v_mfma_f32_16x16x32_bf16 v[204:207], v[180:183], v[220:223], v[204:207]\n" \
  "ds_read_b128 v[220:223], v252 offset:27648\n" \
  "s_waitcnt lgkmcnt(9)\n" \
  "v_mfma_f32_16x16x32_bf16 v[192:195], v[52:55], v[224:227], v[192:195]\n" \
  "v_mfma_f32_16x16x32_bf16 v[196:199], v[116:119], v[224:227], v[196:199]\n" \
  "ds_read_b128 v[224:227], v252 offset:28672\n" \
  "s_waitcnt lgkmcnt(9)\n" \
  "v_mfma_f32_16x16x32_bf16 v[200:203], v[52:55], v[228:231], v[200:203]\n" \
  "v_mfma_f32_16x16x32_bf16 v[204:207], v[116:119], v[228:231], v[204:207]\n" \
  "ds_read_b128 v[228:231], v252 offset:29696\n" \
  "s_waitcnt lgkmcnt(7)\n" \
  "v_mfma_f32_16x16x32_bf16 v[192:195], v[240:243], v[232:235], v[192:195]\n" \
  "s_waitcnt lgkmcnt(6)\n" \
  "v_mfma_f32_16x16x32_bf16 v[196:199], v[244:247], v[232:235], v[196:199]\n" \
  "ds_read_b128 v[232:235], v252 offset:30720\n" \
  "v_mfma_f32_16x16x32_bf16 v[200:203], v[240:243], v[236:239], v[200:203]\n" \
  "v_mfma_f32_16x16x32_bf16 v[204:207], v[244:247], v[236:239], v[204:207]\n" \
  "ds_read_b128 v[236:239], v252 offset:31744\n" \
  "ds_read_b128 v[240:243], v249 offset:49152\n" \
  "ds_read_b128 v[244:247], v249 offset:50176\n" \
  "s_waitcnt lgkmcnt(9)\n" \
  "v_mfma_f32_16x16x32_bf16 v[192:195], v[56:59], v[208:211], v[192:195]\n" \
  "v_mfma_f32_16x16x32_bf16 v[196:199], v[120:123], v[208:211], v[196:199]\n" \
  "s_waitcnt lgkmcnt(8)\n" \
  "v_mfma_f32_16x16x32_bf16 v[200:203], v[56:59], v[212:215], v[200:203]\n" \
  "v_mfma_f32_16x16x32_bf16 v[204:207], v[120:123], v[212:215], v[204:207]\n" \
  "s_waitcnt vmcnt(1)\n" \
  "s_waitcnt lgkmcnt(0)\n" \
  "s_barrier\n" \
  "ds_read_b128 v[208:211], v252 offset:0\n" \
  "ds_read_b128 v[212:215], v252 offset:1024\n" \
  "s_add_u32 m0, %[mw], 16384\n" \
  "s_xor_b32 %[cnt], %[sn], 256\n" \
  "buffer_load_dwordx4 v248, %[rx], %[cnt] offen lds\n" \
  "s_add_u32 m0, %[mw], 24576\n" \
  "s_xor_b32 %[cnt], %[sn], 384\n" \
  "buffer_load_dwordx4 v248, %[rx], %[cnt] offen lds\n" \
  "v_mfma_f32_16x16x32_bf16 v[192:195], v[184:187], v[216:219], v[192:195]\n" \
  "v_mfma_f32_16x16x32_bf16 v[196:199], v[188:191], v[216:219], v[196:199]\n" \
  "ds_read_b128 v[216:219], v252 offset:2048\n" \
  "v_mfma_f32_16x16x32_bf16 v[200:203], v[184:187], v[220:223], v[200:203]\n" \
  "v_mfma_f32_16x16x32_bf16 v[204:207], v[188:191], v[220:223], v[204:207]\n" \
  "ds_read_b128 v[220:223], v252 offset:3072\n" \
  "v_mfma_f32_16x16x32_bf16 v[192:195], v[60:63], v[224:227], v[192:195]\n" \
  "v_mfma_f32_16x16x32_bf16 v[196:199], v[124:127], v[224:227], v[196:199]\n" \
  "ds_read_b128 v[224:227], v252 offset:4096\n" \
  "v_mfma_f32_16x16x32_bf16 v[200:203], v[60:63], v[228:231], v[200:203]\n" \
  "v_mfma_f32_16x16x32_bf16 v[204:207], v[124:127], v[228:231], v[204:207]\n" \
  "ds_read_b128 v[228:231], v252 offset:5120\n" \
  "v_mfma_f32_16x16x32_bf16 v[192:195], v[240:243], v[232:235], v[192:195]\n" \
  "v_mfma_f32_16x16x32_bf16 v[196:199], v[244:247], v[232:235], v[196:199]\n" \
  "ds_read_b128 v[232:235], v252 offset:6144\n" \
  "v_mfma_f32_16x16x32_bf16 v[200:203], v[240:243], v[236:239], v[200:203]\n" \
  "v_mfma_f32_16x16x32_bf16 v[204:207], v[244:247], v[236:239], v[204:207]\n" \
  "ds_read_b128 v[236:239], v252 offset:7168\n" \
  "v_cvt_pk_bf16_f32 v192, v192, v193\n" \
  "v_cvt_pk_bf16_f32 v193, v194, v195\n" \
  "v_cvt_pk_bf16_f32 v194, v196, v197\n" \
  "v_cvt_pk_bf16_f32 v195, v198, v199\n" \
  "buffer_store_dwordx4 v[192:195], v251, %[rs], %[so] offen sc1\n" \
  "v_cvt_pk_bf16_f32 v200, v200, v201\n" \
  "v_cvt_pk_bf16_f32 v201, v202, v203\n" \
  "s_nop 0\n" \
  "v_cvt_pk_bf16_f32 v202, v204, v205\n" \
  "v_cvt_pk_bf16_f32 v203, v206, v207\n" \
  "s_add_u32 %[cnt], %[so], %c[rt]\n" \
  "buffer_store_dwordx4 v[200:203], v251, %[rs], %[cnt] offen sc1\n" \
  "s_mov_b32 m0, %[keep]\n" \
  "s_waitcnt lgkmcnt(0)\n"
#define ST_XA \
  "s_mov_b32 %[keep], m0\n" \
  "ds_read_b128 v[240:243], v250 offset:0\n" \
  "ds_read_b128 v[244:247], v250 offset:1024\n" \
  "v_mfma_f32_16x16x32_bf16 v[192:195], v[0:3], v[208:211], 0\n" \
  "v_mfma_f32_16x16x32_bf16 v[196:199], v[64:67], v[208:211], 0\n" \
  "ds_read_b128 v[208:211], v252 offset:8192\n" \
  "v_mfma_f32_16x16x32_bf16 v[200:203], v[0:3], v[212:215], 0\n" \
  "v_mfma_f32_16x16x32_bf16 v[204:207], v[64:67], v[212:215], 0\n" \
  "ds_read_b128 v[212:215], v252 offset:9216\n" \
  "v_mfma_f32_16x16x32_bf16 v[192:195], v[128:131], v[216:219], v[192:195]\n" \
  "v_mfma_f32_16x16x32_bf16 v[196:199], v[132:135], v[216:219], v[196:199]\n" \
  "ds_read_b128 v[216:219], v252 offset:10240\n" \
  "v_mfma_f32_16x16x32_bf16 v[200:203], v[128:131], v[220:223], v[200:203]\n" \
  "v_mfma_f32_16x16x32_bf16 v[204:207], v[132:135], v[220:223], v[204:207]\n" \
  "ds_read_b128 v[220:223], v252 offset:11264\n" \
  "v_mfma_f32_16x16x32_bf16 v[192:195], v[4:7], v[224:227], v[192:195]\n" \
  "v_mfma_f32_16x16x32_bf16 v[196:199], v[68:71], v[224:227], v[196:199]\n" \
  "v_mfma_f32_16x16x32_bf16 v[200:203], v[4:7], v[228:231], v[200:203]\n" \
  "v_mfma_f32_16x16x32_bf16 v[204:207], v[68:71], v[228:231], v[204:207]\n" \
  "s_waitcnt vmcnt(0)\n" \
  "s_waitcnt lgkmcnt(0)\n" \
  "s_barrier\n" \
  "ds_read_b128 v[224:227], v252 offset:12288\n" \
  "ds_read_b128 v[228:231], v252 offset:13312\n" \
  "s_add_u32 m0, %[mw], 0\n" \
  "s_xor_b32 %[cnt], %[sc], 384\n" \
  "buffer_load_dwordx4 v248, %[rx], %[cnt] offen lds\n" \
  "s_add_u32 m0, %[mw], 4096\n" \
  "s_xor_b32 %[cnt], %[sc], 448\n" \
  "buffer_load_dwordx4 v248, %[rx], %[cnt] offen lds\n" \
  "v_mfma_f32_16x16x32_bf16 v[192:195], v[240:243], v[232:235], v[192:195]\n" \
  "v_mfma_f32_16x16x32_bf16 v[196:199], v[244:247], v[232:235], v[196:199]\n" \
  "ds_read_b128 v[232:235], v252 offset:14336\n" \
  "v_mfma_f32_16x16x32_bf16 v[200:203], v[240:243], v[236:239], v[200:203]\n" \
  "v_mfma_f32_16x16x32_bf16 v[204:207], v[244:247], v[236:239], v[204:207]\n" \
  "ds_read_b128 v[236:239], v252 offset:15360\n" \
  "ds_read_b128 v[240:243], v250 offset:16384\n" \
  "ds_read_b128 v[244:247], v250 offset:17408\n" \
  "v_mfma_f32_16x16x32_bf16 v[192:195], v[8:11], v[208:211], v[192:195]\n" \
  "v_mfma_f32_16x16x32_bf16 v[196:199], v[72:75], v[208:211], v[196:199]\n" \
  "ds_read_b128 v[208:211], v252 offset:16384\n" \
  "v_mfma_f32_16x16x32_bf16 v[200:203], v[8:11], v[212:215], v[200:203]\n" \
  "v_mfma_f32_16x16x32_bf16 v[204:207], v[72:75], v[212:215], v[204:207]\n" \
  "ds_read_b128 v[212:215], v252 offset:17408\n" \
  "v_mfma_f32_16x16x32_bf16 v[192:195], v[136:139], v[216:219], v[192:195]\n" \
  "v_mfma_f32_16x16x32_bf16 v[196:199], v[140:143], v[216:219], v[196:199]\n" \
  "ds_read_b128 v[216:219], v252 offset:18432\n" \
  "v_mfma_f32_16x16x32_bf16 v[200:203], v[136:139], v[220:223], v[200:203]\n" \
  "v_mfma_f32_16x16x32_bf16 v[204:207], v[140:143], v[220:223], v[204:207]\n" \
  "ds_read_b128 v[220:223], v252 offset:19456\n" \
  "s_waitcnt lgkmcnt(9)\n" \
  "v_mfma_f32_16x16x32_bf16 v[192:195], v[12:15], v[224:227], v[192:195]\n" \
  "v_mfma_f32_16x16x32_bf16 v[196:199], v[76:79], v[224:227], v[196:199]\n" \
  "ds_read_b128 v[224:227], v252 offset:20480\n" \
  "s_waitcnt lgkmcnt(9)\n" \
  "v_mfma_f32_16x16x32_bf16 v[200:203], v[12:15], v[228:231], v[200:203]\n" \
  "v_mfma_f32_16x16x32_bf16 v[204:207], v[76:79], v[228:231], v[204:207]\n" \
  "ds_read_b128 v[228:231], v252 offset:21504\n" \
  "s_waitcnt lgkmcnt(7)\n" \
  "v_mfma_f32_16x16x32_bf16 v[192:195], v[240:243], v[232:235], v[192:195]\n" \
  "s_waitcnt lgkmcnt(6)\n" \
  "v_mfma_f32_16x16x32_bf16 v[196:199], v[244:247], v[232:235], v[196:199]\n" \
  "ds_read_b128 v[232:235], v252 offset:22528\n" \
  "v_mfma_f32_16x16x32_bf16 v[200:203], v[240:243], v[236:239], v[200:203]\n" \
  "v_mfma_f32_16x16x32_bf16 v[204:207], v[244:247], v[236:239], v[204:207]\n" \
  "ds_read_b128 v[236:239], v252 offset:23552\n" \
  "ds_read_b128 v[240:243], v250 offset:32768\n" \
  "ds_read_b128 v[244:247], v250 offset:33792\n" \
  "s_waitcnt lgkmcnt(9)\n" \
  "v_mfma_f32_16x16x32_bf16 v[192:195], v[16:19], v[208:211], v[192:195]\n" \
  "v_mfma_f32_16x16x32_bf16 v[196:199], v[80:83], v[208:211], v[196:199]\n" \
  "s_waitcnt lgkmcnt(8)\n" \
  "v_mfma_f32_16x16x32_bf16 v[200:203], v[16:19], v[212:215], v[200:203]\n" \
  "v_mfma_f32_16x16x32_bf16 v[204:207], v[80:83], v[212:215], v[204:207]\n" \
  "s_waitcnt vmcnt(0)\n" \
  "s_waitcnt lgkmcnt(0)\n" \
  "s_barrier\n" \
  "ds_read_b128 v[208:211], v252 offset:0\n" \
  "ds_read_b128 v[212:215], v252 offset:1024\n" \
  "s_add_u32 m0, %[mw], 12288\n" \
  "s_xor_b32 %[cnt], %[sc], 512\n" \
  "buffer_load_dwordx4 v248, %[rx], %[cnt] offen lds\n" \
  "s_add_u32 m0, %[mw], 16384\n" \
  "s_xor_b32 %[cnt], %[sc], 576\n" \
  "buffer_load_dwordx4 v248, %[rx], %[cnt] offen lds\n" \
  "s_add_u32 m0, %[mw], 20480\n" \
  "s_xor_b32 %[cnt], %[sc], 640\n" \
  "buffer_load_dwordx4 v248, %[rx], %[cnt] offen lds\n" \
  "v_mfma_f32_16x16x32_bf16 v[192:195], v[144:147], v[216:219], v[192:195]\n" \
  "v_mfma_f32_16x16x32_bf16 v[196:199], v[148:151], v[216:219], v[196:199]\n" \
  "ds_read_b128 v[216:219], v252 offset:2048\n" \
  "v_mfma_f32_16x16x32_bf16 v[200:203], v[144:147], v[220:223], v[200:203]\n" \
  "v_mfma_f32_16x16x32_bf16 v[204:207], v[148:151], v[220:223], v[204:207]\n" \
  "ds_read_b128 v[220:223], v252 offset:3072\n" \
  "v_mfma_f32_16x16x32_bf16 v[192:195], v[20:23], v[224:227], v[192:195]\n" \
  "v_mfma_f32_16x16x32_bf16 v[196:199], v[84:87], v[224:227], v[196:199]\n" \
  "ds_read_b128 v[224:227], v252 offset:4096\n" \
  "v_mfma_f32_16x16x32_bf16 v[200:203], v[20:23], v[228:231], v[200:203]\n" \
  "v_mfma_f32_16x16x32_bf16 v[204:207], v[84:87], v[228:231], v[204:207]\n" \
  "ds_read_b128 v[228:231], v252 offset:5120\n" \
  "v_mfma_f32_16x16x32_bf16 v[192:195], v[240:243], v[232:235], v[192:195]\n" \
  "v_mfma_f32_16x16x32_bf16 v[196:199], v[244:247], v[232:235], v[196:199]\n" \
  "ds_read_b128 v[232:235], v252 offset:6144\n" \
  "v_mfma_f32_16x16x32_bf16 v[200:203], v[240:243], v[236:239], v[200:203]\n" \
  "v_mfma_f32_16x16x32_bf16 v[204:207], v[244:247], v[236:239], v[204:207]\n" \
  "ds_read_b128 v[236:239], v252 offset:7168\n" \
  "ds_read_b128 v[240:243], v250 offset:49152\n" \
  "ds_read_b128 v[244:247], v250 offset:50176\n" \
  "s_waitcnt lgkmcnt(9)\n" \
  "v_mfma_f32_16x16x32_bf16 v[192:195], v[24:27], v[208:211], v[192:195]\n" \
  "v_mfma_f32_16x16x32_bf16 v[196:199], v[88:91], v[208:211], v[196:199]\n" \
  "s_waitcnt lgkmcnt(8)\n" \
  "v_mfma_f32_16x16x32_bf16 v[200:203], v[24:27], v[212:215], v[200:203]\n" \
  "v_mfma_f32_16x16x32_bf16 v[204:207], v[88:91], v[212:215], v[204:207]\n" \
  "s_waitcnt vmcnt(0)\n" \
  "s_waitcnt lgkmcnt(0)\n" \
  "s_barrier\n" \
  "ds_read_b128 v[208:211], v252 offset:12288\n" \
  "ds_read_b128 v[212:215], v252 offset:13312\n" \
  "s_add_u32 m0, %[mw], 0\n" \
  "s_xor_b32 %[cnt], %[sc], 704\n" \
  "buffer_load_dwordx4 v248, %[rx], %[cnt] offen lds\n" \
  "s_add_u32 m0, %[mw], 4096\n" \
  "s_xor_b32 %[cnt], %[sc], 768\n" \
  "buffer_load_dwordx4 v248, %[rx], %[cnt] offen lds\n" \
  "s_add_u32 m0, %[mw], 8192\n" \
  "s_xor_b32 %[cnt], %[sc], 832\n" \
  "buffer_load_dwordx4 v248, %[rx], %[cnt] offen lds\n" \
  "v_lshl_or_b32 v253, v251, 3, %[tm]\n" \
  "buffer_load_dword v253, v253, %[rx], %[tc] offen\n" \
  "v_mfma_f32_16x16x32_bf16 v[192:195], v[152:155], v[216:219], v[192:195]\n" \
  "v_mfma_f32_16x16x32_bf16 v[196:199], v[156:159], v[216:219], v[196:199]\n" \
  "ds_read_b128 v[216:219], v252 offset:14336\n" \
  "v_mfma_f32_16x16x32_bf16 v[200:203], v[152:155], v[220:223], v[200:203]\n" \
  "v_mfma_f32_16x16x32_bf16 v[204:207], v[156:159], v[220:223], v[204:207]\n" \
  "ds_read_b128 v[220:223], v252 offset:15360\n" \
  "v_mfma_f32_16x16x32_bf16 v[192:195], v[28:31], v[224:227], v[192:195]\n" \
  "v_mfma_f32_16x16x32_bf16 v[196:199], v[92:95], v[224:227], v[196:199]\n" \
  "ds_read_b128 v[224:227], v252 offset:16384\n" \
  "v_mfma_f32_16x16x32_bf16 v[200:203], v[28:31], v[228:231], v[200:203]\n" \
  "v_mfma_f32_16x16x32_bf16 v[204:207], v[92:95], v[228:231], v[204:207]\n" \
  "ds_read_b128 v[228:231], v252 offset:17408\n" \
  "v_mfma_f32_16x16x32_bf16 v[192:195], v[240:243], v[232:235], v[192:195]\n" \
  "v_mfma_f32_16x16x32_bf16 v[196:199], v[244:247], v[232:235], v[196:199]\n" \
  "ds_read_b128 v[232:235], v252 offset:18432\n" \
  "v_mfma_f32_16x16x32_bf16 v[200:203], v[240:243], v[236:239], v[200:203]\n" \
  "v_mfma_f32_16x16x32_bf16 v[204:207], v[244:247], v[236:239], v[204:207]\n" \
  "ds_read_b128 v[236:239], v252 offset:19456\n" \
  "ds_read_b128 v[240:243], v249 offset:0\n" \
  "ds_read_b128 v[244:247], v249 offset:1024\n" \
  "s_waitcnt lgkmcnt(9)\n" \
  "v_mfma_f32_16x16x32_bf16 v[192:195], v[32:35], v[208:211], v[192:195]\n" \
  "v_mfma_f32_16x16x32_bf16 v[196:199], v[96:99], v[208:211], v[196:199]\n" \
  "ds_read_b128 v[208:211], v252 offset:20480\n" \
  "s_waitcnt lgkmcnt(9)\n" \
  "v_mfma_f32_16x16x32_bf16 v[200:203], v[32:35], v[212:215], v[200:203]\n" \
  "v_mfma_f32_16x16x32_bf16 v[204:207], v[96:99], v[212:215], v[204:207]\n" \
  "ds_read_b128 v[212:215], v252 offset:21504\n" \
  "s_waitcnt lgkmcnt(9)\n" \
  "v_mfma_f32_16x16x32_bf16 v[192:195], v[160:163], v[216:219], v[192:195]\n" \
  "v_mfma_f32_16x16x32_bf16 v[196:199], v[164:167], v[216:219], v[196:199]\n" \
  "ds_read_b128 v[216:219], v252 offset:22528\n" \
  "s_waitcnt lgkmcnt(9)\n" \
  "v_mfma_f32_16x16x32_bf16 v[200:203], v[160:163], v[220:223], v[200:203]\n" \
  "v_mfma_f32_16x16x32_bf16 v[204:207], v[164:167], v[220:223], v[204:207]\n" \
  "ds_read_b128 v[220:223], v252 offset:23552\n" \
  "s_waitcnt lgkmcnt(9)\n" \
  "v_mfma_f32_16x16x32_bf16 v[192:195], v[36:39], v[224:227], v[192:195]\n" \
  "v_mfma_f32_16x16x32_bf16 v[196:199], v[100:103], v[224:227], v[196:199]\n" \
  "s_waitcnt lgkmcnt(8)\n" \
  "v_mfma_f32_16x16x32_bf16 v[200:203], v[36:39], v[228:231], v[200:203]\n" \
  "v_mfma_f32_16x16x32_bf16 v[204:207], v[100:103], v[228:231], v[204:207]\n" \
  "s_waitcnt vmcnt(1)\n" \
  "s_waitcnt lgkmcnt(0)\n" \
  "s_barrier\n" \
  "ds_read_b128 v[224:227], v252 offset:0\n" \
  "ds_read_b128 v[228:231], v252 offset:1024\n" \
  "s_add_u32 m0, %[mw], 12288\n" \
  "s_xor_b32 %[cnt], %[sc], 896\n" \
  "buffer_load_dwordx4 v248, %[rx], %[cnt] offen lds\n" \
  "s_add_u32 m0, %[mw], 16384\n" \
  "s_xor_b32 %[cnt], %[sc], 960\n" \
  "buffer_load_dwordx4 v248, %[rx], %[cnt] offen lds\n" \
  "v_mfma_f32_16x16x32_bf16 v[192:195], v[240:243], v[232:235], v[192:195]\n" \
  "v_mfma_f32_16x16x32_bf16 v[196:199], v[244:247], v[232:235], v[196:199]\n" \
  "ds_read_b128 v[232:235], v252 offset:2048\n" \
  "v_mfma_f32_16x16x32_bf16 v[200:203], v[240:243], v[236:239], v[200:203]\n" \
  "v_mfma_f32_16x16x32_bf16 v[204:207], v[244:247], v[236:239], v[204:207]\n" \
  "ds_read_b128 v[236:239], v252 offset:3072\n" \
  "ds_read_b128 v[240:243], v249 offset:16384\n" \
  "ds_read_b128 v[244:247], v249 offset:17408\n" \
  "v_mfma_f32_16x16x32_bf16 v[192:195], v[40:43], v[208:211], v[192:195]\n" \
  "v_mfma_f32_16x16x32_bf16 v[196:199], v[104:107], v[208:211], v[196:199]\n" \
  "ds_read_b128 v[208:211], v252 offset:4096\n" \
  "v_mfma_f32_16x16x32_bf16 v[200:203], v[40:43], v[212:215], v[200:203]\n" \
  "v_mfma_f32_16x16x32_bf16 v[204:207], v[104:107], v[212:215], v[204:207]\n" \
  "ds_read_b128 v[212:215], v252 offset:5120\n" \
  "v_mfma_f32_16x16x32_bf16 v[192:195], v[168:171], v[216:219], v[192:195]\n" \
  "v_mfma_f32_16x16x32_bf16 v[196:199], v[172:175], v[216:219], v[196:199]\n" \
  "ds_read_b128 v[216:219], v252 offset:6144\n" \
  "v_mfma_f32_16x16x32_bf16 v[200:203], v[168:171], v[220:223], v[200:203]\n" \
  "v_mfma_f32_16x16x32_bf16 v[204:207], v[172:175], v[220:223], v[204:207]\n" \
  "ds_read_b128 v[220:223], v252 offset:7168\n" \
  "s_waitcnt lgkmcnt(9)\n" \
  "v_mfma_f32_16x16x32_bf16 v[192:195], v[44:47], v[224:227], v[192:195]\n" \
  "v_mfma_f32_16x16x32_bf16 v[196:199], v[108:111], v[224:227], v[196:199]\n" \
  "ds_read_b128 v[224:227], v252 offset:8192\n" \
  "s_waitcnt lgkmcnt(9)\n" \
  "v_mfma_f32_16x16x32_bf16 v[200:203], v[44:47], v[228:231], v[200:203]\n" \
  "v_mfma_f32_16x16x32_bf16 v[204:207], v[108:111], v[228:231], v[204:207]\n" \
  "ds_read_b128 v[228:231], v252 offset:9216\n" \
  "s_waitcnt lgkmcnt(7)\n" \
  "v_mfma_f32_16x16x32_bf16 v[192:195], v[240:243], v[232:235], v[192:195]\n" \
  "s_waitcnt lgkmcnt(6)\n" \
  "v_mfma_f32_16x16x32_bf16 v[196:199], v[244:247], v[232:235], v[196:199]\n" \
  "ds_read_b128 v[232:235], v252 offset:10240\n" \
  "v_mfma_f32_16x16x32_bf16 v[200:203], v[240:243], v[236:239], v[200:203]\n" \
  "v_mfma_f32_16x16x32_bf16 v[204:207], v[244:247], v[236:239], v[204:207]\n" \
  "ds_read_b128 v[236:239], v252 offset:11264\n" \
  "ds_read_b128 v[240:243], v249 offset:32768\n" \
  "ds_read_b128 v[244:247], v249 offset:33792\n" \
  "s_waitcnt lgkmcnt(9)\n" \
  "v_mfma_f32_16x16x32_bf16 v[192:195], v[48:51], v[208:211], v[192:195]\n" \
  "v_mfma_f32_16x16x32_bf16 v[196:199], v[112:115], v[208:211], v[196:199]\n" \
  "s_waitcnt lgkmcnt(8)\n" \
  "v_mfma_f32_16x16x32_bf16 v[200:203], v[48:51], v[212:215], v[200:203]\n" \
  "v_mfma_f32_16x16x32_bf16 v[204:207], v[112:115], v[212:215], v[204:207]\n" \
  "s_waitcnt vmcnt(0)\n" \
  "s_waitcnt lgkmcnt(0)\n" \
  "s_barrier\n" \
  "ds_read_b128 v[208:211], v252 offset:12288\n" \
  "ds_read_b128 v[212:215], v252 offset:13312\n" \
  "s_add_u32 m0, %[mw], 0\n" \
  "s_xor_b32 %[cnt], %[sn], 0\n" \
  "buffer_load_dwordx4 v248, %[rx], %[cnt] offen lds\n" \
  "s_add_u32 m0, %[mw], 4096\n" \
  "s_xor_b32 %[cnt], %[sn], 64\n" \
  "buffer_load_dwordx4 v248, %[rx], %[cnt] offen lds\n" \
  "s_add_u32 m0, %[mw], 8192\n" \
  "s_xor_b32 %[cnt], %[sn], 128\n" \
  "buffer_load_dwordx4 v248, %[rx], %[cnt] offen lds\n" \
  "v_mfma_f32_16x16x32_bf16 v[192:195], v[176:179], v[216:219], v[192:195]\n" \
  "v_mfma_f32_16x16x32_bf16 v[196:199], v[180:183], v[216:219], v[196:199]\n" \
  "ds_read_b128 v[216:219], v252 offset:14336\n" \
  "v_mfma_f32_16x16x32_bf16 v[200:203], v[176:179], v[220:223], v[200:203]\n" \
  "v_mfma_f32_16x16x32_bf16 v[204:207], v[180:183], v[220:223], v[204:207]\n" \
  "ds_read_b128 v[220:223], v252 offset:15360\n" \
  "v_mfma_f32_16x16x32_bf16 v[192:195], v[52:55], v[224:227], v[192:195]\n" \
  "v_mfma_f32_16x16x32_bf16 v[196:199], v[116:119], v[224:227], v[196:199]\n" \
  "ds_read_b128 v[224:227], v252 offset:16384\n" \
  "v_mfma_f32_16x16x32_bf16 v[200:203], v[52:55], v[228:231], v[200:203]\n" \
  "v_mfma_f32_16x16x32_bf16 v[204:207], v[116:119], v[228:231], v[204:207]\n" \
  "ds_read_b128 v[228:231], v252 offset:17408\n" \
  "v_mfma_f32_16x16x32_bf16 v[192:195], v[240:243], v[232:235], v[192:195]\n" \
  "v_mfma_f32_16x16x32_bf16 v[196:199], v[244:247], v[232:235], v[196:199]\n" \
  "ds_read_b128 v[232:235], v252 offset:18432\n" \
  "v_mfma_f32_16x16x32_bf16 v[200:203], v[240:243], v[236:239], v[200:203]\n" \
  "v_mfma_f32_16x16x32_bf16 v[204:207], v[244:247], v[236:239], v[204:207]\n" \
  "ds_read_b128 v[236:239], v252 offset:19456\n" \
  "ds_read_b128 v[240:243], v249 offset:49152\n" \
  "ds_read_b128 v[244:247], v249 offset:50176\n" \
  "s_waitcnt lgkmcnt(9)\n" \
  "v_mfma_f32_16x16x32_bf16 v[192:195], v[56:59], v[208:211], v[192:195]\n" \
  "v_mfma_f32_16x16x32_bf16 v[196:199], v[120:123], v[208:211], v[196:199]\n" \
  "s_waitcnt lgkmcnt(8)\n" \
  "v_mfma_f32_16x16x32_bf16 v[200:203], v[56:59], v[212:215], v[200:203]\n" \
  "v_mfma_f32_16x16x32_bf16 v[204:207], v[120:123], v[212:215], v[204:207]\n" \
  "s_waitcnt vmcnt(0)\n" \
  "s_waitcnt lgkmcnt(0)\n" \
  "s_barrier\n" \
  "ds_read_b128 v[208:211], v252 offset:0\n" \
  "ds_read_b128 v[212:215], v252 offset:1024\n" \
  "s_add_u32 m0, %[mw], 12288\n" \
  "s_xor_b32 %[cnt], %[sn], 192\n" \
  "buffer_load_dwordx4 v248, %[rx], %[cnt] offen lds\n" \
  "s_add_u32 m0, %[mw], 16384\n" \
  "s_xor_b32 %[cnt], %[sn], 256\n" \
  "buffer_load_dwordx4 v248, %[rx], %[cnt] offen lds\n" \
  "s_add_u32 m0, %[mw], 20480\n" \
  "s_xor_b32 %[cnt], %[sn], 320\n" \
  "buffer_load_dwordx4 v248, %[rx], %[cnt] offen lds\n" \
  "v_mfma_f32_16x16x32_bf16 v[192:195], v[184:187], v[216:219], v[192:195]\n" \
  "v_mfma_f32_16x16x32_bf16 v[196:199], v[188:191], v[216:219], v[196:199]\n" \
  "ds_read_b128 v[216:219], v252 offset:2048\n" \
  "v_mfma_f32_16x16x32_bf16 v[200:203], v[184:187], v[220:223], v[200:203]\n" \
  "v_mfma_f32_16x16x32_bf16 v[204:207], v[188:191], v[220:223], v[204:207]\n" \
  "ds_read_b128 v[220:223], v252 offset:3072\n" \
  "v_mfma_f32_16x16x32_bf16 v[192:195], v[60:63], v[224:227], v[192:195]\n" \
  "v_mfma_f32_16x16x32_bf16 v[196:199], v[124:127], v[224:227], v[196:199]\n" \
  "ds_read_b128 v[224:227], v252 offset:4096\n" \
  "v_mfma_f32_16x16x32_bf16 v[200:203], v[60:63], v[228:231], v[200:203]\n" \
  "v_mfma_f32_16x16x32_bf16 v[204:207], v[124:127], v[228:231], v[204:207]\n" \
  "ds_read_b128 v[228:231], v252 offset:5120\n" \
  "v_mfma_f32_16x16x32_bf16 v[192:195], v[240:243], v[232:235], v[192:195]\n" \
  "v_mfma_f32_16x16x32_bf16 v[196:199], v[244:247], v[232:235], v[196:199]\n" \
  "ds_read_b128 v[232:235], v252 offset:6144\n" \
  "v_mfma_f32_16x16x32_bf16 v[200:203], v[240:243], v[236:239], v[200:203]\n" \
  "v_mfma_f32_16x16x32_bf16 v[204:207], v[244:247], v[236:239], v[204:207]\n" \
  "ds_read_b128 v[236:239], v252 offset:7168\n" \
  "v_cvt_pk_bf16_f32 v192, v192, v193\n" \
  "v_cvt_pk_bf16_f32 v193, v194, v195\n" \
  "v_cvt_pk_bf16_f32 v194, v196, v197\n" \
  "v_cvt_pk_bf16_f32 v195, v198, v199\n" \
  "v_add_u32 v196, %[so], v251\n" \
  "ds_write_b128 v196, v[192:195] offset:24576\n" \
  "v_cvt_pk_bf16_f32 v200, v200, v201\n" \
  "v_cvt_pk_bf16_f32 v201, v202, v203\n" \
  "v_cvt_pk_bf16_f32 v202, v204, v205\n" \
  "v_cvt_pk_bf16_f32 v203, v206, v207\n" \
  "ds_write_b128 v196, v[200:203] offset:28672\n" \
  "s_mov_b32 m0, %[keep]\n" \
  "s_waitcnt lgkmcnt(0)\n"
#define ST_XB \
  "s_barrier\n" \
  "ds_read_b128 v[192:195], v251 offset:24576\n" \
  "ds_read_b128 v[208:211], v251 offset:28672\n" \
  "ds_read_b128 v[196:199], v251 offset:25600\n" \
  "ds_read_b128 v[212:215], v251 offset:29696\n" \
  "ds_read_b128 v[200:203], v251 offset:26624\n" \
  "ds_read_b128 v[216:219], v251 offset:30720\n" \
  "ds_read_b128 v[204:207], v251 offset:27648\n" \
  "ds_read_b128 v[220:223], v251 offset:31744\n" \
  "ds_read_b128 v[240:243], v250 offset:0\n" \
  "ds_read_b128 v[244:247], v250 offset:16384\n" \
  "s_waitcnt lgkmcnt(9)\n" \
  "v_mfma_f32_16x16x32_bf16 v[224:227], v[192:195], v[0:3], 0\n" \
  "s_waitcnt lgkmcnt(8)\n" \
  "v_mfma_f32_16x16x32_bf16 v[228:231], v[208:211], v[0:3], 0\n" \
  "s_waitcnt lgkmcnt(7)\n" \
  "v_mfma_f32_16x16x32_bf16 v[224:227], v[196:199], v[4:7], v[224:227]\n" \
  "s_waitcnt lgkmcnt(6)\n" \
  "v_mfma_f32_16x16x32_bf16 v[228:231], v[212:215], v[4:7], v[228:231]\n" \
  "s_waitcnt lgkmcnt(5)\n" \
  "v_mfma_f32_16x16x32_bf16 v[224:227], v[200:203], v[8:11], v[224:227]\n" \
  "s_waitcnt lgkmcnt(4)\n" \
  "v_mfma_f32_16x16x32_bf16 v[228:231], v[216:219], v[8:11], v[228:231]\n" \
  "s_waitcnt lgkmcnt(3)\n" \
  "v_mfma_f32_16x16x32_bf16 v[224:227], v[204:207], v[12:15], v[224:227]\n" \
  "s_waitcnt lgkmcnt(2)\n" \
  "v_mfma_f32_16x16x32_bf16 v[228:231], v[220:223], v[12:15], v[228:231]\n" \
  "v_mfma_f32_16x16x32_bf16 v[232:235], v[192:195], v[128:131], 0\n" \
  "v_mfma_f32_16x16x32_bf16 v[236:239], v[208:211], v[128:131], 0\n" \
  "v_mfma_f32_16x16x32_bf16 v[232:235], v[200:203], v[136:139], v[232:235]\n" \
  "v_cvt_pk_bf16_f32 v224, v224, v225\n" \
  "v_cvt_pk_bf16_f32 v226, v226, v227\n" \
  "v_mfma_f32_16x16x32_bf16 v[236:239], v[216:219], v[136:139], v[236:239]\n" \
  "v_cvt_pk_bf16_f32 v225, v228, v229\n" \
  "v_cvt_pk_bf16_f32 v227, v230, v231\n" \
  "s_waitcnt lgkmcnt(1)\n" \
  "v_mfma_f32_16x16x32_bf16 v[232:235], v[196:199], v[240:243], v[232:235]\n" \
  "s_add_u32 %[cnt], %[so], 0*%c[js]+0*%c[ss]\n" \
  "buffer_store_dwordx4 v[224:227], v251, %[rs], %[cnt] offen sc1\n" \
  "v_mfma_f32_16x16x32_bf16 v[236:239], v[212:215], v[240:243], v[236:239]\n" \
  "s_waitcnt lgkmcnt(0)\n" \
  "v_mfma_f32_16x16x32_bf16 v[232:235], v[204:207], v[244:247], v[232:235]\n" \
  "v_mfma_f32_16x16x32_bf16 v[236:239], v[220:223], v[244:247], v[236:239]\n" \
  "ds_read_b128 v[240:243], v250 offset:1024\n" \
  "ds_read_b128 v[244:247], v250 offset:17408\n" \
  "v_mfma_f32_16x16x32_bf16 v[224:227], v[192:195], v[64:67], 0\n" \
  "v_mfma_f32_16x16x32_bf16 v[228:231], v[208:211], v[64:67], 0\n" \
  "v_mfma_f32_16x16x32_bf16 v[224:227], v[196:199], v[68:71], v[224:227]\n" \
  "v_cvt_pk_bf16_f32 v232, v232, v233\n" \
  "v_cvt_pk_bf16_f32 v234, v234, v235\n" \
  "v_mfma_f32_16x16x32_bf16 v[228:231], v[212:215], v[68:71], v[228:231]\n" \
  "v_cvt_pk_bf16_f32 v233, v236, v237\n" \
  "v_cvt_pk_bf16_f32 v235, v238, v239\n" \
  "v_mfma_f32_16x16x32_bf16 v[224:227], v[200:203], v[72:75], v[224:227]\n" \
  "s_add_u32 %[cnt], %[so], 4*%c[js]+0*%c[ss]\n" \
  "buffer_store_dwordx4 v[232:235], v251, %[rs], %[cnt] offen sc1\n" \
  "v_mfma_f32_16x16x32_bf16 v[228:231], v[216:219], v[72:75], v[228:231]\n" \
  "v_mfma_f32_16x16x32_bf16 v[224:227], v[204:207], v[76:79], v[224:227]\n" \
  "v_mfma_f32_16x16x32_bf16 v[228:231], v[220:223], v[76:79], v[228:231]\n" \
  "v_lshlrev_b32 v253, 24, v251\n" \
  "s_waitcnt vmcnt(2)\n" \
  "buffer_store_byte v253, v253, %[rs], %[fo] offen sc1\n" \
  "s_barrier\n" \
  "v_mfma_f32_16x16x32_bf16 v[232:235], v[192:195], v[132:135], 0\n" \
  "v_mfma_f32_16x16x32_bf16 v[236:239], v[208:211], v[132:135], 0\n" \
  "v_mfma_f32_16x16x32_bf16 v[232:235], v[200:203], v[140:143], v[232:235]\n" \
  "v_cvt_pk_bf16_f32 v224, v224, v225\n" \
  "v_cvt_pk_bf16_f32 v226, v226, v227\n" \
  "v_mfma_f32_16x16x32_bf16 v[236:239], v[216:219], v[140:143], v[236:239]\n" \
  "v_cvt_pk_bf16_f32 v225, v228, v229\n" \
  "v_cvt_pk_bf16_f32 v227, v230, v231\n" \
  "s_waitcnt lgkmcnt(1)\n" \
  "v_mfma_f32_16x16x32_bf16 v[232:235], v[196:199], v[240:243], v[232:235]\n" \
  "s_add_u32 %[cnt], %[so], 0*%c[js]+1*%c[ss]\n" \
  "buffer_store_dwordx4 v[224:227], v251, %[rs], %[cnt] offen sc1\n" \
  "v_mfma_f32_16x16x32_bf16 v[236:239], v[212:215], v[240:243], v[236:239]\n" \
  "s_waitcnt lgkmcnt(0)\n" \
  "v_mfma_f32_16x16x32_bf16 v[232:235], v[204:207], v[244:247], v[232:235]\n" \
  "v_mfma_f32_16x16x32_bf16 v[236:239], v[220:223], v[244:247], v[236:239]\n" \
  "ds_read_b128 v[240:243], v250 offset:32768\n" \
  "ds_read_b128 v[244:247], v250 offset:49152\n" \
  "v_mfma_f32_16x16x32_bf16 v[224:227], v[192:195], v[16:19], 0\n" \
  "v_mfma_f32_16x16x32_bf16 v[228:231], v[208:211], v[16:19], 0\n" \
  "v_mfma_f32_16x16x32_bf16 v[224:227], v[196:199], v[20:23], v[224:227]\n" \
  "v_cvt_pk_bf16_f32 v232, v232, v233\n" \
  "v_cvt_pk_bf16_f32 v234, v234, v235\n" \
  "v_mfma_f32_16x16x32_bf16 v[228:231], v[212:215], v[20:23], v[228:231]\n" \
  "v_cvt_pk_bf16_f32 v233, v236, v237\n" \
  "v_cvt_pk_bf16_f32 v235, v238, v239\n" \
  "v_mfma_f32_16x16x32_bf16 v[224:227], v[200:203], v[24:27], v[224:227]\n" \
  "s_add_u32 %[cnt], %[so], 4*%c[js]+1*%c[ss]\n" \
  "buffer_store_dwordx4 v[232:235], v251, %[rs], %[cnt] offen sc1\n" \
  "v_mfma_f32_16x16x32_bf16 v[228:231], v[216:219], v[24:27], v[228:231]\n" \
  "v_mfma_f32_16x16x32_bf16 v[224:227], v[204:207], v[28:31], v[224:227]\n" \
  "v_mfma_f32_16x16x32_bf16 v[228:231], v[220:223], v[28:31], v[228:231]\n" \
  "s_barrier\n" \
  "v_mfma_f32_16x16x32_bf16 v[232:235], v[192:195], v[144:147], 0\n" \
  "v_mfma_f32_16x16x32_bf16 v[236:239], v[208:211], v[144:147], 0\n" \
  "v_mfma_f32_16x16x32_bf16 v[232:235], v[200:203], v[152:155], v[232:235]\n" \
  "v_cvt_pk_bf16_f32 v224, v224, v225\n" \
  "v_cvt_pk_bf16_f32 v226, v226, v227\n" \
  "v_mfma_f32_16x16x32_bf16 v[236:239], v[216:219], v[152:155], v[236:239]\n" \
  "v_cvt_pk_bf16_f32 v225, v228, v229\n" \
  "v_cvt_pk_bf16_f32 v227, v230, v231\n" \
  "s_waitcnt lgkmcnt(1)\n" \
  "v_mfma_f32_16x16x32_bf16 v[232:235], v[196:199], v[240:243], v[232:235]\n" \
  "s_add_u32 %[cnt], %[so], 1*%c[js]+0*%c[ss]\n" \
  "buffer_store_dwordx4 v[224:227], v251, %[rs], %[cnt] offen sc1\n" \
  "v_mfma_f32_16x16x32_bf16 v[236:239], v[212:215], v[240:243], v[236:239]\n" \
  "s_waitcnt lgkmcnt(0)\n" \
  "v_mfma_f32_16x16x32_bf16 v[232:235], v[204:207], v[244:247], v[232:235]\n" \
  "v_mfma_f32_16x16x32_bf16 v[236:239], v[220:223], v[244:247], v[236:239]\n" \
  "ds_read_b128 v[240:243], v250 offset:33792\n" \
  "ds_read_b128 v[244:247], v250 offset:50176\n" \
  "v_mfma_f32_16x16x32_bf16 v[224:227], v[192:195], v[80:83], 0\n" \
  "v_mfma_f32_16x16x32_bf16 v[228:231], v[208:211], v[80:83], 0\n" \
  "v_mfma_f32_16x16x32_bf16 v[224:227], v[196:199], v[84:87], v[224:227]\n" \
  "v_cvt_pk_bf16_f32 v232, v232, v233\n" \
  "v_cvt_pk_bf16_f32 v234, v234, v235\n" \
  "v_mfma_f32_16x16x32_bf16 v[228:231], v[212:215], v[84:87], v[228:231]\n" \
  "v_cvt_pk_bf16_f32 v233, v236, v237\n" \
  "v_cvt_pk_bf16_f32 v235, v238, v239\n" \
  "v_mfma_f32_16x16x32_bf16 v[224:227], v[200:203], v[88:91], v[224:227]\n" \
  "s_add_u32 %[cnt], %[so], 5*%c[js]+0*%c[ss]\n" \
  "buffer_store_dwordx4 v[232:235], v251, %[rs], %[cnt] offen sc1\n" \
  "v_mfma_f32_16x16x32_bf16 v[228:231], v[216:219], v[88:91], v[228:231]\n" \
  "v_mfma_f32_16x16x32_bf16 v[224:227], v[204:207], v[92:95], v[224:227]\n" \
  "v_mfma_f32_16x16x32_bf16 v[228:231], v[220:223], v[92:95], v[228:231]\n" \
  "v_mfma_f32_16x16x32_bf16 v[232:235], v[192:195], v[148:151], 0\n" \
  "v_mfma_f32_16x16x32_bf16 v[236:239], v[208:211], v[148:151], 0\n" \
  "v_mfma_f32_16x16x32_bf16 v[232:235], v[200:203], v[156:159], v[232:235]\n" \
  "v_cvt_pk_bf16_f32 v224, v224, v225\n" \
  "v_cvt_pk_bf16_f32 v226, v226, v227\n" \
  "v_mfma_f32_16x16x32_bf16 v[236:239], v[216:219], v[156:159], v[236:239]\n" \
  "v_cvt_pk_bf16_f32 v225, v228, v229\n" \
  "v_cvt_pk_bf16_f32 v227, v230, v231\n" \
  "s_waitcnt lgkmcnt(1)\n" \
  "v_mfma_f32_16x16x32_bf16 v[232:235], v[196:199], v[240:243], v[232:235]\n" \
  "s_add_u32 %[cnt], %[so], 1*%c[js]+1*%c[ss]\n" \
  "buffer_store_dwordx4 v[224:227], v251, %[rs], %[cnt] offen sc1\n" \
  "v_mfma_f32_16x16x32_bf16 v[236:239], v[212:215], v[240:243], v[236:239]\n" \
  "s_waitcnt lgkmcnt(0)\n" \
  "v_mfma_f32_16x16x32_bf16 v[232:235], v[204:207], v[244:247], v[232:235]\n" \
  "v_mfma_f32_16x16x32_bf16 v[236:239], v[220:223], v[244:247], v[236:239]\n" \
  "ds_read_b128 v[240:243], v249 offset:0\n" \
  "ds_read_b128 v[244:247], v249 offset:16384\n" \
  "s_barrier\n" \
  "v_mfma_f32_16x16x32_bf16 v[224:227], v[192:195], v[32:35], 0\n" \
  "v_mfma_f32_16x16x32_bf16 v[228:231], v[208:211], v[32:35], 0\n" \
  "v_mfma_f32_16x16x32_bf16 v[224:227], v[196:199], v[36:39], v[224:227]\n" \
  "v_cvt_pk_bf16_f32 v232, v232, v233\n" \
  "v_cvt_pk_bf16_f32 v234, v234, v235\n" \
  "v_mfma_f32_16x16x32_bf16 v[228:231], v[212:215], v[36:39], v[228:231]\n" \
  "v_cvt_pk_bf16_f32 v233, v236, v237\n" \
  "v_cvt_pk_bf16_f32 v235, v238, v239\n" \
  "v_mfma_f32_16x16x32_bf16 v[224:227], v[200:203], v[40:43], v[224:227]\n" \
  "s_add_u32 %[cnt], %[so], 5*%c[js]+1*%c[ss]\n" \
  "buffer_store_dwordx4 v[232:235], v251, %[rs], %[cnt] offen sc1\n" \
  "v_mfma_f32_16x16x32_bf16 v[228:231], v[216:219], v[40:43], v[228:231]\n" \
  "v_mfma_f32_16x16x32_bf16 v[224:227], v[204:207], v[44:47], v[224:227]\n" \
  "v_mfma_f32_16x16x32_bf16 v[228:231], v[220:223], v[44:47], v[228:231]\n" \
  "v_mfma_f32_16x16x32_bf16 v[232:235], v[192:195], v[160:163], 0\n" \
  "v_mfma_f32_16x16x32_bf16 v[236:239], v[208:211], v[160:163], 0\n" \
  "v_mfma_f32_16x16x32_bf16 v[232:235], v[200:203], v[168:171], v[232:235]\n" \
  "v_cvt_pk_bf16_f32 v224, v224, v225\n" \
  "v_cvt_pk_bf16_f32 v226, v226, v227\n" \
  "v_mfma_f32_16x16x32_bf16 v[236:239], v[216:219], v[168:171], v[236:239]\n" \
  "v_cvt_pk_bf16_f32 v225, v228, v229\n" \
  "v_cvt_pk_bf16_f32 v227, v230, v231\n" \
  "s_waitcnt lgkmcnt(1)\n" \
  "v_mfma_f32_16x16x32_bf16 v[232:235], v[196:199], v[240:243], v[232:235]\n" \
  "s_add_u32 %[cnt], %[so], 2*%c[js]+0*%c[ss]\n" \
  "buffer_store_dwordx4 v[224:227], v251, %[rs], %[cnt] offen sc1\n" \
  "v_mfma_f32_16x16x32_bf16 v[236:239], v[212:215], v[240:243], v[236:239]\n" \
  "s_waitcnt lgkmcnt(0)\n" \
  "v_mfma_f32_16x16x32_bf16 v[232:235], v[204:207], v[244:247], v[232:235]\n" \
  "v_mfma_f32_16x16x32_bf16 v[236:239], v[220:223], v[244:247], v[236:239]\n" \
  "ds_read_b128 v[240:243], v249 offset:1024\n" \
  "ds_read_b128 v[244:247], v249 offset:17408\n" \
  "v_mfma_f32_16x16x32_bf16 v[224:227], v[192:195], v[96:99], 0\n" \
  "v_mfma_f32_16x16x32_bf16 v[228:231], v[208:211], v[96:99], 0\n" \
  "v_mfma_f32_16x16x32_bf16 v[224:227], v[196:199], v[100:103], v[224:227]\n" \
  "v_cvt_pk_bf16_f32 v232, v232, v233\n" \
  "v_cvt_pk_bf16_f32 v234, v234, v235\n" \
  "v_mfma_f32_16x16x32_bf16 v[228:231], v[212:215], v[100:103], v[228:231]\n" \
  "v_cvt_pk_bf16_f32 v233, v236, v237\n" \
  "v_cvt_pk_bf16_f32 v235, v238, v239\n" \
  "v_mfma_f32_16x16x32_bf16 v[224:227], v[200:203], v[104:107], v[224:227]\n" \
  "s_add_u32 %[cnt], %[so], 6*%c[js]+0*%c[ss]\n" \
  "buffer_store_dwordx4 v[232:235], v251, %[rs], %[cnt] offen sc1\n" \
  "v_mfma_f32_16x16x32_bf16 v[228:231], v[216:219], v[104:107], v[228:231]\n" \
  "v_mfma_f32_16x16x32_bf16 v[224:227], v[204:207], v[108:111], v[224:227]\n" \
  "v_mfma_f32_16x16x32_bf16 v[228:231], v[220:223], v[108:111], v[228:231]\n" \
  "s_barrier\n" \
  "v_mfma_f32_16x16x32_bf16 v[232:235], v[192:195], v[164:167], 0\n" \
  "v_mfma_f32_16x16x32_bf16 v[236:239], v[208:211], v[164:167], 0\n" \
  "v_mfma_f32_16x16x32_bf16 v[232:235], v[200:203], v[172:175], v[232:235]\n" \
  "v_cvt_pk_bf16_f32 v224, v224, v225\n" \
  "v_cvt_pk_bf16_f32 v226, v226, v227\n" \
  "v_mfma_f32_16x16x32_bf16 v[236:239], v[216:219], v[172:175], v[236:239]\n" \
  "v_cvt_pk_bf16_f32 v225, v228, v229\n" \
  "v_cvt_pk_bf16_f32 v227, v230, v231\n" \
  "s_waitcnt lgkmcnt(1)\n" \
  "v_mfma_f32_16x16x32_bf16 v[232:235], v[196:199], v[240:243], v[232:235]\n" \
  "s_add_u32 %[cnt], %[so], 2*%c[js]+1*%c[ss]\n" \
  "buffer_store_dwordx4 v[224:227], v251, %[rs], %[cnt] offen sc1\n" \
  "v_mfma_f32_16x16x32_bf16 v[236:239], v[212:215], v[240:243], v[236:239]\n" \
  "s_waitcnt lgkmcnt(0)\n" \
  "v_mfma_f32_16x16x32_bf16 v[232:235], v[204:207], v[244:247], v[232:235]\n" \
  "v_mfma_f32_16x16x32_bf16 v[236:239], v[220:223], v[244:247], v[236:239]\n" \
  "ds_read_b128 v[240:243], v249 offset:32768\n" \
  "ds_read_b128 v[244:247], v249 offset:49152\n" \
  "v_mfma_f32_16x16x32_bf16 v[224:227], v[192:195], v[48:51], 0\n" \
  "v_mfma_f32_16x16x32_bf16 v[228:231], v[208:211], v[48:51], 0\n" \
  "v_mfma_f32_16x16x32_bf16 v[224:227], v[196:199], v[52:55], v[224:227]\n" \
  "v_cvt_pk_bf16_f32 v232, v232, v233\n" \
  "v_cvt_pk_bf16_f32 v234, v234, v235\n" \
  "v_mfma_f32_16x16x32_bf16 v[228:231], v[212:215], v[52:55], v[228:231]\n" \
  "v_cvt_pk_bf16_f32 v233, v236, v237\n" \
  "v_cvt_pk_bf16_f32 v235, v238, v239\n" \
  "v_mfma_f32_16x16x32_bf16 v[224:227], v[200:203], v[56:59], v[224:227]\n" \
  "s_add_u32 %[cnt], %[so], 6*%c[js]+1*%c[ss]\n" \
  "buffer_store_dwordx4 v[232:235], v251, %[rs], %[cnt] offen sc1\n" \
  "v_mfma_f32_16x16x32_bf16 v[228:231], v[216:219], v[56:59], v[228:231]\n" \
  "v_mfma_f32_16x16x32_bf16 v[224:227], v[204:207], v[60:63], v[224:227]\n" \
  "v_mfma_f32_16x16x32_bf16 v[228:231], v[220:223], v[60:63], v[228:231]\n" \
  "s_barrier\n" \
  "v_mfma_f32_16x16x32_bf16 v[232:235], v[192:195], v[176:179], 0\n" \
  "v_mfma_f32_16x16x32_bf16 v[236:239], v[208:211], v[176:179], 0\n" \
  "v_mfma_f32_16x16x32_bf16 v[232:235], v[200:203], v[184:187], v[232:235]\n" \
  "v_cvt_pk_bf16_f32 v224, v224, v225\n" \
  "v_cvt_pk_bf16_f32 v226, v226, v227\n" \
  "v_mfma_f32_16x16x32_bf16 v[236:239], v[216:219], v[184:187], v[236:239]\n" \
  "v_cvt_pk_bf16_f32 v225, v228, v229\n" \
  "v_cvt_pk_bf16_f32 v227, v230, v231\n" \
  "s_waitcnt lgkmcnt(1)\n" \
  "v_mfma_f32_16x16x32_bf16 v[232:235], v[196:199], v[240:243], v[232:235]\n" \
  "s_add_u32 %[cnt], %[so], 3*%c[js]+0*%c[ss]\n" \
  "buffer_store_dwordx4 v[224:227], v251, %[rs], %[cnt] offen sc1\n" \
  "v_mfma_f32_16x16x32_bf16 v[236:239], v[212:215], v[240:243], v[236:239]\n" \
  "s_waitcnt lgkmcnt(0)\n" \
  "v_mfma_f32_16x16x32_bf16 v[232:235], v[204:207], v[244:247], v[232:235]\n" \
  "v_mfma_f32_16x16x32_bf16 v[236:239], v[220:223], v[244:247], v[236:239]\n" \
  "ds_read_b128 v[240:243], v249 offset:33792\n" \
  "ds_read_b128 v[244:247], v249 offset:50176\n" \
  "v_mfma_f32_16x16x32_bf16 v[224:227], v[192:195], v[112:115], 0\n" \
  "v_mfma_f32_16x16x32_bf16 v[228:231], v[208:211], v[112:115], 0\n" \
  "v_mfma_f32_16x16x32_bf16 v[224:227], v[196:199], v[116:119], v[224:227]\n" \
  "v_cvt_pk_bf16_f32 v232, v232, v233\n" \
  "v_cvt_pk_bf16_f32 v234, v234, v235\n" \
  "v_mfma_f32_16x16x32_bf16 v[228:231], v[212:215], v[116:119], v[228:231]\n" \
  "v_cvt_pk_bf16_f32 v233, v236, v237\n" \
  "v_cvt_pk_bf16_f32 v235, v238, v239\n" \
  "v_mfma_f32_16x16x32_bf16 v[224:227], v[200:203], v[120:123], v[224:227]\n" \
  "s_add_u32 %[cnt], %[so], 7*%c[js]+0*%c[ss]\n" \
  "buffer_store_dwordx4 v[232:235], v251, %[rs], %[cnt] offen sc1\n" \
  "v_mfma_f32_16x16x32_bf16 v[228:231], v[216:219], v[120:123], v[228:231]\n" \
  "v_mfma_f32_16x16x32_bf16 v[224:227], v[204:207], v[124:127], v[224:227]\n" \
  "v_mfma_f32_16x16x32_bf16 v[228:231], v[220:223], v[124:127], v[228:231]\n" \
  "v_mfma_f32_16x16x32_bf16 v[232:235], v[192:195], v[180:183], 0\n" \
  "v_mfma_f32_16x16x32_bf16 v[236:239], v[208:211], v[180:183], 0\n" \
  "v_mfma_f32_16x16x32_bf16 v[232:235], v[200:203], v[188:191], v[232:235]\n" \
  "v_cvt_pk_bf16_f32 v224, v224, v225\n" \
  "v_cvt_pk_bf16_f32 v226, v226, v227\n" \
  "v_mfma_f32_16x16x32_bf16 v[236:239], v[216:219], v[188:191], v[236:239]\n" \
  "v_cvt_pk_bf16_f32 v225, v228, v229\n" \
  "v_cvt_pk_bf16_f32 v227, v230, v231\n" \
  "s_waitcnt lgkmcnt(1)\n" \
  "v_mfma_f32_16x16x32_bf16 v[232:235], v[196:199], v[240:243], v[232:235]\n" \
  "s_add_u32 %[cnt], %[so], 3*%c[js]+1*%c[ss]\n" \
  "buffer_store_dwordx4 v[224:227], v251, %[rs], %[cnt] offen sc1\n" \
  "v_mfma_f32_16x16x32_bf16 v[236:239], v[212:215], v[240:243], v[236:239]\n" \
  "s_waitcnt lgkmcnt(0)\n" \
  "v_mfma_f32_16x16x32_bf16 v[232:235], v[204:207], v[244:247], v[232:235]\n" \
  "v_mfma_f32_16x16x32_bf16 v[236:239], v[220:223], v[244:247], v[236:239]\n" \
  "s_nop 4\n" \
  "v_cvt_pk_bf16_f32 v232, v232, v233\n" \
  "v_cvt_pk_bf16_f32 v234, v234, v235\n" \
  "s_nop 1\n" \
  "v_cvt_pk_bf16_f32 v233, v236, v237\n" \
  "v_cvt_pk_bf16_f32 v235, v238, v239\n" \
  "s_add_u32 %[cnt], %[so], 7*%c[js]+1*%c[ss]\n" \
  "buffer_store_dwordx4 v[232:235], v251, %[rs], %[cnt] offen sc1\n" \
  "s_nop 1\n"
#define ST_XBF \
  "s_barrier\n" \
  "ds_read_b128 v[192:195], v251 offset:24576\n" \
  "ds_read_b128 v[208:211], v251 offset:28672\n" \
  "ds_read_b128 v[196:199], v251 offset:25600\n" \
  "ds_read_b128 v[212:215], v251 offset:29696\n" \
  "ds_read_b128 v[200:203], v251 offset:26624\n" \
  "ds_read_b128 v[216:219], v251 offset:30720\n" \
  "ds_read_b128 v[204:207], v251 offset:27648\n" \
  "ds_read_b128 v[220:223], v251 offset:31744\n" \
  "ds_read_b128 v[240:243], v250 offset:0\n" \
  "ds_read_b128 v[244:247], v250 offset:16384\n" \
  "s_waitcnt lgkmcnt(9)\n" \
  "v_mfma_f32_16x16x32_bf16 v[224:227], v[192:195], v[0:3], 0\n" \
  "s_waitcnt lgkmcnt(8)\n" \
  "v_mfma_f32_16x16x32_bf16 v[228:231], v[208:211], v[0:3], 0\n" \
  "s_waitcnt lgkmcnt(7)\n" \
  "v_mfma_f32_16x16x32_bf16 v[224:227], v[196:199], v[4:7], v[224:227]\n" \
  "s_waitcnt lgkmcnt(6)\n" \
  "v_mfma_f32_16x16x32_bf16 v[228:231], v[212:215], v[4:7], v[228:231]\n" \
  "s_waitcnt lgkmcnt(5)\n" \
  "v_mfma_f32_16x16x32_bf16 v[224:227], v[200:203], v[8:11], v[224:227]\n" \
  "s_waitcnt lgkmcnt(4)\n" \
  "v_mfma_f32_16x16x32_bf16 v[228:231], v[216:219], v[8:11], v[228:231]\n" \
  "s_waitcnt lgkmcnt(3)\n" \
  "v_mfma_f32_16x16x32_bf16 v[224:227], v[204:207], v[12:15], v[224:227]\n" \
  "s_waitcnt lgkmcnt(2)\n" \
  "v_mfma_f32_16x16x32_bf16 v[228:231], v[220:223], v[12:15], v[228:231]\n" \
  "v_mfma_f32_16x16x32_bf16 v[232:235], v[192:195], v[128:131], 0\n" \
  "v_mfma_f32_16x16x32_bf16 v[236:239], v[208:211], v[128:131], 0\n" \
  "v_mfma_f32_16x16x32_bf16 v[232:235], v[200:203], v[136:139], v[232:235]\n" \
  "v_cvt_pk_bf16_f32 v224, v224, v225\n" \
  "v_cvt_pk_bf16_f32 v226, v226, v227\n" \
  "v_mfma_f32_16x16x32_bf16 v[236:239], v[216:219], v[136:139], v[236:239]\n" \
  "v_cvt_pk_bf16_f32 v225, v228, v229\n" \
  "v_cvt_pk_bf16_f32 v227, v230, v231\n" \
  "s_waitcnt lgkmcnt(1)\n" \
  "v_mfma_f32_16x16x32_bf16 v[232:235], v[196:199], v[240:243], v[232:235]\n" \
  "s_add_u32 %[cnt], %[so], 0*%c[js]+0*%c[ss]\n" \
  "buffer_store_dwordx4 v[224:227], v251, %[rs], %[cnt] offen sc1\n" \
  "v_mfma_f32_16x16x32_bf16 v[236:239], v[212:215], v[240:243], v[236:239]\n" \
  "s_waitcnt lgkmcnt(0)\n" \
  "v_mfma_f32_16x16x32_bf16 v[232:235], v[204:207], v[244:247], v[232:235]\n" \
  "v_mfma_f32_16x16x32_bf16 v[236:239], v[220:223], v[244:247], v[236:239]\n" \
  "ds_read_b128 v[240:243], v250 offset:1024\n" \
  "ds_read_b128 v[244:247], v250 offset:17408\n" \
  "v_mfma_f32_16x16x32_bf16 v[224:227], v[192:195], v[64:67], 0\n" \
  "v_mfma_f32_16x16x32_bf16 v[228:231], v[208:211], v[64:67], 0\n" \
  "v_mfma_f32_16x16x32_bf16 v[224:227], v[196:199], v[68:71], v[224:227]\n" \
  "v_cvt_pk_bf16_f32 v232, v232, v233\n" \
  "v_cvt_pk_bf16_f32 v234, v234, v235\n" \
  "v_mfma_f32_16x16x32_bf16 v[228:231], v[212:215], v[68:71], v[228:231]\n" \
  "v_cvt_pk_bf16_f32 v233, v236, v237\n" \
  "v_cvt_pk_bf16_f32 v235, v238, v239\n" \
  "v_mfma_f32_16x16x32_bf16 v[224:227], v[200:203], v[72:75], v[224:227]\n" \
  "s_add_u32 %[cnt], %[so], 4*%c[js]+0*%c[ss]\n" \
  "buffer_store_dwordx4 v[232:235], v251, %[rs], %[cnt] offen sc1\n" \
  "v_mfma_f32_16x16x32_bf16 v[228:231], v[216:219], v[72:75], v[228:231]\n" \
  "v_mfma_f32_16x16x32_bf16 v[224:227], v[204:207], v[76:79], v[224:227]\n" \
  "v_mfma_f32_16x16x32_bf16 v[228:231], v[220:223], v[76:79], v[228:231]\n" \
  "v_mfma_f32_16x16x32_bf16 v[232:235], v[192:195], v[132:135], 0\n" \
  "v_mfma_f32_16x16x32_bf16 v[236:239], v[208:211], v[132:135], 0\n" \
  "v_mfma_f32_16x16x32_bf16 v[232:235], v[200:203], v[140:143], v[232:235]\n" \
  "v_cvt_pk_bf16_f32 v224, v224, v225\n" \
  "v_cvt_pk_bf16_f32 v226, v226, v227\n" \
  "v_mfma_f32_16x16x32_bf16 v[236:239], v[216:219], v[140:143], v[236:239]\n" \
  "v_cvt_pk_bf16_f32 v225, v228, v229\n" \
  "v_cvt_pk_bf16_f32 v227, v230, v231\n" \
  "s_waitcnt lgkmcnt(1)\n" \
  "v_mfma_f32_16x16x32_bf16 v[232:235], v[196:199], v[240:243], v[232:235]\n" \
  "s_add_u32 %[cnt], %[so], 0*%c[js]+1*%c[ss]\n" \
  "buffer_store_dwordx4 v[224:227], v251, %[rs], %[cnt] offen sc1\n" \
  "v_mfma_f32_16x16x32_bf16 v[236:239], v[212:215], v[240:243], v[236:239]\n" \
  "s_waitcnt lgkmcnt(0)\n" \
  "v_mfma_f32_16x16x32_bf16 v[232:235], v[204:207], v[244:247], v[232:235]\n" \
  "v_mfma_f32_16x16x32_bf16 v[236:239], v[220:223], v[244:247], v[236:239]\n" \
  "ds_read_b128 v[240:243], v250 offset:32768\n" \
  "ds_read_b128 v[244:247], v250 offset:49152\n" \
  "s_barrier\n" \
  "v_mfma_f32_16x16x32_bf16 v[224:227], v[192:195], v[16:19], 0\n" \
  "v_mfma_f32_16x16x32_bf16 v[228:231], v[208:211], v[16:19], 0\n" \
  "v_mfma_f32_16x16x32_bf16 v[224:227], v[196:199], v[20:23], v[224:227]\n" \
  "v_cvt_pk_bf16_f32 v232, v232, v233\n" \
  "v_cvt_pk_bf16_f32 v234, v234, v235\n" \
  "v_mfma_f32_16x16x32_bf16 v[228:231], v[212:215], v[20:23], v[228:231]\n" \
  "v_cvt_pk_bf16_f32 v233, v236, v237\n" \
  "v_cvt_pk_bf16_f32 v235, v238, v239\n" \
  "v_mfma_f32_16x16x32_bf16 v[224:227], v[200:203], v[24:27], v[224:227]\n" \
  "s_add_u32 %[cnt], %[so], 4*%c[js]+1*%c[ss]\n" \
  "buffer_store_dwordx4 v[232:235], v251, %[rs], %[cnt] offen sc1\n" \
  "v_mfma_f32_16x16x32_bf16 v[228:231], v[216:219], v[24:27], v[228:231]\n" \
  "v_mfma_f32_16x16x32_bf16 v[224:227], v[204:207], v[28:31], v[224:227]\n" \
  "v_mfma_f32_16x16x32_bf16 v[228:231], v[220:223], v[28:31], v[228:231]\n" \
  "v_mfma_f32_16x16x32_bf16 v[232:235], v[192:195], v[144:147], 0\n" \
  "v_mfma_f32_16x16x32_bf16 v[236:239], v[208:211], v[144:147], 0\n" \
  "v_mfma_f32_16x16x32_bf16 v[232:235], v[200:203], v[152:155], v[232:235]\n" \
  "v_cvt_pk_bf16_f32 v224, v224, v225\n" \
  "v_cvt_pk_bf16_f32 v226, v226, v227\n" \
  "v_mfma_f32_16x16x32_bf16 v[236:239], v[216:219], v[152:155], v[236:239]\n" \
  "v_cvt_pk_bf16_f32 v225, v228, v229\n" \
  "v_cvt_pk_bf16_f32 v227, v230, v231\n" \
  "s_waitcnt lgkmcnt(1)\n" \
  "v_mfma_f32_16x16x32_bf16 v[232:235], v[196:199], v[240:243], v[232:235]\n" \
  "s_add_u32 %[cnt], %[so], 1*%c[js]+0*%c[ss]\n" \
  "buffer_store_dwordx4 v[224:227], v251, %[rs], %[cnt] offen sc1\n" \
  "v_mfma_f32_16x16x32_bf16 v[236:239], v[212:215], v[240:243], v[236:239]\n" \
  "s_waitcnt lgkmcnt(0)\n" \
  "v_mfma_f32_16x16x32_bf16 v[232:235], v[204:207], v[244:247], v[232:235]\n" \
  "v_mfma_f32_16x16x32_bf16 v[236:239], v[220:223], v[244:247], v[236:239]\n" \
  "ds_read_b128 v[240:243], v250 offset:33792\n" \
  "ds_read_b128 v[244:247], v250 offset:50176\n" \
  "v_mfma_f32_16x16x32_bf16 v[224:227], v[192:195], v[80:83], 0\n" \
  "v_mfma_f32_16x16x32_bf16 v[228:231], v[208:211], v[80:83], 0\n" \
  "v_mfma_f32_16x16x32_bf16 v[224:227], v[196:199], v[84:87], v[224:227]\n" \
  "v_cvt_pk_bf16_f32 v232, v232, v233\n" \
  "v_cvt_pk_bf16_f32 v234, v234, v235\n" \
  "v_mfma_f32_16x16x32_bf16 v[228:231], v[212:215], v[84:87], v[228:231]\n" \
  "v_cvt_pk_bf16_f32 v233, v236, v237\n" \
  "v_cvt_pk_bf16_f32 v235, v238, v239\n" \
  "v_mfma_f32_16x16x32_bf16 v[224:227], v[200:203], v[88:91], v[224:227]\n" \
  "s_add_u32 %[cnt], %[so], 5*%c[js]+0*%c[ss]\n" \
  "buffer_store_dwordx4 v[232:235], v251, %[rs], %[cnt] offen sc1\n" \
  "v_mfma_f32_16x16x32_bf16 v[228:231], v[216:219], v[88:91], v[228:231]\n" \
  "v_mfma_f32_16x16x32_bf16 v[224:227], v[204:207], v[92:95], v[224:227]\n" \
  "v_mfma_f32_16x16x32_bf16 v[228:231], v[220:223], v[92:95], v[228:231]\n" \
  "v_mfma_f32_16x16x32_bf16 v[232:235], v[192:195], v[148:151], 0\n" \
  "v_mfma_f32_16x16x32_bf16 v[236:239], v[208:211], v[148:151], 0\n" \
  "v_mfma_f32_16x16x32_bf16 v[232:235], v[200:203], v[156:159], v[232:235]\n" \
  "v_cvt_pk_bf16_f32 v224, v224, v225\n" \
  "v_cvt_pk_bf16_f32 v226, v226, v227\n" \
  "v_mfma_f32_16x16x32_bf16 v[236:239], v[216:219], v[156:159], v[236:239]\n" \
  "v_cvt_pk_bf16_f32 v225, v228, v229\n" \
  "v_cvt_pk_bf16_f32 v227, v230, v231\n" \
  "s_waitcnt lgkmcnt(1)\n" \
  "v_mfma_f32_16x16x32_bf16 v[232:235], v[196:199], v[240:243], v[232:235]\n" \
  "s_add_u32 %[cnt], %[so], 1*%c[js]+1*%c[ss]\n" \
  "buffer_store_dwordx4 v[224:227], v251, %[rs], %[cnt] offen sc1\n" \
  "v_mfma_f32_16x16x32_bf16 v[236:239], v[212:215], v[240:243], v[236:239]\n" \
  "s_waitcnt lgkmcnt(0)\n" \
  "v_mfma_f32_16x16x32_bf16 v[232:235], v[204:207], v[244:247], v[232:235]\n" \
  "v_mfma_f32_16x16x32_bf16 v[236:239], v[220:223], v[244:247], v[236:239]\n" \
  "ds_read_b128 v[240:243], v249 offset:0\n" \
  "ds_read_b128 v[244:247], v249 offset:16384\n" \
  "s_barrier\n" \
  "v_mfma_f32_16x16x32_bf16 v[224:227], v[192:195], v[32:35], 0\n" \
  "v_mfma_f32_16x16x32_bf16 v[228:231], v[208:211], v[32:35], 0\n" \
  "v_mfma_f32_16x16x32_bf16 v[224:227], v[196:199], v[36:39], v[224:227]\n" \
  "v_cvt_pk_bf16_f32 v232, v232, v233\n" \
  "v_cvt_pk_bf16_f32 v234, v234, v235\n" \
  "v_mfma_f32_16x16x32_bf16 v[228:231], v[212:215], v[36:39], v[228:231]\n" \
  "v_cvt_pk_bf16_f32 v233, v236, v237\n" \
  "v_cvt_pk_bf16_f32 v235, v238, v239\n" \
  "v_mfma_f32_16x16x32_bf16 v[224:227], v[200:203], v[40:43], v[224:227]\n" \
  "s_add_u32 %[cnt], %[so], 5*%c[js]+1*%c[ss]\n" \
  "buffer_store_dwordx4 v[232:235], v251, %[rs], %[cnt] offen sc1\n" \
  "v_mfma_f32_16x16x32_bf16 v[228:231], v[216:219], v[40:43], v[228:231]\n" \
  "v_mfma_f32_16x16x32_bf16 v[224:227], v[204:207], v[44:47], v[224:227]\n" \
  "v_mfma_f32_16x16x32_bf16 v[228:231], v[220:223], v[44:47], v[228:231]\n" \
  "v_mfma_f32_16x16x32_bf16 v[232:235], v[192:195], v[160:163], 0\n" \
  "v_mfma_f32_16x16x32_bf16 v[236:239], v[208:211], v[160:163], 0\n" \
  "v_mfma_f32_16x16x32_bf16 v[232:235], v[200:203], v[168:171], v[232:235]\n" \
  "v_cvt_pk_bf16_f32 v224, v224, v225\n" \
  "v_cvt_pk_bf16_f32 v226, v226, v227\n" \
  "v_mfma_f32_16x16x32_bf16 v[236:239], v[216:219], v[168:171], v[236:239]\n" \
  "v_cvt_pk_bf16_f32 v225, v228, v229\n" \
  "v_cvt_pk_bf16_f32 v227, v230, v231\n" \
  "s_waitcnt lgkmcnt(1)\n" \
  "v_mfma_f32_16x16x32_bf16 v[232:235], v[196:199], v[240:243], v[232:235]\n" \
  "s_add_u32 %[cnt], %[so], 2*%c[js]+0*%c[ss]\n" \
  "buffer_store_dwordx4 v[224:227], v251, %[rs], %[cnt] offen sc1\n" \
  "v_mfma_f32_16x16x32_bf16 v[236:239], v[212:215], v[240:243], v[236:239]\n" \
  "s_waitcnt lgkmcnt(0)\n" \
  "v_mfma_f32_16x16x32_bf16 v[232:235], v[204:207], v[244:247], v[232:235]\n" \
  "v_mfma_f32_16x16x32_bf16 v[236:239], v[220:223], v[244:247], v[236:239]\n" \
  "ds_read_b128 v[240:243], v249 offset:1024\n" \
  "ds_read_b128 v[244:247], v249 offset:17408\n" \
  "v_mfma_f32_16x16x32_bf16 v[224:227], v[192:195], v[96:99], 0\n" \
  "v_mfma_f32_16x16x32_bf16 v[228:231], v[208:211], v[96:99], 0\n" \
  "v_mfma_f32_16x16x32_bf16 v[224:227], v[196:199], v[100:103], v[224:227]\n" \
  "v_cvt_pk_bf16_f32 v232, v232, v233\n" \
  "v_cvt_pk_bf16_f32 v234, v234, v235\n" \
  "v_mfma_f32_16x16x32_bf16 v[228:231], v[212:215], v[100:103], v[228:231]\n" \
  "v_cvt_pk_bf16_f32 v233, v236, v237\n" \
  "v_cvt_pk_bf16_f32 v235, v238, v239\n" \
  "v_mfma_f32_16x16x32_bf16 v[224:227], v[200:203], v[104:107], v[224:227]\n" \
  "s_add_u32 %[cnt], %[so], 6*%c[js]+0*%c[ss]\n" \
  "buffer_store_dwordx4 v[232:235], v251, %[rs], %[cnt] offen sc1\n" \
  "v_mfma_f32_16x16x32_bf16 v[228:231], v[216:219], v[104:107], v[228:231]\n" \
  "v_mfma_f32_16x16x32_bf16 v[224:227], v[204:207], v[108:111], v[224:227]\n" \
  "v_mfma_f32_16x16x32_bf16 v[228:231], v[220:223], v[108:111], v[228:231]\n" \
  "v_mfma_f32_16x16x32_bf16 v[232:235], v[192:195], v[164:167], 0\n" \
  "v_mfma_f32_16x16x32_bf16 v[236:239], v[208:211], v[164:167], 0\n" \
  "v_mfma_f32_16x16x32_bf16 v[232:235], v[200:203], v[172:175], v[232:235]\n" \
  "v_cvt_pk_bf16_f32 v224, v224, v225\n" \
  "v_cvt_pk_bf16_f32 v226, v226, v227\n" \
  "v_mfma_f32_16x16x32_bf16 v[236:239], v[216:219], v[172:175], v[236:239]\n" \
  "v_cvt_pk_bf16_f32 v225, v228, v229\n" \
  "v_cvt_pk_bf16_f32 v227, v230, v231\n" \
  "s_waitcnt lgkmcnt(1)\n" \
  "v_mfma_f32_16x16x32_bf16 v[232:235], v[196:199], v[240:243], v[232:235]\n" \
  "s_add_u32 %[cnt], %[so], 2*%c[js]+1*%c[ss]\n" \
  "buffer_store_dwordx4 v[224:227], v251, %[rs], %[cnt] offen sc1\n" \
  "v_mfma_f32_16x16x32_bf16 v[236:239], v[212:215], v[240:243], v[236:239]\n" \
  "s_waitcnt lgkmcnt(0)\n" \
  "v_mfma_f32_16x16x32_bf16 v[232:235], v[204:207], v[244:247], v[232:235]\n" \
  "v_mfma_f32_16x16x32_bf16 v[236:239], v[220:223], v[244:247], v[236:239]\n" \
  "ds_read_b128 v[240:243], v249 offset:32768\n" \
  "ds_read_b128 v[244:247], v249 offset:49152\n" \
  "s_barrier\n" \
  "v_mfma_f32_16x16x32_bf16 v[224:227], v[192:195], v[48:51], 0\n" \
  "v_mfma_f32_16x16x32_bf16 v[228:231], v[208:211], v[48:51], 0\n" \
  "v_mfma_f32_16x16x32_bf16 v[224:227], v[196:199], v[52:55], v[224:227]\n" \
  "v_cvt_pk_bf16_f32 v232, v232, v233\n" \
  "v_cvt_pk_bf16_f32 v234, v234, v235\n" \
  "v_mfma_f32_16x16x32_bf16 v[228:231], v[212:215], v[52:55], v[228:231]\n" \
  "v_cvt_pk_bf16_f32 v233, v236, v237\n" \
  "v_cvt_pk_bf16_f32 v235, v238, v239\n" \
  "v_mfma_f32_16x16x32_bf16 v[224:227], v[200:203], v[56:59], v[224:227]\n" \
  "s_add_u32 %[cnt], %[so], 6*%c[js]+1*%c[ss]\n" \
  "buffer_store_dwordx4 v[232:235], v251, %[rs], %[cnt] offen sc1\n" \
  "v_mfma_f32_16x16x32_bf16 v[228:231], v[216:219], v[56:59], v[228:231]\n" \
  "v_mfma_f32_16x16x32_bf16 v[224:227], v[204:207], v[60:63], v[224:227]\n" \
  "v_mfma_f32_16x16x32_bf16 v[228:231], v[220:223], v[60:63], v[228:231]\n" \
  "v_mfma_f32_16x16x32_bf16 v[232:235], v[192:195], v[176:179], 0\n" \
  "v_mfma_f32_16x16x32_bf16 v[236:239], v[208:211], v[176:179], 0\n" \
  "v_mfma_f32_16x16x32_bf16 v[232:235], v[200:203], v[184:187], v[232:235]\n" \
  "v_cvt_pk_bf16_f32 v224, v224, v225\n" \
  "v_cvt_pk_bf16_f32 v226, v226, v227\n" \
  "v_mfma_f32_16x16x32_bf16 v[236:239], v[216:219], v[184:187], v[236:239]\n" \
  "v_cvt_pk_bf16_f32 v225, v228, v229\n" \
  "v_cvt_pk_bf16_f32 v227, v230, v231\n" \
  "s_waitcnt lgkmcnt(1)\n" \
  "v_mfma_f32_16x16x32_bf16 v[232:235], v[196:199], v[240:243], v[232:235]\n" \
  "s_add_u32 %[cnt], %[so], 3*%c[js]+0*%c[ss]\n" \
  "buffer_store_dwordx4 v[224:227], v251, %[rs], %[cnt] offen sc1\n" \
  "v_mfma_f32_16x16x32_bf16 v[236:239], v[212:215], v[240:243], v[236:239]\n" \
  "s_waitcnt lgkmcnt(0)\n" \
  "v_mfma_f32_16x16x32_bf16 v[232:235], v[204:207], v[244:247], v[232:235]\n" \
  "v_mfma_f32_16x16x32_bf16 v[236:239], v[220:223], v[244:247], v[236:239]\n" \
  "ds_read_b128 v[240:243], v249 offset:33792\n" \
  "ds_read_b128 v[244:247], v249 offset:50176\n" \
  "v_mfma_f32_16x16x32_bf16 v[224:227], v[192:195], v[112:115], 0\n" \
  "v_mfma_f32_16x16x32_bf16 v[228:231], v[208:211], v[112:115], 0\n" \
  "v_mfma_f32_16x16x32_bf16 v[224:227], v[196:199], v[116:119], v[224:227]\n" \
  "v_cvt_pk_bf16_f32 v232, v232, v233\n" \
  "v_cvt_pk_bf16_f32 v234, v234, v235\n" \
  "v_mfma_f32_16x16x32_bf16 v[228:231], v[212:215], v[116:119], v[228:231]\n" \
  "v_cvt_pk_bf16_f32 v233, v236, v237\n" \
  "v_cvt_pk_bf16_f32 v235, v238, v239\n" \
  "v_mfma_f32_16x16x32_bf16 v[224:227], v[200:203], v[120:123], v[224:227]\n" \
  "s_add_u32 %[cnt], %[so], 7*%c[js]+0*%c[ss]\n" \
  "buffer_store_dwordx4 v[232:235], v251, %[rs], %[cnt] offen sc1\n" \
  "v_mfma_f32_16x16x32_bf16 v[228:231], v[216:219], v[120:123], v[228:231]\n" \
  "v_mfma_f32_16x16x32_bf16 v[224:227], v[204:207], v[124:127], v[224:227]\n" \
  "v_mfma_f32_16x16x32_bf16 v[228:231], v[220:223], v[124:127], v[228:231]\n" \
  "v_mfma_f32_16x16x32_bf16 v[232:235], v[192:195], v[180:183], 0\n" \
  "v_mfma_f32_16x16x32_bf16 v[236:239], v[208:211], v[180:183], 0\n" \
  "v_mfma_f32_16x16x32_bf16 v[232:235], v[200:203], v[188:191], v[232:235]\n" \
  "v_cvt_pk_bf16_f32 v224, v224, v225\n" \
  "v_cvt_pk_bf16_f32 v226, v226, v227\n" \
  "v_mfma_f32_16x16x32_bf16 v[236:239], v[216:219], v[188:191], v[236:239]\n" \
  "v_cvt_pk_bf16_f32 v225, v228, v229\n" \
  "v_cvt_pk_bf16_f32 v227, v230, v231\n" \
  "s_waitcnt lgkmcnt(1)\n" \
  "v_mfma_f32_16x16x32_bf16 v[232:235], v[196:199], v[240:243], v[232:235]\n" \
  "s_add_u32 %[cnt], %[so], 3*%c[js]+1*%c[ss]\n" \
  "buffer_store_dwordx4 v[224:227], v251, %[rs], %[cnt] offen sc1\n" \
  "v_mfma_f32_16x16x32_bf16 v[236:239], v[212:215], v[240:243], v[236:239]\n" \
  "s_waitcnt lgkmcnt(0)\n" \
  "v_mfma_f32_16x16x32_bf16 v[232:235], v[204:207], v[244:247], v[232:235]\n" \
  "v_mfma_f32_16x16x32_bf16 v[236:239], v[220:223], v[244:247], v[236:239]\n" \
  "s_nop 4\n" \
  "v_cvt_pk_bf16_f32 v232, v232, v233\n" \
  "v_cvt_pk_bf16_f32 v234, v234, v235\n" \
  "s_nop 1\n" \
  "v_cvt_pk_bf16_f32 v233, v236, v237\n" \
  "v_cvt_pk_bf16_f32 v235, v238, v239\n" \
  "s_add_u32 %[cnt], %[so], 7*%c[js]+1*%c[ss]\n" \
  "buffer_store_dwordx4 v[232:235], v251, %[rs], %[cnt] offen sc1\n" \
  "s_barrier\n" \
  "s_nop 1\n"
#define ST_XBS \
  "ds_read_b128 v[192:195], v251 offset:24576\n" \
  "ds_read_b128 v[208:211], v251 offset:28672\n" \
  "ds_read_b128 v[196:199], v251 offset:25600\n" \
  "ds_read_b128 v[212:215], v251 offset:29696\n" \
  "ds_read_b128 v[200:203], v251 offset:26624\n" \
  "ds_read_b128 v[216:219], v251 offset:30720\n" \
  "ds_read_b128 v[204:207], v251 offset:27648\n" \
  "ds_read_b128 v[220:223], v251 offset:31744\n" \
  "ds_read_b128 v[240:243], v250 offset:0\n" \
  "ds_read_b128 v[244:247], v250 offset:16384\n" \
  "s_waitcnt lgkmcnt(9)\n" \
  "v_mfma_f32_16x16x32_bf16 v[224:227], v[192:195], v[0:3], 0\n" \
  "s_waitcnt lgkmcnt(8)\n" \
  "v_mfma_f32_16x16x32_bf16 v[228:231], v[208:211], v[0:3], 0\n" \
  "s_waitcnt lgkmcnt(7)\n" \
  "v_mfma_f32_16x16x32_bf16 v[224:227], v[196:199], v[4:7], v[224:227]\n" \
  "s_waitcnt lgkmcnt(6)\n" \
  "v_mfma_f32_16x16x32_bf16 v[228:231], v[212:215], v[4:7], v[228:231]\n" \
  "s_waitcnt lgkmcnt(5)\n" \
  "v_mfma_f32_16x16x32_bf16 v[224:227], v[200:203], v[8:11], v[224:227]\n" \
  "s_waitcnt lgkmcnt(4)\n" \
  "v_mfma_f32_16x16x32_bf16 v[228:231], v[216:219], v[8:11], v[228:231]\n" \
  "s_waitcnt lgkmcnt(3)\n" \
  "v_mfma_f32_16x16x32_bf16 v[224:227], v[204:207], v[12:15], v[224:227]\n" \
  "s_waitcnt lgkmcnt(2)\n" \
  "v_mfma_f32_16x16x32_bf16 v[228:231], v[220:223], v[12:15], v[228:231]\n" \
  "v_mfma_f32_16x16x32_bf16 v[232:235], v[192:195], v[128:131], 0\n" \
  "v_mfma_f32_16x16x32_bf16 v[236:239], v[208:211], v[128:131], 0\n" \
  "v_mfma_f32_16x16x32_bf16 v[232:235], v[200:203], v[136:139], v[232:235]\n" \
  "v_cvt_pk_bf16_f32 v224, v224, v225\n" \
  "v_cvt_pk_bf16_f32 v226, v226, v227\n" \
  "v_mfma_f32_16x16x32_bf16 v[236:239], v[216:219], v[136:139], v[236:239]\n" \
  "v_cvt_pk_bf16_f32 v225, v228, v229\n" \
  "v_cvt_pk_bf16_f32 v227, v230, v231\n" \
  "s_waitcnt lgkmcnt(1)\n" \
  "v_mfma_f32_16x16x32_bf16 v[232:235], v[196:199], v[240:243], v[232:235]\n" \
  "s_add_u32 %[cnt], %[so], 0*%c[js]+0*%c[ss]\n" \
  "buffer_store_dwordx4 v[224:227], v251, %[rs], %[cnt] offen sc1\n" \
  "v_mfma_f32_16x16x32_bf16 v[236:239], v[212:215], v[240:243], v[236:239]\n" \
  "s_waitcnt lgkmcnt(0)\n" \
  "v_mfma_f32_16x16x32_bf16 v[232:235], v[204:207], v[244:247], v[232:235]\n" \
  "v_mfma_f32_16x16x32_bf16 v[236:239], v[220:223], v[244:247], v[236:239]\n" \
  "ds_read_b128 v[240:243], v250 offset:1024\n" \
  "ds_read_b128 v[244:247], v250 offset:17408\n" \
  "v_mfma_f32_16x16x32_bf16 v[224:227], v[192:195], v[64:67], 0\n" \
  "v_mfma_f32_16x16x32_bf16 v[228:231], v[208:211], v[64:67], 0\n" \
  "v_mfma_f32_16x16x32_bf16 v[224:227], v[196:199], v[68:71], v[224:227]\n" \
  "v_cvt_pk_bf16_f32 v232, v232, v233\n" \
  "v_cvt_pk_bf16_f32 v234, v234, v235\n" \
  "v_mfma_f32_16x16x32_bf16 v[228:231], v[212:215], v[68:71], v[228:231]\n" \
  "v_cvt_pk_bf16_f32 v233, v236, v237\n" \
  "v_cvt_pk_bf16_f32 v235, v238, v239\n" \
  "v_mfma_f32_16x16x32_bf16 v[224:227], v[200:203], v[72:75], v[224:227]\n" \
  "s_add_u32 %[cnt], %[so], 4*%c[js]+0*%c[ss]\n" \
  "buffer_store_dwordx4 v[232:235], v251, %[rs], %[cnt] offen sc1\n" \
  "v_mfma_f32_16x16x32_bf16 v[228:231], v[216:219], v[72:75], v[228:231]\n" \
  "v_mfma_f32_16x16x32_bf16 v[224:227], v[204:207], v[76:79], v[224:227]\n" \
  "v_mfma_f32_16x16x32_bf16 v[228:231], v[220:223], v[76:79], v[228:231]\n" \
  "v_mfma_f32_16x16x32_bf16 v[232:235], v[192:195], v[132:135], 0\n" \
  "v_mfma_f32_16x16x32_bf16 v[236:239], v[208:211], v[132:135], 0\n" \
  "v_mfma_f32_16x16x32_bf16 v[232:235], v[200:203], v[140:143], v[232:235]\n" \
  "v_cvt_pk_bf16_f32 v224, v224, v225\n" \
  "v_cvt_pk_bf16_f32 v226, v226, v227\n" \
  "v_mfma_f32_16x16x32_bf16 v[236:239], v[216:219], v[140:143], v[236:239]\n" \
  "v_cvt_pk_bf16_f32 v225, v228, v229\n" \
  "v_cvt_pk_bf16_f32 v227, v230, v231\n" \
  "s_waitcnt lgkmcnt(1)\n" \
  "v_mfma_f32_16x16x32_bf16 v[232:235], v[196:199], v[240:243], v[232:235]\n" \
  "s_add_u32 %[cnt], %[so], 0*%c[js]+1*%c[ss]\n" \
  "buffer_store_dwordx4 v[224:227], v251, %[rs], %[cnt] offen sc1\n" \
  "v_mfma_f32_16x16x32_bf16 v[236:239], v[212:215], v[240:243], v[236:239]\n" \
  "s_waitcnt lgkmcnt(0)\n" \
  "v_mfma_f32_16x16x32_bf16 v[232:235], v[204:207], v[244:247], v[232:235]\n" \
  "v_mfma_f32_16x16x32_bf16 v[236:239], v[220:223], v[244:247], v[236:239]\n" \
  "ds_read_b128 v[240:243], v250 offset:32768\n" \
  "ds_read_b128 v[244:247], v250 offset:49152\n" \
  "v_mfma_f32_16x16x32_bf16 v[224:227], v[192:195], v[16:19], 0\n" \
  "v_mfma_f32_16x16x32_bf16 v[228:231], v[208:211], v[16:19], 0\n" \
  "v_mfma_f32_16x16x32_bf16 v[224:227], v[196:199], v[20:23], v[224:227]\n" \
  "v_cvt_pk_bf16_f32 v232, v232, v233\n" \
  "v_cvt_pk_bf16_f32 v234, v234, v235\n" \
  "v_mfma_f32_16x16x32_bf16 v[228:231], v[212:215], v[20:23], v[228:231]\n" \
  "v_cvt_pk_bf16_f32 v233, v236, v237\n" \
  "v_cvt_pk_bf16_f32 v235, v238, v239\n" \
  "v_mfma_f32_16x16x32_bf16 v[224:227], v[200:203], v[24:27], v[224:227]\n" \
  "s_add_u32 %[cnt], %[so], 4*%c[js]+1*%c[ss]\n" \
  "buffer_store_dwordx4 v[232:235], v251, %[rs], %[cnt] offen sc1\n" \
  "v_mfma_f32_16x16x32_bf16 v[228:231], v[216:219], v[24:27], v[228:231]\n" \
  "v_mfma_f32_16x16x32_bf16 v[224:227], v[204:207], v[28:31], v[224:227]\n" \
  "v_mfma_f32_16x16x32_bf16 v[228:231], v[220:223], v[28:31], v[228:231]\n" \
  "v_mfma_f32_16x16x32_bf16 v[232:235], v[192:195], v[144:147], 0\n" \
  "v_mfma_f32_16x16x32_bf16 v[236:239], v[208:211], v[144:147], 0\n" \
  "v_mfma_f32_16x16x32_bf16 v[232:235], v[200:203], v[152:155], v[232:235]\n" \
  "v_cvt_pk_bf16_f32 v224, v224, v225\n" \
  "v_cvt_pk_bf16_f32 v226, v226, v227\n" \
  "v_mfma_f32_16x16x32_bf16 v[236:239], v[216:219], v[152:155], v[236:239]\n" \
  "v_cvt_pk_bf16_f32 v225, v228, v229\n" \
  "v_cvt_pk_bf16_f32 v227, v230, v231\n" \
  "s_waitcnt lgkmcnt(1)\n" \
  "v_mfma_f32_16x16x32_bf16 v[232:235], v[196:199], v[240:243], v[232:235]\n" \
  "s_add_u32 %[cnt], %[so], 1*%c[js]+0*%c[ss]\n" \
  "buffer_store_dwordx4 v[224:227], v251, %[rs], %[cnt] offen sc1\n" \
  "v_mfma_f32_16x16x32_bf16 v[236:239], v[212:215], v[240:243], v[236:239]\n" \
  "s_waitcnt lgkmcnt(0)\n" \
  "v_mfma_f32_16x16x32_bf16 v[232:235], v[204:207], v[244:247], v[232:235]\n" \
  "v_mfma_f32_16x16x32_bf16 v[236:239], v[220:223], v[244:247], v[236:239]\n" \
  "ds_read_b128 v[240:243], v250 offset:33792\n" \
  "ds_read_b128 v[244:247], v250 offset:50176\n" \
  "v_mfma_f32_16x16x32_bf16 v[224:227], v[192:195], v[80:83], 0\n" \
  "v_mfma_f32_16x16x32_bf16 v[228:231], v[208:211], v[80:83], 0\n" \
  "v_mfma_f32_16x16x32_bf16 v[224:227], v[196:199], v[84:87], v[224:227]\n" \
  "v_cvt_pk_bf16_f32 v232, v232, v233\n" \
  "v_cvt_pk_bf16_f32 v234, v234, v235\n" \
  "v_mfma_f32_16x16x32_bf16 v[228:231], v[212:215], v[84:87], v[228:231]\n" \
  "v_cvt_pk_bf16_f32 v233, v236, v237\n" \
  "v_cvt_pk_bf16_f32 v235, v238, v239\n" \
  "v_mfma_f32_16x16x32_bf16 v[224:227], v[200:203], v[88:91], v[224:227]\n" \
  "s_add_u32 %[cnt], %[so], 5*%c[js]+0*%c[ss]\n" \
  "buffer_store_dwordx4 v[232:235], v251, %[rs], %[cnt] offen sc1\n" \
  "v_mfma_f32_16x16x32_bf16 v[228:231], v[216:219], v[88:91], v[228:231]\n" \
  "v_mfma_f32_16x16x32_bf16 v[224:227], v[204:207], v[92:95], v[224:227]\n" \
  "v_mfma_f32_16x16x32_bf16 v[228:231], v[220:223], v[92:95], v[228:231]\n" \
  "v_mfma_f32_16x16x32_bf16 v[232:235], v[192:195], v[148:151], 0\n" \
  "v_mfma_f32_16x16x32_bf16 v[236:239], v[208:211], v[148:151], 0\n" \
  "v_mfma_f32_16x16x32_bf16 v[232:235], v[200:203], v[156:159], v[232:235]\n" \
  "v_cvt_pk_bf16_f32 v224, v224, v225\n" \
  "v_cvt_pk_bf16_f32 v226, v226, v227\n" \
  "v_mfma_f32_16x16x32_bf16 v[236:239], v[216:219], v[156:159], v[236:239]\n" \
  "v_cvt_pk_bf16_f32 v225, v228, v229\n" \
  "v_cvt_pk_bf16_f32 v227, v230, v231\n" \
  "s_waitcnt lgkmcnt(1)\n" \
  "v_mfma_f32_16x16x32_bf16 v[232:235], v[196:199], v[240:243], v[232:235]\n" \
  "s_add_u32 %[cnt], %[so], 1*%c[js]+1*%c[ss]\n" \
  "buffer_store_dwordx4 v[224:227], v251, %[rs], %[cnt] offen sc1\n" \
  "v_mfma_f32_16x16x32_bf16 v[236:239], v[212:215], v[240:243], v[236:239]\n" \
  "s_waitcnt lgkmcnt(0)\n" \
  "v_mfma_f32_16x16x32_bf16 v[232:235], v[204:207], v[244:247], v[232:235]\n" \
  "v_mfma_f32_16x16x32_bf16 v[236:239], v[220:223], v[244:247], v[236:239]\n" \
  "ds_read_b128 v[240:243], v249 offset:0\n" \
  "ds_read_b128 v[244:247], v249 offset:16384\n" \
  "s_barrier\n" \
  "v_mfma_f32_16x16x32_bf16 v[224:227], v[192:195], v[32:35], 0\n" \
  "v_mfma_f32_16x16x32_bf16 v[228:231], v[208:211], v[32:35], 0\n" \
  "v_mfma_f32_16x16x32_bf16 v[224:227], v[196:199], v[36:39], v[224:227]\n" \
  "v_cvt_pk_bf16_f32 v232, v232, v233\n" \
  "v_cvt_pk_bf16_f32 v234, v234, v235\n" \
  "v_mfma_f32_16x16x32_bf16 v[228:231], v[212:215], v[36:39], v[228:231]\n" \
  "v_cvt_pk_bf16_f32 v233, v236, v237\n" \
  "v_cvt_pk_bf16_f32 v235, v238, v239\n" \
  "v_mfma_f32_16x16x32_bf16 v[224:227], v[200:203], v[40:43], v[224:227]\n" \
  "s_add_u32 %[cnt], %[so], 5*%c[js]+1*%c[ss]\n" \
  "buffer_store_dwordx4 v[232:235], v251, %[rs], %[cnt] offen sc1\n" \
  "v_mfma_f32_16x16x32_bf16 v[228:231], v[216:219], v[40:43], v[228:231]\n" \
  "v_mfma_f32_16x16x32_bf16 v[224:227], v[204:207], v[44:47], v[224:227]\n" \
  "v_mfma_f32_16x16x32_bf16 v[228:231], v[220:223], v[44:47], v[228:231]\n" \
  "v_mfma_f32_16x16x32_bf16 v[232:235], v[192:195], v[160:163], 0\n" \
  "v_mfma_f32_16x16x32_bf16 v[236:239], v[208:211], v[160:163], 0\n" \
  "v_mfma_f32_16x16x32_bf16 v[232:235], v[200:203], v[168:171], v[232:235]\n" \
  "v_cvt_pk_bf16_f32 v224, v224, v225\n" \
  "v_cvt_pk_bf16_f32 v226, v226, v227\n" \
  "v_mfma_f32_16x16x32_bf16 v[236:239], v[216:219], v[168:171], v[236:239]\n" \
  "v_cvt_pk_bf16_f32 v225, v228, v229\n" \
  "v_cvt_pk_bf16_f32 v227, v230, v231\n" \
  "s_waitcnt lgkmcnt(1)\n" \
  "v_mfma_f32_16x16x32_bf16 v[232:235], v[196:199], v[240:243], v[232:235]\n" \
  "s_add_u32 %[cnt], %[so], 2*%c[js]+0*%c[ss]\n" \
  "buffer_store_dwordx4 v[224:227], v251, %[rs], %[cnt] offen sc1\n" \
  "v_mfma_f32_16x16x32_bf16 v[236:239], v[212:215], v[240:243], v[236:239]\n" \
  "s_waitcnt lgkmcnt(0)\n" \
  "v_mfma_f32_16x16x32_bf16 v[232:235], v[204:207], v[244:247], v[232:235]\n" \
  "v_mfma_f32_16x16x32_bf16 v[236:239], v[220:223], v[244:247], v[236:239]\n" \
  "ds_read_b128 v[240:243], v249 offset:1024\n" \
  "ds_read_b128 v[244:247], v249 offset:17408\n" \
  "v_mfma_f32_16x16x32_bf16 v[224:227], v[192:195], v[96:99], 0\n" \
  "v_mfma_f32_16x16x32_bf16 v[228:231], v[208:211], v[96:99], 0\n" \
  "v_mfma_f32_16x16x32_bf16 v[224:227], v[196:199], v[100:103], v[224:227]\n" \
  "v_cvt_pk_bf16_f32 v232, v232, v233\n" \
  "v_cvt_pk_bf16_f32 v234, v234, v235\n" \
  "v_mfma_f32_16x16x32_bf16 v[228:231], v[212:215], v[100:103], v[228:231]\n" \
  "v_cvt_pk_bf16_f32 v233, v236, v237\n" \
  "v_cvt_pk_bf16_f32 v235, v238, v239\n" \
  "v_mfma_f32_16x16x32_bf16 v[224:227], v[200:203], v[104:107], v[224:227]\n" \
  "s_add_u32 %[cnt], %[so], 6*%c[js]+0*%c[ss]\n" \
  "buffer_store_dwordx4 v[232:235], v251, %[rs], %[cnt] offen sc1\n" \
  "v_mfma_f32_16x16x32_bf16 v[228:231], v[216:219], v[104:107], v[228:231]\n" \
  "v_mfma_f32_16x16x32_bf16 v[224:227], v[204:207], v[108:111], v[224:227]\n" \
  "v_mfma_f32_16x16x32_bf16 v[228:231], v[220:223], v[108:111], v[228:231]\n" \
  "v_mfma_f32_16x16x32_bf16 v[232:235], v[192:195], v[164:167], 0\n" \
  "v_mfma_f32_16x16x32_bf16 v[236:239], v[208:211], v[164:167], 0\n" \
  "v_mfma_f32_16x16x32_bf16 v[232:235], v[200:203], v[172:175], v[232:235]\n" \
  "v_cvt_pk_bf16_f32 v224, v224, v225\n" \
  "v_cvt_pk_bf16_f32 v226, v226, v227\n" \
  "v_mfma_f32_16x16x32_bf16 v[236:239], v[216:219], v[172:175], v[236:239]\n" \
  "v_cvt_pk_bf16_f32 v225, v228, v229\n" \
  "v_cvt_pk_bf16_f32 v227, v230, v231\n" \
  "s_waitcnt lgkmcnt(1)\n" \
  "v_mfma_f32_16x16x32_bf16 v[232:235], v[196:199], v[240:243], v[232:235]\n" \
  "s_add_u32 %[cnt], %[so], 2*%c[js]+1*%c[ss]\n" \
  "buffer_store_dwordx4 v[224:227], v251, %[rs], %[cnt] offen sc1\n" \
  "v_mfma_f32_16x16x32_bf16 v[236:239], v[212:215], v[240:243], v[236:239]\n" \
  "s_waitcnt lgkmcnt(0)\n" \
  "v_mfma_f32_16x16x32_bf16 v[232:235], v[204:207], v[244:247], v[232:235]\n" \
  "v_mfma_f32_16x16x32_bf16 v[236:239], v[220:223], v[244:247], v[236:239]\n" \
  "ds_read_b128 v[240:243], v249 offset:32768\n" \
  "ds_read_b128 v[244:247], v249 offset:49152\n" \
  "s_barrier\n" \
  "v_mfma_f32_16x16x32_bf16 v[224:227], v[192:195], v[48:51], 0\n" \
  "v_mfma_f32_16x16x32_bf16 v[228:231], v[208:211], v[48:51], 0\n" \
  "v_mfma_f32_16x16x32_bf16 v[224:227], v[196:199], v[52:55], v[224:227]\n" \
  "v_cvt_pk_bf16_f32 v232, v232, v233\n" \
  "v_cvt_pk_bf16_f32 v234, v234, v235\n" \
  "v_mfma_f32_16x16x32_bf16 v[228:231], v[212:215], v[52:55], v[228:231]\n" \
  "v_cvt_pk_bf16_f32 v233, v236, v237\n" \
  "v_cvt_pk_bf16_f32 v235, v238, v239\n" \
  "v_mfma_f32_16x16x32_bf16 v[224:227], v[200:203], v[56:59], v[224:227]\n" \
  "s_add_u32 %[cnt], %[so], 6*%c[js]+1*%c[ss]\n" \
  "buffer_store_dwordx4 v[232:235], v251, %[rs], %[cnt] offen sc1\n" \
  "v_mfma_f32_16x16x32_bf16 v[228:231], v[216:219], v[56:59], v[228:231]\n" \
  "v_mfma_f32_16x16x32_bf16 v[224:227], v[204:207], v[60:63], v[224:227]\n" \
  "v_mfma_f32_16x16x32_bf16 v[228:231], v[220:223], v[60:63], v[228:231]\n" \
  "v_mfma_f32_16x16x32_bf16 v[232:235], v[192:195], v[176:179], 0\n" \
  "v_mfma_f32_16x16x32_bf16 v[236:239], v[208:211], v[176:179], 0\n" \
  "v_mfma_f32_16x16x32_bf16 v[232:235], v[200:203], v[184:187], v[232:235]\n" \
  "v_cvt_pk_bf16_f32 v224, v224, v225\n" \
  "v_cvt_pk_bf16_f32 v226, v226, v227\n" \
  "v_mfma_f32_16x16x32_bf16 v[236:239], v[216:219], v[184:187], v[236:239]\n" \
  "v_cvt_pk_bf16_f32 v225, v228, v229\n" \
  "v_cvt_pk_bf16_f32 v227, v230, v231\n" \
  "s_waitcnt lgkmcnt(1)\n" \
  "v_mfma_f32_16x16x32_bf16 v[232:235], v[196:199], v[240:243], v[232:235]\n" \
  "s_add_u32 %[cnt], %[so], 3*%c[js]+0*%c[ss]\n" \
  "buffer_store_dwordx4 v[224:227], v251, %[rs], %[cnt] offen sc1\n" \
  "v_mfma_f32_16x16x32_bf16 v[236:239], v[212:215], v[240:243], v[236:239]\n" \
  "s_waitcnt lgkmcnt(0)\n" \
  "v_mfma_f32_16x16x32_bf16 v[232:235], v[204:207], v[244:247], v[232:235]\n" \
  "v_mfma_f32_16x16x32_bf16 v[236:239], v[220:223], v[244:247], v[236:239]\n" \
  "ds_read_b128 v[240:243], v249 offset:33792\n" \
  "ds_read_b128 v[244:247], v249 offset:50176\n" \
  "v_mfma_f32_16x16x32_bf16 v[224:227], v[192:195], v[112:115], 0\n" \
  "v_mfma_f32_16x16x32_bf16 v[228:231], v[208:211], v[112:115], 0\n" \
  "v_mfma_f32_16x16x32_bf16 v[224:227], v[196:199], v[116:119], v[224:227]\n" \
  "v_cvt_pk_bf16_f32 v232, v232, v233\n" \
  "v_cvt_pk_bf16_f32 v234, v234, v235\n" \
  "v_mfma_f32_16x16x32_bf16 v[228:231], v[212:215], v[116:119], v[228:231]\n" \
  "v_cvt_pk_bf16_f32 v233, v236, v237\n" \
  "v_cvt_pk_bf16_f32 v235, v238, v239\n" \
  "v_mfma_f32_16x16x32_bf16 v[224:227], v[200:203], v[120:123], v[224:227]\n" \
  "s_add_u32 %[cnt], %[so], 7*%c[js]+0*%c[ss]\n" \
  "buffer_store_dwordx4 v[232:235], v251, %[rs], %[cnt] offen sc1\n" \
  "v_mfma_f32_16x16x32_bf16 v[228:231], v[216:219], v[120:123], v[228:231]\n" \
  "v_mfma_f32_16x16x32_bf16 v[224:227], v[204:207], v[124:127], v[224:227]\n" \
  "v_mfma_f32_16x16x32_bf16 v[228:231], v[220:223], v[124:127], v[228:231]\n" \
  "v_mfma_f32_16x16x32_bf16 v[232:235], v[192:195], v[180:183], 0\n" \
  "v_mfma_f32_16x16x32_bf16 v[236:239], v[208:211], v[180:183], 0\n" \
  "v_mfma_f32_16x16x32_bf16 v[232:235], v[200:203], v[188:191], v[232:235]\n" \
  "v_cvt_pk_bf16_f32 v224, v224, v225\n" \
  "v_cvt_pk_bf16_f32 v226, v226, v227\n" \
  "v_mfma_f32_16x16x32_bf16 v[236:239], v[216:219], v[188:191], v[236:239]\n" \
  "v_cvt_pk_bf16_f32 v225, v228, v229\n" \
  "v_cvt_pk_bf16_f32 v227, v230, v231\n" \
  "s_waitcnt lgkmcnt(1)\n" \
  "v_mfma_f32_16x16x32_bf16 v[232:235], v[196:199], v[240:243], v[232:235]\n" \
  "s_add_u32 %[cnt], %[so], 3*%c[js]+1*%c[ss]\n" \
  "buffer_store_dwordx4 v[224:227], v251, %[rs], %[cnt] offen sc1\n" \
  "v_mfma_f32_16x16x32_bf16 v[236:239], v[212:215], v[240:243], v[236:239]\n" \
  "s_waitcnt lgkmcnt(0)\n" \
  "v_mfma_f32_16x16x32_bf16 v[232:235], v[204:207], v[244:247], v[232:235]\n" \
  "v_mfma_f32_16x16x32_bf16 v[236:239], v[220:223], v[244:247], v[236:239]\n" \
  "s_nop 4\n" \
  "v_cvt_pk_bf16_f32 v232, v232, v233\n" \
  "v_cvt_pk_bf16_f32 v234, v234, v235\n" \
  "s_nop 1\n" \
  "v_cvt_pk_bf16_f32 v233, v236, v237\n" \
  "v_cvt_pk_bf16_f32 v235, v238, v239\n" \
  "s_add_u32 %[cnt], %[so], 7*%c[js]+1*%c[ss]\n" \
  "buffer_store_dwordx4 v[232:235], v251, %[rs], %[cnt] offen sc1\n" \
  "s_nop 1\n"
#define ST_XB0 \
  "ds_read_b128 v[192:195], v251 offset:24576\n" \
  "ds_read_b128 v[208:211], v251 offset:28672\n" \
  "ds_read_b128 v[196:199], v251 offset:25600\n" \
  "ds_read_b128 v[212:215], v251 offset:29696\n" \
  "ds_read_b128 v[200:203], v251 offset:26624\n" \
  "ds_read_b128 v[216:219], v251 offset:30720\n" \
  "ds_read_b128 v[204:207], v251 offset:27648\n" \
  "ds_read_b128 v[220:223], v251 offset:31744\n" \
  "ds_read_b128 v[240:243], v250 offset:0\n" \
  "ds_read_b128 v[244:247], v250 offset:16384\n" \
  "s_waitcnt lgkmcnt(9)\n" \
  "v_mfma_f32_16x16x32_bf16 v[224:227], v[192:195], v[0:3], 0\n" \
  "s_waitcnt lgkmcnt(8)\n" \
  "v_mfma_f32_16x16x32_bf16 v[228:231], v[208:211], v[0:3], 0\n" \
  "s_waitcnt lgkmcnt(7)\n" \
  "v_mfma_f32_16x16x32_bf16 v[224:227], v[196:199], v[4:7], v[224:227]\n" \
  "s_waitcnt lgkmcnt(6)\n" \
  "v_mfma_f32_16x16x32_bf16 v[228:231], v[212:215], v[4:7], v[228:231]\n" \
  "s_waitcnt lgkmcnt(5)\n" \
  "v_mfma_f32_16x16x32_bf16 v[224:227], v[200:203], v[8:11], v[224:227]\n" \
  "s_waitcnt lgkmcnt(4)\n" \
  "v_mfma_f32_16x16x32_bf16 v[228:231], v[216:219], v[8:11], v[228:231]\n" \
  "s_waitcnt lgkmcnt(3)\n" \
  "v_mfma_f32_16x16x32_bf16 v[224:227], v[204:207], v[12:15], v[224:227]\n" \
  "s_waitcnt lgkmcnt(2)\n" \
  "v_mfma_f32_16x16x32_bf16 v[228:231], v[220:223], v[12:15], v[228:231]\n" \
  "v_mfma_f32_16x16x32_bf16 v[232:235], v[192:195], v[128:131], 0\n" \
  "v_mfma_f32_16x16x32_bf16 v[236:239], v[208:211], v[128:131], 0\n" \
  "v_mfma_f32_16x16x32_bf16 v[232:235], v[200:203], v[136:139], v[232:235]\n" \
  "v_cvt_pk_bf16_f32 v224, v224, v225\n" \
  "v_cvt_pk_bf16_f32 v226, v226, v227\n" \
  "v_mfma_f32_16x16x32_bf16 v[236:239], v[216:219], v[136:139], v[236:239]\n" \
  "v_cvt_pk_bf16_f32 v225, v228, v229\n" \
  "v_cvt_pk_bf16_f32 v227, v230, v231\n" \
  "s_waitcnt lgkmcnt(1)\n" \
  "v_mfma_f32_16x16x32_bf16 v[232:235], v[196:199], v[240:243], v[232:235]\n" \
  "s_add_u32 %[cnt], %[so], 0*%c[js]+0*%c[ss]\n" \
  "buffer_store_dwordx4 v[224:227], v251, %[rs], %[cnt] offen sc1\n" \
  "v_mfma_f32_16x16x32_bf16 v[236:239], v[212:215], v[240:243], v[236:239]\n" \
  "s_waitcnt lgkmcnt(0)\n" \
  "v_mfma_f32_16x16x32_bf16 v[232:235], v[204:207], v[244:247], v[232:235]\n" \
  "v_mfma_f32_16x16x32_bf16 v[236:239], v[220:223], v[244:247], v[236:239]\n" \
  "ds_read_b128 v[240:243], v250 offset:1024\n" \
  "ds_read_b128 v[244:247], v250 offset:17408\n" \
  "v_mfma_f32_16x16x32_bf16 v[224:227], v[192:195], v[64:67], 0\n" \
  "v_mfma_f32_16x16x32_bf16 v[228:231], v[208:211], v[64:67], 0\n" \
  "v_mfma_f32_16x16x32_bf16 v[224:227], v[196:199], v[68:71], v[224:227]\n" \
  "v_cvt_pk_bf16_f32 v232, v232, v233\n" \
  "v_cvt_pk_bf16_f32 v234, v234, v235\n" \
  "v_mfma_f32_16x16x32_bf16 v[228:231], v[212:215], v[68:71], v[228:231]\n" \
  "v_cvt_pk_bf16_f32 v233, v236, v237\n" \
  "v_cvt_pk_bf16_f32 v235, v238, v239\n" \
  "v_mfma_f32_16x16x32_bf16 v[224:227], v[200:203], v[72:75], v[224:227]\n" \
  "s_add_u32 %[cnt], %[so], 4*%c[js]+0*%c[ss]\n" \
  "buffer_store_dwordx4 v[232:235], v251, %[rs], %[cnt] offen sc1\n" \
  "v_mfma_f32_16x16x32_bf16 v[228:231], v[216:219], v[72:75], v[228:231]\n" \
  "v_mfma_f32_16x16x32_bf16 v[224:227], v[204:207], v[76:79], v[224:227]\n" \
  "v_mfma_f32_16x16x32_bf16 v[228:231], v[220:223], v[76:79], v[228:231]\n" \
  "v_mfma_f32_16x16x32_bf16 v[232:235], v[192:195], v[132:135], 0\n" \
  "v_mfma_f32_16x16x32_bf16 v[236:239], v[208:211], v[132:135], 0\n" \
  "v_mfma_f32_16x16x32_bf16 v[232:235], v[200:203], v[140:143], v[232:235]\n" \
  "v_cvt_pk_bf16_f32 v224, v224, v225\n" \
  "v_cvt_pk_bf16_f32 v226, v226, v227\n" \
  "v_mfma_f32_16x16x32_bf16 v[236:239], v[216:219], v[140:143], v[236:239]\n" \
  "v_cvt_pk_bf16_f32 v225, v228, v229\n" \
  "v_cvt_pk_bf16_f32 v227, v230, v231\n" \
  "s_waitcnt lgkmcnt(1)\n" \
  "v_mfma_f32_16x16x32_bf16 v[232:235], v[196:199], v[240:243], v[232:235]\n" \
  "s_add_u32 %[cnt], %[so], 0*%c[js]+1*%c[ss]\n" \
  "buffer_store_dwordx4 v[224:227], v251, %[rs], %[cnt] offen sc1\n" \
  "v_mfma_f32_16x16x32_bf16 v[236:239], v[212:215], v[240:243], v[236:239]\n" \
  "s_waitcnt lgkmcnt(0)\n" \
  "v_mfma_f32_16x16x32_bf16 v[232:235], v[204:207], v[244:247], v[232:235]\n" \
  "v_mfma_f32_16x16x32_bf16 v[236:239], v[220:223], v[244:247], v[236:239]\n" \
  "ds_read_b128 v[240:243], v250 offset:32768\n" \
  "ds_read_b128 v[244:247], v250 offset:49152\n" \
  "v_mfma_f32_16x16x32_bf16 v[224:227], v[192:195], v[16:19], 0\n" \
  "v_mfma_f32_16x16x32_bf16 v[228:231], v[208:211], v[16:19], 0\n" \
  "v_mfma_f32_16x16x32_bf16 v[224:227], v[196:199], v[20:23], v[224:227]\n" \
  "v_cvt_pk_bf16_f32 v232, v232, v233\n" \
  "v_cvt_pk_bf16_f32 v234, v234, v235\n" \
  "v_mfma_f32_16x16x32_bf16 v[228:231], v[212:215], v[20:23], v[228:231]\n" \
  "v_cvt_pk_bf16_f32 v233, v236, v237\n" \
  "v_cvt_pk_bf16_f32 v235, v238, v239\n" \
  "v_mfma_f32_16x16x32_bf16 v[224:227], v[200:203], v[24:27], v[224:227]\n" \
  "s_add_u32 %[cnt], %[so], 4*%c[js]+1*%c[ss]\n" \
  "buffer_store_dwordx4 v[232:235], v251, %[rs], %[cnt] offen sc1\n" \
  "v_mfma_f32_16x16x32_bf16 v[228:231], v[216:219], v[24:27], v[228:231]\n" \
  "v_mfma_f32_16x16x32_bf16 v[224:227], v[204:207], v[28:31], v[224:227]\n" \
  "v_mfma_f32_16x16x32_bf16 v[228:231], v[220:223], v[28:31], v[228:231]\n" \
  "v_mfma_f32_16x16x32_bf16 v[232:235], v[192:195], v[144:147], 0\n" \
  "v_mfma_f32_16x16x32_bf16 v[236:239], v[208:211], v[144:147], 0\n" \
  "v_mfma_f32_16x16x32_bf16 v[232:235], v[200:203], v[152:155], v[232:235]\n" \
  "v_cvt_pk_bf16_f32 v224, v224, v225\n" \
  "v_cvt_pk_bf16_f32 v226, v226, v227\n" \
  "v_mfma_f32_16x16x32_bf16 v[236:239], v[216:219], v[152:155], v[236:239]\n" \
  "v_cvt_pk_bf16_f32 v225, v228, v229\n" \
  "v_cvt_pk_bf16_f32 v227, v230, v231\n" \
  "s_waitcnt lgkmcnt(1)\n" \
  "v_mfma_f32_16x16x32_bf16 v[232:235], v[196:199], v[240:243], v[232:235]\n" \
  "s_add_u32 %[cnt], %[so], 1*%c[js]+0*%c[ss]\n" \
  "buffer_store_dwordx4 v[224:227], v251, %[rs], %[cnt] offen sc1\n" \
  "v_mfma_f32_16x16x32_bf16 v[236:239], v[212:215], v[240:243], v[236:239]\n" \
  "s_waitcnt lgkmcnt(0)\n" \
  "v_mfma_f32_16x16x32_bf16 v[232:235], v[204:207], v[244:247], v[232:235]\n" \
  "v_mfma_f32_16x16x32_bf16 v[236:239], v[220:223], v[244:247], v[236:239]\n" \
  "ds_read_b128 v[240:243], v250 offset:33792\n" \
  "ds_read_b128 v[244:247], v250 offset:50176\n" \
  "v_mfma_f32_16x16x32_bf16 v[224:227], v[192:195], v[80:83], 0\n" \
  "v_mfma_f32_16x16x32_bf16 v[228:231], v[208:211], v[80:83], 0\n" \
  "v_mfma_f32_16x16x32_bf16 v[224:227], v[196:199], v[84:87], v[224:227]\n" \
  "v_cvt_pk_bf16_f32 v232, v232, v233\n" \
  "v_cvt_pk_bf16_f32 v234, v234, v235\n" \
  "v_mfma_f32_16x16x32_bf16 v[228:231], v[212:215], v[84:87], v[228:231]\n" \
  "v_cvt_pk_bf16_f32 v233, v236, v237\n" \
  "v_cvt_pk_bf16_f32 v235, v238, v239\n" \
  "v_mfma_f32_16x16x32_bf16 v[224:227], v[200:203], v[88:91], v[224:227]\n" \
  "s_add_u32 %[cnt], %[so], 5*%c[js]+0*%c[ss]\n" \
  "buffer_store_dwordx4 v[232:235], v251, %[rs], %[cnt] offen sc1\n" \
  "v_mfma_f32_16x16x32_bf16 v[228:231], v[216:219], v[88:91], v[228:231]\n" \
  "v_mfma_f32_16x16x32_bf16 v[224:227], v[204:207], v[92:95], v[224:227]\n" \
  "v_mfma_f32_16x16x32_bf16 v[228:231], v[220:223], v[92:95], v[228:231]\n" \
  "v_mfma_f32_16x16x32_bf16 v[232:235], v[192:195], v[148:151], 0\n" \
  "v_mfma_f32_16x16x32_bf16 v[236:239], v[208:211], v[148:151], 0\n" \
  "v_mfma_f32_16x16x32_bf16 v[232:235], v[200:203], v[156:159], v[232:235]\n" \
  "v_cvt_pk_bf16_f32 v224, v224, v225\n" \
  "v_cvt_pk_bf16_f32 v226, v226, v227\n" \
  "v_mfma_f32_16x16x32_bf16 v[236:239], v[216:219], v[156:159], v[236:239]\n" \
  "v_cvt_pk_bf16_f32 v225, v228, v229\n" \
  "v_cvt_pk_bf16_f32 v227, v230, v231\n" \
  "s_waitcnt lgkmcnt(1)\n" \
  "v_mfma_f32_16x16x32_bf16 v[232:235], v[196:199], v[240:243], v[232:235]\n" \
  "s_add_u32 %[cnt], %[so], 1*%c[js]+1*%c[ss]\n" \
  "buffer_store_dwordx4 v[224:227], v251, %[rs], %[cnt] offen sc1\n" \
  "v_mfma_f32_16x16x32_bf16 v[236:239], v[212:215], v[240:243], v[236:239]\n" \
  "s_waitcnt lgkmcnt(0)\n" \
  "v_mfma_f32_16x16x32_bf16 v[232:235], v[204:207], v[244:247], v[232:235]\n" \
  "v_mfma_f32_16x16x32_bf16 v[236:239], v[220:223], v[244:247], v[236:239]\n" \
  "ds_read_b128 v[240:243], v249 offset:0\n" \
  "ds_read_b128 v[244:247], v249 offset:16384\n" \
  "v_mfma_f32_16x16x32_bf16 v[224:227], v[192:195], v[32:35], 0\n" \
  "v_mfma_f32_16x16x32_bf16 v[228:231], v[208:211], v[32:35], 0\n" \
  "v_mfma_f32_16x16x32_bf16 v[224:227], v[196:199], v[36:39], v[224:227]\n" \
  "v_cvt_pk_bf16_f32 v232, v232, v233\n" \
  "v_cvt_pk_bf16_f32 v234, v234, v235\n" \
  "v_mfma_f32_16x16x32_bf16 v[228:231], v[212:215], v[36:39], v[228:231]\n" \
  "v_cvt_pk_bf16_f32 v233, v236, v237\n" \
  "v_cvt_pk_bf16_f32 v235, v238, v239\n" \
  "v_mfma_f32_16x16x32_bf16 v[224:227], v[200:203], v[40:43], v[224:227]\n" \
  "s_add_u32 %[cnt], %[so], 5*%c[js]+1*%c[ss]\n" \
  "buffer_store_dwordx4 v[232:235], v251, %[rs], %[cnt] offen sc1\n" \
  "v_mfma_f32_16x16x32_bf16 v[228:231], v[216:219], v[40:43], v[228:231]\n" \
  "v_mfma_f32_16x16x32_bf16 v[224:227], v[204:207], v[44:47], v[224:227]\n" \
  "v_mfma_f32_16x16x32_bf16 v[228:231], v[220:223], v[44:47], v[228:231]\n" \
  "v_mfma_f32_16x16x32_bf16 v[232:235], v[192:195], v[160:163], 0\n" \
  "v_mfma_f32_16x16x32_bf16 v[236:239], v[208:211], v[160:163], 0\n" \
  "v_mfma_f32_16x16x32_bf16 v[232:235], v[200:203], v[168:171], v[232:235]\n" \
  "v_cvt_pk_bf16_f32 v224, v224, v225\n" \
  "v_cvt_pk_bf16_f32 v226, v226, v227\n" \
  "v_mfma_f32_16x16x32_bf16 v[236:239], v[216:219], v[168:171], v[236:239]\n" \
  "v_cvt_pk_bf16_f32 v225, v228, v229\n" \
  "v_cvt_pk_bf16_f32 v227, v230, v231\n" \
  "s_waitcnt lgkmcnt(1)\n" \
  "v_mfma_f32_16x16x32_bf16 v[232:235], v[196:199], v[240:243], v[232:235]\n" \
  "s_add_u32 %[cnt], %[so], 2*%c[js]+0*%c[ss]\n" \
  "buffer_store_dwordx4 v[224:227], v251, %[rs], %[cnt] offen sc1\n" \
  "v_mfma_f32_16x16x32_bf16 v[236:239], v[212:215], v[240:243], v[236:239]\n" \
  "s_waitcnt lgkmcnt(0)\n" \
  "v_mfma_f32_16x16x32_bf16 v[232:235], v[204:207], v[244:247], v[232:235]\n" \
  "v_mfma_f32_16x16x32_bf16 v[236:239], v[220:223], v[244:247], v[236:239]\n" \
  "ds_read_b128 v[240:243], v249 offset:1024\n" \
  "ds_read_b128 v[244:247], v249 offset:17408\n" \
  "v_mfma_f32_16x16x32_bf16 v[224:227], v[192:195], v[96:99], 0\n" \
  "v_mfma_f32_16x16x32_bf16 v[228:231], v[208:211], v[96:99], 0\n" \
  "v_mfma_f32_16x16x32_bf16 v[224:227], v[196:199], v[100:103], v[224:227]\n" \
  "v_cvt_pk_bf16_f32 v232, v232, v233\n" \
  "v_cvt_pk_bf16_f32 v234, v234, v235\n" \
  "v_mfma_f32_16x16x32_bf16 v[228:231], v[212:215], v[100:103], v[228:231]\n" \
  "v_cvt_pk_bf16_f32 v233, v236, v237\n" \
  "v_cvt_pk_bf16_f32 v235, v238, v239\n" \
  "v_mfma_f32_16x16x32_bf16 v[224:227], v[200:203], v[104:107], v[224:227]\n" \
  "s_add_u32 %[cnt], %[so], 6*%c[js]+0*%c[ss]\n" \
  "buffer_store_dwordx4 v[232:235], v251, %[rs], %[cnt] offen sc1\n" \
  "v_mfma_f32_16x16x32_bf16 v[228:231], v[216:219], v[104:107], v[228:231]\n" \
  "v_mfma_f32_16x16x32_bf16 v[224:227], v[204:207], v[108:111], v[224:227]\n" \
  "v_mfma_f32_16x16x32_bf16 v[228:231], v[220:223], v[108:111], v[228:231]\n" \
  "v_mfma_f32_16x16x32_bf16 v[232:235], v[192:195], v[164:167], 0\n" \
  "v_mfma_f32_16x16x32_bf16 v[236:239], v[208:211], v[164:167], 0\n" \
  "v_mfma_f32_16x16x32_bf16 v[232:235], v[200:203], v[172:175], v[232:235]\n" \
  "v_cvt_pk_bf16_f32 v224, v224, v225\n" \
  "v_cvt_pk_bf16_f32 v226, v226, v227\n" \
  "v_mfma_f32_16x16x32_bf16 v[236:239], v[216:219], v[172:175], v[236:239]\n" \
  "v_cvt_pk_bf16_f32 v225, v228, v229\n" \
  "v_cvt_pk_bf16_f32 v227, v230, v231\n" \
  "s_waitcnt lgkmcnt(1)\n" \
  "v_mfma_f32_16x16x32_bf16 v[232:235], v[196:199], v[240:243], v[232:235]\n" \
  "s_add_u32 %[cnt], %[so], 2*%c[js]+1*%c[ss]\n" \
  "buffer_store_dwordx4 v[224:227], v251, %[rs], %[cnt] offen sc1\n" \
  "v_mfma_f32_16x16x32_bf16 v[236:239], v[212:215], v[240:243], v[236:239]\n" \
  "s_waitcnt lgkmcnt(0)\n" \
  "v_mfma_f32_16x16x32_bf16 v[232:235], v[204:207], v[244:247], v[232:235]\n" \
  "v_mfma_f32_16x16x32_bf16 v[236:239], v[220:223], v[244:247], v[236:239]\n" \
  "ds_read_b128 v[240:243], v249 offset:32768\n" \
  "ds_read_b128 v[244:247], v249 offset:49152\n" \
  "v_mfma_f32_16x16x32_bf16 v[224:227], v[192:195], v[48:51], 0\n" \
  "v_mfma_f32_16x16x32_bf16 v[228:231], v[208:211], v[48:51], 0\n" \
  "v_mfma_f32_16x16x32_bf16 v[224:227], v[196:199], v[52:55], v[224:227]\n" \
  "v_cvt_pk_bf16_f32 v232, v232, v233\n" \
  "v_cvt_pk_bf16_f32 v234, v234, v235\n" \
  "v_mfma_f32_16x16x32_bf16 v[228:231], v[212:215], v[52:55], v[228:231]\n" \
  "v_cvt_pk_bf16_f32 v233, v236, v237\n" \
  "v_cvt_pk_bf16_f32 v235, v238, v239\n" \
  "v_mfma_f32_16x16x32_bf16 v[224:227], v[200:203], v[56:59], v[224:227]\n" \
  "s_add_u32 %[cnt], %[so], 6*%c[js]+1*%c[ss]\n" \
  "buffer_store_dwordx4 v[232:235], v251, %[rs], %[cnt] offen sc1\n" \
  "v_mfma_f32_16x16x32_bf16 v[228:231], v[216:219], v[56:59], v[228:231]\n" \
  "v_mfma_f32_16x16x32_bf16 v[224:227], v[204:207], v[60:63], v[224:227]\n" \
  "v_mfma_f32_16x16x32_bf16 v[228:231], v[220:223], v[60:63], v[228:231]\n" \
  "v_mfma_f32_16x16x32_bf16 v[232:235], v[192:195], v[176:179], 0\n" \
  "v_mfma_f32_16x16x32_bf16 v[236:239], v[208:211], v[176:179], 0\n" \
  "v_mfma_f32_16x16x32_bf16 v[232:235], v[200:203], v[184:187], v[232:235]\n" \
  "v_cvt_pk_bf16_f32 v224, v224, v225\n" \
  "v_cvt_pk_bf16_f32 v226, v226, v227\n" \
  "v_mfma_f32_16x16x32_bf16 v[236:239], v[216:219], v[184:187], v[236:239]\n" \
  "v_cvt_pk_bf16_f32 v225, v228, v229\n" \
  "v_cvt_pk_bf16_f32 v227, v230, v231\n" \
  "s_waitcnt lgkmcnt(1)\n" \
  "v_mfma_f32_16x16x32_bf16 v[232:235], v[196:199], v[240:243], v[232:235]\n" \
  "s_add_u32 %[cnt], %[so], 3*%c[js]+0*%c[ss]\n" \
  "buffer_store_dwordx4 v[224:227], v251, %[rs], %[cnt] offen sc1\n" \
  "v_mfma_f32_16x16x32_bf16 v[236:239], v[212:215], v[240:243], v[236:239]\n" \
  "s_waitcnt lgkmcnt(0)\n" \
  "v_mfma_f32_16x16x32_bf16 v[232:235], v[204:207], v[244:247], v[232:235]\n" \
  "v_mfma_f32_16x16x32_bf16 v[236:239], v[220:223], v[244:247], v[236:239]\n" \
  "ds_read_b128 v[240:243], v249 offset:33792\n" \
  "ds_read_b128 v[244:247], v249 offset:50176\n" \
  "v_mfma_f32_16x16x32_bf16 v[224:227], v[192:195], v[112:115], 0\n" \
  "v_mfma_f32_16x16x32_bf16 v[228:231], v[208:211], v[112:115], 0\n" \
  "v_mfma_f32_16x16x32_bf16 v[224:227], v[196:199], v[116:119], v[224:227]\n" \
  "v_cvt_pk_bf16_f32 v232, v232, v233\n" \
  "v_cvt_pk_bf16_f32 v234, v234, v235\n" \
  "v_mfma_f32_16x16x32_bf16 v[228:231], v[212:215], v[116:119], v[228:231]\n" \
  "v_cvt_pk_bf16_f32 v233, v236, v237\n" \
  "v_cvt_pk_bf16_f32 v235, v238, v239\n" \
  "v_mfma_f32_16x16x32_bf16 v[224:227], v[200:203], v[120:123], v[224:227]\n" \
  "s_add_u32 %[cnt], %[so], 7*%c[js]+0*%c[ss]\n" \
  "buffer_store_dwordx4 v[232:235], v251, %[rs], %[cnt] offen sc1\n" \
  "v_mfma_f32_16x16x32_bf16 v[228:231], v[216:219], v[120:123], v[228:231]\n" \
  "v_mfma_f32_16x16x32_bf16 v[224:227], v[204:207], v[124:127], v[224:227]\n" \
  "v_mfma_f32_16x16x32_bf16 v[228:231], v[220:223], v[124:127], v[228:231]\n" \
  "v_mfma_f32_16x16x32_bf16 v[232:235], v[192:195], v[180:183], 0\n" \
  "v_mfma_f32_16x16x32_bf16 v[236:239], v[208:211], v[180:183], 0\n" \
  "v_mfma_f32_16x16x32_bf16 v[232:235], v[200:203], v[188:191], v[232:235]\n" \
  "v_cvt_pk_bf16_f32 v224, v224, v225\n" \
  "v_cvt_pk_bf16_f32 v226, v226, v227\n" \
  "v_mfma_f32_16x16x32_bf16 v[236:239], v[216:219], v[188:191], v[236:239]\n" \
  "v_cvt_pk_bf16_f32 v225, v228, v229\n" \
  "v_cvt_pk_bf16_f32 v227, v230, v231\n" \
  "s_waitcnt lgkmcnt(1)\n" \
  "v_mfma_f32_16x16x32_bf16 v[232:235], v[196:199], v[240:243], v[232:235]\n" \
  "s_add_u32 %[cnt], %[so], 3*%c[js]+1*%c[ss]\n" \
  "buffer_store_dwordx4 v[224:227], v251, %[rs], %[cnt] offen sc1\n" \
  "v_mfma_f32_16x16x32_bf16 v[236:239], v[212:215], v[240:243], v[236:239]\n" \
  "s_waitcnt lgkmcnt(0)\n" \
  "v_mfma_f32_16x16x32_bf16 v[232:235], v[204:207], v[244:247], v[232:235]\n" \
  "v_mfma_f32_16x16x32_bf16 v[236:239], v[220:223], v[244:247], v[236:239]\n" \
  "s_nop 4\n" \
  "v_cvt_pk_bf16_f32 v232, v232, v233\n" \
  "v_cvt_pk_bf16_f32 v234, v234, v235\n" \
  "s_nop 1\n" \
  "v_cvt_pk_bf16_f32 v233, v236, v237\n" \
  "v_cvt_pk_bf16_f32 v235, v238, v239\n" \
  "s_add_u32 %[cnt], %[so], 7*%c[js]+1*%c[ss]\n" \
  "buffer_store_dwordx4 v[232:235], v251, %[rs], %[cnt] offen sc1\n" \
  "s_nop 1\n"
#define GDM 2
#define XBF_NB 5
#define XBS_NB 2
#define F_ORD "01ab2cd3"
#define VO_CLOB "v192", "v193", "v194", "v195", "v196", "v197", "v198", "v199", "v200", "v201", "v202", "v203", "v204", "v205", "v206", "v207", "v208", "v209", "v210", "v211", "v212", "v213", "v214", "v215", "v216", "v217", "v218", "v219", "v220", "v221", "v222", "v223", "v224", "v225", "v226", "v227", "v228", "v229", "v230", "v231", "v232", "v233", "v234", "v235", "v236", "v237", "v238", "v239"
#define QK_CLOB "v192", "v193", "v194", "v195", "v196", "v197", "v198", "v199", "v200", "v201", "v202", "v203", "v204", "v205", "v206", "v207", "v208", "v209", "v210", "v211", "v212", "v213", "v214", "v215", "v216", "v217", "v218", "v219", "v220", "v221", "v222", "v223", "v224", "v225", "v226", "v227", "v228", "v229", "v230", "v231", "v232", "v233", "v234", "v235", "v236", "v237", "v238", "v239"
#define QKK_CLOB "v192", "v193", "v194", "v195", "v196", "v197", "v198", "v199", "v200", "v201", "v202", "v203", "v204", "v205", "v206", "v207", "v208", "v209", "v210", "v211", "v212", "v213", "v214", "v215", "v216", "v217", "v218", "v219", "v220", "v221", "v222", "v223", "v224", "v225", "v226", "v227", "v228", "v229", "v230", "v231", "v232", "v233", "v234", "v235", "v236", "v238", "v239"
#define QX_CLOB "v216", "v217", "v218", "v219", "v220", "v221", "v222", "v223", "v224", "v225", "v226", "v227", "v228", "v229", "v230", "v231", "v232", "v233", "v234", "v235", "v236", "v237", "v238", "v239", "v241"
#define ST_CLOB "v192", "v193", "v194", "v195", "v196", "v197", "v198", "v199", "v200", "v201", "v202", "v203", "v204", "v205", "v206", "v207", "v240", "v241", "v242", "v243", "v244", "v245", "v246", "v247", "v253"
#define XB_CLOB "v192", "v193", "v194", "v195", "v196", "v197", "v198", "v199", "v200", "v201", "v202", "v203", "v204", "v205", "v206", "v207", "v208", "v209", "v210", "v211", "v212", "v213", "v214", "v215", "v216", "v217", "v218", "v219", "v220", "v221", "v222", "v223", "v224", "v225", "v226", "v227", "v228", "v229", "v230", "v231", "v232", "v233", "v234", "v235", "v236", "v237", "v238", "v239", "v240", "v241", "v242", "v243", "v244", "v245", "v246", "v247", "v253"
#define OF_CLOB "v32", "v33", "v34", "v35", "v36", "v37", "v38", "v39", "v40", "v41", "v42", "v43", "v44", "v45", "v46", "v47", "v48", "v49", "v50", "v51", "v52", "v53", "v54", "v55", "v56", "v57", "v58", "v59", "v60", "v61", "v62", "v63", "v64", "v65", "v66", "v67", "v68", "v69", "v70", "v71", "v72", "v73", "v74", "v75", "v76", "v77", "v78", "v79", "v80", "v81", "v82", "v83", "v84", "v85", "v86", "v87", "v88", "v89", "v90", "v91", "v92", "v93", "v94", "v95", "v96", "v97", "v98", "v99", "v100", "v101", "v102", "v103", "v104", "v105", "v106", "v107", "v108", "v109", "v110", "v111"
#define OE_CLOB "v192", "v193", "v194", "v195", "v196", "v197", "v198", "v199", "v200", "v201", "v202", "v203", "v204", "v205", "v206", "v207", "v208", "v209", "v210", "v211", "v212", "v213", "v214", "v215", "v216", "v217", "v218", "v219", "v220", "v221", "v222", "v223", "v224", "v225", "v226", "v227", "v228", "v229", "v230", "v231", "v232", "v233", "v234", "v235", "v236", "v237", "v238", "v239", "v240"
constexpr int L_W = 32768;
constexpr int L_HI = 16384;
#define MARG 130
#define ED0 820
#define EA0 1320
#define SDK 448
#define TOL 145
#define XLATE 200
#define TFR 800
#define QT 0
#define HSU 8
#define WNT 3
#define VAL 1
#define KROT 3
#define KSL2 12
#define KSL 2
#define NCY 1825
#define NCYV NCY
#define NRY 22
#define NRX 30
#define NF1O 2
#define NPUB 2
#define NXF 4
#define RSL 2
#define PFL 4
#define EVT2R
__device__ __forceinline__ int slow_head(const u16* wm) { return (int)(((3u - (unsigned)((uintptr_t)wm >> 7)) & 7u) >> 1); }
template <int role>
__device__ __forceinline__ void wroles(const int h, const int par, const int sl, const int xg, const int w, const u64 nonce, const u16* __restrict__ x,
                                       const u16* __restrict__ Wq, const u16* __restrict__ Wk, const u16* __restrict__ Wv,
                                       const u16* __restrict__ Wo, char* ws) {
  LAS char* const L = (LAS char*)g_lds;
  gu32* tmo = (gu32*)(ws + OFF_TMO);
  const __amdgpu_buffer_rsrc_t rs = __builtin_amdgcn_make_buffer_rsrc(ws, 0, (int)WS_USED, 0x00020000);
  constexpr bool vo = role & 1;
  v8u wa[8], wb[16];
  constexpr bool fuse = role >= 2;
  constexpr int RG = fuse ? 4 : 5, RB = fuse ? 32768 : 0;
  f32x4 fac[2][2];
  v2u nin = {0u, 0u}, hsv;
  unsigned arm = 0;
  v8u t13;
  PLDECL;
  const int krot = h & KROT;
  {
    const int lane = olane();
    const int l15 = lane & 15, g = lane >> 4, trq = l15 >> 2, trp = lane & 3;
    const char* bl = (const char*)((vo ? Wv : Wq) + h * DH);
    const char* br = (const char*)(vo ? Wo + (size_t)h * DH * D : Wk + h * DH);
    const int rot = role == 1 ? (h >> 4) * 4 : 0;
    int vl[2], vr[2];
#pragma unroll
    for (int q = 0; q < 2; ++q) {
      const int row = 4 * (2 * w + q) + g;
      const int cb = ((((l15 >> 1) ^ ((row & 3) | ((row >> 1) & 4))) << 5) | ((l15 & 1) << 4));
      const int i = row & 7;
      const int prow = (row & 32) + ((i & 4) << 2) + ((row >> 1) & 12) + (i & 3);
      vl[q] = row * 8192 + cb;
      vr[q] = vo ? (role == 1 ? row : prow) * 2048 + cb : vl[q];
    }
#define W_ISSUE(u)                                                                                   \
  {                                                                                                  \
    const char* pl = bl + (size_t)((u) ^ krot) * 64 * 8192;                                          \
    const char* pr = br + (role == 1 ? (size_t)(((u) >> 1) & 1) * 64 * 2048 + (2 * ((u) >> 2) + ((u) & 1)) * 256          \
                                     : (vo ? (size_t)((u) & 1) * 64 * 2048 + ((u) >> 1) * 256 : (size_t)((u) ^ krot) * 64 * 8192)); \
    _Pragma("unroll") for (int q = 0; q < 2; ++q) {                                                  \
      glds16<((WNT >> (role >> 1)) & 1) != 0>(pl, vl[q], RB + ((u) % RG) * 32768 + (2 * w + q) * 1024);                                \
      glds16<((WNT >> (role >> 1)) & 1) != 0>(pr, vr[q], RB + ((u) % RG) * 32768 + 16384 + (2 * w + q) * 1024);                        \
    }                                                                                                \
  }
    const int ta = role == 1 ? 8 + (w ^ rot) : 2 * w, tb = role == 1 ? 2 * (w & 3) : 2 * w + 1;
    const int sw = trq | ((g & 1) << 2);
    int pa = (ta >> 3) * 16384 + (8 * g + trq) * 256 + (((ta & 7) ^ sw) << 5) + trp * 8;
    int pb = (tb >> 3) * 16384 + (8 * g + trq) * 256 + (((tb & 7) ^ sw) << 5) + trp * 8;
    const int pw = L_W + w * 2048 + lane * 16;
#define W_FRAG(dst, p, u)                                                                            \
  {                                                                                                  \
    int p_ = p + RB + ((u) % RG) * 32768;                                                             \
    asm volatile("" : "+v"(p_));                                                                     \
    const v2u t0 = trd(L + p_), t1 = trd(L + p_ + 1024), t2 = trd(L + p_ + 8192), t3 = trd(L + p_ + 9216); \
    dst = v8u{t0[0], t0[1], t1[0], t1[1], t2[0], t2[1], t3[0], t3[1]};                               \
  }
#define W_PUT(c)                                           \
  {                                                        \
    int p_ = pw + ((c) - 8) * 16384;                       \
    asm volatile("" : "+v"(p_));                           \
    *(LAS v4u*)(L + p_) = lo4(tmp[(c) - 8]);               \
    *(LAS v4u*)(L + p_ + 1024) = hi4(tmp[(c) - 8]);        \
  }
    PL(0);
    const int fxv = (16 * (w & 1) + (lane >> 2)) * 2048 + (((lane ^ -g) & 3) << 4) + ((w >> 1) & 1) * 64;
    const int fxr = l15 * 64 + (((g ^ -trq) & 3) << 4);
#define X_ISSUE(j) glds16((const char*)x + par * 65536 + ((2 * (j) + (w >> 2)) ^ krot) * 128, fxv, (((j) + 1) & 3) * 8192 + w * 1024)
#define HWU 99
#define W_HELP(u)                                                                                                     \
  if (fuse && w >= 5) {                                                                                               \
    const int h0 = slow_head(Wq), rk = (h - h0 - 1) & 3;                                                              \
    const int q = (((role & 1) * 4 + par * 2 + ((h >> 2) & 1)) * 3 + rk) * 3 + w - 5, m = q / 24;                     \
    const u16* wm = m == 2 ? Wv : (m ? Wk : Wq);                                                                      \
    const int hs = (h & 24) + slow_head(wm) + 4 * ((q / 12) & 1);                                                     \
    if (rk < 3)                                                                                                       \
      touch4((const char*)wm + hs * 256 + (size_t)((15 - (q % 12 >> 1)) ^ (hs & KROT)) * 524288,                      \
             ((q & 1) * 32 + (lane >> 1)) * 8192 + (lane & 1) * 128, RB + ((u) % RG) * 32768 + 2 * w * 1024);          \
  }
    if (HWU == -1) W_HELP(0)
    W_ISSUE(0);
    if (fuse) {
#pragma unroll
      for (int j = 0; j < 4; ++j) X_ISSUE(j);
    }
#pragma unroll
    for (int u = 1; u < RG; ++u) W_ISSUE(u);
    PL(1);
    v8u tmp[8];
#pragma unroll
    for (int u = 0; u < 16; ++u) {
      {
        int young = 4 * (u + RG - 1 < 16 ? RG - 1 : 15 - u);
        if (fuse)
          for (int i = u - RG; i < u; ++i) young += i >= 0 && i < 8 && (i & 1);
        if (u > HSU && u < HSU + RG) young += 1;
        if (role < 2 && u > 13) young += 3;
        if (VFAR && role == 3 && u > 12 && u < 15) young += 2;
        asm volatile("s_waitcnt vmcnt(%0)" ::"n"(young) : "memory");
      }
      RAW_BARRIER();
      if ((u & 3) == 0 || u == 15) PL(2 + (u + 1) / 4);
      if constexpr (role == 1) {
#define WO_(n) ((n) < 8 ? wa[(n) & 7] : ((n) < 10 ? wb[14 + ((n) & 1)] : tmp[(n) >= 10 ? (n) - 10 : 0]))
#define WV_(n) ((n) < 14 ? wb[(n) < 14 ? (n) : 0] : tmp[6 + ((n) & 1)])
        if (u == 12) { W_PUT(8); W_PUT(9); }
        W_FRAG(WO_(u), pa, u)
        if (VAL ? (w >> 2) == (((u ^ krot) >> 2) & 1) : (w >> 2) == (u >> 3)) {
          const int sl = VAL ? (u & 3) + 4 * (u >> 3) : u & 7;
          W_FRAG(WV_(2 * sl), pb, u)
          W_FRAG(WV_(2 * sl + 1), (pb ^ 32), u)
        }
        if (u & 1) {
          v8u& f0 = WO_(u & ~1);
          v8u& f1 = WO_(u);
          const v8u n0 = {f0[0], f1[0], f0[1], f1[1], f0[2], f1[2], f0[3], f1[3]};
          const v8u n1 = {f0[4], f1[4], f0[5], f1[5], f0[6], f1[6], f0[7], f1[7]};
          f0 = n0;
          f1 = n1;
          asm volatile("" : "+v"(f0), "+v"(f1));
        }
        if (u == 13) { W_PUT(10); W_PUT(11); }
        if (u == 15) { W_PUT(12); W_PUT(13); W_PUT(14); W_PUT(15); }
      } else if constexpr (role == 0) {
        if (u == 12) { W_PUT(8); W_PUT(9); }
        if (u == 13) { W_PUT(10); W_PUT(11); }
        if (u == 14) { W_PUT(12); W_PUT(13); }
        if (u == 15) { W_PUT(14); }
        if (u < 8) W_FRAG(wa[u < 8 ? u : 0], pa, u) else W_FRAG(tmp[u >= 8 ? u - 8 : 0], pa, u)
        W_FRAG(wb[u], pb, u)
        if (u == 15) W_PUT(15);
      } else {
        if (u == 15 && role == 2 && w == 0) hs_wait(rs, nonce, hsv);
        if (u >= 13) W_PUT(u >= 13 ? 2 * u - 18 : 8);
        if (u == 13 || u == 14) W_PUT(u >= 13 ? 2 * u - 17 : 8);
        v8u fa, fb;
        W_FRAG(fa, pa, u)
        W_FRAG(fb, pb, u)
        if (u < 8) {
          wa[u & 7] = fa;
          wb[u] = fb;
        } else {
          wb[u] = cat8(lo4(fa), lo4(fb));
          tmp[u & 7] = cat8(hi4(fa), hi4(fb));
        }
        if (role == 2 || w < 4) {
#pragma unroll
          for (int hf = 0; hf < 2; ++hf)
#pragma unroll
            for (int rt = 0; rt < 2; ++rt) {
              const v4u xq = *(const LAS v4u*)(L + fxr + ((u + 2) & 7) * 4096 + hf * 2048 + rt * 1024);
              fac[0][rt] = mma(hf ? hi4(fa) : lo4(fa), xq, u + hf == 0 ? f32x4{0.f, 0.f, 0.f, 0.f} : fac[0][rt]);
              fac[1][rt] = mma(hf ? hi4(fb) : lo4(fb), xq, u + hf == 0 ? f32x4{0.f, 0.f, 0.f, 0.f} : fac[1][rt]);
            }
          asm volatile("" : "+v"(fac[0][0]), "+v"(fac[0][1]), "+v"(fac[1][0]), "+v"(fac[1][1]));
          if (u == 15) {
            PL(16);
            const int l16 = olane() * 16;
#pragma unroll
            for (int rt = 0; rt < 2; ++rt) {
              const v4u o = {pk2(fac[0][rt][0], fac[0][rt][1]), pk2(fac[0][rt][2], fac[0][rt][3]), pk2(fac[1][rt][0], fac[1][rt][1]),
                             pk2(fac[1][rt][2], fac[1][rt][3])};
              if (role == 3) *(LAS v4u*)(L + 24576 + rt * 4096 + w * 1024 + l16) = o;
              else st128(o, rs, l16, qk_addr(w >> 2, par, h) + rt * QK_RT + (w & 3) * 1024);
            }
            if (role == 2) {
              W_PUT(13);
              VMCNT(0);
            }
          }
        } else if (u == 15) {
          W_PUT(13);
          if (w == 7) {
            const bool ok = hs_wait(rs, nonce, hsv);
            if (VFAR) {
              if (__builtin_expect(!ok || !__all((nin[0] != 0xffffffffu) & (nin[1] != 0xffffffffu)), 0)) nin = numa_load(rs, tmo);
              *(volatile LAS int*)(L + L_W + 5 * 16384) = numa_far(nin);
            }
          }
        }
      }
      if (u == HSU) hsv = __builtin_amdgcn_raw_buffer_load_b64(rs, w == (role == 3 ? 7 : 0) ? 0 : OOB, OFF_ACK + (int)BIDX * 8, AUX_SC1);
      if (role < 2 && u == 13) {
        const int a = w == 0 ? olane() * 4 : OOB;
        arm = __builtin_amdgcn_raw_buffer_load_b32(rs, a & OOB, CNT_ARM, AUX_SC1);
        nin[0] = __builtin_amdgcn_raw_buffer_load_b32(rs, a, (int)OFF_CAL, AUX_SC1);
        nin[1] = __builtin_amdgcn_raw_buffer_load_b32(rs, (a & OOB) | (((a >> 5) & (NXC - 1)) * 4), (int)OFF_XID, AUX_SC1);
      }
      if (VFAR && role == 3 && u == 12) {
        const int a = w == 7 ? olane() * 4 : OOB;
        nin[0] = __builtin_amdgcn_raw_buffer_load_b32(rs, a, (int)OFF_CAL, AUX_SC1);
        nin[1] = __builtin_amdgcn_raw_buffer_load_b32(rs, (a & OOB) | (((a >> 5) & (NXC - 1)) * 4), (int)OFF_XID, AUX_SC1);
      }
      if (u + RG < 16) {
        RAW_BARRIER();
        if (HWU == u) W_HELP(u + RG)
        W_ISSUE(u + RG);
        if (fuse && u < 8 && (u & 1)) X_ISSUE(4 + (u >> 1));
      }
    }
    PL(17);
    RAW_BARRIER();
    PL(18);
    if (VFAR && role == 3 && w >= 4) arm = __builtin_amdgcn_readfirstlane(*(volatile LAS int*)(L + L_W + 5 * 16384));
    if (role == 2 && w == 0) __builtin_amdgcn_raw_buffer_store_b32(0u, rs, olane() << 28, fqk(h, par), AUX_SC1);
    if constexpr (fuse) {
      if (role == 2 || w < 4) {
        constexpr int B0 = role == 3 ? 6 : 8, PER = role == 3 ? 2 : 4, SL = role == 3 ? 12288 : 16384;
        const int ln = olane();
        const int xv = (16 * (w & 1) + (ln >> 2)) * 2048 + (((ln ^ -(ln >> 4)) & 3) << 4) + ((w >> 1) & 1) * 1024 + (role == 3 ? 0 : (w >> 2) * 64);
#pragma unroll
        for (int k = 0; k < 2; ++k)
#pragma unroll
          for (int j = 0; j < B0 / PER; ++j)
            glds16((const char*)x + (2 + par) * 65536 + (((k * B0 + j * PER) / 2 * 64) ^ (krot * 128)), xv, k * SL + j * PER * 2048 + w * 1024);
        PL(19);
        if (role == 3) t13 = tmp[5];
      }
      W_PUT(14);
      W_PUT(15);
    }
  }
  const int lane = olane();
  const int l15 = lane & 15, g = lane >> 4, trq = l15 >> 2, trp = lane & 3;
  int pw0 = L_W + w * 2048 + lane * 16, pw1 = pw0 + 65536;
  asm volatile("" : "+v"(pw0), "+v"(pw1));
#define WA_P(c) (L + ((c) < 12 ? pw0 : pw1) + ((c) & 3) * 16384)
#define WA_LDS(c) cat8(*(const LAS v4u*)WA_P(c), *(const LAS v4u*)(WA_P(c) + 1024))
  const int r_a = (int)(((uintptr_t)ws >> 19) & 7);
  const int r_s = r_a + (int)(OFF_STEP / UNIT);

  typedef int v4i __attribute__((ext_vector_type(4)));
#define WPIN "{v[0:31]}"(W0), "{v[32:63]}"(W1), "{v[64:95]}"(W2), "{v[96:127]}"(W3), "{v[128:159]}"(W4), "{v[160:191]}"(W5)
  const v4u rsv = {(unsigned)(uintptr_t)ws, (unsigned)((uintptr_t)ws >> 32) & 0xffffu, (unsigned)WS_USED, 0x00020000u};
  if constexpr (role >= 2) {
    constexpr bool isx = role == 3;
    const v32u W0 = cat32(wa[0], wa[1], wa[2], wa[3]), W1 = cat32(wa[4], wa[5], wa[6], wa[7]), W2 = cat32(wb[0], wb[1], wb[2], wb[3]);
    const v32u W3 = cat32(wb[4], wb[5], wb[6], wb[7]), W4 = cat32(wb[8], wb[9], wb[10], wb[11]), W5 = cat32(wb[12], wb[13], wb[14], wb[15]);
    const v4u rxv = {(unsigned)(uintptr_t)x, (unsigned)((uintptr_t)x >> 32) & 0xffffu, (unsigned)(S * D * 2), 0x00020000u};
    v4i adr;
    adr[3] = lane * 16;
    adr[2] = pw0;
    adr[1] = pw1;
    adr[0] = (16 * (w & 1) + (lane >> 2)) * 2048 + (((lane ^ -g) & 3) << 4) + ((w >> 1) & 1) * 1024 + (isx ? 0 : (w >> 2) * 64);
    const int xr = l15 * 64 + (((g ^ -trq) & 3) << 4);
#define FOF(sg) (isx ? fvw(w & 3, h, sg) : fqk(h, sg))
    const int nrel = isx ? TN(8, NRX) : TN(7, NRY);
    const int nit = par ? nrel : NSEG - nrel;
#define SEGOF(i) ((i) < nrel ? 2 * (i) + par : (i) + nrel)
#define ST_FLAG(sg)                                                                                                   \
  {                                                                                                                   \
    VMCNT(0);                                                                                                         \
    RAW_BARRIER();                                                                                                    \
    if (w == 0) __builtin_amdgcn_raw_buffer_store_b32(0u, rs, olane() << 28, FOF(sg), AUX_SC1);               \
  }
#define XB_OPS(sg, sp)                                                                                                  \
  : [cnt] "=&s"(cnt) EVOUT                                                                                              \
  : WPIN, "{v[248:251]}"(adr), [rs] "s"(rsv), [so] "s"(vw_addr(vfr, 2 * (w - 4), sg, h >> 2) + (h & 3) * 1024),               \
    [js] "i"((VFAR ? 64 : GST) * 262144), [ss] "i"(VFAR ? 65536 : 32768), [fo] "s"(FOF(sp))                                                 \
  : XB_CLOB, "scc", "memory"
#define XB_FLAG(sg) __builtin_amdgcn_raw_buffer_store_b8(0, rs, olane() << 28, FOF(sg), AUX_SC1)
    PL(7);
    if (isx && w >= 4) {
      const int vfr = (__builtin_amdgcn_readfirstlane(arm) ^ (VFAR == 2 ? 8192 : 0)) | (r_a << 19);
      {
        EVDECL;
        unsigned cnt;
        PL(20);
        asm volatile(ST_XBS XB_OPS(par, par));
        PL(21);
      }
      int it = 1;
#pragma unroll 1
      for (;;) {
        VMCNT(0);
        XB_FLAG(SEGOF(it - 1));
        if (it == 1) { PL(9); PLDUMP(4, 0x300200); }
        if (it == 2) PTW(14, 4);
        if (it == 3) PTW(15, 4);
        for (int k = it == 1 ? XBS_NB - 1 : XBF_NB; k < 6; ++k) BARE_BARRIER();
        if (++it > NXF) break;
        EVDECL;
        unsigned cnt;
        asm volatile(ST_XBF XB_OPS(SEGOF(it - 1), par));
        EVT2R
      }
#pragma unroll 1
      for (; it < nit; ++it) {
        EVDECL;
        unsigned cnt;
        asm volatile(ST_XB XB_OPS(SEGOF(it - 1), SEGOF(it - 2)));
        EVT2R
      }
      BARE_BARRIER();
      {
        EVDECL;
        unsigned cnt;
        asm volatile(ST_XB0 XB_OPS(SEGOF(nit - 1), par));
      }
      BARE_BARRIER();
      VMCNT(0);
      XB_FLAG(SEGOF(nit - 2));
      XB_FLAG(SEGOF(nit - 1));
      return;
    }
    if (w == 1) cnt_inc(ws, CNT_HS);
    VMCNT(0);
    RAW_BARRIER();
    if (isx) {
      *(LAS v4u*)(L + pw1 + 16384) = lo4(t13);
      *(LAS v4u*)(L + pw1 + 17408) = hi4(t13);
    }
    PL(10);
    PLDUMP(0, 0xf05ff);
    {
      v32u xf;
#pragma unroll
      for (int q = 0; q < 8; ++q) {
        const v4u t = *(const LAS v4u*)(L + xr + q * 1024);
#pragma unroll
        for (int j = 0; j < 4; ++j) xf[4 * q + j] = t[j];
      }
#pragma unroll 1
      for (int it = 1; it < nit; ++it) {
        EVDECL;
        const int seg = SEGOF(it);
        unsigned cnt, keep;
#define ST_OPS                                                                                                                 \
  : [cnt] "=&s"(cnt), [keep] "=&s"(keep), "+{v[208:239]}"(xf) EVOUT                                                            \
  : WPIN, "{v[248:251]}"(adr), "{v252}"(xr), [rs] "s"(rsv), [rx] "s"(rxv), [sc] "s"(seg * 65536 + krot * 128),                    \
    [sn] "s"(SEGOF(min(it + 1, nit - 1)) * 65536 + krot * 128),                                                                  \
    [mw] "s"(w * 1024), [so] "s"(isx ? w * 1024 : qk_addr(w >> 2, seg, h) + (w & 3) * 1024), [rt] "i"(QK_RT), \
    [fo] "s"(FOF(SEGOF(it - 1))), [fm] "s"(w == 0 ? 0 : 1 << 30), [tm] "s"(w == 3 ? 0 : 1 << 30),            \
    [tc] "s"(min(SEGOF(it + 2), NSEG - 1) * 65536 + sl * 8192)                                                                   \
  : ST_CLOB, "scc", "memory"
        if constexpr (isx) asm volatile(ST_XA ST_OPS);
        else asm volatile(ST_Y ST_OPS);
        EVT2R
        if (it < 4) PTW(10 + it, 2);
        if (!isx && it < NPUB) ST_FLAG(seg)
      }
    }
    if (isx) {
      RAW_BARRIER();
      BARE_BARRIER();
    } else ST_FLAG(SEGOF(nit - 1))
    return;
  }

#define TSK 700
#define KDM 1
#define TC2 0
#define KF0 4
  volatile LAS unsigned* sword = (volatile LAS unsigned*)(L + L_HI + 6408);
  if (w == 0) {
    const bool ok = hs_wait(rs, nonce, hsv);
    cnt_inc(ws, CNT_HS);
    if (__builtin_expect(!ok || !__all((arm + 1u >= 64u) & (nin[0] != 0xffffffffu) & (nin[1] != 0xffffffffu)), 0)) {
      arm_wait(rs, tmo);
      nin = numa_load(rs, tmo);
    }
    sword[0] = numa_perm(nin, (LAS unsigned*)(L + L_HI + 6656));
    sword[1] = 0u;
  }
  Clk ck = {0u, 0u, 92500u, 46432u};
  PL(7);
  RAW_BARRIER();
  PL(8);
  PLDUMP(0, 0xf01ff);
  const unsigned perm = __builtin_amdgcn_readfirstlane(sword[0]);
  const int cown = cls_of(perm, xg);
  const int l16 = lane * 16;
  const int sidx = ((l15 & 2) ? 0xeeee : 0x4444) | ((l15 & 8) ? 0xeeee0000 : 0x44440000);
  LAS char* const sm = L + l16;
  constexpr int TWV = 5;
  typedef int v8i __attribute__((ext_vector_type(8)));
  const int mg_ = NS2F(TN(0, MARG)), tol_ = NS2F(TN(13, TOL)), xl_ = NS2F(TN(14, XLATE));
  Pace pc = {0u, NS2F(ED0), NS2F(EA0), 0};
  unsigned tg = 0, tw = 0;
#define MEM_OFF(c) (R_MEM + ((c) >> 2) * 32768 + ((c) & 3) * 1024)
  if (w == 0) *(volatile LAS u64*)(L + L_HI + 7176 + l16) = 0ull;
#define DMDECL unsigned keep; int fs = s + 1
#define DMOUT , [keep] "=&s"(keep), [fs] "+s"(fs)
#define DMIN(x) , [o3] "s"(x)
#define TLB_TOUCH                    \
  if (((r_s + s + 2) & 3) == 0)      \
    tch = __builtin_amdgcn_raw_buffer_load_b32(rs, 0, so + 2 * (int)UNIT + R_DUM + (role * 32 + h) * 128, AUX_SC1);
#define TQOUT
#define TQACC
#define STCNT
#define ST_ADR (lst * 32 + 32768 + 2048)
#define TIMING_WAVE                                                                                  \
  {                                                                                                  \
    if (__builtin_expect(anysent(st[0]), 0)) _Pragma("clang loop unroll(disable)") for (unsigned spins = 0;;) { \
      if (spins) __builtin_amdgcn_s_sleep(4);                                                        \
      STCNT                                                                                          \
      if (spin_fail(spins, tmo, 0x30009)) break;                                                     \
      const int lst = olane();                                                                       \
      OPQ(va, ST_ADR - 2048);                                                                        \
      st = __builtin_amdgcn_raw_buffer_load_b64(rs, va, so + R_ST + ((cown ^ xr) << 12), AUX_SC1);   \
      if (!anysent(st[0])) break;                                                                       \
    }                                                                                                \
    TQACC                                                                                            \
    unsigned m0_ = (ck.r << 4) + 65536u - st[0], m1_ = ~st[1];                                       \
    wave_min2(m0_, m1_);                                                                             \
    const unsigned rf = (ck.r << 4) + 65536u - __builtin_amdgcn_readfirstlane(m0_);                  \
    pace_step<false>(pc, s, rf, ~__builtin_amdgcn_readfirstlane(m1_), mg_, tol_, xl_);               \
    EV(6);                                                                                           \
    EVDUMP(s);                                                                                       \
    EVCLK(s, ck);                                                                                    \
  }
#define TIMING_GO                                                                                    \
  if (w == TWV && s > 0) {                                                                           \
    tg = clk_cyc(ck, (int)(pc.rf - (ck.r << 4)) + pc.dq);                                            \
    const int lf = (int)(tg - cyc32()) & ~1;                                                         \
    if (lf > TSK || (int)(tg - ck.m) > 7000 || s < 4) {                    \
      wait_cyc(tg - TFR, lf - TFR);                                                                  \
      if (__builtin_expect(s < 3, 0)) clk_update(ck, s == 1);              \
      else clk_step(ck);                                                                             \
      tg = clk_cyc(ck, (int)(pc.rf - (ck.r << 4)) + pc.dq);                                          \
    }                                                                                                \
    tw = (unsigned)max(CMAX - (int)(tg - ck.m), 0);                                                  \
  }

  if constexpr (role == 0) {
    const v32u W0 = cat32(wa[0], wa[1], wa[2], wa[3]), W1 = cat32(wa[4], wa[5], wa[6], wa[7]), W2 = cat32(wb[0], wb[1], wb[2], wb[3]);
    const v32u W3 = cat32(wb[4], wb[5], wb[6], wb[7]), W4 = cat32(wb[8], wb[9], wb[10], wb[11]), W5 = cat32(wb[12], wb[13], wb[14], wb[15]);
    v8i adr;
    adr[7] = l16;
    adr[6] = l16 + w * 1024;
    adr[5] = pw0;
    adr[4] = sidx;
    adr[3] = L_HI + (w >> 2) * 3072 + (32 * (w & 3) + l15) * 24 + 4 * g;
    adr[2] = (4 * g + trq) * 24 + (trp & 1) * 8;
#define CORD(i) ((((i) & 1) ? (i) >> 1 : 8 + ((i) >> 1)) ^ krot)
    const int mo0 = MEM_OFF(CORD(w));
#define K_STAGE(sn)                                                                                                   \
  {                                                                                                                   \
    v4u kn[4];                                                                                                        \
    _Pragma("clang loop unroll(disable)") for (unsigned spins = 0; krdy <= (sn);) {    \
      OPQ(vfl, fqk(h, min((sn) + (olane() & 15), NSEG - 1)));                                           \
      const u64 miss = __builtin_amdgcn_ballot_w64(__builtin_amdgcn_raw_buffer_load_b32(rs, vfl, 0, AUX_SC1) != 0u);  \
      krdy = (sn) + (miss ? __builtin_ctzll(miss) : 16);                                                              \
      if (krdy > (sn)) break;                                                                                         \
      __builtin_amdgcn_s_sleep(8);                                                                                    \
      if (spin_fail(spins, tmo, 0x30000)) break;                                                                      \
    }                                                                                                                 \
    const int lk = olane() * 16;                                                                                      \
    if (KDM) {                                         \
      unsigned keep;                                                                                                  \
      asm volatile("s_mov_b32 %0, m0\n\ts_mov_b32 m0, %4\n\ts_nop 4\n\t"                                              \
                   "buffer_load_dwordx4 %1, %2, %3 offen sc1 lds\n\tbuffer_load_dwordx4 %1, %2, %3 offen offset:1024 sc1 lds\n\t" \
                   "buffer_load_dwordx4 %1, %2, %3 offen offset:2048 sc1 lds\n\tbuffer_load_dwordx4 %1, %2, %3 offen offset:3072 sc1 lds\n\t" \
                   "s_mov_b32 m0, %0"                                                                                 \
                   : "=&s"(keep)                                                                                      \
                   : "v"(lk), "s"(rsv), "s"(qk_addr(1, sn, h) + (w & 1) * QK_RT), "s"(L_HI + 8192 + (w & 1) * 4096)    \
                   : "memory");                                                                                       \
    } else {                                                                                                          \
      _Pragma("unroll") for (int q = 0; q < 4; ++q) kn[q] = __builtin_amdgcn_raw_buffer_load_b128(                    \
          rs, lk + q * 1024, qk_addr(1, sn, h) + (w & 1) * QK_RT, AUX_SC1);                                           \
      _Pragma("unroll") for (int q = 0; q < 4; ++q) *(LAS v4u*)(L + L_HI + 8192 + (w & 1) * 4096 + q * 1024 + lk) = kn[q]; \
    }                                                                                                                 \
  }
    int krdy = 1;
    const bool kst = w >= 4 && ((w ^ TWV) & 2);
    PTW(9, 7);
    if (kst) {
      wait_flag<KF0>(rs, fqk(h, 0), tmo, 0x30001);
      sword[1] = 1u;
      K_STAGE(0)
    }
    PTW(10, 7);
    if (w == TWV) {
      _Pragma("clang loop unroll(disable)") for (unsigned spins = 0; !__builtin_amdgcn_readfirstlane(sword[1]) && spins < kSpinCap; ++spins)
        __builtin_amdgcn_s_sleep(2);
      clk_edge(ck.r, ck.m, 2);
    }
    if (w < 2) {
      const int lane = olane(), l15 = lane & 15, l16 = lane * 16;
      const bool up = lane >= 32;
      adr[1] = R_EX + (h >> 3) * 32768 + (h & 7) * 512 + w * 256 + (lane & 32) * 4 + (lane & 7) * 16 + (lane & 24) / 2;
      const int rq = (lane & 1) + 2 * (lane & 6);
      adr[0] = up ? R_PM + (h >> 3) * 1024 + (rq + (l15 >> 3) * 2 + 16 * ((h & 7) >> 1)) * 16 + (h & 1) * 8 + (lane & 16) / 4
                  : (lane < 8 ? R_SC + SC_POS(h >> 2, lane) + (2 * (h & 1) + ((h >> 1) & 1)) * 4 : OOB);
      const unsigned msk = up ? 0x7fffffffu : ((lane & 1) ? 0x7fff0000u : 0xffffu);
      const int kxo = L_HI + 8192 + w * 4096 + l16;
#pragma unroll 1
      for (int s = 0; s < NSEG; ++s) {
        EVDECL;
        const int so = (int)OFF_STEP + s * (int)UNIT, xr = xreg(r_s + s);
#define CG(r) (so + ((cls_of(perm, (r) ^ (2 * w)) ^ xr) << 12))
        unsigned cnt;
        u64 wd;
        DMDECL;
        asm volatile(QK_A
                     : [cnt] "=&s"(cnt), [wd] "=&s"(wd) DMOUT EVOUT
                     : WPIN, "{v[244:251]}"(adr), "{v243}"(pw1), "{v242}"(kxo), "{v240}"(msk), [rs] "s"(rsv),
                       [o0] "s"(so + mo0 + ((cown ^ xr) << 12)), [c0] "s"(CG(0)), [c1] "s"(CG(1)), [c2] "s"(CG(2)), [c3] "s"(CG(3)),
                       [hf] "s"(0xff00ff00ff00ff00ull), [up] "s"(0xffffffff00000000ull)
                     : QK_CLOB, "v241", "vcc", "scc", "memory");
        if (s < 4) PT(11 + s);
        EVDUMP(s);
      }
    } else if (w < 4) {
      const float scale = 0.08838834764831845f, L2E = 1.4426950408889634f;
      adr[0] = adr[1] = 0;
#pragma unroll 1
      for (int s = 0; s < NSEG; ++s) {
        {
          EVDECL;
          const int so = (int)OFF_STEP + s * (int)UNIT, xr = xreg(r_s + s);
          unsigned cnt;
          v16u qv;
          f32x4 sc[3];
          DMDECL;
          asm volatile(QK_X
                       : [cnt] "=&s"(cnt) DMOUT, "={v[200:215]}"(qv), "={v[192:195]}"(sc[0]), "={v[196:199]}"(sc[1]) EVOUT
                       : WPIN, "{v[244:251]}"(adr), "{v243}"(pw1), [rs] "s"(rsv), [o0] "s"(so + mo0 + ((cown ^ xr) << 12)),
                         [qo] "s"(qk_addr(0, s, h) + (w - 2) * QK_RT)
                       : QX_CLOB, "vcc", "scc", "memory");
          const int ln = olane(), l15 = ln & 15, g = ln >> 4, l16 = ln * 16;
          v4u qx[4];
#pragma unroll
          for (int ks = 0; ks < 4; ++ks) qx[ks] = v4u{qv[4 * ks], qv[4 * ks + 1], qv[4 * ks + 2], qv[4 * ks + 3]};
          LAS char* const q_rd = L + L_HI + adr[2];
#define QFRAG(o, ks) [&] { const v2u tx = trd(q_rd + (o) + (ks) * 768), ty = trd(q_rd + (o) + (ks) * 768 + 384); return v4u{tx[0], tx[1], ty[0], ty[1]}; }()
          sc[2] = f32x4{0.f, 0.f, 0.f, 0.f};
#pragma unroll
          for (int ks = 0; ks < 4; ++ks) sc[2] = mma(QFRAG(3072, ks), qx[ks], sc[2]);
          EV(4);
          const int tok = 16 * (w - 2) + l15 - 4 * g;
#pragma unroll
          for (int t = 0; t < 3; ++t)
#pragma unroll
            for (int j = 0; j < 4; ++j) {
              float v = rbf(mul1(rbf(sc[t][j]), scale));
              if (t < 2 && 16 * t + j > tok) v = -1000000000.0f;
              sc[t][j] = v;
            }
          float mx;
          asm("v_max3_f32 %0, %1, %2, %3\n\tv_max3_f32 %0, %0, %4, %5\n\tv_max3_f32 %0, %0, %6, %7\n\tv_max3_f32 %0, %0, %8, %9\n\t"
              "v_max3_f32 %0, %0, %10, %11\n\tv_max_f32 %0, %0, %12\n\ts_nop 1"
              : "=&v"(mx)
              : "v"(sc[0][0]), "v"(sc[0][1]), "v"(sc[0][2]), "v"(sc[0][3]), "v"(sc[1][0]), "v"(sc[1][1]), "v"(sc[1][2]), "v"(sc[1][3]),
                "v"(sc[2][0]), "v"(sc[2][1]), "v"(sc[2][2]), "v"(sc[2][3]));
          mx = xmax16_32(mx);
          const float nmx = -mx * L2E;
#pragma unroll
          for (int t = 0; t < 3; ++t)
#pragma unroll
            for (int j = 0; j < 4; ++j) sc[t][j] = __builtin_amdgcn_exp2f(__builtin_fmaf(sc[t][j], L2E, nmx));
          const f32x4 s4 = sc[0] + sc[1] + 0.5f * sc[2];
          const float inv = __builtin_amdgcn_rcpf(xsum16_32((s4[0] + s4[1]) + (s4[2] + s4[3])));
#pragma unroll
          for (int t = 0; t < 3; ++t) sc[t] *= inv;
          const v4u o = {pk2(sc[0][0], sc[0][1]), pk2(sc[1][0], sc[1][1]), pk2(sc[0][2], sc[0][3]), pk2(sc[1][2], sc[1][3])};
          const v2u o2 = {pk2(sc[2][0], sc[2][1]), pk2(sc[2][2], sc[2][3])};
          const int pb = so + R_PX + (w - 2) * 40960;
          st128(o, rs, l16, pb + h * 1024);
          __builtin_amdgcn_raw_buffer_store_b64(o2, rs, g >= 2 ? l15 * 16 + (g & 1) * 8 : OOB,
                                                pb + (32 + (h >> 2)) * 1024 + (h & 3) * 256, AUX_SC1);
          EV(6);
          EVDUMP(s);
        }
      }
    } else {
      const int mo1 = MEM_OFF(CORD(w + 4)), mo2 = MEM_OFF(CORD(w + 8)), mo3 = MEM_OFF(CORD(w - 4));
      adr[0] = 0;
#pragma unroll 1
      for (int s = 0; s < NSEG; ++s) {
        EVDECL;
        const int so = (int)OFF_STEP + s * (int)UNIT, xr = xreg(r_s + s), sc_ = so + ((cown ^ xr) << 12);
        const int lst = olane();
        int a1 = ((cls_of(perm, lst & 3) ^ xr) << 12) | ((lst & 4) << 9) | ((lst & 8) << 12) | (((lst >> 4) | (w != TWV)) << 28);
        const bool fq = kst && krdy <= s + 2;
        int a0 = fq ? fqk(h, min(s + 1 + (lst & 15), NSEG - 1)) : ST_ADR | ((w != TWV) << 30);
        v2u st;
        unsigned cnt, tcd, tca = (lst & 31) * 128 + (lst >> 5) * QK_RT;
        int o0 = sc_ + mo0, o1 = sc_ + mo1, o2 = sc_ + mo2, sst = so + R_ST + role * 1024 + h * 32, ss = kst ? 0 : sc_ + R_ST;
        int tc = kst && !TC2 ? qk_addr(w & 1, min(s + 1, NSEG - 1), h) : 0;
        asm volatile("" : "+v"(a1), "+v"(a0), "+{v242}"(tca));
        adr[1] = a1;
        adr[0] = a0;
        asm volatile("" : "+s"(o0), "+s"(o1), "+s"(o2), "+s"(sst), "+s"(ss), "+s"(tc));
        DMDECL;
        TIMING_GO
        asm volatile(QK_K
                     : [cnt] "=&s"(cnt) DMOUT, "={v[240:241]}"(st), "={v237}"(tcd) EVOUT TQOUT
                     : WPIN, "{v[244:251]}"(adr), "{v243}"(pw1), "{v242}"(tca), [rs] "s"(rsv), [o0] "s"(o0), [o1] "s"(o1), [o2] "s"(o2),
                       [ckm] "s"(ck.m - NCY), [inv] "s"(ck.inv << 16), [r16] "s"(ck.r << 4), [sst] "s"(sst), [tg] "s"(tg), [tw] "s"(tw),
                       [ss] "s"(ss), [tc] "s"(tc) DMIN(sc_ + mo3)
                     : QKK_CLOB, "vcc", "scc", "memory");
        if (s < NSEG - 1 && w == TWV) {
          __builtin_amdgcn_s_sleep(KSL);
          TIMING_WAVE
        } else if (s < NSEG - 1 && kst) {
          if (fq) {
            const u64 miss = __builtin_amdgcn_ballot_w64(st[0] != 0u);
            krdy = s + 1 + (miss ? __builtin_ctzll(miss) : 16);
          }
          if (KSL2) __builtin_amdgcn_s_sleep(KSL2);
          if (KDM) asm volatile("s_waitcnt vmcnt(0)" ::: "memory");
          K_STAGE(s + 1)
          asm volatile("" ::"{v237}"(tcd));
          if (TC2 == 1) {
            const unsigned tch = __builtin_amdgcn_raw_buffer_load_b32(rs, tca, qk_addr(w & 1, min(s + 1 + (w & 1), NSEG - 1), h), AUX_SC1);
            asm volatile("" ::"v"(tch));
          }
        } else {
          __builtin_amdgcn_s_sleep(KSL);
          unsigned tch = 0;
          TLB_TOUCH
          asm volatile("" ::"v"(tch));
        }
        if (w != TWV) EV(6);
        if (w != TWV) EVDUMP(s);
      }
    }
  } else {
    const v32u W0 = cat32(wb[0], wb[1], wb[2], wb[3]), W1 = cat32(wb[4], wb[5], wb[6], wb[7]), W2 = cat32(wb[8], wb[9], wb[10], wb[11]);
    const v32u W3 = cat32(wb[12], wb[13], wa[0], wa[1]), W4 = cat32(wa[2], wa[3], wa[4], wa[5]), W5 = cat32(wa[6], wa[7], wb[14], wb[15]);
    v8i adr;
    adr[7] = l16;
    adr[6] = l16 + w * 1024;
    adr[5] = pw0;
    adr[4] = sidx;
#pragma unroll
    for (int q = 0; q < 2; ++q) {
      const int c = 8 * (2 * q + (g >> 1) + ((g & 1) ? 0 : 4)) + (w ^ ((h >> 4) * 4));
      const int blk = (c % PO) * 2 + (h >> 4);
      adr[3 - q] = (int)OFF_VM + blk * 32768 + ((cls_of(perm, c / PO) ^ xreg(r_a + (int)(OFF_VM / UNIT) + (blk >> 4))) << 12) +
                   (h & 15) * 256 + l15 * 16;
    }
    adr[1] = (16 * (g & 1) + 4 * (g >> 1) + trq) * 24 + (trp & 1) * 8;
    adr[0] = L_HI + (32 * (w & 3) + l15) * 24 + 4 * g;
#define VCH(i) (VAL ? ((((i) - 1) ^ krot) & 3) | (((i) & 8) >> 1) | ((((i) - 1) & 4) << 1) : ((i) & 8) | ((((i) - 1) & 7) ^ krot))
    const int mo0 = MEM_OFF(VCH(w)), mo1 = MEM_OFF(VCH(w + 4)), mo2 = MEM_OFF(VCH(w + 8)), mo3 = MEM_OFF(VCH(w & 3));
    if (w == TWV) clk_edge(ck.r, ck.m, 2);
    if (w < 4) {
#pragma unroll 1
      for (int s = 0; s < NSEG; ++s) {
        EVDECL;
        const int so = (int)OFF_STEP + s * (int)UNIT, xr = xreg(r_s + s);
        unsigned cnt;
        DMDECL;
        asm volatile(VO_A
                     : [cnt] "=&s"(cnt) DMOUT EVOUT
                     : WPIN, "{v[244:251]}"(adr), "{v243}"(pw1), [rs] "s"(rsv), [o0] "s"(so + mo0 + ((cown ^ xr) << 12)), [vs] "s"(vm_slot(s))
                     : VO_CLOB, "v241", "vcc", "scc", "memory");
        if (s < 4) PT(11 + s);
        if (w == 0) {
          unsigned tch = 0;
          TLB_TOUCH
          asm volatile("" ::"v"(tch));
        }
        EVDUMP(s);
      }
    } else {
#pragma unroll 1
      for (int s = 0; s < NSEG; ++s) {
        EVDECL;
        const int so = (int)OFF_STEP + s * (int)UNIT, xr = xreg(r_s + s);
        const int lst = olane();
        int vst = ((cls_of(perm, lst & 3) ^ xr) << 12) | ((lst & 4) << 9) | ((lst & 8) << 12) | (((lst >> 4) | (w != TWV)) << 28);
        unsigned cnt;
        v2u st = {(unsigned)(ST_ADR | ((w != TWV) << 30)), 0u};
        int o0 = so + mo0 + ((cown ^ xr) << 12), o1 = o0 - mo0 + mo1, o2 = o0 - mo0 + mo2, vs = vm_slot(s), sst = so + R_ST + role * 1024 + h * 32;
        int ss = so + R_ST + ((cown ^ xr) << 12);
        asm volatile("" : "+{v[240:241]}"(st), "+{v242}"(vst));
        asm volatile("" : "+s"(o0), "+s"(o1), "+s"(o2), "+s"(vs), "+s"(sst), "+s"(ss));
        DMDECL;
        TIMING_GO
        asm volatile(VO_B
                     : [cnt] "=&s"(cnt) DMOUT, "+{v[240:241]}"(st) EVOUT TQOUT
                     : WPIN, "{v[244:251]}"(adr), "{v243}"(pw1), "{v242}"(vst), [rs] "s"(rsv), [o0] "s"(o0), [o1] "s"(o1), [o2] "s"(o2),
                       [vs] "s"(vs), [ckm] "s"(ck.m - NCYV), [inv] "s"(ck.inv << 16), [r16] "s"(ck.r << 4), [sst] "s"(sst), [tg] "s"(tg),
                       [tw] "s"(tw), [ss] "s"(ss) DMIN(o0 - mo0 + mo3)
                     : VO_CLOB, "vcc", "scc", "memory");
        if (w == TWV && s < NSEG - 1) TIMING_WAVE
        if (w != TWV) EVDUMP(s);
      }
    }
  }
  __builtin_amdgcn_endpgm();
}
typedef f32x2 c2;
#define PKOP(name, txt)                                   \
  __device__ __forceinline__ c2 name(c2 a, c2 b) {        \
    c2 d;                                                 \
    asm("v_pk_add_f32 %0, %1, %2 " txt : "=v"(d) : "v"(a), "v"(b)); \
    return d;                                             \
  }
PKOP(c_addi, "op_sel:[0,1] op_sel_hi:[1,0] neg_lo:[0,1]")
PKOP(c_addc, "neg_hi:[0,1]")
PKOP(c_sepb, "op_sel:[1,1] op_sel_hi:[0,0] neg_hi:[1,0]")
PKOP(c_mixc, "op_sel:[0,1] op_sel_hi:[1,0] neg_hi:[1,0]")
#define T_ADD(d, a, b) "v_pk_add_f32 %" #d ", %" #a ", %" #b "\n\t"
#define T_SUB(d, a, b) "v_pk_add_f32 %" #d ", %" #a ", %" #b " neg_lo:[0,1] neg_hi:[0,1]\n\t"
#define T_ROT(d, a, b, n) "v_pk_add_f32 %" #d ", %" #a ", %" #b " op_sel:[0,1] op_sel_hi:[1,0] " n ":[0,1]\n\t"
#define T_MUL(d, a, w) "v_pk_mul_f32 %" #d ", %" #a ", %" #w " op_sel_hi:[1,0]\n\t"
#define T_FRE(d, a, w, c) "v_pk_fma_f32 %" #d ", %" #a ", %" #w ", %" #c " op_sel_hi:[1,0,1]\n\t"
#define T_FIM(d, a, w, c, n) "v_pk_fma_f32 %" #d ", %" #a ", %" #w ", %" #c " op_sel:[1,1,0] op_sel_hi:[0,1,1] " n ":[1,0,0]\n\t"
#define T_2MN(d, a, t) "v_pk_fma_f32 %" #d ", %" #a ", 2.0, %" #t " op_sel_hi:[1,0,1] neg_lo:[0,0,1] neg_hi:[0,0,1]\n\t"
#define T_TAIL(n, m) T_ADD(0, 4, 5) T_ROT(1, 2, 3, m) T_SUB(4, 4, 5) T_ROT(3, 2, 3, n)
#define T_D0(n, m) T_ADD(4, 0, 2) T_ADD(5, 1, 3) T_SUB(2, 0, 2) T_SUB(3, 1, 3) T_TAIL(n, m)
#define T_D1(n, m) T_MUL(5, 1, 7) T_ROT(4, 0, 2, m) T_FIM(1, 1, 7, 5, n) T_ROT(2, 0, 2, n) T_FRE(5, 3, 9, 1) T_FIM(5, 3, 9, 5, n) T_2MN(3, 1, 5) T_TAIL(n, m)
#define T_D2(n, m) T_MUL(5, 1, 7) T_FRE(4, 2, 8, 0) T_FIM(1, 1, 7, 5, n) T_FIM(4, 2, 8, 4, n) T_FRE(5, 3, 9, 1) T_2MN(2, 0, 4) T_FIM(5, 3, 9, 5, n) \
    T_ADD(0, 4, 5) T_2MN(3, 1, 5) T_SUB(4, 4, 5) T_ROT(1, 2, 3, m) T_ROT(3, 2, 3, n)
#define T_D3(n, m) T_MUL(4, 0, 6) T_MUL(5, 1, 7) T_FIM(0, 0, 6, 4, n) T_FIM(1, 1, 7, 5, n) T_FRE(4, 2, 8, 0) T_FRE(5, 3, 9, 1) \
    T_FIM(4, 2, 8, 4, n) T_FIM(5, 3, 9, 5, n) T_2MN(2, 0, 4) T_2MN(3, 1, 5) T_TAIL(n, m)
#define D4_ASM(T, C)                                                                                                       \
  {                                                                                                                        \
    if constexpr (INV) asm(T("neg_hi", "neg_lo") : "+v"(x0), "+v"(x1), "+v"(x2), "+v"(x3), "=&v"(t0), "=&v"(t1) : C(w0), C(w1), C(w2), C(w3)); \
    else asm(T("neg_lo", "neg_hi") : "+v"(x0), "+v"(x1), "+v"(x2), "+v"(x3), "=&v"(t0), "=&v"(t1) : C(w0), C(w1), C(w2), C(w3));               \
  }
__device__ __forceinline__ constexpr float cos32(int m) {
  constexpr float C[9] = {1.f, 0.98078528040323044f, 0.92387953251128674f, 0.83146961230254524f, 0.70710678118654752f,
                          0.55557023301960218f, 0.38268343236508977f, 0.19509032201612825f, 0.f};
  m &= 31;
  if (m > 16) m = 32 - m;
  return m > 8 ? -C[16 - m] : C[m];
}
#define W32(m) c2{cos32(m), -cos32((m) - 8)}
template <bool INV, int M0, int M1, int M2, int M3>
__device__ __forceinline__ void dft4k(c2& x0, c2& x1, c2& x2, c2& x3) {
  c2 t0, t1;
  if constexpr (M3 == 0) {
    const int w0 = 0, w1 = 0, w2 = 0, w3 = 0;
    D4_ASM(T_D0, "n")
  } else {
    const c2 w1 = W32(M1), w3 = W32(M3), w0 = M0 ? W32(M0) : w1, w2 = M0 == 0 && M2 == 8 ? w1 : W32(M2);
    if constexpr (M0 != 0) D4_ASM(T_D3, "s")
    else if constexpr (M2 == 8) D4_ASM(T_D1, "s")
    else D4_ASM(T_D2, "s")
  }
  x2 = t0;
}
template <bool INV, bool W0>
__device__ __forceinline__ void dft4w(c2& x0, c2& x1, c2& x2, c2& x3, c2 w0, c2 w1, c2 w2, c2 w3) {
  c2 t0, t1;
  if constexpr (W0) D4_ASM(T_D3, "v")
  else D4_ASM(T_D2, "v")
  x2 = t0;
}
#define T_B2(n, m) T_FRE(4, 1, 6, 0) T_FRE(5, 3, 7, 2) T_FIM(4, 1, 6, 4, n) T_FIM(5, 3, 7, 5, n) T_2MN(1, 0, 4) T_2MN(3, 2, 5)
#define T_B0(n, m) T_ADD(4, 0, 1) T_ROT(5, 2, 3, m) T_SUB(1, 0, 1) T_ROT(3, 2, 3, n)
template <bool INV, int K>
__device__ __forceinline__ void bf2(c2& x0, c2& x1, c2& x2, c2& x3, c2 w0, c2 w1) {
  c2 t0, t1;
  const c2 w2 = w0, w3 = w0;
  if constexpr (K == 0) D4_ASM(T_B2, "v")
  else if constexpr (K == 1) D4_ASM(T_B2, "s")
  else D4_ASM(T_B0, "s")
  x0 = t0;
  x2 = t1;
}
__device__ __forceinline__ void mul2(c2& d0, c2& d1, c2 a0, c2 w0, c2 a1, c2 w1) {
  asm(T_MUL(0, 2, 3) T_MUL(1, 4, 5) T_FIM(0, 2, 3, 0, "neg_lo") T_FIM(1, 4, 5, 1, "neg_lo") : "=&v"(d0), "=&v"(d1) : "v"(a0), "v"(w0), "v"(a1), "v"(w1));
}
template <int N, bool INV, int TW>
__device__ __forceinline__ void dftn(c2 (&v)[N], const c2* w) {
  constexpr int Q = N / 4;
#pragma unroll
  for (int n = 0; n < Q; ++n) {
    if (TW == 0) dft4k<INV, 0, 0, 0, 0>(v[n], v[Q + n], v[2 * Q + n], v[3 * Q + n]);
    else if (TW == 1 && n == 0) dft4w<INV, false>(v[n], v[Q + n], v[2 * Q + n], v[3 * Q + n], w[Q], w[Q + n], w[2 * Q + n], w[3 * Q + n]);
    else dft4w<INV, true>(v[n], v[Q + n], v[2 * Q + n], v[3 * Q + n], w[n], w[Q + n], w[2 * Q + n], w[3 * Q + n]);
  }
  c2 o[N];
  if constexpr (N == 32) {
#define D8A(K1)                                                                                          \
  dft4k<INV, 0, 2 * K1, 4 * K1, 6 * K1>(v[8 * K1], v[8 * K1 + 2], v[8 * K1 + 4], v[8 * K1 + 6]);          \
  dft4k<INV, K1, 3 * K1, 5 * K1, 7 * K1>(v[8 * K1 + 1], v[8 * K1 + 3], v[8 * K1 + 5], v[8 * K1 + 7]);
    D8A(0) D8A(1) D8A(2) D8A(3)
#pragma unroll
    for (int k = 0; k < 32; k += 8) {
      bf2<INV, 2>(v[k], v[k + 1], v[k + 4], v[k + 5], W32(4), W32(12));
      bf2<INV, 1>(v[k + 2], v[k + 3], v[k + 6], v[k + 7], W32(4), W32(12));
    }
#pragma unroll
    for (int i = 0; i < 32; ++i) o[(i >> 3) + 2 * (i & 6) + 16 * (i & 1)] = v[i];
  } else {
    dft4k<INV, 0, 0, 0, 0>(v[0], v[1], v[2], v[3]);
    dft4k<INV, 0, 2, 4, 6>(v[4], v[5], v[6], v[7]);
    dft4k<INV, 0, 4, 8, 12>(v[8], v[9], v[10], v[11]);
    dft4k<INV, 0, 6, 12, 18>(v[12], v[13], v[14], v[15]);
#pragma unroll
    for (int i = 0; i < 16; ++i) o[(i >> 2) + 4 * (i & 3)] = v[i];
  }
#pragma unroll
  for (int i = 0; i < N; ++i) v[i] = o[i];
}
template <bool H32>
__device__ __forceinline__ void c_swap(c2& a, c2& b) {
#pragma unroll
  for (int j = 0; j < 2; ++j) {
    const unsigned x = __float_as_uint(a[j]), y = __float_as_uint(b[j]);
    const auto r = H32 ? __builtin_amdgcn_permlane32_swap(x, y, false, false) : __builtin_amdgcn_permlane16_swap(x, y, false, false);
    a[j] = __uint_as_float(r[0]);
    b[j] = __uint_as_float(r[1]);
  }
}
#define LANE_NOP()                  \
  do {                              \
    SB();                           \
    asm volatile("s_nop 1");        \
    SB();                           \
  } while (0)
__device__ __forceinline__ c2 lds_c2(int a) { return *(volatile LAS c2*)((LAS char*)g_lds + a); }
template <int N>
__device__ __forceinline__ void lds_put(const c2 (&v)[N], int a) {
#pragma unroll
  for (int i = 0; i < N / 2; ++i) *(volatile LAS f32x4*)((LAS char*)g_lds + a + 16 * i) = f32x4{v[2 * i][0], v[2 * i][1], v[2 * i + 1][0], v[2 * i + 1][1]};
}
constexpr int XS = 32768, WS_ = 16384, WT_OFF = 18 << 20;
__device__ __forceinline__ int side_adr(int hi, int lo, int sd) { return hi + ((lo >> 13) << 14) + (lo & 8191) + (sd ^ ((hi >> 8) & 8192)); }
constexpr int LDS_TW = 0;
constexpr int LDS_EB = 4096;
constexpr int LDS_STG = 4096 + 8 * 9216;
__device__ __forceinline__ void tw2048(c2 (&w)[32], int lane) {
#pragma unroll
  for (int k = 1; k <= 8; ++k) w[k] = lds_c2(LDS_TW + lane * k * 8);
  const c2 h = lds_c2(LDS_TW + (lane & 31) * 128);
  mul2(w[0], w[16], h, h, w[8], w[8]);
#pragma unroll
  for (int k = 9; k < 15; k += 2) mul2(w[k], w[k + 1], w[8], w[k - 8], w[8], w[k - 7]);
  mul2(w[15], w[24], w[8], w[7], w[16], w[8]);
#pragma unroll
  for (int k = 1; k < 8; ++k) mul2(w[16 + k], w[24 + k], w[16], w[k], w[24], w[k]);
}
template <bool INV>
__device__ __forceinline__ void fft2048(c2 (&v)[32], const c2 (&w)[32], int eb, int lane) {
  dftn<32, INV, 0>(v, w);
  lds_put<32>(v, eb + lane * 272);
  const int ra = eb + (lane >> 5) * 4352 + (lane & 31) * 8;
#pragma unroll
  for (int k = 0; k < 16; ++k) {
    v[k] = lds_c2(ra + k * 272);
    v[k + 16] = lds_c2(ra + k * 272 + 8704);
  }
#pragma unroll
  for (int k = 0; k < 16; k += 2) bf2<INV, 0>(v[k], v[k + 16], v[k + 1], v[k + 17], w[0], w[0]);
  LANE_NOP();
#pragma unroll
  for (int k = 0; k < 16; ++k) c_swap<true>(v[k], v[k + 16]);
  dftn<32, INV, 1>(v, w);
}
template <bool SC>
__device__ __forceinline__ void tw1024(c2 (&w)[16], c2 (&u)[4], int lane, float s) {
#pragma unroll
  for (int k = 1; k <= 4; ++k) w[k] = lds_c2(LDS_TW + lane * k * 16);
  u[1] = lds_c2(LDS_TW + (lane & 15) * 256);
  u[0] = u[1];
  mul2(u[2], w[8], u[1], u[1], w[4], w[4]);
  mul2(u[3], w[12], u[2], u[1], w[8], w[4]);
  w[0] = c2{s, 0.f};
  if (SC) {
#pragma unroll
    for (int k = 1; k < 4; ++k) w[k] *= s;
  }
  c2 z;
  mul2(w[5], w[6], w[4], w[1], w[4], w[2]);
  mul2(w[7], z, w[4], w[3], w[4], w[3]);
#pragma unroll
  for (int k = 1; k < 4; ++k) mul2(w[8 + k], w[12 + k], w[8], w[k], w[12], w[k]);
  if (SC) {
#pragma unroll
    for (int k = 4; k < 16; k += 4) w[k] *= s;
  }
}
template <bool INV>
__device__ __forceinline__ void fft1024(c2 (&v)[16], const c2 (&w)[16], const c2 (&u)[4], int eb, int lane) {
  dftn<16, INV, 0>(v, w);
  lds_put<16>(v, eb + lane * 144);
  const int ra = eb + ((lane >> 1) & 8) * 144 + (lane >> 5) * 576 + (lane & 15) * 8;
  c2 t[4][4];
#pragma unroll
  for (int a = 0; a < 4; ++a)
#pragma unroll
    for (int r = 0; r < 4; ++r) t[a][r] = lds_c2(ra + (a + 16 * r) * 144);
#pragma unroll
  for (int a = 0; a < 4; ++a) {
    dft4w<INV, false>(t[a][0], t[a][1], t[a][2], t[a][3], u[0], u[1], u[2], u[3]);
#pragma unroll
    for (int q = 0; q < 4; ++q) v[a + 8 * (q & 1) + 4 * (q >> 1)] = t[a][q];
  }
  LANE_NOP();
#pragma unroll
  for (int i = 0; i < 8; ++i) c_swap<false>(v[i], v[i + 8]);
#pragma unroll
  for (int i = 0; i < 8; ++i) c_swap<true>(v[(i & 3) + 2 * (i & 4)], v[(i & 3) + 2 * (i & 4) + 4]);
  dftn<16, INV, INV ? 2 : 1>(v, w);
}

__device__ __forceinline__ void fft_table(int w) {
  const int m = w * 64 + olane();
  float sn, cs;
  sincospif(-(float)m * (1.f / 1024.f), &sn, &cs);
  *(LAS c2*)((LAS char*)g_lds + LDS_TW + m * 8) = c2{cs, sn};
  RAW_BARRIER();
}
__device__ __forceinline__ void count_done(char* ws, int cnt_off, int w) {
  VMCNT(0);
  RAW_BARRIER();
  if (w == 0) cnt_inc(ws, cnt_off);
}
__device__ __forceinline__ void stg_adr(int lane, int w, int& up, int& mid, int& dn) {
  up = LDS_STG + lane * 128 + ((w ^ (lane >> 1)) & 7) * 16;
  dn = LDS_STG + (64 - lane) * 128 + ((w ^ ((64 - lane) >> 1)) & 7) * 16 + 8;
  mid = dn + 57344 - (lane == 0 ? 8 : 0);
}

#define P1A 2
#define P1B 0
#define P1X 0
#define FT(i)
#define FTV(i, x)
__device__ __forceinline__ void fft_pass1(char* ws, char* x1, int sd, const u16* __restrict__ x, const u16* __restrict__ wr,
                                          const u16* __restrict__ wi, int task, int w) {
  LAS char* const L = (LAS char*)g_lds;
  const __amdgpu_buffer_rsrc_t rx = __builtin_amdgcn_make_buffer_rsrc(x1, 0, 513 * XS, 0x00020000);
  const __amdgpu_buffer_rsrc_t rt = __builtin_amdgcn_make_buffer_rsrc(x1 + WT_OFF, 0, 513 * WS_, 0x00020000);
  const __amdgpu_buffer_rsrc_t ri = __builtin_amdgcn_make_buffer_rsrc((void*)x, 0, S * D * 2, 0x00020000);
  const __amdgpu_buffer_rsrc_t rr = __builtin_amdgcn_make_buffer_rsrc((void*)wr, 0, S * DF * 2, 0x00020000);
  const __amdgpu_buffer_rsrc_t rm = __builtin_amdgcn_make_buffer_rsrc((void*)wi, 0, S * DF * 2, 0x00020000);
  const int lane = olane();
  const int p = (P1X & 4) ? w : task * 8 + w, r0 = (p & 63) + 128 * (p >> 6);
  unsigned xa[16], xb[16], fr[2][9], fi[2][9];
  FT(0);
#pragma unroll
  for (int t = 0; t < 16; ++t) {
    xa[t] = __builtin_amdgcn_raw_buffer_load_b16(ri, lane * 2 + t * 128, r0 * 2048, P1A);
    xb[t] = __builtin_amdgcn_raw_buffer_load_b16(ri, lane * 2 + t * 128, r0 * 2048 + 131072, P1A);
  }
  const int l8 = lane == 0 ? 1024 : OOB;
#pragma unroll
  for (int r = 0; r < 2; ++r)
#pragma unroll
    for (int t = 0; t < 9; ++t) {
      fr[r][t] = __builtin_amdgcn_raw_buffer_load_b16(rr, t < 8 ? lane * 2 + t * 128 : l8, (r0 + 64 * r) * (DF * 2), P1B);
      fi[r][t] = __builtin_amdgcn_raw_buffer_load_b16(rm, t < 8 ? lane * 2 + t * 128 : l8, (r0 + 64 * r) * (DF * 2), P1B);
    }
  FT(1);
  c2 tw[16], tu[4], v[16];
  tw1024<false>(tw, tu, lane, 1.f);
  FTV(2, xb[15]);
#pragma unroll
  for (int t = 0; t < 16; ++t) v[t] = c2{__uint_as_float(xa[t] << 16), __uint_as_float(xb[t] << 16)};
  fft1024<false>(v, tw, tu, LDS_EB + w * 9216, lane);
  int up, mid, dn;
  stg_adr(lane, w, up, mid, dn);
#pragma unroll
  for (int j = 0; j < 16; ++j) *(volatile LAS c2*)(L + (j < 8 ? up + 8192 * j : (j == 8 ? mid : dn + 8192 * (15 - j)))) = v[j];
  FT(3);
  RAW_BARRIER();
  FTV(4, fi[1][8]);
  {
    const int fa = LDS_EB + lane * 64 + ((w ^ lane) & 6) * 8 + (w & 1) * 8;
#pragma unroll
    for (int t = 0; t < 9; ++t)
      if (t < 8 || lane == 0) *(LAS v2u*)(L + fa + t * 4096) = v2u{fr[0][t] | (fi[0][t] << 16), fr[1][t] | (fi[1][t] << 16)};
  }
  const int kq = w * 8 + (lane >> 3);
  {
    const int ra = LDS_STG + kq * 128 + ((lane ^ (kq >> 1)) & 7) * 16, ga = kq * XS + (lane & 7) * 16;
    v4u d[9];
#pragma unroll
    for (int i = 0; i < 9; ++i) d[i] = *(const LAS v4u*)(L + ra + i * 8192);
#pragma unroll
    for (int i = 0; i < 9; ++i)
      if ((i < 8 || w == 0) && !(P1X & 1))
        st128(d[i], rx, ga + (i == 8 ? 512 * XS : 0), side_adr((i & 7) * (64 * XS), task * 128, sd));
  }
  FT(5);
  RAW_BARRIER();
  {
    const int k4 = w * 16 + (lane >> 2);
    const int ra = LDS_EB + k4 * 64 + (((2 * lane) ^ k4) & 6) * 8, ga = k4 * WS_ + (lane & 3) * 16;
    v4u d[5];
#pragma unroll
    for (int i = 0; i < 5; ++i) d[i] = *(const LAS v4u*)(L + ra + i * 8192);
#pragma unroll
    for (int i = 0; i < 5; ++i)
      if ((i < 4 || w == 0) && !(P1X & 2))
        st128(d[i], rt, ga + (i == 4 ? 512 * WS_ : 0), side_adr((i & 3) * (128 * WS_), task * 64, sd ^ 8192));
  }
  FT(6);
  FT(7);
}
__device__ __forceinline__ void fft_pass2(char* ws, char* x1, int sd, int task, int w) {
  const __amdgpu_buffer_rsrc_t rx = __builtin_amdgcn_make_buffer_rsrc(x1, 0, 513 * XS, 0x00020000);
  const __amdgpu_buffer_rsrc_t rt = __builtin_amdgcn_make_buffer_rsrc(x1 + WT_OFF, 0, 513 * WS_, 0x00020000);
  const int lane = olane();
  const int k = task * 8 + w;
  if (k > 512) return;
  f32x4 d[16];
  FT(0);
#pragma unroll
  for (int t = 0; t < 16; ++t)
    d[t] = __builtin_bit_cast(f32x4, __builtin_amdgcn_raw_buffer_load_b128(rx, lane * 16 + (t & 3) * 1024, side_adr(k * XS, (t >> 2) * 4096, sd), AUX_SC1));
  c2 tw[32], v[32];
  tw2048(tw, lane);
  FT(1);
  const bool edge = (k & 511) == 0;
  FTV(2, d[15]);
  if (edge) {
#pragma unroll
    for (int t = 0; t < 16; ++t) {
      d[t][2] = d[t][0];
      d[t][3] = d[t][1];
    }
  }
#pragma unroll
  for (int t = 0; t < 16; ++t) {
    v[2 * t] = c_addc(c2{d[t][0], d[t][1]}, c2{d[t][2], d[t][3]});
    v[2 * t + 1] = c_sepb(c2{d[t][0], d[t][1]}, c2{d[t][2], d[t][3]});
  }
  SB();
  v2u f[16];
#pragma unroll
  for (int t = 0; t < 16; ++t) f[t] = __builtin_amdgcn_raw_buffer_load_b64(rt, lane * 8 + (t & 7) * 512, side_adr(k * WS_, (t >> 3) * 4096, sd ^ 8192), AUX_SC1);
  SB();
  const int eb = LDS_EB + w * 17408;
  fft2048<false>(v, tw, eb, lane);
  FTV(3, v[31]);
  FTV(4, f[15]);
#pragma unroll
  for (int t = 0; t < 16; ++t)
    mul2(v[2 * t], v[2 * t + 1], v[2 * t], c2{__uint_as_float(f[t][0] << 16), __uint_as_float(f[t][0] & 0xffff0000u)}, v[2 * t + 1],
         c2{__uint_as_float(f[t][1] << 16), __uint_as_float(f[t][1] & 0xffff0000u)});
  fft2048<true>(v, tw, eb, lane);
  FTV(5, v[31]);
  if (edge) {
#pragma unroll
    for (int t = 0; t < 32; ++t) v[t][1] = 0.f;
  }
#pragma unroll
  for (int t = 0; t < 16; ++t) {
    const c2 a = c_addi(v[2 * t], v[2 * t + 1]), b = c_mixc(v[2 * t], v[2 * t + 1]);
    st128(__builtin_bit_cast(v4u, f32x4{a[0], a[1], b[0], b[1]}), rx, lane * 16 + (t & 3) * 1024, side_adr(k * XS, (t >> 2) * 4096, sd));
  }
  FT(6);
  FT(7);
}
__device__ __forceinline__ void fft_pass3(char* ws, char* x1, int sd, u16* __restrict__ out, int task, int w) {
  LAS char* const L = (LAS char*)g_lds;
  const __amdgpu_buffer_rsrc_t rx = __builtin_amdgcn_make_buffer_rsrc(x1, 0, 513 * XS, 0x00020000);
  const __amdgpu_buffer_rsrc_t ro = __builtin_amdgcn_make_buffer_rsrc(out, 0, S * 4096, 0x00020000);
  const int lane = olane();
  const int kq = w * 8 + (lane >> 3);
  const int ra = LDS_STG + kq * 128 + ((lane ^ (kq >> 1)) & 7) * 16, ga = kq * XS + (lane & 7) * 16;
  v4u d[9];
  FT(0);
#pragma unroll
  for (int i = 0; i < 9; ++i) d[i] = __builtin_amdgcn_raw_buffer_load_b128(rx, ga + (i == 8 ? 512 * XS : 0), side_adr((i & 7) * (64 * XS), task * 128, sd), AUX_SC1);
  c2 tw[16], tu[4], v[16];
  tw1024<true>(tw, tu, lane, 1.f / (2.f * 2048.f * 1024.f));
  FT(1);
  FTV(2, d[8]);
#pragma unroll
  for (int i = 0; i < 9; ++i)
    if (i < 8 || w == 0) *(LAS v4u*)(L + ra + i * 8192) = d[i];
  int up, mid, dn;
  stg_adr(lane, w, up, mid, dn);
  FT(3);
  RAW_BARRIER();
  FT(4);
#pragma unroll
  for (int j = 0; j < 16; ++j) v[j] = lds_c2(j < 8 ? up + 8192 * j : (j == 8 ? mid : dn + 8192 * (15 - j)));
  fft1024<true>(v, tw, tu, LDS_EB + w * 9216, lane);
  FTV(5, v[15]);
  const int p = task * 8 + w, r0 = (p & 63) + 128 * (p >> 6);
#pragma unroll
  for (int t = 0; t < 16; ++t) {
    const unsigned o = pk2(v[t][0], v[t][1]);
    __builtin_amdgcn_raw_buffer_store_b16((u16)o, ro, lane * 2 + t * 128, r0 * 4096, 0);
    __builtin_amdgcn_raw_buffer_store_b16((u16)(o >> 16), ro, lane * 2 + t * 128, r0 * 4096 + 262144, 0);
  }
  FT(6);
  FT(7);
}

#define W7P 3
#define VTC 0
#define NXA 0
#define ARA 0
#define STA 0
#define SLR 2
#define RSO 5
#define NCAL 6
#define DOX2 300
#define DTC 100
#define NX1 4
#define NXG 2
#define NXK 5
#define OFD 400
#define STI 650
#define TNX 0
#define NXP 0
#define PACE 150
#define PFB 4
#define ROT 0
#define SPC 0
#define CV1 -88
#define CV2 108
#define EDL 0
#define EVB(k) EV(k)
constexpr int L_D = 16384, L_R = 32768, L_F = 122880;
__device__ __forceinline__ volatile LAS unsigned* flw(int i, int m) {
  return (volatile LAS unsigned*)((LAS char*)g_lds + L_F + i * 4 + (olane() & m) * 4);
}
template <int SLP = 0>
__device__ __forceinline__ void lds_wait_ge(int i, int m, unsigned v, gu32* tmo, unsigned code) {
  volatile LAS unsigned* p = flw(i, m);
  _Pragma("clang loop unroll(disable)") for (unsigned spins = 0;;) {
    if (__all(*p >= v)) break;
    if (SLP) __builtin_amdgcn_s_sleep(SLP);
    if (++spins > (kSpinCap << 5)) {
      tmo_report(tmo, code);
      break;
    }
  }
  asm volatile("" ::: "memory");
}
#define HLP 6
#define HLT 0
__device__ __forceinline__ void help(int c, int w, const u16* Wq, const u16* Wk, const u16* Wv) {
  const int j = c & 7, m = j >> 1, xg = c / PO, ln = olane();
  if ((c & 8) && w >= 1 && w <= HLP) {
    const u16* wm = m == 2 ? Wv : ((m == 3 ? j : m) & 1 ? Wk : Wq);
    const int hs = m == 3 ? 31 : PQ * xg + slow_head(wm) + 4 * (j & 1);
    if (m < 3 || xg == NXC - 1)
      for (int q = 0; q < 2; ++q)
        touch4((const char*)wm + hs * 256 + (size_t)((16 - w) ^ (hs & KROT)) * 524288, (q * 32 + (ln >> 1)) * 8192 + (ln & 1) * 128, 65536 + w * 256);
  }
}
template <bool PRE>
__device__ __forceinline__ unsigned owner(const int c, const int w, const u64 nonce, const u16* __restrict__ mem0, char* ws,
                                          u16* __restrict__ out, unsigned perm, const u16* Wq, const u16* Wk, const u16* Wv) {
  LAS char* const L = (LAS char*)g_lds;
  gu32* tmo = (gu32*)(ws + OFF_TMO);
  const int lane = olane();
  const int l15 = lane & 15, g = lane >> 4;
  const __amdgpu_buffer_rsrc_t rs = __builtin_amdgcn_make_buffer_rsrc(ws, 0, (int)WS_USED, 0x00020000);
  const int xg = c / PO, li = c % PO;
  const int r_a = (int)(((uintptr_t)ws >> 19) & 7);
  const int r_s = r_a + (int)(OFF_STEP / UNIT);
  volatile LAS unsigned* fl = (volatile LAS unsigned*)(L + L_F);
  unsigned m0v = 0;
  if constexpr (PRE) {
  if (w == 7) m0v = *(const unsigned*)(mem0 + (lane >> 3) * D + 16 * c + 2 * (lane & 7));
  if (c == 0 && w == 6) hs_lead<4>(rs, true, CNT_HSO, 64u);
  unsigned cal = 0;
  if (w == 0)
    st128(v4u{(unsigned)lane, 0u, 0u, 0u}, rs, (lane & 31) * 16 + (lane >> 5) * 32768, (int)OFF_SCR + (((li & 7) ^ xreg(r_a)) << 12) + (c >> 3) * 512);
  if (w == 0 && lane == 0) {
    const int cl = li & 7;
    int off = (int)OFF_SCR + ((cl ^ xreg(r_a)) << 12) + (c >> 3) * 512;
    unsigned best = 0xfffeu;
#pragma unroll 1
    for (int r = 0; r < NCAL; ++r) {
      VMCNT(0);
      const u64 t1 = __builtin_amdgcn_s_memtime();
      asm volatile("s_waitcnt lgkmcnt(0)" : "+v"(off)::"memory");
      const unsigned v = __builtin_amdgcn_raw_buffer_load_b32(rs, off, (r & 3) * 128 + (r >> 2) * 32768, AUX_SC1);
      asm volatile("s_waitcnt vmcnt(0)" ::"v"(v) : "memory");
      const u64 t2 = __builtin_amdgcn_s_memtime();
      best = min(best, (unsigned)(t2 - t1));
    }
    if (HLT == 0 && li >= 8) best = 0xfffeu;
    cal = ((unsigned)cl << 16) | best;
  }
  if (w == 0) {
    PT(0);
    hs_wait(rs, nonce, v2u{~(unsigned)nonce, 0u});
    cnt_inc(ws, CNT_HSO);
    PT(1);
    unsigned id;
    asm volatile("s_getreg_b32 %0, hwreg(HW_REG_XCC_ID)" : "=s"(id));
    if (lane == 0) {
      __builtin_amdgcn_raw_buffer_store_b32(((id & 15u) << 19) | cal, rs, c * 4, (int)OFF_CAL, AUX_SC1);
      if (li == 0) __builtin_amdgcn_raw_buffer_store_b32(id & 15u, rs, xg * 4, (int)OFF_XID, AUX_SC1);
    }
  }
  static_assert(NF1O == 64 || NF1O == 2, "one task per owner but the first two, or none");
  __syncthreads();
  if (HLT == 1) help(c, w, Wq, Wk, Wv);
  if (w == 7) {
    const unsigned pm = numa_perm(numa_load(rs, tmo), (LAS unsigned*)(L + L_F + 512));
    fl[10] = pm;
  }
  __syncthreads();
  perm = __builtin_amdgcn_readfirstlane(fl[10]);
  PT(2);
  if (HLT == 2) help(c, w, Wq, Wk, Wv);
  }
  const int xr0 = xreg(r_s + (PRE ? 0 : NARM + (li & 1)));
  const v4u sent = {0xffffffffu, 0xffffffffu, 0xffffffffu, 0xffffffffu};
  const int ibs = (int)OFF_VM + li * 65536 + ((cls_of(perm, xg) ^ xreg(r_a + (int)(OFF_VM / UNIT) + (li >> 3))) << 12);

  int am = (int)OFF_STEP + R_MEM + (c >> 4) * 32768 + ((cls_of(perm, g) ^ xr0) << 12) + (c & 15) * 256 + l15 * 16;
  int ap[2];
#pragma unroll
  for (int q = 0; q < 2; ++q) {
    const int o = c * 384 + (lane & 31) * 16;
    ap[q] = (lane & 31) < 24 && c < 59 ? (int)OFF_STEP + R_EX + (o >> 12) * 32768 + ((cls_of(perm, 2 * q + (lane >> 5)) ^ xr0) << 12) + (o & 4095)
                             : OOB;
  }
  int as = lane < 8 * NXC ? (int)OFF_STEP + R_ST + ((cls_of(perm, lane >> 3) ^ xr0) << 12) + (c >> 5) * 32768 + (c & 31) * 128 + (lane & 7) * 16
                          : OOB;
  const int ax = (int)OFF_STEP + R_PX + c * 1280 + lane * 16;
  const int ax2 = lane < 16 ? ax + 1024 : (lane < 24 ? (int)OFF_STEP + R_DUM + c * 128 + (lane - 16) * 16 : OOB);
#define ARM_REC(r)                                                                                                   \
  {                                                                                                                  \
    const int so_ = (r) * (int)UNIT, dx_ = (xreg(r_s + (r)) ^ xreg(r_s + (r) + 1)) << 12;                            \
    __builtin_amdgcn_raw_buffer_store_b128(sent, rs, am, so_, AUX_SC1);                                              \
    am ^= dx_;                                                                                                       \
    _Pragma("unroll") for (int q = 0; q < 2; ++q) {                                                                  \
      __builtin_amdgcn_raw_buffer_store_b128(sent, rs, ap[q], so_, AUX_SC1);                                         \
      ap[q] ^= dx_;                                                                                                  \
    }                                                                                                                \
    __builtin_amdgcn_raw_buffer_store_b128(sent, rs, as, so_, AUX_SC1);                                              \
    as ^= dx_;                                                                                                       \
    __builtin_amdgcn_raw_buffer_store_b128(sent, rs, ax, so_, AUX_SC1);                                              \
    __builtin_amdgcn_raw_buffer_store_b128(sent, rs, ax2, so_, AUX_SC1);                                             \
  }
  if (PRE && w == 7) {
    {
      const int row = lane >> 3, np = lane & 7;
#pragma unroll
      for (int r = 0; r < NXC; ++r)
        __builtin_amdgcn_raw_buffer_store_b32(min(m0v, 0xfffffffeu), rs,
                                              (int)OFF_STEP + R_MEM + (c >> 4) * 32768 + ((cls_of(perm, r) ^ xreg(r_s)) << 12) +
                                                  (c & 15) * 256 + ((row & 1) + 2 * (np & 1) + 4 * (row >> 1)) * 16 + (np >> 1) * 4,
                                              0, AUX_SC1);
    }
    am |= 1 << 30;
#pragma unroll 1
    for (int r = 0; r < NARM + (li & 1); ++r) {
      ARM_REC(r)
      am &= ~(1 << 30);
    }
#pragma unroll
    for (int k = 0; k < 32; ++k)
      __builtin_amdgcn_raw_buffer_store_b128(sent, rs, lane * 16 + (k & 3) * 1024, ibs + vm_slot(k >> 3) + ((k >> 2) & 1) * 32768, AUX_SC1);
    VMCNT(0);
    PTW(3, 7);
    cnt_inc(ws, CNT_ARM);
  }
  if constexpr (PRE) return perm;
  PT(5);
  __syncthreads();
  if (w == 7 && lane < 32) fl[lane] = 0;
  __syncthreads();

  const int team = li & 1, c0 = c & ~1;
  const int l16 = lane * 16;
  const int cown = cls_of(perm, xg);
  if (w < 2) {
    const int r = (l15 & 1) + 2 * (l15 >> 2), kh = (l15 >> 1) & 1;
    const int nl = (kh ^ ((r >> 1) & 1)) + 2 * ((g & 1) ^ (r >> 2));
    typedef int v8i __attribute__((ext_vector_type(8)));
    v8i adr;
    adr[0] = l16;
    adr[1] = 32768 + SC_POS(2 * nl + (g >> 1), r);
    adr[2] = l16;
    adr[3] = L_D + w * 8192 + l16;
    adr[4] = ((l15 & 3) + 4 * g) * 16 + (l15 >> 2) * 4;
    adr[5] = (l15 & 1) ? 0x03020706 : 0x05040100;
    adr[6] = (l15 & 2) ? 0x4444eeee : 0xeeee4444;
    adr[7] = L_F + 48;
    u64 mk[4];
#pragma unroll
    for (int k = 0; k < 4; ++k) mk[k] = __builtin_amdgcn_ballot_w64(nl == k);
    const v4u rsv = {(unsigned)(uintptr_t)ws, (unsigned)((uintptr_t)ws >> 32) & 0xffffu, (unsigned)WS_USED, 0x00020000u};
    const int dtc_ = TN(12, DTC);
    const int eg = (c0 >> 2) & 1;
#pragma unroll 1
    for (int s = team; s < NSEG; s += 2) {
      EVDECL;
      const int so = (int)OFF_STEP + s * (int)UNIT;
      int sp = so + R_PM + ((cown ^ xreg(r_s + s)) << 12);
      int sv = ibs + (w - team) * 65536 + vm_slot(s);
      int sv1 = sv + eg * 32768, sv2 = sv + (1 - eg) * 32768, sp1 = sp + eg * 2048, sp2 = sp + (1 - eg) * 2048;
      const int xn = xreg(r_s + s + 1);
      int sm[NXC];
#pragma unroll
      for (int q = 0; q < NXC; ++q) {
        sm[q] = so + (int)UNIT + R_MEM + (c0 >> 4) * 32768 + ((cls_of(perm, q) ^ xn) << 12) + ((c0 + w) & 15) * 256;
        asm volatile("" : "+s"(sm[q]));
      }
      asm volatile("" : "+s"(sp), "+s"(sv1), "+s"(sv2), "+s"(sp1), "+s"(sp2));
      if (s >= 2) lds_wait_ge<4>(20, 3, s - 1, tmo, 0x30017);
      if (VTC && w == 0) {
        const unsigned tc = __builtin_amdgcn_raw_buffer_load_b32(rs, 0, sv ^ 8192, AUX_SC1);
        asm volatile("" ::"v"(tc));
      }
      v4u bl[8];
      if (s < NSEG - 1) {
        v32u blv;
        unsigned cnt, bad;
        asm volatile(OW_F
                     : [cnt] "=&s"(cnt), [st] "=&s"(bad), "={v[0:31]}"(blv) EVOUT
                     : "{v[112:119]}"(adr), [rs] "s"(rsv), [sv1] "s"(sv1), [sv2] "s"(sv2), [sp] "s"(sp), [sp1] "s"(sp1), [sp2] "s"(sp2),
                       [sm0] "s"(sm[0]), [sm1] "s"(sm[1]), [sm2] "s"(sm[2]), [sm3] "s"(sm[3]), [m0] "s"(mk[0]), [m1] "s"(mk[1]),
                       [m2] "s"(mk[2]), [m3] "s"(mk[3])
                     : OF_CLOB, "vcc", "scc", "memory");
#pragma unroll
        for (int k = 0; k < 8; ++k) bl[k] = v4u{blv[4 * k], blv[4 * k + 1], blv[4 * k + 2], blv[4 * k + 3]};
        if (__builtin_expect(bad != 0u, 0)) {
          v4u xs, pv[4];
          _Pragma("clang loop unroll(disable)") for (unsigned spins = 0;;) {
            OPQ(va, l16);
            OPQ(v1, adr[1]);
            OPQ(v2, adr[2]);
#pragma unroll
            for (int k = 0; k < 8; ++k) bl[k] = __builtin_amdgcn_raw_buffer_load_b128(rs, va + (k & 3) * 1024, k < 4 ? sv1 : sv2, AUX_SC1);
            xs = __builtin_amdgcn_raw_buffer_load_b128(rs, v1, sp, AUX_SC1);
#pragma unroll
            for (int k = 0; k < 4; ++k) pv[k] = __builtin_amdgcn_raw_buffer_load_b128(rs, v2 + (k & 1) * 1024, k < 2 ? sp1 : sp2, AUX_SC1);
            unsigned m = mx4(0u, xs);
#pragma unroll
            for (int k = 0; k < 4; ++k) m = mx4(m, pv[k]);
#pragma unroll
            for (int k = 0; k < 8; ++k) m = mx4(m, bl[k]);
            if (!anysent(m)) break;
            __builtin_amdgcn_s_sleep(RSO);
            if (spin_fail(spins, tmo, 0x20000 + s)) break;
          }
          f32x4 a0 = {0.f, 0.f, 0.f, 0.f};
#pragma unroll
          for (int i = 0; i < 8; ++i) {
            const int k = F_ORD[i] >= 'a' ? F_ORD[i] - 'a' : F_ORD[i] - '0';
            if (F_ORD[i] >= 'a') {
              const v4u z = v4u{nl == k ? xs[0] : 0u, nl == k ? xs[1] : 0u, nl == k ? xs[2] : 0u, nl == k ? xs[3] : 0u};
              const v8u db = cat8(*(const LAS v4u*)(L + adr[3] + k * 2048), *(const LAS v4u*)(L + adr[3] + k * 2048 + 1024));
              a0 = (k & 1) ? smf1(z, db, a0, adr[6]) : smf(z, db, a0, adr[6]);
            } else
              a0 = smf(pv[k], cat8(bl[2 * k], bl[2 * k + 1]), a0, adr[6]);
          }
          const unsigned own = pk2(a0[0] + a0[2], a0[1] + a0[3]);
          const unsigned nb = (unsigned)__builtin_amdgcn_mov_dpp((int)own, 0xb1, 0xf, 0xf, true);
          const unsigned o = __builtin_amdgcn_perm(nb, own, (unsigned)adr[5]);
#pragma unroll
          for (int q = 0; q < NXC; ++q) __builtin_amdgcn_raw_buffer_store_b32(o, rs, adr[4], sm[q], AUX_SC1);
          EV(3);
        }
      } else {
        BARE_BARRIER();
        wait_cyc(*flw(12, 0));
        _Pragma("clang loop unroll(disable)") for (unsigned spins = 0;;) {
          OPQ(va, l16);
          unsigned m = 0;
#pragma unroll
          for (int k = 0; k < 8; ++k) bl[k] = __builtin_amdgcn_raw_buffer_load_b128(rs, va + (k & 3) * 1024, k < 4 ? sv1 : sv2, AUX_SC1);
#pragma unroll
          for (int k = 0; k < 8; ++k) m = mx4(m, bl[k]);
          if (!anysent(m)) break;
          __builtin_amdgcn_s_sleep(RSO);
          if (spin_fail(spins, tmo, 0x20000 + s)) break;
        }
      }
      const unsigned tp = cyc32();
      if (s < 4) PT(9 + (s >> 1));
      *flw(2 + w, 0) = s + 1;
      if (s < NSEG - 1) {
        const v4u tv = *(volatile LAS v4u*)flw(12, 0);
        const int lr = olane();
        __builtin_amdgcn_raw_buffer_store_b32(min(((tp - tv[1]) * tv[2]) >> 16, 60000u), rs,
                                              ((cls_of(perm, lr & 3) ^ xn) << 12) | ((lr & 4) << 9) | ((lr & 8) << 12) | ((lr >> 4) << 28),
                                              so + (int)UNIT + R_ST + (c0 + w) * 32 + 4, AUX_SC1);
      }
      OPQ(vs, l16);
#pragma unroll
      for (int k = 0; k < 8; ++k) *(LAS v4u*)(L + w * 8192 + ((k ^ (4 * eg)) * 1024) + vs) = bl[k];
      asm volatile("s_waitcnt lgkmcnt(0)" ::: "memory");
      *flw(4 + w, 0) = s + 1;
      if (s < NSEG - 4) {
        lds_wait_ge(2, 1, s + 1, tmo, 0x3001a);
        v4u se;
        asm volatile("v_mov_b32 %0, -1" : "=v"(se[0]));
        se[1] = se[2] = se[3] = se[0];
#pragma unroll
        for (int k = 0; k < 8; ++k) __builtin_amdgcn_raw_buffer_store_b128(se, rs, vs + (k & 3) * 1024, sv + (k >> 2) * 32768, AUX_SC1 | ARA);
        asm volatile("s_nop 1" ::"v"(se));
      }
      if (s < NSEG - 2) {
        wait_cyc(tp + dtc_);
        const int lt = olane();
        const unsigned tc = __builtin_amdgcn_raw_buffer_load_b32(rs, lt < 40 ? lt * 128 : OOB, so + R_PX + ((li & ~1) + w) * 5120, AUX_SC1);
        asm volatile("" ::"v"(tc));
      }
      EVDUMP(s);
    }
  } else if (w < 6) {
    const int n = w - 2;
    const v4u rsv = {(unsigned)(uintptr_t)ws, (unsigned)((uintptr_t)ws >> 32) & 0xffffu, (unsigned)WS_USED, 0x00020000u};
    const int vfr = (int)((perm & 8192u) ^ (VFAR == 2 ? 8192u : 0u)) | (r_a << 19);
    const int fv0 = fvw((c0 >> 1) & 3, 8 * n, team);
    const int dox_ = TN(10, DOX2), pace_ = TN(11, PACE);
    v4u raw[2][8];
#define RAW_LD(dst, sg)                                                                                       \
  _Pragma("unroll") for (int t = 0; t < 2; ++t) _Pragma("unroll") for (int k = 0; k < 8; ++k) dst[t][k] =     \
      __builtin_amdgcn_raw_buffer_load_b128(rs, l16 + (k & 3) * 1024, vw_addr(vfr, c0 + t, sg, 2 * n + (k >> 2)), AUX_SC1);
    wait_flag<KF0>(rs, fv0 + (lane & 7), tmo, 0x30018);
    PTW(6, 2);
    RAW_LD(raw, team)
    int nrdy = team;
#pragma unroll 1
    for (int s = team; s < NSEG; s += 2) {
      EVDECL;
      const int so = (int)OFF_STEP + s * (int)UNIT;
      if (s < NSEG - 1) {
        const int ln = olane();
        OPQ(eidx, (ln & 4) ? 0xeeee : 0x4444);
        OPQ(vea, (ln & 4) * 128 + (ln >> 4) * 128 + ((ln & 3) + 4 * (((ln >> 3) & 1) ^ (n >> 1))) * 16);
        int se = so + R_EX + n * 32768 + ((cown ^ xreg(r_s + s)) << 12);
        v8u bx[2][4];
#pragma unroll
        for (int t = 0; t < 2; ++t)
#pragma unroll
          for (int k = 0; k < 4; ++k) {
            const v4u u = raw[t][2 * k], v = raw[t][2 * k + 1];
            bx[t][k] = v8u{u[0], v[0], u[2], v[2], u[1], v[1], u[3], v[3]};
            asm volatile("" : "+v"(bx[t][k]));
          }
        OPQ(vd, L_D + n * 2048 + ln * 16);
        asm volatile("" : "+s"(se));
        EV(3);
        if (s < 4) PTW(7 + (s >> 1), 2);
        unsigned bad;
        asm volatile(OW_E
                     : [st] "=&s"(bad) EVOUT
                     : "{v241}"(vea), "{v242}"(vd), "{v243}"(eidx), [rs] "s"(rsv), [se] "s"(se), [b00] "v"(bx[0][0]), [b01] "v"(bx[0][1]),
                       [b02] "v"(bx[0][2]), [b03] "v"(bx[0][3]), [b10] "v"(bx[1][0]), [b11] "v"(bx[1][1]), [b12] "v"(bx[1][2]),
                       [b13] "v"(bx[1][3])
                     : OE_CLOB, "vcc", "scc", "memory");
        if (__builtin_expect(bad != 0u, 0)) {
          v4u pv[4];
          _Pragma("clang loop unroll(disable)") for (unsigned spins = 0;;) {
            __builtin_amdgcn_s_sleep(RSO);
            asm volatile("" : "+v"(vea));
            unsigned m = 0;
#pragma unroll
            for (int k = 0; k < 4; ++k) pv[k] = __builtin_amdgcn_raw_buffer_load_b128(rs, vea + k * 1024, se, AUX_SC1);
#pragma unroll
            for (int k = 0; k < 4; ++k) m = mx4(m, pv[k]);
            if (!anysent(m) || spin_fail(spins, tmo, 0x10000 + s)) break;
          }
#pragma unroll
          for (int q = 0; q < 4; ++q) {
            const f32x4 z = {0.f, 0.f, 0.f, 0.f};
            const f32x4 d = smf(pv[2 * (q & 1)], bx[q >> 1][2 * (q & 1)], z, eidx), e = smf(pv[2 * (q & 1) + 1], bx[q >> 1][2 * (q & 1) + 1], z, eidx);
            *(LAS v4u*)(L + vd + (q >> 1) * 8192 + (q & 1) * 1024) = v4u{pk2(d[0], d[1]), pk2(d[2], d[3]), pk2(e[0], e[1]), pk2(e[2], e[3])};
          }
          EV(2);
          RAW_BARRIER();
        }
      } else BARE_BARRIER();
      v4u nx[2][8];
      const int sn = min(s + 2, NSEG - 1);
      unsigned tpb = 0;
#define VW_FLAGS                                                                                                      \
  {                                                                                                                   \
    const int lf = olane();                                                                                           \
    OPQ(vfl, fv0 + min((s >> 1) + 1 + (lf >> 3), NSEG / 2 - 1) * 8 + (lf & 7));                                        \
    const u64 miss = __builtin_amdgcn_ballot_w64(__builtin_amdgcn_raw_buffer_load_b8(rs, vfl, 0, AUX_SC1) != 0);      \
    nrdy = s + 2 * (miss ? __builtin_ctzll(miss) >> 3 : 8);                                                           \
  }
      if (s < NSEG - 2) {
        __builtin_amdgcn_s_sleep(5);
        lds_wait_ge(2, 1, s + 1, tmo, 0x30014);
        tpb = cyc32();
        const unsigned tw = tpb + dox_;
        if (nrdy < s + 2) VW_FLAGS
        wait_cyc(tw);
      }
      EVB(6);
      OPQ(va, s < NSEG - 2 && nrdy >= s + 2 ? l16 : OOB);
#define NX_LD(dst, q0, q1)                                                                              \
  _Pragma("unroll") for (int q = q0; q < q1; ++q) dst[q >> 3][q & 7] = __builtin_amdgcn_raw_buffer_load_b128( \
      rs, va + (q & 3) * 1024, vw_addr(vfr, c0 + (q >> 3), sn, 2 * n + ((q >> 2) & 1)), AUX_SC1 | NXA);
      NX_LD(nx, 0, NX1)
      SB();
      if (s < NSEG - 2) wait_cyc(cyc32() + ((n + (li >> 1)) & 3) * pace_);
      else lds_wait_ge(4, 1, s + 1, tmo, 0x30014);
      EVB(4);
      f32x4 ac[2][2];
      const int sx = so + R_PX + n * 8192, sy = so + R_PX + 32768 + n * 2048;
      v4u pv[2][10], vm[2][2];
      OPQ(vb, l16);
#define P_LD                                                                                         \
  _Pragma("unroll") for (int k = 0; k < 10; ++k) _Pragma("unroll") for (int rt = 0; rt < 2; ++rt) pv[rt][k] = \
      __builtin_amdgcn_raw_buffer_load_b128(rs, vb + (k & 3) * 1024, (k < 8 ? sx + (k >> 2) * 4096 : sy) + rt * 40960, AUX_SC1);
#define P_MM(k0, k1)                                                                                 \
  _Pragma("unroll") for (int k = k0; k < k1; ++k) _Pragma("unroll") for (int rt = 0; rt < 2; ++rt) {  \
    _Pragma("unroll") for (int t = 0; t < 2; ++t)                                                    \
      ac[rt][t] = mma(k < 8 ? raw[t][k & 7] : vm[t][k & 1], pv[rt][k], k == 0 ? f32x4{0.f, 0.f, 0.f, 0.f} : ac[rt][t]); \
    asm volatile("" : "+v"(ac[rt][0]), "+v"(ac[rt][1]));                                             \
  }
      P_LD
      SB();
      lds_wait_ge(4, 1, s + 1, tmo, 0x30014);
#pragma unroll
      for (int t = 0; t < 2; ++t)
#pragma unroll
        for (int k = 0; k < 2; ++k) vm[t][k] = *(const LAS v4u*)(L + vb + t * 8192 + n * 2048 + k * 1024);
      SB();
      P_MM(0, 10)
      if (__builtin_expect(__builtin_amdgcn_ballot_w64(__builtin_isunordered(ac[0][0][0], ac[1][0][0])) != 0ull, 0)) {
        _Pragma("clang loop unroll(disable)") for (unsigned spins = 0;;) {
          asm volatile("" : "+v"(vb));
          P_LD
          unsigned m = 0;
#pragma unroll
          for (int k = 0; k < 10; ++k) m = mx4(mx4(m, pv[0][k]), pv[1][k]);
          if (!anysent(m)) break;
          __builtin_amdgcn_s_sleep(8);
          if (spin_fail(spins, tmo, 0x30015)) break;
        }
        P_MM(0, 10)
      }
      EVB(7);
      if (__builtin_expect(s < NSEG - 2 && nrdy < s + 2, 0)) {
        _Pragma("clang loop unroll(disable)") for (unsigned spins = 0;;) {
          VW_FLAGS
          if (nrdy >= s + 2) break;
          __builtin_amdgcn_s_sleep(8);
          if (spin_fail(spins, tmo, 0x30018)) break;
        }
        va = vb;
        NX_LD(nx, 0, NX1)
      }
      NX_LD(raw, NX1, 16)
      SB();
      OPQ(vr, L_R + (s & 2) * 8192 + l16);
#pragma unroll
      for (int q = 0; q < 4; ++q) *(LAS f32x4*)(L + vr + q * 4096 + n * 1024) = ac[q >> 1][q & 1];
      asm volatile("s_waitcnt lgkmcnt(0)" ::: "memory");
      fl[20 + n] = s + 1;
      lds_wait_ge(20, 3, s + 1, tmo, 0x30016);
      {
        const LAS f32x4* pr = (const LAS f32x4*)(L + vr + n * 4096);
        const f32x4 o = (pr[0] + pr[64]) + (pr[128] + pr[192]);
        const int lo = olane();
        *(v2u*)(out + (size_t)(s * 32 + 16 * (n >> 1) + (lo & 15)) * 2048 + 1024 + 16 * (c0 + (n & 1)) + 4 * (lo >> 4)) =
            v2u{pk2(o[0], o[1]), pk2(o[2], o[3])};
      }
      EVB(5);
#pragma unroll
      for (int q = 0; q < NX1; ++q) raw[q >> 3][q & 7] = nx[q >> 3][q & 7];
      EVDUMP(s);
    }
  } else if (w == 6) {
    if (c == 1) hs_lead<64>(rs, false, CNT_HS, 192u);
  } else {
    int narm = NARM + team;
    if (W7P) __builtin_amdgcn_s_setprio(W7P);
    const int sdk_ = NS2F(TN(2, SDK)), cv1_ = NS2F(TN(3, CV1)), cv2_ = NS2F(TN(4, CV2));
    arm_wait(rs, tmo);
    PTW(4, 7);
    Clk ck;
    clk_init(ck);
    unsigned t1 = cyc32();
    const int mg_ = NS2F(TN(0, MARG)), tol_ = NS2F(TN(13, TOL)), xl_ = NS2F(TN(14, XLATE));
    Pace pc = {0u, NS2F(ED0), NS2F(EA0), 0};
    const int sti_ = TN(15, STI), ofd_ = NS2F(TN(9, OFD));
#pragma unroll 1
    for (int s = 0; s < NSEG; ++s) {
      EVDECL;
      const bool on = (s & 1) == team;
      if (!on || s < 3) clk_update(ck, s < 3);
      int k1 = (cv1_ * (int)ck.cq) >> 16, k2 = (cv2_ * (int)ck.cq) >> 16;
      int sst = (int)OFF_STEP + s * (int)UNIT + R_ST + ((cown ^ xreg(r_s + s)) << 12);
      asm volatile("" : "+s"(k1), "+s"(k2), "+s"(sst));
      OPQ(va, lane * 32);
      OPQ(vt, L_F + 48);
      wait_cyc(t1);
      EV(0);
      v2u st = __builtin_amdgcn_raw_buffer_load_b64(rs, va, sst, AUX_SC1 | STA);
      clk_fresh(ck);
      asm volatile("" : "+s"(ck.r), "+s"(ck.m));
      v2u st2;
      if (SPC) {
        asm volatile("" : "+v"(va));
        st2 = __builtin_amdgcn_raw_buffer_load_b64(rs, va, sst + 2048, AUX_SC1 | STA);
      }
      asm volatile("" : "+v"(st));
      if (__builtin_expect(anysent(st[0]), 0)) {
        if (SPC) st = st2;
        if (!SPC || anysent(st[0])) _Pragma("clang loop unroll(disable)") for (unsigned spins = 1 + SPC;; ++spins) {
          asm volatile("" : "+v"(va));
          if (spins <= 2 || !on) wait_cyc(t1 + spins * sti_);
          else if (spins > 64) __builtin_amdgcn_s_sleep(4);
          else if (SLR) __builtin_amdgcn_s_sleep(SLR);
          st = __builtin_amdgcn_raw_buffer_load_b64(rs, va, sst + (ROT ? ((spins & 1) ? 2048 : ((spins & 2) ? 32768 : 0)) : (spins == 1 ? 2048 : (spins == 2 ? 32768 : 0))), AUX_SC1 | STA);
          if (!anysent(st[0])) break;
          if (spins > kSpinCap) {
            tmo_report(tmo, 0x3000b);
            break;
          }
        }
        if (s == 0) clk_fresh(ck);
      }
      if (s < 4) PTW(11 + s, 7);
      const int dm = stamp_max(ck, st[0]);
      const unsigned tr = clk_cyc(ck, dm);
      if (on) {
        *(volatile LAS v4u*)(L + vt) = v4u{tr + k2, tr, ck.inv, 0u};
        EV(1);
        wait_cyc(tr + k1);
        RAW_BARRIER();
        if (s < NSEG - 1) BARE_BARRIER();
        if (narm < NSEG) {
          __builtin_amdgcn_s_sleep(5);
          lds_wait_ge(2, 1, s + 1, tmo, 0x3001b);
#pragma unroll 1
          for (int q = 0; q < 2; ++q) {
            if (narm < NSEG) ARM_REC(narm)
            ++narm;
          }
        }
      }
      if (SPC) asm volatile("" ::"v"(st2));
      pace_step(pc, s, (ck.r << 4) + dm, st[1], mg_, tol_, xl_);
      t1 = clk_cyc(ck, dm + pc.dq + pc.ea - sdk_ + (on ? ofd_ : 0));
      EVDUMP(s);
      EVCLK(s, ck);
    }
  }
  return 0u;
}
__global__ __launch_bounds__(512, 2) void mega_kernel(const u16* __restrict__ x, const u16* __restrict__ mem0,
                                                      const u16* __restrict__ Wq, const u16* __restrict__ Wk,
                                                      const u16* __restrict__ Wv, const u16* __restrict__ Wo,
                                                      char* __restrict__ ws, u16* __restrict__ out,
                                                      const u16* __restrict__ ax, const u16* __restrict__ awr,
                                                      const u16* __restrict__ awi) {
  ws += (size_t)(-(uintptr_t)ws) & (UNIT - 1);
  const int bx = BIDX;
  const int w = __builtin_amdgcn_readfirstlane(threadIdx.x >> 6);
  const int xg = bx & 7, idx = bx >> 3;
#define TEND
#define TMID(k)
#define CODEPF 16
  if (w == 7 && idx < CODEPF) {
    u64 pc;
    unsigned len, keep;
    asm volatile("s_getpc_b64 %0\n.Lcode_here:\n\ts_mov_b32 %1, .Lcode_end-.Lcode_here\n\t.subsection 1\n.Lcode_end:\n\t.subsection 0" : "=s"(pc), "=s"(len));
    const v4u rc = {(unsigned)pc, (unsigned)(pc >> 32) & 0xffffu, len, 0x00020000u};
    asm volatile("s_mov_b32 %0, m0\n\ts_mov_b32 m0, %3\n\ts_nop 0\n\tbuffer_load_dword %1, %2, 0 offen lds\n\ts_mov_b32 m0, %0"
                 : "=&s"(keep)
                 : "v"(olane() * 128 + idx * 8192), "s"(rc), "s"(32768 + 7 * 2048)
                 : "memory");
  }
  const u64 nonce = hs_post(__builtin_amdgcn_make_buffer_rsrc(ws, 0, 65536, 0x00020000), w, xg >= NXC && (idx & PQ) ? 7 : 0);
  TMID(0);
  int role, h, par = 0;
  if (xg < NXC) {
    role = idx >= 2 * PQ ? 4 : idx >= PQ;
    h = xg * PQ + idx % PQ;
  } else {
    role = 2 + (idx / PQ & 1);
    par = idx / (2 * PQ);
    h = (xg - NXC) * PQ + idx % PQ;
  }
  if (role == 0) wroles<0>(h, par, idx & 7, xg, w, nonce, x, Wq, Wk, Wv, Wo, ws);
  if (role == 1) wroles<1>(h, par, idx & 7, xg, w, nonce, x, Wq, Wk, Wv, Wo, ws);
  if (role == 2) wroles<2>(h, par, idx & 7, xg, w, nonce, x, Wq, Wk, Wv, Wo, ws);
  if (role == 3) wroles<3>(h, par, idx & 7, xg, w, nonce, x, Wq, Wk, Wv, Wo, ws);
  const int oc = xg * PO + idx - 2 * PQ;
  unsigned perm = 0;
  if (role == 4 && HLT == 0) help(oc, w, Wq, Wk, Wv);
  if (role == 4) {
    perm = owner<true>(oc, w, nonce, mem0, ws, out, 0u, Wq, Wk, Wv);
    VMCNT(0);
#define PFT 1400
#define PFM 5
#define PFG 0
#define PFS 2
    const int w_ = w, oc_ = oc;
    if (PFT && (PFS >= 2 ? (((oc & 1) ? (PFS == 2 ? 0xb3 : 0xbb) : (PFS == 2 ? 0x13 : 0x11)) >> w) & 1 : PFS ? oc & 1 : w < 6 && ((PFM >> (w >> 1)) & 1))) {
      const int w = PFS ? w_ & 5 : w_, oc = PFS ? oc_ ^ ((w_ >> 1) & 1) : oc_;
      const u16* src = w < 2 ? x : (w < 4 ? ax : (w == 4 ? awr : awi));
      const __amdgpu_buffer_rsrc_t ri = __builtin_amdgcn_make_buffer_rsrc((void*)src, 0, w < 4 ? S * D * 2 : S * DF * 2, 0x00020000);
      _Pragma("clang loop unroll(disable)") for (int k = 0; k < 4096 && now32() - (unsigned)nonce < PFT + (w >> 1) * PFG; ++k) __builtin_amdgcn_s_sleep(4);
      unsigned t[5];
#pragma unroll
      for (int q = 0; q < 5; ++q)
        t[q] = __builtin_amdgcn_raw_buffer_load_b32(ri, olane() * 128 + oc * 8192 + (q + (w < 4 ? (w & 1) * 4 : 0)) * 524288 + (w < 4 && q == 4 ? OOB : 0), 0, 0);
      asm volatile("" ::"v"(t[0]), "v"(t[1]), "v"(t[2]), "v"(t[3]), "v"(t[4]));
    }
#define CPT 1450
    if (CPT && CODEPF && (PFS == 1 ? !(oc & 1) && w >= 5 && w < 7 : w == 6)) {
      _Pragma("clang loop unroll(disable)") for (int k = 0; k < 4096 && now32() - (unsigned)nonce < CPT; ++k) __builtin_amdgcn_s_sleep(4);
      u64 pc;
      unsigned len, off;
      asm volatile("s_getpc_b64 %0\n.Lcode_2:\n\ts_mov_b32 %1, .Lcode_end-.Lcode_here\n\ts_mov_b32 %2, .Lcode_2-.Lcode_here" : "=s"(pc), "=s"(len), "=s"(off));
      const unsigned t = __builtin_amdgcn_raw_buffer_load_b32(__builtin_amdgcn_make_buffer_rsrc((void*)(pc - (long long)(int)off), 0, len, 0x00020000),
                                                              olane() * 128 + (PFS == 1 ? (oc & 14) + (w & 1) : idx & 15) * 8192, 0, 0);
      asm volatile("" ::"v"(t));
    }
  }
  TMID(2);
  const __amdgpu_buffer_rsrc_t rs = __builtin_amdgcn_make_buffer_rsrc(ws, 0, 65536, 0x00020000);
  gu32* tmo = (gu32*)(ws + OFF_TMO);
  volatile LAS int* sact = (volatile LAS int*)(g_lds + 163584);
  unsigned idle = 0;
  bool tab = false;
#define FSIDE 0
  char* const x1 = ws + OFF_X1 + ((size_t)(-(uintptr_t)(ws + OFF_X1)) & ((2u << 20) - 1));
  int sd = 0;
#define TLOG(id)
  auto pick = [&]() __attribute__((always_inline)) {
    int off = CNT_FFT1 + (olane() & 7) * 128;
    asm volatile("" : "+v"(off));
    const unsigned v = __builtin_amdgcn_raw_buffer_load_b32(rs, off, 0, AUX_SC1) + 1u;
    const unsigned d1 = __builtin_amdgcn_readlane(v, 0), d2 = __builtin_amdgcn_readlane(v, 1);
    const unsigned q1 = __builtin_amdgcn_readlane(v, 4), q2 = __builtin_amdgcn_readlane(v, 5), q3 = __builtin_amdgcn_readlane(v, 6);
    int kind = -1, qoff = 0;
    unsigned lim = 0;
    if (q1 < 130u - NF1O) kind = 1, qoff = Q_FFT1, lim = 130u - NF1O;
    else if (d1 >= 128u && q2 < 65u) kind = 2, qoff = Q_FFT2, lim = 65u;
    else if (d2 >= 65u && q3 < 128u) kind = 3, qoff = Q_FFT3, lim = 128u;
    else if (q3 >= 128u) kind = -2;
    int act = kind;
    if (kind >= 0) {
      unsigned t = 0;
      if (olane() == 0) t = __hip_atomic_fetch_add((gu32*)(ws + qoff), 1u, RLX_AGENT) + 1u;
      t = __builtin_amdgcn_readfirstlane(t);
      act = t < lim ? (kind << 16) | (int)t : -3;
    }
    return act;
  };
  for (;;) {
    if (w == 0) *sact = role == 4 ? (oc > 1 && oc < NF1O ? 0x18000 | oc : -2) : pick();
    RAW_BARRIER();
    const int act = __builtin_amdgcn_readfirstlane(*sact);
    RAW_BARRIER();
    if (act == -2) break;
    if (act >= 0) {
      const int kind = act >> 16, t = act & 0xffff;
      TLOG(1000 + kind);
      if (!tab) {
        if (w == 7) sact[1] = numa_far(numa_load(rs, tmo));
        fft_table(w);
        sd = (__builtin_amdgcn_readfirstlane(sact[1]) ^ (int)((uintptr_t)x1 >> 8) ^ FSIDE) & 8192;
      }
      tab = true;
      if (kind == 1) {
        fft_pass1(ws, x1, sd, ax, awr, awi, (t & 0x8000) ? t & 0xff : (t < 2 ? t : NF1O - 2 + t), w);
        count_done(ws, CNT_FFT1, w);
      } else if (kind == 2) {
        fft_pass2(ws, x1, sd, t == 0 ? 64 : t - 1, w);
        count_done(ws, CNT_FFT2, w);
      } else fft_pass3(ws, x1, sd, out, t, w);
      TLOG(999);
      if (role == 4) break;
    } else if (act == -1) {
      __builtin_amdgcn_s_sleep(64);
      if (spin_fail(idle, tmo, 0x30022)) break;
    }
  }
  if (role == 4) {
    owner<false>(oc, w, nonce, mem0, ws, out, perm, Wq, Wk, Wv);
    return;
  }
  TEND;
}

extern "C" void kernel_launch(void* const* d_in, const int* in_sizes, int n_in, void* d_out, int out_size, void* d_ws,
                              size_t ws_size, hipStream_t stream) {
  const u16* A_x = (const u16*)d_in[0];
  const u16* A_wr = (const u16*)d_in[1];
  const u16* A_wi = (const u16*)d_in[2];
  const u16* B_x = (const u16*)d_in[3];
  const u16* B_mem0 = (const u16*)d_in[4];
  const u16* Wq = (const u16*)d_in[5];
  const u16* Wk = (const u16*)d_in[6];
  const u16* Wv = (const u16*)d_in[7];
  const u16* Wo = (const u16*)d_in[8];
  u16* out = (u16*)d_out;
  char* ws = (char*)d_ws;

  mega_kernel<<<256, 512, 0, stream>>>(B_x, B_mem0, Wq, Wk, Wv, Wo, ws, out, A_x, A_wr, A_wi);
}
```
